```python
import torch

def _softmax(x, axis=-1):
    return torch.softmax(x, dim=axis)

def _gelu(x):
    return torch.nn.functional.gelu(x, approximate='tanh')

def _silu(x):
    return torch.nn.functional.silu(x)

def _sigmoid(x):
    return torch.sigmoid(x)

def _layernorm(x, axis=-1, eps=1e-05):
    m = x.mean(dim=axis, keepdim=True)
    v = x.var(dim=axis, keepdim=True, unbiased=False)
    return (x - m) / torch.sqrt(v + eps)

def _scatter_add(tgt, idx, src, axis=0):
    return tgt.clone().index_add_(axis, idx, src)

def _segment_sum(src, seg, n):
    out = torch.zeros((n,) + src.shape[1:], dtype=src.dtype)
    return out.index_add_(0, seg, src)

def _pad(x, pw):
    flat = tuple((v for pair in reversed(pw) for v in pair))
    return torch.nn.functional.pad(x, flat)
kernel_name = 'standalone__dilated_atrous__vision'

def setup_inputs():

    _w = lambda a: (lambda t: t.to(torch.bfloat16) if t.is_floating_point() else t)(torch.as_tensor(a))
    H = W = 512
    C = 96
    k = 3
    return {'x': _w((lambda *s: torch.randn(*s, dtype=torch.bfloat16) * 0.05)(1, H, W, C)), 'w': _w((lambda *s: torch.randn(*s, dtype=torch.bfloat16) * 0.05)(k, k, C, C) * (1.0 / (k * C ** 0.5)))}

def reference(x, w):
    B, H, W, C = x.shape
    k = 3
    rate = 2
    pad = (k - 1) * rate // 2
    xp = _pad(x, ((0, 0), (pad, pad), (pad, pad), (0, 0)))
    out = torch.zeros_like(x)
    for i in range(k):
        for j in range(k):
            tap = xp[:, i * rate:i * rate + H, j * rate:j * rate + W, :].clone()
            out = out + torch.einsum('bhwc,co->bhwo', tap, w[i, j])
    return out
```

```cpp
#include <hip/hip_runtime.h>
#include <hip/hip_bf16.h>
#include <stdint.h>

__global__ void __launch_bounds__(256, 1) __attribute__((aligned(4096)))
conv_kernel(const char* __restrict__ x, const char* __restrict__ w, char* __restrict__ out, char* __restrict__ dbg) {
  __shared__ __attribute__((aligned(1024))) char smem[163840];
  asm volatile("" :: "v"((unsigned)(uintptr_t)smem) : "memory");
  asm volatile(
      "s_load_dwordx8 s[20:27], %2, 0x0\n"
      "v_readfirstlane_b32 s40, %0\n"
      "v_and_b32 v5, 63, %0\n"
      "v_lshlrev_b32 v8, 4, v5\n"
      "s_lshr_b32 s40, s40, 6\n"
      "s_lshl_b32 s43, s40, 3\n"
      "s_or_b32 s43, s43, 0x80000\n"
      "v_bitop3_b32 v9, v8, v5, 32 bitop3:0x78\n"
      "v_xor_b32 v89, 32, v8\n"
      "s_mul_i32 s92, s40, 3072\n"
      "s_lshr_b32 s41, s40, 1\n"
      "s_and_b32 s42, s40, 1\n"
      "s_mov_b32 s30, 165888\n"
      "s_mov_b32 s31, 0x20000\n"
      "s_lshr_b32 s49, %1, 3\n"
      "s_lshl2_add_u32 s49, s49, s40\n"
      "v_lshlrev_b32 v36, 7, v5\n"
      "s_mul_i32 s50, s49, 384\n"
      "v_min_u32 v37, 256, v36\n"
      "v_add_u32 v37, s50, v37\n"
      "v_min_u32 v66, 1280, v36\n"
      "s_mul_i32 s96, s49, 1408\n"
      "s_getpc_b64 s[54:55]\n"
      "Lconv_start:\n"
      "v_min_u32 v37, Lconv_end-Lconv_start-4, v37\n"
      "s_mov_b32 m0, 162816\n"
      "s_nop 0\n"
      "global_load_lds_dword v37, s[54:55]\n"
      "s_bfe_u32 s99, %1, 0x30003\n"
      "s_mul_i32 s49, s40, 192\n"
      "v_add_u32 v36, s49, v5\n"
      "v_add_u32 v38, 128, v36\n"
      "v_mul_u32_u24 v37, 669, v36\n"
      "v_lshrrev_b32 v37, 16, v37\n"
      "v_mad_i32_i24 v37, v37, -2, v38\n"
      "v_lshlrev_b32 v16, 4, v37\n"
      "v_add_u32 v36, 64, v36\n"
      "v_mul_u32_u24 v37, 669, v36\n"
      "v_lshrrev_b32 v37, 16, v37\n"
      "v_mad_i32_i24 v37, v37, -2, v38\n"
      "v_lshlrev_b32 v17, 4, v37\n"
      "v_add_u32 v36, 64, v36\n"
      "v_mul_u32_u24 v37, 669, v36\n"
      "v_lshrrev_b32 v37, 16, v37\n"
      "v_mad_i32_i24 v37, v37, -2, v38\n"
      "v_lshlrev_b32 v18, 4, v37\n"
      "s_or_b32 s50, s99, s40\n"
      "s_cmp_eq_u32 s50, 0\n"
      "s_cselect_b32 s16, 0xffffff, 0\n"
      "s_mov_b32 s17, 0\n"
      "s_cmp_eq_u32 s99, 7\n"
      "s_cselect_b32 s19, 0xffffff00, 0\n"
      "s_mov_b32 s18, 0\n"
      "v_add_u32 v19, 14080, v8\n"
      "v_cndmask_b32 v16, v16, 2.0, s[16:17]\n"
      "v_cndmask_b32 v19, v19, 2.0, s[18:19]\n"
      "v_and_b32 v36, 16, v8\n"
      "v_mov_b32 v52, 0xffff\n"
      "v_lshlrev_b32 v52, v36, v52\n"
      "s_mov_b32 s16, 0x3000003\n"
      "s_mov_b32 s17, 0x3000003\n"
      "v_cndmask_b32 v48, 0, v52, s[16:17]\n"
      "s_lshl_b64 s[16:17], s[16:17], 2\n"
      "v_cndmask_b32 v49, 0, v52, s[16:17]\n"
      "s_lshl_b64 s[16:17], s[16:17], 2\n"
      "v_cndmask_b32 v50, 0, v52, s[16:17]\n"
      "s_lshl_b64 s[16:17], s[16:17], 2\n"
      "v_cndmask_b32 v51, 0, v52, s[16:17]\n"
      "v_bfe_u32 v53, v5, 5, 1\n"
      "v_xor_b32 v53, s41, v53\n"
      "v_sub_u32 v53, 0, v53\n"
      "v_and_b32 v36, 0x1000100, v53\n"
      "v_xor_b32 v36, 0x3fa83fa8, v36\n"
      "v_and_b32 v68, v36, v48\n"
      "v_and_b32 v69, v36, v49\n"
      "v_and_b32 v70, v36, v50\n"
      "v_and_b32 v71, v36, v51\n"
      "v_xor_b32 v72, 0x80008000, v68\n"
      "v_xor_b32 v73, 0x80008000, v69\n"
      "v_xor_b32 v74, 0x80008000, v70\n"
      "v_xor_b32 v75, 0x80008000, v71\n"
      "v_bfe_i32 v36, v5, 5, 1\n"
      "v_and_b32 v36, 0x3f283f28, v36\n"
      "v_xor_b32 v37, 0x3f283f28, v36\n"
      "v_and_b32 v76, v37, v48\n"
      "v_and_b32 v77, v37, v49\n"
      "v_and_b32 v78, v37, v50\n"
      "v_and_b32 v79, v37, v51\n"
      "v_and_b32 v80, v36, v48\n"
      "v_and_b32 v81, v36, v49\n"
      "v_and_b32 v82, v36, v50\n"
      "v_and_b32 v83, v36, v51\n"
      "s_mul_i32 s50, s42, 96\n"
      "v_bfe_u32 v36, v5, 1, 3\n"
      "v_and_b32 v37, 1, v5\n"
      "v_lshl_add_u32 v36, v37, 3, v36\n"
      "v_mul_u32_u24 v36, 192, v36\n"
      "v_add_u32 v36, 46144, v36\n"
      "v_lshlrev_b32 v37, 5, v37\n"
      "v_and_b32 v38, 16, v5\n"
      "v_add_u32 v38, s50, v38\n"
      "v_and_b32 v39, 32, v5\n"
      "v_mul_u32_u24 v40, 192, v39\n"
      "v_add_u32 v40, v40, v36\n"
      "v_xad_u32 v84, v38, v37, v40\n"
      "v_add_u32 v41, 32, v38\n"
      "v_xad_u32 v85, v41, v37, v40\n"
      "v_add_u32 v41, v39, v38\n"
      "v_xad_u32 v86, v41, v37, v36\n"
      "s_mul_i32 s49, s41, 6144\n"
      "s_add_u32 s49, s49, 46144\n"
      "v_lshrrev_b32 v36, 3, v5\n"
      "v_and_b32 v38, 4, v5\n"
      "v_lshl_add_u32 v36, v38, 1, v36\n"
      "v_mul_u32_u24 v36, 192, v36\n"
      "v_lshlrev_b32 v37, 3, v5\n"
      "v_and_b32 v38, 8, v37\n"
      "v_add3_u32 v36, v36, v38, s49\n"
      "v_and_b32 v38, 32, v37\n"
      "v_and_b32 v37, 16, v37\n"
      "v_add_u32 v37, s50, v37\n"
      "v_xad_u32 v87, v37, v38, v36\n"
      "v_add_u32 v39, 32, v37\n"
      "v_xad_u32 v88, v39, v38, v36\n"
      "s_waitcnt lgkmcnt(0)\n"
      "s_mov_b32 s28, s22\n"
      "s_and_b32 s29, s23, 0xffff\n"
      "buffer_load_dword v66, s[28:31], s96 offen lds\n"
      "s_add_u32 m0, s92, 73824\n"
      "s_bfe_u32 s97, 0x120c0600, s43\n"
      "s_mul_i32 s97, s97, 3072\n"
      "buffer_load_dwordx4 v8, s[28:31], s97 offen offset:0 lds\n"
      "buffer_load_dwordx4 v9, s[28:31], s97 offen offset:1024 lds\n"
      "buffer_load_dwordx4 v89, s[28:31], s97 offen offset:2048 lds\n"
      "s_add_u32 m0, s92, 86112\n"
      "s_bfe_u32 s97, 0x2a241e18, s43\n"
      "s_mul_i32 s97, s97, 3072\n"
      "buffer_load_dwordx4 v8, s[28:31], s97 offen offset:0 lds\n"
      "buffer_load_dwordx4 v9, s[28:31], s97 offen offset:1024 lds\n"
      "buffer_load_dwordx4 v89, s[28:31], s97 offen offset:2048 lds\n"
      "s_and_b32 s53, %1, 7\n"
      "s_lshl_b32 s53, s53, 6\n"
      "s_bfe_u32 s50, %1, 0x10006\n"
      "s_add_u32 s53, s53, s50\n"
      "s_lshr_b32 s50, %1, 7\n"
      "s_lshl_b32 s50, s50, 5\n"
      "s_add_u32 s53, s53, s50\n"
      "s_mul_i32 s50, s99, 12288\n"
      "s_sub_u32 s50, s50, 2432\n"
      "s_ashr_i32 s49, s50, 31\n"
      "s_add_u32 s32, s20, s50\n"
      "s_addc_u32 s33, s21, s49\n"
      "s_and_b32 s33, s33, 0xffff\n"
      "s_add_u32 s34, s53, 33\n"
      "s_min_u32 s34, s34, 512\n"
      "s_mul_i32 s34, s34, 98304\n"
      "s_sub_u32 s34, s34, s50\n"
      "s_mov_b32 s35, 0x20000\n"
      "s_sub_u32 s47, s53, 2\n"
      "s_mul_i32 s47, s47, 98304\n"
      "s_mul_i32 s46, s40, 3072\n"
      "s_add_u32 s46, s46, 110720\n"
      "s_cmp_ge_u32 s53, 2\n"
      "s_cselect_b32 s48, s47, 2.0\n"
      "s_add_u32 m0, s46, 13280\n"
      "s_mov_b32 s51, s48\n"
      "buffer_load_dwordx4 v16, s[32:35], s51 offen lds\n"
      "v_lshrrev_b32 v36, 1, v5\n"
      "v_and_b32 v37, 4, v36\n"
      "v_and_or_b32 v37, v5, 2, v37\n"
      "v_mul_u32_u24 v37, 1568, v37\n"
      "v_xor_b32 v36, v36, v5\n"
      "v_and_b32 v36, 4, v36\n"
      "v_mad_u32_u24 v37, v36, 48, v37\n"
      "s_mul_i32 s50, s42, 1568\n"
      "s_add_u32 s50, s50, 110720\n"
      "v_and_b32 v36, 16, v5\n"
      "v_lshl_add_u32 v37, v36, 1, v37\n"
      "v_and_b32 v36, 16, v8\n"
      "v_add3_u32 v37, v37, v36, s50\n"
      "v_and_b32 v36, 32, v5\n"
      "v_mad_u32_u24 v38, v36, 24, v37\n"
      "v_mad_u32_u24 v39, v36, 25, v37\n"
      "s_mov_b64 exec, 0\n"
      "s_cmp_eq_u32 s40, 0\n"
      "s_cbranch_scc1 Lconv_p1\n"
      "s_cmp_eq_u32 s40, 1\n"
      "s_cbranch_scc1 Lconv_p2\n"
      "s_cmp_eq_u32 s40, 2\n"
      "s_cbranch_scc1 Lconv_pa0\n"
      "s_cmp_eq_u32 s40, 3\n"
      "s_cbranch_scc1 Lconv_pa1\n"
      "Lconv_r0:\n"
      "s_mov_b64 exec, -1\n"
      "s_add_u32 m0, s92, 98400\n"
      "s_bfe_u32 s97, 0xd070130, s43\n"
      "s_mul_i32 s97, s97, 3072\n"
      "buffer_load_dwordx4 v8, s[28:31], s97 offen offset:0 lds\n"
      "buffer_load_dwordx4 v9, s[28:31], s97 offen offset:1024 lds\n"
      "buffer_load_dwordx4 v89, s[28:31], s97 offen offset:2048 lds\n"
      "s_add_u32 m0, s46, 13280\n"
      "s_nop 0\n"
      "buffer_load_dwordx4 v17, s[32:35], s51 offen offset:1024 lds\n"
      "s_mul_i32 s17, s42, 0xcc00cc00\n"
      "s_mov_b32 s16, 0\n"
      "v_mov_b32 v36, 768\n"
      "v_mov_b32 v37, 736\n"
      "v_cndmask_b32 v36, v36, v37, s[16:17]\n"
      "v_add_u32 v39, v39, v36\n"
      "s_cmp_eq_u32 s41, 0\n"
      "s_cselect_b64 vcc, -1, 0\n"
      "v_add_u32 v40, 384, v38\n"
      "v_add_u32 v41, 384, v39\n"
      "v_cndmask_b32 v2, v40, v39, vcc\n"
      "v_cndmask_b32 v3, v39, v40, vcc\n"
      "v_cndmask_b32 v4, v41, v38, vcc\n"
      "v_and_b32 v36, 3, v5\n"
      "v_mul_u32_u24 v36, 18464, v36\n"
      "v_bfe_u32 v37, v5, 2, 2\n"
      "v_lshrrev_b32 v38, 1, v37\n"
      "v_xor_b32 v37, v37, v38\n"
      "v_lshl_add_u32 v36, v37, 6, v36\n"
      "s_mul_i32 s50, s41, 2304\n"
      "v_and_b32 v37, 48, v5\n"
      "v_add3_u32 v6, v36, v37, s50\n"
      "v_add_u32 v7, 9216, v6\n"
      "s_mov_b32 s16, 0xc0003\n"
      "s_mov_b32 s17, 0xc0003\n"
      "v_cndmask_b32 v48, 0, v52, s[16:17]\n"
      "s_lshl_b64 s[16:17], s[16:17], 4\n"
      "v_cndmask_b32 v49, 0, v52, s[16:17]\n"
      "s_lshl_b64 s[16:17], s[16:17], 4\n"
      "v_cndmask_b32 v50, 0, v52, s[16:17]\n"
      "s_lshl_b64 s[16:17], s[16:17], 4\n"
      "v_cndmask_b32 v51, 0, v52, s[16:17]\n"
      "v_and_b32 v36, 0xff00ff00, v53\n"
      "v_xor_b32 v36, 0xc080c080, v36\n"
      "v_and_b32 v38, v36, v48\n"
      "v_and_b32 v39, v36, v49\n"
      "v_and_b32 v40, v36, v50\n"
      "v_and_b32 v41, v36, v51\n"
      "v_accvgpr_write_b32 a72, v38\n"
      "v_accvgpr_write_b32 a73, v39\n"
      "v_accvgpr_write_b32 a74, v40\n"
      "v_accvgpr_write_b32 a75, v41\n"
      "v_and_b32 v36, 0xff00ff00, v53\n"
      "v_xor_b32 v36, 0xc05dc05d, v36\n"
      "v_and_b32 v38, v36, v48\n"
      "v_and_b32 v39, v36, v49\n"
      "v_and_b32 v40, v36, v50\n"
      "v_and_b32 v41, v36, v51\n"
      "v_accvgpr_write_b32 a76, v38\n"
      "v_accvgpr_write_b32 a77, v39\n"
      "v_accvgpr_write_b32 a78, v40\n"
      "v_accvgpr_write_b32 a79, v41\n"
      "v_and_b32 v36, 0x81008100, v53\n"
      "v_xor_b32 v36, 0x3f803f80, v36\n"
      "v_and_b32 v38, v36, v48\n"
      "v_and_b32 v39, v36, v49\n"
      "v_and_b32 v40, v36, v50\n"
      "v_and_b32 v41, v36, v51\n"
      "v_accvgpr_write_b32 a80, v38\n"
      "v_accvgpr_write_b32 a81, v39\n"
      "v_accvgpr_write_b32 a82, v40\n"
      "v_accvgpr_write_b32 a83, v41\n"
      "s_mov_b32 s44, 0x3fdd0000\n"
      "v_lshlrev_b32 v36, 5, v5\n"
      "v_and_b32 v36, 0x180, v36\n"
      "v_lshlrev_b32 v37, 3, v5\n"
      "v_and_b32 v37, 16, v37\n"
      "v_sub_u32 v38, 0x3e71, v36\n"
      "v_add_u32 v39, 0x3cf1, v36\n"
      "v_mov_b32 v40, 0x3f80\n"
      "s_mov_b64 vcc, 0xffff\n"
      "s_mov_b32 s16, 0xffff0000\n"
      "s_mov_b32 s17, 0xf0f0\n"
      "v_xor_b32 v41, 0x8000, v38\n"
      "v_cndmask_b32 v38, v38, v41, s[16:17]\n"
      "v_cndmask_b32 v38, v38, v40, vcc\n"
      "v_lshlrev_b32 v38, v37, v38\n"
      "v_xor_b32 v41, 0x8000, v39\n"
      "s_xor_b32 s17, s17, 0xffff\n"
      "v_cndmask_b32 v39, v39, v41, s[16:17]\n"
      "v_cndmask_b32 v39, v39, v40, vcc\n"
      "v_lshlrev_b32 v39, v37, v39\n"
      "s_mov_b32 s18, 0x55550005\n"
      "s_mov_b32 s19, 0x5555\n"
      "v_cndmask_b32 v42, 0, v38, s[18:19]\n"
      "s_lshl_b64 s[16:17], s[18:19], 1\n"
      "v_cndmask_b32 v43, 0, v38, s[16:17]\n"
      "v_accvgpr_write_b32 a84, v42\n"
      "v_accvgpr_write_b32 a85, v43\n"
      "s_mov_b32 s18, 0x55555000\n"
      "v_cndmask_b32 v42, 0, v39, s[18:19]\n"
      "s_lshl_b64 s[16:17], s[18:19], 1\n"
      "v_cndmask_b32 v43, 0, v39, s[16:17]\n"
      "v_accvgpr_write_b32 a86, v42\n"
      "v_accvgpr_write_b32 a87, v43\n"
      "v_lshrrev_b32 v36, 4, v5\n"
      "v_min_u32 v37, 2, v36\n"
      "v_lshlrev_b32 v37, 10, v37\n"
      "s_lshl_b32 s50, s41, 7\n"
      "s_lshl_b32 s49, s42, 8\n"
      "s_add_u32 s50, s50, s49\n"
      "s_add_u32 s50, s50, 98416\n"
      "v_and_b32 v38, 8, v5\n"
      "v_lshl_add_u32 v37, v38, 3, v37\n"
      "v_add_u32 v37, s50, v37\n"
      "s_movk_i32 s50, -12296\n"
      "s_mov_b32 s49, 43040\n"
      "v_mov_b32 v42, s49\n"
      "v_bfe_u32 v39, v5, 1, 2\n"
      "v_lshrrev_b32 v40, 2, v38\n"
      "v_add_u32 v39, v39, v40\n"
      "v_lshlrev_b32 v40, 5, v5\n"
      "v_and_b32 v40, 32, v40\n"
      "v_cmp_lt_u32 vcc, 2, v39\n"
      "v_mad_i32_i24 v39, v39, s50, v37\n"
      "v_add_u32 v39, v39, v40\n"
      "v_cndmask_b32 v41, 0, v42, vcc\n"
      "v_add_u32 v10, v39, v41\n"
      "v_add_u32 v11, 3072, v10\n"
      "v_bfe_u32 v39, v5, 2, 1\n"
      "v_lshrrev_b32 v40, 1, v38\n"
      "v_sub_u32 v39, v39, v40\n"
      "v_add_u32 v39, 4, v39\n"
      "v_and_b32 v40, 48, v8\n"
      "v_cmp_lt_u32 vcc, 2, v39\n"
      "v_mad_i32_i24 v39, v39, s50, v37\n"
      "v_add_u32 v39, v39, v40\n"
      "v_cndmask_b32 v41, 0, v42, vcc\n"
      "v_add_u32 v12, v39, v41\n"
      "v_add_u32 v13, 3072, v12\n"
      "v_mul_u32_u24 v36, 384, v36\n"
      "v_and_b32 v37, 15, v5\n"
      "v_lshl_add_u32 v39, v37, 3, v36\n"
      "v_add_u32 v15, 128, v39\n"
      "v_add_lshl_u32 v37, v37, v38, 4\n"
      "v_add_u32 v14, v37, v36\n"
      "s_mul_i32 s99, s99, 12288\n"
      "s_mul_i32 s50, s53, 98304\n"
      "s_add_u32 s50, s50, s99\n"
      "s_mul_i32 s49, s40, 3072\n"
      "s_add_u32 s50, s50, s49\n"
      "s_add_u32 s36, s24, s50\n"
      "s_addc_u32 s37, s25, 0\n"
      "s_and_b32 s37, s37, 0xffff\n"
      "s_sub_u32 s38, 50331648, s50\n"
      "s_mov_b32 s39, 0x20000\n"
      "s_mul_i32 s45, s42, 6152\n"
      "s_lshl_b32 s50, s41, 9\n"
      "s_add_u32 s45, s45, s50\n"
      "s_add_u32 s45, s45, 41056\n"
      "s_mov_b32 s98, 0\n"
      "s_mov_b32 s91, 26560\n"
      "s_mul_i32 s18, s42, 18464\n"
      "s_mul_i32 s50, s41, 2304\n"
      "s_add_u32 s18, s18, s50\n"
      "s_lshl1_add_u32 s19, s18, 9216\n"
      "s_add_u32 s58, s46, 26560\n"
      "s_lshl1_add_u32 s93, s46, 26560\n"
      "s_mul_i32 s59, s42, -13280\n"
      "s_add_u32 s59, s59, 136256\n"
      "s_lshl1_add_u32 s94, s59, 26560\n"
      "s_mul_i32 s50, s42, 26560\n"
      "s_add_u32 s59, s59, s50\n"
      "s_mul_i32 s95, s42, 196608\n"
      "s_sub_u32 s95, 196608, s95\n"
      "s_mov_b32 s52, 4294770688\n"
      "s_cmp_lt_u32 s40, 3\n"
      "s_cselect_b32 s97, 27680, 0\n"
      "s_add_u32 s97, s97, s92\n"
      "s_add_u32 m0, s97, 36928\n"
      "s_bfe_u32 s97, 0x251f1913, s43\n"
      "s_mul_i32 s97, s97, 3072\n"
      "buffer_load_dwordx4 v8, s[28:31], s97 offen offset:0 lds\n"
      "buffer_load_dwordx4 v9, s[28:31], s97 offen offset:1024 lds\n"
      "buffer_load_dwordx4 v89, s[28:31], s97 offen offset:2048 lds\n"
      "s_add_u32 m0, s46, 13280\n"
      "s_nop 0\n"
      "buffer_load_dwordx4 v18, s[32:35], s51 offen offset:2048 lds\n"
      "s_cmp_lt_u32 s40, 2\n"
      "s_cselect_b32 s97, 27680, 0\n"
      "s_add_u32 s97, s97, s92\n"
      "s_add_u32 m0, s97, 21536\n"
      "s_bfe_u32 s97, 0x802312b, s43\n"
      "s_mul_i32 s97, s97, 3072\n"
      "buffer_load_dwordx4 v8, s[28:31], s97 offen offset:0 lds\n"
      "buffer_load_dwordx4 v9, s[28:31], s97 offen offset:1024 lds\n"
      "buffer_load_dwordx4 v89, s[28:31], s97 offen offset:2048 lds\n"
      "s_add_u32 m0, s46, 26560\n"
      "s_add_u32 s51, s47, 196608\n"
      "buffer_load_dwordx4 v16, s[32:35], s51 offen lds\n"
      "s_cmp_lt_u32 s40, 1\n"
      "s_cselect_b32 s97, 27680, 0\n"
      "s_add_u32 s97, s97, s92\n"
      "s_add_u32 m0, s97, 6144\n"
      "s_bfe_u32 s97, 0x201a140e, s43\n"
      "s_mul_i32 s97, s97, 3072\n"
      "buffer_load_dwordx4 v8, s[28:31], s97 offen offset:0 lds\n"
      "buffer_load_dwordx4 v9, s[28:31], s97 offen offset:1024 lds\n"
      "buffer_load_dwordx4 v89, s[28:31], s97 offen offset:2048 lds\n"
      "s_add_u32 m0, s46, 26560\n"
      "s_nop 0\n"
      "buffer_load_dwordx4 v17, s[32:35], s51 offen offset:1024 lds\n"
      "s_sub_u32 vcc_lo, 0, s41\n"
      "s_mov_b32 vcc_hi, 0\n"
      ".p2align 3\n"
      "s_waitcnt vmcnt(17)\n"
      "s_barrier\n"
      "s_cmp_lt_u32 s40, 3\n"
      "s_cselect_b32 s97, -9248, 0\n"
      "ds_read_b128 a[0:3], v86 offset:30752\n"
      "ds_read_b128 a[4:7], v84 offset:27680\n"
      "ds_read_b128 a[8:11], v85 offset:27680\n"
      "ds_read_b128 a[12:15], v86 offset:30816\n"
      "s_add_u32 s97, s97, s92\n"
      "s_add_u32 m0, s97, 9248\n"
      "s_bfe_u32 s97, 0x3322c26, s43\n"
      "s_mul_i32 s97, s97, 3072\n"
      "s_mul_i32 s96, s42, 196608\n"
      "s_waitcnt lgkmcnt(3)\n"
      "v_mfma_f32_16x16x32_bf16 v[20:23], a[0:3], v[76:79], 0\n"
      "s_waitcnt lgkmcnt(2)\n"
      "ds_read_b128 a[16:19], v84 offset:27744\n"
      "ds_read_b128 a[20:23], v86 offset:39968\n"
      "buffer_load_dwordx4 v8, s[28:31], s97 offen offset:0 lds\n"
      "v_mfma_f32_16x16x32_bf16 v[24:27], a[4:7], v[68:71], v[20:23]\n"
      "ds_read_b128 a[24:27], v84 offset:36896\n"
      "ds_read_b128 a[28:31], v85 offset:36896\n"
      "v_mfma_f32_16x16x32_bf16 v[20:23], a[4:7], v[72:75], v[20:23]\n"
      "ds_read_b64_tr_b16 a[88:89], v87 offset:27680\n"
      "ds_read_b64_tr_b16 a[92:93], v88 offset:27680\n"
      "buffer_load_dwordx4 v9, s[28:31], s97 offen offset:1024 lds\n"
      "v_mfma_f32_16x16x32_bf16 v[28:31], a[0:3], v[80:83], 0\n"
      "s_waitcnt lgkmcnt(7)\n"
      "ds_read_b64_tr_b16 a[96:97], v87 offset:27744\n"
      "ds_read_b64_tr_b16 a[124:125], v87 offset:36896\n"
      "v_mfma_f32_16x16x32_bf16 v[32:35], a[8:11], v[68:71], v[28:31]\n"
      "ds_read_b64_tr_b16 a[128:129], v88 offset:36896\n"
      "ds_read_b128 a[32:35], v86 offset:40032\n"
      "v_mfma_f32_16x16x32_bf16 v[28:31], a[8:11], v[72:75], v[28:31]\n"
      "s_waitcnt lgkmcnt(10)\n"
      "ds_read_b128 a[0:3], v84 offset:36960\n"
      "ds_read_b64_tr_b16 a[132:133], v87 offset:36960\n"
      "buffer_load_dwordx4 v89, s[28:31], s97 offen offset:2048 lds\n"
      "v_mfma_f32_16x16x32_bf16 v[54:57], a[12:15], v[76:79], 0\n"
      "s_waitcnt lgkmcnt(10)\n"
      "s_add_u32 m0, s46, 26560\n"
      "v_cvt_pk_bf16_f32 v96, v20, v21\n"
      "v_cvt_pk_bf16_f32 v97, v22, v23\n"
      "v_mfma_f32_16x16x32_bf16 v[58:61], a[16:19], v[68:71], v[54:57]\n"
      "v_cvt_pk_bf16_f32 v92, v24, v25\n"
      "v_cvt_pk_bf16_f32 v93, v26, v27\n"
      "v_mfma_f32_16x16x32_bf16 v[54:57], a[16:19], v[72:75], v[54:57]\n"
      "buffer_load_dwordx4 v18, s[32:35], s51 offen offset:2048 lds\n"
      "v_cvt_pk_bf16_f32 v104, v28, v29\n"
      "v_cvt_pk_bf16_f32 v105, v30, v31\n"
      "v_mfma_f32_16x16x32_bf16 v[62:65], a[20:23], v[76:79], 0\n"
      "s_waitcnt lgkmcnt(4)\n"
      "s_cmp_lt_u32 s40, 2\n"
      "s_cselect_b32 s97, -9248, 0\n"
      "s_add_u32 s97, s97, s92\n"
      "s_add_u32 m0, s97, 30784\n"
      "s_bfe_u32 s97, 0x1b150f09, s43\n"
      "s_mul_i32 s97, s97, 3072\n"
      "v_cvt_pk_bf16_f32 v100, v32, v33\n"
      "v_cvt_pk_bf16_f32 v101, v34, v35\n"
      "v_mfma_f32_16x16x32_bf16 v[20:23], a[24:27], v[68:71], v[62:65]\n"
      "v_cvt_pk_bf16_f32 v108, v54, v55\n"
      "v_cvt_pk_bf16_f32 v109, v56, v57\n"
      "v_mfma_f32_16x16x32_bf16 v[62:65], a[24:27], v[72:75], v[62:65]\n"
      "s_waitcnt lgkmcnt(0)\n"
      "s_waitcnt vmcnt(17)\n"
      "s_cbranch_vccnz Lconv_h1\n"
      "s_barrier\n"
      "Lconv_h1:\n"
      "buffer_load_dwordx4 v8, s[28:31], s97 offen offset:0 lds\n"
      "v_mfma_f32_16x16x32_bf16 v[24:27], a[20:23], v[80:83], 0\n"
      "v_cvt_pk_bf16_f32 v58, v58, v59\n"
      "v_cvt_pk_bf16_f32 v60, v60, v61\n"
      "v_mfma_f32_16x16x32_bf16 v[28:31], a[28:31], v[68:71], v[24:27]\n"
      "s_cbranch_vccz Lconv_h2\n"
      "s_barrier\n"
      "Lconv_h2:\n"
      "ds_read_b128 a[4:7], v86 offset:49184\n"
      "ds_read_b128 a[8:11], v84 offset:46112\n"
      "ds_read_b128 a[12:15], v85 offset:46112\n"
      "ds_read_b128 a[16:19], v86 offset:49248\n"
      "v_mfma_f32_16x16x32_bf16 v[24:27], a[28:31], v[72:75], v[24:27]\n"
      "ds_read_b128 a[20:23], v84 offset:46176\n"
      "ds_read_b128 a[24:27], v86 offset:58400\n"
      "buffer_load_dwordx4 v9, s[28:31], s97 offen offset:1024 lds\n"
      "v_accvgpr_write_b32 a196, v58\n"
      "v_mfma_f32_16x16x32_bf16 v[32:35], a[32:35], v[76:79], 0\n"
      "ds_read_b128 a[28:31], v84 offset:55328\n"
      "ds_read_b128 a[32:35], v85 offset:55328\n"
      "v_accvgpr_write_b32 a197, v60\n"
      "v_cvt_pk_bf16_f32 v116, v62, v63\n"
      "v_mfma_f32_16x16x32_bf16 v[54:57], a[0:3], v[68:71], v[32:35]\n"
      "ds_read_b64_tr_b16 a[160:161], v87 offset:46112\n"
      "ds_read_b64_tr_b16 a[164:165], v88 offset:46112\n"
      "buffer_load_dwordx4 v89, s[28:31], s97 offen offset:2048 lds\n"
      "v_cvt_pk_bf16_f32 v117, v64, v65\n"
      "v_mfma_f32_16x16x32_bf16 v[32:35], a[0:3], v[72:75], v[32:35]\n"
      "s_waitcnt lgkmcnt(9)\n"
      "s_mul_i32 s97, s42, 13280\n"
      "s_add_u32 m0, s97, 136256\n"
      "ds_read_b64_tr_b16 a[168:169], v87 offset:46176\n"
      "ds_read_b64_tr_b16 a[90:91], v87 offset:55328\n"
      "s_add_u32 s96, s96, 0\n"
      "s_add_u32 s96, s96, s47\n"
      "s_cmp_eq_u32 s42, 0\n"
      "s_cselect_b32 s96, s48, s96\n"
      "v_cvt_pk_bf16_f32 v112, v20, v21\n"
      "v_cvt_pk_bf16_f32 v113, v22, v23\n"
      "v_mfma_f32_16x16x32_bf16 v[58:61], a[4:7], v[76:79], 0\n"
      "s_waitcnt lgkmcnt(10)\n"
      "ds_read_b64_tr_b16 a[94:95], v88 offset:55328\n"
      "ds_read_b128 a[0:3], v86 offset:58464\n"
      "v_cvt_pk_bf16_f32 v120, v24, v25\n"
      "v_cvt_pk_bf16_f32 v121, v26, v27\n"
      "v_mfma_f32_16x16x32_bf16 v[62:65], a[8:11], v[68:71], v[58:61]\n"
      "ds_read_b64_tr_b16 a[98:99], v87 offset:55392\n"
      "buffer_load_dwordx4 v19, s[32:35], s96 offen lds\n"
      "v_cvt_pk_bf16_f32 v28, v28, v29\n"
      "v_cvt_pk_bf16_f32 v30, v30, v31\n"
      "v_mfma_f32_16x16x32_bf16 v[58:61], a[8:11], v[72:75], v[58:61]\n"
      "s_cmp_lt_u32 s40, 1\n"
      "s_cselect_b32 s97, -9248, 0\n"
      "s_add_u32 s97, s97, s92\n"
      "s_add_u32 m0, s97, 52320\n"
      "s_bfe_u32 s97, 0x332d2721, s43\n"
      "s_mul_i32 s97, s97, 3072\n"
      "s_mul_i32 s96, s42, 196608\n"
      "v_accvgpr_write_b32 a204, v28\n"
      "v_accvgpr_write_b32 a205, v30\n"
      "v_cvt_pk_bf16_f32 v128, v32, v33\n"
      "v_cvt_pk_bf16_f32 v129, v34, v35\n"
      "v_mfma_f32_16x16x32_bf16 v[20:23], a[4:7], v[80:83], 0\n"
      "s_waitcnt lgkmcnt(7)\n"
      "s_nop 0\n"
      "ds_read_b128 a[4:7], v84 offset:55392\n"
      "v_cvt_pk_bf16_f32 v124, v54, v55\n"
      "v_mfma_f32_16x16x32_bf16 v[24:27], a[12:15], v[68:71], v[20:23]\n"
      "buffer_load_dwordx4 v8, s[28:31], s97 offen offset:0 lds\n"
      "v_cvt_pk_bf16_f32 v125, v56, v57\n"
      "v_mfma_f32_16x16x32_bf16 v[20:23], a[12:15], v[72:75], v[20:23]\n"
      "v_cvt_pk_bf16_f32 v132, v58, v59\n"
      "v_cvt_pk_bf16_f32 v133, v60, v61\n"
      "v_mfma_f32_16x16x32_bf16 v[28:31], a[16:19], v[76:79], 0\n"
      "v_cvt_pk_bf16_f32 v62, v62, v63\n"
      "v_cvt_pk_bf16_f32 v64, v64, v65\n"
      "v_mfma_f32_16x16x32_bf16 v[32:35], a[20:23], v[68:71], v[28:31]\n"
      "buffer_load_dwordx4 v9, s[28:31], s97 offen offset:1024 lds\n"
      "v_accvgpr_write_b32 a212, v62\n"
      "v_accvgpr_write_b32 a213, v64\n"
      "v_mfma_f32_16x16x32_bf16 v[28:31], a[20:23], v[72:75], v[28:31]\n"
      "v_cvt_pk_bf16_f32 v140, v20, v21\n"
      "v_cvt_pk_bf16_f32 v141, v22, v23\n"
      "v_mfma_f32_16x16x32_bf16 v[54:57], a[24:27], v[76:79], 0\n"
      "v_cvt_pk_bf16_f32 v136, v24, v25\n"
      "v_cvt_pk_bf16_f32 v137, v26, v27\n"
      "v_mfma_f32_16x16x32_bf16 v[58:61], a[28:31], v[68:71], v[54:57]\n"
      "buffer_load_dwordx4 v89, s[28:31], s97 offen offset:2048 lds\n"
      "v_cvt_pk_bf16_f32 v32, v32, v33\n"
      "v_mfma_f32_16x16x32_bf16 v[54:57], a[28:31], v[72:75], v[54:57]\n"
      "s_waitcnt lgkmcnt(0)\n"
      "s_waitcnt vmcnt(12)\n"
      "s_cbranch_vccnz Lconv_h3\n"
      "s_barrier\n"
      "Lconv_h3:\n"
      "s_add_u32 m0, s46, 39840\n"
      "s_add_u32 s51, s47, 393216\n"
      "v_cvt_pk_bf16_f32 v144, v28, v29\n"
      "v_mfma_f32_16x16x32_bf16 v[62:65], a[24:27], v[80:83], 0\n"
      "buffer_load_dwordx4 v16, s[32:35], s51 offen lds\n"
      "v_cvt_pk_bf16_f32 v145, v30, v31\n"
      "v_mfma_f32_16x16x32_bf16 v[20:23], a[32:35], v[68:71], v[62:65]\n"
      "s_cbranch_vccz Lconv_h4\n"
      "s_barrier\n"
      "Lconv_h4:\n"
      "s_add_u32 m0, s92, 73824\n"
      "s_bfe_u32 s97, 0x16100a04, s43\n"
      "ds_read_b128 a[8:11], v86 offset:21536\n"
      "ds_read_b128 a[12:15], v84 offset:18464\n"
      "ds_read_b128 a[16:19], v85 offset:18464\n"
      "ds_read_b128 a[20:23], v86 offset:21600\n"
      "s_mul_i32 s97, s97, 3072\n"
      "v_mfma_f32_16x16x32_bf16 v[62:65], a[32:35], v[72:75], v[62:65]\n"
      "ds_read_b128 a[24:27], v84 offset:18528\n"
      "ds_read_b128 a[28:31], v86 offset:3072\n"
      "v_cvt_pk_bf16_f32 v34, v34, v35\n"
      "v_accvgpr_write_b32 a216, v32\n"
      "v_mfma_f32_16x16x32_bf16 v[24:27], a[0:3], v[76:79], 0\n"
      "ds_read_b128 a[32:35], v84 offset:0\n"
      "ds_read_b128 a[0:3], v85 offset:0\n"
      "buffer_load_dwordx4 v8, s[28:31], s97 offen offset:0 lds\n"
      "v_accvgpr_write_b32 a217, v34\n"
      "v_mfma_f32_16x16x32_bf16 v[28:31], a[4:7], v[68:71], v[24:27]\n"
      "ds_read_b64_tr_b16 a[126:127], v87 offset:18464\n"
      "ds_read_b64_tr_b16 a[130:131], v88 offset:18464\n"
      "v_cvt_pk_bf16_f32 v98, v54, v55\n"
      "v_cvt_pk_bf16_f32 v99, v56, v57\n"
      "v_mfma_f32_16x16x32_bf16 v[24:27], a[4:7], v[72:75], v[24:27]\n"
      "s_waitcnt lgkmcnt(9)\n"
      "ds_read_b128 a[4:7], v86 offset:3136\n"
      "ds_read_b64_tr_b16 a[134:135], v87 offset:18528\n"
      "v_cvt_pk_bf16_f32 v94, v58, v59\n"
      "v_cvt_pk_bf16_f32 v95, v60, v61\n"
      "v_mfma_f32_16x16x32_bf16 v[32:35], a[8:11], v[76:79], 0\n"
      "s_waitcnt lgkmcnt(10)\n"
      "ds_read_b64_tr_b16 a[162:163], v87 offset:0\n"
      "ds_read_b64_tr_b16 a[166:167], v88 offset:0\n"
      "buffer_load_dwordx4 v9, s[28:31], s97 offen offset:1024 lds\n"
      "v_cvt_pk_bf16_f32 v106, v62, v63\n"
      "v_mfma_f32_16x16x32_bf16 v[54:57], a[12:15], v[68:71], v[32:35]\n"
      "ds_read_b64_tr_b16 a[170:171], v87 offset:64\n"
      "v_cvt_pk_bf16_f32 v107, v64, v65\n"
      "v_cvt_pk_bf16_f32 v102, v20, v21\n"
      "v_cvt_pk_bf16_f32 v103, v22, v23\n"
      "v_mfma_f32_16x16x32_bf16 v[32:35], a[12:15], v[72:75], v[32:35]\n"
      "v_cvt_pk_bf16_f32 v110, v24, v25\n"
      "v_cvt_pk_bf16_f32 v111, v26, v27\n"
      "v_cvt_pk_bf16_f32 v28, v28, v29\n"
      "v_cvt_pk_bf16_f32 v30, v30, v31\n"
      "v_mfma_f32_16x16x32_bf16 v[58:61], a[8:11], v[80:83], 0\n"
      "Lconv_p1:\n"
      "s_cbranch_execz Lconv_r0\n"
      "s_nop 0\n"
      "s_waitcnt lgkmcnt(7)\n"
      "s_nop 0\n"
      "ds_read_b128 a[8:11], v84 offset:64\n"
      "buffer_load_dwordx4 v89, s[28:31], s97 offen offset:2048 lds\n"
      "v_accvgpr_write_b32 a198, v28\n"
      "v_accvgpr_write_b32 a199, v30\n"
      "v_mfma_f32_16x16x32_bf16 v[62:65], a[16:19], v[68:71], v[58:61]\n"
      "s_add_u32 m0, s46, 39840\n"
      "v_cvt_pk_bf16_f32 v118, v32, v33\n"
      "v_cvt_pk_bf16_f32 v119, v34, v35\n"
      "v_add_u32 v86, -46144, v86\n"
      "v_cvt_pk_bf16_f32 v114, v54, v55\n"
      "v_mfma_f32_16x16x32_bf16 v[58:61], a[16:19], v[72:75], v[58:61]\n"
      "ds_read_b128 a[12:15], v86 offset:30752\n"
      "v_cvt_pk_bf16_f32 v115, v56, v57\n"
      "v_add_u32 v84, -46144, v84\n"
      "v_cvt_pk_bf16_f32 v62, v62, v63\n"
      "v_mfma_f32_16x16x32_bf16 v[20:23], a[20:23], v[76:79], 0\n"
      "ds_read_b128 a[16:19], v84 offset:27680\n"
      "buffer_load_dwordx4 v17, s[32:35], s51 offen offset:1024 lds\n"
      "v_mfma_f32_16x16x32_bf16 v[24:27], a[24:27], v[68:71], v[20:23]\n"
      "s_add_u32 m0, s92, 86112\n"
      "s_bfe_u32 s97, 0x2e28221c, s43\n"
      "s_mul_i32 s97, s97, 3072\n"
      "v_cvt_pk_bf16_f32 v122, v58, v59\n"
      "v_cvt_pk_bf16_f32 v123, v60, v61\n"
      "v_mfma_f32_16x16x32_bf16 v[20:23], a[24:27], v[72:75], v[20:23]\n"
      "ds_read_b128 a[24:27], v86 offset:30816\n"
      "buffer_load_dwordx4 v8, s[28:31], s97 offen offset:0 lds\n"
      "v_cvt_pk_bf16_f32 v64, v64, v65\n"
      "v_accvgpr_write_b32 a206, v62\n"
      "v_mfma_f32_16x16x32_bf16 v[28:31], a[28:31], v[76:79], 0\n"
      "v_accvgpr_write_b32 a207, v64\n"
      "v_add_u32 v85, -46144, v85\n"
      "v_mfma_f32_16x16x32_bf16 v[32:35], a[32:35], v[68:71], v[28:31]\n"
      "ds_read_b128 a[20:23], v85 offset:27680\n"
      "v_cvt_pk_bf16_f32 v130, v20, v21\n"
      "v_cvt_pk_bf16_f32 v131, v22, v23\n"
      "v_mfma_f32_16x16x32_bf16 v[28:31], a[32:35], v[72:75], v[28:31]\n"
      "buffer_load_dwordx4 v9, s[28:31], s97 offen offset:1024 lds\n"
      "v_add_u32 v87, -46144, v87\n"
      "v_add_u32 v88, -46144, v88\n"
      "v_mfma_f32_16x16x32_bf16 v[54:57], a[28:31], v[80:83], 0\n"
      "ds_read_b64_tr_b16 a[100:101], v87 offset:27680\n"
      "ds_read_b64_tr_b16 a[104:105], v88 offset:27680\n"
      "v_mfma_f32_16x16x32_bf16 v[58:61], a[0:3], v[68:71], v[54:57]\n"
      "ds_read_b128 a[28:31], v84 offset:27744\n"
      "ds_read_b64_tr_b16 a[108:109], v87 offset:27744\n"
      "v_cvt_pk_bf16_f32 v126, v24, v25\n"
      "v_cvt_pk_bf16_f32 v127, v26, v27\n"
      "v_mfma_f32_16x16x32_bf16 v[54:57], a[0:3], v[72:75], v[54:57]\n"
      "s_waitcnt lgkmcnt(5)\n"
      "s_nop 0\n"
      "buffer_load_dwordx4 v89, s[28:31], s97 offen offset:2048 lds\n"
      "v_cvt_pk_bf16_f32 v134, v28, v29\n"
      "v_cvt_pk_bf16_f32 v135, v30, v31\n"
      "v_mfma_f32_16x16x32_bf16 v[62:65], a[4:7], v[76:79], 0\n"
      "s_add_u32 m0, s46, 39840\n"
      "v_cvt_pk_bf16_f32 v32, v32, v33\n"
      "v_cvt_pk_bf16_f32 v34, v34, v35\n"
      "v_mfma_f32_16x16x32_bf16 v[20:23], a[8:11], v[68:71], v[62:65]\n"
      "v_accvgpr_write_b32 a214, v32\n"
      "v_accvgpr_write_b32 a215, v34\n"
      "v_mfma_f32_16x16x32_bf16 v[62:65], a[8:11], v[72:75], v[62:65]\n"
      "buffer_load_dwordx4 v18, s[32:35], s51 offen offset:2048 lds\n"
      "v_cvt_pk_bf16_f32 v142, v54, v55\n"
      "v_cvt_pk_bf16_f32 v143, v56, v57\n"
      "v_mfma_f32_16x16x32_bf16 v[24:27], a[12:15], v[76:79], 0\n"
      "s_add_u32 m0, s92, 98400\n"
      "s_bfe_u32 s97, 0x110b0534, s43\n"
      "s_mul_i32 s97, s97, 3072\n"
      "v_cvt_pk_bf16_f32 v138, v58, v59\n"
      "v_cvt_pk_bf16_f32 v139, v60, v61\n"
      "v_mfma_f32_16x16x32_bf16 v[28:31], a[16:19], v[68:71], v[24:27]\n"
      "v_cvt_pk_bf16_f32 v146, v62, v63\n"
      "v_cvt_pk_bf16_f32 v147, v64, v65\n"
      "v_mfma_f32_16x16x32_bf16 v[24:27], a[16:19], v[72:75], v[24:27]\n"
      "s_waitcnt lgkmcnt(0)\n"
      "s_waitcnt vmcnt(9)\n"
      "s_cbranch_vccnz Lconv_h5\n"
      "s_barrier\n"
      "Lconv_h5:\n"
      "buffer_load_dwordx4 v8, s[28:31], s97 offen offset:0 lds\n"
      "v_mfma_f32_16x16x32_bf16 v[32:35], a[12:15], v[80:83], 0\n"
      "v_cvt_pk_bf16_f32 v20, v20, v21\n"
      "v_cvt_pk_bf16_f32 v22, v22, v23\n"
      "v_mfma_f32_16x16x32_bf16 v[54:57], a[20:23], v[68:71], v[32:35]\n"
      "s_cbranch_vccz Lconv_h6\n"
      "s_barrier\n"
      "Lconv_h6:\n"
      "ds_read_b128 a[32:35], v86 offset:12288\n"
      "ds_read_b128 a[0:3], v84 offset:9216\n"
      "ds_read_b128 a[4:7], v85 offset:9216\n"
      "ds_read_b128 a[8:11], v86 offset:12352\n"
      "buffer_load_dwordx4 v9, s[28:31], s97 offen offset:1024 lds\n"
      "v_mfma_f32_16x16x32_bf16 v[32:35], a[20:23], v[72:75], v[32:35]\n"
      "ds_read_b128 a[12:15], v84 offset:9280\n"
      "ds_read_b128 a[16:19], v86 offset:3072\n"
      "v_accvgpr_write_b32 a218, v20\n"
      "v_accvgpr_write_b32 a219, v22\n"
      "v_mfma_f32_16x16x32_bf16 v[58:61], a[24:27], v[76:79], 0\n"
      "ds_read_b128 a[20:23], v84 offset:0\n"
      "ds_read_b128 a[24:27], v85 offset:0\n"
      "v_cvt_pk_bf16_f32 v152, v24, v25\n"
      "v_cvt_pk_bf16_f32 v153, v26, v27\n"
      "v_mfma_f32_16x16x32_bf16 v[62:65], a[28:31], v[68:71], v[58:61]\n"
      "ds_read_b64_tr_b16 a[136:137], v87 offset:9216\n"
      "ds_read_b64_tr_b16 a[140:141], v88 offset:9216\n"
      "buffer_load_dwordx4 v89, s[28:31], s97 offen offset:2048 lds\n"
      "v_cvt_pk_bf16_f32 v148, v28, v29\n"
      "v_mfma_f32_16x16x32_bf16 v[58:61], a[28:31], v[72:75], v[58:61]\n"
      "s_waitcnt lgkmcnt(9)\n"
      "s_add_u32 m0, s46, 0\n"
      "s_add_u32 s51, s47, 589824\n"
      "ds_read_b128 a[28:31], v86 offset:3136\n"
      "ds_read_b64_tr_b16 a[144:145], v87 offset:9280\n"
      "v_cvt_pk_bf16_f32 v149, v30, v31\n"
      "v_cvt_pk_bf16_f32 v156, v32, v33\n"
      "v_mfma_f32_16x16x32_bf16 v[20:23], a[32:35], v[76:79], 0\n"
      "s_waitcnt lgkmcnt(10)\n"
      "ds_read_b64_tr_b16 a[172:173], v87 offset:0\n"
      "ds_read_b64_tr_b16 a[176:177], v88 offset:0\n"
      "v_cvt_pk_bf16_f32 v157, v34, v35\n"
      "v_mfma_f32_16x16x32_bf16 v[24:27], a[0:3], v[68:71], v[20:23]\n"
      "ds_read_b64_tr_b16 a[180:181], v87 offset:64\n"
      "ds_read_b64_tr_b16 a[102:103], v87 offset:18464\n"
      "buffer_load_dwordx4 v16, s[32:35], s51 offen lds\n"
      "v_cvt_pk_bf16_f32 v54, v54, v55\n"
      "v_mfma_f32_16x16x32_bf16 v[20:23], a[0:3], v[72:75], v[20:23]\n"
      "s_cmp_lt_u32 s40, 3\n"
      "s_cselect_b32 s97, 27680, 0\n"
      "ds_read_b128 a[0:3], v86 offset:21536\n"
      "ds_read_b64_tr_b16 a[106:107], v88 offset:18464\n"
      "s_add_u32 s97, s97, s92\n"
      "s_add_u32 m0, s97, 36928\n"
      "s_bfe_u32 s97, 0x29231d17, s43\n"
      "s_mul_i32 s97, s97, 3072\n"
      "v_cvt_pk_bf16_f32 v56, v56, v57\n"
      "v_accvgpr_write_b32 a200, v54\n"
      "v_mfma_f32_16x16x32_bf16 v[28:31], a[32:35], v[80:83], 0\n"
      "s_waitcnt lgkmcnt(10)\n"
      "ds_read_b128 a[32:35], v84 offset:64\n"
      "ds_read_b64_tr_b16 a[110:111], v87 offset:18528\n"
      "v_accvgpr_write_b32 a201, v56\n"
      "v_cvt_pk_bf16_f32 v164, v58, v59\n"
      "v_mfma_f32_16x16x32_bf16 v[32:35], a[4:7], v[68:71], v[28:31]\n"
      "ds_read_b64_tr_b16 a[138:139], v87 offset:36928\n"
      "ds_read_b64_tr_b16 a[142:143], v88 offset:36928\n"
      "buffer_load_dwordx4 v8, s[28:31], s97 offen offset:0 lds\n"
      "v_cvt_pk_bf16_f32 v165, v60, v61\n"
      "v_mfma_f32_16x16x32_bf16 v[28:31], a[4:7], v[72:75], v[28:31]\n"
      "ds_read_b128 a[4:7], v84 offset:18464\n"
      "ds_read_b64_tr_b16 a[146:147], v87 offset:36992\n"
      "v_cvt_pk_bf16_f32 v160, v62, v63\n"
      "v_cvt_pk_bf16_f32 v161, v64, v65\n"
      "v_mfma_f32_16x16x32_bf16 v[54:57], a[8:11], v[76:79], 0\n"
      "ds_read_b128 a[8:11], v85 offset:18464\n"
      "ds_read_b64_tr_b16 a[174:175], v87 offset:55392\n"
      "v_cvt_pk_bf16_f32 v168, v20, v21\n"
      "v_cvt_pk_bf16_f32 v169, v22, v23\n"
      "v_mfma_f32_16x16x32_bf16 v[58:61], a[12:15], v[68:71], v[54:57]\n"
      "ds_read_b64_tr_b16 a[178:179], v88 offset:55392\n"
      "ds_read_b64_tr_b16 a[182:183], v87 offset:55456\n"
      "buffer_load_dwordx4 v9, s[28:31], s97 offen offset:1024 lds\n"
      "v_cvt_pk_bf16_f32 v24, v24, v25\n"
      "v_mfma_f32_16x16x32_bf16 v[54:57], a[12:15], v[72:75], v[54:57]\n"
      "ds_read_b128 a[12:15], v86 offset:21600\n"
      "v_cvt_pk_bf16_f32 v26, v26, v27\n"
      "v_accvgpr_write_b32 a208, v24\n"
      "v_accvgpr_write_b32 a209, v26\n"
      "v_mfma_f32_16x16x32_bf16 v[62:65], a[16:19], v[76:79], 0\n"
      "buffer_load_dwordx4 v89, s[28:31], s97 offen offset:2048 lds\n"
      "v_cvt_pk_bf16_f32 v176, v28, v29\n"
      "v_cvt_pk_bf16_f32 v177, v30, v31\n"
      "v_cvt_pk_bf16_f32 v172, v32, v33\n"
      "v_mfma_f32_16x16x32_bf16 v[20:23], a[20:23], v[68:71], v[62:65]\n"
      "s_add_u32 m0, s46, 0\n"
      "v_cvt_pk_bf16_f32 v173, v34, v35\n"
      "v_cvt_pk_bf16_f32 v54, v54, v55\n"
      "v_cvt_pk_bf16_f32 v56, v56, v57\n"
      "v_mfma_f32_16x16x32_bf16 v[62:65], a[20:23], v[72:75], v[62:65]\n"
      "ds_read_b128 a[20:23], v86 offset:40000\n"
      "v_accvgpr_write_b32 a228, v54\n"
      "v_accvgpr_write_b32 a229, v56\n"
      "v_mfma_f32_16x16x32_bf16 v[24:27], a[16:19], v[80:83], 0\n"
      "ds_read_b128 a[16:19], v84 offset:18528\n"
      "buffer_load_dwordx4 v17, s[32:35], s51 offen offset:1024 lds\n"
      "v_mfma_f32_16x16x32_bf16 v[28:31], a[24:27], v[68:71], v[24:27]\n"
      "s_cmp_lt_u32 s40, 2\n"
      "s_cselect_b32 s97, 6144, 0\n"
      "s_add_u32 s97, s97, s92\n"
      "s_add_u32 m0, s97, 43072\n"
      "s_bfe_u32 s97, 0x352f352f, s43\n"
      "s_mul_i32 s97, s97, 3072\n"
      "v_cvt_pk_bf16_f32 v180, v58, v59\n"
      "v_cvt_pk_bf16_f32 v181, v60, v61\n"
      "v_mfma_f32_16x16x32_bf16 v[24:27], a[24:27], v[72:75], v[24:27]\n"
      "s_waitcnt lgkmcnt(2)\n"
      "ds_read_b128 a[24:27], v84 offset:36928\n"
      "v_cvt_pk_bf16_f32 v188, v62, v63\n"
      "v_cvt_pk_bf16_f32 v189, v64, v65\n"
      "v_cvt_pk_bf16_f32 v184, v20, v21\n"
      "v_mfma_f32_16x16x32_bf16 v[32:35], a[28:31], v[76:79], 0\n"
      "ds_read_b128 a[28:31], v85 offset:36928\n"
      "buffer_load_dwordx4 v8, s[28:31], s97 offen offset:0 lds\n"
      "v_cvt_pk_bf16_f32 v185, v22, v23\n"
      "v_mfma_f32_16x16x32_bf16 v[54:57], a[32:35], v[68:71], v[32:35]\n"
      "v_cvt_pk_bf16_f32 v24, v24, v25\n"
      "v_cvt_pk_bf16_f32 v26, v26, v27\n"
      "v_mfma_f32_16x16x32_bf16 v[32:35], a[32:35], v[72:75], v[32:35]\n"
      "ds_read_b128 a[32:35], v86 offset:40064\n"
      "v_accvgpr_write_b32 a236, v24\n"
      "v_accvgpr_write_b32 a237, v26\n"
      "v_mfma_f32_16x16x32_bf16 v[58:61], a[0:3], v[76:79], 0\n"
      "buffer_load_dwordx4 v9, s[28:31], s97 offen offset:1024 lds\n"
      "v_cvt_pk_bf16_f32 v192, v28, v29\n"
      "v_mfma_f32_16x16x32_bf16 v[62:65], a[4:7], v[68:71], v[58:61]\n"
      "v_cvt_pk_bf16_f32 v193, v30, v31\n"
      "v_cvt_pk_bf16_f32 v200, v32, v33\n"
      "v_mfma_f32_16x16x32_bf16 v[58:61], a[4:7], v[72:75], v[58:61]\n"
      "ds_read_b128 a[4:7], v86 offset:58464\n"
      "v_cvt_pk_bf16_f32 v201, v34, v35\n"
      "v_mfma_f32_16x16x32_bf16 v[20:23], a[0:3], v[80:83], 0\n"
      "ds_read_b128 a[0:3], v84 offset:36992\n"
      "buffer_load_dwordx4 v89, s[28:31], s97 offen offset:2048 lds\n"
      "v_mfma_f32_16x16x32_bf16 v[24:27], a[8:11], v[68:71], v[20:23]\n"
      "s_add_u32 m0, s46, 0\n"
      "s_mul_i32 s97, s42, -39840\n"
      "v_cvt_pk_bf16_f32 v196, v54, v55\n"
      "v_cvt_pk_bf16_f32 v197, v56, v57\n"
      "v_cvt_pk_bf16_f32 v154, v58, v59\n"
      "v_cvt_pk_bf16_f32 v155, v60, v61\n"
      "v_mfma_f32_16x16x32_bf16 v[20:23], a[8:11], v[72:75], v[20:23]\n"
      "ds_read_b128 a[8:11], v84 offset:55392\n"
      "buffer_load_dwordx4 v18, s[32:35], s51 offen offset:2048 lds\n"
      "v_cvt_pk_bf16_f32 v150, v62, v63\n"
      "v_cvt_pk_bf16_f32 v151, v64, v65\n"
      "v_mfma_f32_16x16x32_bf16 v[28:31], a[12:15], v[76:79], 0\n"
      "s_waitcnt lgkmcnt(3)\n"
      "s_add_u32 m0, s97, 162816\n"
      "s_add_u32 s96, s96, 393216\n"
      "ds_read_b128 a[12:15], v85 offset:55392\n"
      "s_add_u32 s96, s96, s47\n"
      "s_mul_i32 s97, s42, 13280\n"
      "v_cvt_pk_bf16_f32 v158, v20, v21\n"
      "v_mfma_f32_16x16x32_bf16 v[32:35], a[16:19], v[68:71], v[28:31]\n"
      "v_cvt_pk_bf16_f32 v159, v22, v23\n"
      "v_cvt_pk_bf16_f32 v24, v24, v25\n"
      "v_mfma_f32_16x16x32_bf16 v[28:31], a[16:19], v[72:75], v[28:31]\n"
      "ds_read_b128 a[16:19], v86 offset:58528\n"
      "buffer_load_dwordx4 v19, s[32:35], s96 offen lds\n"
      "v_cvt_pk_bf16_f32 v26, v26, v27\n"
      "v_accvgpr_write_b32 a202, v24\n"
      "v_mfma_f32_16x16x32_bf16 v[54:57], a[20:23], v[76:79], 0\n"
      "s_mov_b32 m0, s18\n"
      "s_mul_i32 s96, s42, 196608\n"
      "v_accvgpr_write_b32 a203, v26\n"
      "v_cvt_pk_bf16_f32 v166, v28, v29\n"
      "v_mfma_f32_16x16x32_bf16 v[58:61], a[24:27], v[68:71], v[54:57]\n"
      "v_cvt_pk_bf16_f32 v167, v30, v31\n"
      "v_cvt_pk_bf16_f32 v162, v32, v33\n"
      "v_cvt_pk_bf16_f32 v163, v34, v35\n"
      "v_mfma_f32_16x16x32_bf16 v[54:57], a[24:27], v[72:75], v[54:57]\n"
      "v_add_u32 v86, 46144, v86\n"
      "v_add_u32 v85, 46144, v85\n"
      "v_mfma_f32_16x16x32_bf16 v[62:65], a[20:23], v[80:83], 0\n"
      "ds_read_b128 a[20:23], v84 offset:55456\n"
      "v_cvt_pk_bf16_f32 v58, v58, v59\n"
      "v_mfma_f32_16x16x32_bf16 v[20:23], a[28:31], v[68:71], v[62:65]\n"
      "v_cvt_pk_bf16_f32 v60, v60, v61\n"
      "v_accvgpr_write_b32 a210, v58\n"
      "v_mfma_f32_16x16x32_bf16 v[62:65], a[28:31], v[72:75], v[62:65]\n"
      "v_cvt_pk_bf16_f32 v170, v54, v55\n"
      "v_cvt_pk_bf16_f32 v171, v56, v57\n"
      "v_mfma_f32_16x16x32_bf16 v[24:27], a[32:35], v[76:79], 0\n"
      "s_waitcnt lgkmcnt(1)\n"
      "v_accvgpr_write_b32 a211, v60\n"
      "v_cvt_pk_bf16_f32 v174, v20, v21\n"
      "v_mfma_f32_16x16x32_bf16 v[28:31], a[0:3], v[68:71], v[24:27]\n"
      "v_cvt_pk_bf16_f32 v175, v22, v23\n"
      "v_cvt_pk_bf16_f32 v178, v62, v63\n"
      "v_mfma_f32_16x16x32_bf16 v[24:27], a[0:3], v[72:75], v[24:27]\n"
      "v_cvt_pk_bf16_f32 v179, v64, v65\n"
      "v_add_u32 v84, 46144, v84\n"
      "v_mfma_f32_16x16x32_bf16 v[32:35], a[4:7], v[76:79], 0\n"
      "v_add_u32 v87, 46144, v87\n"
      "v_add_u32 v88, 46144, v88\n"
      "v_mfma_f32_16x16x32_bf16 v[54:57], a[8:11], v[68:71], v[32:35]\n"
      "v_cvt_pk_bf16_f32 v182, v28, v29\n"
      "v_cvt_pk_bf16_f32 v183, v30, v31\n"
      "v_mfma_f32_16x16x32_bf16 v[32:35], a[8:11], v[72:75], v[32:35]\n"
      "s_waitcnt lgkmcnt(0)\n"
      "s_waitcnt vmcnt(14)\n"
      "s_cbranch_vccnz Lconv_h7\n"
      "s_barrier\n"
      "Lconv_h7:\n"
      "v_cvt_pk_bf16_f32 v24, v24, v25\n"
      "v_mfma_f32_16x16x32_bf16 v[58:61], a[4:7], v[80:83], 0\n"
      "v_cvt_pk_bf16_f32 v26, v26, v27\n"
      "v_accvgpr_write_b32 a230, v24\n"
      "v_mfma_f32_16x16x32_bf16 v[62:65], a[12:15], v[68:71], v[58:61]\n"
      "s_cbranch_vccz Lconv_h8\n"
      "s_barrier\n"
      "Lconv_h8:\n"
      "s_nop 0\n"
      "ds_read_b128 a[48:51], v2 offset:26560\n"
      "ds_read_b128 a[52:55], v3 offset:26560\n"
      "ds_read_b128 a[56:59], v4 offset:26560\n"
      "ds_read_b128 a[36:39], v2 offset:13280\n"
      "v_mfma_f32_16x16x32_bf16 v[58:61], a[12:15], v[72:75], v[58:61]\n"
      "ds_read_b128 a[40:43], v3 offset:13280\n"
      "ds_read_b128 a[44:47], v4 offset:13280\n"
      "v_accvgpr_write_b32 a231, v26\n"
      "v_cvt_pk_bf16_f32 v186, v54, v55\n"
      "v_mfma_f32_16x16x32_bf16 v[20:23], a[16:19], v[76:79], 0\n"
      "ds_read_b128 a[24:27], v86 offset:30752\n"
      "ds_read_b128 a[28:31], v84 offset:27680\n"
      "v_cvt_pk_bf16_f32 v187, v56, v57\n"
      "v_mfma_f32_16x16x32_bf16 v[24:27], a[20:23], v[68:71], v[20:23]\n"
      "ds_read_b128 a[32:35], v85 offset:27680\n"
      "ds_read_b128 a[0:3], v86 offset:30816\n"
      "v_cvt_pk_bf16_f32 v58, v58, v59\n"
      "v_accvgpr_write_b32 a238, v58\n"
      "v_mfma_f32_16x16x32_bf16 v[20:23], a[20:23], v[72:75], v[20:23]\n"
      "s_waitcnt lgkmcnt(9)\n"
      "ds_read_b128 a[4:7], v84 offset:27744\n"
      "ds_read_b128 a[8:11], v86 offset:39968\n"
      "v_cvt_pk_bf16_f32 v194, v62, v63\n"
      "v_cvt_pk_bf16_f32 v60, v60, v61\n"
      "v_mfma_f32_16x16x32_bf16 v[52:55], a[48:51], a[72:75], 0\n"
      "s_waitcnt lgkmcnt(10)\n"
      "ds_read_b128 a[48:51], v2 offset:13344\n"
      "ds_read_b128 a[12:15], v84 offset:36896\n"
      "v_accvgpr_write_b32 a239, v60\n"
      "v_mfma_f32_16x16x32_bf16 v[56:59], a[52:55], a[76:79], 0\n"
      "s_waitcnt lgkmcnt(10)\n"
      "ds_read_b128 a[52:55], v3 offset:13344\n"
      "ds_read_b128 a[16:19], v85 offset:36896\n"
      "v_cvt_pk_bf16_f32 v195, v64, v65\n"
      "v_cvt_pk_bf16_f32 v190, v32, v33\n"
      "v_mfma_f32_16x16x32_bf16 v[60:63], a[56:59], a[80:83], v[52:55]\n"
      "ds_read_b128 a[56:59], v4 offset:13344\n"
      "ds_read_b64_tr_b16 a[112:113], v87 offset:27680\n"
      "v_fma_f32 v64, s44, v52, v56\n"
      "v_fma_f32 v65, s44, v53, v57\n"
      "v_mfma_f32_16x16x32_bf16 v[36:39], a[36:39], a[72:75], 0\n"
      "s_waitcnt lgkmcnt(10)\n"
      "ds_read_b128 a[36:39], v2 offset:26624\n"
      "ds_read_b64_tr_b16 a[116:117], v88 offset:27680\n"
      "v_fma_f32 v66, s44, v54, v58\n"
      "v_fma_f32 v67, s44, v55, v59\n"
      "v_mfma_f32_16x16x32_bf16 v[40:43], a[40:43], a[76:79], 0\n"
      "ds_read_b128 a[40:43], v3 offset:26624\n"
      "ds_read_b64_tr_b16 a[120:121], v87 offset:27744\n"
      "v_fma_f32 v56, s44, v52, -v56\n"
      "v_fma_f32 v57, s44, v53, -v57\n"
      "v_mfma_f32_16x16x32_bf16 v[44:47], a[44:47], a[80:83], v[36:39]\n"
      "s_waitcnt lgkmcnt(3)\n"
      "ds_read_b128 a[44:47], v4 offset:26624\n"
      "ds_read_b64_tr_b16 a[148:149], v87 offset:36896\n"
      "v_fma_f32 v58, s44, v54, -v58\n"
      "v_fma_f32 v59, s44, v55, -v59\n"
      "v_fma_f32 v48, s44, v36, v40\n"
      "v_fma_f32 v49, s44, v37, v41\n"
      "v_fma_f32 v50, s44, v38, v42\n"
      "v_fma_f32 v51, s44, v39, v43\n"
      "v_fma_f32 v40, s44, v36, -v40\n"
      "v_fma_f32 v41, s44, v37, -v41\n"
      "v_fma_f32 v42, s44, v38, -v42\n"
      "v_fma_f32 v43, s44, v39, -v43\n"
      "v_mfma_f32_16x16x32_bf16 v[36:39], a[36:39], a[72:75], 0\n"
      "s_waitcnt lgkmcnt(3)\n"
      "ds_read_b128 a[36:39], v2 offset:13408\n"
      "ds_read_b64_tr_b16 a[152:153], v88 offset:36896\n"
      "v_cvt_pk_bf16_f32 v64, v64, v65\n"
      "v_cvt_pk_bf16_f32 v65, v66, v67\n"
      "v_cvt_pk_bf16_f32 v56, v56, v57\n"
      "v_cvt_pk_bf16_f32 v57, v58, v59\n"
      "v_cvt_pk_bf16_f32 v60, v60, v61\n"
      "v_cvt_pk_bf16_f32 v61, v62, v63\n"
      "ds_write_addtid_b32 v64 offset:5376\n"
      "ds_write_addtid_b32 v65 offset:42304\n"
      "ds_write_addtid_b32 v56 offset:6144\n"
      "ds_write_addtid_b32 v57 offset:43072\n"
      "ds_write_addtid_b32 v60 offset:4608\n"
      "ds_write_addtid_b32 v61 offset:41536\n"
      "v_cvt_pk_bf16_f32 v40, v40, v41\n"
      "v_cvt_pk_bf16_f32 v41, v42, v43\n"
      "s_mov_b32 m0, s18\n"
      "s_nop 0\n"
      "ds_write_addtid_b32 v40 offset:1536\n"
      "ds_write_addtid_b32 v41 offset:38464\n"
      "v_mfma_f32_16x16x32_bf16 v[40:43], a[40:43], a[76:79], 0\n"
      "s_waitcnt lgkmcnt(11)\n"
      "ds_read_b128 a[40:43], v3 offset:13408\n"
      "ds_read_b128 a[20:23], v86 offset:40032\n"
      "v_cvt_pk_bf16_f32 v44, v44, v45\n"
      "v_cvt_pk_bf16_f32 v45, v46, v47\n"
      "ds_write_addtid_b32 v44 offset:0\n"
      "ds_write_addtid_b32 v45 offset:36928\n"
      "v_mfma_f32_16x16x32_bf16 v[44:47], a[44:47], a[80:83], v[36:39]\n"
      "ds_read_b128 a[44:47], v4 offset:13408\n"
      "ds_read_b64_tr_b16 a[156:157], v87 offset:36960\n"
      "v_cvt_pk_bf16_f32 v48, v48, v49\n"
      "v_cvt_pk_bf16_f32 v49, v50, v51\n"
      "v_mfma_f32_16x16x32_bf16 v[52:55], a[48:51], a[72:75], 0\n"
      "ds_write_addtid_b32 v48 offset:768\n"
      "ds_write_addtid_b32 v49 offset:37696\n"
      "ds_read_b128 a[48:51], v2 offset:26688\n"
      "s_mov_b32 m0, s18\n"
      "v_cvt_pk_bf16_f32 v191, v34, v35\n"
      "v_mfma_f32_16x16x32_bf16 v[56:59], a[52:55], a[76:79], 0\n"
      "ds_read_b128 a[52:55], v3 offset:26688\n"
      "v_fma_f32 v48, s44, v36, v40\n"
      "v_fma_f32 v49, s44, v37, v41\n"
      "v_fma_f32 v50, s44, v38, v42\n"
      "v_mfma_f32_16x16x32_bf16 v[60:63], a[56:59], a[80:83], v[52:55]\n"
      "ds_read_b128 a[56:59], v4 offset:26688\n"
      "v_fma_f32 v51, s44, v39, v43\n"
      "v_fma_f32 v40, s44, v36, -v40\n"
      "v_fma_f32 v41, s44, v37, -v41\n"
      "v_mfma_f32_16x16x32_bf16 v[28:31], a[24:27], v[76:79], 0\n"
      "v_fma_f32 v42, s44, v38, -v42\n"
      "v_fma_f32 v43, s44, v39, -v43\n"
      "v_cvt_pk_bf16_f32 v48, v48, v49\n"
      "v_cvt_pk_bf16_f32 v49, v50, v51\n"
      "v_mfma_f32_16x16x32_bf16 v[32:35], a[28:31], v[68:71], v[28:31]\n"
      "ds_write_addtid_b32 v48 offset:5632\n"
      "ds_write_addtid_b32 v49 offset:42560\n"
      "v_cvt_pk_bf16_f32 v40, v40, v41\n"
      "v_cvt_pk_bf16_f32 v41, v42, v43\n"
      "v_mfma_f32_16x16x32_bf16 v[28:31], a[28:31], v[72:75], v[28:31]\n"
      "ds_write_addtid_b32 v40 offset:6400\n"
      "ds_write_addtid_b32 v41 offset:43328\n"
      "v_cvt_pk_bf16_f32 v44, v44, v45\n"
      "v_cvt_pk_bf16_f32 v45, v46, v47\n"
      "ds_write_addtid_b32 v44 offset:4864\n"
      "ds_write_addtid_b32 v45 offset:41792\n"
      "v_fma_f32 v64, s44, v52, v56\n"
      "v_fma_f32 v65, s44, v53, v57\n"
      "v_fma_f32 v66, s44, v54, v58\n"
      "v_fma_f32 v67, s44, v55, v59\n"
      "v_fma_f32 v56, s44, v52, -v56\n"
      "v_fma_f32 v57, s44, v53, -v57\n"
      "v_fma_f32 v58, s44, v54, -v58\n"
      "v_fma_f32 v59, s44, v55, -v59\n"
      "v_cvt_pk_bf16_f32 v56, v56, v57\n"
      "v_cvt_pk_bf16_f32 v57, v58, v59\n"
      "s_mov_b32 m0, s18\n"
      "s_nop 0\n"
      "ds_write_addtid_b32 v56 offset:1792\n"
      "ds_write_addtid_b32 v57 offset:38720\n"
      "v_mfma_f32_16x16x32_bf16 v[54:57], a[24:27], v[80:83], 0\n"
      "ds_read_b128 a[24:27], v84 offset:36960\n"
      "v_cvt_pk_bf16_f32 v60, v60, v61\n"
      "v_cvt_pk_bf16_f32 v61, v62, v63\n"
      "v_cvt_pk_bf16_f32 v64, v64, v65\n"
      "ds_write_addtid_b32 v60 offset:256\n"
      "ds_write_addtid_b32 v61 offset:37184\n"
      "v_mfma_f32_16x16x32_bf16 v[58:61], a[32:35], v[68:71], v[54:57]\n"
      "ds_write_addtid_b32 v64 offset:1024\n"
      "v_cvt_pk_bf16_f32 v65, v66, v67\n"
      "v_cvt_pk_bf16_f32 v202, v20, v21\n"
      "v_cvt_pk_bf16_f32 v203, v22, v23\n"
      "v_mfma_f32_16x16x32_bf16 v[54:57], a[32:35], v[72:75], v[54:57]\n"
      "ds_write_addtid_b32 v65 offset:37952\n"
      "s_mov_b32 m0, s18\n"
      "v_cvt_pk_bf16_f32 v198, v24, v25\n"
      "v_cvt_pk_bf16_f32 v208, v58, v59\n"
      "v_cvt_pk_bf16_f32 v209, v60, v61\n"
      "v_mfma_f32_16x16x32_bf16 v[62:65], a[0:3], v[76:79], 0\n"
      "v_cvt_pk_bf16_f32 v199, v26, v27\n"
      "v_cvt_pk_bf16_f32 v28, v28, v29\n"
      "v_mfma_f32_16x16x32_bf16 v[20:23], a[4:7], v[68:71], v[62:65]\n"
      "v_cvt_pk_bf16_f32 v212, v54, v55\n"
      "v_cvt_pk_bf16_f32 v213, v56, v57\n"
      "v_mfma_f32_16x16x32_bf16 v[62:65], a[4:7], v[72:75], v[62:65]\n"
      "s_waitcnt lgkmcnt(13)\n"
      "v_cvt_pk_bf16_f32 v30, v30, v31\n"
      "v_accvgpr_write_b32 a220, v28\n"
      "v_accvgpr_write_b32 a221, v30\n"
      "v_mfma_f32_16x16x32_bf16 v[52:55], a[48:51], a[72:75], 0\n"
      "v_cvt_pk_bf16_f32 v204, v32, v33\n"
      "v_cvt_pk_bf16_f32 v205, v34, v35\n"
      "v_mfma_f32_16x16x32_bf16 v[56:59], a[52:55], a[76:79], 0\n"
      "v_cvt_pk_bf16_f32 v62, v62, v63\n"
      "v_accvgpr_write_b32 a224, v62\n"
      "v_cvt_pk_bf16_f32 v64, v64, v65\n"
      "v_accvgpr_write_b32 a225, v64\n"
      "v_mfma_f32_16x16x32_bf16 v[60:63], a[56:59], a[80:83], v[52:55]\n"
      "v_cvt_pk_bf16_f32 v216, v20, v21\n"
      "v_cvt_pk_bf16_f32 v217, v22, v23\n"
      "v_mfma_f32_16x16x32_bf16 v[36:39], a[36:39], a[72:75], 0\n"
      "v_fma_f32 v64, s44, v52, v56\n"
      "v_fma_f32 v65, s44, v53, v57\n"
      "v_fma_f32 v66, s44, v54, v58\n"
      "v_fma_f32 v67, s44, v55, v59\n"
      "v_mfma_f32_16x16x32_bf16 v[40:43], a[40:43], a[76:79], 0\n"
      "v_fma_f32 v56, s44, v52, -v56\n"
      "v_fma_f32 v57, s44, v53, -v57\n"
      "v_fma_f32 v58, s44, v54, -v58\n"
      "v_fma_f32 v59, s44, v55, -v59\n"
      "v_mfma_f32_16x16x32_bf16 v[44:47], a[44:47], a[80:83], v[36:39]\n"
      "v_cvt_pk_bf16_f32 v64, v64, v65\n"
      "v_cvt_pk_bf16_f32 v65, v66, v67\n"
      "v_cvt_pk_bf16_f32 v56, v56, v57\n"
      "v_cvt_pk_bf16_f32 v57, v58, v59\n"
      "v_mfma_f32_16x16x32_bf16 v[24:27], a[8:11], v[76:79], 0\n"
      "ds_write_addtid_b32 v64 offset:5888\n"
      "ds_write_addtid_b32 v65 offset:42816\n"
      "ds_write_addtid_b32 v56 offset:6656\n"
      "ds_write_addtid_b32 v57 offset:43584\n"
      "v_mfma_f32_16x16x32_bf16 v[28:31], a[12:15], v[68:71], v[24:27]\n"
      "v_cvt_pk_bf16_f32 v60, v60, v61\n"
      "v_cvt_pk_bf16_f32 v61, v62, v63\n"
      "v_fma_f32 v48, s44, v36, v40\n"
      "v_fma_f32 v49, s44, v37, v41\n"
      "ds_write_addtid_b32 v60 offset:5120\n"
      "ds_write_addtid_b32 v61 offset:42048\n"
      "v_fma_f32 v50, s44, v38, v42\n"
      "v_fma_f32 v51, s44, v39, v43\n"
      "v_fma_f32 v40, s44, v36, -v40\n"
      "v_fma_f32 v41, s44, v37, -v41\n"
      "v_fma_f32 v42, s44, v38, -v42\n"
      "v_fma_f32 v43, s44, v39, -v43\n"
      "v_cvt_pk_bf16_f32 v48, v48, v49\n"
      "v_cvt_pk_bf16_f32 v49, v50, v51\n"
      "v_cvt_pk_bf16_f32 v40, v40, v41\n"
      "v_cvt_pk_bf16_f32 v41, v42, v43\n"
      "v_cvt_pk_bf16_f32 v44, v44, v45\n"
      "v_cvt_pk_bf16_f32 v45, v46, v47\n"
      "s_mov_b32 m0, s18\n"
      "s_nop 0\n"
      "ds_write_addtid_b32 v48 offset:1280\n"
      "ds_write_addtid_b32 v49 offset:38208\n"
      "ds_write_addtid_b32 v40 offset:2048\n"
      "ds_write_addtid_b32 v41 offset:38976\n"
      "ds_write_addtid_b32 v44 offset:512\n"
      "ds_write_addtid_b32 v45 offset:37440\n"
      "v_mfma_f32_16x16x32_bf16 v[24:27], a[12:15], v[72:75], v[24:27]\n"
      "Lconv_p2:\n"
      "s_cbranch_execz Lconv_r0\n"
      "s_nop 0\n"
      "s_waitcnt lgkmcnt(0)\n"
      "s_waitcnt vmcnt(2)\n"
      "s_cbranch_vccnz Lconv_h9\n"
      "s_barrier\n"
      "Lconv_h9:\n"
      "s_add_u32 m0, s46, 13280\n"
      "s_add_u32 s51, s47, 786432\n"
      "v_cvt_pk_bf16_f32 v220, v28, v29\n"
      "v_mfma_f32_16x16x32_bf16 v[32:35], a[8:11], v[80:83], 0\n"
      "v_cvt_pk_bf16_f32 v224, v24, v25\n"
      "v_cvt_pk_bf16_f32 v225, v26, v27\n"
      "v_mfma_f32_16x16x32_bf16 v[54:57], a[16:19], v[68:71], v[32:35]\n"
      "s_cbranch_vccz Lconv_h10\n"
      "s_barrier\n"
      "Lconv_h10:\n"
      "ds_read_b128 a[28:31], v86 offset:49184\n"
      "ds_read_b128 a[32:35], v84 offset:46112\n"
      "ds_read_b128 a[0:3], v85 offset:46112\n"
      "ds_read_b128 a[4:7], v86 offset:49248\n"
      "v_mfma_f32_16x16x32_bf16 v[32:35], a[16:19], v[72:75], v[32:35]\n"
      "ds_read_b128 a[8:11], v84 offset:46176\n"
      "ds_read_b128 a[12:15], v86 offset:58400\n"
      "v_mfma_f32_16x16x32_bf16 v[58:61], a[20:23], v[76:79], 0\n"
      "ds_read_b128 a[16:19], v84 offset:55328\n"
      "ds_read_b128 a[20:23], v85 offset:55328\n"
      "v_mfma_f32_16x16x32_bf16 v[62:65], a[24:27], v[68:71], v[58:61]\n"
      "ds_read_b64_tr_b16 a[184:185], v87 offset:46112\n"
      "ds_read_b64_tr_b16 a[188:189], v88 offset:46112\n"
      "v_cvt_pk_bf16_f32 v221, v30, v31\n"
      "v_mfma_f32_16x16x32_bf16 v[58:61], a[24:27], v[72:75], v[58:61]\n"
      "s_waitcnt lgkmcnt(9)\n"
      "ds_read_b128 a[24:27], v86 offset:58464\n"
      "ds_read_b64_tr_b16 a[192:193], v87 offset:46176\n"
      "buffer_load_dwordx4 v16, s[32:35], s51 offen lds\n"
      "v_cvt_pk_bf16_f32 v32, v32, v33\n"
      "v_mfma_f32_16x16x32_bf16 v[20:23], a[28:31], v[76:79], 0\n"
      "s_waitcnt lgkmcnt(10)\n"
      "s_add_u32 m0, s46, 13280\n"
      "ds_read_b64_tr_b16 a[114:115], v87 offset:55328\n"
      "ds_read_b64_tr_b16 a[118:119], v88 offset:55328\n"
      "v_cvt_pk_bf16_f32 v34, v34, v35\n"
      "v_mfma_f32_16x16x32_bf16 v[24:27], a[32:35], v[68:71], v[20:23]\n"
      "ds_read_b64_tr_b16 a[122:123], v87 offset:55392\n"
      "ds_read_b64_tr_b16 a[150:151], v87 offset:18464\n"
      "buffer_load_dwordx4 v17, s[32:35], s51 offen offset:1024 lds\n"
      "v_accvgpr_write_b32 a232, v32\n"
      "v_mfma_f32_16x16x32_bf16 v[20:23], a[32:35], v[72:75], v[20:23]\n"
      "s_add_u32 m0, s46, 13280\n"
      "ds_read_b128 a[32:35], v86 offset:21536\n"
      "ds_read_b64_tr_b16 a[154:155], v88 offset:18464\n"
      "v_accvgpr_write_b32 a233, v34\n"
      "v_cvt_pk_bf16_f32 v228, v54, v55\n"
      "v_mfma_f32_16x16x32_bf16 v[28:31], a[28:31], v[80:83], 0\n"
      "s_waitcnt lgkmcnt(10)\n"
      "s_nop 0\n"
      "ds_read_b128 a[28:31], v84 offset:55392\n"
      "ds_read_b64_tr_b16 a[158:159], v87 offset:18528\n"
      "v_cvt_pk_bf16_f32 v229, v56, v57\n"
      "v_cvt_pk_bf16_f32 v236, v58, v59\n"
      "v_mfma_f32_16x16x32_bf16 v[32:35], a[0:3], v[68:71], v[28:31]\n"
      "ds_read_b64_tr_b16 a[186:187], v87 offset:0\n"
      "ds_read_b64_tr_b16 a[190:191], v88 offset:0\n"
      "buffer_load_dwordx4 v18, s[32:35], s51 offen offset:2048 lds\n"
      "v_cvt_pk_bf16_f32 v237, v60, v61\n"
      "v_mfma_f32_16x16x32_bf16 v[28:31], a[0:3], v[72:75], v[28:31]\n"
      "s_add_u32 m0, s46, 26560\n"
      "s_add_u32 s51, s47, 983040\n"
      "ds_read_b128 a[0:3], v84 offset:18464\n"
      "ds_read_b64_tr_b16 a[194:195], v87 offset:64\n"
      "v_cvt_pk_bf16_f32 v232, v62, v63\n"
      "v_cvt_pk_bf16_f32 v233, v64, v65\n"
      "v_mfma_f32_16x16x32_bf16 v[54:57], a[4:7], v[76:79], 0\n"
      "ds_read_b128 a[4:7], v85 offset:18464\n"
      "v_cvt_pk_bf16_f32 v20, v20, v21\n"
      "v_cvt_pk_bf16_f32 v22, v22, v23\n"
      "v_mfma_f32_16x16x32_bf16 v[58:61], a[8:11], v[68:71], v[54:57]\n"
      "buffer_load_dwordx4 v16, s[32:35], s51 offen lds\n"
      "v_accvgpr_write_b32 a240, v20\n"
      "v_accvgpr_write_b32 a241, v22\n"
      "v_mfma_f32_16x16x32_bf16 v[54:57], a[8:11], v[72:75], v[54:57]\n"
      "s_add_u32 m0, s46, 26560\n"
      "ds_read_b128 a[8:11], v86 offset:21600\n"
      "v_cvt_pk_bf16_f32 v240, v24, v25\n"
      "v_cvt_pk_bf16_f32 v241, v26, v27\n"
      "v_cvt_pk_bf16_f32 v248, v28, v29\n"
      "v_mfma_f32_16x16x32_bf16 v[62:65], a[12:15], v[76:79], 0\n"
      "v_cvt_pk_bf16_f32 v249, v30, v31\n"
      "v_cvt_pk_bf16_f32 v244, v32, v33\n"
      "v_mfma_f32_16x16x32_bf16 v[20:23], a[16:19], v[68:71], v[62:65]\n"
      "buffer_load_dwordx4 v17, s[32:35], s51 offen offset:1024 lds\n"
      "v_cvt_pk_bf16_f32 v245, v34, v35\n"
      "v_mfma_f32_16x16x32_bf16 v[62:65], a[16:19], v[72:75], v[62:65]\n"
      "s_add_u32 m0, s46, 26560\n"
      "ds_read_b128 a[16:19], v86 offset:3072\n"
      "v_cvt_pk_bf16_f32 v54, v54, v55\n"
      "v_cvt_pk_bf16_f32 v56, v56, v57\n"
      "v_accvgpr_write_b32 a244, v54\n"
      "v_mfma_f32_16x16x32_bf16 v[24:27], a[12:15], v[80:83], 0\n"
      "ds_read_b128 a[12:15], v84 offset:18528\n"
      "v_accvgpr_write_b32 a245, v56\n"
      "v_mfma_f32_16x16x32_bf16 v[28:31], a[20:23], v[68:71], v[24:27]\n"
      "buffer_load_dwordx4 v18, s[32:35], s51 offen offset:2048 lds\n"
      "v_cvt_pk_bf16_f32 v252, v58, v59\n"
      "v_cvt_pk_bf16_f32 v253, v60, v61\n"
      "v_mfma_f32_16x16x32_bf16 v[24:27], a[20:23], v[72:75], v[24:27]\n"
      "s_waitcnt lgkmcnt(2)\n"
      "s_add_u32 m0, s97, 136256\n"
      "s_add_u32 s96, s96, 786432\n"
      "ds_read_b128 a[20:23], v84 offset:0\n"
      "s_add_u32 s96, s96, s47\n"
      "v_cvt_pk_bf16_f32 v62, v62, v63\n"
      "v_cvt_pk_bf16_f32 v64, v64, v65\n"
      "v_mfma_f32_16x16x32_bf16 v[32:35], a[24:27], v[76:79], 0\n"
      "ds_read_b128 a[24:27], v85 offset:0\n"
      "buffer_load_dwordx4 v19, s[32:35], s96 offen lds\n"
      "v_mfma_f32_16x16x32_bf16 v[54:57], a[28:31], v[68:71], v[32:35]\n"
      "v_accvgpr_write_b32 a222, v62\n"
      "v_accvgpr_write_b32 a223, v64\n"
      "v_mfma_f32_16x16x32_bf16 v[32:35], a[28:31], v[72:75], v[32:35]\n"
      "ds_read_b128 a[28:31], v86 offset:3136\n"
      "v_cvt_pk_bf16_f32 v206, v20, v21\n"
      "v_cvt_pk_bf16_f32 v207, v22, v23\n"
      "v_cvt_pk_bf16_f32 v214, v24, v25\n"
      "v_mfma_f32_16x16x32_bf16 v[58:61], a[32:35], v[76:79], 0\n"
      "v_cvt_pk_bf16_f32 v215, v26, v27\n"
      "v_cvt_pk_bf16_f32 v210, v28, v29\n"
      "v_mfma_f32_16x16x32_bf16 v[62:65], a[0:3], v[68:71], v[58:61]\n"
      "v_cvt_pk_bf16_f32 v211, v30, v31\n"
      "v_cvt_pk_bf16_f32 v32, v32, v33\n"
      "v_mfma_f32_16x16x32_bf16 v[58:61], a[0:3], v[72:75], v[58:61]\n"
      "v_cvt_pk_bf16_f32 v34, v34, v35\n"
      "v_accvgpr_write_b32 a226, v32\n"
      "v_mfma_f32_16x16x32_bf16 v[20:23], a[32:35], v[80:83], 0\n"
      "ds_read_b128 a[32:35], v84 offset:64\n"
      "v_accvgpr_write_b32 a227, v34\n"
      "v_mfma_f32_16x16x32_bf16 v[24:27], a[4:7], v[68:71], v[20:23]\n"
      "v_cvt_pk_bf16_f32 v218, v54, v55\n"
      "v_cvt_pk_bf16_f32 v219, v56, v57\n"
      "v_mfma_f32_16x16x32_bf16 v[20:23], a[4:7], v[72:75], v[20:23]\n"
      "v_cvt_pk_bf16_f32 v226, v58, v59\n"
      "v_cvt_pk_bf16_f32 v227, v60, v61\n"
      "v_mfma_f32_16x16x32_bf16 v[28:31], a[8:11], v[76:79], 0\n"
      "s_waitcnt lgkmcnt(1)\n"
      "v_cvt_pk_bf16_f32 v222, v62, v63\n"
      "v_cvt_pk_bf16_f32 v223, v64, v65\n"
      "v_mfma_f32_16x16x32_bf16 v[32:35], a[12:15], v[68:71], v[28:31]\n"
      "v_cvt_pk_bf16_f32 v230, v24, v25\n"
      "v_cvt_pk_bf16_f32 v20, v20, v21\n"
      "v_mfma_f32_16x16x32_bf16 v[28:31], a[12:15], v[72:75], v[28:31]\n"
      "ds_read_b128 a[12:15], v6 offset:0\n"
      "v_cvt_pk_bf16_f32 v22, v22, v23\n"
      "v_mfma_f32_16x16x32_bf16 v[54:57], a[16:19], v[76:79], 0\n"
      "v_accvgpr_write_b32 a234, v20\n"
      "v_accvgpr_write_b32 a235, v22\n"
      "v_mfma_f32_16x16x32_bf16 v[58:61], a[20:23], v[68:71], v[54:57]\n"
      "v_cvt_pk_bf16_f32 v231, v26, v27\n"
      "v_cvt_pk_bf16_f32 v234, v32, v33\n"
      "v_mfma_f32_16x16x32_bf16 v[54:57], a[20:23], v[72:75], v[54:57]\n"
      "s_waitcnt lgkmcnt(0)\n"
      "s_waitcnt vmcnt(7)\n"
      "s_cbranch_vccnz Lconv_h11\n"
      "s_barrier\n"
      "Lconv_h11:\n"
      "ds_read_b128 a[20:23], v6 offset:512\n"
      "v_cvt_pk_bf16_f32 v238, v28, v29\n"
      "v_mfma_f32_16x16x32_bf16 v[62:65], a[16:19], v[80:83], 0\n"
      "ds_read_b128 a[16:19], v6 offset:256\n"
      "v_cvt_pk_bf16_f32 v239, v30, v31\n"
      "v_mfma_f32_16x16x32_bf16 v[20:23], a[24:27], v[68:71], v[62:65]\n"
      "s_cbranch_vccz Lconv_h12\n"
      "s_barrier\n"
      "Lconv_h12:\n"
      "s_nop 0\n"
      "v_cvt_pk_bf16_f32 v235, v34, v35\n"
      "v_mfma_f32_16x16x32_bf16 v[62:65], a[24:27], v[72:75], v[62:65]\n"
      "ds_read_b128 a[24:27], v6 offset:4608\n"
      "v_cvt_pk_bf16_f32 v242, v58, v59\n"
      "v_cvt_pk_bf16_f32 v243, v60, v61\n"
      "v_mfma_f32_16x16x32_bf16 v[24:27], a[28:31], v[76:79], 0\n"
      "ds_read_b128 a[28:31], v6 offset:4864\n"
      "v_cvt_pk_bf16_f32 v54, v54, v55\n"
      "v_mfma_f32_16x16x32_bf16 v[28:31], a[32:35], v[68:71], v[24:27]\n"
      "v_cvt_pk_bf16_f32 v56, v56, v57\n"
      "v_accvgpr_write_b32 a242, v54\n"
      "v_mfma_f32_16x16x32_bf16 v[24:27], a[32:35], v[72:75], v[24:27]\n"
      "ds_read_b128 a[32:35], v6 offset:5120\n"
      "v_accvgpr_write_b32 a243, v56\n"
      "v_cvt_pk_bf16_f32 v246, v20, v21\n"
      "v_cvt_pk_bf16_f32 v247, v22, v23\n"
      "v_cvt_pk_bf16_f32 v250, v62, v63\n"
      "v_cvt_pk_bf16_f32 v251, v64, v65\n"
      "v_cvt_pk_bf16_f32 v254, v28, v29\n"
      "v_cvt_pk_bf16_f32 v255, v30, v31\n"
      "v_cvt_pk_bf16_f32 v24, v24, v25\n"
      "v_cvt_pk_bf16_f32 v26, v26, v27\n"
      "v_accvgpr_write_b32 a246, v24\n"
      "v_accvgpr_write_b32 a247, v26\n"
      "s_mov_b32 m0, s18\n"
      "ds_read_b128 a[48:51], v2 offset:39840\n"
      "v_lshlrev_b32 v9, 7, v5\n"
      "v_mfma_f32_16x16x32_bf16 v[68:71], a[12:15], a[88:91], 0\n"
      "s_waitcnt lgkmcnt(4)\n"
      "ds_read_b128 a[52:55], v3 offset:39840\n"
      "v_min_u32 v9, Lconv_u0-Lconv_t0, v9\n"
      "v_mfma_f32_16x16x32_bf16 v[68:71], a[16:19], a[100:103], v[68:71]\n"
      "s_mul_i32 s97, s41, Lconv_t1-Lconv_t0\n"
      "ds_read_b128 a[56:59], v4 offset:39840\n"
      "v_add_u32 v9, Lconv_t0-Lconv_start, v9\n"
      "v_mfma_f32_16x16x32_bf16 v[68:71], a[20:23], a[112:115], v[68:71]\n"
      "s_waitcnt lgkmcnt(5)\n"
      "ds_read_b128 a[36:39], v2 offset:39904\n"
      "v_add_u32 v9, s97, v9\n"
      "v_mfma_f32_16x16x32_bf16 v[68:71], a[24:27], a[124:127], v[68:71]\n"
      "s_waitcnt lgkmcnt(5)\n"
      "ds_read_b128 a[40:43], v3 offset:39904\n"
      "v_mfma_f32_16x16x32_bf16 v[68:71], a[28:31], a[136:139], v[68:71]\n"
      "s_waitcnt lgkmcnt(4)\n"
      "ds_read_b128 a[44:47], v4 offset:39904\n"
      "v_mfma_f32_16x16x32_bf16 v[52:55], a[48:51], a[72:75], 0\n"
      "ds_read_b128 a[0:3], v6 offset:768\n"
      "v_mfma_f32_16x16x32_bf16 v[68:71], a[32:35], a[148:151], v[68:71]\n"
      "ds_read_b128 a[48:51], v2 offset:39968\n"
      "v_mfma_f32_16x16x32_bf16 v[72:75], a[12:15], a[92:95], 0\n"
      "s_waitcnt lgkmcnt(6)\n"
      "ds_read_b128 a[4:7], v6 offset:1024\n"
      "v_mfma_f32_16x16x32_bf16 v[56:59], a[52:55], a[76:79], 0\n"
      "ds_read_b128 a[8:11], v6 offset:1280\n"
      "s_nop 0\n"
      "v_mfma_f32_16x16x32_bf16 v[72:75], a[16:19], a[104:107], v[72:75]\n"
      "ds_read_b128 a[52:55], v3 offset:39968\n"
      "s_nop 0\n"
      "v_mfma_f32_16x16x32_bf16 v[72:75], a[20:23], a[116:119], v[72:75]\n"
      "s_waitcnt lgkmcnt(7)\n"
      "ds_read_b128 a[60:63], v6 offset:1536\n"
      "v_fma_f32 v64, s44, v52, v56\n"
      "v_mfma_f32_16x16x32_bf16 v[60:63], a[56:59], a[80:83], v[52:55]\n"
      "ds_read_b128 a[56:59], v4 offset:39968\n"
      "v_fma_f32 v65, s44, v53, v57\n"
      "v_mfma_f32_16x16x32_bf16 v[72:75], a[24:27], a[128:131], v[72:75]\n"
      "v_cvt_pk_bf16_f32 v64, v64, v65\n"
      "v_fma_f32 v66, s44, v54, v58\n"
      "v_mfma_f32_16x16x32_bf16 v[72:75], a[28:31], a[140:143], v[72:75]\n"
      "ds_write_addtid_b32 v64 offset:9984\n"
      "v_fma_f32 v67, s44, v55, v59\n"
      "v_mfma_f32_16x16x32_bf16 v[72:75], a[32:35], a[152:155], v[72:75]\n"
      "v_cvt_pk_bf16_f32 v65, v66, v67\n"
      "v_fma_f32 v56, s44, v52, -v56\n"
      "v_mfma_f32_16x16x32_bf16 v[36:39], a[36:39], a[72:75], 0\n"
      "ds_write_addtid_b32 v65 offset:46912\n"
      "v_fma_f32 v57, s44, v53, -v57\n"
      "v_mfma_f32_16x16x32_bf16 v[76:79], a[12:15], a[96:99], 0\n"
      "ds_read_b128 a[12:15], v6 offset:5376\n"
      "v_cvt_pk_bf16_f32 v56, v56, v57\n"
      "v_mfma_f32_16x16x32_bf16 v[76:79], a[16:19], a[108:111], v[76:79]\n"
      "s_waitcnt lgkmcnt(5)\n"
      "ds_read_b128 a[16:19], v6 offset:5632\n"
      "v_fma_f32 v58, s44, v54, -v58\n"
      "v_mfma_f32_16x16x32_bf16 v[40:43], a[40:43], a[76:79], 0\n"
      "ds_write_addtid_b32 v56 offset:10752\n"
      "v_fma_f32 v59, s44, v55, -v59\n"
      "v_mfma_f32_16x16x32_bf16 v[76:79], a[20:23], a[120:123], v[76:79]\n"
      "ds_read_b128 a[20:23], v6 offset:5888\n"
      "v_cvt_pk_bf16_f32 v57, v58, v59\n"
      "v_mfma_f32_16x16x32_bf16 v[76:79], a[24:27], a[132:135], v[76:79]\n"
      "ds_write_addtid_b32 v57 offset:47680\n"
      "v_cvt_pk_bf16_f32 v60, v60, v61\n"
      "v_mfma_f32_16x16x32_bf16 v[44:47], a[44:47], a[80:83], v[36:39]\n"
      "ds_write_addtid_b32 v60 offset:9216\n"
      "v_cvt_pk_bf16_f32 v61, v62, v63\n"
      "v_mfma_f32_16x16x32_bf16 v[76:79], a[28:31], a[144:147], v[76:79]\n"
      "ds_write_addtid_b32 v61 offset:46144\n"
      "v_fma_f32 v48, s44, v36, v40\n"
      "v_mfma_f32_16x16x32_bf16 v[76:79], a[32:35], a[156:159], v[76:79]\n"
      "ds_read_b128 a[36:39], v2 offset:0\n"
      "v_fma_f32 v49, s44, v37, v41\n"
      "v_mfma_f32_16x16x32_bf16 v[80:83], a[0:3], v[92:95], 0\n"
      "v_cvt_pk_bf16_f32 v48, v48, v49\n"
      "v_fma_f32 v50, s44, v38, v42\n"
      "v_mfma_f32_16x16x32_bf16 v[52:55], a[48:51], a[72:75], 0\n"
      "ds_write_addtid_b32 v48 offset:10240\n"
      "v_fma_f32 v51, s44, v39, v43\n"
      "v_mfma_f32_16x16x32_bf16 v[80:83], a[4:7], v[148:151], v[80:83]\n"
      "ds_read_b128 a[40:43], v3 offset:0\n"
      "v_cvt_pk_bf16_f32 v49, v50, v51\n"
      "v_mfma_f32_16x16x32_bf16 v[80:83], a[8:11], v[204:207], v[80:83]\n"
      "ds_write_addtid_b32 v49 offset:47168\n"
      "v_fma_f32 v40, s44, v36, -v40\n"
      "v_mfma_f32_16x16x32_bf16 v[56:59], a[52:55], a[76:79], 0\n"
      "s_waitcnt lgkmcnt(7)\n"
      "v_fma_f32 v41, s44, v37, -v41\n"
      "v_cvt_pk_bf16_f32 v40, v40, v41\n"
      "v_mfma_f32_16x16x32_bf16 v[80:83], a[12:15], v[112:115], v[80:83]\n"
      "ds_write_addtid_b32 v40 offset:11008\n"
      "v_fma_f32 v42, s44, v38, -v42\n"
      "v_mfma_f32_16x16x32_bf16 v[80:83], a[16:19], a[208:211], v[80:83]\n"
      "v_fma_f32 v43, s44, v39, -v43\n"
      "v_cvt_pk_bf16_f32 v41, v42, v43\n"
      "v_mfma_f32_16x16x32_bf16 v[60:63], a[56:59], a[80:83], v[52:55]\n"
      "ds_write_addtid_b32 v41 offset:47936\n"
      "v_cvt_pk_bf16_f32 v44, v44, v45\n"
      "v_mfma_f32_16x16x32_bf16 v[80:83], a[20:23], v[220:223], v[80:83]\n"
      "ds_write_addtid_b32 v44 offset:9472\n"
      "v_cvt_pk_bf16_f32 v45, v46, v47\n"
      "v_mfma_f32_16x16x32_bf16 v[84:87], a[0:3], v[100:103], 0\n"
      "ds_read_b128 a[44:47], v4 offset:0\n"
      "v_fma_f32 v64, s44, v52, v56\n"
      "v_mfma_f32_16x16x32_bf16 v[84:87], a[4:7], a[200:203], v[84:87]\n"
      "ds_write_addtid_b32 v45 offset:46400\n"
      "v_fma_f32 v65, s44, v53, v57\n"
      "v_mfma_f32_16x16x32_bf16 v[84:87], a[8:11], v[208:211], v[84:87]\n"
      "ds_read_b128 a[64:67], v6 offset:1792\n"
      "v_cvt_pk_bf16_f32 v64, v64, v65\n"
      "v_mfma_f32_16x16x32_bf16 v[84:87], a[12:15], a[204:207], v[84:87]\n"
      "ds_read_b128 a[68:71], v6 offset:2048\n"
      "v_fma_f32 v66, s44, v54, v58\n"
      "v_mfma_f32_16x16x32_bf16 v[84:87], a[16:19], v[172:175], v[84:87]\n"
      "ds_read_b128 a[24:27], v6 offset:6144\n"
      "v_fma_f32 v67, s44, v55, v59\n"
      "v_mfma_f32_16x16x32_bf16 v[84:87], a[20:23], v[228:231], v[84:87]\n"
      "ds_read_b128 a[48:51], v2 offset:64\n"
      "v_cvt_pk_bf16_f32 v65, v66, v67\n"
      "v_mfma_f32_16x16x32_bf16 v[24:27], a[0:3], a[196:199], 0\n"
      "ds_write_addtid_b32 v64 offset:10496\n"
      "v_fma_f32 v56, s44, v52, -v56\n"
      "v_mfma_f32_16x16x32_bf16 v[24:27], a[4:7], v[160:163], v[24:27]\n"
      "s_waitcnt lgkmcnt(11)\n"
      "ds_read_b128 a[28:31], v6 offset:6400\n"
      "v_fma_f32 v57, s44, v53, -v57\n"
      "v_mfma_f32_16x16x32_bf16 v[36:39], a[36:39], a[72:75], 0\n"
      "ds_write_addtid_b32 v65 offset:47424\n"
      "v_cvt_pk_bf16_f32 v56, v56, v57\n"
      "v_mfma_f32_16x16x32_bf16 v[24:27], a[8:11], v[216:219], v[24:27]\n"
      "ds_read_b128 a[32:35], v6 offset:6656\n"
      "v_fma_f32 v58, s44, v54, -v58\n"
      "v_mfma_f32_16x16x32_bf16 v[24:27], a[12:15], v[124:127], v[24:27]\n"
      "ds_write_addtid_b32 v56 offset:11264\n"
      "v_fma_f32 v59, s44, v55, -v59\n"
      "v_mfma_f32_16x16x32_bf16 v[40:43], a[40:43], a[76:79], 0\n"
      "ds_read_b128 a[52:55], v3 offset:64\n"
      "v_cvt_pk_bf16_f32 v57, v58, v59\n"
      "v_mfma_f32_16x16x32_bf16 v[24:27], a[16:19], v[180:183], v[24:27]\n"
      "ds_write_addtid_b32 v57 offset:48192\n"
      "v_cvt_pk_bf16_f32 v60, v60, v61\n"
      "v_mfma_f32_16x16x32_bf16 v[24:27], a[20:23], v[232:235], v[24:27]\n"
      "s_waitcnt lgkmcnt(7)\n"
      "ds_write_addtid_b32 v60 offset:9728\n"
      "v_cvt_pk_bf16_f32 v61, v62, v63\n"
      "v_mfma_f32_16x16x32_bf16 v[44:47], a[44:47], a[80:83], v[36:39]\n"
      "ds_write_addtid_b32 v61 offset:46656\n"
      "v_fma_f32 v48, s44, v36, v40\n"
      "v_mfma_f32_16x16x32_bf16 v[28:31], a[60:63], v[96:99], 0\n"
      "ds_read_b128 a[56:59], v4 offset:64\n"
      "v_fma_f32 v49, s44, v37, v41\n"
      "v_mfma_f32_16x16x32_bf16 v[28:31], a[64:67], v[152:155], v[28:31]\n"
      "ds_read_b128 a[36:39], v2 offset:128\n"
      "v_cvt_pk_bf16_f32 v48, v48, v49\n"
      "v_mfma_f32_16x16x32_bf16 v[28:31], a[68:71], a[220:223], v[28:31]\n"
      "ds_read_b128 a[40:43], v3 offset:128\n"
      "v_fma_f32 v50, s44, v38, v42\n"
      "v_mfma_f32_16x16x32_bf16 v[28:31], a[24:27], v[116:119], v[28:31]\n"
      "s_waitcnt lgkmcnt(0)\n"
      "s_cbranch_vccnz Lconv_h13\n"
      "s_barrier\n"
      "Lconv_h13:\n"
      "v_fma_f32 v51, s44, v39, v43\n"
      "v_mfma_f32_16x16x32_bf16 v[52:55], a[48:51], a[72:75], 0\n"
      "ds_read_b128 a[44:47], v4 offset:128\n"
      "v_cvt_pk_bf16_f32 v49, v50, v51\n"
      "v_mfma_f32_16x16x32_bf16 v[28:31], a[28:31], v[168:171], v[28:31]\n"
      "ds_write_addtid_b32 v48 offset:14592\n"
      "v_fma_f32 v40, s44, v36, -v40\n"
      "v_mfma_f32_16x16x32_bf16 v[28:31], a[32:35], v[224:227], v[28:31]\n"
      "ds_write_addtid_b32 v49 offset:51520\n"
      "v_fma_f32 v41, s44, v37, -v41\n"
      "v_mfma_f32_16x16x32_bf16 v[56:59], a[52:55], a[76:79], 0\n"
      "s_cbranch_vccz Lconv_h14\n"
      "s_barrier\n"
      "Lconv_h14:\n"
      "v_cvt_pk_bf16_f32 v40, v40, v41\n"
      "v_mfma_f32_16x16x32_bf16 v[20:23], a[60:63], v[104:107], 0\n"
      "s_mov_b64 vcc, 0x2000000\n"
      "ds_write_addtid_b32 v40 offset:15360\n"
      "v_fma_f32 v42, s44, v38, -v42\n"
      "v_mfma_f32_16x16x32_bf16 v[20:23], a[64:67], v[156:159], v[20:23]\n"
      "ds_read_b128 a[0:3], v7 offset:0\n"
      "v_fma_f32 v43, s44, v39, -v43\n"
      "v_mfma_f32_16x16x32_bf16 v[60:63], a[56:59], a[80:83], v[52:55]\n"
      "v_cvt_pk_bf16_f32 v41, v42, v43\n"
      "v_cvt_pk_bf16_f32 v44, v44, v45\n"
      "v_mfma_f32_16x16x32_bf16 v[20:23], a[68:71], v[212:215], v[20:23]\n"
      "ds_write_addtid_b32 v41 offset:52288\n"
      "v_cvt_pk_bf16_f32 v45, v46, v47\n"
      "v_mfma_f32_16x16x32_bf16 v[20:23], a[24:27], v[120:123], v[20:23]\n"
      "ds_write_addtid_b32 v44 offset:13824\n"
      "ds_write_addtid_b32 v45 offset:50752\n"
      "v_mfma_f32_16x16x32_bf16 v[20:23], a[28:31], v[176:179], v[20:23]\n"
      "ds_read_b128 a[4:7], v7 offset:256\n"
      "v_fma_f32 v64, s44, v52, v56\n"
      "v_mfma_f32_16x16x32_bf16 v[36:39], a[36:39], a[72:75], 0\n"
      "ds_read_b128 a[8:11], v7 offset:512\n"
      "v_fma_f32 v65, s44, v53, v57\n"
      "v_mfma_f32_16x16x32_bf16 v[20:23], a[32:35], a[232:235], v[20:23]\n"
      "v_fma_f32 v66, s44, v54, v58\n"
      "v_fma_f32 v67, s44, v55, v59\n"
      "v_mfma_f32_16x16x32_bf16 v[32:35], a[60:63], v[108:111], 0\n"
      "v_fma_f32 v56, s44, v52, -v56\n"
      "v_fma_f32 v57, s44, v53, -v57\n"
      "v_mfma_f32_16x16x32_bf16 v[40:43], a[40:43], a[76:79], 0\n"
      "v_fma_f32 v58, s44, v54, -v58\n"
      "v_fma_f32 v59, s44, v55, -v59\n"
      "v_mfma_f32_16x16x32_bf16 v[32:35], a[64:67], v[164:167], v[32:35]\n"
      "v_cvt_pk_bf16_f32 v64, v64, v65\n"
      "v_cvt_pk_bf16_f32 v65, v66, v67\n"
      "v_mfma_f32_16x16x32_bf16 v[32:35], a[68:71], a[224:227], v[32:35]\n"
      "s_waitcnt lgkmcnt(5)\n"
      "v_cvt_pk_bf16_f32 v56, v56, v57\n"
      "v_cvt_pk_bf16_f32 v57, v58, v59\n"
      "v_mfma_f32_16x16x32_bf16 v[44:47], a[44:47], a[80:83], v[36:39]\n"
      "ds_write_addtid_b32 v64 offset:14848\n"
      "v_cvt_pk_bf16_f32 v60, v60, v61\n"
      "v_mfma_f32_16x16x32_bf16 v[32:35], a[24:27], v[128:131], v[32:35]\n"
      "ds_write_addtid_b32 v65 offset:51776\n"
      "v_cvt_pk_bf16_f32 v61, v62, v63\n"
      "v_mfma_f32_16x16x32_bf16 v[32:35], a[28:31], a[228:231], v[32:35]\n"
      "ds_write_addtid_b32 v56 offset:15616\n"
      "ds_write_addtid_b32 v57 offset:52544\n"
      "v_mfma_f32_16x16x32_bf16 v[32:35], a[32:35], v[236:239], v[32:35]\n"
      "ds_write_addtid_b32 v60 offset:14080\n"
      "ds_write_addtid_b32 v61 offset:51008\n"
      "v_mfma_f32_16x16x32_bf16 v[68:71], a[0:3], a[160:163], v[68:71]\n"
      "s_waitcnt lgkmcnt(6)\n"
      "ds_read_b128 a[12:15], v7 offset:768\n"
      "v_fma_f32 v48, s44, v36, v40\n"
      "v_mfma_f32_16x16x32_bf16 v[68:71], a[4:7], a[172:175], v[68:71]\n"
      "s_add_u32 m0, s46, 39840\n"
      "ds_read_b128 a[16:19], v7 offset:1024\n"
      "v_fma_f32 v49, s44, v37, v41\n"
      "v_mfma_f32_16x16x32_bf16 v[68:71], a[8:11], a[184:187], v[68:71]\n"
      "s_add_u32 s51, s47, 1179648\n"
      "ds_read_b128 a[20:23], v7 offset:1280\n"
      "v_cvt_pk_bf16_f32 v48, v48, v49\n"
      "v_mfma_f32_16x16x32_bf16 v[72:75], a[0:3], a[164:167], v[72:75]\n"
      "buffer_load_dwordx4 v16, s[32:35], s51 offen lds\n"
      "v_fma_f32 v50, s44, v38, v42\n"
      "v_mfma_f32_16x16x32_bf16 v[72:75], a[4:7], a[176:179], v[72:75]\n"
      "s_add_u32 s48, s47, 3342336\n"
      "v_fma_f32 v51, s44, v39, v43\n"
      "v_cvt_pk_bf16_f32 v49, v50, v51\n"
      "v_mfma_f32_16x16x32_bf16 v[72:75], a[8:11], a[188:191], v[72:75]\n"
      "s_add_u32 s47, s47, 3145728\n"
      "v_fma_f32 v40, s44, v36, -v40\n"
      "v_fma_f32 v41, s44, v37, -v41\n"
      "v_mfma_f32_16x16x32_bf16 v[76:79], a[0:3], a[168:171], v[76:79]\n"
      "buffer_load_dwordx4 v17, s[32:35], s51 offen offset:1024 lds\n"
      "v_cvt_pk_bf16_f32 v40, v40, v41\n"
      "v_mfma_f32_16x16x32_bf16 v[76:79], a[4:7], a[180:183], v[76:79]\n"
      "ds_read_b128 a[0:3], v7 offset:1536\n"
      "v_fma_f32 v42, s44, v38, -v42\n"
      "v_mfma_f32_16x16x32_bf16 v[76:79], a[8:11], a[192:195], v[76:79]\n"
      "s_waitcnt lgkmcnt(3)\n"
      "ds_read_b128 a[4:7], v7 offset:1792\n"
      "v_fma_f32 v43, s44, v39, -v43\n"
      "v_mfma_f32_16x16x32_bf16 v[80:83], a[12:15], a[212:215], v[80:83]\n"
      "s_waitcnt lgkmcnt(3)\n"
      "ds_read_b128 a[8:11], v7 offset:2048\n"
      "v_cvt_pk_bf16_f32 v41, v42, v43\n"
      "v_mfma_f32_16x16x32_bf16 v[80:83], a[16:19], v[184:187], v[80:83]\n"
      "s_waitcnt lgkmcnt(3)\n"
      "v_cvt_pk_bf16_f32 v44, v44, v45\n"
      "v_cvt_pk_bf16_f32 v45, v46, v47\n"
      "v_mfma_f32_16x16x32_bf16 v[80:83], a[20:23], v[240:243], v[80:83]\n"
      "buffer_load_dwordx4 v18, s[32:35], s51 offen offset:2048 lds\n"
      "v_cvt_pk_bf16_f32 v68, v68, v69\n"
      "v_mfma_f32_16x16x32_bf16 v[84:87], a[12:15], v[136:139], v[84:87]\n"
      "s_mov_b32 m0, s18\n"
      "v_cvt_pk_bf16_f32 v69, v70, v71\n"
      "v_cvt_pk_bf16_f32 v72, v72, v73\n"
      "v_mfma_f32_16x16x32_bf16 v[84:87], a[16:19], v[192:195], v[84:87]\n"
      "ds_write_addtid_b32 v48 offset:15104\n"
      "ds_write_addtid_b32 v49 offset:52032\n"
      "v_mfma_f32_16x16x32_bf16 v[84:87], a[20:23], v[244:247], v[84:87]\n"
      "ds_write_addtid_b32 v40 offset:15872\n"
      "ds_write_addtid_b32 v41 offset:52800\n"
      "v_mfma_f32_16x16x32_bf16 v[24:27], a[12:15], a[216:219], v[24:27]\n"
      "ds_write_addtid_b32 v44 offset:14336\n"
      "ds_write_addtid_b32 v45 offset:51264\n"
      "v_mfma_f32_16x16x32_bf16 v[24:27], a[16:19], v[196:199], v[24:27]\n"
      "s_mov_b32 m0, s45\n"
      "v_cvt_pk_bf16_f32 v73, v74, v75\n"
      "v_cvt_pk_bf16_f32 v76, v76, v77\n"
      "v_mfma_f32_16x16x32_bf16 v[24:27], a[20:23], v[252:255], v[24:27]\n"
      "s_waitcnt lgkmcnt(6)\n"
      "ds_write_addtid_b32 v68 offset:57360\n"
      "ds_write_addtid_b32 v69 offset:57616\n"
      "v_mfma_f32_16x16x32_bf16 v[28:31], a[0:3], v[132:135], v[28:31]\n"
      "ds_write_addtid_b32 v72 offset:45064\n"
      "ds_write_addtid_b32 v73 offset:45320\n"
      "v_mfma_f32_16x16x32_bf16 v[28:31], a[4:7], v[188:191], v[28:31]\n"
      "ds_write_addtid_b32 v76 offset:32768\n"
      "v_cvt_pk_bf16_f32 v77, v78, v79\n"
      "v_mfma_f32_16x16x32_bf16 v[28:31], a[8:11], a[240:243], v[28:31]\n"
      "ds_write_addtid_b32 v77 offset:33024\n"
      "v_cvt_pk_bf16_f32 v80, v80, v81\n"
      "v_mfma_f32_16x16x32_bf16 v[20:23], a[0:3], v[140:143], v[20:23]\n"
      "ds_write_addtid_b32 v80 offset:58384\n"
      "v_cvt_pk_bf16_f32 v81, v82, v83\n"
      "v_mfma_f32_16x16x32_bf16 v[20:23], a[4:7], a[236:239], v[20:23]\n"
      "ds_write_addtid_b32 v81 offset:58640\n"
      "v_cvt_pk_bf16_f32 v84, v84, v85\n"
      "v_mfma_f32_16x16x32_bf16 v[20:23], a[8:11], v[248:251], v[20:23]\n"
      "ds_write_addtid_b32 v84 offset:46088\n"
      "v_cvt_pk_bf16_f32 v85, v86, v87\n"
      "v_mfma_f32_16x16x32_bf16 v[32:35], a[0:3], v[144:147], v[32:35]\n"
      "ds_write_addtid_b32 v85 offset:46344\n"
      "v_cvt_pk_bf16_f32 v24, v24, v25\n"
      "v_mfma_f32_16x16x32_bf16 v[32:35], a[4:7], v[200:203], v[32:35]\n"
      "ds_write_addtid_b32 v24 offset:33792\n"
      "v_cvt_pk_bf16_f32 v25, v26, v27\n"
      "v_mfma_f32_16x16x32_bf16 v[32:35], a[8:11], a[244:247], v[32:35]\n"
      "ds_write_addtid_b32 v25 offset:34048\n"
      "v_cvt_pk_bf16_f32 v28, v28, v29\n"
      "v_cvt_pk_bf16_f32 v29, v30, v31\n"
      "ds_write_addtid_b32 v28 offset:59408\n"
      "ds_write_addtid_b32 v29 offset:59664\n"
      "v_cvt_pk_bf16_f32 v20, v20, v21\n"
      "v_cvt_pk_bf16_f32 v21, v22, v23\n"
      "ds_write_addtid_b32 v20 offset:47112\n"
      "ds_write_addtid_b32 v21 offset:47368\n"
      "s_waitcnt vmcnt(3)\n"
      "s_waitcnt lgkmcnt(0)\n"
      "s_cmp_eq_u32 s41, 1\n"
      "s_cbranch_scc1 Lconv_s1\n"
      "Lconv_s0:\n"
      "s_branch Lconv_loop0\n"
      "Lconv_s1:\n"
      "s_branch Lconv_loop1\n"
      ".p2align 12\n"
      "Lconv_pa0:\n"
      "s_cbranch_execz Lconv_r0\n"
      "s_nop 0\n"
      "Lconv_loop0:\n"
      "v_mfma_f32_16x16x32_bf16 v[20:23], a[24:27], v[96:99], 0\n"
      "s_add_u32 s51, s51, 196608\n"
      "s_nop 1\n"
      "v_mfma_f32_16x16x32_bf16 v[20:23], a[28:31], v[152:155], v[20:23]\n"
      "s_sub_u32 s58, s93, s58\n"
      "v_cvt_pk_bf16_f32 v32, v32, v33\n"
      "v_cvt_pk_bf16_f32 v33, v34, v35\n"
      "v_mfma_f32_16x16x32_bf16 v[20:23], a[32:35], a[220:223], v[20:23]\n"
      "s_sub_u32 s59, s94, s59\n"
      "ds_write_addtid_b32 v32 offset:34816\n"
      "v_mfma_f32_16x16x32_bf16 v[20:23], a[0:3], v[116:119], v[20:23]\n"
      "s_sub_u32 s47, s51, 3342336\n"
      "ds_write_addtid_b32 v33 offset:35072\n"
      "v_mfma_f32_16x16x32_bf16 v[20:23], a[4:7], v[168:171], v[20:23]\n"
      "s_cmp_gt_u32 s51, s48\n"
      "v_mfma_f32_16x16x32_bf16 v[20:23], a[8:11], v[224:227], v[20:23]\n"
      "s_cselect_b32 s53, s47, s51\n"
      "v_mfma_f32_16x16x32_bf16 v[24:27], a[24:27], v[104:107], 0\n"
      "s_waitcnt vmcnt(8) lgkmcnt(0)\n"
      "s_barrier\n"
      "v_mfma_f32_16x16x32_bf16 v[24:27], a[28:31], v[156:159], v[24:27]\n"
      "s_mov_b32 m0, s58\n"
      "ds_read_b128 a[12:15], v7 offset:6144\n"
      "v_mfma_f32_16x16x32_bf16 v[24:27], a[32:35], v[212:215], v[24:27]\n"
      "ds_read_b128 a[16:19], v7 offset:6400\n"
      "v_mfma_f32_16x16x32_bf16 v[24:27], a[0:3], v[120:123], v[24:27]\n"
      "ds_read_b128 a[20:23], v7 offset:6656\n"
      "v_mfma_f32_16x16x32_bf16 v[24:27], a[4:7], v[176:179], v[24:27]\n"
      "ds_read_b128 a[48:51], v2 offset:13280\n"
      "v_mfma_f32_16x16x32_bf16 v[24:27], a[8:11], a[232:235], v[24:27]\n"
      "ds_read_b128 a[52:55], v3 offset:13280\n"
      "v_mfma_f32_16x16x32_bf16 v[68:71], a[84:87], a[60:63], 0\n"
      "ds_read_b128 a[56:59], v4 offset:13280\n"
      "s_nop 0\n"
      "v_mfma_f32_16x16x32_bf16 v[28:31], a[24:27], v[108:111], 0\n"
      "ds_read2_b64 a[64:67], v10 offset0:0 offset1:64\n"
      "s_nop 0\n"
      "v_mfma_f32_16x16x32_bf16 v[28:31], a[28:31], v[164:167], v[28:31]\n"
      "buffer_load_dwordx4 v16, s[32:35], s53 offen lds\n"
      "v_mfma_f32_16x16x32_bf16 v[28:31], a[32:35], a[224:227], v[28:31]\n"
      "s_mov_b32 m0, s45\n"
      "ds_read2_b64 a[68:71], v10 offset0:1 offset1:65\n"
      "v_cvt_pk_bf16_f32 v85, v68, v69\n"
      "v_mfma_f32_16x16x32_bf16 v[28:31], a[0:3], v[128:131], v[28:31]\n"
      "ds_read_b128 a[24:27], v6 offset:5376\n"
      "v_cvt_pk_bf16_f32 v91, v70, v71\n"
      "v_mfma_f32_16x16x32_bf16 v[28:31], a[4:7], a[228:231], v[28:31]\n"
      "ds_read_b128 a[28:31], v6 offset:5632\n"
      "v_mfma_f32_16x16x32_bf16 v[28:31], a[8:11], v[236:239], v[28:31]\n"
      "s_waitcnt lgkmcnt(6)\n"
      "ds_read_b128 a[32:35], v6 offset:5888\n"
      "v_mfma_f32_16x16x32_bf16 v[20:23], a[12:15], v[132:135], v[20:23]\n"
      "buffer_store_dwordx2 v[84:85], v15, s[36:39], s52 offen sc1\n"
      "v_mfma_f32_16x16x32_bf16 v[20:23], a[16:19], v[188:191], v[20:23]\n"
      "ds_read_b128 a[0:3], v7 offset:768\n"
      "v_mfma_f32_16x16x32_bf16 v[20:23], a[20:23], a[240:243], v[20:23]\n"
      "ds_read_b128 a[4:7], v7 offset:1024\n"
      "s_nop 0\n"
      "v_mfma_f32_16x16x32_bf16 v[52:55], a[48:51], a[72:75], 0\n"
      "s_waitcnt lgkmcnt(6)\n"
      "ds_read_b128 a[8:11], v7 offset:1280\n"
      "s_nop 0\n"
      "v_mfma_f32_16x16x32_bf16 v[56:59], a[52:55], a[76:79], 0\n"
      "ds_read_b128 a[36:39], v2 offset:13344\n"
      "v_cvt_pk_bf16_f32 v20, v20, v21\n"
      "v_mfma_f32_16x16x32_bf16 v[60:63], a[56:59], a[80:83], v[52:55]\n"
      "buffer_store_dwordx2 v[90:91], v15, s[36:39], s52 offen offset:1536 sc1\n"
      "v_cvt_pk_bf16_f32 v21, v22, v23\n"
      "v_mfma_f32_16x16x32_bf16 v[24:27], a[12:15], v[140:143], v[24:27]\n"
      "s_add_u32 s52, s52, 196608\n"
      "ds_write_addtid_b32 v20 offset:62480\n"
      "v_fma_f32 v64, s44, v52, v56\n"
      "v_mfma_f32_16x16x32_bf16 v[24:27], a[16:19], a[236:239], v[24:27]\n"
      "s_sub_u32 s96, s53, s95\n"
      "ds_write_addtid_b32 v21 offset:62736\n"
      "v_fma_f32 v65, s44, v53, v57\n"
      "v_mfma_f32_16x16x32_bf16 v[24:27], a[20:23], v[248:251], v[24:27]\n"
      "ds_read_b128 a[40:43], v3 offset:13344\n"
      "v_cvt_pk_bf16_f32 v64, v64, v65\n"
      "v_mfma_f32_16x16x32_bf16 v[72:75], a[84:87], a[64:67], 0\n"
      "ds_read_b128 a[44:47], v4 offset:13344\n"
      "v_fma_f32 v66, s44, v54, v58\n"
      "v_mfma_f32_16x16x32_bf16 v[28:31], a[12:15], v[144:147], v[28:31]\n"
      "ds_read_b128 a[12:15], v7 offset:5376\n"
      "v_fma_f32 v67, s44, v55, v59\n"
      "v_mfma_f32_16x16x32_bf16 v[28:31], a[16:19], v[200:203], v[28:31]\n"
      "ds_read_b128 a[16:19], v7 offset:5632\n"
      "v_cvt_pk_bf16_f32 v24, v24, v25\n"
      "v_mfma_f32_16x16x32_bf16 v[28:31], a[20:23], a[244:247], v[28:31]\n"
      "s_waitcnt lgkmcnt(6)\n"
      "ds_read_b128 a[20:23], v7 offset:5888\n"
      "v_cvt_pk_bf16_f32 v25, v26, v27\n"
      "v_mfma_f32_16x16x32_bf16 v[76:79], a[84:87], a[68:71], 0\n"
      "ds_write_addtid_b32 v24 offset:50184\n"
      "ds_write_addtid_b32 v25 offset:50440\n"
      "v_mfma_f32_16x16x32_bf16 v[32:35], a[24:27], v[92:95], 0\n"
      "s_mov_b32 m0, s18\n"
      "ds_read2_b64 a[60:63], v10 offset0:2 offset1:66\n"
      "v_cvt_pk_bf16_f32 v65, v66, v67\n"
      "v_mfma_f32_16x16x32_bf16 v[32:35], a[28:31], v[148:151], v[32:35]\n"
      "ds_write_addtid_b32 v64 offset:768\n"
      "v_fma_f32 v56, s44, v52, -v56\n"
      "v_mfma_f32_16x16x32_bf16 v[32:35], a[32:35], v[204:207], v[32:35]\n"
      "ds_write_addtid_b32 v65 offset:37696\n"
      "v_fma_f32 v57, s44, v53, -v57\n"
      "v_mfma_f32_16x16x32_bf16 v[32:35], a[0:3], v[112:115], v[32:35]\n"
      "v_fma_f32 v58, s44, v54, -v58\n"
      "v_fma_f32 v59, s44, v55, -v59\n"
      "v_mfma_f32_16x16x32_bf16 v[32:35], a[4:7], a[208:211], v[32:35]\n"
      "v_cvt_pk_bf16_f32 v56, v56, v57\n"
      "v_cvt_pk_bf16_f32 v57, v58, v59\n"
      "v_mfma_f32_16x16x32_bf16 v[32:35], a[8:11], v[220:223], v[32:35]\n"
      "s_waitcnt lgkmcnt(6)\n"
      "ds_write_addtid_b32 v56 offset:1536\n"
      "v_cvt_pk_bf16_f32 v60, v60, v61\n"
      "v_mfma_f32_16x16x32_bf16 v[32:35], a[12:15], a[212:215], v[32:35]\n"
      "ds_write_addtid_b32 v57 offset:38464\n"
      "v_cvt_pk_bf16_f32 v61, v62, v63\n"
      "v_mfma_f32_16x16x32_bf16 v[32:35], a[16:19], v[184:187], v[32:35]\n"
      "s_waitcnt lgkmcnt(7)\n"
      "ds_write_addtid_b32 v60 offset:0\n"
      "ds_write_addtid_b32 v61 offset:36928\n"
      "v_mfma_f32_16x16x32_bf16 v[32:35], a[20:23], v[240:243], v[32:35]\n"
      "s_mov_b32 m0, s45\n"
      "ds_read_b128 a[48:51], v2 offset:13408\n"
      "v_cvt_pk_bf16_f32 v28, v28, v29\n"
      "v_mfma_f32_16x16x32_bf16 v[20:23], a[24:27], v[100:103], 0\n"
      "ds_write_addtid_b32 v28 offset:37888\n"
      "v_cvt_pk_bf16_f32 v29, v30, v31\n"
      "v_mfma_f32_16x16x32_bf16 v[20:23], a[28:31], a[200:203], v[20:23]\n"
      "ds_write_addtid_b32 v29 offset:38144\n"
      "v_cvt_pk_bf16_f32 v32, v32, v33\n"
      "v_mfma_f32_16x16x32_bf16 v[20:23], a[32:35], v[208:211], v[20:23]\n"
      "s_mov_b32 m0, s58\n"
      "ds_read_b128 a[56:59], v4 offset:13408\n"
      "v_cvt_pk_bf16_f32 v33, v34, v35\n"
      "v_mfma_f32_16x16x32_bf16 v[20:23], a[0:3], a[204:207], v[20:23]\n"
      "buffer_load_dwordx4 v17, s[32:35], s53 offen offset:1024 lds\n"
      "v_cvt_pk_bf16_f32 v80, v72, v73\n"
      "v_mfma_f32_16x16x32_bf16 v[20:23], a[4:7], v[172:175], v[20:23]\n"
      "s_mov_b32 m0, s45\n"
      "ds_read2_b64 a[64:67], v10 offset0:3 offset1:67\n"
      "v_cvt_pk_bf16_f32 v81, v76, v77\n"
      "v_mfma_f32_16x16x32_bf16 v[20:23], a[8:11], v[228:231], v[20:23]\n"
      "ds_write_addtid_b32 v32 offset:61456\n"
      "v_cvt_pk_bf16_f32 v86, v74, v75\n"
      "v_mfma_f32_16x16x32_bf16 v[20:23], a[12:15], v[136:139], v[20:23]\n"
      "ds_write_addtid_b32 v33 offset:61712\n"
      "v_cvt_pk_bf16_f32 v87, v78, v79\n"
      "v_mfma_f32_16x16x32_bf16 v[20:23], a[16:19], v[192:195], v[20:23]\n"
      "ds_read_b128 a[52:55], v3 offset:13408\n"
      "v_add_u32_e32 v2, s91, v2\n"
      "v_mfma_f32_16x16x32_bf16 v[20:23], a[20:23], v[244:247], v[20:23]\n"
      "ds_read2_b64 a[68:71], v12 offset0:0 offset1:64\n"
      "v_add_u32_e64 v3, s91, v3\n"
      "v_mfma_f32_16x16x32_bf16 v[36:39], a[36:39], a[72:75], 0\n"
      "v_add_u32_e32 v4, s91, v4\n"
      "s_nop 0\n"
      "v_mfma_f32_16x16x32_bf16 v[40:43], a[40:43], a[76:79], 0\n"
      "s_sub_u32 s91, 0, s91\n"
      "v_mfma_f32_16x16x32_bf16 v[44:47], a[44:47], a[80:83], v[36:39]\n"
      "s_waitcnt lgkmcnt(5)\n"
      "v_cvt_pk_bf16_f32 v20, v20, v21\n"
      "v_cvt_pk_bf16_f32 v21, v22, v23\n"
      "v_mfma_f32_16x16x32_bf16 v[68:71], a[84:87], a[60:63], 0\n"
      "ds_write_addtid_b32 v20 offset:49160\n"
      "v_mfma_f32_16x16x32_bf16 v[24:27], a[24:27], a[196:199], 0\n"
      "ds_read_b128 a[24:27], v7 offset:4608\n"
      "v_fma_f32 v48, s44, v36, v40\n"
      "v_mfma_f32_16x16x32_bf16 v[24:27], a[28:31], v[160:163], v[24:27]\n"
      "ds_read_b128 a[28:31], v7 offset:4864\n"
      "v_fma_f32 v49, s44, v37, v41\n"
      "v_mfma_f32_16x16x32_bf16 v[24:27], a[32:35], v[216:219], v[24:27]\n"
      "ds_read_b128 a[32:35], v7 offset:5120\n"
      "v_cvt_pk_bf16_f32 v48, v48, v49\n"
      "v_mfma_f32_16x16x32_bf16 v[24:27], a[0:3], v[124:127], v[24:27]\n"
      "ds_read_b128 a[0:3], v6 offset:4608\n"
      "v_fma_f32 v50, s44, v38, v42\n"
      "v_mfma_f32_16x16x32_bf16 v[24:27], a[4:7], v[180:183], v[24:27]\n"
      "ds_read_b128 a[4:7], v6 offset:4864\n"
      "v_fma_f32 v51, s44, v39, v43\n"
      "v_mfma_f32_16x16x32_bf16 v[24:27], a[8:11], v[232:235], v[24:27]\n"
      "ds_read_b128 a[8:11], v6 offset:5120\n"
      "v_cvt_pk_bf16_f32 v49, v50, v51\n"
      "v_mfma_f32_16x16x32_bf16 v[24:27], a[12:15], a[216:219], v[24:27]\n"
      "ds_read_b128 a[12:15], v7 offset:0\n"
      "v_fma_f32 v40, s44, v36, -v40\n"
      "v_mfma_f32_16x16x32_bf16 v[24:27], a[16:19], v[196:199], v[24:27]\n"
      "ds_read_b128 a[16:19], v7 offset:256\n"
      "v_fma_f32 v41, s44, v37, -v41\n"
      "v_mfma_f32_16x16x32_bf16 v[24:27], a[20:23], v[252:255], v[24:27]\n"
      "ds_read_b128 a[20:23], v7 offset:512\n"
      "v_cvt_pk_bf16_f32 v40, v40, v41\n"
      "v_mfma_f32_16x16x32_bf16 v[52:55], a[48:51], a[72:75], 0\n"
      "ds_write_addtid_b32 v21 offset:49416\n"
      "v_fma_f32 v42, s44, v38, -v42\n"
      "v_mfma_f32_16x16x32_bf16 v[60:63], a[56:59], a[80:83], v[52:55]\n"
      "s_waitcnt lgkmcnt(7)\n"
      "v_fma_f32 v43, s44, v39, -v43\n"
      "v_cvt_pk_bf16_f32 v24, v24, v25\n"
      "v_mfma_f32_16x16x32_bf16 v[72:75], a[84:87], a[64:67], 0\n"
      "s_mov_b32 m0, s58\n"
      "v_cvt_pk_bf16_f32 v25, v26, v27\n"
      "v_cvt_pk_bf16_f32 v41, v42, v43\n"
      "v_mfma_f32_16x16x32_bf16 v[28:31], a[24:27], a[160:163], 0\n"
      "buffer_load_dwordx4 v18, s[32:35], s53 offen offset:2048 lds\n"
      "v_cvt_pk_bf16_f32 v44, v44, v45\n"
      "v_mfma_f32_16x16x32_bf16 v[28:31], a[28:31], a[172:175], v[28:31]\n"
      "s_mov_b32 m0, s45\n"
      "v_cvt_pk_bf16_f32 v82, v68, v69\n"
      "v_cvt_pk_bf16_f32 v83, v72, v73\n"
      "v_mfma_f32_16x16x32_bf16 v[28:31], a[32:35], a[184:187], v[28:31]\n"
      "s_waitcnt lgkmcnt(3)\n"
      "ds_write_addtid_b32 v24 offset:36864\n"
      "ds_write_addtid_b32 v25 offset:37120\n"
      "v_mfma_f32_16x16x32_bf16 v[28:31], a[0:3], a[88:91], v[28:31]\n"
      "s_mov_b32 m0, s18\n"
      "v_cvt_pk_bf16_f32 v45, v46, v47\n"
      "v_cvt_pk_bf16_f32 v88, v70, v71\n"
      "v_mfma_f32_16x16x32_bf16 v[28:31], a[4:7], a[100:103], v[28:31]\n"
      "ds_write_addtid_b32 v48 offset:1024\n"
      "ds_write_addtid_b32 v49 offset:37952\n"
      "v_mfma_f32_16x16x32_bf16 v[28:31], a[8:11], a[112:115], v[28:31]\n"
      "ds_write_addtid_b32 v40 offset:1792\n"
      "ds_write_addtid_b32 v41 offset:38720\n"
      "v_mfma_f32_16x16x32_bf16 v[28:31], a[12:15], a[124:127], v[28:31]\n"
      "s_waitcnt lgkmcnt(7)\n"
      "ds_write_addtid_b32 v44 offset:256\n"
      "ds_write_addtid_b32 v45 offset:37184\n"
      "v_mfma_f32_16x16x32_bf16 v[28:31], a[16:19], a[136:139], v[28:31]\n"
      "buffer_store_dwordx4 v[80:83], v14, s[36:39], s52 offen sc1\n"
      "v_cvt_pk_bf16_f32 v89, v74, v75\n"
      "v_mfma_f32_16x16x32_bf16 v[28:31], a[20:23], a[148:151], v[28:31]\n"
      "s_mov_b32 m0, s45\n"
      "ds_read2_b64 a[60:63], v12 offset0:1 offset1:65\n"
      "v_cvt_pk_bf16_f32 v60, v60, v61\n"
      "v_mfma_f32_16x16x32_bf16 v[56:59], a[52:55], a[76:79], 0\n"
      "v_cvt_pk_bf16_f32 v61, v62, v63\n"
      "v_swap_b32 v6, v7\n"
      "v_mfma_f32_16x16x32_bf16 v[20:23], a[24:27], a[164:167], 0\n"
      "v_mfma_f32_16x16x32_bf16 v[20:23], a[28:31], a[176:179], v[20:23]\n"
      "v_cvt_pk_bf16_f32 v28, v28, v29\n"
      "v_cvt_pk_bf16_f32 v29, v30, v31\n"
      "v_mfma_f32_16x16x32_bf16 v[20:23], a[32:35], a[188:191], v[20:23]\n"
      "buffer_store_dwordx4 v[86:89], v14, s[36:39], s52 offen offset:1536 sc1\n"
      "v_fma_f32 v64, s44, v52, v56\n"
      "v_mfma_f32_16x16x32_bf16 v[20:23], a[0:3], a[92:95], v[20:23]\n"
      "ds_write_addtid_b32 v28 offset:60432\n"
      "ds_write_addtid_b32 v29 offset:60688\n"
      "v_mfma_f32_16x16x32_bf16 v[20:23], a[4:7], a[104:107], v[20:23]\n"
      "s_mov_b32 m0, s18\n"
      "v_fma_f32 v65, s44, v53, v57\n"
      "v_cvt_pk_bf16_f32 v64, v64, v65\n"
      "v_mfma_f32_16x16x32_bf16 v[20:23], a[8:11], a[116:119], v[20:23]\n"
      "ds_write_addtid_b32 v64 offset:1280\n"
      "v_fma_f32 v66, s44, v54, v58\n"
      "v_mfma_f32_16x16x32_bf16 v[20:23], a[12:15], a[128:131], v[20:23]\n"
      "v_fma_f32 v67, s44, v55, v59\n"
      "v_cvt_pk_bf16_f32 v65, v66, v67\n"
      "v_mfma_f32_16x16x32_bf16 v[20:23], a[16:19], a[140:143], v[20:23]\n"
      "ds_write_addtid_b32 v65 offset:38208\n"
      "v_fma_f32 v56, s44, v52, -v56\n"
      "v_mfma_f32_16x16x32_bf16 v[20:23], a[20:23], a[152:155], v[20:23]\n"
      "v_fma_f32 v57, s44, v53, -v57\n"
      "v_cvt_pk_bf16_f32 v56, v56, v57\n"
      "v_mfma_f32_16x16x32_bf16 v[76:79], a[84:87], a[68:71], 0\n"
      "ds_write_addtid_b32 v56 offset:2048\n"
      "v_fma_f32 v58, s44, v54, -v58\n"
      "v_mfma_f32_16x16x32_bf16 v[32:35], a[24:27], a[168:171], 0\n"
      "ds_write_addtid_b32 v60 offset:512\n"
      "v_fma_f32 v59, s44, v55, -v59\n"
      "v_mfma_f32_16x16x32_bf16 v[32:35], a[28:31], a[180:183], v[32:35]\n"
      "ds_write_addtid_b32 v61 offset:37440\n"
      "v_cvt_pk_bf16_f32 v57, v58, v59\n"
      "v_mfma_f32_16x16x32_bf16 v[32:35], a[32:35], a[192:195], v[32:35]\n"
      "ds_write_addtid_b32 v57 offset:38976\n"
      "v_cvt_pk_bf16_f32 v20, v20, v21\n"
      "v_mfma_f32_16x16x32_bf16 v[32:35], a[0:3], a[96:99], v[32:35]\n"
      "s_mov_b32 m0, s45\n"
      "v_cvt_pk_bf16_f32 v21, v22, v23\n"
      "v_cvt_pk_bf16_f32 v84, v76, v77\n"
      "v_mfma_f32_16x16x32_bf16 v[32:35], a[4:7], a[108:111], v[32:35]\n"
      "s_mov_b32 m0, s59\n"
      "v_cvt_pk_bf16_f32 v90, v78, v79\n"
      "v_mfma_f32_16x16x32_bf16 v[32:35], a[8:11], a[120:123], v[32:35]\n"
      "buffer_load_dwordx4 v19, s[32:35], s96 offen lds\n"
      "v_mfma_f32_16x16x32_bf16 v[32:35], a[12:15], a[132:135], v[32:35]\n"
      "s_mov_b32 m0, s45\n"
      "v_mfma_f32_16x16x32_bf16 v[32:35], a[16:19], a[144:147], v[32:35]\n"
      "ds_write_addtid_b32 v20 offset:48136\n"
      "ds_write_addtid_b32 v21 offset:48392\n"
      "v_mfma_f32_16x16x32_bf16 v[32:35], a[20:23], a[156:159], v[32:35]\n"
      "s_nop 1\n"
      "v_mfma_f32_16x16x32_bf16 v[20:23], a[12:15], a[88:91], 0\n"
      "s_add_u32 s51, s51, 196608\n"
      "s_nop 1\n"
      "v_mfma_f32_16x16x32_bf16 v[20:23], a[16:19], a[100:103], v[20:23]\n"
      "s_sub_u32 s47, s51, 3342336\n"
      "v_cvt_pk_bf16_f32 v32, v32, v33\n"
      "v_cvt_pk_bf16_f32 v33, v34, v35\n"
      "v_mfma_f32_16x16x32_bf16 v[20:23], a[20:23], a[112:115], v[20:23]\n"
      "s_cmp_gt_u32 s51, s48\n"
      "ds_write_addtid_b32 v32 offset:35840\n"
      "v_mfma_f32_16x16x32_bf16 v[20:23], a[24:27], a[124:127], v[20:23]\n"
      "s_cselect_b32 s53, s47, s51\n"
      "ds_write_addtid_b32 v33 offset:36096\n"
      "v_mfma_f32_16x16x32_bf16 v[20:23], a[28:31], a[136:139], v[20:23]\n"
      "s_mul_i32 vcc_lo, vcc_lo, 2\n"
      "v_mfma_f32_16x16x32_bf16 v[20:23], a[32:35], a[148:151], v[20:23]\n"
      "v_mfma_f32_16x16x32_bf16 v[24:27], a[12:15], a[92:95], 0\n"
      "s_waitcnt vmcnt(19) lgkmcnt(0)\n"
      "s_barrier\n"
      "v_mfma_f32_16x16x32_bf16 v[24:27], a[16:19], a[104:107], v[24:27]\n"
      "s_add_u32 m0, s58, 13280\n"
      "ds_read_b128 a[0:3], v7 offset:0\n"
      "v_mfma_f32_16x16x32_bf16 v[24:27], a[20:23], a[116:119], v[24:27]\n"
      "ds_read_b128 a[4:7], v7 offset:256\n"
      "v_mfma_f32_16x16x32_bf16 v[24:27], a[24:27], a[128:131], v[24:27]\n"
      "ds_read_b128 a[8:11], v7 offset:512\n"
      "v_mfma_f32_16x16x32_bf16 v[24:27], a[28:31], a[140:143], v[24:27]\n"
      "ds_read_b128 a[36:39], v2 offset:0\n"
      "v_mfma_f32_16x16x32_bf16 v[24:27], a[32:35], a[152:155], v[24:27]\n"
      "ds_read_b128 a[40:43], v3 offset:0\n"
      "v_mfma_f32_16x16x32_bf16 v[68:71], a[84:87], a[60:63], 0\n"
      "ds_read_b128 a[44:47], v4 offset:0\n"
      "s_nop 0\n"
      "v_mfma_f32_16x16x32_bf16 v[28:31], a[12:15], a[96:99], 0\n"
      "ds_read2_b64 a[64:67], v11 offset0:0 offset1:64\n"
      "s_nop 0\n"
      "v_mfma_f32_16x16x32_bf16 v[28:31], a[16:19], a[108:111], v[28:31]\n"
      "buffer_load_dwordx4 v16, s[32:35], s53 offen lds\n"
      "v_mfma_f32_16x16x32_bf16 v[28:31], a[20:23], a[120:123], v[28:31]\n"
      "s_mov_b32 m0, s45\n"
      "ds_read2_b64 a[68:71], v11 offset0:1 offset1:65\n"
      "v_cvt_pk_bf16_f32 v85, v68, v69\n"
      "v_mfma_f32_16x16x32_bf16 v[28:31], a[24:27], a[132:135], v[28:31]\n"
      "ds_read_b128 a[12:15], v6 offset:768\n"
      "v_cvt_pk_bf16_f32 v91, v70, v71\n"
      "v_mfma_f32_16x16x32_bf16 v[28:31], a[28:31], a[144:147], v[28:31]\n"
      "ds_read_b128 a[16:19], v6 offset:1024\n"
      "v_mfma_f32_16x16x32_bf16 v[28:31], a[32:35], a[156:159], v[28:31]\n"
      "s_waitcnt lgkmcnt(6)\n"
      "ds_read_b128 a[20:23], v6 offset:1280\n"
      "v_mfma_f32_16x16x32_bf16 v[20:23], a[0:3], a[160:163], v[20:23]\n"
      "buffer_store_dwordx2 v[84:85], v15, s[36:39], s52 offen sc1\n"
      "v_mfma_f32_16x16x32_bf16 v[20:23], a[4:7], a[172:175], v[20:23]\n"
      "ds_read_b128 a[24:27], v6 offset:5376\n"
      "v_mfma_f32_16x16x32_bf16 v[20:23], a[8:11], a[184:187], v[20:23]\n"
      "ds_read_b128 a[28:31], v6 offset:5632\n"
      "s_nop 0\n"
      "v_mfma_f32_16x16x32_bf16 v[36:39], a[36:39], a[72:75], 0\n"
      "s_waitcnt lgkmcnt(6)\n"
      "ds_read_b128 a[32:35], v6 offset:5888\n"
      "s_nop 0\n"
      "v_mfma_f32_16x16x32_bf16 v[40:43], a[40:43], a[76:79], 0\n"
      "ds_read_b128 a[48:51], v2 offset:64\n"
      "v_cvt_pk_bf16_f32 v20, v20, v21\n"
      "v_mfma_f32_16x16x32_bf16 v[44:47], a[44:47], a[80:83], v[36:39]\n"
      "buffer_store_dwordx2 v[90:91], v15, s[36:39], s52 offen offset:1536 sc1\n"
      "v_cvt_pk_bf16_f32 v21, v22, v23\n"
      "v_mfma_f32_16x16x32_bf16 v[24:27], a[0:3], a[164:167], v[24:27]\n"
      "s_add_u32 s52, s52, 196608\n"
      "ds_write_addtid_b32 v20 offset:57360\n"
      "v_fma_f32 v48, s44, v36, v40\n"
      "v_mfma_f32_16x16x32_bf16 v[24:27], a[4:7], a[176:179], v[24:27]\n"
      "ds_write_addtid_b32 v21 offset:57616\n"
      "v_fma_f32 v49, s44, v37, v41\n"
      "v_mfma_f32_16x16x32_bf16 v[24:27], a[8:11], a[188:191], v[24:27]\n"
      "ds_read_b128 a[52:55], v3 offset:64\n"
      "v_cvt_pk_bf16_f32 v48, v48, v49\n"
      "v_mfma_f32_16x16x32_bf16 v[72:75], a[84:87], a[64:67], 0\n"
      "ds_read_b128 a[56:59], v4 offset:64\n"
      "v_fma_f32 v50, s44, v38, v42\n"
      "v_mfma_f32_16x16x32_bf16 v[28:31], a[0:3], a[168:171], v[28:31]\n"
      "ds_read_b128 a[0:3], v7 offset:768\n"
      "v_fma_f32 v51, s44, v39, v43\n"
      "v_mfma_f32_16x16x32_bf16 v[28:31], a[4:7], a[180:183], v[28:31]\n"
      "ds_read_b128 a[4:7], v7 offset:1024\n"
      "v_cvt_pk_bf16_f32 v24, v24, v25\n"
      "v_mfma_f32_16x16x32_bf16 v[28:31], a[8:11], a[192:195], v[28:31]\n"
      "s_waitcnt lgkmcnt(6)\n"
      "ds_read_b128 a[8:11], v7 offset:1280\n"
      "v_cvt_pk_bf16_f32 v25, v26, v27\n"
      "v_mfma_f32_16x16x32_bf16 v[76:79], a[84:87], a[68:71], 0\n"
      "ds_write_addtid_b32 v24 offset:45064\n"
      "ds_write_addtid_b32 v25 offset:45320\n"
      "v_mfma_f32_16x16x32_bf16 v[32:35], a[12:15], v[92:95], 0\n"
      "s_mov_b32 m0, s18\n"
      "ds_read2_b64 a[60:63], v11 offset0:2 offset1:66\n"
      "v_cvt_pk_bf16_f32 v49, v50, v51\n"
      "v_mfma_f32_16x16x32_bf16 v[32:35], a[16:19], v[148:151], v[32:35]\n"
      "ds_write_addtid_b32 v48 offset:5376\n"
      "v_fma_f32 v40, s44, v36, -v40\n"
      "v_mfma_f32_16x16x32_bf16 v[32:35], a[20:23], v[204:207], v[32:35]\n"
      "ds_write_addtid_b32 v49 offset:42304\n"
      "v_fma_f32 v41, s44, v37, -v41\n"
      "v_mfma_f32_16x16x32_bf16 v[32:35], a[24:27], v[112:115], v[32:35]\n"
      "v_fma_f32 v42, s44, v38, -v42\n"
      "v_fma_f32 v43, s44, v39, -v43\n"
      "v_mfma_f32_16x16x32_bf16 v[32:35], a[28:31], a[208:211], v[32:35]\n"
      "v_cvt_pk_bf16_f32 v40, v40, v41\n"
      "v_cvt_pk_bf16_f32 v41, v42, v43\n"
      "v_mfma_f32_16x16x32_bf16 v[32:35], a[32:35], v[220:223], v[32:35]\n"
      "s_waitcnt lgkmcnt(6)\n"
      "ds_write_addtid_b32 v40 offset:6144\n"
      "v_cvt_pk_bf16_f32 v44, v44, v45\n"
      "v_mfma_f32_16x16x32_bf16 v[32:35], a[0:3], a[212:215], v[32:35]\n"
      "ds_write_addtid_b32 v41 offset:43072\n"
      "v_cvt_pk_bf16_f32 v45, v46, v47\n"
      "v_mfma_f32_16x16x32_bf16 v[32:35], a[4:7], v[184:187], v[32:35]\n"
      "s_waitcnt lgkmcnt(7)\n"
      "ds_write_addtid_b32 v44 offset:4608\n"
      "ds_write_addtid_b32 v45 offset:41536\n"
      "v_mfma_f32_16x16x32_bf16 v[32:35], a[8:11], v[240:243], v[32:35]\n"
      "s_mov_b32 m0, s45\n"
      "ds_read_b128 a[36:39], v2 offset:128\n"
      "v_cvt_pk_bf16_f32 v28, v28, v29\n"
      "v_mfma_f32_16x16x32_bf16 v[20:23], a[12:15], v[100:103], 0\n"
      "ds_write_addtid_b32 v28 offset:32768\n"
      "v_cvt_pk_bf16_f32 v29, v30, v31\n"
      "v_mfma_f32_16x16x32_bf16 v[20:23], a[16:19], a[200:203], v[20:23]\n"
      "ds_write_addtid_b32 v29 offset:33024\n"
      "v_cvt_pk_bf16_f32 v32, v32, v33\n"
      "v_mfma_f32_16x16x32_bf16 v[20:23], a[20:23], v[208:211], v[20:23]\n"
      "s_add_u32 m0, s58, 13280\n"
      "ds_read_b128 a[44:47], v4 offset:128\n"
      "v_cvt_pk_bf16_f32 v33, v34, v35\n"
      "v_mfma_f32_16x16x32_bf16 v[20:23], a[24:27], a[204:207], v[20:23]\n"
      "buffer_load_dwordx4 v17, s[32:35], s53 offen offset:1024 lds\n"
      "v_cvt_pk_bf16_f32 v80, v72, v73\n"
      "v_mfma_f32_16x16x32_bf16 v[20:23], a[28:31], v[172:175], v[20:23]\n"
      "s_mov_b32 m0, s45\n"
      "ds_read2_b64 a[64:67], v11 offset0:3 offset1:67\n"
      "v_cvt_pk_bf16_f32 v81, v76, v77\n"
      "v_mfma_f32_16x16x32_bf16 v[20:23], a[32:35], v[228:231], v[20:23]\n"
      "ds_write_addtid_b32 v32 offset:58384\n"
      "v_cvt_pk_bf16_f32 v86, v74, v75\n"
      "v_mfma_f32_16x16x32_bf16 v[20:23], a[0:3], v[136:139], v[20:23]\n"
      "ds_write_addtid_b32 v33 offset:58640\n"
      "v_cvt_pk_bf16_f32 v87, v78, v79\n"
      "v_mfma_f32_16x16x32_bf16 v[20:23], a[4:7], v[192:195], v[20:23]\n"
      "ds_read_b128 a[40:43], v3 offset:128\n"
      "v_mfma_f32_16x16x32_bf16 v[20:23], a[8:11], v[244:247], v[20:23]\n"
      "global_load_dword v8, v9, s[54:55]\n"
      "s_nop 0\n"
      "v_mfma_f32_16x16x32_bf16 v[52:55], a[48:51], a[72:75], 0\n"
      "ds_read2_b64 a[68:71], v13 offset0:0 offset1:64\n"
      "s_nop 0\n"
      "v_mfma_f32_16x16x32_bf16 v[56:59], a[52:55], a[76:79], 0\n"
      "s_nop 0\n"
      "v_mfma_f32_16x16x32_bf16 v[60:63], a[56:59], a[80:83], v[52:55]\n"
      "s_waitcnt lgkmcnt(5)\n"
      "v_cvt_pk_bf16_f32 v20, v20, v21\n"
      "v_cvt_pk_bf16_f32 v21, v22, v23\n"
      "v_mfma_f32_16x16x32_bf16 v[68:71], a[84:87], a[60:63], 0\n"
      "ds_write_addtid_b32 v20 offset:46088\n"
      "v_mfma_f32_16x16x32_bf16 v[24:27], a[12:15], a[196:199], 0\n"
      "ds_read_b128 a[12:15], v6 offset:1536\n"
      "v_fma_f32 v64, s44, v52, v56\n"
      "v_mfma_f32_16x16x32_bf16 v[24:27], a[16:19], v[160:163], v[24:27]\n"
      "ds_read_b128 a[16:19], v6 offset:1792\n"
      "v_fma_f32 v65, s44, v53, v57\n"
      "v_mfma_f32_16x16x32_bf16 v[24:27], a[20:23], v[216:219], v[24:27]\n"
      "ds_read_b128 a[20:23], v6 offset:2048\n"
      "v_cvt_pk_bf16_f32 v64, v64, v65\n"
      "v_mfma_f32_16x16x32_bf16 v[24:27], a[24:27], v[124:127], v[24:27]\n"
      "ds_read_b128 a[24:27], v6 offset:6144\n"
      "v_fma_f32 v66, s44, v54, v58\n"
      "v_mfma_f32_16x16x32_bf16 v[24:27], a[28:31], v[180:183], v[24:27]\n"
      "ds_read_b128 a[28:31], v6 offset:6400\n"
      "v_fma_f32 v67, s44, v55, v59\n"
      "v_mfma_f32_16x16x32_bf16 v[24:27], a[32:35], v[232:235], v[24:27]\n"
      "ds_read_b128 a[32:35], v6 offset:6656\n"
      "v_cvt_pk_bf16_f32 v65, v66, v67\n"
      "v_mfma_f32_16x16x32_bf16 v[24:27], a[0:3], a[216:219], v[24:27]\n"
      "ds_read_b128 a[0:3], v7 offset:1536\n"
      "v_fma_f32 v56, s44, v52, -v56\n"
      "v_mfma_f32_16x16x32_bf16 v[24:27], a[4:7], v[196:199], v[24:27]\n"
      "ds_read_b128 a[4:7], v7 offset:1792\n"
      "v_fma_f32 v57, s44, v53, -v57\n"
      "v_mfma_f32_16x16x32_bf16 v[24:27], a[8:11], v[252:255], v[24:27]\n"
      "ds_read_b128 a[8:11], v7 offset:2048\n"
      "v_cvt_pk_bf16_f32 v56, v56, v57\n"
      "v_mfma_f32_16x16x32_bf16 v[36:39], a[36:39], a[72:75], 0\n"
      "ds_write_addtid_b32 v21 offset:46344\n"
      "v_fma_f32 v58, s44, v54, -v58\n"
      "v_mfma_f32_16x16x32_bf16 v[44:47], a[44:47], a[80:83], v[36:39]\n"
      "s_waitcnt lgkmcnt(7)\n"
      "v_fma_f32 v59, s44, v55, -v59\n"
      "v_cvt_pk_bf16_f32 v24, v24, v25\n"
      "v_mfma_f32_16x16x32_bf16 v[72:75], a[84:87], a[64:67], 0\n"
      "s_add_u32 m0, s58, 13280\n"
      "v_cvt_pk_bf16_f32 v25, v26, v27\n"
      "v_cvt_pk_bf16_f32 v57, v58, v59\n"
      "v_mfma_f32_16x16x32_bf16 v[28:31], a[12:15], v[96:99], 0\n"
      "buffer_load_dwordx4 v18, s[32:35], s53 offen offset:2048 lds\n"
      "v_cvt_pk_bf16_f32 v60, v60, v61\n"
      "v_mfma_f32_16x16x32_bf16 v[28:31], a[16:19], v[152:155], v[28:31]\n"
      "s_mov_b32 m0, s45\n"
      "v_cvt_pk_bf16_f32 v82, v68, v69\n"
      "v_cvt_pk_bf16_f32 v83, v72, v73\n"
      "v_mfma_f32_16x16x32_bf16 v[28:31], a[20:23], a[220:223], v[28:31]\n"
      "s_waitcnt lgkmcnt(3)\n"
      "ds_write_addtid_b32 v24 offset:33792\n"
      "ds_write_addtid_b32 v25 offset:34048\n"
      "v_mfma_f32_16x16x32_bf16 v[28:31], a[24:27], v[116:119], v[28:31]\n"
      "s_mov_b32 m0, s18\n"
      "v_cvt_pk_bf16_f32 v61, v62, v63\n"
      "v_cvt_pk_bf16_f32 v88, v70, v71\n"
      "v_mfma_f32_16x16x32_bf16 v[28:31], a[28:31], v[168:171], v[28:31]\n"
      "ds_write_addtid_b32 v64 offset:5632\n"
      "ds_write_addtid_b32 v65 offset:42560\n"
      "v_mfma_f32_16x16x32_bf16 v[28:31], a[32:35], v[224:227], v[28:31]\n"
      "ds_write_addtid_b32 v56 offset:6400\n"
      "ds_write_addtid_b32 v57 offset:43328\n"
      "v_mfma_f32_16x16x32_bf16 v[28:31], a[0:3], v[132:135], v[28:31]\n"
      "s_waitcnt lgkmcnt(7)\n"
      "ds_write_addtid_b32 v60 offset:4864\n"
      "ds_write_addtid_b32 v61 offset:41792\n"
      "v_mfma_f32_16x16x32_bf16 v[28:31], a[4:7], v[188:191], v[28:31]\n"
      "buffer_store_dwordx4 v[80:83], v14, s[36:39], s52 offen sc1\n"
      "v_cvt_pk_bf16_f32 v89, v74, v75\n"
      "v_mfma_f32_16x16x32_bf16 v[28:31], a[8:11], a[240:243], v[28:31]\n"
      "s_mov_b32 m0, s45\n"
      "ds_read2_b64 a[60:63], v13 offset0:1 offset1:65\n"
      "v_cvt_pk_bf16_f32 v44, v44, v45\n"
      "v_mfma_f32_16x16x32_bf16 v[40:43], a[40:43], a[76:79], 0\n"
      "v_cvt_pk_bf16_f32 v45, v46, v47\n"
      "s_nop 0\n"
      "v_mfma_f32_16x16x32_bf16 v[20:23], a[12:15], v[104:107], 0\n"
      "v_mfma_f32_16x16x32_bf16 v[20:23], a[16:19], v[156:159], v[20:23]\n"
      "v_cvt_pk_bf16_f32 v28, v28, v29\n"
      "v_cvt_pk_bf16_f32 v29, v30, v31\n"
      "v_mfma_f32_16x16x32_bf16 v[20:23], a[20:23], v[212:215], v[20:23]\n"
      "buffer_store_dwordx4 v[86:89], v14, s[36:39], s52 offen offset:1536 sc1\n"
      "v_fma_f32 v48, s44, v36, v40\n"
      "v_mfma_f32_16x16x32_bf16 v[20:23], a[24:27], v[120:123], v[20:23]\n"
      "ds_write_addtid_b32 v28 offset:59408\n"
      "ds_write_addtid_b32 v29 offset:59664\n"
      "v_mfma_f32_16x16x32_bf16 v[20:23], a[28:31], v[176:179], v[20:23]\n"
      "s_mov_b32 m0, s18\n"
      "v_fma_f32 v49, s44, v37, v41\n"
      "v_cvt_pk_bf16_f32 v48, v48, v49\n"
      "v_mfma_f32_16x16x32_bf16 v[20:23], a[32:35], a[232:235], v[20:23]\n"
      "s_sub_u32 s18, s19, s18\n"
      "ds_write_addtid_b32 v48 offset:5888\n"
      "v_fma_f32 v50, s44, v38, v42\n"
      "v_mfma_f32_16x16x32_bf16 v[20:23], a[0:3], v[140:143], v[20:23]\n"
      "v_fma_f32 v51, s44, v39, v43\n"
      "v_cvt_pk_bf16_f32 v49, v50, v51\n"
      "v_mfma_f32_16x16x32_bf16 v[20:23], a[4:7], a[236:239], v[20:23]\n"
      "ds_write_addtid_b32 v49 offset:42816\n"
      "v_fma_f32 v40, s44, v36, -v40\n"
      "v_mfma_f32_16x16x32_bf16 v[20:23], a[8:11], v[248:251], v[20:23]\n"
      "v_fma_f32 v41, s44, v37, -v41\n"
      "v_cvt_pk_bf16_f32 v40, v40, v41\n"
      "v_mfma_f32_16x16x32_bf16 v[76:79], a[84:87], a[68:71], 0\n"
      "ds_write_addtid_b32 v40 offset:6656\n"
      "v_fma_f32 v42, s44, v38, -v42\n"
      "v_mfma_f32_16x16x32_bf16 v[32:35], a[12:15], v[108:111], 0\n"
      "ds_write_addtid_b32 v44 offset:5120\n"
      "v_fma_f32 v43, s44, v39, -v43\n"
      "v_mfma_f32_16x16x32_bf16 v[32:35], a[16:19], v[164:167], v[32:35]\n"
      "ds_write_addtid_b32 v45 offset:42048\n"
      "v_cvt_pk_bf16_f32 v41, v42, v43\n"
      "v_mfma_f32_16x16x32_bf16 v[32:35], a[20:23], a[224:227], v[32:35]\n"
      "ds_write_addtid_b32 v41 offset:43584\n"
      "v_cvt_pk_bf16_f32 v20, v20, v21\n"
      "v_mfma_f32_16x16x32_bf16 v[32:35], a[24:27], v[128:131], v[32:35]\n"
      "s_mov_b32 m0, s45\n"
      "v_cvt_pk_bf16_f32 v21, v22, v23\n"
      "v_cvt_pk_bf16_f32 v84, v76, v77\n"
      "v_mfma_f32_16x16x32_bf16 v[32:35], a[28:31], a[228:231], v[32:35]\n"
      "ds_write_addtid_b32 v20 offset:47112\n"
      "v_cvt_pk_bf16_f32 v90, v78, v79\n"
      "v_mfma_f32_16x16x32_bf16 v[32:35], a[32:35], v[236:239], v[32:35]\n"
      "ds_write_addtid_b32 v21 offset:47368\n"
      "v_mfma_f32_16x16x32_bf16 v[32:35], a[0:3], v[144:147], v[32:35]\n"
      "v_mfma_f32_16x16x32_bf16 v[32:35], a[4:7], v[200:203], v[32:35]\n"
      "v_mfma_f32_16x16x32_bf16 v[32:35], a[8:11], a[244:247], v[32:35]\n"
      "s_nop 1\n"
      "s_cbranch_vccnz Lconv_loop0\n"
      "Lconv_t0:\n"
      "s_nop 1\n"
      "v_mfma_f32_16x16x32_bf16 v[20:23], a[24:27], v[96:99], 0\n"
      "s_nop 1\n"
      "v_mfma_f32_16x16x32_bf16 v[20:23], a[28:31], v[152:155], v[20:23]\n"
      "v_cvt_pk_bf16_f32 v32, v32, v33\n"
      "v_cvt_pk_bf16_f32 v33, v34, v35\n"
      "v_mfma_f32_16x16x32_bf16 v[20:23], a[32:35], a[220:223], v[20:23]\n"
      "ds_write_addtid_b32 v32 offset:34816\n"
      "v_mfma_f32_16x16x32_bf16 v[20:23], a[0:3], v[116:119], v[20:23]\n"
      "ds_write_addtid_b32 v33 offset:35072\n"
      "v_mfma_f32_16x16x32_bf16 v[20:23], a[4:7], v[168:171], v[20:23]\n"
      "v_mfma_f32_16x16x32_bf16 v[20:23], a[8:11], v[224:227], v[20:23]\n"
      "v_mfma_f32_16x16x32_bf16 v[24:27], a[24:27], v[104:107], 0\n"
      "s_waitcnt lgkmcnt(0)\n"
      "s_barrier\n"
      "v_mfma_f32_16x16x32_bf16 v[24:27], a[28:31], v[156:159], v[24:27]\n"
      "ds_read2_b64 a[64:67], v10 offset0:0 offset1:64\n"
      "v_mfma_f32_16x16x32_bf16 v[24:27], a[32:35], v[212:215], v[24:27]\n"
      "ds_read_b128 a[12:15], v7 offset:6144\n"
      "v_mfma_f32_16x16x32_bf16 v[24:27], a[0:3], v[120:123], v[24:27]\n"
      "ds_read2_b64 a[68:71], v10 offset0:1 offset1:65\n"
      "v_mfma_f32_16x16x32_bf16 v[24:27], a[4:7], v[176:179], v[24:27]\n"
      "ds_read_b128 a[16:19], v7 offset:6400\n"
      "v_mfma_f32_16x16x32_bf16 v[24:27], a[8:11], a[232:235], v[24:27]\n"
      "ds_read_b128 a[20:23], v7 offset:6656\n"
      "v_mfma_f32_16x16x32_bf16 v[68:71], a[84:87], a[60:63], 0\n"
      "ds_read2_b64 a[60:63], v10 offset0:2 offset1:66\n"
      "s_nop 0\n"
      "v_mfma_f32_16x16x32_bf16 v[28:31], a[24:27], v[108:111], 0\n"
      "s_waitcnt lgkmcnt(5)\n"
      "ds_read_b128 a[24:27], v6 offset:5376\n"
      "s_nop 0\n"
      "v_mfma_f32_16x16x32_bf16 v[72:75], a[84:87], a[64:67], 0\n"
      "ds_read2_b64 a[64:67], v10 offset0:3 offset1:67\n"
      "v_cvt_pk_bf16_f32 v85, v68, v69\n"
      "v_mfma_f32_16x16x32_bf16 v[28:31], a[28:31], v[164:167], v[28:31]\n"
      "buffer_store_dwordx2 v[84:85], v15, s[36:39], s52 offen sc1\n"
      "v_cvt_pk_bf16_f32 v91, v70, v71\n"
      "v_mfma_f32_16x16x32_bf16 v[28:31], a[32:35], a[224:227], v[28:31]\n"
      "ds_read_b128 a[28:31], v6 offset:5632\n"
      "v_mfma_f32_16x16x32_bf16 v[28:31], a[0:3], v[128:131], v[28:31]\n"
      "ds_read_b128 a[32:35], v6 offset:5888\n"
      "v_cvt_pk_bf16_f32 v80, v72, v73\n"
      "v_mfma_f32_16x16x32_bf16 v[28:31], a[4:7], a[228:231], v[28:31]\n"
      "ds_read_b128 a[0:3], v7 offset:768\n"
      "v_cvt_pk_bf16_f32 v86, v74, v75\n"
      "v_mfma_f32_16x16x32_bf16 v[28:31], a[8:11], v[236:239], v[28:31]\n"
      "s_waitcnt lgkmcnt(6)\n"
      "ds_read_b128 a[4:7], v7 offset:1024\n"
      "v_mfma_f32_16x16x32_bf16 v[20:23], a[12:15], v[132:135], v[20:23]\n"
      "buffer_store_dwordx2 v[90:91], v15, s[36:39], s52 offen offset:1536 sc1\n"
      "v_mfma_f32_16x16x32_bf16 v[76:79], a[84:87], a[68:71], 0\n"
      "s_waitcnt lgkmcnt(5)\n"
      "ds_read2_b64 a[68:71], v12 offset0:0 offset1:64\n"
      "s_nop 0\n"
      "v_mfma_f32_16x16x32_bf16 v[68:71], a[84:87], a[60:63], 0\n"
      "s_add_u32 s52, s52, 196608\n"
      "ds_read2_b64 a[60:63], v12 offset0:1 offset1:65\n"
      "s_nop 0\n"
      "v_mfma_f32_16x16x32_bf16 v[20:23], a[16:19], v[188:191], v[20:23]\n"
      "ds_read_b128 a[8:11], v7 offset:1280\n"
      "v_cvt_pk_bf16_f32 v81, v76, v77\n"
      "v_mfma_f32_16x16x32_bf16 v[20:23], a[20:23], a[240:243], v[20:23]\n"
      "v_cvt_pk_bf16_f32 v87, v78, v79\n"
      "v_cvt_pk_bf16_f32 v82, v68, v69\n"
      "v_mfma_f32_16x16x32_bf16 v[24:27], a[12:15], v[140:143], v[24:27]\n"
      "s_waitcnt lgkmcnt(5)\n"
      "v_cvt_pk_bf16_f32 v88, v70, v71\n"
      "s_nop 0\n"
      "v_mfma_f32_16x16x32_bf16 v[72:75], a[84:87], a[64:67], 0\n"
      "v_mfma_f32_16x16x32_bf16 v[24:27], a[16:19], a[236:239], v[24:27]\n"
      "v_cvt_pk_bf16_f32 v20, v20, v21\n"
      "v_cvt_pk_bf16_f32 v21, v22, v23\n"
      "v_mfma_f32_16x16x32_bf16 v[24:27], a[20:23], v[248:251], v[24:27]\n"
      "ds_write_addtid_b32 v20 offset:62480\n"
      "s_nop 0\n"
      "v_mfma_f32_16x16x32_bf16 v[28:31], a[12:15], v[144:147], v[28:31]\n"
      "s_waitcnt lgkmcnt(3)\n"
      "ds_read_b128 a[12:15], v7 offset:5376\n"
      "v_cvt_pk_bf16_f32 v83, v72, v73\n"
      "v_mfma_f32_16x16x32_bf16 v[76:79], a[84:87], a[68:71], 0\n"
      "buffer_store_dwordx4 v[80:83], v14, s[36:39], s52 offen sc1\n"
      "v_cvt_pk_bf16_f32 v89, v74, v75\n"
      "v_mfma_f32_16x16x32_bf16 v[28:31], a[16:19], v[200:203], v[28:31]\n"
      "ds_read_b128 a[16:19], v7 offset:5632\n"
      "v_cvt_pk_bf16_f32 v24, v24, v25\n"
      "v_mfma_f32_16x16x32_bf16 v[28:31], a[20:23], a[244:247], v[28:31]\n"
      "ds_read_b128 a[20:23], v7 offset:5888\n"
      "v_cvt_pk_bf16_f32 v25, v26, v27\n"
      "v_mfma_f32_16x16x32_bf16 v[32:35], a[24:27], v[92:95], 0\n"
      "s_waitcnt lgkmcnt(4)\n"
      "ds_write_addtid_b32 v21 offset:62736\n"
      "v_cvt_pk_bf16_f32 v84, v76, v77\n"
      "v_mfma_f32_16x16x32_bf16 v[68:71], a[84:87], a[60:63], 0\n"
      "ds_write_addtid_b32 v24 offset:50184\n"
      "v_cvt_pk_bf16_f32 v90, v78, v79\n"
      "v_mfma_f32_16x16x32_bf16 v[32:35], a[28:31], v[148:151], v[32:35]\n"
      "buffer_store_dwordx4 v[86:89], v14, s[36:39], s52 offen offset:1536 sc1\n"
      "v_cvt_pk_bf16_f32 v28, v28, v29\n"
      "v_mfma_f32_16x16x32_bf16 v[32:35], a[32:35], v[204:207], v[32:35]\n"
      "ds_write_addtid_b32 v25 offset:50440\n"
      "v_cvt_pk_bf16_f32 v29, v30, v31\n"
      "v_mfma_f32_16x16x32_bf16 v[32:35], a[0:3], v[112:115], v[32:35]\n"
      "v_cvt_pk_bf16_f32 v85, v68, v69\n"
      "v_cvt_pk_bf16_f32 v91, v70, v71\n"
      "v_mfma_f32_16x16x32_bf16 v[32:35], a[4:7], a[208:211], v[32:35]\n"
      "ds_write_addtid_b32 v28 offset:37888\n"
      "v_mfma_f32_16x16x32_bf16 v[32:35], a[8:11], v[220:223], v[32:35]\n"
      "s_waitcnt lgkmcnt(5)\n"
      "ds_write_addtid_b32 v29 offset:38144\n"
      "v_mfma_f32_16x16x32_bf16 v[32:35], a[12:15], a[212:215], v[32:35]\n"
      "buffer_store_dwordx2 v[84:85], v15, s[36:39], s52 offen sc1\n"
      "v_mfma_f32_16x16x32_bf16 v[32:35], a[16:19], v[184:187], v[32:35]\n"
      "s_waitcnt lgkmcnt(5)\n"
      "v_mfma_f32_16x16x32_bf16 v[32:35], a[20:23], v[240:243], v[32:35]\n"
      "s_nop 1\n"
      "v_mfma_f32_16x16x32_bf16 v[20:23], a[24:27], v[100:103], 0\n"
      "s_nop 1\n"
      "v_mfma_f32_16x16x32_bf16 v[20:23], a[28:31], a[200:203], v[20:23]\n"
      "s_nop 0\n"
      "v_mfma_f32_16x16x32_bf16 v[20:23], a[32:35], v[208:211], v[20:23]\n"
      "buffer_store_dwordx2 v[90:91], v15, s[36:39], s52 offen offset:1536 sc1\n"
      "v_cvt_pk_bf16_f32 v32, v32, v33\n"
      "v_mfma_f32_16x16x32_bf16 v[20:23], a[0:3], a[204:207], v[20:23]\n"
      "ds_write_addtid_b32 v32 offset:61456\n"
      "v_cvt_pk_bf16_f32 v33, v34, v35\n"
      "v_mfma_f32_16x16x32_bf16 v[20:23], a[4:7], v[172:175], v[20:23]\n"
      "ds_write_addtid_b32 v33 offset:61712\n"
      "v_mfma_f32_16x16x32_bf16 v[20:23], a[8:11], v[228:231], v[20:23]\n"
      "v_mfma_f32_16x16x32_bf16 v[20:23], a[12:15], v[136:139], v[20:23]\n"
      "v_mfma_f32_16x16x32_bf16 v[20:23], a[16:19], v[192:195], v[20:23]\n"
      "v_mfma_f32_16x16x32_bf16 v[20:23], a[20:23], v[244:247], v[20:23]\n"
      "s_nop 1\n"
      "v_mfma_f32_16x16x32_bf16 v[24:27], a[24:27], a[196:199], 0\n"
      "ds_read_b128 a[24:27], v7 offset:4608\n"
      "s_nop 0\n"
      "v_mfma_f32_16x16x32_bf16 v[24:27], a[28:31], v[160:163], v[24:27]\n"
      "ds_read_b128 a[28:31], v7 offset:4864\n"
      "v_mfma_f32_16x16x32_bf16 v[24:27], a[32:35], v[216:219], v[24:27]\n"
      "ds_read_b128 a[32:35], v7 offset:5120\n"
      "v_cvt_pk_bf16_f32 v20, v20, v21\n"
      "v_mfma_f32_16x16x32_bf16 v[24:27], a[0:3], v[124:127], v[24:27]\n"
      "ds_read_b128 a[0:3], v6 offset:4608\n"
      "v_cvt_pk_bf16_f32 v21, v22, v23\n"
      "v_mfma_f32_16x16x32_bf16 v[24:27], a[4:7], v[180:183], v[24:27]\n"
      "ds_read_b128 a[4:7], v6 offset:4864\n"
      "v_mfma_f32_16x16x32_bf16 v[24:27], a[8:11], v[232:235], v[24:27]\n"
      "ds_read_b128 a[8:11], v6 offset:5120\n"
      "v_mfma_f32_16x16x32_bf16 v[24:27], a[12:15], a[216:219], v[24:27]\n"
      "ds_read_b128 a[12:15], v7 offset:0\n"
      "v_mfma_f32_16x16x32_bf16 v[24:27], a[16:19], v[196:199], v[24:27]\n"
      "ds_read_b128 a[16:19], v7 offset:256\n"
      "v_mfma_f32_16x16x32_bf16 v[24:27], a[20:23], v[252:255], v[24:27]\n"
      "s_waitcnt lgkmcnt(7)\n"
      "ds_read_b128 a[20:23], v7 offset:512\n"
      "s_nop 0\n"
      "v_mfma_f32_16x16x32_bf16 v[28:31], a[24:27], a[160:163], 0\n"
      "s_waitcnt lgkmcnt(7)\n"
      "ds_write_addtid_b32 v20 offset:49160\n"
      "ds_write_addtid_b32 v21 offset:49416\n"
      "v_mfma_f32_16x16x32_bf16 v[28:31], a[28:31], a[172:175], v[28:31]\n"
      "s_waitcnt lgkmcnt(8)\n"
      "v_cvt_pk_bf16_f32 v24, v24, v25\n"
      "v_cvt_pk_bf16_f32 v25, v26, v27\n"
      "v_mfma_f32_16x16x32_bf16 v[28:31], a[32:35], a[184:187], v[28:31]\n"
      "s_waitcnt lgkmcnt(7)\n"
      "ds_write_addtid_b32 v24 offset:36864\n"
      "v_mfma_f32_16x16x32_bf16 v[28:31], a[0:3], a[88:91], v[28:31]\n"
      "s_waitcnt lgkmcnt(7)\n"
      "ds_write_addtid_b32 v25 offset:37120\n"
      "v_mfma_f32_16x16x32_bf16 v[28:31], a[4:7], a[100:103], v[28:31]\n"
      "s_waitcnt lgkmcnt(7)\n"
      "v_mfma_f32_16x16x32_bf16 v[28:31], a[8:11], a[112:115], v[28:31]\n"
      "s_waitcnt lgkmcnt(6)\n"
      "v_mfma_f32_16x16x32_bf16 v[28:31], a[12:15], a[124:127], v[28:31]\n"
      "s_waitcnt lgkmcnt(5)\n"
      "v_mfma_f32_16x16x32_bf16 v[28:31], a[16:19], a[136:139], v[28:31]\n"
      "s_waitcnt lgkmcnt(4)\n"
      "v_mfma_f32_16x16x32_bf16 v[28:31], a[20:23], a[148:151], v[28:31]\n"
      "s_nop 1\n"
      "v_mfma_f32_16x16x32_bf16 v[20:23], a[24:27], a[164:167], 0\n"
      "s_nop 1\n"
      "v_mfma_f32_16x16x32_bf16 v[20:23], a[28:31], a[176:179], v[20:23]\n"
      "s_nop 0\n"
      "v_mfma_f32_16x16x32_bf16 v[20:23], a[32:35], a[188:191], v[20:23]\n"
      "v_cvt_pk_bf16_f32 v28, v28, v29\n"
      "v_cvt_pk_bf16_f32 v29, v30, v31\n"
      "v_mfma_f32_16x16x32_bf16 v[20:23], a[0:3], a[92:95], v[20:23]\n"
      "ds_write_addtid_b32 v28 offset:60432\n"
      "v_mfma_f32_16x16x32_bf16 v[20:23], a[4:7], a[104:107], v[20:23]\n"
      "ds_write_addtid_b32 v29 offset:60688\n"
      "v_mfma_f32_16x16x32_bf16 v[20:23], a[8:11], a[116:119], v[20:23]\n"
      "v_mfma_f32_16x16x32_bf16 v[20:23], a[12:15], a[128:131], v[20:23]\n"
      "v_mfma_f32_16x16x32_bf16 v[20:23], a[16:19], a[140:143], v[20:23]\n"
      "v_mfma_f32_16x16x32_bf16 v[20:23], a[20:23], a[152:155], v[20:23]\n"
      "s_nop 1\n"
      "v_mfma_f32_16x16x32_bf16 v[32:35], a[24:27], a[168:171], 0\n"
      "s_nop 1\n"
      "v_mfma_f32_16x16x32_bf16 v[32:35], a[28:31], a[180:183], v[32:35]\n"
      "s_nop 0\n"
      "v_mfma_f32_16x16x32_bf16 v[32:35], a[32:35], a[192:195], v[32:35]\n"
      "v_cvt_pk_bf16_f32 v20, v20, v21\n"
      "v_cvt_pk_bf16_f32 v21, v22, v23\n"
      "v_mfma_f32_16x16x32_bf16 v[32:35], a[0:3], a[96:99], v[32:35]\n"
      "ds_write_addtid_b32 v20 offset:48136\n"
      "v_mfma_f32_16x16x32_bf16 v[32:35], a[4:7], a[108:111], v[32:35]\n"
      "ds_write_addtid_b32 v21 offset:48392\n"
      "v_mfma_f32_16x16x32_bf16 v[32:35], a[8:11], a[120:123], v[32:35]\n"
      "v_mfma_f32_16x16x32_bf16 v[32:35], a[12:15], a[132:135], v[32:35]\n"
      "v_mfma_f32_16x16x32_bf16 v[32:35], a[16:19], a[144:147], v[32:35]\n"
      "v_mfma_f32_16x16x32_bf16 v[32:35], a[20:23], a[156:159], v[32:35]\n"
      "s_nop 1\n"
      "s_nop 5\n"
      "v_cvt_pk_bf16_f32 v32, v32, v33\n"
      "v_cvt_pk_bf16_f32 v33, v34, v35\n"
      "ds_write_addtid_b32 v32 offset:35840\n"
      "ds_write_addtid_b32 v33 offset:36096\n"
      "s_waitcnt lgkmcnt(0)\n"
      "s_barrier\n"
      "ds_read2_b64 a[0:3], v11 offset0:0 offset1:64\n"
      "ds_read2_b64 a[4:7], v11 offset0:1 offset1:65\n"
      "ds_read2_b64 a[8:11], v11 offset0:2 offset1:66\n"
      "ds_read2_b64 a[12:15], v11 offset0:3 offset1:67\n"
      "ds_read2_b64 a[16:19], v13 offset0:0 offset1:64\n"
      "ds_read2_b64 a[20:23], v13 offset0:1 offset1:65\n"
      "s_waitcnt lgkmcnt(5)\n"
      "v_mfma_f32_16x16x32_bf16 v[72:75], a[84:87], a[0:3], 0\n"
      "s_waitcnt lgkmcnt(4)\n"
      "v_mfma_f32_16x16x32_bf16 v[76:79], a[84:87], a[4:7], 0\n"
      "s_waitcnt lgkmcnt(3)\n"
      "v_mfma_f32_16x16x32_bf16 v[68:71], a[84:87], a[8:11], 0\n"
      "s_waitcnt lgkmcnt(2)\n"
      "s_nop 2\n"
      "v_cvt_pk_bf16_f32 v80, v72, v73\n"
      "v_cvt_pk_bf16_f32 v86, v74, v75\n"
      "v_mfma_f32_16x16x32_bf16 v[72:75], a[84:87], a[12:15], 0\n"
      "s_waitcnt lgkmcnt(1)\n"
      "v_cvt_pk_bf16_f32 v81, v76, v77\n"
      "v_cvt_pk_bf16_f32 v87, v78, v79\n"
      "v_cvt_pk_bf16_f32 v82, v68, v69\n"
      "v_cvt_pk_bf16_f32 v88, v70, v71\n"
      "v_mfma_f32_16x16x32_bf16 v[76:79], a[84:87], a[16:19], 0\n"
      "s_waitcnt lgkmcnt(0)\n"
      "v_mfma_f32_16x16x32_bf16 v[68:71], a[84:87], a[20:23], 0\n"
      "s_add_u32 s52, s52, 196608\n"
      "v_cvt_pk_bf16_f32 v83, v72, v73\n"
      "v_cvt_pk_bf16_f32 v89, v74, v75\n"
      "buffer_store_dwordx4 v[80:83], v14, s[36:39], s52 offen sc1\n"
      "buffer_store_dwordx4 v[86:89], v14, s[36:39], s52 offen offset:1536 sc1\n"
      "s_nop 0\n"
      "v_cvt_pk_bf16_f32 v84, v76, v77\n"
      "v_cvt_pk_bf16_f32 v90, v78, v79\n"
      "v_cvt_pk_bf16_f32 v85, v68, v69\n"
      "v_cvt_pk_bf16_f32 v91, v70, v71\n"
      "buffer_store_dwordx2 v[84:85], v15, s[36:39], s52 offen sc1\n"
      "buffer_store_dwordx2 v[90:91], v15, s[36:39], s52 offen offset:1536 sc1\n"
      "Lconv_u0:\n"
      "s_waitcnt vmcnt(0)\n"
      "s_endpgm\n"
      ".p2align 12\n"
      "Lconv_pa1:\n"
      "s_cbranch_execz Lconv_r0\n"
      "s_nop 0\n"
      "Lconv_loop1:\n"
      "v_mfma_f32_16x16x32_bf16 v[20:23], a[24:27], v[96:99], 0\n"
      "s_add_u32 s51, s51, 196608\n"
      "s_nop 1\n"
      "v_mfma_f32_16x16x32_bf16 v[20:23], a[28:31], v[152:155], v[20:23]\n"
      "s_sub_u32 s58, s93, s58\n"
      "v_cvt_pk_bf16_f32 v32, v32, v33\n"
      "v_cvt_pk_bf16_f32 v33, v34, v35\n"
      "v_mfma_f32_16x16x32_bf16 v[20:23], a[32:35], a[220:223], v[20:23]\n"
      "s_sub_u32 s59, s94, s59\n"
      "v_mfma_f32_16x16x32_bf16 v[20:23], a[0:3], v[116:119], v[20:23]\n"
      "s_sub_u32 s47, s51, 3342336\n"
      "v_mfma_f32_16x16x32_bf16 v[20:23], a[4:7], v[168:171], v[20:23]\n"
      "s_cmp_gt_u32 s51, s48\n"
      "ds_write_addtid_b32 v32 offset:34816\n"
      "v_mfma_f32_16x16x32_bf16 v[20:23], a[8:11], v[224:227], v[20:23]\n"
      "s_cselect_b32 s53, s47, s51\n"
      "v_mfma_f32_16x16x32_bf16 v[24:27], a[24:27], v[104:107], 0\n"
      "s_add_u32 s52, s52, 196608\n"
      "ds_write_addtid_b32 v33 offset:35072\n"
      "v_mfma_f32_16x16x32_bf16 v[24:27], a[28:31], v[156:159], v[24:27]\n"
      "v_mfma_f32_16x16x32_bf16 v[24:27], a[32:35], v[212:215], v[24:27]\n"
      "v_mfma_f32_16x16x32_bf16 v[24:27], a[0:3], v[120:123], v[24:27]\n"
      "v_mfma_f32_16x16x32_bf16 v[24:27], a[4:7], v[176:179], v[24:27]\n"
      "s_waitcnt vmcnt(15) lgkmcnt(0)\n"
      "s_barrier\n"
      "v_mfma_f32_16x16x32_bf16 v[24:27], a[8:11], a[232:235], v[24:27]\n"
      "s_mov_b32 m0, s58\n"
      "ds_read_b128 a[48:51], v2 offset:13280\n"
      "v_mfma_f32_16x16x32_bf16 v[28:31], a[24:27], v[108:111], 0\n"
      "ds_read_b128 a[56:59], v4 offset:13280\n"
      "v_mfma_f32_16x16x32_bf16 v[28:31], a[28:31], v[164:167], v[28:31]\n"
      "ds_read_b128 a[12:15], v7 offset:6144\n"
      "v_mfma_f32_16x16x32_bf16 v[28:31], a[32:35], a[224:227], v[28:31]\n"
      "ds_read_b128 a[16:19], v7 offset:6400\n"
      "v_mfma_f32_16x16x32_bf16 v[28:31], a[0:3], v[128:131], v[28:31]\n"
      "ds_read_b128 a[20:23], v7 offset:6656\n"
      "v_mfma_f32_16x16x32_bf16 v[28:31], a[4:7], a[228:231], v[28:31]\n"
      "ds_read_b128 a[52:55], v3 offset:13280\n"
      "v_mfma_f32_16x16x32_bf16 v[28:31], a[8:11], v[236:239], v[28:31]\n"
      "s_waitcnt lgkmcnt(5)\n"
      "ds_read2_b64 a[60:63], v10 offset0:0 offset1:64\n"
      "v_mfma_f32_16x16x32_bf16 v[52:55], a[48:51], a[72:75], 0\n"
      "s_waitcnt lgkmcnt(5)\n"
      "ds_read2_b64 a[64:67], v10 offset0:1 offset1:65\n"
      "v_mfma_f32_16x16x32_bf16 v[60:63], a[56:59], a[80:83], v[52:55]\n"
      "s_waitcnt lgkmcnt(5)\n"
      "ds_read_b128 a[24:27], v6 offset:5376\n"
      "v_mfma_f32_16x16x32_bf16 v[20:23], a[12:15], v[132:135], v[20:23]\n"
      "s_waitcnt lgkmcnt(5)\n"
      "ds_read_b128 a[28:31], v6 offset:5632\n"
      "v_mfma_f32_16x16x32_bf16 v[20:23], a[16:19], v[188:191], v[20:23]\n"
      "s_waitcnt lgkmcnt(5)\n"
      "ds_read_b128 a[32:35], v6 offset:5888\n"
      "v_cvt_pk_bf16_f32 v60, v60, v61\n"
      "v_mfma_f32_16x16x32_bf16 v[20:23], a[20:23], a[240:243], v[20:23]\n"
      "s_waitcnt lgkmcnt(5)\n"
      "ds_read_b128 a[0:3], v7 offset:768\n"
      "v_cvt_pk_bf16_f32 v61, v62, v63\n"
      "v_mfma_f32_16x16x32_bf16 v[56:59], a[52:55], a[76:79], 0\n"
      "ds_read_b128 a[4:7], v7 offset:1024\n"
      "s_nop 0\n"
      "v_mfma_f32_16x16x32_bf16 v[24:27], a[12:15], v[140:143], v[24:27]\n"
      "ds_read_b128 a[8:11], v7 offset:1280\n"
      "v_cvt_pk_bf16_f32 v20, v20, v21\n"
      "v_mfma_f32_16x16x32_bf16 v[24:27], a[16:19], a[236:239], v[24:27]\n"
      "buffer_load_dwordx4 v16, s[32:35], s53 offen lds\n"
      "v_cvt_pk_bf16_f32 v21, v22, v23\n"
      "v_mfma_f32_16x16x32_bf16 v[24:27], a[20:23], v[248:251], v[24:27]\n"
      "s_waitcnt lgkmcnt(6)\n"
      "ds_read_b128 a[36:39], v2 offset:13344\n"
      "v_fma_f32 v64, s44, v52, v56\n"
      "v_mfma_f32_16x16x32_bf16 v[68:71], a[84:87], a[60:63], 0\n"
      "s_mov_b32 m0, s45\n"
      "ds_read_b128 a[40:43], v3 offset:13344\n"
      "v_fma_f32 v65, s44, v53, v57\n"
      "v_mfma_f32_16x16x32_bf16 v[28:31], a[12:15], v[144:147], v[28:31]\n"
      "s_mov_b32 m0, s58\n"
      "ds_read_b128 a[12:15], v7 offset:5376\n"
      "v_cvt_pk_bf16_f32 v24, v24, v25\n"
      "v_mfma_f32_16x16x32_bf16 v[28:31], a[16:19], v[200:203], v[28:31]\n"
      "ds_read_b128 a[16:19], v7 offset:5632\n"
      "v_cvt_pk_bf16_f32 v25, v26, v27\n"
      "v_mfma_f32_16x16x32_bf16 v[28:31], a[20:23], a[244:247], v[28:31]\n"
      "ds_read_b128 a[20:23], v7 offset:5888\n"
      "v_cvt_pk_bf16_f32 v64, v64, v65\n"
      "v_mfma_f32_16x16x32_bf16 v[72:75], a[84:87], a[64:67], 0\n"
      "s_waitcnt lgkmcnt(6)\n"
      "ds_read_b128 a[44:47], v4 offset:13344\n"
      "v_fma_f32 v66, s44, v54, v58\n"
      "v_mfma_f32_16x16x32_bf16 v[32:35], a[24:27], v[92:95], 0\n"
      "ds_read2_b64 a[68:71], v10 offset0:2 offset1:66\n"
      "v_cvt_pk_bf16_f32 v28, v28, v29\n"
      "v_mfma_f32_16x16x32_bf16 v[32:35], a[28:31], v[148:151], v[32:35]\n"
      "ds_read2_b64 a[60:63], v10 offset0:3 offset1:67\n"
      "v_cvt_pk_bf16_f32 v29, v30, v31\n"
      "v_mfma_f32_16x16x32_bf16 v[32:35], a[32:35], v[204:207], v[32:35]\n"
      "buffer_load_dwordx4 v17, s[32:35], s53 offen offset:1024 lds\n"
      "v_fma_f32 v67, s44, v55, v59\n"
      "v_mfma_f32_16x16x32_bf16 v[32:35], a[0:3], v[112:115], v[32:35]\n"
      "s_mov_b32 m0, s45\n"
      "v_cvt_pk_bf16_f32 v65, v66, v67\n"
      "v_fma_f32 v56, s44, v52, -v56\n"
      "v_mfma_f32_16x16x32_bf16 v[32:35], a[4:7], a[208:211], v[32:35]\n"
      "s_waitcnt lgkmcnt(5)\n"
      "ds_write_addtid_b32 v20 offset:62480\n"
      "v_fma_f32 v57, s44, v53, -v57\n"
      "v_mfma_f32_16x16x32_bf16 v[32:35], a[8:11], v[220:223], v[32:35]\n"
      "ds_write_addtid_b32 v21 offset:62736\n"
      "v_cvt_pk_bf16_f32 v56, v56, v57\n"
      "v_mfma_f32_16x16x32_bf16 v[32:35], a[12:15], a[212:215], v[32:35]\n"
      "s_waitcnt lgkmcnt(5)\n"
      "ds_write_addtid_b32 v24 offset:50184\n"
      "v_fma_f32 v58, s44, v54, -v58\n"
      "v_mfma_f32_16x16x32_bf16 v[32:35], a[16:19], v[184:187], v[32:35]\n"
      "ds_write_addtid_b32 v25 offset:50440\n"
      "v_fma_f32 v59, s44, v55, -v59\n"
      "v_mfma_f32_16x16x32_bf16 v[32:35], a[20:23], v[240:243], v[32:35]\n"
      "ds_write_addtid_b32 v28 offset:37888\n"
      "v_cvt_pk_bf16_f32 v57, v58, v59\n"
      "v_mfma_f32_16x16x32_bf16 v[36:39], a[36:39], a[72:75], 0\n"
      "ds_write_addtid_b32 v29 offset:38144\n"
      "v_cvt_pk_bf16_f32 v80, v68, v69\n"
      "v_mfma_f32_16x16x32_bf16 v[40:43], a[40:43], a[76:79], 0\n"
      "s_waitcnt lgkmcnt(6)\n"
      "ds_read_b128 a[48:51], v2 offset:13408\n"
      "v_cvt_pk_bf16_f32 v32, v32, v33\n"
      "v_mfma_f32_16x16x32_bf16 v[44:47], a[44:47], a[80:83], v[36:39]\n"
      "s_mov_b32 m0, s18\n"
      "ds_read_b128 a[56:59], v4 offset:13408\n"
      "v_cvt_pk_bf16_f32 v86, v70, v71\n"
      "v_mfma_f32_16x16x32_bf16 v[76:79], a[84:87], a[68:71], 0\n"
      "ds_write_addtid_b32 v64 offset:768\n"
      "v_cvt_pk_bf16_f32 v33, v34, v35\n"
      "v_mfma_f32_16x16x32_bf16 v[68:71], a[84:87], a[60:63], 0\n"
      "ds_write_addtid_b32 v65 offset:37696\n"
      "v_cvt_pk_bf16_f32 v81, v72, v73\n"
      "v_mfma_f32_16x16x32_bf16 v[20:23], a[24:27], v[100:103], 0\n"
      "ds_write_addtid_b32 v56 offset:1536\n"
      "v_fma_f32 v48, s44, v36, v40\n"
      "v_mfma_f32_16x16x32_bf16 v[20:23], a[28:31], a[200:203], v[20:23]\n"
      "ds_write_addtid_b32 v57 offset:38464\n"
      "v_cvt_pk_bf16_f32 v82, v76, v77\n"
      "v_mfma_f32_16x16x32_bf16 v[20:23], a[32:35], v[208:211], v[20:23]\n"
      "ds_write_addtid_b32 v60 offset:0\n"
      "v_cvt_pk_bf16_f32 v83, v68, v69\n"
      "v_mfma_f32_16x16x32_bf16 v[20:23], a[0:3], a[204:207], v[20:23]\n"
      "ds_write_addtid_b32 v61 offset:36928\n"
      "v_fma_f32 v49, s44, v37, v41\n"
      "v_mfma_f32_16x16x32_bf16 v[20:23], a[4:7], v[172:175], v[20:23]\n"
      "s_mov_b32 m0, s45\n"
      "ds_read_b128 a[52:55], v3 offset:13408\n"
      "v_cvt_pk_bf16_f32 v48, v48, v49\n"
      "v_mfma_f32_16x16x32_bf16 v[20:23], a[8:11], v[228:231], v[20:23]\n"
      "s_mov_b32 m0, s58\n"
      "v_cvt_pk_bf16_f32 v87, v74, v75\n"
      "v_fma_f32 v50, s44, v38, v42\n"
      "v_mfma_f32_16x16x32_bf16 v[20:23], a[12:15], v[136:139], v[20:23]\n"
      "buffer_load_dwordx4 v18, s[32:35], s53 offen offset:2048 lds\n"
      "v_fma_f32 v51, s44, v39, v43\n"
      "v_mfma_f32_16x16x32_bf16 v[20:23], a[16:19], v[192:195], v[20:23]\n"
      "s_mov_b32 m0, s45\n"
      "ds_read2_b64 a[64:67], v12 offset0:0 offset1:64\n"
      "v_cvt_pk_bf16_f32 v49, v50, v51\n"
      "v_mfma_f32_16x16x32_bf16 v[20:23], a[20:23], v[244:247], v[20:23]\n"
      "s_waitcnt lgkmcnt(8)\n"
      "ds_write_addtid_b32 v32 offset:61456\n"
      "v_cvt_pk_bf16_f32 v88, v78, v79\n"
      "v_mfma_f32_16x16x32_bf16 v[52:55], a[48:51], a[72:75], 0\n"
      "ds_write_addtid_b32 v33 offset:61712\n"
      "v_cvt_pk_bf16_f32 v89, v70, v71\n"
      "v_mfma_f32_16x16x32_bf16 v[60:63], a[56:59], a[80:83], v[52:55]\n"
      "buffer_store_dwordx4 v[80:83], v14, s[36:39], s52 offen sc1\n"
      "v_fma_f32 v40, s44, v36, -v40\n"
      "v_mfma_f32_16x16x32_bf16 v[24:27], a[24:27], a[196:199], 0\n"
      "s_mov_b32 m0, s18\n"
      "ds_read_b128 a[24:27], v7 offset:4608\n"
      "v_fma_f32 v41, s44, v37, -v41\n"
      "v_mfma_f32_16x16x32_bf16 v[24:27], a[28:31], v[160:163], v[24:27]\n"
      "ds_read_b128 a[28:31], v7 offset:4864\n"
      "v_cvt_pk_bf16_f32 v40, v40, v41\n"
      "v_mfma_f32_16x16x32_bf16 v[24:27], a[32:35], v[216:219], v[24:27]\n"
      "ds_read_b128 a[32:35], v7 offset:5120\n"
      "v_fma_f32 v42, s44, v38, -v42\n"
      "v_mfma_f32_16x16x32_bf16 v[24:27], a[0:3], v[124:127], v[24:27]\n"
      "ds_read_b128 a[0:3], v6 offset:4608\n"
      "v_fma_f32 v43, s44, v39, -v43\n"
      "v_mfma_f32_16x16x32_bf16 v[24:27], a[4:7], v[180:183], v[24:27]\n"
      "ds_read_b128 a[4:7], v6 offset:4864\n"
      "v_cvt_pk_bf16_f32 v41, v42, v43\n"
      "v_mfma_f32_16x16x32_bf16 v[24:27], a[8:11], v[232:235], v[24:27]\n"
      "ds_read_b128 a[8:11], v6 offset:5120\n"
      "v_cvt_pk_bf16_f32 v44, v44, v45\n"
      "v_mfma_f32_16x16x32_bf16 v[24:27], a[12:15], a[216:219], v[24:27]\n"
      "ds_read_b128 a[12:15], v7 offset:0\n"
      "v_cvt_pk_bf16_f32 v45, v46, v47\n"
      "v_mfma_f32_16x16x32_bf16 v[24:27], a[16:19], v[196:199], v[24:27]\n"
      "ds_read_b128 a[16:19], v7 offset:256\n"
      "v_cvt_pk_bf16_f32 v20, v20, v21\n"
      "v_mfma_f32_16x16x32_bf16 v[24:27], a[20:23], v[252:255], v[24:27]\n"
      "s_waitcnt lgkmcnt(7)\n"
      "ds_read_b128 a[20:23], v7 offset:512\n"
      "v_cvt_pk_bf16_f32 v21, v22, v23\n"
      "v_mfma_f32_16x16x32_bf16 v[56:59], a[52:55], a[76:79], 0\n"
      "ds_write_addtid_b32 v48 offset:1024\n"
      "v_cvt_pk_bf16_f32 v60, v60, v61\n"
      "v_mfma_f32_16x16x32_bf16 v[28:31], a[24:27], a[160:163], 0\n"
      "s_waitcnt lgkmcnt(7)\n"
      "ds_write_addtid_b32 v49 offset:37952\n"
      "v_cvt_pk_bf16_f32 v24, v24, v25\n"
      "v_mfma_f32_16x16x32_bf16 v[28:31], a[28:31], a[172:175], v[28:31]\n"
      "buffer_store_dwordx4 v[86:89], v14, s[36:39], s52 offen offset:1536 sc1\n"
      "v_cvt_pk_bf16_f32 v25, v26, v27\n"
      "v_mfma_f32_16x16x32_bf16 v[28:31], a[32:35], a[184:187], v[28:31]\n"
      "s_waitcnt lgkmcnt(6)\n"
      "ds_write_addtid_b32 v40 offset:1792\n"
      "v_fma_f32 v64, s44, v52, v56\n"
      "v_mfma_f32_16x16x32_bf16 v[28:31], a[0:3], a[88:91], v[28:31]\n"
      "ds_write_addtid_b32 v41 offset:38720\n"
      "v_fma_f32 v65, s44, v53, v57\n"
      "v_mfma_f32_16x16x32_bf16 v[28:31], a[4:7], a[100:103], v[28:31]\n"
      "s_waitcnt lgkmcnt(6)\n"
      "ds_write_addtid_b32 v44 offset:256\n"
      "v_cvt_pk_bf16_f32 v64, v64, v65\n"
      "v_mfma_f32_16x16x32_bf16 v[28:31], a[8:11], a[112:115], v[28:31]\n"
      "ds_write_addtid_b32 v45 offset:37184\n"
      "v_fma_f32 v66, s44, v54, v58\n"
      "v_mfma_f32_16x16x32_bf16 v[28:31], a[12:15], a[124:127], v[28:31]\n"
      "s_waitcnt lgkmcnt(6)\n"
      "ds_read2_b64 a[68:71], v12 offset0:1 offset1:65\n"
      "v_fma_f32 v67, s44, v55, v59\n"
      "v_mfma_f32_16x16x32_bf16 v[28:31], a[16:19], a[136:139], v[28:31]\n"
      "s_mov_b32 m0, s45\n"
      "v_cvt_pk_bf16_f32 v65, v66, v67\n"
      "v_fma_f32 v56, s44, v52, -v56\n"
      "v_mfma_f32_16x16x32_bf16 v[28:31], a[20:23], a[148:151], v[28:31]\n"
      "ds_write_addtid_b32 v20 offset:49160\n"
      "v_fma_f32 v57, s44, v53, -v57\n"
      "v_mfma_f32_16x16x32_bf16 v[72:75], a[84:87], a[64:67], 0\n"
      "ds_write_addtid_b32 v21 offset:49416\n"
      "v_cvt_pk_bf16_f32 v56, v56, v57\n"
      "v_mfma_f32_16x16x32_bf16 v[20:23], a[24:27], a[164:167], 0\n"
      "ds_write_addtid_b32 v24 offset:36864\n"
      "v_fma_f32 v58, s44, v54, -v58\n"
      "v_mfma_f32_16x16x32_bf16 v[20:23], a[28:31], a[176:179], v[20:23]\n"
      "ds_write_addtid_b32 v25 offset:37120\n"
      "v_cvt_pk_bf16_f32 v28, v28, v29\n"
      "v_mfma_f32_16x16x32_bf16 v[20:23], a[32:35], a[188:191], v[20:23]\n"
      "ds_write_addtid_b32 v28 offset:60432\n"
      "v_cvt_pk_bf16_f32 v29, v30, v31\n"
      "v_mfma_f32_16x16x32_bf16 v[20:23], a[0:3], a[92:95], v[20:23]\n"
      "ds_write_addtid_b32 v29 offset:60688\n"
      "v_fma_f32 v59, s44, v55, -v59\n"
      "v_mfma_f32_16x16x32_bf16 v[20:23], a[4:7], a[104:107], v[20:23]\n"
      "s_mov_b32 m0, s18\n"
      "v_cvt_pk_bf16_f32 v57, v58, v59\n"
      "v_cvt_pk_bf16_f32 v61, v62, v63\n"
      "v_mfma_f32_16x16x32_bf16 v[20:23], a[8:11], a[116:119], v[20:23]\n"
      "ds_write_addtid_b32 v64 offset:1280\n"
      "v_add_u32_e64 v2, s91, v2\n"
      "v_mfma_f32_16x16x32_bf16 v[20:23], a[12:15], a[128:131], v[20:23]\n"
      "ds_write_addtid_b32 v65 offset:38208\n"
      "v_add_u32_e64 v4, s91, v4\n"
      "v_mfma_f32_16x16x32_bf16 v[20:23], a[16:19], a[140:143], v[20:23]\n"
      "ds_write_addtid_b32 v56 offset:2048\n"
      "v_cvt_pk_bf16_f32 v84, v72, v73\n"
      "v_mfma_f32_16x16x32_bf16 v[20:23], a[20:23], a[152:155], v[20:23]\n"
      "s_waitcnt lgkmcnt(9)\n"
      "ds_write_addtid_b32 v57 offset:38976\n"
      "v_swap_b32 v6, v7\n"
      "v_mfma_f32_16x16x32_bf16 v[76:79], a[84:87], a[68:71], 0\n"
      "ds_write_addtid_b32 v60 offset:512\n"
      "v_cvt_pk_bf16_f32 v90, v74, v75\n"
      "v_mfma_f32_16x16x32_bf16 v[32:35], a[24:27], a[168:171], 0\n"
      "ds_write_addtid_b32 v61 offset:37440\n"
      "v_cvt_pk_bf16_f32 v20, v20, v21\n"
      "v_mfma_f32_16x16x32_bf16 v[32:35], a[28:31], a[180:183], v[32:35]\n"
      "s_mov_b32 m0, s45\n"
      "v_cvt_pk_bf16_f32 v21, v22, v23\n"
      "v_cvt_pk_bf16_f32 v85, v76, v77\n"
      "v_mfma_f32_16x16x32_bf16 v[32:35], a[32:35], a[192:195], v[32:35]\n"
      "buffer_store_dwordx2 v[84:85], v15, s[36:39], s52 offen sc1\n"
      "v_cvt_pk_bf16_f32 v91, v78, v79\n"
      "v_mfma_f32_16x16x32_bf16 v[32:35], a[0:3], a[96:99], v[32:35]\n"
      "v_add_u32_e64 v3, s91, v3\n"
      "v_mfma_f32_16x16x32_bf16 v[32:35], a[4:7], a[108:111], v[32:35]\n"
      "s_sub_u32 s91, 0, s91\n"
      "ds_write_addtid_b32 v20 offset:48136\n"
      "v_mfma_f32_16x16x32_bf16 v[32:35], a[8:11], a[120:123], v[32:35]\n"
      "ds_write_addtid_b32 v21 offset:48392\n"
      "v_mfma_f32_16x16x32_bf16 v[32:35], a[12:15], a[132:135], v[32:35]\n"
      "buffer_store_dwordx2 v[90:91], v15, s[36:39], s52 offen offset:1536 sc1\n"
      "v_mfma_f32_16x16x32_bf16 v[32:35], a[16:19], a[144:147], v[32:35]\n"
      "v_mfma_f32_16x16x32_bf16 v[32:35], a[20:23], a[156:159], v[32:35]\n"
      "s_nop 1\n"
      "v_mfma_f32_16x16x32_bf16 v[20:23], a[12:15], a[88:91], 0\n"
      "s_add_u32 s51, s51, 196608\n"
      "s_nop 1\n"
      "v_mfma_f32_16x16x32_bf16 v[20:23], a[16:19], a[100:103], v[20:23]\n"
      "s_sub_u32 s47, s51, 3342336\n"
      "v_cvt_pk_bf16_f32 v32, v32, v33\n"
      "v_cvt_pk_bf16_f32 v33, v34, v35\n"
      "v_mfma_f32_16x16x32_bf16 v[20:23], a[20:23], a[112:115], v[20:23]\n"
      "s_cmp_gt_u32 s51, s48\n"
      "v_mfma_f32_16x16x32_bf16 v[20:23], a[24:27], a[124:127], v[20:23]\n"
      "s_cselect_b32 s53, s47, s51\n"
      "ds_write_addtid_b32 v32 offset:35840\n"
      "v_mfma_f32_16x16x32_bf16 v[20:23], a[28:31], a[136:139], v[20:23]\n"
      "s_mul_i32 vcc_lo, vcc_lo, 2\n"
      "v_mfma_f32_16x16x32_bf16 v[20:23], a[32:35], a[148:151], v[20:23]\n"
      "ds_write_addtid_b32 v33 offset:36096\n"
      "v_mfma_f32_16x16x32_bf16 v[24:27], a[12:15], a[92:95], 0\n"
      "v_mfma_f32_16x16x32_bf16 v[24:27], a[16:19], a[104:107], v[24:27]\n"
      "v_mfma_f32_16x16x32_bf16 v[24:27], a[20:23], a[116:119], v[24:27]\n"
      "v_mfma_f32_16x16x32_bf16 v[24:27], a[24:27], a[128:131], v[24:27]\n"
      "v_mfma_f32_16x16x32_bf16 v[24:27], a[28:31], a[140:143], v[24:27]\n"
      "s_waitcnt vmcnt(19) lgkmcnt(0)\n"
      "s_barrier\n"
      "v_mfma_f32_16x16x32_bf16 v[24:27], a[32:35], a[152:155], v[24:27]\n"
      "s_add_u32 m0, s58, 13280\n"
      "ds_read_b128 a[36:39], v2 offset:0\n"
      "v_mfma_f32_16x16x32_bf16 v[28:31], a[12:15], a[96:99], 0\n"
      "ds_read_b128 a[44:47], v4 offset:0\n"
      "v_mfma_f32_16x16x32_bf16 v[28:31], a[16:19], a[108:111], v[28:31]\n"
      "ds_read_b128 a[0:3], v7 offset:0\n"
      "v_mfma_f32_16x16x32_bf16 v[28:31], a[20:23], a[120:123], v[28:31]\n"
      "ds_read_b128 a[4:7], v7 offset:256\n"
      "v_mfma_f32_16x16x32_bf16 v[28:31], a[24:27], a[132:135], v[28:31]\n"
      "ds_read_b128 a[8:11], v7 offset:512\n"
      "v_mfma_f32_16x16x32_bf16 v[28:31], a[28:31], a[144:147], v[28:31]\n"
      "ds_read_b128 a[40:43], v3 offset:0\n"
      "v_mfma_f32_16x16x32_bf16 v[28:31], a[32:35], a[156:159], v[28:31]\n"
      "s_waitcnt lgkmcnt(5)\n"
      "ds_read2_b64 a[60:63], v11 offset0:0 offset1:64\n"
      "v_mfma_f32_16x16x32_bf16 v[36:39], a[36:39], a[72:75], 0\n"
      "s_waitcnt lgkmcnt(5)\n"
      "ds_read2_b64 a[64:67], v11 offset0:1 offset1:65\n"
      "v_mfma_f32_16x16x32_bf16 v[44:47], a[44:47], a[80:83], v[36:39]\n"
      "s_waitcnt lgkmcnt(5)\n"
      "ds_read_b128 a[12:15], v6 offset:768\n"
      "v_mfma_f32_16x16x32_bf16 v[20:23], a[0:3], a[160:163], v[20:23]\n"
      "s_waitcnt lgkmcnt(5)\n"
      "ds_read_b128 a[16:19], v6 offset:1024\n"
      "v_mfma_f32_16x16x32_bf16 v[20:23], a[4:7], a[172:175], v[20:23]\n"
      "s_waitcnt lgkmcnt(5)\n"
      "ds_read_b128 a[20:23], v6 offset:1280\n"
      "v_cvt_pk_bf16_f32 v44, v44, v45\n"
      "v_mfma_f32_16x16x32_bf16 v[20:23], a[8:11], a[184:187], v[20:23]\n"
      "s_waitcnt lgkmcnt(5)\n"
      "ds_read_b128 a[24:27], v6 offset:5376\n"
      "v_cvt_pk_bf16_f32 v45, v46, v47\n"
      "v_mfma_f32_16x16x32_bf16 v[40:43], a[40:43], a[76:79], 0\n"
      "ds_read_b128 a[28:31], v6 offset:5632\n"
      "s_nop 0\n"
      "v_mfma_f32_16x16x32_bf16 v[24:27], a[0:3], a[164:167], v[24:27]\n"
      "ds_read_b128 a[32:35], v6 offset:5888\n"
      "v_cvt_pk_bf16_f32 v20, v20, v21\n"
      "v_mfma_f32_16x16x32_bf16 v[24:27], a[4:7], a[176:179], v[24:27]\n"
      "buffer_load_dwordx4 v16, s[32:35], s53 offen lds\n"
      "v_cvt_pk_bf16_f32 v21, v22, v23\n"
      "v_mfma_f32_16x16x32_bf16 v[24:27], a[8:11], a[188:191], v[24:27]\n"
      "s_waitcnt lgkmcnt(6)\n"
      "ds_read_b128 a[48:51], v2 offset:64\n"
      "v_fma_f32 v48, s44, v36, v40\n"
      "v_mfma_f32_16x16x32_bf16 v[68:71], a[84:87], a[60:63], 0\n"
      "s_mov_b32 m0, s45\n"
      "ds_read_b128 a[52:55], v3 offset:64\n"
      "v_fma_f32 v49, s44, v37, v41\n"
      "v_mfma_f32_16x16x32_bf16 v[28:31], a[0:3], a[168:171], v[28:31]\n"
      "s_add_u32 m0, s58, 13280\n"
      "ds_read_b128 a[0:3], v7 offset:768\n"
      "v_cvt_pk_bf16_f32 v24, v24, v25\n"
      "v_mfma_f32_16x16x32_bf16 v[28:31], a[4:7], a[180:183], v[28:31]\n"
      "s_add_u32 s52, s52, 196608\n"
      "ds_read_b128 a[4:7], v7 offset:1024\n"
      "v_cvt_pk_bf16_f32 v25, v26, v27\n"
      "v_mfma_f32_16x16x32_bf16 v[28:31], a[8:11], a[192:195], v[28:31]\n"
      "ds_read_b128 a[8:11], v7 offset:1280\n"
      "v_cvt_pk_bf16_f32 v48, v48, v49\n"
      "v_mfma_f32_16x16x32_bf16 v[72:75], a[84:87], a[64:67], 0\n"
      "s_waitcnt lgkmcnt(6)\n"
      "ds_read_b128 a[56:59], v4 offset:64\n"
      "v_fma_f32 v50, s44, v38, v42\n"
      "v_mfma_f32_16x16x32_bf16 v[32:35], a[12:15], v[92:95], 0\n"
      "ds_read2_b64 a[68:71], v11 offset0:2 offset1:66\n"
      "v_cvt_pk_bf16_f32 v28, v28, v29\n"
      "v_mfma_f32_16x16x32_bf16 v[32:35], a[16:19], v[148:151], v[32:35]\n"
      "ds_read2_b64 a[60:63], v11 offset0:3 offset1:67\n"
      "v_cvt_pk_bf16_f32 v29, v30, v31\n"
      "v_mfma_f32_16x16x32_bf16 v[32:35], a[20:23], v[204:207], v[32:35]\n"
      "buffer_load_dwordx4 v17, s[32:35], s53 offen offset:1024 lds\n"
      "v_fma_f32 v51, s44, v39, v43\n"
      "v_mfma_f32_16x16x32_bf16 v[32:35], a[24:27], v[112:115], v[32:35]\n"
      "s_mov_b32 m0, s45\n"
      "v_cvt_pk_bf16_f32 v49, v50, v51\n"
      "v_fma_f32 v40, s44, v36, -v40\n"
      "v_mfma_f32_16x16x32_bf16 v[32:35], a[28:31], a[208:211], v[32:35]\n"
      "s_waitcnt lgkmcnt(5)\n"
      "ds_write_addtid_b32 v20 offset:57360\n"
      "v_fma_f32 v41, s44, v37, -v41\n"
      "v_mfma_f32_16x16x32_bf16 v[32:35], a[32:35], v[220:223], v[32:35]\n"
      "ds_write_addtid_b32 v21 offset:57616\n"
      "v_cvt_pk_bf16_f32 v40, v40, v41\n"
      "v_mfma_f32_16x16x32_bf16 v[32:35], a[0:3], a[212:215], v[32:35]\n"
      "s_waitcnt lgkmcnt(5)\n"
      "ds_write_addtid_b32 v24 offset:45064\n"
      "v_fma_f32 v42, s44, v38, -v42\n"
      "v_mfma_f32_16x16x32_bf16 v[32:35], a[4:7], v[184:187], v[32:35]\n"
      "ds_write_addtid_b32 v25 offset:45320\n"
      "v_fma_f32 v43, s44, v39, -v43\n"
      "v_mfma_f32_16x16x32_bf16 v[32:35], a[8:11], v[240:243], v[32:35]\n"
      "ds_write_addtid_b32 v28 offset:32768\n"
      "v_cvt_pk_bf16_f32 v41, v42, v43\n"
      "v_mfma_f32_16x16x32_bf16 v[52:55], a[48:51], a[72:75], 0\n"
      "ds_write_addtid_b32 v29 offset:33024\n"
      "v_cvt_pk_bf16_f32 v80, v68, v69\n"
      "v_mfma_f32_16x16x32_bf16 v[56:59], a[52:55], a[76:79], 0\n"
      "s_waitcnt lgkmcnt(6)\n"
      "ds_read_b128 a[36:39], v2 offset:128\n"
      "v_cvt_pk_bf16_f32 v32, v32, v33\n"
      "v_mfma_f32_16x16x32_bf16 v[60:63], a[56:59], a[80:83], v[52:55]\n"
      "s_mov_b32 m0, s18\n"
      "ds_read_b128 a[44:47], v4 offset:128\n"
      "v_cvt_pk_bf16_f32 v86, v70, v71\n"
      "v_mfma_f32_16x16x32_bf16 v[76:79], a[84:87], a[68:71], 0\n"
      "ds_write_addtid_b32 v48 offset:5376\n"
      "v_cvt_pk_bf16_f32 v33, v34, v35\n"
      "v_mfma_f32_16x16x32_bf16 v[68:71], a[84:87], a[60:63], 0\n"
      "ds_write_addtid_b32 v49 offset:42304\n"
      "v_cvt_pk_bf16_f32 v81, v72, v73\n"
      "v_mfma_f32_16x16x32_bf16 v[20:23], a[12:15], v[100:103], 0\n"
      "ds_write_addtid_b32 v40 offset:6144\n"
      "v_fma_f32 v64, s44, v52, v56\n"
      "v_mfma_f32_16x16x32_bf16 v[20:23], a[16:19], a[200:203], v[20:23]\n"
      "ds_write_addtid_b32 v41 offset:43072\n"
      "v_cvt_pk_bf16_f32 v82, v76, v77\n"
      "v_mfma_f32_16x16x32_bf16 v[20:23], a[20:23], v[208:211], v[20:23]\n"
      "ds_write_addtid_b32 v44 offset:4608\n"
      "v_cvt_pk_bf16_f32 v83, v68, v69\n"
      "v_mfma_f32_16x16x32_bf16 v[20:23], a[24:27], a[204:207], v[20:23]\n"
      "ds_write_addtid_b32 v45 offset:41536\n"
      "v_fma_f32 v65, s44, v53, v57\n"
      "v_mfma_f32_16x16x32_bf16 v[20:23], a[28:31], v[172:175], v[20:23]\n"
      "s_mov_b32 m0, s45\n"
      "ds_read_b128 a[40:43], v3 offset:128\n"
      "v_cvt_pk_bf16_f32 v64, v64, v65\n"
      "v_mfma_f32_16x16x32_bf16 v[20:23], a[32:35], v[228:231], v[20:23]\n"
      "s_add_u32 m0, s58, 13280\n"
      "v_cvt_pk_bf16_f32 v87, v74, v75\n"
      "v_fma_f32 v66, s44, v54, v58\n"
      "v_mfma_f32_16x16x32_bf16 v[20:23], a[0:3], v[136:139], v[20:23]\n"
      "buffer_load_dwordx4 v18, s[32:35], s53 offen offset:2048 lds\n"
      "v_fma_f32 v67, s44, v55, v59\n"
      "v_mfma_f32_16x16x32_bf16 v[20:23], a[4:7], v[192:195], v[20:23]\n"
      "s_mov_b32 m0, s45\n"
      "ds_read2_b64 a[64:67], v13 offset0:0 offset1:64\n"
      "v_cvt_pk_bf16_f32 v65, v66, v67\n"
      "v_mfma_f32_16x16x32_bf16 v[20:23], a[8:11], v[244:247], v[20:23]\n"
      "s_waitcnt lgkmcnt(8)\n"
      "ds_write_addtid_b32 v32 offset:58384\n"
      "v_cvt_pk_bf16_f32 v88, v78, v79\n"
      "v_mfma_f32_16x16x32_bf16 v[36:39], a[36:39], a[72:75], 0\n"
      "ds_write_addtid_b32 v33 offset:58640\n"
      "v_cvt_pk_bf16_f32 v89, v70, v71\n"
      "v_mfma_f32_16x16x32_bf16 v[44:47], a[44:47], a[80:83], v[36:39]\n"
      "global_load_dword v8, v9, s[54:55]\n"
      "v_fma_f32 v56, s44, v52, -v56\n"
      "v_mfma_f32_16x16x32_bf16 v[24:27], a[12:15], a[196:199], 0\n"
      "s_mov_b32 m0, s18\n"
      "ds_read_b128 a[12:15], v6 offset:1536\n"
      "v_fma_f32 v57, s44, v53, -v57\n"
      "v_mfma_f32_16x16x32_bf16 v[24:27], a[16:19], v[160:163], v[24:27]\n"
      "ds_read_b128 a[16:19], v6 offset:1792\n"
      "v_cvt_pk_bf16_f32 v56, v56, v57\n"
      "v_mfma_f32_16x16x32_bf16 v[24:27], a[20:23], v[216:219], v[24:27]\n"
      "ds_read_b128 a[20:23], v6 offset:2048\n"
      "v_fma_f32 v58, s44, v54, -v58\n"
      "v_mfma_f32_16x16x32_bf16 v[24:27], a[24:27], v[124:127], v[24:27]\n"
      "ds_read_b128 a[24:27], v6 offset:6144\n"
      "v_fma_f32 v59, s44, v55, -v59\n"
      "v_mfma_f32_16x16x32_bf16 v[24:27], a[28:31], v[180:183], v[24:27]\n"
      "ds_read_b128 a[28:31], v6 offset:6400\n"
      "v_cvt_pk_bf16_f32 v57, v58, v59\n"
      "v_mfma_f32_16x16x32_bf16 v[24:27], a[32:35], v[232:235], v[24:27]\n"
      "ds_read_b128 a[32:35], v6 offset:6656\n"
      "v_cvt_pk_bf16_f32 v60, v60, v61\n"
      "v_mfma_f32_16x16x32_bf16 v[24:27], a[0:3], a[216:219], v[24:27]\n"
      "ds_read_b128 a[0:3], v7 offset:1536\n"
      "v_cvt_pk_bf16_f32 v61, v62, v63\n"
      "v_mfma_f32_16x16x32_bf16 v[24:27], a[4:7], v[196:199], v[24:27]\n"
      "ds_read_b128 a[4:7], v7 offset:1792\n"
      "v_cvt_pk_bf16_f32 v20, v20, v21\n"
      "v_mfma_f32_16x16x32_bf16 v[24:27], a[8:11], v[252:255], v[24:27]\n"
      "s_waitcnt lgkmcnt(7)\n"
      "ds_read_b128 a[8:11], v7 offset:2048\n"
      "v_cvt_pk_bf16_f32 v21, v22, v23\n"
      "v_mfma_f32_16x16x32_bf16 v[40:43], a[40:43], a[76:79], 0\n"
      "buffer_store_dwordx4 v[80:83], v14, s[36:39], s52 offen sc1\n"
      "v_cvt_pk_bf16_f32 v44, v44, v45\n"
      "v_mfma_f32_16x16x32_bf16 v[28:31], a[12:15], v[96:99], 0\n"
      "s_waitcnt lgkmcnt(6)\n"
      "ds_write_addtid_b32 v64 offset:5632\n"
      "v_cvt_pk_bf16_f32 v24, v24, v25\n"
      "v_mfma_f32_16x16x32_bf16 v[28:31], a[16:19], v[152:155], v[28:31]\n"
      "buffer_store_dwordx4 v[86:89], v14, s[36:39], s52 offen offset:1536 sc1\n"
      "v_cvt_pk_bf16_f32 v25, v26, v27\n"
      "v_mfma_f32_16x16x32_bf16 v[28:31], a[20:23], a[220:223], v[28:31]\n"
      "s_waitcnt lgkmcnt(5)\n"
      "ds_write_addtid_b32 v65 offset:42560\n"
      "v_fma_f32 v48, s44, v36, v40\n"
      "v_mfma_f32_16x16x32_bf16 v[28:31], a[24:27], v[116:119], v[28:31]\n"
      "ds_write_addtid_b32 v56 offset:6400\n"
      "v_fma_f32 v49, s44, v37, v41\n"
      "v_mfma_f32_16x16x32_bf16 v[28:31], a[28:31], v[168:171], v[28:31]\n"
      "s_waitcnt lgkmcnt(5)\n"
      "ds_write_addtid_b32 v57 offset:43328\n"
      "v_cvt_pk_bf16_f32 v48, v48, v49\n"
      "v_mfma_f32_16x16x32_bf16 v[28:31], a[32:35], v[224:227], v[28:31]\n"
      "ds_write_addtid_b32 v60 offset:4864\n"
      "v_fma_f32 v50, s44, v38, v42\n"
      "v_mfma_f32_16x16x32_bf16 v[28:31], a[0:3], v[132:135], v[28:31]\n"
      "s_waitcnt lgkmcnt(5)\n"
      "ds_write_addtid_b32 v61 offset:41792\n"
      "v_fma_f32 v51, s44, v39, v43\n"
      "v_mfma_f32_16x16x32_bf16 v[28:31], a[4:7], v[188:191], v[28:31]\n"
      "s_mov_b32 m0, s45\n"
      "ds_read2_b64 a[68:71], v13 offset0:1 offset1:65\n"
      "v_cvt_pk_bf16_f32 v49, v50, v51\n"
      "v_mfma_f32_16x16x32_bf16 v[28:31], a[8:11], a[240:243], v[28:31]\n"
      "ds_write_addtid_b32 v20 offset:46088\n"
      "v_fma_f32 v40, s44, v36, -v40\n"
      "v_mfma_f32_16x16x32_bf16 v[72:75], a[84:87], a[64:67], 0\n"
      "ds_write_addtid_b32 v21 offset:46344\n"
      "v_fma_f32 v41, s44, v37, -v41\n"
      "v_mfma_f32_16x16x32_bf16 v[20:23], a[12:15], v[104:107], 0\n"
      "ds_write_addtid_b32 v24 offset:33792\n"
      "v_cvt_pk_bf16_f32 v40, v40, v41\n"
      "v_mfma_f32_16x16x32_bf16 v[20:23], a[16:19], v[156:159], v[20:23]\n"
      "ds_write_addtid_b32 v25 offset:34048\n"
      "v_cvt_pk_bf16_f32 v28, v28, v29\n"
      "v_mfma_f32_16x16x32_bf16 v[20:23], a[20:23], v[212:215], v[20:23]\n"
      "ds_write_addtid_b32 v28 offset:59408\n"
      "v_cvt_pk_bf16_f32 v29, v30, v31\n"
      "v_mfma_f32_16x16x32_bf16 v[20:23], a[24:27], v[120:123], v[20:23]\n"
      "ds_write_addtid_b32 v29 offset:59664\n"
      "v_fma_f32 v42, s44, v38, -v42\n"
      "v_mfma_f32_16x16x32_bf16 v[20:23], a[28:31], v[176:179], v[20:23]\n"
      "s_mov_b32 m0, s18\n"
      "v_fma_f32 v43, s44, v39, -v43\n"
      "v_cvt_pk_bf16_f32 v41, v42, v43\n"
      "v_mfma_f32_16x16x32_bf16 v[20:23], a[32:35], a[232:235], v[20:23]\n"
      "s_sub_u32 s18, s19, s18\n"
      "ds_write_addtid_b32 v48 offset:5888\n"
      "v_cvt_pk_bf16_f32 v45, v46, v47\n"
      "v_mfma_f32_16x16x32_bf16 v[20:23], a[0:3], v[140:143], v[20:23]\n"
      "ds_write_addtid_b32 v49 offset:42816\n"
      "v_cvt_pk_bf16_f32 v84, v72, v73\n"
      "v_mfma_f32_16x16x32_bf16 v[20:23], a[4:7], a[236:239], v[20:23]\n"
      "ds_write_addtid_b32 v40 offset:6656\n"
      "v_cvt_pk_bf16_f32 v90, v74, v75\n"
      "v_mfma_f32_16x16x32_bf16 v[20:23], a[8:11], v[248:251], v[20:23]\n"
      "s_waitcnt lgkmcnt(9)\n"
      "ds_write_addtid_b32 v41 offset:43584\n"
      "s_nop 0\n"
      "v_mfma_f32_16x16x32_bf16 v[76:79], a[84:87], a[68:71], 0\n"
      "ds_write_addtid_b32 v44 offset:5120\n"
      "s_nop 0\n"
      "v_mfma_f32_16x16x32_bf16 v[32:35], a[12:15], v[108:111], 0\n"
      "ds_write_addtid_b32 v45 offset:42048\n"
      "v_cvt_pk_bf16_f32 v20, v20, v21\n"
      "v_mfma_f32_16x16x32_bf16 v[32:35], a[16:19], v[164:167], v[32:35]\n"
      "s_mov_b32 m0, s45\n"
      "v_cvt_pk_bf16_f32 v21, v22, v23\n"
      "v_cvt_pk_bf16_f32 v85, v76, v77\n"
      "v_mfma_f32_16x16x32_bf16 v[32:35], a[20:23], a[224:227], v[32:35]\n"
      "buffer_store_dwordx2 v[84:85], v15, s[36:39], s52 offen sc1\n"
      "v_cvt_pk_bf16_f32 v91, v78, v79\n"
      "v_mfma_f32_16x16x32_bf16 v[32:35], a[24:27], v[128:131], v[32:35]\n"
      "v_mfma_f32_16x16x32_bf16 v[32:35], a[28:31], a[228:231], v[32:35]\n"
      "ds_write_addtid_b32 v20 offset:47112\n"
      "v_mfma_f32_16x16x32_bf16 v[32:35], a[32:35], v[236:239], v[32:35]\n"
      "ds_write_addtid_b32 v21 offset:47368\n"
      "v_mfma_f32_16x16x32_bf16 v[32:35], a[0:3], v[144:147], v[32:35]\n"
      "buffer_store_dwordx2 v[90:91], v15, s[36:39], s52 offen offset:1536 sc1\n"
      "v_mfma_f32_16x16x32_bf16 v[32:35], a[4:7], v[200:203], v[32:35]\n"
      "v_mfma_f32_16x16x32_bf16 v[32:35], a[8:11], a[244:247], v[32:35]\n"
      "s_nop 1\n"
      "s_cbranch_vccnz Lconv_loop1\n"
      "Lconv_t1:\n"
      "s_add_u32 s52, s52, 196608\n"
      "s_nop 1\n"
      "v_mfma_f32_16x16x32_bf16 v[20:23], a[24:27], v[96:99], 0\n"
      "s_nop 0\n"
      "v_mfma_f32_16x16x32_bf16 v[20:23], a[28:31], v[152:155], v[20:23]\n"
      "v_cvt_pk_bf16_f32 v32, v32, v33\n"
      "v_cvt_pk_bf16_f32 v33, v34, v35\n"
      "v_mfma_f32_16x16x32_bf16 v[20:23], a[32:35], a[220:223], v[20:23]\n"
      "ds_write_addtid_b32 v32 offset:34816\n"
      "v_mfma_f32_16x16x32_bf16 v[20:23], a[0:3], v[116:119], v[20:23]\n"
      "ds_write_addtid_b32 v33 offset:35072\n"
      "v_mfma_f32_16x16x32_bf16 v[20:23], a[4:7], v[168:171], v[20:23]\n"
      "v_mfma_f32_16x16x32_bf16 v[20:23], a[8:11], v[224:227], v[20:23]\n"
      "v_mfma_f32_16x16x32_bf16 v[24:27], a[24:27], v[104:107], 0\n"
      "v_mfma_f32_16x16x32_bf16 v[24:27], a[28:31], v[156:159], v[24:27]\n"
      "v_mfma_f32_16x16x32_bf16 v[24:27], a[32:35], v[212:215], v[24:27]\n"
      "v_mfma_f32_16x16x32_bf16 v[24:27], a[0:3], v[120:123], v[24:27]\n"
      "v_mfma_f32_16x16x32_bf16 v[24:27], a[4:7], v[176:179], v[24:27]\n"
      "s_waitcnt lgkmcnt(0)\n"
      "s_barrier\n"
      "ds_read2_b64 a[60:63], v10 offset0:0 offset1:64\n"
      "v_mfma_f32_16x16x32_bf16 v[24:27], a[8:11], a[232:235], v[24:27]\n"
      "s_waitcnt lgkmcnt(0)\n"
      "ds_read2_b64 a[64:67], v10 offset0:1 offset1:65\n"
      "v_mfma_f32_16x16x32_bf16 v[68:71], a[84:87], a[60:63], 0\n"
      "ds_read2_b64 a[68:71], v10 offset0:2 offset1:66\n"
      "v_mfma_f32_16x16x32_bf16 v[28:31], a[24:27], v[108:111], 0\n"
      "ds_read_b128 a[12:15], v7 offset:6144\n"
      "v_mfma_f32_16x16x32_bf16 v[28:31], a[28:31], v[164:167], v[28:31]\n"
      "ds_read_b128 a[16:19], v7 offset:6400\n"
      "v_mfma_f32_16x16x32_bf16 v[28:31], a[32:35], a[224:227], v[28:31]\n"
      "ds_read_b128 a[20:23], v7 offset:6656\n"
      "v_mfma_f32_16x16x32_bf16 v[28:31], a[0:3], v[128:131], v[28:31]\n"
      "ds_read2_b64 a[60:63], v10 offset0:3 offset1:67\n"
      "v_cvt_pk_bf16_f32 v80, v68, v69\n"
      "v_mfma_f32_16x16x32_bf16 v[28:31], a[4:7], a[228:231], v[28:31]\n"
      "ds_read_b128 a[24:27], v6 offset:5376\n"
      "v_cvt_pk_bf16_f32 v86, v70, v71\n"
      "v_mfma_f32_16x16x32_bf16 v[28:31], a[8:11], v[236:239], v[28:31]\n"
      "s_waitcnt lgkmcnt(6)\n"
      "ds_read_b128 a[28:31], v6 offset:5632\n"
      "v_mfma_f32_16x16x32_bf16 v[72:75], a[84:87], a[64:67], 0\n"
      "s_waitcnt lgkmcnt(6)\n"
      "ds_read2_b64 a[64:67], v12 offset0:0 offset1:64\n"
      "v_mfma_f32_16x16x32_bf16 v[76:79], a[84:87], a[68:71], 0\n"
      "s_waitcnt lgkmcnt(6)\n"
      "ds_read2_b64 a[68:71], v12 offset0:1 offset1:65\n"
      "v_mfma_f32_16x16x32_bf16 v[20:23], a[12:15], v[132:135], v[20:23]\n"
      "s_waitcnt lgkmcnt(6)\n"
      "ds_read_b128 a[32:35], v6 offset:5888\n"
      "v_cvt_pk_bf16_f32 v81, v72, v73\n"
      "v_mfma_f32_16x16x32_bf16 v[20:23], a[16:19], v[188:191], v[20:23]\n"
      "s_waitcnt lgkmcnt(6)\n"
      "ds_read_b128 a[0:3], v7 offset:768\n"
      "v_cvt_pk_bf16_f32 v87, v74, v75\n"
      "v_mfma_f32_16x16x32_bf16 v[20:23], a[20:23], a[240:243], v[20:23]\n"
      "s_waitcnt lgkmcnt(6)\n"
      "ds_read_b128 a[4:7], v7 offset:1024\n"
      "v_cvt_pk_bf16_f32 v82, v76, v77\n"
      "v_mfma_f32_16x16x32_bf16 v[68:71], a[84:87], a[60:63], 0\n"
      "ds_read_b128 a[8:11], v7 offset:1280\n"
      "v_cvt_pk_bf16_f32 v88, v78, v79\n"
      "v_mfma_f32_16x16x32_bf16 v[24:27], a[12:15], v[140:143], v[24:27]\n"
      "v_mfma_f32_16x16x32_bf16 v[24:27], a[16:19], a[236:239], v[24:27]\n"
      "v_cvt_pk_bf16_f32 v20, v20, v21\n"
      "v_cvt_pk_bf16_f32 v21, v22, v23\n"
      "v_mfma_f32_16x16x32_bf16 v[24:27], a[20:23], v[248:251], v[24:27]\n"
      "s_waitcnt lgkmcnt(5)\n"
      "ds_write_addtid_b32 v20 offset:62480\n"
      "v_cvt_pk_bf16_f32 v83, v68, v69\n"
      "v_mfma_f32_16x16x32_bf16 v[72:75], a[84:87], a[64:67], 0\n"
      "ds_write_addtid_b32 v21 offset:62736\n"
      "v_cvt_pk_bf16_f32 v89, v70, v71\n"
      "v_mfma_f32_16x16x32_bf16 v[28:31], a[12:15], v[144:147], v[28:31]\n"
      "ds_read_b128 a[12:15], v7 offset:5376\n"
      "v_cvt_pk_bf16_f32 v24, v24, v25\n"
      "v_mfma_f32_16x16x32_bf16 v[28:31], a[16:19], v[200:203], v[28:31]\n"
      "ds_read_b128 a[16:19], v7 offset:5632\n"
      "v_cvt_pk_bf16_f32 v25, v26, v27\n"
      "v_mfma_f32_16x16x32_bf16 v[28:31], a[20:23], a[244:247], v[28:31]\n"
      "s_waitcnt lgkmcnt(5)\n"
      "ds_read_b128 a[20:23], v7 offset:5888\n"
      "v_cvt_pk_bf16_f32 v84, v72, v73\n"
      "v_mfma_f32_16x16x32_bf16 v[76:79], a[84:87], a[68:71], 0\n"
      "buffer_store_dwordx4 v[80:83], v14, s[36:39], s52 offen sc1\n"
      "v_cvt_pk_bf16_f32 v90, v74, v75\n"
      "v_mfma_f32_16x16x32_bf16 v[32:35], a[24:27], v[92:95], 0\n"
      "ds_write_addtid_b32 v24 offset:50184\n"
      "v_cvt_pk_bf16_f32 v28, v28, v29\n"
      "v_mfma_f32_16x16x32_bf16 v[32:35], a[28:31], v[148:151], v[32:35]\n"
      "buffer_store_dwordx4 v[86:89], v14, s[36:39], s52 offen offset:1536 sc1\n"
      "v_cvt_pk_bf16_f32 v29, v30, v31\n"
      "v_mfma_f32_16x16x32_bf16 v[32:35], a[32:35], v[204:207], v[32:35]\n"
      "ds_write_addtid_b32 v25 offset:50440\n"
      "v_cvt_pk_bf16_f32 v85, v76, v77\n"
      "v_mfma_f32_16x16x32_bf16 v[32:35], a[0:3], v[112:115], v[32:35]\n"
      "ds_write_addtid_b32 v28 offset:37888\n"
      "v_cvt_pk_bf16_f32 v91, v78, v79\n"
      "v_mfma_f32_16x16x32_bf16 v[32:35], a[4:7], a[208:211], v[32:35]\n"
      "s_waitcnt lgkmcnt(5)\n"
      "v_mfma_f32_16x16x32_bf16 v[32:35], a[8:11], v[220:223], v[32:35]\n"
      "ds_write_addtid_b32 v29 offset:38144\n"
      "v_mfma_f32_16x16x32_bf16 v[32:35], a[12:15], a[212:215], v[32:35]\n"
      "s_waitcnt lgkmcnt(4)\n"
      "v_mfma_f32_16x16x32_bf16 v[32:35], a[16:19], v[184:187], v[32:35]\n"
      "buffer_store_dwordx2 v[84:85], v15, s[36:39], s52 offen sc1\n"
      "v_mfma_f32_16x16x32_bf16 v[32:35], a[20:23], v[240:243], v[32:35]\n"
      "s_nop 1\n"
      "v_mfma_f32_16x16x32_bf16 v[20:23], a[24:27], v[100:103], 0\n"
      "buffer_store_dwordx2 v[90:91], v15, s[36:39], s52 offen offset:1536 sc1\n"
      "s_nop 0\n"
      "v_mfma_f32_16x16x32_bf16 v[20:23], a[28:31], a[200:203], v[20:23]\n"
      "s_nop 0\n"
      "v_mfma_f32_16x16x32_bf16 v[20:23], a[32:35], v[208:211], v[20:23]\n"
      "v_cvt_pk_bf16_f32 v32, v32, v33\n"
      "v_cvt_pk_bf16_f32 v33, v34, v35\n"
      "v_mfma_f32_16x16x32_bf16 v[20:23], a[0:3], a[204:207], v[20:23]\n"
      "ds_write_addtid_b32 v32 offset:61456\n"
      "v_mfma_f32_16x16x32_bf16 v[20:23], a[4:7], v[172:175], v[20:23]\n"
      "ds_write_addtid_b32 v33 offset:61712\n"
      "v_mfma_f32_16x16x32_bf16 v[20:23], a[8:11], v[228:231], v[20:23]\n"
      "v_mfma_f32_16x16x32_bf16 v[20:23], a[12:15], v[136:139], v[20:23]\n"
      "v_mfma_f32_16x16x32_bf16 v[20:23], a[16:19], v[192:195], v[20:23]\n"
      "v_mfma_f32_16x16x32_bf16 v[20:23], a[20:23], v[244:247], v[20:23]\n"
      "s_nop 1\n"
      "v_mfma_f32_16x16x32_bf16 v[24:27], a[24:27], a[196:199], 0\n"
      "ds_read_b128 a[24:27], v7 offset:4608\n"
      "s_nop 0\n"
      "v_mfma_f32_16x16x32_bf16 v[24:27], a[28:31], v[160:163], v[24:27]\n"
      "ds_read_b128 a[28:31], v7 offset:4864\n"
      "v_mfma_f32_16x16x32_bf16 v[24:27], a[32:35], v[216:219], v[24:27]\n"
      "ds_read_b128 a[32:35], v7 offset:5120\n"
      "v_cvt_pk_bf16_f32 v20, v20, v21\n"
      "v_mfma_f32_16x16x32_bf16 v[24:27], a[0:3], v[124:127], v[24:27]\n"
      "ds_read_b128 a[0:3], v6 offset:4608\n"
      "v_cvt_pk_bf16_f32 v21, v22, v23\n"
      "v_mfma_f32_16x16x32_bf16 v[24:27], a[4:7], v[180:183], v[24:27]\n"
      "ds_read_b128 a[4:7], v6 offset:4864\n"
      "v_mfma_f32_16x16x32_bf16 v[24:27], a[8:11], v[232:235], v[24:27]\n"
      "ds_read_b128 a[8:11], v6 offset:5120\n"
      "v_mfma_f32_16x16x32_bf16 v[24:27], a[12:15], a[216:219], v[24:27]\n"
      "ds_read_b128 a[12:15], v7 offset:0\n"
      "v_mfma_f32_16x16x32_bf16 v[24:27], a[16:19], v[196:199], v[24:27]\n"
      "ds_read_b128 a[16:19], v7 offset:256\n"
      "v_mfma_f32_16x16x32_bf16 v[24:27], a[20:23], v[252:255], v[24:27]\n"
      "s_waitcnt lgkmcnt(7)\n"
      "ds_read_b128 a[20:23], v7 offset:512\n"
      "s_nop 0\n"
      "v_mfma_f32_16x16x32_bf16 v[28:31], a[24:27], a[160:163], 0\n"
      "s_waitcnt lgkmcnt(7)\n"
      "ds_write_addtid_b32 v20 offset:49160\n"
      "s_nop 0\n"
      "v_mfma_f32_16x16x32_bf16 v[28:31], a[28:31], a[172:175], v[28:31]\n"
      "s_waitcnt lgkmcnt(7)\n"
      "ds_write_addtid_b32 v21 offset:49416\n"
      "v_cvt_pk_bf16_f32 v24, v24, v25\n"
      "v_mfma_f32_16x16x32_bf16 v[28:31], a[32:35], a[184:187], v[28:31]\n"
      "s_waitcnt lgkmcnt(7)\n"
      "v_cvt_pk_bf16_f32 v25, v26, v27\n"
      "v_mfma_f32_16x16x32_bf16 v[28:31], a[0:3], a[88:91], v[28:31]\n"
      "s_waitcnt lgkmcnt(6)\n"
      "ds_write_addtid_b32 v24 offset:36864\n"
      "v_mfma_f32_16x16x32_bf16 v[28:31], a[4:7], a[100:103], v[28:31]\n"
      "s_waitcnt lgkmcnt(6)\n"
      "v_mfma_f32_16x16x32_bf16 v[28:31], a[8:11], a[112:115], v[28:31]\n"
      "s_waitcnt lgkmcnt(5)\n"
      "ds_write_addtid_b32 v25 offset:37120\n"
      "v_mfma_f32_16x16x32_bf16 v[28:31], a[12:15], a[124:127], v[28:31]\n"
      "s_waitcnt lgkmcnt(5)\n"
      "v_mfma_f32_16x16x32_bf16 v[28:31], a[16:19], a[136:139], v[28:31]\n"
      "s_waitcnt lgkmcnt(4)\n"
      "v_mfma_f32_16x16x32_bf16 v[28:31], a[20:23], a[148:151], v[28:31]\n"
      "s_nop 1\n"
      "v_mfma_f32_16x16x32_bf16 v[20:23], a[24:27], a[164:167], 0\n"
      "s_nop 1\n"
      "v_mfma_f32_16x16x32_bf16 v[20:23], a[28:31], a[176:179], v[20:23]\n"
      "s_nop 0\n"
      "v_mfma_f32_16x16x32_bf16 v[20:23], a[32:35], a[188:191], v[20:23]\n"
      "v_cvt_pk_bf16_f32 v28, v28, v29\n"
      "v_cvt_pk_bf16_f32 v29, v30, v31\n"
      "v_mfma_f32_16x16x32_bf16 v[20:23], a[0:3], a[92:95], v[20:23]\n"
      "ds_write_addtid_b32 v28 offset:60432\n"
      "v_mfma_f32_16x16x32_bf16 v[20:23], a[4:7], a[104:107], v[20:23]\n"
      "ds_write_addtid_b32 v29 offset:60688\n"
      "v_mfma_f32_16x16x32_bf16 v[20:23], a[8:11], a[116:119], v[20:23]\n"
      "v_mfma_f32_16x16x32_bf16 v[20:23], a[12:15], a[128:131], v[20:23]\n"
      "v_mfma_f32_16x16x32_bf16 v[20:23], a[16:19], a[140:143], v[20:23]\n"
      "v_mfma_f32_16x16x32_bf16 v[20:23], a[20:23], a[152:155], v[20:23]\n"
      "s_nop 1\n"
      "v_mfma_f32_16x16x32_bf16 v[32:35], a[24:27], a[168:171], 0\n"
      "s_nop 1\n"
      "v_mfma_f32_16x16x32_bf16 v[32:35], a[28:31], a[180:183], v[32:35]\n"
      "s_nop 0\n"
      "v_mfma_f32_16x16x32_bf16 v[32:35], a[32:35], a[192:195], v[32:35]\n"
      "v_cvt_pk_bf16_f32 v20, v20, v21\n"
      "v_cvt_pk_bf16_f32 v21, v22, v23\n"
      "v_mfma_f32_16x16x32_bf16 v[32:35], a[0:3], a[96:99], v[32:35]\n"
      "ds_write_addtid_b32 v20 offset:48136\n"
      "v_mfma_f32_16x16x32_bf16 v[32:35], a[4:7], a[108:111], v[32:35]\n"
      "ds_write_addtid_b32 v21 offset:48392\n"
      "v_mfma_f32_16x16x32_bf16 v[32:35], a[8:11], a[120:123], v[32:35]\n"
      "v_mfma_f32_16x16x32_bf16 v[32:35], a[12:15], a[132:135], v[32:35]\n"
      "v_mfma_f32_16x16x32_bf16 v[32:35], a[16:19], a[144:147], v[32:35]\n"
      "v_mfma_f32_16x16x32_bf16 v[32:35], a[20:23], a[156:159], v[32:35]\n"
      "s_nop 1\n"
      "s_nop 5\n"
      "v_cvt_pk_bf16_f32 v32, v32, v33\n"
      "v_cvt_pk_bf16_f32 v33, v34, v35\n"
      "ds_write_addtid_b32 v32 offset:35840\n"
      "ds_write_addtid_b32 v33 offset:36096\n"
      "s_waitcnt lgkmcnt(0)\n"
      "s_barrier\n"
      "ds_read2_b64 a[0:3], v11 offset0:0 offset1:64\n"
      "ds_read2_b64 a[4:7], v11 offset0:1 offset1:65\n"
      "ds_read2_b64 a[8:11], v11 offset0:2 offset1:66\n"
      "ds_read2_b64 a[12:15], v11 offset0:3 offset1:67\n"
      "ds_read2_b64 a[16:19], v13 offset0:0 offset1:64\n"
      "ds_read2_b64 a[20:23], v13 offset0:1 offset1:65\n"
      "s_waitcnt lgkmcnt(5)\n"
      "v_mfma_f32_16x16x32_bf16 v[68:71], a[84:87], a[0:3], 0\n"
      "s_waitcnt lgkmcnt(4)\n"
      "v_mfma_f32_16x16x32_bf16 v[72:75], a[84:87], a[4:7], 0\n"
      "s_waitcnt lgkmcnt(3)\n"
      "v_mfma_f32_16x16x32_bf16 v[76:79], a[84:87], a[8:11], 0\n"
      "s_waitcnt lgkmcnt(2)\n"
      "s_nop 2\n"
      "v_cvt_pk_bf16_f32 v80, v68, v69\n"
      "v_cvt_pk_bf16_f32 v86, v70, v71\n"
      "v_mfma_f32_16x16x32_bf16 v[68:71], a[84:87], a[12:15], 0\n"
      "s_waitcnt lgkmcnt(1)\n"
      "v_cvt_pk_bf16_f32 v81, v72, v73\n"
      "v_cvt_pk_bf16_f32 v87, v74, v75\n"
      "v_cvt_pk_bf16_f32 v82, v76, v77\n"
      "v_cvt_pk_bf16_f32 v88, v78, v79\n"
      "v_mfma_f32_16x16x32_bf16 v[72:75], a[84:87], a[16:19], 0\n"
      "s_waitcnt lgkmcnt(0)\n"
      "v_mfma_f32_16x16x32_bf16 v[76:79], a[84:87], a[20:23], 0\n"
      "s_add_u32 s52, s52, 196608\n"
      "v_cvt_pk_bf16_f32 v83, v68, v69\n"
      "v_cvt_pk_bf16_f32 v89, v70, v71\n"
      "buffer_store_dwordx4 v[80:83], v14, s[36:39], s52 offen sc1\n"
      "buffer_store_dwordx4 v[86:89], v14, s[36:39], s52 offen offset:1536 sc1\n"
      "s_nop 0\n"
      "v_cvt_pk_bf16_f32 v84, v72, v73\n"
      "v_cvt_pk_bf16_f32 v90, v74, v75\n"
      "v_cvt_pk_bf16_f32 v85, v76, v77\n"
      "v_cvt_pk_bf16_f32 v91, v78, v79\n"
      "buffer_store_dwordx2 v[84:85], v15, s[36:39], s52 offen sc1\n"
      "buffer_store_dwordx2 v[90:91], v15, s[36:39], s52 offen offset:1536 sc1\n"
      "Lconv_u1:\n"
      "Lconv_tail:\n"
      "s_waitcnt vmcnt(0) lgkmcnt(0)\n"
      "Lconv_end:\n"
      :
      : "v"(threadIdx.x), "s"(blockIdx.x), "s"(__builtin_amdgcn_kernarg_segment_ptr())
      : "v1", "v2", "v3", "v4", "v5", "v6", "v7", "v8", "v9", "v10", "v11", "v12", "v13", "v14", "v15", "v16", "v17", "v18", "v19", "v20", "v21", "v22", "v23", "v24", "v25", "v26", "v27", "v28", "v29", "v30", "v31", "v32", "v33", "v34", "v35", "v36", "v37", "v38", "v39", "v40", "v41", "v42", "v43", "v44", "v45", "v46", "v47", "v48", "v49", "v50", "v51", "v52", "v53", "v54", "v55", "v56", "v57", "v58", "v59", "v60", "v61", "v62", "v63", "v64", "v65", "v66", "v67", "v68", "v69", "v70", "v71", "v72", "v73", "v74", "v75", "v76", "v77", "v78", "v79", "v80", "v81", "v82", "v83", "v84", "v85", "v86", "v87", "v88", "v89", "v90", "v91", "v92", "v93", "v94", "v95", "v96", "v97", "v98", "v99", "v100", "v101", "v102", "v103", "v104", "v105", "v106", "v107", "v108", "v109", "v110", "v111", "v112", "v113", "v114", "v115", "v116", "v117", "v118", "v119", "v120", "v121", "v122", "v123", "v124", "v125", "v126", "v127", "v128", "v129", "v130", "v131", "v132", "v133", "v134", "v135", "v136", "v137", "v138", "v139", "v140", "v141", "v142", "v143", "v144", "v145", "v146", "v147", "v148", "v149", "v150", "v151", "v152", "v153", "v154", "v155", "v156", "v157", "v158", "v159", "v160", "v161", "v162", "v163", "v164", "v165", "v166", "v167", "v168", "v169", "v170", "v171", "v172", "v173", "v174", "v175", "v176", "v177", "v178", "v179", "v180", "v181", "v182", "v183", "v184", "v185", "v186", "v187", "v188", "v189", "v190", "v191", "v192", "v193", "v194", "v195", "v196", "v197", "v198", "v199", "v200", "v201", "v202", "v203", "v204", "v205", "v206", "v207", "v208", "v209", "v210", "v211", "v212", "v213", "v214", "v215", "v216", "v217", "v218", "v219", "v220", "v221", "v222", "v223", "v224", "v225", "v226", "v227", "v228", "v229", "v230", "v231", "v232", "v233", "v234", "v235", "v236", "v237", "v238", "v239", "v240", "v241", "v242", "v243", "v244", "v245", "v246", "v247", "v248", "v249", "v250", "v251", "v252", "v253", "v254", "v255", "a0", "a1", "a2", "a3", "a4", "a5", "a6", "a7", "a8", "a9", "a10", "a11", "a12", "a13", "a14", "a15", "a16", "a17", "a18", "a19", "a20", "a21", "a22", "a23", "a24", "a25", "a26", "a27", "a28", "a29", "a30", "a31", "a32", "a33", "a34", "a35", "a36", "a37", "a38", "a39", "a40", "a41", "a42", "a43", "a44", "a45", "a46", "a47", "a48", "a49", "a50", "a51", "a52", "a53", "a54", "a55", "a56", "a57", "a58", "a59", "a60", "a61", "a62", "a63", "a64", "a65", "a66", "a67", "a68", "a69", "a70", "a71", "a72", "a73", "a74", "a75", "a76", "a77", "a78", "a79", "a80", "a81", "a82", "a83", "a84", "a85", "a86", "a87", "a88", "a89", "a90", "a91", "a92", "a93", "a94", "a95", "a96", "a97", "a98", "a99", "a100", "a101", "a102", "a103", "a104", "a105", "a106", "a107", "a108", "a109", "a110", "a111", "a112", "a113", "a114", "a115", "a116", "a117", "a118", "a119", "a120", "a121", "a122", "a123", "a124", "a125", "a126", "a127", "a128", "a129", "a130", "a131", "a132", "a133", "a134", "a135", "a136", "a137", "a138", "a139", "a140", "a141", "a142", "a143", "a144", "a145", "a146", "a147", "a148", "a149", "a150", "a151", "a152", "a153", "a154", "a155", "a156", "a157", "a158", "a159", "a160", "a161", "a162", "a163", "a164", "a165", "a166", "a167", "a168", "a169", "a170", "a171", "a172", "a173", "a174", "a175", "a176", "a177", "a178", "a179", "a180", "a181", "a182", "a183", "a184", "a185", "a186", "a187", "a188", "a189", "a190", "a191", "a192", "a193", "a194", "a195", "a196", "a197", "a198", "a199", "a200", "a201", "a202", "a203", "a204", "a205", "a206", "a207", "a208", "a209", "a210", "a211", "a212", "a213", "a214", "a215", "a216", "a217", "a218", "a219", "a220", "a221", "a222", "a223", "a224", "a225", "a226", "a227", "a228", "a229", "a230", "a231", "a232", "a233", "a234", "a235", "a236", "a237", "a238", "a239", "a240", "a241", "a242", "a243", "a244", "a245", "a246", "a247", "a248", "a249", "a250", "a251", "a252", "a253", "a254", "a255", "s16", "s17", "s18", "s19", "s20", "s21", "s22", "s23", "s24", "s25", "s26", "s27", "s28", "s29", "s30", "s31", "s34", "s35", "s36", "s37", "s38", "s39", "s40", "s41", "s42", "s43", "s44", "s45", "s46", "s47", "s48", "s49", "s50", "s51", "s52", "s53", "s54", "s55", "s56", "s57", "s58", "s59", "s60", "s61", "s62", "s63", "s64", "s65", "s66", "s67", "s68", "s69", "s70", "s71", "s72", "s73", "s74", "s75", "s76", "s77", "s78", "s79", "s80", "s81", "s82", "s83", "s84", "s85", "s86", "s87", "s88", "s89", "s90", "s91", "s92", "s93", "s94", "s95", "s96", "s97", "s98", "s99", "scc", "vcc", "memory");
}

extern "C" void kernel_launch(void* const* d_in, const int* in_sizes, int n_in, void* d_out,
                              int out_size, void* d_ws, size_t ws_size, hipStream_t stream) {
  conv_kernel<<<256, 256, 0, stream>>>((const char*)d_in[0], (const char*)d_in[1], (char*)d_out, (char*)d_ws);
}
```

```cpp
#include <hip/hip_runtime.h>
#include <hip/hip_bf16.h>
#include <stdint.h>

__global__ void __launch_bounds__(256, 1) __attribute__((aligned(4096)))
conv_kernel(const char* __restrict__ x, const char* __restrict__ w, char* __restrict__ out, char* __restrict__ dbg) {
  __shared__ __attribute__((aligned(1024))) char smem[163840];
  asm volatile("" :: "v"((unsigned)(uintptr_t)smem) : "memory");
  asm volatile(
      "s_load_dwordx8 s[20:27], %2, 0x0\n"
      "v_readfirstlane_b32 s40, %0\n"
      "v_and_b32 v5, 63, %0\n"
      "v_lshlrev_b32 v8, 4, v5\n"
      "s_lshr_b32 s40, s40, 6\n"
      "s_lshl_b32 s43, s40, 3\n"
      "s_or_b32 s43, s43, 0x80000\n"
      "v_bitop3_b32 v9, v8, v5, 32 bitop3:0x78\n"
      "v_xor_b32 v89, 32, v8\n"
      "s_mul_i32 s92, s40, 3072\n"
      "s_lshr_b32 s41, s40, 1\n"
      "s_and_b32 s42, s40, 1\n"
      "s_mov_b32 s30, 165888\n"
      "s_mov_b32 s31, 0x20000\n"
      "s_lshr_b32 s49, %1, 3\n"
      "s_lshl2_add_u32 s49, s49, s40\n"
      "v_lshlrev_b32 v36, 7, v5\n"
      "s_mul_i32 s50, s49, 384\n"
      "v_min_u32 v37, 256, v36\n"
      "v_add_u32 v37, s50, v37\n"
      "v_min_u32 v66, 1280, v36\n"
      "s_mul_i32 s96, s49, 1408\n"
      "s_getpc_b64 s[54:55]\n"
      "Lconv_start:\n"
      "v_min_u32 v37, Lconv_end-Lconv_start-4, v37\n"
      "s_mov_b32 m0, 162816\n"
      "s_nop 0\n"
      "global_load_lds_dword v37, s[54:55]\n"
      "s_bfe_u32 s99, %1, 0x30003\n"
      "s_mul_i32 s49, s40, 192\n"
      "v_add_u32 v36, s49, v5\n"
      "v_add_u32 v38, 128, v36\n"
      "v_mul_u32_u24 v37, 669, v36\n"
      "v_lshrrev_b32 v37, 16, v37\n"
      "v_mad_i32_i24 v37, v37, -2, v38\n"
      "v_lshlrev_b32 v16, 4, v37\n"
      "v_add_u32 v36, 64, v36\n"
      "v_mul_u32_u24 v37, 669, v36\n"
      "v_lshrrev_b32 v37, 16, v37\n"
      "v_mad_i32_i24 v37, v37, -2, v38\n"
      "v_lshlrev_b32 v17, 4, v37\n"
      "v_add_u32 v36, 64, v36\n"
      "v_mul_u32_u24 v37, 669, v36\n"
      "v_lshrrev_b32 v37, 16, v37\n"
      "v_mad_i32_i24 v37, v37, -2, v38\n"
      "v_lshlrev_b32 v18, 4, v37\n"
      "s_or_b32 s50, s99, s40\n"
      "s_cmp_eq_u32 s50, 0\n"
      "s_cselect_b32 s16, 0xffffff, 0\n"
      "s_mov_b32 s17, 0\n"
      "s_cmp_eq_u32 s99, 7\n"
      "s_cselect_b32 s19, 0xffffff00, 0\n"
      "s_mov_b32 s18, 0\n"
      "v_add_u32 v19, 14080, v8\n"
      "v_cndmask_b32 v16, v16, 2.0, s[16:17]\n"
      "v_cndmask_b32 v19, v19, 2.0, s[18:19]\n"
      "v_and_b32 v36, 16, v8\n"
      "v_mov_b32 v52, 0xffff\n"
      "v_lshlrev_b32 v52, v36, v52\n"
      "s_mov_b32 s16, 0x3000003\n"
      "s_mov_b32 s17, 0x3000003\n"
      "v_cndmask_b32 v48, 0, v52, s[16:17]\n"
      "s_lshl_b64 s[16:17], s[16:17], 2\n"
      "v_cndmask_b32 v49, 0, v52, s[16:17]\n"
      "s_lshl_b64 s[16:17], s[16:17], 2\n"
      "v_cndmask_b32 v50, 0, v52, s[16:17]\n"
      "s_lshl_b64 s[16:17], s[16:17], 2\n"
      "v_cndmask_b32 v51, 0, v52, s[16:17]\n"
      "v_bfe_u32 v53, v5, 5, 1\n"
      "v_xor_b32 v53, s41, v53\n"
      "v_sub_u32 v53, 0, v53\n"
      "v_and_b32 v36, 0x1000100, v53\n"
      "v_xor_b32 v36, 0x3fa83fa8, v36\n"
      "v_and_b32 v68, v36, v48\n"
      "v_and_b32 v69, v36, v49\n"
      "v_and_b32 v70, v36, v50\n"
      "v_and_b32 v71, v36, v51\n"
      "v_xor_b32 v72, 0x80008000, v68\n"
      "v_xor_b32 v73, 0x80008000, v69\n"
      "v_xor_b32 v74, 0x80008000, v70\n"
      "v_xor_b32 v75, 0x80008000, v71\n"
      "v_bfe_i32 v36, v5, 5, 1\n"
      "v_and_b32 v36, 0x3f283f28, v36\n"
      "v_xor_b32 v37, 0x3f283f28, v36\n"
      "v_and_b32 v76, v37, v48\n"
      "v_and_b32 v77, v37, v49\n"
      "v_and_b32 v78, v37, v50\n"
      "v_and_b32 v79, v37, v51\n"
      "v_and_b32 v80, v36, v48\n"
      "v_and_b32 v81, v36, v49\n"
      "v_and_b32 v82, v36, v50\n"
      "v_and_b32 v83, v36, v51\n"
      "s_mul_i32 s50, s42, 96\n"
      "v_bfe_u32 v36, v5, 1, 3\n"
      "v_and_b32 v37, 1, v5\n"
      "v_lshl_add_u32 v36, v37, 3, v36\n"
      "v_mul_u32_u24 v36, 192, v36\n"
      "v_add_u32 v36, 46144, v36\n"
      "v_lshlrev_b32 v37, 5, v37\n"
      "v_and_b32 v38, 16, v5\n"
      "v_add_u32 v38, s50, v38\n"
      "v_and_b32 v39, 32, v5\n"
      "v_mul_u32_u24 v40, 192, v39\n"
      "v_add_u32 v40, v40, v36\n"
      "v_xad_u32 v84, v38, v37, v40\n"
      "v_add_u32 v41, 32, v38\n"
      "v_xad_u32 v85, v41, v37, v40\n"
      "v_add_u32 v41, v39, v38\n"
      "v_xad_u32 v86, v41, v37, v36\n"
      "s_mul_i32 s49, s41, 6144\n"
      "s_add_u32 s49, s49, 46144\n"
      "v_lshrrev_b32 v36, 3, v5\n"
      "v_and_b32 v38, 4, v5\n"
      "v_lshl_add_u32 v36, v38, 1, v36\n"
      "v_mul_u32_u24 v36, 192, v36\n"
      "v_lshlrev_b32 v37, 3, v5\n"
      "v_and_b32 v38, 8, v37\n"
      "v_add3_u32 v36, v36, v38, s49\n"
      "v_and_b32 v38, 32, v37\n"
      "v_and_b32 v37, 16, v37\n"
      "v_add_u32 v37, s50, v37\n"
      "v_xad_u32 v87, v37, v38, v36\n"
      "v_add_u32 v39, 32, v37\n"
      "v_xad_u32 v88, v39, v38, v36\n"
      "s_waitcnt lgkmcnt(0)\n"
      "s_mov_b32 s28, s22\n"
      "s_and_b32 s29, s23, 0xffff\n"
      "buffer_load_dword v66, s[28:31], s96 offen lds\n"
      "s_add_u32 m0, s92, 73824\n"
      "s_bfe_u32 s97, 0x120c0600, s43\n"
      "s_mul_i32 s97, s97, 3072\n"
      "buffer_load_dwordx4 v8, s[28:31], s97 offen offset:0 lds\n"
      "buffer_load_dwordx4 v9, s[28:31], s97 offen offset:1024 lds\n"
      "buffer_load_dwordx4 v89, s[28:31], s97 offen offset:2048 lds\n"
      "s_add_u32 m0, s92, 86112\n"
      "s_bfe_u32 s97, 0x2a241e18, s43\n"
      "s_mul_i32 s97, s97, 3072\n"
      "buffer_load_dwordx4 v8, s[28:31], s97 offen offset:0 lds\n"
      "buffer_load_dwordx4 v9, s[28:31], s97 offen offset:1024 lds\n"
      "buffer_load_dwordx4 v89, s[28:31], s97 offen offset:2048 lds\n"
      "s_and_b32 s53, %1, 7\n"
      "s_lshl_b32 s53, s53, 6\n"
      "s_bfe_u32 s50, %1, 0x10006\n"
      "s_add_u32 s53, s53, s50\n"
      "s_lshr_b32 s50, %1, 7\n"
      "s_lshl_b32 s50, s50, 5\n"
      "s_add_u32 s53, s53, s50\n"
      "s_mul_i32 s50, s99, 12288\n"
      "s_sub_u32 s50, s50, 2432\n"
      "s_ashr_i32 s49, s50, 31\n"
      "s_add_u32 s32, s20, s50\n"
      "s_addc_u32 s33, s21, s49\n"
      "s_and_b32 s33, s33, 0xffff\n"
      "s_add_u32 s34, s53, 33\n"
      "s_min_u32 s34, s34, 512\n"
      "s_mul_i32 s34, s34, 98304\n"
      "s_sub_u32 s34, s34, s50\n"
      "s_mov_b32 s35, 0x20000\n"
      "s_sub_u32 s47, s53, 2\n"
      "s_mul_i32 s47, s47, 98304\n"
      "s_mul_i32 s46, s40, 3072\n"
      "s_add_u32 s46, s46, 110720\n"
      "s_mul_i32 s99, s99, 12288\n"
      "s_mul_i32 s50, s53, 98304\n"
      "s_add_u32 s50, s50, s99\n"
      "s_mul_i32 s49, s40, 3072\n"
      "s_add_u32 s50, s50, s49\n"
      "s_add_u32 s36, s24, s50\n"
      "s_addc_u32 s37, s25, 0\n"
      "s_and_b32 s37, s37, 0xffff\n"
      "s_sub_u32 s38, 50331648, s50\n"
      "s_mov_b32 s39, 0x20000\n"
      "v_lshrrev_b32 v36, 1, v5\n"
      "v_and_b32 v37, 4, v36\n"
      "v_and_or_b32 v37, v5, 2, v37\n"
      "v_mul_u32_u24 v37, 1568, v37\n"
      "v_xor_b32 v36, v36, v5\n"
      "v_and_b32 v36, 4, v36\n"
      "v_mad_u32_u24 v37, v36, 48, v37\n"
      "s_mul_i32 s50, s42, 1568\n"
      "s_mov_b64 exec, 0\n"
      "s_cmp_eq_u32 s40, 0\n"
      "s_cbranch_scc1 Lconv_p1\n"
      "s_cmp_eq_u32 s40, 1\n"
      "s_cbranch_scc1 Lconv_p2\n"
      "s_cmp_eq_u32 s40, 2\n"
      "s_cbranch_scc1 Lconv_pa0\n"
      "s_cmp_eq_u32 s40, 3\n"
      "s_cbranch_scc1 Lconv_pa1\n"
      "Lconv_r0:\n"
      "s_mov_b64 exec, -1\n"
      "s_add_u32 m0, s92, 98400\n"
      "s_bfe_u32 s97, 0xd070130, s43\n"
      "s_mul_i32 s97, s97, 3072\n"
      "buffer_load_dwordx4 v8, s[28:31], s97 offen offset:0 lds\n"
      "buffer_load_dwordx4 v9, s[28:31], s97 offen offset:1024 lds\n"
      "buffer_load_dwordx4 v89, s[28:31], s97 offen offset:2048 lds\n"
      "s_add_u32 s50, s50, 110720\n"
      "v_and_b32 v36, 16, v5\n"
      "v_lshl_add_u32 v37, v36, 1, v37\n"
      "v_and_b32 v36, 16, v8\n"
      "v_add3_u32 v37, v37, v36, s50\n"
      "v_and_b32 v36, 32, v5\n"
      "v_mad_u32_u24 v38, v36, 24, v37\n"
      "v_mad_u32_u24 v39, v36, 25, v37\n"
      "s_mul_i32 s17, s42, 0xcc00cc00\n"
      "s_mov_b32 s16, 0\n"
      "v_mov_b32 v36, 768\n"
      "v_mov_b32 v37, 736\n"
      "v_cndmask_b32 v36, v36, v37, s[16:17]\n"
      "v_add_u32 v39, v39, v36\n"
      "s_cmp_eq_u32 s41, 0\n"
      "s_cselect_b64 vcc, -1, 0\n"
      "v_add_u32 v40, 384, v38\n"
      "v_add_u32 v41, 384, v39\n"
      "v_cndmask_b32 v2, v40, v39, vcc\n"
      "v_cndmask_b32 v3, v39, v40, vcc\n"
      "v_cndmask_b32 v4, v41, v38, vcc\n"
      "v_and_b32 v36, 3, v5\n"
      "v_mul_u32_u24 v36, 18464, v36\n"
      "v_bfe_u32 v37, v5, 2, 2\n"
      "v_lshrrev_b32 v38, 1, v37\n"
      "v_xor_b32 v37, v37, v38\n"
      "v_lshl_add_u32 v36, v37, 6, v36\n"
      "s_mul_i32 s50, s41, 2304\n"
      "v_and_b32 v37, 48, v5\n"
      "v_add3_u32 v6, v36, v37, s50\n"
      "v_add_u32 v7, 9216, v6\n"
      "s_mov_b32 s16, 0xc0003\n"
      "s_mov_b32 s17, 0xc0003\n"
      "v_cndmask_b32 v48, 0, v52, s[16:17]\n"
      "s_lshl_b64 s[16:17], s[16:17], 4\n"
      "v_cndmask_b32 v49, 0, v52, s[16:17]\n"
      "s_lshl_b64 s[16:17], s[16:17], 4\n"
      "v_cndmask_b32 v50, 0, v52, s[16:17]\n"
      "s_lshl_b64 s[16:17], s[16:17], 4\n"
      "v_cndmask_b32 v51, 0, v52, s[16:17]\n"
      "v_and_b32 v36, 0xff00ff00, v53\n"
      "v_xor_b32 v36, 0xc080c080, v36\n"
      "v_and_b32 v38, v36, v48\n"
      "v_and_b32 v39, v36, v49\n"
      "v_and_b32 v40, v36, v50\n"
      "v_and_b32 v41, v36, v51\n"
      "v_accvgpr_write_b32 a72, v38\n"
      "v_accvgpr_write_b32 a73, v39\n"
      "v_accvgpr_write_b32 a74, v40\n"
      "v_accvgpr_write_b32 a75, v41\n"
      "v_and_b32 v36, 0xff00ff00, v53\n"
      "v_xor_b32 v36, 0xc05dc05d, v36\n"
      "v_and_b32 v38, v36, v48\n"
      "v_and_b32 v39, v36, v49\n"
      "v_and_b32 v40, v36, v50\n"
      "v_and_b32 v41, v36, v51\n"
      "v_accvgpr_write_b32 a76, v38\n"
      "v_accvgpr_write_b32 a77, v39\n"
      "v_accvgpr_write_b32 a78, v40\n"
      "v_accvgpr_write_b32 a79, v41\n"
      "v_and_b32 v36, 0x81008100, v53\n"
      "v_xor_b32 v36, 0x3f803f80, v36\n"
      "v_and_b32 v38, v36, v48\n"
      "v_and_b32 v39, v36, v49\n"
      "v_and_b32 v40, v36, v50\n"
      "v_and_b32 v41, v36, v51\n"
      "v_accvgpr_write_b32 a80, v38\n"
      "v_accvgpr_write_b32 a81, v39\n"
      "v_accvgpr_write_b32 a82, v40\n"
      "v_accvgpr_write_b32 a83, v41\n"
      "s_mov_b32 s44, 0x3fdd0000\n"
      "v_lshlrev_b32 v36, 5, v5\n"
      "v_and_b32 v36, 0x180, v36\n"
      "v_lshlrev_b32 v37, 3, v5\n"
      "v_and_b32 v37, 16, v37\n"
      "v_sub_u32 v38, 0x3e71, v36\n"
      "v_add_u32 v39, 0x3cf1, v36\n"
      "v_mov_b32 v40, 0x3f80\n"
      "s_mov_b64 vcc, 0xffff\n"
      "s_mov_b32 s16, 0xffff0000\n"
      "s_mov_b32 s17, 0xf0f0\n"
      "v_xor_b32 v41, 0x8000, v38\n"
      "v_cndmask_b32 v38, v38, v41, s[16:17]\n"
      "v_cndmask_b32 v38, v38, v40, vcc\n"
      "v_lshlrev_b32 v38, v37, v38\n"
      "v_xor_b32 v41, 0x8000, v39\n"
      "s_xor_b32 s17, s17, 0xffff\n"
      "v_cndmask_b32 v39, v39, v41, s[16:17]\n"
      "v_cndmask_b32 v39, v39, v40, vcc\n"
      "v_lshlrev_b32 v39, v37, v39\n"
      "s_mov_b32 s18, 0x55550005\n"
      "s_mov_b32 s19, 0x5555\n"
      "v_cndmask_b32 v42, 0, v38, s[18:19]\n"
      "s_lshl_b64 s[16:17], s[18:19], 1\n"
      "v_cndmask_b32 v43, 0, v38, s[16:17]\n"
      "v_accvgpr_write_b32 a84, v42\n"
      "v_accvgpr_write_b32 a85, v43\n"
      "s_mov_b32 s18, 0x55555000\n"
      "v_cndmask_b32 v42, 0, v39, s[18:19]\n"
      "s_lshl_b64 s[16:17], s[18:19], 1\n"
      "v_cndmask_b32 v43, 0, v39, s[16:17]\n"
      "v_accvgpr_write_b32 a86, v42\n"
      "v_accvgpr_write_b32 a87, v43\n"
      "v_lshrrev_b32 v36, 4, v5\n"
      "v_min_u32 v37, 2, v36\n"
      "v_lshlrev_b32 v37, 10, v37\n"
      "s_lshl_b32 s50, s41, 7\n"
      "s_lshl_b32 s49, s42, 8\n"
      "s_add_u32 s50, s50, s49\n"
      "s_add_u32 s50, s50, 98416\n"
      "v_and_b32 v38, 8, v5\n"
      "v_lshl_add_u32 v37, v38, 3, v37\n"
      "v_add_u32 v37, s50, v37\n"
      "s_movk_i32 s50, -12296\n"
      "s_mov_b32 s49, 43040\n"
      "v_mov_b32 v42, s49\n"
      "v_bfe_u32 v39, v5, 1, 2\n"
      "v_lshrrev_b32 v40, 2, v38\n"
      "v_add_u32 v39, v39, v40\n"
      "v_lshlrev_b32 v40, 5, v5\n"
      "v_and_b32 v40, 32, v40\n"
      "v_cmp_lt_u32 vcc, 2, v39\n"
      "v_mad_i32_i24 v39, v39, s50, v37\n"
      "v_add_u32 v39, v39, v40\n"
      "v_cndmask_b32 v41, 0, v42, vcc\n"
      "v_add_u32 v10, v39, v41\n"
      "v_add_u32 v11, 3072, v10\n"
      "v_bfe_u32 v39, v5, 2, 1\n"
      "v_lshrrev_b32 v40, 1, v38\n"
      "v_sub_u32 v39, v39, v40\n"
      "v_add_u32 v39, 4, v39\n"
      "v_and_b32 v40, 48, v8\n"
      "v_cmp_lt_u32 vcc, 2, v39\n"
      "v_mad_i32_i24 v39, v39, s50, v37\n"
      "v_add_u32 v39, v39, v40\n"
      "v_cndmask_b32 v41, 0, v42, vcc\n"
      "v_add_u32 v12, v39, v41\n"
      "v_add_u32 v13, 3072, v12\n"
      "v_mul_u32_u24 v36, 384, v36\n"
      "v_and_b32 v37, 15, v5\n"
      "v_lshl_add_u32 v39, v37, 3, v36\n"
      "v_add_u32 v15, 128, v39\n"
      "v_add_lshl_u32 v37, v37, v38, 4\n"
      "v_add_u32 v14, v37, v36\n"
      "s_mul_i32 s45, s42, 6152\n"
      "s_lshl_b32 s50, s41, 9\n"
      "s_add_u32 s45, s45, s50\n"
      "s_add_u32 s45, s45, 41056\n"
      "s_mov_b32 s98, 0\n"
      "s_mov_b32 s91, 26560\n"
      "s_mul_i32 s18, s42, 18464\n"
      "s_mul_i32 s50, s41, 2304\n"
      "s_add_u32 s18, s18, s50\n"
      "s_lshl1_add_u32 s19, s18, 9216\n"
      "s_add_u32 s58, s46, 26560\n"
      "s_lshl1_add_u32 s93, s46, 26560\n"
      "s_mul_i32 s59, s42, -13280\n"
      "s_add_u32 s59, s59, 136256\n"
      "s_lshl1_add_u32 s94, s59, 26560\n"
      "s_mul_i32 s50, s42, 26560\n"
      "s_add_u32 s59, s59, s50\n"
      "s_mul_i32 s95, s42, 196608\n"
      "s_sub_u32 s95, 196608, s95\n"
      "s_mov_b32 s52, 4294770688\n"
      "s_cmp_lt_u32 s40, 3\n"
      "s_cselect_b32 s97, 27680, 0\n"
      "s_add_u32 s97, s97, s92\n"
      "s_add_u32 m0, s97, 36928\n"
      "s_bfe_u32 s97, 0x251f1913, s43\n"
      "s_mul_i32 s97, s97, 3072\n"
      "buffer_load_dwordx4 v8, s[28:31], s97 offen offset:0 lds\n"
      "buffer_load_dwordx4 v9, s[28:31], s97 offen offset:1024 lds\n"
      "buffer_load_dwordx4 v89, s[28:31], s97 offen offset:2048 lds\n"
      "s_cmp_lt_u32 s40, 2\n"
      "s_cselect_b32 s97, 27680, 0\n"
      "s_add_u32 s97, s97, s92\n"
      "s_add_u32 m0, s97, 21536\n"
      "s_bfe_u32 s97, 0x802312b, s43\n"
      "s_mul_i32 s97, s97, 3072\n"
      "buffer_load_dwordx4 v8, s[28:31], s97 offen offset:0 lds\n"
      "buffer_load_dwordx4 v9, s[28:31], s97 offen offset:1024 lds\n"
      "buffer_load_dwordx4 v89, s[28:31], s97 offen offset:2048 lds\n"
      "s_cmp_lt_u32 s40, 1\n"
      "s_cselect_b32 s97, 27680, 0\n"
      "s_add_u32 s97, s97, s92\n"
      "s_add_u32 m0, s97, 6144\n"
      "s_bfe_u32 s97, 0x201a140e, s43\n"
      "s_mul_i32 s97, s97, 3072\n"
      "buffer_load_dwordx4 v8, s[28:31], s97 offen offset:0 lds\n"
      "buffer_load_dwordx4 v9, s[28:31], s97 offen offset:1024 lds\n"
      "buffer_load_dwordx4 v89, s[28:31], s97 offen offset:2048 lds\n"
      "s_sub_u32 vcc_lo, 0, s41\n"
      "s_mov_b32 vcc_hi, 0\n"
      ".p2align 3\n"
      "s_waitcnt vmcnt(12)\n"
      "s_barrier\n"
      "s_cmp_lt_u32 s40, 3\n"
      "s_cselect_b32 s97, -9248, 0\n"
      "ds_read_b128 a[0:3], v86 offset:30752\n"
      "ds_read_b128 a[4:7], v84 offset:27680\n"
      "ds_read_b128 a[8:11], v85 offset:27680\n"
      "ds_read_b128 a[12:15], v86 offset:30816\n"
      "s_add_u32 s97, s97, s92\n"
      "s_add_u32 m0, s97, 9248\n"
      "s_bfe_u32 s97, 0x3322c26, s43\n"
      "s_mul_i32 s97, s97, 3072\n"
      "s_mov_b32 s51, s47\n"
      "s_waitcnt lgkmcnt(3)\n"
      "v_mfma_f32_16x16x32_bf16 v[20:23], a[0:3], v[76:79], 0\n"
      "s_waitcnt lgkmcnt(2)\n"
      "ds_read_b128 a[16:19], v84 offset:27744\n"
      "ds_read_b128 a[20:23], v86 offset:39968\n"
      "buffer_load_dwordx4 v8, s[28:31], s97 offen offset:0 lds\n"
      "v_mfma_f32_16x16x32_bf16 v[24:27], a[4:7], v[68:71], v[20:23]\n"
      "ds_read_b128 a[24:27], v84 offset:36896\n"
      "ds_read_b128 a[28:31], v85 offset:36896\n"
      "v_mfma_f32_16x16x32_bf16 v[20:23], a[4:7], v[72:75], v[20:23]\n"
      "ds_read_b64_tr_b16 a[88:89], v87 offset:27680\n"
      "ds_read_b64_tr_b16 a[92:93], v88 offset:27680\n"
      "buffer_load_dwordx4 v9, s[28:31], s97 offen offset:1024 lds\n"
      "v_mfma_f32_16x16x32_bf16 v[28:31], a[0:3], v[80:83], 0\n"
      "s_waitcnt lgkmcnt(7)\n"
      "ds_read_b64_tr_b16 a[96:97], v87 offset:27744\n"
      "ds_read_b64_tr_b16 a[124:125], v87 offset:36896\n"
      "v_mfma_f32_16x16x32_bf16 v[32:35], a[8:11], v[68:71], v[28:31]\n"
      "ds_read_b64_tr_b16 a[128:129], v88 offset:36896\n"
      "ds_read_b128 a[32:35], v86 offset:40032\n"
      "v_mfma_f32_16x16x32_bf16 v[28:31], a[8:11], v[72:75], v[28:31]\n"
      "s_waitcnt lgkmcnt(10)\n"
      "ds_read_b128 a[0:3], v84 offset:36960\n"
      "ds_read_b64_tr_b16 a[132:133], v87 offset:36960\n"
      "buffer_load_dwordx4 v89, s[28:31], s97 offen offset:2048 lds\n"
      "v_mfma_f32_16x16x32_bf16 v[54:57], a[12:15], v[76:79], 0\n"
      "s_waitcnt lgkmcnt(10)\n"
      "s_cmp_lt_u32 s40, 2\n"
      "s_cselect_b32 s97, -9248, 0\n"
      "s_add_u32 s97, s97, s92\n"
      "s_add_u32 m0, s97, 30784\n"
      "s_bfe_u32 s97, 0x1b150f09, s43\n"
      "s_mul_i32 s97, s97, 3072\n"
      "v_cvt_pk_bf16_f32 v96, v20, v21\n"
      "v_cvt_pk_bf16_f32 v97, v22, v23\n"
      "v_mfma_f32_16x16x32_bf16 v[58:61], a[16:19], v[68:71], v[54:57]\n"
      "v_cvt_pk_bf16_f32 v92, v24, v25\n"
      "v_cvt_pk_bf16_f32 v93, v26, v27\n"
      "v_mfma_f32_16x16x32_bf16 v[54:57], a[16:19], v[72:75], v[54:57]\n"
      "buffer_load_dwordx4 v8, s[28:31], s97 offen offset:0 lds\n"
      "v_cvt_pk_bf16_f32 v104, v28, v29\n"
      "v_cvt_pk_bf16_f32 v105, v30, v31\n"
      "v_mfma_f32_16x16x32_bf16 v[62:65], a[20:23], v[76:79], 0\n"
      "s_waitcnt lgkmcnt(4)\n"
      "v_cvt_pk_bf16_f32 v100, v32, v33\n"
      "v_cvt_pk_bf16_f32 v101, v34, v35\n"
      "v_mfma_f32_16x16x32_bf16 v[20:23], a[24:27], v[68:71], v[62:65]\n"
      "v_cvt_pk_bf16_f32 v108, v54, v55\n"
      "v_cvt_pk_bf16_f32 v109, v56, v57\n"
      "v_mfma_f32_16x16x32_bf16 v[62:65], a[24:27], v[72:75], v[62:65]\n"
      "s_waitcnt lgkmcnt(0)\n"
      "s_waitcnt vmcnt(13)\n"
      "s_cbranch_vccnz Lconv_h1\n"
      "s_barrier\n"
      "Lconv_h1:\n"
      "buffer_load_dwordx4 v9, s[28:31], s97 offen offset:1024 lds\n"
      "v_mfma_f32_16x16x32_bf16 v[24:27], a[20:23], v[80:83], 0\n"
      "v_cvt_pk_bf16_f32 v58, v58, v59\n"
      "v_cvt_pk_bf16_f32 v60, v60, v61\n"
      "v_mfma_f32_16x16x32_bf16 v[28:31], a[28:31], v[68:71], v[24:27]\n"
      "s_cbranch_vccz Lconv_h2\n"
      "s_barrier\n"
      "Lconv_h2:\n"
      "ds_read_b128 a[4:7], v86 offset:49184\n"
      "ds_read_b128 a[8:11], v84 offset:46112\n"
      "ds_read_b128 a[12:15], v85 offset:46112\n"
      "ds_read_b128 a[16:19], v86 offset:49248\n"
      "v_mfma_f32_16x16x32_bf16 v[24:27], a[28:31], v[72:75], v[24:27]\n"
      "ds_read_b128 a[20:23], v84 offset:46176\n"
      "ds_read_b128 a[24:27], v86 offset:58400\n"
      "buffer_load_dwordx4 v89, s[28:31], s97 offen offset:2048 lds\n"
      "v_accvgpr_write_b32 a196, v58\n"
      "v_mfma_f32_16x16x32_bf16 v[32:35], a[32:35], v[76:79], 0\n"
      "s_cmp_lt_u32 s40, 1\n"
      "s_cselect_b32 s97, -9248, 0\n"
      "ds_read_b128 a[28:31], v84 offset:55328\n"
      "ds_read_b128 a[32:35], v85 offset:55328\n"
      "s_add_u32 s97, s97, s92\n"
      "s_add_u32 m0, s97, 52320\n"
      "s_bfe_u32 s97, 0x332d2721, s43\n"
      "s_mul_i32 s97, s97, 3072\n"
      "v_accvgpr_write_b32 a197, v60\n"
      "v_cvt_pk_bf16_f32 v116, v62, v63\n"
      "v_mfma_f32_16x16x32_bf16 v[54:57], a[0:3], v[68:71], v[32:35]\n"
      "ds_read_b64_tr_b16 a[160:161], v87 offset:46112\n"
      "ds_read_b64_tr_b16 a[164:165], v88 offset:46112\n"
      "buffer_load_dwordx4 v8, s[28:31], s97 offen offset:0 lds\n"
      "v_cvt_pk_bf16_f32 v117, v64, v65\n"
      "v_mfma_f32_16x16x32_bf16 v[32:35], a[0:3], v[72:75], v[32:35]\n"
      "s_waitcnt lgkmcnt(9)\n"
      "ds_read_b64_tr_b16 a[168:169], v87 offset:46176\n"
      "ds_read_b64_tr_b16 a[90:91], v87 offset:55328\n"
      "v_cvt_pk_bf16_f32 v112, v20, v21\n"
      "v_cvt_pk_bf16_f32 v113, v22, v23\n"
      "v_mfma_f32_16x16x32_bf16 v[58:61], a[4:7], v[76:79], 0\n"
      "s_waitcnt lgkmcnt(10)\n"
      "ds_read_b64_tr_b16 a[94:95], v88 offset:55328\n"
      "ds_read_b128 a[0:3], v86 offset:58464\n"
      "v_cvt_pk_bf16_f32 v120, v24, v25\n"
      "v_cvt_pk_bf16_f32 v121, v26, v27\n"
      "v_mfma_f32_16x16x32_bf16 v[62:65], a[8:11], v[68:71], v[58:61]\n"
      "ds_read_b64_tr_b16 a[98:99], v87 offset:55392\n"
      "buffer_load_dwordx4 v9, s[28:31], s97 offen offset:1024 lds\n"
      "v_cvt_pk_bf16_f32 v28, v28, v29\n"
      "v_cvt_pk_bf16_f32 v30, v30, v31\n"
      "v_mfma_f32_16x16x32_bf16 v[58:61], a[8:11], v[72:75], v[58:61]\n"
      "v_accvgpr_write_b32 a204, v28\n"
      "v_accvgpr_write_b32 a205, v30\n"
      "v_cvt_pk_bf16_f32 v128, v32, v33\n"
      "v_cvt_pk_bf16_f32 v129, v34, v35\n"
      "v_mfma_f32_16x16x32_bf16 v[20:23], a[4:7], v[80:83], 0\n"
      "s_waitcnt lgkmcnt(7)\n"
      "ds_read_b128 a[4:7], v84 offset:55392\n"
      "v_cvt_pk_bf16_f32 v124, v54, v55\n"
      "v_mfma_f32_16x16x32_bf16 v[24:27], a[12:15], v[68:71], v[20:23]\n"
      "buffer_load_dwordx4 v89, s[28:31], s97 offen offset:2048 lds\n"
      "v_cvt_pk_bf16_f32 v125, v56, v57\n"
      "v_mfma_f32_16x16x32_bf16 v[20:23], a[12:15], v[72:75], v[20:23]\n"
      "s_add_u32 m0, s46, 13280\n"
      "v_cvt_pk_bf16_f32 v132, v58, v59\n"
      "v_cvt_pk_bf16_f32 v133, v60, v61\n"
      "v_mfma_f32_16x16x32_bf16 v[28:31], a[16:19], v[76:79], 0\n"
      "v_cvt_pk_bf16_f32 v62, v62, v63\n"
      "v_cvt_pk_bf16_f32 v64, v64, v65\n"
      "v_mfma_f32_16x16x32_bf16 v[32:35], a[20:23], v[68:71], v[28:31]\n"
      "buffer_load_dwordx4 v16, s[32:35], s51 offen lds\n"
      "v_accvgpr_write_b32 a212, v62\n"
      "v_accvgpr_write_b32 a213, v64\n"
      "v_mfma_f32_16x16x32_bf16 v[28:31], a[20:23], v[72:75], v[28:31]\n"
      "s_add_u32 m0, s46, 13280\n"
      "v_cvt_pk_bf16_f32 v140, v20, v21\n"
      "v_cvt_pk_bf16_f32 v141, v22, v23\n"
      "v_mfma_f32_16x16x32_bf16 v[54:57], a[24:27], v[76:79], 0\n"
      "v_cvt_pk_bf16_f32 v136, v24, v25\n"
      "v_cvt_pk_bf16_f32 v137, v26, v27\n"
      "v_mfma_f32_16x16x32_bf16 v[58:61], a[28:31], v[68:71], v[54:57]\n"
      "buffer_load_dwordx4 v17, s[32:35], s51 offen offset:1024 lds\n"
      "v_cvt_pk_bf16_f32 v144, v28, v29\n"
      "v_mfma_f32_16x16x32_bf16 v[54:57], a[28:31], v[72:75], v[54:57]\n"
      "s_waitcnt lgkmcnt(0)\n"
      "s_waitcnt vmcnt(11)\n"
      "s_cbranch_vccnz Lconv_h3\n"
      "s_barrier\n"
      "Lconv_h3:\n"
      "s_add_u32 m0, s46, 13280\n"
      "v_cvt_pk_bf16_f32 v145, v30, v31\n"
      "v_mfma_f32_16x16x32_bf16 v[62:65], a[24:27], v[80:83], 0\n"
      "buffer_load_dwordx4 v18, s[32:35], s51 offen offset:2048 lds\n"
      "v_cvt_pk_bf16_f32 v32, v32, v33\n"
      "v_mfma_f32_16x16x32_bf16 v[20:23], a[32:35], v[68:71], v[62:65]\n"
      "s_cbranch_vccz Lconv_h4\n"
      "s_barrier\n"
      "Lconv_h4:\n"
      "s_add_u32 m0, s46, 26560\n"
      "s_add_u32 s51, s47, 196608\n"
      "ds_read_b128 a[8:11], v86 offset:21536\n"
      "ds_read_b128 a[12:15], v84 offset:18464\n"
      "ds_read_b128 a[16:19], v85 offset:18464\n"
      "ds_read_b128 a[20:23], v86 offset:21600\n"
      "v_mfma_f32_16x16x32_bf16 v[62:65], a[32:35], v[72:75], v[62:65]\n"
      "ds_read_b128 a[24:27], v84 offset:18528\n"
      "ds_read_b128 a[28:31], v86 offset:3072\n"
      "v_cvt_pk_bf16_f32 v34, v34, v35\n"
      "v_accvgpr_write_b32 a216, v32\n"
      "v_mfma_f32_16x16x32_bf16 v[24:27], a[0:3], v[76:79], 0\n"
      "ds_read_b128 a[32:35], v84 offset:0\n"
      "ds_read_b128 a[0:3], v85 offset:0\n"
      "buffer_load_dwordx4 v16, s[32:35], s51 offen lds\n"
      "v_accvgpr_write_b32 a217, v34\n"
      "v_mfma_f32_16x16x32_bf16 v[28:31], a[4:7], v[68:71], v[24:27]\n"
      "s_add_u32 m0, s46, 26560\n"
      "ds_read_b64_tr_b16 a[126:127], v87 offset:18464\n"
      "ds_read_b64_tr_b16 a[130:131], v88 offset:18464\n"
      "v_cvt_pk_bf16_f32 v98, v54, v55\n"
      "v_cvt_pk_bf16_f32 v99, v56, v57\n"
      "v_mfma_f32_16x16x32_bf16 v[24:27], a[4:7], v[72:75], v[24:27]\n"
      "s_waitcnt lgkmcnt(9)\n"
      "ds_read_b128 a[4:7], v86 offset:3136\n"
      "ds_read_b64_tr_b16 a[134:135], v87 offset:18528\n"
      "v_cvt_pk_bf16_f32 v94, v58, v59\n"
      "v_cvt_pk_bf16_f32 v95, v60, v61\n"
      "v_mfma_f32_16x16x32_bf16 v[32:35], a[8:11], v[76:79], 0\n"
      "s_waitcnt lgkmcnt(10)\n"
      "ds_read_b64_tr_b16 a[162:163], v87 offset:0\n"
      "ds_read_b64_tr_b16 a[166:167], v88 offset:0\n"
      "buffer_load_dwordx4 v17, s[32:35], s51 offen offset:1024 lds\n"
      "v_cvt_pk_bf16_f32 v106, v62, v63\n"
      "v_mfma_f32_16x16x32_bf16 v[54:57], a[12:15], v[68:71], v[32:35]\n"
      "s_add_u32 m0, s46, 26560\n"
      "s_sub_u32 s96, s51, s95\n"
      "ds_read_b64_tr_b16 a[170:171], v87 offset:64\n"
      "v_cvt_pk_bf16_f32 v107, v64, v65\n"
      "v_cvt_pk_bf16_f32 v102, v20, v21\n"
      "v_cvt_pk_bf16_f32 v103, v22, v23\n"
      "v_mfma_f32_16x16x32_bf16 v[32:35], a[12:15], v[72:75], v[32:35]\n"
      "v_cvt_pk_bf16_f32 v110, v24, v25\n"
      "v_cvt_pk_bf16_f32 v111, v26, v27\n"
      "v_cvt_pk_bf16_f32 v28, v28, v29\n"
      "v_cvt_pk_bf16_f32 v30, v30, v31\n"
      "v_mfma_f32_16x16x32_bf16 v[58:61], a[8:11], v[80:83], 0\n"
      "s_waitcnt lgkmcnt(7)\n"
      "ds_read_b128 a[8:11], v84 offset:64\n"
      "buffer_load_dwordx4 v18, s[32:35], s51 offen offset:2048 lds\n"
      "v_accvgpr_write_b32 a198, v28\n"
      "v_accvgpr_write_b32 a199, v30\n"
      "v_mfma_f32_16x16x32_bf16 v[62:65], a[16:19], v[68:71], v[58:61]\n"
      "s_mov_b32 m0, s59\n"
      "s_nop 0\n"
      "v_cvt_pk_bf16_f32 v118, v32, v33\n"
      "v_cvt_pk_bf16_f32 v119, v34, v35\n"
      "v_add_u32 v86, -46144, v86\n"
      "v_cvt_pk_bf16_f32 v114, v54, v55\n"
      "v_mfma_f32_16x16x32_bf16 v[58:61], a[16:19], v[72:75], v[58:61]\n"
      "ds_read_b128 a[12:15], v86 offset:30752\n"
      "v_cvt_pk_bf16_f32 v115, v56, v57\n"
      "v_add_u32 v84, -46144, v84\n"
      "v_cvt_pk_bf16_f32 v62, v62, v63\n"
      "v_mfma_f32_16x16x32_bf16 v[20:23], a[20:23], v[76:79], 0\n"
      "ds_read_b128 a[16:19], v84 offset:27680\n"
      "buffer_load_dwordx4 v19, s[32:35], s96 offen lds\n"
      "v_mfma_f32_16x16x32_bf16 v[24:27], a[24:27], v[68:71], v[20:23]\n"
      "Lconv_p1:\n"
      "s_cbranch_execz Lconv_r0\n"
      "s_nop 0\n"
      "s_add_u32 m0, s92, 73824\n"
      "s_bfe_u32 s97, 0x16100a04, s43\n"
      "s_mul_i32 s97, s97, 3072\n"
      "v_cvt_pk_bf16_f32 v122, v58, v59\n"
      "v_cvt_pk_bf16_f32 v123, v60, v61\n"
      "v_mfma_f32_16x16x32_bf16 v[20:23], a[24:27], v[72:75], v[20:23]\n"
      "ds_read_b128 a[24:27], v86 offset:30816\n"
      "buffer_load_dwordx4 v8, s[28:31], s97 offen offset:0 lds\n"
      "v_cvt_pk_bf16_f32 v64, v64, v65\n"
      "v_accvgpr_write_b32 a206, v62\n"
      "v_mfma_f32_16x16x32_bf16 v[28:31], a[28:31], v[76:79], 0\n"
      "v_accvgpr_write_b32 a207, v64\n"
      "v_add_u32 v85, -46144, v85\n"
      "v_mfma_f32_16x16x32_bf16 v[32:35], a[32:35], v[68:71], v[28:31]\n"
      "ds_read_b128 a[20:23], v85 offset:27680\n"
      "v_cvt_pk_bf16_f32 v130, v20, v21\n"
      "v_cvt_pk_bf16_f32 v131, v22, v23\n"
      "v_mfma_f32_16x16x32_bf16 v[28:31], a[32:35], v[72:75], v[28:31]\n"
      "buffer_load_dwordx4 v9, s[28:31], s97 offen offset:1024 lds\n"
      "v_add_u32 v87, -46144, v87\n"
      "v_add_u32 v88, -46144, v88\n"
      "v_mfma_f32_16x16x32_bf16 v[54:57], a[28:31], v[80:83], 0\n"
      "ds_read_b64_tr_b16 a[100:101], v87 offset:27680\n"
      "ds_read_b64_tr_b16 a[104:105], v88 offset:27680\n"
      "v_mfma_f32_16x16x32_bf16 v[58:61], a[0:3], v[68:71], v[54:57]\n"
      "ds_read_b128 a[28:31], v84 offset:27744\n"
      "ds_read_b64_tr_b16 a[108:109], v87 offset:27744\n"
      "v_cvt_pk_bf16_f32 v126, v24, v25\n"
      "v_cvt_pk_bf16_f32 v127, v26, v27\n"
      "v_mfma_f32_16x16x32_bf16 v[54:57], a[0:3], v[72:75], v[54:57]\n"
      "s_waitcnt lgkmcnt(5)\n"
      "s_nop 0\n"
      "buffer_load_dwordx4 v89, s[28:31], s97 offen offset:2048 lds\n"
      "v_cvt_pk_bf16_f32 v134, v28, v29\n"
      "v_cvt_pk_bf16_f32 v135, v30, v31\n"
      "v_mfma_f32_16x16x32_bf16 v[62:65], a[4:7], v[76:79], 0\n"
      "s_add_u32 m0, s92, 86112\n"
      "s_bfe_u32 s97, 0x2e28221c, s43\n"
      "s_mul_i32 s97, s97, 3072\n"
      "v_cvt_pk_bf16_f32 v32, v32, v33\n"
      "v_cvt_pk_bf16_f32 v34, v34, v35\n"
      "v_mfma_f32_16x16x32_bf16 v[20:23], a[8:11], v[68:71], v[62:65]\n"
      "v_accvgpr_write_b32 a214, v32\n"
      "v_accvgpr_write_b32 a215, v34\n"
      "v_mfma_f32_16x16x32_bf16 v[62:65], a[8:11], v[72:75], v[62:65]\n"
      "buffer_load_dwordx4 v8, s[28:31], s97 offen offset:0 lds\n"
      "v_cvt_pk_bf16_f32 v142, v54, v55\n"
      "v_cvt_pk_bf16_f32 v143, v56, v57\n"
      "v_mfma_f32_16x16x32_bf16 v[24:27], a[12:15], v[76:79], 0\n"
      "v_cvt_pk_bf16_f32 v138, v58, v59\n"
      "v_cvt_pk_bf16_f32 v139, v60, v61\n"
      "v_mfma_f32_16x16x32_bf16 v[28:31], a[16:19], v[68:71], v[24:27]\n"
      "v_cvt_pk_bf16_f32 v20, v20, v21\n"
      "v_cvt_pk_bf16_f32 v146, v62, v63\n"
      "v_mfma_f32_16x16x32_bf16 v[24:27], a[16:19], v[72:75], v[24:27]\n"
      "s_waitcnt lgkmcnt(0)\n"
      "s_waitcnt vmcnt(11)\n"
      "s_cbranch_vccnz Lconv_h5\n"
      "s_barrier\n"
      "Lconv_h5:\n"
      "buffer_load_dwordx4 v9, s[28:31], s97 offen offset:1024 lds\n"
      "v_mfma_f32_16x16x32_bf16 v[32:35], a[12:15], v[80:83], 0\n"
      "v_cvt_pk_bf16_f32 v147, v64, v65\n"
      "v_cvt_pk_bf16_f32 v22, v22, v23\n"
      "v_mfma_f32_16x16x32_bf16 v[54:57], a[20:23], v[68:71], v[32:35]\n"
      "s_cbranch_vccz Lconv_h6\n"
      "s_barrier\n"
      "Lconv_h6:\n"
      "ds_read_b128 a[32:35], v86 offset:12288\n"
      "ds_read_b128 a[0:3], v84 offset:9216\n"
      "ds_read_b128 a[4:7], v85 offset:9216\n"
      "ds_read_b128 a[8:11], v86 offset:12352\n"
      "buffer_load_dwordx4 v89, s[28:31], s97 offen offset:2048 lds\n"
      "v_mfma_f32_16x16x32_bf16 v[32:35], a[20:23], v[72:75], v[32:35]\n"
      "s_add_u32 m0, s92, 98400\n"
      "s_bfe_u32 s97, 0x110b0534, s43\n"
      "ds_read_b128 a[12:15], v84 offset:9280\n"
      "ds_read_b128 a[16:19], v86 offset:3072\n"
      "s_mul_i32 s97, s97, 3072\n"
      "v_accvgpr_write_b32 a218, v20\n"
      "v_accvgpr_write_b32 a219, v22\n"
      "v_mfma_f32_16x16x32_bf16 v[58:61], a[24:27], v[76:79], 0\n"
      "ds_read_b128 a[20:23], v84 offset:0\n"
      "ds_read_b128 a[24:27], v85 offset:0\n"
      "v_cvt_pk_bf16_f32 v152, v24, v25\n"
      "v_cvt_pk_bf16_f32 v153, v26, v27\n"
      "v_mfma_f32_16x16x32_bf16 v[62:65], a[28:31], v[68:71], v[58:61]\n"
      "ds_read_b64_tr_b16 a[136:137], v87 offset:9216\n"
      "ds_read_b64_tr_b16 a[140:141], v88 offset:9216\n"
      "buffer_load_dwordx4 v8, s[28:31], s97 offen offset:0 lds\n"
      "v_cvt_pk_bf16_f32 v148, v28, v29\n"
      "v_mfma_f32_16x16x32_bf16 v[58:61], a[28:31], v[72:75], v[58:61]\n"
      "s_waitcnt lgkmcnt(9)\n"
      "ds_read_b128 a[28:31], v86 offset:3136\n"
      "ds_read_b64_tr_b16 a[144:145], v87 offset:9280\n"
      "v_cvt_pk_bf16_f32 v149, v30, v31\n"
      "v_cvt_pk_bf16_f32 v156, v32, v33\n"
      "v_mfma_f32_16x16x32_bf16 v[20:23], a[32:35], v[76:79], 0\n"
      "s_waitcnt lgkmcnt(10)\n"
      "ds_read_b64_tr_b16 a[172:173], v87 offset:0\n"
      "ds_read_b64_tr_b16 a[176:177], v88 offset:0\n"
      "v_cvt_pk_bf16_f32 v157, v34, v35\n"
      "v_mfma_f32_16x16x32_bf16 v[24:27], a[0:3], v[68:71], v[20:23]\n"
      "ds_read_b64_tr_b16 a[180:181], v87 offset:64\n"
      "ds_read_b64_tr_b16 a[102:103], v87 offset:18464\n"
      "buffer_load_dwordx4 v9, s[28:31], s97 offen offset:1024 lds\n"
      "v_cvt_pk_bf16_f32 v54, v54, v55\n"
      "v_mfma_f32_16x16x32_bf16 v[20:23], a[0:3], v[72:75], v[20:23]\n"
      "ds_read_b128 a[0:3], v86 offset:21536\n"
      "ds_read_b64_tr_b16 a[106:107], v88 offset:18464\n"
      "v_cvt_pk_bf16_f32 v56, v56, v57\n"
      "v_accvgpr_write_b32 a200, v54\n"
      "v_mfma_f32_16x16x32_bf16 v[28:31], a[32:35], v[80:83], 0\n"
      "s_waitcnt lgkmcnt(10)\n"
      "ds_read_b128 a[32:35], v84 offset:64\n"
      "ds_read_b64_tr_b16 a[110:111], v87 offset:18528\n"
      "v_accvgpr_write_b32 a201, v56\n"
      "v_cvt_pk_bf16_f32 v164, v58, v59\n"
      "v_mfma_f32_16x16x32_bf16 v[32:35], a[4:7], v[68:71], v[28:31]\n"
      "ds_read_b64_tr_b16 a[138:139], v87 offset:36928\n"
      "ds_read_b64_tr_b16 a[142:143], v88 offset:36928\n"
      "buffer_load_dwordx4 v89, s[28:31], s97 offen offset:2048 lds\n"
      "v_cvt_pk_bf16_f32 v165, v60, v61\n"
      "v_mfma_f32_16x16x32_bf16 v[28:31], a[4:7], v[72:75], v[28:31]\n"
      "s_cmp_lt_u32 s40, 3\n"
      "s_cselect_b32 s97, 27680, 0\n"
      "ds_read_b128 a[4:7], v84 offset:18464\n"
      "ds_read_b64_tr_b16 a[146:147], v87 offset:36992\n"
      "s_add_u32 s97, s97, s92\n"
      "s_nop 0\n"
      "s_add_u32 m0, s97, 36928\n"
      "s_bfe_u32 s97, 0x29231d17, s43\n"
      "s_mul_i32 s97, s97, 3072\n"
      "v_cvt_pk_bf16_f32 v160, v62, v63\n"
      "v_cvt_pk_bf16_f32 v161, v64, v65\n"
      "v_mfma_f32_16x16x32_bf16 v[54:57], a[8:11], v[76:79], 0\n"
      "ds_read_b128 a[8:11], v85 offset:18464\n"
      "ds_read_b64_tr_b16 a[174:175], v87 offset:55392\n"
      "v_cvt_pk_bf16_f32 v168, v20, v21\n"
      "v_cvt_pk_bf16_f32 v169, v22, v23\n"
      "v_mfma_f32_16x16x32_bf16 v[58:61], a[12:15], v[68:71], v[54:57]\n"
      "ds_read_b64_tr_b16 a[178:179], v88 offset:55392\n"
      "ds_read_b64_tr_b16 a[182:183], v87 offset:55456\n"
      "buffer_load_dwordx4 v8, s[28:31], s97 offen offset:0 lds\n"
      "v_cvt_pk_bf16_f32 v24, v24, v25\n"
      "v_mfma_f32_16x16x32_bf16 v[54:57], a[12:15], v[72:75], v[54:57]\n"
      "ds_read_b128 a[12:15], v86 offset:21600\n"
      "v_cvt_pk_bf16_f32 v26, v26, v27\n"
      "v_accvgpr_write_b32 a208, v24\n"
      "v_accvgpr_write_b32 a209, v26\n"
      "v_mfma_f32_16x16x32_bf16 v[62:65], a[16:19], v[76:79], 0\n"
      "buffer_load_dwordx4 v9, s[28:31], s97 offen offset:1024 lds\n"
      "v_cvt_pk_bf16_f32 v176, v28, v29\n"
      "v_cvt_pk_bf16_f32 v177, v30, v31\n"
      "v_mfma_f32_16x16x32_bf16 v[20:23], a[20:23], v[68:71], v[62:65]\n"
      "v_cvt_pk_bf16_f32 v172, v32, v33\n"
      "v_cvt_pk_bf16_f32 v173, v34, v35\n"
      "v_mfma_f32_16x16x32_bf16 v[62:65], a[20:23], v[72:75], v[62:65]\n"
      "ds_read_b128 a[20:23], v86 offset:40000\n"
      "v_cvt_pk_bf16_f32 v54, v54, v55\n"
      "v_cvt_pk_bf16_f32 v56, v56, v57\n"
      "v_mfma_f32_16x16x32_bf16 v[24:27], a[16:19], v[80:83], 0\n"
      "ds_read_b128 a[16:19], v84 offset:18528\n"
      "buffer_load_dwordx4 v89, s[28:31], s97 offen offset:2048 lds\n"
      "v_accvgpr_write_b32 a228, v54\n"
      "v_accvgpr_write_b32 a229, v56\n"
      "v_mfma_f32_16x16x32_bf16 v[28:31], a[24:27], v[68:71], v[24:27]\n"
      "s_cmp_lt_u32 s40, 2\n"
      "s_cselect_b32 s97, 6144, 0\n"
      "s_add_u32 s97, s97, s92\n"
      "s_add_u32 m0, s97, 43072\n"
      "s_bfe_u32 s97, 0x352f352f, s43\n"
      "s_mul_i32 s97, s97, 3072\n"
      "v_cvt_pk_bf16_f32 v180, v58, v59\n"
      "v_cvt_pk_bf16_f32 v181, v60, v61\n"
      "v_mfma_f32_16x16x32_bf16 v[24:27], a[24:27], v[72:75], v[24:27]\n"
      "s_waitcnt lgkmcnt(2)\n"
      "s_nop 0\n"
      "ds_read_b128 a[24:27], v84 offset:36928\n"
      "v_cvt_pk_bf16_f32 v188, v62, v63\n"
      "v_cvt_pk_bf16_f32 v189, v64, v65\n"
      "v_cvt_pk_bf16_f32 v184, v20, v21\n"
      "v_mfma_f32_16x16x32_bf16 v[32:35], a[28:31], v[76:79], 0\n"
      "ds_read_b128 a[28:31], v85 offset:36928\n"
      "buffer_load_dwordx4 v8, s[28:31], s97 offen offset:0 lds\n"
      "v_cvt_pk_bf16_f32 v185, v22, v23\n"
      "v_mfma_f32_16x16x32_bf16 v[54:57], a[32:35], v[68:71], v[32:35]\n"
      "v_cvt_pk_bf16_f32 v24, v24, v25\n"
      "v_cvt_pk_bf16_f32 v26, v26, v27\n"
      "v_mfma_f32_16x16x32_bf16 v[32:35], a[32:35], v[72:75], v[32:35]\n"
      "ds_read_b128 a[32:35], v86 offset:40064\n"
      "v_accvgpr_write_b32 a236, v24\n"
      "v_accvgpr_write_b32 a237, v26\n"
      "v_mfma_f32_16x16x32_bf16 v[58:61], a[0:3], v[76:79], 0\n"
      "buffer_load_dwordx4 v9, s[28:31], s97 offen offset:1024 lds\n"
      "v_cvt_pk_bf16_f32 v192, v28, v29\n"
      "v_mfma_f32_16x16x32_bf16 v[62:65], a[4:7], v[68:71], v[58:61]\n"
      "v_cvt_pk_bf16_f32 v193, v30, v31\n"
      "v_cvt_pk_bf16_f32 v200, v32, v33\n"
      "v_mfma_f32_16x16x32_bf16 v[58:61], a[4:7], v[72:75], v[58:61]\n"
      "ds_read_b128 a[4:7], v86 offset:58464\n"
      "v_cvt_pk_bf16_f32 v201, v34, v35\n"
      "v_mfma_f32_16x16x32_bf16 v[20:23], a[0:3], v[80:83], 0\n"
      "ds_read_b128 a[0:3], v84 offset:36992\n"
      "buffer_load_dwordx4 v89, s[28:31], s97 offen offset:2048 lds\n"
      "v_mfma_f32_16x16x32_bf16 v[24:27], a[8:11], v[68:71], v[20:23]\n"
      "s_add_u32 m0, s46, 39840\n"
      "s_add_u32 s51, s47, 393216\n"
      "v_cvt_pk_bf16_f32 v196, v54, v55\n"
      "v_cvt_pk_bf16_f32 v197, v56, v57\n"
      "v_cvt_pk_bf16_f32 v154, v58, v59\n"
      "v_cvt_pk_bf16_f32 v155, v60, v61\n"
      "v_mfma_f32_16x16x32_bf16 v[20:23], a[8:11], v[72:75], v[20:23]\n"
      "ds_read_b128 a[8:11], v84 offset:55392\n"
      "buffer_load_dwordx4 v16, s[32:35], s51 offen lds\n"
      "v_cvt_pk_bf16_f32 v150, v62, v63\n"
      "v_cvt_pk_bf16_f32 v151, v64, v65\n"
      "v_mfma_f32_16x16x32_bf16 v[28:31], a[12:15], v[76:79], 0\n"
      "s_waitcnt lgkmcnt(3)\n"
      "s_nop 0\n"
      "s_add_u32 m0, s46, 39840\n"
      "ds_read_b128 a[12:15], v85 offset:55392\n"
      "v_cvt_pk_bf16_f32 v158, v20, v21\n"
      "v_mfma_f32_16x16x32_bf16 v[32:35], a[16:19], v[68:71], v[28:31]\n"
      "v_cvt_pk_bf16_f32 v159, v22, v23\n"
      "v_cvt_pk_bf16_f32 v24, v24, v25\n"
      "v_mfma_f32_16x16x32_bf16 v[28:31], a[16:19], v[72:75], v[28:31]\n"
      "ds_read_b128 a[16:19], v86 offset:58528\n"
      "buffer_load_dwordx4 v17, s[32:35], s51 offen offset:1024 lds\n"
      "v_cvt_pk_bf16_f32 v26, v26, v27\n"
      "v_accvgpr_write_b32 a202, v24\n"
      "v_mfma_f32_16x16x32_bf16 v[54:57], a[20:23], v[76:79], 0\n"
      "s_add_u32 m0, s46, 39840\n"
      "v_accvgpr_write_b32 a203, v26\n"
      "v_cvt_pk_bf16_f32 v162, v32, v33\n"
      "v_mfma_f32_16x16x32_bf16 v[58:61], a[24:27], v[68:71], v[54:57]\n"
      "v_cvt_pk_bf16_f32 v166, v28, v29\n"
      "v_cvt_pk_bf16_f32 v167, v30, v31\n"
      "v_cvt_pk_bf16_f32 v163, v34, v35\n"
      "v_mfma_f32_16x16x32_bf16 v[54:57], a[24:27], v[72:75], v[54:57]\n"
      "buffer_load_dwordx4 v18, s[32:35], s51 offen offset:2048 lds\n"
      "v_add_u32 v86, 46144, v86\n"
      "v_add_u32 v85, 46144, v85\n"
      "v_mfma_f32_16x16x32_bf16 v[62:65], a[20:23], v[80:83], 0\n"
      "s_add_u32 m0, s46, 0\n"
      "s_add_u32 s51, s47, 589824\n"
      "ds_read_b128 a[20:23], v84 offset:55456\n"
      "v_cvt_pk_bf16_f32 v58, v58, v59\n"
      "v_mfma_f32_16x16x32_bf16 v[20:23], a[28:31], v[68:71], v[62:65]\n"
      "v_cvt_pk_bf16_f32 v170, v54, v55\n"
      "v_cvt_pk_bf16_f32 v171, v56, v57\n"
      "v_mfma_f32_16x16x32_bf16 v[62:65], a[28:31], v[72:75], v[62:65]\n"
      "buffer_load_dwordx4 v16, s[32:35], s51 offen lds\n"
      "v_cvt_pk_bf16_f32 v60, v60, v61\n"
      "v_accvgpr_write_b32 a210, v58\n"
      "v_mfma_f32_16x16x32_bf16 v[24:27], a[32:35], v[76:79], 0\n"
      "s_waitcnt lgkmcnt(1)\n"
      "s_add_u32 m0, s46, 0\n"
      "v_accvgpr_write_b32 a211, v60\n"
      "v_cvt_pk_bf16_f32 v174, v20, v21\n"
      "v_mfma_f32_16x16x32_bf16 v[28:31], a[0:3], v[68:71], v[24:27]\n"
      "v_cvt_pk_bf16_f32 v178, v62, v63\n"
      "v_cvt_pk_bf16_f32 v179, v64, v65\n"
      "v_mfma_f32_16x16x32_bf16 v[24:27], a[0:3], v[72:75], v[24:27]\n"
      "buffer_load_dwordx4 v17, s[32:35], s51 offen offset:1024 lds\n"
      "v_cvt_pk_bf16_f32 v175, v22, v23\n"
      "v_add_u32 v84, 46144, v84\n"
      "v_mfma_f32_16x16x32_bf16 v[32:35], a[4:7], v[76:79], 0\n"
      "s_add_u32 m0, s46, 0\n"
      "v_cvt_pk_bf16_f32 v182, v28, v29\n"
      "v_cvt_pk_bf16_f32 v183, v30, v31\n"
      "v_mfma_f32_16x16x32_bf16 v[54:57], a[8:11], v[68:71], v[32:35]\n"
      "buffer_load_dwordx4 v18, s[32:35], s51 offen offset:2048 lds\n"
      "v_cvt_pk_bf16_f32 v24, v24, v25\n"
      "v_mfma_f32_16x16x32_bf16 v[32:35], a[8:11], v[72:75], v[32:35]\n"
      "s_waitcnt lgkmcnt(0)\n"
      "s_waitcnt vmcnt(15)\n"
      "s_cbranch_vccnz Lconv_h7\n"
      "s_barrier\n"
      "Lconv_h7:\n"
      "s_sub_u32 m0, s94, s59\n"
      "s_sub_u32 s96, s51, s95\n"
      "v_cvt_pk_bf16_f32 v26, v26, v27\n"
      "v_mfma_f32_16x16x32_bf16 v[58:61], a[4:7], v[80:83], 0\n"
      "v_accvgpr_write_b32 a230, v24\n"
      "v_accvgpr_write_b32 a231, v26\n"
      "v_mfma_f32_16x16x32_bf16 v[62:65], a[12:15], v[68:71], v[58:61]\n"
      "s_cbranch_vccz Lconv_h8\n"
      "s_barrier\n"
      "Lconv_h8:\n"
      "ds_read_b128 a[48:51], v2 offset:26560\n"
      "ds_read_b128 a[52:55], v3 offset:26560\n"
      "ds_read_b128 a[56:59], v4 offset:26560\n"
      "ds_read_b128 a[36:39], v2 offset:13280\n"
      "buffer_load_dwordx4 v19, s[32:35], s96 offen lds\n"
      "v_mfma_f32_16x16x32_bf16 v[58:61], a[12:15], v[72:75], v[58:61]\n"
      "s_mov_b32 m0, s18\n"
      "ds_read_b128 a[40:43], v3 offset:13280\n"
      "ds_read_b128 a[44:47], v4 offset:13280\n"
      "v_cvt_pk_bf16_f32 v186, v54, v55\n"
      "v_cvt_pk_bf16_f32 v187, v56, v57\n"
      "v_mfma_f32_16x16x32_bf16 v[20:23], a[16:19], v[76:79], 0\n"
      "ds_read_b128 a[24:27], v86 offset:30752\n"
      "ds_read_b128 a[28:31], v84 offset:27680\n"
      "v_cvt_pk_bf16_f32 v58, v58, v59\n"
      "v_accvgpr_write_b32 a238, v58\n"
      "v_mfma_f32_16x16x32_bf16 v[24:27], a[20:23], v[68:71], v[20:23]\n"
      "ds_read_b128 a[32:35], v85 offset:27680\n"
      "ds_read_b128 a[0:3], v86 offset:30816\n"
      "v_cvt_pk_bf16_f32 v194, v62, v63\n"
      "v_cvt_pk_bf16_f32 v60, v60, v61\n"
      "v_mfma_f32_16x16x32_bf16 v[20:23], a[20:23], v[72:75], v[20:23]\n"
      "s_waitcnt lgkmcnt(9)\n"
      "ds_read_b128 a[4:7], v84 offset:27744\n"
      "ds_read_b128 a[8:11], v86 offset:39968\n"
      "v_accvgpr_write_b32 a239, v60\n"
      "v_cvt_pk_bf16_f32 v195, v64, v65\n"
      "v_mfma_f32_16x16x32_bf16 v[52:55], a[48:51], a[72:75], 0\n"
      "s_waitcnt lgkmcnt(10)\n"
      "ds_read_b128 a[48:51], v2 offset:13344\n"
      "ds_read_b128 a[12:15], v84 offset:36896\n"
      "v_mfma_f32_16x16x32_bf16 v[56:59], a[52:55], a[76:79], 0\n"
      "s_waitcnt lgkmcnt(10)\n"
      "ds_read_b128 a[52:55], v3 offset:13344\n"
      "ds_read_b128 a[16:19], v85 offset:36896\n"
      "v_cvt_pk_bf16_f32 v190, v32, v33\n"
      "v_cvt_pk_bf16_f32 v191, v34, v35\n"
      "v_mfma_f32_16x16x32_bf16 v[60:63], a[56:59], a[80:83], v[52:55]\n"
      "ds_read_b128 a[56:59], v4 offset:13344\n"
      "ds_read_b128 a[20:23], v86 offset:40032\n"
      "v_fma_f32 v64, s44, v52, v56\n"
      "v_fma_f32 v65, s44, v53, v57\n"
      "v_mfma_f32_16x16x32_bf16 v[36:39], a[36:39], a[72:75], 0\n"
      "s_waitcnt lgkmcnt(10)\n"
      "ds_read_b128 a[36:39], v2 offset:26624\n"
      "v_fma_f32 v66, s44, v54, v58\n"
      "v_fma_f32 v67, s44, v55, v59\n"
      "v_fma_f32 v56, s44, v52, -v56\n"
      "v_mfma_f32_16x16x32_bf16 v[40:43], a[40:43], a[76:79], 0\n"
      "ds_read_b128 a[40:43], v3 offset:26624\n"
      "v_fma_f32 v57, s44, v53, -v57\n"
      "v_fma_f32 v58, s44, v54, -v58\n"
      "v_fma_f32 v59, s44, v55, -v59\n"
      "v_mfma_f32_16x16x32_bf16 v[44:47], a[44:47], a[80:83], v[36:39]\n"
      "s_waitcnt lgkmcnt(1)\n"
      "ds_read_b128 a[44:47], v4 offset:26624\n"
      "v_cvt_pk_bf16_f32 v64, v64, v65\n"
      "v_cvt_pk_bf16_f32 v65, v66, v67\n"
      "v_cvt_pk_bf16_f32 v56, v56, v57\n"
      "v_fma_f32 v48, s44, v36, v40\n"
      "v_fma_f32 v49, s44, v37, v41\n"
      "v_fma_f32 v50, s44, v38, v42\n"
      "v_fma_f32 v51, s44, v39, v43\n"
      "v_fma_f32 v40, s44, v36, -v40\n"
      "v_fma_f32 v41, s44, v37, -v41\n"
      "v_fma_f32 v42, s44, v38, -v42\n"
      "v_fma_f32 v43, s44, v39, -v43\n"
      "v_mfma_f32_16x16x32_bf16 v[36:39], a[36:39], a[72:75], 0\n"
      "s_waitcnt lgkmcnt(1)\n"
      "ds_write_addtid_b32 v64 offset:5376\n"
      "ds_write_addtid_b32 v65 offset:42304\n"
      "ds_write_addtid_b32 v56 offset:6144\n"
      "v_cvt_pk_bf16_f32 v57, v58, v59\n"
      "v_cvt_pk_bf16_f32 v60, v60, v61\n"
      "v_cvt_pk_bf16_f32 v61, v62, v63\n"
      "ds_write_addtid_b32 v57 offset:43072\n"
      "ds_write_addtid_b32 v60 offset:4608\n"
      "ds_write_addtid_b32 v61 offset:41536\n"
      "v_cvt_pk_bf16_f32 v40, v40, v41\n"
      "v_cvt_pk_bf16_f32 v41, v42, v43\n"
      "s_mov_b32 m0, s18\n"
      "s_nop 0\n"
      "ds_write_addtid_b32 v40 offset:1536\n"
      "ds_write_addtid_b32 v41 offset:38464\n"
      "v_mfma_f32_16x16x32_bf16 v[40:43], a[40:43], a[76:79], 0\n"
      "s_waitcnt lgkmcnt(8)\n"
      "ds_read_b128 a[36:39], v2 offset:13408\n"
      "ds_read_b128 a[40:43], v3 offset:13408\n"
      "v_cvt_pk_bf16_f32 v44, v44, v45\n"
      "v_cvt_pk_bf16_f32 v45, v46, v47\n"
      "ds_write_addtid_b32 v44 offset:0\n"
      "ds_write_addtid_b32 v45 offset:36928\n"
      "v_mfma_f32_16x16x32_bf16 v[44:47], a[44:47], a[80:83], v[36:39]\n"
      "ds_read_b128 a[44:47], v4 offset:13408\n"
      "v_cvt_pk_bf16_f32 v48, v48, v49\n"
      "v_cvt_pk_bf16_f32 v49, v50, v51\n"
      "v_fma_f32 v50, s44, v38, v42\n"
      "v_mfma_f32_16x16x32_bf16 v[52:55], a[48:51], a[72:75], 0\n"
      "ds_write_addtid_b32 v48 offset:768\n"
      "ds_write_addtid_b32 v49 offset:37696\n"
      "ds_read_b128 a[48:51], v2 offset:26688\n"
      "s_mov_b32 m0, s18\n"
      "s_nop 0\n"
      "v_fma_f32 v48, s44, v36, v40\n"
      "v_mfma_f32_16x16x32_bf16 v[56:59], a[52:55], a[76:79], 0\n"
      "ds_read_b128 a[52:55], v3 offset:26688\n"
      "v_fma_f32 v49, s44, v37, v41\n"
      "v_fma_f32 v51, s44, v39, v43\n"
      "v_fma_f32 v40, s44, v36, -v40\n"
      "v_mfma_f32_16x16x32_bf16 v[60:63], a[56:59], a[80:83], v[52:55]\n"
      "ds_read_b128 a[56:59], v4 offset:26688\n"
      "v_fma_f32 v41, s44, v37, -v41\n"
      "v_fma_f32 v42, s44, v38, -v42\n"
      "v_fma_f32 v43, s44, v39, -v43\n"
      "v_mfma_f32_16x16x32_bf16 v[28:31], a[24:27], v[76:79], 0\n"
      "v_cvt_pk_bf16_f32 v48, v48, v49\n"
      "v_cvt_pk_bf16_f32 v49, v50, v51\n"
      "v_cvt_pk_bf16_f32 v40, v40, v41\n"
      "v_cvt_pk_bf16_f32 v41, v42, v43\n"
      "v_mfma_f32_16x16x32_bf16 v[32:35], a[28:31], v[68:71], v[28:31]\n"
      "ds_write_addtid_b32 v48 offset:5632\n"
      "ds_write_addtid_b32 v49 offset:42560\n"
      "ds_write_addtid_b32 v40 offset:6400\n"
      "ds_write_addtid_b32 v41 offset:43328\n"
      "v_mfma_f32_16x16x32_bf16 v[28:31], a[28:31], v[72:75], v[28:31]\n"
      "v_cvt_pk_bf16_f32 v44, v44, v45\n"
      "v_cvt_pk_bf16_f32 v45, v46, v47\n"
      "v_fma_f32 v64, s44, v52, v56\n"
      "v_fma_f32 v65, s44, v53, v57\n"
      "ds_write_addtid_b32 v44 offset:4864\n"
      "ds_write_addtid_b32 v45 offset:41792\n"
      "v_fma_f32 v66, s44, v54, v58\n"
      "v_fma_f32 v67, s44, v55, v59\n"
      "v_fma_f32 v56, s44, v52, -v56\n"
      "v_fma_f32 v57, s44, v53, -v57\n"
      "v_fma_f32 v58, s44, v54, -v58\n"
      "v_fma_f32 v59, s44, v55, -v59\n"
      "v_cvt_pk_bf16_f32 v56, v56, v57\n"
      "v_cvt_pk_bf16_f32 v57, v58, v59\n"
      "s_mov_b32 m0, s18\n"
      "s_nop 0\n"
      "ds_write_addtid_b32 v56 offset:1792\n"
      "ds_write_addtid_b32 v57 offset:38720\n"
      "v_mfma_f32_16x16x32_bf16 v[54:57], a[24:27], v[80:83], 0\n"
      "ds_read_b128 a[24:27], v84 offset:36960\n"
      "v_cvt_pk_bf16_f32 v60, v60, v61\n"
      "v_cvt_pk_bf16_f32 v61, v62, v63\n"
      "v_cvt_pk_bf16_f32 v64, v64, v65\n"
      "ds_write_addtid_b32 v60 offset:256\n"
      "ds_write_addtid_b32 v61 offset:37184\n"
      "v_mfma_f32_16x16x32_bf16 v[58:61], a[32:35], v[68:71], v[54:57]\n"
      "ds_write_addtid_b32 v64 offset:1024\n"
      "v_cvt_pk_bf16_f32 v65, v66, v67\n"
      "v_cvt_pk_bf16_f32 v202, v20, v21\n"
      "v_cvt_pk_bf16_f32 v203, v22, v23\n"
      "v_mfma_f32_16x16x32_bf16 v[54:57], a[32:35], v[72:75], v[54:57]\n"
      "ds_write_addtid_b32 v65 offset:37952\n"
      "s_mov_b32 m0, s18\n"
      "v_add_u32 v87, 46144, v87\n"
      "v_cvt_pk_bf16_f32 v208, v58, v59\n"
      "v_cvt_pk_bf16_f32 v209, v60, v61\n"
      "v_mfma_f32_16x16x32_bf16 v[62:65], a[0:3], v[76:79], 0\n"
      "ds_read_b64_tr_b16 a[112:113], v87 offset:27680\n"
      "ds_read_b64_tr_b16 a[120:121], v87 offset:27744\n"
      "v_cvt_pk_bf16_f32 v212, v54, v55\n"
      "v_cvt_pk_bf16_f32 v213, v56, v57\n"
      "v_mfma_f32_16x16x32_bf16 v[20:23], a[4:7], v[68:71], v[62:65]\n"
      "ds_read_b64_tr_b16 a[148:149], v87 offset:36896\n"
      "ds_read_b64_tr_b16 a[156:157], v87 offset:36960\n"
      "v_add_u32 v88, 46144, v88\n"
      "v_cvt_pk_bf16_f32 v198, v24, v25\n"
      "v_mfma_f32_16x16x32_bf16 v[62:65], a[4:7], v[72:75], v[62:65]\n"
      "s_waitcnt lgkmcnt(14)\n"
      "ds_read_b64_tr_b16 a[116:117], v88 offset:27680\n"
      "ds_read_b64_tr_b16 a[152:153], v88 offset:36896\n"
      "v_cvt_pk_bf16_f32 v199, v26, v27\n"
      "v_cvt_pk_bf16_f32 v28, v28, v29\n"
      "v_mfma_f32_16x16x32_bf16 v[52:55], a[48:51], a[72:75], 0\n"
      "v_cvt_pk_bf16_f32 v30, v30, v31\n"
      "v_accvgpr_write_b32 a220, v28\n"
      "v_cvt_pk_bf16_f32 v62, v62, v63\n"
      "v_accvgpr_write_b32 a224, v62\n"
      "v_mfma_f32_16x16x32_bf16 v[56:59], a[52:55], a[76:79], 0\n"
      "v_cvt_pk_bf16_f32 v64, v64, v65\n"
      "v_accvgpr_write_b32 a225, v64\n"
      "v_accvgpr_write_b32 a221, v30\n"
      "v_cvt_pk_bf16_f32 v204, v32, v33\n"
      "v_mfma_f32_16x16x32_bf16 v[60:63], a[56:59], a[80:83], v[52:55]\n"
      "v_cvt_pk_bf16_f32 v205, v34, v35\n"
      "v_cvt_pk_bf16_f32 v216, v20, v21\n"
      "v_cvt_pk_bf16_f32 v217, v22, v23\n"
      "v_fma_f32 v64, s44, v52, v56\n"
      "v_mfma_f32_16x16x32_bf16 v[36:39], a[36:39], a[72:75], 0\n"
      "v_fma_f32 v65, s44, v53, v57\n"
      "v_fma_f32 v66, s44, v54, v58\n"
      "v_fma_f32 v67, s44, v55, v59\n"
      "v_fma_f32 v56, s44, v52, -v56\n"
      "v_mfma_f32_16x16x32_bf16 v[40:43], a[40:43], a[76:79], 0\n"
      "v_fma_f32 v57, s44, v53, -v57\n"
      "v_fma_f32 v58, s44, v54, -v58\n"
      "v_fma_f32 v59, s44, v55, -v59\n"
      "v_cvt_pk_bf16_f32 v64, v64, v65\n"
      "v_mfma_f32_16x16x32_bf16 v[44:47], a[44:47], a[80:83], v[36:39]\n"
      "ds_write_addtid_b32 v64 offset:5888\n"
      "v_cvt_pk_bf16_f32 v65, v66, v67\n"
      "v_cvt_pk_bf16_f32 v56, v56, v57\n"
      "v_cvt_pk_bf16_f32 v57, v58, v59\n"
      "v_mfma_f32_16x16x32_bf16 v[24:27], a[8:11], v[76:79], 0\n"
      "ds_write_addtid_b32 v65 offset:42816\n"
      "ds_write_addtid_b32 v56 offset:6656\n"
      "ds_write_addtid_b32 v57 offset:43584\n"
      "v_cvt_pk_bf16_f32 v60, v60, v61\n"
      "v_mfma_f32_16x16x32_bf16 v[28:31], a[12:15], v[68:71], v[24:27]\n"
      "ds_write_addtid_b32 v60 offset:5120\n"
      "v_cvt_pk_bf16_f32 v61, v62, v63\n"
      "v_fma_f32 v48, s44, v36, v40\n"
      "v_fma_f32 v49, s44, v37, v41\n"
      "ds_write_addtid_b32 v61 offset:42048\n"
      "v_fma_f32 v50, s44, v38, v42\n"
      "v_fma_f32 v51, s44, v39, v43\n"
      "v_fma_f32 v40, s44, v36, -v40\n"
      "v_fma_f32 v41, s44, v37, -v41\n"
      "v_fma_f32 v42, s44, v38, -v42\n"
      "v_fma_f32 v43, s44, v39, -v43\n"
      "v_cvt_pk_bf16_f32 v48, v48, v49\n"
      "v_cvt_pk_bf16_f32 v49, v50, v51\n"
      "v_cvt_pk_bf16_f32 v40, v40, v41\n"
      "v_cvt_pk_bf16_f32 v41, v42, v43\n"
      "v_cvt_pk_bf16_f32 v44, v44, v45\n"
      "v_cvt_pk_bf16_f32 v45, v46, v47\n"
      "s_mov_b32 m0, s18\n"
      "s_nop 0\n"
      "ds_write_addtid_b32 v48 offset:1280\n"
      "ds_write_addtid_b32 v49 offset:38208\n"
      "ds_write_addtid_b32 v40 offset:2048\n"
      "ds_write_addtid_b32 v41 offset:38976\n"
      "ds_write_addtid_b32 v44 offset:512\n"
      "ds_write_addtid_b32 v45 offset:37440\n"
      "v_mfma_f32_16x16x32_bf16 v[24:27], a[12:15], v[72:75], v[24:27]\n"
      "s_waitcnt lgkmcnt(0)\n"
      "s_waitcnt vmcnt(7)\n"
      "s_cbranch_vccnz Lconv_h9\n"
      "s_barrier\n"
      "Lconv_h9:\n"
      "s_add_u32 m0, s46, 13280\n"
      "s_add_u32 s51, s47, 786432\n"
      "v_cvt_pk_bf16_f32 v220, v28, v29\n"
      "v_mfma_f32_16x16x32_bf16 v[32:35], a[8:11], v[80:83], 0\n"
      "v_cvt_pk_bf16_f32 v221, v30, v31\n"
      "v_cvt_pk_bf16_f32 v224, v24, v25\n"
      "v_mfma_f32_16x16x32_bf16 v[54:57], a[16:19], v[68:71], v[32:35]\n"
      "Lconv_p2:\n"
      "s_cbranch_execz Lconv_r0\n"
      "s_nop 0\n"
      "s_cbranch_vccz Lconv_h10\n"
      "s_barrier\n"
      "Lconv_h10:\n"
      "ds_read_b128 a[28:31], v86 offset:49184\n"
      "ds_read_b128 a[32:35], v84 offset:46112\n"
      "ds_read_b128 a[0:3], v85 offset:46112\n"
      "ds_read_b128 a[4:7], v86 offset:49248\n"
      "v_mfma_f32_16x16x32_bf16 v[32:35], a[16:19], v[72:75], v[32:35]\n"
      "ds_read_b128 a[8:11], v84 offset:46176\n"
      "ds_read_b128 a[12:15], v86 offset:58400\n"
      "v_mfma_f32_16x16x32_bf16 v[58:61], a[20:23], v[76:79], 0\n"
      "ds_read_b128 a[16:19], v84 offset:55328\n"
      "ds_read_b128 a[20:23], v85 offset:55328\n"
      "v_mfma_f32_16x16x32_bf16 v[62:65], a[24:27], v[68:71], v[58:61]\n"
      "ds_read_b64_tr_b16 a[184:185], v87 offset:46112\n"
      "ds_read_b64_tr_b16 a[188:189], v88 offset:46112\n"
      "v_cvt_pk_bf16_f32 v225, v26, v27\n"
      "v_mfma_f32_16x16x32_bf16 v[58:61], a[24:27], v[72:75], v[58:61]\n"
      "s_waitcnt lgkmcnt(9)\n"
      "ds_read_b128 a[24:27], v86 offset:58464\n"
      "ds_read_b64_tr_b16 a[192:193], v87 offset:46176\n"
      "buffer_load_dwordx4 v16, s[32:35], s51 offen lds\n"
      "v_cvt_pk_bf16_f32 v32, v32, v33\n"
      "v_mfma_f32_16x16x32_bf16 v[20:23], a[28:31], v[76:79], 0\n"
      "s_waitcnt lgkmcnt(10)\n"
      "s_add_u32 m0, s46, 13280\n"
      "ds_read_b64_tr_b16 a[114:115], v87 offset:55328\n"
      "ds_read_b64_tr_b16 a[118:119], v88 offset:55328\n"
      "v_cvt_pk_bf16_f32 v34, v34, v35\n"
      "v_mfma_f32_16x16x32_bf16 v[24:27], a[32:35], v[68:71], v[20:23]\n"
      "ds_read_b64_tr_b16 a[122:123], v87 offset:55392\n"
      "ds_read_b64_tr_b16 a[150:151], v87 offset:18464\n"
      "buffer_load_dwordx4 v17, s[32:35], s51 offen offset:1024 lds\n"
      "v_accvgpr_write_b32 a232, v32\n"
      "v_mfma_f32_16x16x32_bf16 v[20:23], a[32:35], v[72:75], v[20:23]\n"
      "s_add_u32 m0, s46, 13280\n"
      "ds_read_b128 a[32:35], v86 offset:21536\n"
      "ds_read_b64_tr_b16 a[154:155], v88 offset:18464\n"
      "v_accvgpr_write_b32 a233, v34\n"
      "v_cvt_pk_bf16_f32 v228, v54, v55\n"
      "v_mfma_f32_16x16x32_bf16 v[28:31], a[28:31], v[80:83], 0\n"
      "s_waitcnt lgkmcnt(10)\n"
      "s_nop 0\n"
      "ds_read_b128 a[28:31], v84 offset:55392\n"
      "ds_read_b64_tr_b16 a[158:159], v87 offset:18528\n"
      "v_cvt_pk_bf16_f32 v229, v56, v57\n"
      "v_cvt_pk_bf16_f32 v236, v58, v59\n"
      "v_mfma_f32_16x16x32_bf16 v[32:35], a[0:3], v[68:71], v[28:31]\n"
      "ds_read_b64_tr_b16 a[186:187], v87 offset:0\n"
      "ds_read_b64_tr_b16 a[190:191], v88 offset:0\n"
      "buffer_load_dwordx4 v18, s[32:35], s51 offen offset:2048 lds\n"
      "v_cvt_pk_bf16_f32 v237, v60, v61\n"
      "v_mfma_f32_16x16x32_bf16 v[28:31], a[0:3], v[72:75], v[28:31]\n"
      "s_add_u32 m0, s46, 26560\n"
      "s_add_u32 s51, s47, 983040\n"
      "ds_read_b128 a[0:3], v84 offset:18464\n"
      "ds_read_b64_tr_b16 a[194:195], v87 offset:64\n"
      "v_cvt_pk_bf16_f32 v232, v62, v63\n"
      "v_cvt_pk_bf16_f32 v233, v64, v65\n"
      "v_mfma_f32_16x16x32_bf16 v[54:57], a[4:7], v[76:79], 0\n"
      "ds_read_b128 a[4:7], v85 offset:18464\n"
      "v_cvt_pk_bf16_f32 v20, v20, v21\n"
      "v_cvt_pk_bf16_f32 v22, v22, v23\n"
      "v_mfma_f32_16x16x32_bf16 v[58:61], a[8:11], v[68:71], v[54:57]\n"
      "buffer_load_dwordx4 v16, s[32:35], s51 offen lds\n"
      "v_accvgpr_write_b32 a240, v20\n"
      "v_accvgpr_write_b32 a241, v22\n"
      "v_mfma_f32_16x16x32_bf16 v[54:57], a[8:11], v[72:75], v[54:57]\n"
      "s_add_u32 m0, s46, 26560\n"
      "ds_read_b128 a[8:11], v86 offset:21600\n"
      "v_cvt_pk_bf16_f32 v240, v24, v25\n"
      "v_cvt_pk_bf16_f32 v241, v26, v27\n"
      "v_cvt_pk_bf16_f32 v248, v28, v29\n"
      "v_mfma_f32_16x16x32_bf16 v[62:65], a[12:15], v[76:79], 0\n"
      "v_cvt_pk_bf16_f32 v249, v30, v31\n"
      "v_cvt_pk_bf16_f32 v244, v32, v33\n"
      "v_mfma_f32_16x16x32_bf16 v[20:23], a[16:19], v[68:71], v[62:65]\n"
      "buffer_load_dwordx4 v17, s[32:35], s51 offen offset:1024 lds\n"
      "v_cvt_pk_bf16_f32 v245, v34, v35\n"
      "v_mfma_f32_16x16x32_bf16 v[62:65], a[16:19], v[72:75], v[62:65]\n"
      "s_add_u32 m0, s46, 26560\n"
      "s_sub_u32 s96, s51, s95\n"
      "ds_read_b128 a[16:19], v86 offset:3072\n"
      "v_cvt_pk_bf16_f32 v54, v54, v55\n"
      "v_cvt_pk_bf16_f32 v56, v56, v57\n"
      "v_accvgpr_write_b32 a244, v54\n"
      "v_mfma_f32_16x16x32_bf16 v[24:27], a[12:15], v[80:83], 0\n"
      "ds_read_b128 a[12:15], v84 offset:18528\n"
      "v_accvgpr_write_b32 a245, v56\n"
      "v_mfma_f32_16x16x32_bf16 v[28:31], a[20:23], v[68:71], v[24:27]\n"
      "buffer_load_dwordx4 v18, s[32:35], s51 offen offset:2048 lds\n"
      "v_cvt_pk_bf16_f32 v252, v58, v59\n"
      "v_cvt_pk_bf16_f32 v253, v60, v61\n"
      "v_mfma_f32_16x16x32_bf16 v[24:27], a[20:23], v[72:75], v[24:27]\n"
      "s_waitcnt lgkmcnt(2)\n"
      "s_mov_b32 m0, s59\n"
      "s_nop 0\n"
      "ds_read_b128 a[20:23], v84 offset:0\n"
      "v_cvt_pk_bf16_f32 v62, v62, v63\n"
      "v_cvt_pk_bf16_f32 v64, v64, v65\n"
      "v_mfma_f32_16x16x32_bf16 v[32:35], a[24:27], v[76:79], 0\n"
      "ds_read_b128 a[24:27], v85 offset:0\n"
      "buffer_load_dwordx4 v19, s[32:35], s96 offen lds\n"
      "v_mfma_f32_16x16x32_bf16 v[54:57], a[28:31], v[68:71], v[32:35]\n"
      "v_accvgpr_write_b32 a222, v62\n"
      "v_accvgpr_write_b32 a223, v64\n"
      "v_mfma_f32_16x16x32_bf16 v[32:35], a[28:31], v[72:75], v[32:35]\n"
      "ds_read_b128 a[28:31], v86 offset:3136\n"
      "v_cvt_pk_bf16_f32 v206, v20, v21\n"
      "v_cvt_pk_bf16_f32 v207, v22, v23\n"
      "v_cvt_pk_bf16_f32 v214, v24, v25\n"
      "v_mfma_f32_16x16x32_bf16 v[58:61], a[32:35], v[76:79], 0\n"
      "v_cvt_pk_bf16_f32 v215, v26, v27\n"
      "v_cvt_pk_bf16_f32 v210, v28, v29\n"
      "v_mfma_f32_16x16x32_bf16 v[62:65], a[0:3], v[68:71], v[58:61]\n"
      "v_cvt_pk_bf16_f32 v211, v30, v31\n"
      "v_cvt_pk_bf16_f32 v32, v32, v33\n"
      "v_mfma_f32_16x16x32_bf16 v[58:61], a[0:3], v[72:75], v[58:61]\n"
      "v_cvt_pk_bf16_f32 v34, v34, v35\n"
      "v_accvgpr_write_b32 a226, v32\n"
      "v_mfma_f32_16x16x32_bf16 v[20:23], a[32:35], v[80:83], 0\n"
      "ds_read_b128 a[32:35], v84 offset:64\n"
      "v_accvgpr_write_b32 a227, v34\n"
      "v_mfma_f32_16x16x32_bf16 v[24:27], a[4:7], v[68:71], v[20:23]\n"
      "v_cvt_pk_bf16_f32 v218, v54, v55\n"
      "v_cvt_pk_bf16_f32 v219, v56, v57\n"
      "v_mfma_f32_16x16x32_bf16 v[20:23], a[4:7], v[72:75], v[20:23]\n"
      "v_cvt_pk_bf16_f32 v226, v58, v59\n"
      "v_cvt_pk_bf16_f32 v227, v60, v61\n"
      "v_mfma_f32_16x16x32_bf16 v[28:31], a[8:11], v[76:79], 0\n"
      "s_waitcnt lgkmcnt(1)\n"
      "v_cvt_pk_bf16_f32 v222, v62, v63\n"
      "v_cvt_pk_bf16_f32 v223, v64, v65\n"
      "v_mfma_f32_16x16x32_bf16 v[32:35], a[12:15], v[68:71], v[28:31]\n"
      "v_cvt_pk_bf16_f32 v230, v24, v25\n"
      "v_cvt_pk_bf16_f32 v20, v20, v21\n"
      "v_mfma_f32_16x16x32_bf16 v[28:31], a[12:15], v[72:75], v[28:31]\n"
      "ds_read_b128 a[12:15], v6 offset:0\n"
      "v_cvt_pk_bf16_f32 v22, v22, v23\n"
      "v_mfma_f32_16x16x32_bf16 v[54:57], a[16:19], v[76:79], 0\n"
      "v_accvgpr_write_b32 a234, v20\n"
      "v_accvgpr_write_b32 a235, v22\n"
      "v_mfma_f32_16x16x32_bf16 v[58:61], a[20:23], v[68:71], v[54:57]\n"
      "v_cvt_pk_bf16_f32 v231, v26, v27\n"
      "v_cvt_pk_bf16_f32 v234, v32, v33\n"
      "v_mfma_f32_16x16x32_bf16 v[54:57], a[20:23], v[72:75], v[54:57]\n"
      "s_waitcnt lgkmcnt(0)\n"
      "s_waitcnt vmcnt(7)\n"
      "s_cbranch_vccnz Lconv_h11\n"
      "s_barrier\n"
      "Lconv_h11:\n"
      "ds_read_b128 a[20:23], v6 offset:512\n"
      "v_cvt_pk_bf16_f32 v238, v28, v29\n"
      "v_mfma_f32_16x16x32_bf16 v[62:65], a[16:19], v[80:83], 0\n"
      "ds_read_b128 a[16:19], v6 offset:256\n"
      "v_cvt_pk_bf16_f32 v239, v30, v31\n"
      "v_mfma_f32_16x16x32_bf16 v[20:23], a[24:27], v[68:71], v[62:65]\n"
      "s_cbranch_vccz Lconv_h12\n"
      "s_barrier\n"
      "Lconv_h12:\n"
      "s_nop 0\n"
      "v_cvt_pk_bf16_f32 v235, v34, v35\n"
      "v_mfma_f32_16x16x32_bf16 v[62:65], a[24:27], v[72:75], v[62:65]\n"
      "ds_read_b128 a[24:27], v6 offset:4608\n"
      "v_cvt_pk_bf16_f32 v242, v58, v59\n"
      "v_cvt_pk_bf16_f32 v243, v60, v61\n"
      "v_mfma_f32_16x16x32_bf16 v[24:27], a[28:31], v[76:79], 0\n"
      "ds_read_b128 a[28:31], v6 offset:4864\n"
      "v_cvt_pk_bf16_f32 v54, v54, v55\n"
      "v_mfma_f32_16x16x32_bf16 v[28:31], a[32:35], v[68:71], v[24:27]\n"
      "v_cvt_pk_bf16_f32 v56, v56, v57\n"
      "v_accvgpr_write_b32 a242, v54\n"
      "v_mfma_f32_16x16x32_bf16 v[24:27], a[32:35], v[72:75], v[24:27]\n"
      "ds_read_b128 a[32:35], v6 offset:5120\n"
      "v_accvgpr_write_b32 a243, v56\n"
      "v_cvt_pk_bf16_f32 v246, v20, v21\n"
      "v_cvt_pk_bf16_f32 v247, v22, v23\n"
      "v_cvt_pk_bf16_f32 v250, v62, v63\n"
      "v_cvt_pk_bf16_f32 v251, v64, v65\n"
      "v_cvt_pk_bf16_f32 v254, v28, v29\n"
      "v_cvt_pk_bf16_f32 v255, v30, v31\n"
      "v_cvt_pk_bf16_f32 v24, v24, v25\n"
      "v_cvt_pk_bf16_f32 v26, v26, v27\n"
      "v_accvgpr_write_b32 a246, v24\n"
      "v_accvgpr_write_b32 a247, v26\n"
      "s_mov_b32 m0, s18\n"
      "ds_read_b128 a[48:51], v2 offset:39840\n"
      "v_lshlrev_b32 v9, 7, v5\n"
      "v_mfma_f32_16x16x32_bf16 v[68:71], a[12:15], a[88:91], 0\n"
      "s_waitcnt lgkmcnt(4)\n"
      "ds_read_b128 a[52:55], v3 offset:39840\n"
      "v_min_u32 v9, Lconv_u0-Lconv_t0, v9\n"
      "v_mfma_f32_16x16x32_bf16 v[68:71], a[16:19], a[100:103], v[68:71]\n"
      "s_mul_i32 s97, s41, Lconv_t1-Lconv_t0\n"
      "ds_read_b128 a[56:59], v4 offset:39840\n"
      "v_add_u32 v9, Lconv_t0-Lconv_start, v9\n"
      "v_mfma_f32_16x16x32_bf16 v[68:71], a[20:23], a[112:115], v[68:71]\n"
      "s_waitcnt lgkmcnt(5)\n"
      "ds_read_b128 a[36:39], v2 offset:39904\n"
      "v_add_u32 v9, s97, v9\n"
      "v_mfma_f32_16x16x32_bf16 v[68:71], a[24:27], a[124:127], v[68:71]\n"
      "s_waitcnt lgkmcnt(5)\n"
      "ds_read_b128 a[40:43], v3 offset:39904\n"
      "v_mfma_f32_16x16x32_bf16 v[68:71], a[28:31], a[136:139], v[68:71]\n"
      "s_waitcnt lgkmcnt(4)\n"
      "ds_read_b128 a[44:47], v4 offset:39904\n"
      "v_mfma_f32_16x16x32_bf16 v[52:55], a[48:51], a[72:75], 0\n"
      "ds_read_b128 a[0:3], v6 offset:768\n"
      "v_mfma_f32_16x16x32_bf16 v[68:71], a[32:35], a[148:151], v[68:71]\n"
      "ds_read_b128 a[48:51], v2 offset:39968\n"
      "v_mfma_f32_16x16x32_bf16 v[72:75], a[12:15], a[92:95], 0\n"
      "s_waitcnt lgkmcnt(6)\n"
      "ds_read_b128 a[4:7], v6 offset:1024\n"
      "v_mfma_f32_16x16x32_bf16 v[56:59], a[52:55], a[76:79], 0\n"
      "ds_read_b128 a[8:11], v6 offset:1280\n"
      "s_nop 0\n"
      "v_mfma_f32_16x16x32_bf16 v[72:75], a[16:19], a[104:107], v[72:75]\n"
      "ds_read_b128 a[52:55], v3 offset:39968\n"
      "s_nop 0\n"
      "v_mfma_f32_16x16x32_bf16 v[72:75], a[20:23], a[116:119], v[72:75]\n"
      "s_waitcnt lgkmcnt(7)\n"
      "ds_read_b128 a[60:63], v6 offset:1536\n"
      "v_fma_f32 v64, s44, v52, v56\n"
      "v_mfma_f32_16x16x32_bf16 v[60:63], a[56:59], a[80:83], v[52:55]\n"
      "ds_read_b128 a[56:59], v4 offset:39968\n"
      "v_fma_f32 v65, s44, v53, v57\n"
      "v_mfma_f32_16x16x32_bf16 v[72:75], a[24:27], a[128:131], v[72:75]\n"
      "v_cvt_pk_bf16_f32 v64, v64, v65\n"
      "v_fma_f32 v66, s44, v54, v58\n"
      "v_mfma_f32_16x16x32_bf16 v[72:75], a[28:31], a[140:143], v[72:75]\n"
      "ds_write_addtid_b32 v64 offset:9984\n"
      "v_fma_f32 v67, s44, v55, v59\n"
      "v_mfma_f32_16x16x32_bf16 v[72:75], a[32:35], a[152:155], v[72:75]\n"
      "v_cvt_pk_bf16_f32 v65, v66, v67\n"
      "v_fma_f32 v56, s44, v52, -v56\n"
      "v_mfma_f32_16x16x32_bf16 v[36:39], a[36:39], a[72:75], 0\n"
      "ds_write_addtid_b32 v65 offset:46912\n"
      "v_fma_f32 v57, s44, v53, -v57\n"
      "v_mfma_f32_16x16x32_bf16 v[76:79], a[12:15], a[96:99], 0\n"
      "ds_read_b128 a[12:15], v6 offset:5376\n"
      "v_cvt_pk_bf16_f32 v56, v56, v57\n"
      "v_mfma_f32_16x16x32_bf16 v[76:79], a[16:19], a[108:111], v[76:79]\n"
      "s_waitcnt lgkmcnt(5)\n"
      "ds_read_b128 a[16:19], v6 offset:5632\n"
      "v_fma_f32 v58, s44, v54, -v58\n"
      "v_mfma_f32_16x16x32_bf16 v[40:43], a[40:43], a[76:79], 0\n"
      "ds_write_addtid_b32 v56 offset:10752\n"
      "v_fma_f32 v59, s44, v55, -v59\n"
      "v_mfma_f32_16x16x32_bf16 v[76:79], a[20:23], a[120:123], v[76:79]\n"
      "ds_read_b128 a[20:23], v6 offset:5888\n"
      "v_cvt_pk_bf16_f32 v57, v58, v59\n"
      "v_mfma_f32_16x16x32_bf16 v[76:79], a[24:27], a[132:135], v[76:79]\n"
      "ds_write_addtid_b32 v57 offset:47680\n"
      "v_cvt_pk_bf16_f32 v60, v60, v61\n"
      "v_mfma_f32_16x16x32_bf16 v[44:47], a[44:47], a[80:83], v[36:39]\n"
      "ds_write_addtid_b32 v60 offset:9216\n"
      "v_cvt_pk_bf16_f32 v61, v62, v63\n"
      "v_mfma_f32_16x16x32_bf16 v[76:79], a[28:31], a[144:147], v[76:79]\n"
      "ds_write_addtid_b32 v61 offset:46144\n"
      "v_fma_f32 v48, s44, v36, v40\n"
      "v_mfma_f32_16x16x32_bf16 v[76:79], a[32:35], a[156:159], v[76:79]\n"
      "ds_read_b128 a[36:39], v2 offset:0\n"
      "v_fma_f32 v49, s44, v37, v41\n"
      "v_mfma_f32_16x16x32_bf16 v[80:83], a[0:3], v[92:95], 0\n"
      "v_cvt_pk_bf16_f32 v48, v48, v49\n"
      "v_fma_f32 v50, s44, v38, v42\n"
      "v_mfma_f32_16x16x32_bf16 v[52:55], a[48:51], a[72:75], 0\n"
      "ds_write_addtid_b32 v48 offset:10240\n"
      "v_fma_f32 v51, s44, v39, v43\n"
      "v_mfma_f32_16x16x32_bf16 v[80:83], a[4:7], v[148:151], v[80:83]\n"
      "ds_read_b128 a[40:43], v3 offset:0\n"
      "v_cvt_pk_bf16_f32 v49, v50, v51\n"
      "v_mfma_f32_16x16x32_bf16 v[80:83], a[8:11], v[204:207], v[80:83]\n"
      "ds_write_addtid_b32 v49 offset:47168\n"
      "v_fma_f32 v40, s44, v36, -v40\n"
      "v_mfma_f32_16x16x32_bf16 v[56:59], a[52:55], a[76:79], 0\n"
      "s_waitcnt lgkmcnt(7)\n"
      "v_fma_f32 v41, s44, v37, -v41\n"
      "v_cvt_pk_bf16_f32 v40, v40, v41\n"
      "v_mfma_f32_16x16x32_bf16 v[80:83], a[12:15], v[112:115], v[80:83]\n"
      "ds_write_addtid_b32 v40 offset:11008\n"
      "v_fma_f32 v42, s44, v38, -v42\n"
      "v_mfma_f32_16x16x32_bf16 v[80:83], a[16:19], a[208:211], v[80:83]\n"
      "v_fma_f32 v43, s44, v39, -v43\n"
      "v_cvt_pk_bf16_f32 v41, v42, v43\n"
      "v_mfma_f32_16x16x32_bf16 v[60:63], a[56:59], a[80:83], v[52:55]\n"
      "ds_write_addtid_b32 v41 offset:47936\n"
      "v_cvt_pk_bf16_f32 v44, v44, v45\n"
      "v_mfma_f32_16x16x32_bf16 v[80:83], a[20:23], v[220:223], v[80:83]\n"
      "ds_write_addtid_b32 v44 offset:9472\n"
      "v_cvt_pk_bf16_f32 v45, v46, v47\n"
      "v_mfma_f32_16x16x32_bf16 v[84:87], a[0:3], v[100:103], 0\n"
      "ds_read_b128 a[44:47], v4 offset:0\n"
      "v_fma_f32 v64, s44, v52, v56\n"
      "v_mfma_f32_16x16x32_bf16 v[84:87], a[4:7], a[200:203], v[84:87]\n"
      "ds_write_addtid_b32 v45 offset:46400\n"
      "v_fma_f32 v65, s44, v53, v57\n"
      "v_mfma_f32_16x16x32_bf16 v[84:87], a[8:11], v[208:211], v[84:87]\n"
      "ds_read_b128 a[64:67], v6 offset:1792\n"
      "v_cvt_pk_bf16_f32 v64, v64, v65\n"
      "v_mfma_f32_16x16x32_bf16 v[84:87], a[12:15], a[204:207], v[84:87]\n"
      "ds_read_b128 a[68:71], v6 offset:2048\n"
      "v_fma_f32 v66, s44, v54, v58\n"
      "v_mfma_f32_16x16x32_bf16 v[84:87], a[16:19], v[172:175], v[84:87]\n"
      "ds_read_b128 a[24:27], v6 offset:6144\n"
      "v_fma_f32 v67, s44, v55, v59\n"
      "v_mfma_f32_16x16x32_bf16 v[84:87], a[20:23], v[228:231], v[84:87]\n"
      "ds_read_b128 a[48:51], v2 offset:64\n"
      "v_cvt_pk_bf16_f32 v65, v66, v67\n"
      "v_mfma_f32_16x16x32_bf16 v[24:27], a[0:3], a[196:199], 0\n"
      "ds_write_addtid_b32 v64 offset:10496\n"
      "v_fma_f32 v56, s44, v52, -v56\n"
      "v_mfma_f32_16x16x32_bf16 v[24:27], a[4:7], v[160:163], v[24:27]\n"
      "s_waitcnt lgkmcnt(11)\n"
      "ds_read_b128 a[28:31], v6 offset:6400\n"
      "v_fma_f32 v57, s44, v53, -v57\n"
      "v_mfma_f32_16x16x32_bf16 v[36:39], a[36:39], a[72:75], 0\n"
      "ds_write_addtid_b32 v65 offset:47424\n"
      "v_cvt_pk_bf16_f32 v56, v56, v57\n"
      "v_mfma_f32_16x16x32_bf16 v[24:27], a[8:11], v[216:219], v[24:27]\n"
      "ds_read_b128 a[32:35], v6 offset:6656\n"
      "v_fma_f32 v58, s44, v54, -v58\n"
      "v_mfma_f32_16x16x32_bf16 v[24:27], a[12:15], v[124:127], v[24:27]\n"
      "ds_write_addtid_b32 v56 offset:11264\n"
      "v_fma_f32 v59, s44, v55, -v59\n"
      "v_mfma_f32_16x16x32_bf16 v[40:43], a[40:43], a[76:79], 0\n"
      "ds_read_b128 a[52:55], v3 offset:64\n"
      "v_cvt_pk_bf16_f32 v57, v58, v59\n"
      "v_mfma_f32_16x16x32_bf16 v[24:27], a[16:19], v[180:183], v[24:27]\n"
      "ds_write_addtid_b32 v57 offset:48192\n"
      "v_cvt_pk_bf16_f32 v60, v60, v61\n"
      "v_mfma_f32_16x16x32_bf16 v[24:27], a[20:23], v[232:235], v[24:27]\n"
      "s_waitcnt lgkmcnt(7)\n"
      "ds_write_addtid_b32 v60 offset:9728\n"
      "v_cvt_pk_bf16_f32 v61, v62, v63\n"
      "v_mfma_f32_16x16x32_bf16 v[44:47], a[44:47], a[80:83], v[36:39]\n"
      "ds_write_addtid_b32 v61 offset:46656\n"
      "v_fma_f32 v48, s44, v36, v40\n"
      "v_mfma_f32_16x16x32_bf16 v[28:31], a[60:63], v[96:99], 0\n"
      "ds_read_b128 a[56:59], v4 offset:64\n"
      "v_fma_f32 v49, s44, v37, v41\n"
      "v_mfma_f32_16x16x32_bf16 v[28:31], a[64:67], v[152:155], v[28:31]\n"
      "ds_read_b128 a[36:39], v2 offset:128\n"
      "v_cvt_pk_bf16_f32 v48, v48, v49\n"
      "v_mfma_f32_16x16x32_bf16 v[28:31], a[68:71], a[220:223], v[28:31]\n"
      "ds_read_b128 a[40:43], v3 offset:128\n"
      "v_fma_f32 v50, s44, v38, v42\n"
      "v_mfma_f32_16x16x32_bf16 v[28:31], a[24:27], v[116:119], v[28:31]\n"
      "s_waitcnt lgkmcnt(0)\n"
      "s_cbranch_vccnz Lconv_h13\n"
      "s_barrier\n"
      "Lconv_h13:\n"
      "v_fma_f32 v51, s44, v39, v43\n"
      "v_mfma_f32_16x16x32_bf16 v[52:55], a[48:51], a[72:75], 0\n"
      "ds_read_b128 a[44:47], v4 offset:128\n"
      "v_cvt_pk_bf16_f32 v49, v50, v51\n"
      "v_mfma_f32_16x16x32_bf16 v[28:31], a[28:31], v[168:171], v[28:31]\n"
      "ds_write_addtid_b32 v48 offset:14592\n"
      "v_fma_f32 v40, s44, v36, -v40\n"
      "v_mfma_f32_16x16x32_bf16 v[28:31], a[32:35], v[224:227], v[28:31]\n"
      "ds_write_addtid_b32 v49 offset:51520\n"
      "v_fma_f32 v41, s44, v37, -v41\n"
      "v_mfma_f32_16x16x32_bf16 v[56:59], a[52:55], a[76:79], 0\n"
      "s_cbranch_vccz Lconv_h14\n"
      "s_barrier\n"
      "Lconv_h14:\n"
      "v_cvt_pk_bf16_f32 v40, v40, v41\n"
      "v_mfma_f32_16x16x32_bf16 v[20:23], a[60:63], v[104:107], 0\n"
      "s_mov_b64 vcc, 0x2000000\n"
      "ds_write_addtid_b32 v40 offset:15360\n"
      "v_fma_f32 v42, s44, v38, -v42\n"
      "v_mfma_f32_16x16x32_bf16 v[20:23], a[64:67], v[156:159], v[20:23]\n"
      "ds_read_b128 a[0:3], v7 offset:0\n"
      "v_fma_f32 v43, s44, v39, -v43\n"
      "v_mfma_f32_16x16x32_bf16 v[60:63], a[56:59], a[80:83], v[52:55]\n"
      "v_cvt_pk_bf16_f32 v41, v42, v43\n"
      "v_cvt_pk_bf16_f32 v44, v44, v45\n"
      "v_mfma_f32_16x16x32_bf16 v[20:23], a[68:71], v[212:215], v[20:23]\n"
      "ds_write_addtid_b32 v41 offset:52288\n"
      "v_cvt_pk_bf16_f32 v45, v46, v47\n"
      "v_mfma_f32_16x16x32_bf16 v[20:23], a[24:27], v[120:123], v[20:23]\n"
      "ds_write_addtid_b32 v44 offset:13824\n"
      "ds_write_addtid_b32 v45 offset:50752\n"
      "v_mfma_f32_16x16x32_bf16 v[20:23], a[28:31], v[176:179], v[20:23]\n"
      "ds_read_b128 a[4:7], v7 offset:256\n"
      "v_fma_f32 v64, s44, v52, v56\n"
      "v_mfma_f32_16x16x32_bf16 v[36:39], a[36:39], a[72:75], 0\n"
      "ds_read_b128 a[8:11], v7 offset:512\n"
      "v_fma_f32 v65, s44, v53, v57\n"
      "v_mfma_f32_16x16x32_bf16 v[20:23], a[32:35], a[232:235], v[20:23]\n"
      "v_fma_f32 v66, s44, v54, v58\n"
      "v_fma_f32 v67, s44, v55, v59\n"
      "v_mfma_f32_16x16x32_bf16 v[32:35], a[60:63], v[108:111], 0\n"
      "v_fma_f32 v56, s44, v52, -v56\n"
      "v_fma_f32 v57, s44, v53, -v57\n"
      "v_mfma_f32_16x16x32_bf16 v[40:43], a[40:43], a[76:79], 0\n"
      "v_fma_f32 v58, s44, v54, -v58\n"
      "v_fma_f32 v59, s44, v55, -v59\n"
      "v_mfma_f32_16x16x32_bf16 v[32:35], a[64:67], v[164:167], v[32:35]\n"
      "v_cvt_pk_bf16_f32 v64, v64, v65\n"
      "v_cvt_pk_bf16_f32 v65, v66, v67\n"
      "v_mfma_f32_16x16x32_bf16 v[32:35], a[68:71], a[224:227], v[32:35]\n"
      "s_waitcnt lgkmcnt(5)\n"
      "v_cvt_pk_bf16_f32 v56, v56, v57\n"
      "v_cvt_pk_bf16_f32 v57, v58, v59\n"
      "v_mfma_f32_16x16x32_bf16 v[44:47], a[44:47], a[80:83], v[36:39]\n"
      "ds_write_addtid_b32 v64 offset:14848\n"
      "v_cvt_pk_bf16_f32 v60, v60, v61\n"
      "v_mfma_f32_16x16x32_bf16 v[32:35], a[24:27], v[128:131], v[32:35]\n"
      "ds_write_addtid_b32 v65 offset:51776\n"
      "v_cvt_pk_bf16_f32 v61, v62, v63\n"
      "v_mfma_f32_16x16x32_bf16 v[32:35], a[28:31], a[228:231], v[32:35]\n"
      "ds_write_addtid_b32 v56 offset:15616\n"
      "ds_write_addtid_b32 v57 offset:52544\n"
      "v_mfma_f32_16x16x32_bf16 v[32:35], a[32:35], v[236:239], v[32:35]\n"
      "ds_write_addtid_b32 v60 offset:14080\n"
      "ds_write_addtid_b32 v61 offset:51008\n"
      "v_mfma_f32_16x16x32_bf16 v[68:71], a[0:3], a[160:163], v[68:71]\n"
      "s_waitcnt lgkmcnt(6)\n"
      "ds_read_b128 a[12:15], v7 offset:768\n"
      "v_fma_f32 v48, s44, v36, v40\n"
      "v_mfma_f32_16x16x32_bf16 v[68:71], a[4:7], a[172:175], v[68:71]\n"
      "s_add_u32 m0, s46, 39840\n"
      "ds_read_b128 a[16:19], v7 offset:1024\n"
      "v_fma_f32 v49, s44, v37, v41\n"
      "v_mfma_f32_16x16x32_bf16 v[68:71], a[8:11], a[184:187], v[68:71]\n"
      "s_add_u32 s51, s47, 1179648\n"
      "ds_read_b128 a[20:23], v7 offset:1280\n"
      "v_cvt_pk_bf16_f32 v48, v48, v49\n"
      "v_mfma_f32_16x16x32_bf16 v[72:75], a[0:3], a[164:167], v[72:75]\n"
      "buffer_load_dwordx4 v16, s[32:35], s51 offen lds\n"
      "v_fma_f32 v50, s44, v38, v42\n"
      "v_mfma_f32_16x16x32_bf16 v[72:75], a[4:7], a[176:179], v[72:75]\n"
      "s_add_u32 s48, s47, 3342336\n"
      "v_fma_f32 v51, s44, v39, v43\n"
      "v_cvt_pk_bf16_f32 v49, v50, v51\n"
      "v_mfma_f32_16x16x32_bf16 v[72:75], a[8:11], a[188:191], v[72:75]\n"
      "s_add_u32 s47, s47, 3145728\n"
      "v_fma_f32 v40, s44, v36, -v40\n"
      "v_fma_f32 v41, s44, v37, -v41\n"
      "v_mfma_f32_16x16x32_bf16 v[76:79], a[0:3], a[168:171], v[76:79]\n"
      "buffer_load_dwordx4 v17, s[32:35], s51 offen offset:1024 lds\n"
      "v_cvt_pk_bf16_f32 v40, v40, v41\n"
      "v_mfma_f32_16x16x32_bf16 v[76:79], a[4:7], a[180:183], v[76:79]\n"
      "ds_read_b128 a[0:3], v7 offset:1536\n"
      "v_fma_f32 v42, s44, v38, -v42\n"
      "v_mfma_f32_16x16x32_bf16 v[76:79], a[8:11], a[192:195], v[76:79]\n"
      "s_waitcnt lgkmcnt(3)\n"
      "ds_read_b128 a[4:7], v7 offset:1792\n"
      "v_fma_f32 v43, s44, v39, -v43\n"
      "v_mfma_f32_16x16x32_bf16 v[80:83], a[12:15], a[212:215], v[80:83]\n"
      "s_waitcnt lgkmcnt(3)\n"
      "ds_read_b128 a[8:11], v7 offset:2048\n"
      "v_cvt_pk_bf16_f32 v41, v42, v43\n"
      "v_mfma_f32_16x16x32_bf16 v[80:83], a[16:19], v[184:187], v[80:83]\n"
      "s_waitcnt lgkmcnt(3)\n"
      "v_cvt_pk_bf16_f32 v44, v44, v45\n"
      "v_cvt_pk_bf16_f32 v45, v46, v47\n"
      "v_mfma_f32_16x16x32_bf16 v[80:83], a[20:23], v[240:243], v[80:83]\n"
      "buffer_load_dwordx4 v18, s[32:35], s51 offen offset:2048 lds\n"
      "v_cvt_pk_bf16_f32 v68, v68, v69\n"
      "v_mfma_f32_16x16x32_bf16 v[84:87], a[12:15], v[136:139], v[84:87]\n"
      "s_mov_b32 m0, s18\n"
      "v_cvt_pk_bf16_f32 v69, v70, v71\n"
      "v_cvt_pk_bf16_f32 v72, v72, v73\n"
      "v_mfma_f32_16x16x32_bf16 v[84:87], a[16:19], v[192:195], v[84:87]\n"
      "ds_write_addtid_b32 v48 offset:15104\n"
      "ds_write_addtid_b32 v49 offset:52032\n"
      "v_mfma_f32_16x16x32_bf16 v[84:87], a[20:23], v[244:247], v[84:87]\n"
      "ds_write_addtid_b32 v40 offset:15872\n"
      "ds_write_addtid_b32 v41 offset:52800\n"
      "v_mfma_f32_16x16x32_bf16 v[24:27], a[12:15], a[216:219], v[24:27]\n"
      "ds_write_addtid_b32 v44 offset:14336\n"
      "ds_write_addtid_b32 v45 offset:51264\n"
      "v_mfma_f32_16x16x32_bf16 v[24:27], a[16:19], v[196:199], v[24:27]\n"
      "s_mov_b32 m0, s45\n"
      "v_cvt_pk_bf16_f32 v73, v74, v75\n"
      "v_cvt_pk_bf16_f32 v76, v76, v77\n"
      "v_mfma_f32_16x16x32_bf16 v[24:27], a[20:23], v[252:255], v[24:27]\n"
      "s_waitcnt lgkmcnt(6)\n"
      "ds_write_addtid_b32 v68 offset:57360\n"
      "ds_write_addtid_b32 v69 offset:57616\n"
      "v_mfma_f32_16x16x32_bf16 v[28:31], a[0:3], v[132:135], v[28:31]\n"
      "ds_write_addtid_b32 v72 offset:45064\n"
      "ds_write_addtid_b32 v73 offset:45320\n"
      "v_mfma_f32_16x16x32_bf16 v[28:31], a[4:7], v[188:191], v[28:31]\n"
      "ds_write_addtid_b32 v76 offset:32768\n"
      "v_cvt_pk_bf16_f32 v77, v78, v79\n"
      "v_mfma_f32_16x16x32_bf16 v[28:31], a[8:11], a[240:243], v[28:31]\n"
      "ds_write_addtid_b32 v77 offset:33024\n"
      "v_cvt_pk_bf16_f32 v80, v80, v81\n"
      "v_mfma_f32_16x16x32_bf16 v[20:23], a[0:3], v[140:143], v[20:23]\n"
      "ds_write_addtid_b32 v80 offset:58384\n"
      "v_cvt_pk_bf16_f32 v81, v82, v83\n"
      "v_mfma_f32_16x16x32_bf16 v[20:23], a[4:7], a[236:239], v[20:23]\n"
      "ds_write_addtid_b32 v81 offset:58640\n"
      "v_cvt_pk_bf16_f32 v84, v84, v85\n"
      "v_mfma_f32_16x16x32_bf16 v[20:23], a[8:11], v[248:251], v[20:23]\n"
      "ds_write_addtid_b32 v84 offset:46088\n"
      "v_cvt_pk_bf16_f32 v85, v86, v87\n"
      "v_mfma_f32_16x16x32_bf16 v[32:35], a[0:3], v[144:147], v[32:35]\n"
      "ds_write_addtid_b32 v85 offset:46344\n"
      "v_cvt_pk_bf16_f32 v24, v24, v25\n"
      "v_mfma_f32_16x16x32_bf16 v[32:35], a[4:7], v[200:203], v[32:35]\n"
      "ds_write_addtid_b32 v24 offset:33792\n"
      "v_cvt_pk_bf16_f32 v25, v26, v27\n"
      "v_mfma_f32_16x16x32_bf16 v[32:35], a[8:11], a[244:247], v[32:35]\n"
      "ds_write_addtid_b32 v25 offset:34048\n"
      "v_cvt_pk_bf16_f32 v28, v28, v29\n"
      "v_cvt_pk_bf16_f32 v29, v30, v31\n"
      "ds_write_addtid_b32 v28 offset:59408\n"
      "ds_write_addtid_b32 v29 offset:59664\n"
      "v_cvt_pk_bf16_f32 v20, v20, v21\n"
      "v_cvt_pk_bf16_f32 v21, v22, v23\n"
      "ds_write_addtid_b32 v20 offset:47112\n"
      "ds_write_addtid_b32 v21 offset:47368\n"
      "s_waitcnt vmcnt(3)\n"
      "s_waitcnt lgkmcnt(0)\n"
      "s_cmp_eq_u32 s41, 1\n"
      "s_cbranch_scc1 Lconv_s1\n"
      "Lconv_s0:\n"
      "s_branch Lconv_loop0\n"
      "Lconv_s1:\n"
      "s_branch Lconv_loop1\n"
      ".p2align 12\n"
      "Lconv_pa0:\n"
      "s_cbranch_execz Lconv_r0\n"
      "s_nop 0\n"
      "Lconv_loop0:\n"
      "v_mfma_f32_16x16x32_bf16 v[20:23], a[24:27], v[96:99], 0\n"
      "s_add_u32 s51, s51, 196608\n"
      "s_nop 1\n"
      "v_mfma_f32_16x16x32_bf16 v[20:23], a[28:31], v[152:155], v[20:23]\n"
      "s_sub_u32 s58, s93, s58\n"
      "v_cvt_pk_bf16_f32 v32, v32, v33\n"
      "v_cvt_pk_bf16_f32 v33, v34, v35\n"
      "v_mfma_f32_16x16x32_bf16 v[20:23], a[32:35], a[220:223], v[20:23]\n"
      "s_sub_u32 s59, s94, s59\n"
      "ds_write_addtid_b32 v32 offset:34816\n"
      "v_mfma_f32_16x16x32_bf16 v[20:23], a[0:3], v[116:119], v[20:23]\n"
      "s_sub_u32 s47, s51, 3342336\n"
      "ds_write_addtid_b32 v33 offset:35072\n"
      "v_mfma_f32_16x16x32_bf16 v[20:23], a[4:7], v[168:171], v[20:23]\n"
      "s_cmp_gt_u32 s51, s48\n"
      "v_mfma_f32_16x16x32_bf16 v[20:23], a[8:11], v[224:227], v[20:23]\n"
      "s_cselect_b32 s53, s47, s51\n"
      "v_mfma_f32_16x16x32_bf16 v[24:27], a[24:27], v[104:107], 0\n"
      "s_waitcnt vmcnt(8) lgkmcnt(0)\n"
      "s_barrier\n"
      "v_mfma_f32_16x16x32_bf16 v[24:27], a[28:31], v[156:159], v[24:27]\n"
      "s_mov_b32 m0, s58\n"
      "ds_read_b128 a[12:15], v7 offset:6144\n"
      "v_mfma_f32_16x16x32_bf16 v[24:27], a[32:35], v[212:215], v[24:27]\n"
      "ds_read_b128 a[16:19], v7 offset:6400\n"
      "v_mfma_f32_16x16x32_bf16 v[24:27], a[0:3], v[120:123], v[24:27]\n"
      "ds_read_b128 a[20:23], v7 offset:6656\n"
      "v_mfma_f32_16x16x32_bf16 v[24:27], a[4:7], v[176:179], v[24:27]\n"
      "ds_read_b128 a[48:51], v2 offset:13280\n"
      "v_mfma_f32_16x16x32_bf16 v[24:27], a[8:11], a[232:235], v[24:27]\n"
      "ds_read_b128 a[52:55], v3 offset:13280\n"
      "v_mfma_f32_16x16x32_bf16 v[68:71], a[84:87], a[60:63], 0\n"
      "ds_read_b128 a[56:59], v4 offset:13280\n"
      "s_nop 0\n"
      "v_mfma_f32_16x16x32_bf16 v[28:31], a[24:27], v[108:111], 0\n"
      "ds_read2_b64 a[64:67], v10 offset0:0 offset1:64\n"
      "s_nop 0\n"
      "v_mfma_f32_16x16x32_bf16 v[28:31], a[28:31], v[164:167], v[28:31]\n"
      "buffer_load_dwordx4 v16, s[32:35], s53 offen lds\n"
      "v_mfma_f32_16x16x32_bf16 v[28:31], a[32:35], a[224:227], v[28:31]\n"
      "s_mov_b32 m0, s45\n"
      "ds_read2_b64 a[68:71], v10 offset0:1 offset1:65\n"
      "v_cvt_pk_bf16_f32 v85, v68, v69\n"
      "v_mfma_f32_16x16x32_bf16 v[28:31], a[0:3], v[128:131], v[28:31]\n"
      "ds_read_b128 a[24:27], v6 offset:5376\n"
      "v_cvt_pk_bf16_f32 v91, v70, v71\n"
      "v_mfma_f32_16x16x32_bf16 v[28:31], a[4:7], a[228:231], v[28:31]\n"
      "ds_read_b128 a[28:31], v6 offset:5632\n"
      "v_mfma_f32_16x16x32_bf16 v[28:31], a[8:11], v[236:239], v[28:31]\n"
      "s_waitcnt lgkmcnt(6)\n"
      "ds_read_b128 a[32:35], v6 offset:5888\n"
      "v_mfma_f32_16x16x32_bf16 v[20:23], a[12:15], v[132:135], v[20:23]\n"
      "buffer_store_dwordx2 v[84:85], v15, s[36:39], s52 offen sc1\n"
      "v_mfma_f32_16x16x32_bf16 v[20:23], a[16:19], v[188:191], v[20:23]\n"
      "ds_read_b128 a[0:3], v7 offset:768\n"
      "v_mfma_f32_16x16x32_bf16 v[20:23], a[20:23], a[240:243], v[20:23]\n"
      "ds_read_b128 a[4:7], v7 offset:1024\n"
      "s_nop 0\n"
      "v_mfma_f32_16x16x32_bf16 v[52:55], a[48:51], a[72:75], 0\n"
      "s_waitcnt lgkmcnt(6)\n"
      "ds_read_b128 a[8:11], v7 offset:1280\n"
      "s_nop 0\n"
      "v_mfma_f32_16x16x32_bf16 v[56:59], a[52:55], a[76:79], 0\n"
      "ds_read_b128 a[36:39], v2 offset:13344\n"
      "v_cvt_pk_bf16_f32 v20, v20, v21\n"
      "v_mfma_f32_16x16x32_bf16 v[60:63], a[56:59], a[80:83], v[52:55]\n"
      "buffer_store_dwordx2 v[90:91], v15, s[36:39], s52 offen offset:1536 sc1\n"
      "v_cvt_pk_bf16_f32 v21, v22, v23\n"
      "v_mfma_f32_16x16x32_bf16 v[24:27], a[12:15], v[140:143], v[24:27]\n"
      "s_add_u32 s52, s52, 196608\n"
      "ds_write_addtid_b32 v20 offset:62480\n"
      "v_fma_f32 v64, s44, v52, v56\n"
      "v_mfma_f32_16x16x32_bf16 v[24:27], a[16:19], a[236:239], v[24:27]\n"
      "s_sub_u32 s96, s53, s95\n"
      "ds_write_addtid_b32 v21 offset:62736\n"
      "v_fma_f32 v65, s44, v53, v57\n"
      "v_mfma_f32_16x16x32_bf16 v[24:27], a[20:23], v[248:251], v[24:27]\n"
      "ds_read_b128 a[40:43], v3 offset:13344\n"
      "v_cvt_pk_bf16_f32 v64, v64, v65\n"
      "v_mfma_f32_16x16x32_bf16 v[72:75], a[84:87], a[64:67], 0\n"
      "ds_read_b128 a[44:47], v4 offset:13344\n"
      "v_fma_f32 v66, s44, v54, v58\n"
      "v_mfma_f32_16x16x32_bf16 v[28:31], a[12:15], v[144:147], v[28:31]\n"
      "ds_read_b128 a[12:15], v7 offset:5376\n"
      "v_fma_f32 v67, s44, v55, v59\n"
      "v_mfma_f32_16x16x32_bf16 v[28:31], a[16:19], v[200:203], v[28:31]\n"
      "ds_read_b128 a[16:19], v7 offset:5632\n"
      "v_cvt_pk_bf16_f32 v24, v24, v25\n"
      "v_mfma_f32_16x16x32_bf16 v[28:31], a[20:23], a[244:247], v[28:31]\n"
      "s_waitcnt lgkmcnt(6)\n"
      "ds_read_b128 a[20:23], v7 offset:5888\n"
      "v_cvt_pk_bf16_f32 v25, v26, v27\n"
      "v_mfma_f32_16x16x32_bf16 v[76:79], a[84:87], a[68:71], 0\n"
      "ds_write_addtid_b32 v24 offset:50184\n"
      "ds_write_addtid_b32 v25 offset:50440\n"
      "v_mfma_f32_16x16x32_bf16 v[32:35], a[24:27], v[92:95], 0\n"
      "s_mov_b32 m0, s18\n"
      "ds_read2_b64 a[60:63], v10 offset0:2 offset1:66\n"
      "v_cvt_pk_bf16_f32 v65, v66, v67\n"
      "v_mfma_f32_16x16x32_bf16 v[32:35], a[28:31], v[148:151], v[32:35]\n"
      "ds_write_addtid_b32 v64 offset:768\n"
      "v_fma_f32 v56, s44, v52, -v56\n"
      "v_mfma_f32_16x16x32_bf16 v[32:35], a[32:35], v[204:207], v[32:35]\n"
      "ds_write_addtid_b32 v65 offset:37696\n"
      "v_fma_f32 v57, s44, v53, -v57\n"
      "v_mfma_f32_16x16x32_bf16 v[32:35], a[0:3], v[112:115], v[32:35]\n"
      "v_fma_f32 v58, s44, v54, -v58\n"
      "v_fma_f32 v59, s44, v55, -v59\n"
      "v_mfma_f32_16x16x32_bf16 v[32:35], a[4:7], a[208:211], v[32:35]\n"
      "v_cvt_pk_bf16_f32 v56, v56, v57\n"
      "v_cvt_pk_bf16_f32 v57, v58, v59\n"
      "v_mfma_f32_16x16x32_bf16 v[32:35], a[8:11], v[220:223], v[32:35]\n"
      "s_waitcnt lgkmcnt(6)\n"
      "ds_write_addtid_b32 v56 offset:1536\n"
      "v_cvt_pk_bf16_f32 v60, v60, v61\n"
      "v_mfma_f32_16x16x32_bf16 v[32:35], a[12:15], a[212:215], v[32:35]\n"
      "ds_write_addtid_b32 v57 offset:38464\n"
      "v_cvt_pk_bf16_f32 v61, v62, v63\n"
      "v_mfma_f32_16x16x32_bf16 v[32:35], a[16:19], v[184:187], v[32:35]\n"
      "s_waitcnt lgkmcnt(7)\n"
      "ds_write_addtid_b32 v60 offset:0\n"
      "ds_write_addtid_b32 v61 offset:36928\n"
      "v_mfma_f32_16x16x32_bf16 v[32:35], a[20:23], v[240:243], v[32:35]\n"
      "s_mov_b32 m0, s45\n"
      "ds_read_b128 a[48:51], v2 offset:13408\n"
      "v_cvt_pk_bf16_f32 v28, v28, v29\n"
      "v_mfma_f32_16x16x32_bf16 v[20:23], a[24:27], v[100:103], 0\n"
      "ds_write_addtid_b32 v28 offset:37888\n"
      "v_cvt_pk_bf16_f32 v29, v30, v31\n"
      "v_mfma_f32_16x16x32_bf16 v[20:23], a[28:31], a[200:203], v[20:23]\n"
      "ds_write_addtid_b32 v29 offset:38144\n"
      "v_cvt_pk_bf16_f32 v32, v32, v33\n"
      "v_mfma_f32_16x16x32_bf16 v[20:23], a[32:35], v[208:211], v[20:23]\n"
      "s_mov_b32 m0, s58\n"
      "ds_read_b128 a[56:59], v4 offset:13408\n"
      "v_cvt_pk_bf16_f32 v33, v34, v35\n"
      "v_mfma_f32_16x16x32_bf16 v[20:23], a[0:3], a[204:207], v[20:23]\n"
      "buffer_load_dwordx4 v17, s[32:35], s53 offen offset:1024 lds\n"
      "v_cvt_pk_bf16_f32 v80, v72, v73\n"
      "v_mfma_f32_16x16x32_bf16 v[20:23], a[4:7], v[172:175], v[20:23]\n"
      "s_mov_b32 m0, s45\n"
      "ds_read2_b64 a[64:67], v10 offset0:3 offset1:67\n"
      "v_cvt_pk_bf16_f32 v81, v76, v77\n"
      "v_mfma_f32_16x16x32_bf16 v[20:23], a[8:11], v[228:231], v[20:23]\n"
      "ds_write_addtid_b32 v32 offset:61456\n"
      "v_cvt_pk_bf16_f32 v86, v74, v75\n"
      "v_mfma_f32_16x16x32_bf16 v[20:23], a[12:15], v[136:139], v[20:23]\n"
      "ds_write_addtid_b32 v33 offset:61712\n"
      "v_cvt_pk_bf16_f32 v87, v78, v79\n"
      "v_mfma_f32_16x16x32_bf16 v[20:23], a[16:19], v[192:195], v[20:23]\n"
      "ds_read_b128 a[52:55], v3 offset:13408\n"
      "v_add_u32_e32 v2, s91, v2\n"
      "v_mfma_f32_16x16x32_bf16 v[20:23], a[20:23], v[244:247], v[20:23]\n"
      "ds_read2_b64 a[68:71], v12 offset0:0 offset1:64\n"
      "v_add_u32_e64 v3, s91, v3\n"
      "v_mfma_f32_16x16x32_bf16 v[36:39], a[36:39], a[72:75], 0\n"
      "v_add_u32_e32 v4, s91, v4\n"
      "s_nop 0\n"
      "v_mfma_f32_16x16x32_bf16 v[40:43], a[40:43], a[76:79], 0\n"
      "s_sub_u32 s91, 0, s91\n"
      "v_mfma_f32_16x16x32_bf16 v[44:47], a[44:47], a[80:83], v[36:39]\n"
      "s_waitcnt lgkmcnt(5)\n"
      "v_cvt_pk_bf16_f32 v20, v20, v21\n"
      "v_cvt_pk_bf16_f32 v21, v22, v23\n"
      "v_mfma_f32_16x16x32_bf16 v[68:71], a[84:87], a[60:63], 0\n"
      "ds_write_addtid_b32 v20 offset:49160\n"
      "v_mfma_f32_16x16x32_bf16 v[24:27], a[24:27], a[196:199], 0\n"
      "ds_read_b128 a[24:27], v7 offset:4608\n"
      "v_fma_f32 v48, s44, v36, v40\n"
      "v_mfma_f32_16x16x32_bf16 v[24:27], a[28:31], v[160:163], v[24:27]\n"
      "ds_read_b128 a[28:31], v7 offset:4864\n"
      "v_fma_f32 v49, s44, v37, v41\n"
      "v_mfma_f32_16x16x32_bf16 v[24:27], a[32:35], v[216:219], v[24:27]\n"
      "ds_read_b128 a[32:35], v7 offset:5120\n"
      "v_cvt_pk_bf16_f32 v48, v48, v49\n"
      "v_mfma_f32_16x16x32_bf16 v[24:27], a[0:3], v[124:127], v[24:27]\n"
      "ds_read_b128 a[0:3], v6 offset:4608\n"
      "v_fma_f32 v50, s44, v38, v42\n"
      "v_mfma_f32_16x16x32_bf16 v[24:27], a[4:7], v[180:183], v[24:27]\n"
      "ds_read_b128 a[4:7], v6 offset:4864\n"
      "v_fma_f32 v51, s44, v39, v43\n"
      "v_mfma_f32_16x16x32_bf16 v[24:27], a[8:11], v[232:235], v[24:27]\n"
      "ds_read_b128 a[8:11], v6 offset:5120\n"
      "v_cvt_pk_bf16_f32 v49, v50, v51\n"
      "v_mfma_f32_16x16x32_bf16 v[24:27], a[12:15], a[216:219], v[24:27]\n"
      "ds_read_b128 a[12:15], v7 offset:0\n"
      "v_fma_f32 v40, s44, v36, -v40\n"
      "v_mfma_f32_16x16x32_bf16 v[24:27], a[16:19], v[196:199], v[24:27]\n"
      "ds_read_b128 a[16:19], v7 offset:256\n"
      "v_fma_f32 v41, s44, v37, -v41\n"
      "v_mfma_f32_16x16x32_bf16 v[24:27], a[20:23], v[252:255], v[24:27]\n"
      "ds_read_b128 a[20:23], v7 offset:512\n"
      "v_cvt_pk_bf16_f32 v40, v40, v41\n"
      "v_mfma_f32_16x16x32_bf16 v[52:55], a[48:51], a[72:75], 0\n"
      "ds_write_addtid_b32 v21 offset:49416\n"
      "v_fma_f32 v42, s44, v38, -v42\n"
      "v_mfma_f32_16x16x32_bf16 v[60:63], a[56:59], a[80:83], v[52:55]\n"
      "s_waitcnt lgkmcnt(7)\n"
      "v_fma_f32 v43, s44, v39, -v43\n"
      "v_cvt_pk_bf16_f32 v24, v24, v25\n"
      "v_mfma_f32_16x16x32_bf16 v[72:75], a[84:87], a[64:67], 0\n"
      "s_mov_b32 m0, s58\n"
      "v_cvt_pk_bf16_f32 v25, v26, v27\n"
      "v_cvt_pk_bf16_f32 v41, v42, v43\n"
      "v_mfma_f32_16x16x32_bf16 v[28:31], a[24:27], a[160:163], 0\n"
      "buffer_load_dwordx4 v18, s[32:35], s53 offen offset:2048 lds\n"
      "v_cvt_pk_bf16_f32 v44, v44, v45\n"
      "v_mfma_f32_16x16x32_bf16 v[28:31], a[28:31], a[172:175], v[28:31]\n"
      "s_mov_b32 m0, s45\n"
      "v_cvt_pk_bf16_f32 v82, v68, v69\n"
      "v_cvt_pk_bf16_f32 v83, v72, v73\n"
      "v_mfma_f32_16x16x32_bf16 v[28:31], a[32:35], a[184:187], v[28:31]\n"
      "s_waitcnt lgkmcnt(3)\n"
      "ds_write_addtid_b32 v24 offset:36864\n"
      "ds_write_addtid_b32 v25 offset:37120\n"
      "v_mfma_f32_16x16x32_bf16 v[28:31], a[0:3], a[88:91], v[28:31]\n"
      "s_mov_b32 m0, s18\n"
      "v_cvt_pk_bf16_f32 v45, v46, v47\n"
      "v_cvt_pk_bf16_f32 v88, v70, v71\n"
      "v_mfma_f32_16x16x32_bf16 v[28:31], a[4:7], a[100:103], v[28:31]\n"
      "ds_write_addtid_b32 v48 offset:1024\n"
      "ds_write_addtid_b32 v49 offset:37952\n"
      "v_mfma_f32_16x16x32_bf16 v[28:31], a[8:11], a[112:115], v[28:31]\n"
      "ds_write_addtid_b32 v40 offset:1792\n"
      "ds_write_addtid_b32 v41 offset:38720\n"
      "v_mfma_f32_16x16x32_bf16 v[28:31], a[12:15], a[124:127], v[28:31]\n"
      "s_waitcnt lgkmcnt(7)\n"
      "ds_write_addtid_b32 v44 offset:256\n"
      "ds_write_addtid_b32 v45 offset:37184\n"
      "v_mfma_f32_16x16x32_bf16 v[28:31], a[16:19], a[136:139], v[28:31]\n"
      "buffer_store_dwordx4 v[80:83], v14, s[36:39], s52 offen sc1\n"
      "v_cvt_pk_bf16_f32 v89, v74, v75\n"
      "v_mfma_f32_16x16x32_bf16 v[28:31], a[20:23], a[148:151], v[28:31]\n"
      "s_mov_b32 m0, s45\n"
      "ds_read2_b64 a[60:63], v12 offset0:1 offset1:65\n"
      "v_cvt_pk_bf16_f32 v60, v60, v61\n"
      "v_mfma_f32_16x16x32_bf16 v[56:59], a[52:55], a[76:79], 0\n"
      "v_cvt_pk_bf16_f32 v61, v62, v63\n"
      "v_swap_b32 v6, v7\n"
      "v_mfma_f32_16x16x32_bf16 v[20:23], a[24:27], a[164:167], 0\n"
      "v_mfma_f32_16x16x32_bf16 v[20:23], a[28:31], a[176:179], v[20:23]\n"
      "v_cvt_pk_bf16_f32 v28, v28, v29\n"
      "v_cvt_pk_bf16_f32 v29, v30, v31\n"
      "v_mfma_f32_16x16x32_bf16 v[20:23], a[32:35], a[188:191], v[20:23]\n"
      "buffer_store_dwordx4 v[86:89], v14, s[36:39], s52 offen offset:1536 sc1\n"
      "v_fma_f32 v64, s44, v52, v56\n"
      "v_mfma_f32_16x16x32_bf16 v[20:23], a[0:3], a[92:95], v[20:23]\n"
      "ds_write_addtid_b32 v28 offset:60432\n"
      "ds_write_addtid_b32 v29 offset:60688\n"
      "v_mfma_f32_16x16x32_bf16 v[20:23], a[4:7], a[104:107], v[20:23]\n"
      "s_mov_b32 m0, s18\n"
      "v_fma_f32 v65, s44, v53, v57\n"
      "v_cvt_pk_bf16_f32 v64, v64, v65\n"
      "v_mfma_f32_16x16x32_bf16 v[20:23], a[8:11], a[116:119], v[20:23]\n"
      "ds_write_addtid_b32 v64 offset:1280\n"
      "v_fma_f32 v66, s44, v54, v58\n"
      "v_mfma_f32_16x16x32_bf16 v[20:23], a[12:15], a[128:131], v[20:23]\n"
      "v_fma_f32 v67, s44, v55, v59\n"
      "v_cvt_pk_bf16_f32 v65, v66, v67\n"
      "v_mfma_f32_16x16x32_bf16 v[20:23], a[16:19], a[140:143], v[20:23]\n"
      "ds_write_addtid_b32 v65 offset:38208\n"
      "v_fma_f32 v56, s44, v52, -v56\n"
      "v_mfma_f32_16x16x32_bf16 v[20:23], a[20:23], a[152:155], v[20:23]\n"
      "v_fma_f32 v57, s44, v53, -v57\n"
      "v_cvt_pk_bf16_f32 v56, v56, v57\n"
      "v_mfma_f32_16x16x32_bf16 v[76:79], a[84:87], a[68:71], 0\n"
      "ds_write_addtid_b32 v56 offset:2048\n"
      "v_fma_f32 v58, s44, v54, -v58\n"
      "v_mfma_f32_16x16x32_bf16 v[32:35], a[24:27], a[168:171], 0\n"
      "ds_write_addtid_b32 v60 offset:512\n"
      "v_fma_f32 v59, s44, v55, -v59\n"
      "v_mfma_f32_16x16x32_bf16 v[32:35], a[28:31], a[180:183], v[32:35]\n"
      "ds_write_addtid_b32 v61 offset:37440\n"
      "v_cvt_pk_bf16_f32 v57, v58, v59\n"
      "v_mfma_f32_16x16x32_bf16 v[32:35], a[32:35], a[192:195], v[32:35]\n"
      "ds_write_addtid_b32 v57 offset:38976\n"
      "v_cvt_pk_bf16_f32 v20, v20, v21\n"
      "v_mfma_f32_16x16x32_bf16 v[32:35], a[0:3], a[96:99], v[32:35]\n"
      "s_mov_b32 m0, s45\n"
      "v_cvt_pk_bf16_f32 v21, v22, v23\n"
      "v_cvt_pk_bf16_f32 v84, v76, v77\n"
      "v_mfma_f32_16x16x32_bf16 v[32:35], a[4:7], a[108:111], v[32:35]\n"
      "s_mov_b32 m0, s59\n"
      "v_cvt_pk_bf16_f32 v90, v78, v79\n"
      "v_mfma_f32_16x16x32_bf16 v[32:35], a[8:11], a[120:123], v[32:35]\n"
      "buffer_load_dwordx4 v19, s[32:35], s96 offen lds\n"
      "v_mfma_f32_16x16x32_bf16 v[32:35], a[12:15], a[132:135], v[32:35]\n"
      "s_mov_b32 m0, s45\n"
      "v_mfma_f32_16x16x32_bf16 v[32:35], a[16:19], a[144:147], v[32:35]\n"
      "ds_write_addtid_b32 v20 offset:48136\n"
      "ds_write_addtid_b32 v21 offset:48392\n"
      "v_mfma_f32_16x16x32_bf16 v[32:35], a[20:23], a[156:159], v[32:35]\n"
      "s_nop 1\n"
      "v_mfma_f32_16x16x32_bf16 v[20:23], a[12:15], a[88:91], 0\n"
      "s_add_u32 s51, s51, 196608\n"
      "s_nop 1\n"
      "v_mfma_f32_16x16x32_bf16 v[20:23], a[16:19], a[100:103], v[20:23]\n"
      "s_sub_u32 s47, s51, 3342336\n"
      "v_cvt_pk_bf16_f32 v32, v32, v33\n"
      "v_cvt_pk_bf16_f32 v33, v34, v35\n"
      "v_mfma_f32_16x16x32_bf16 v[20:23], a[20:23], a[112:115], v[20:23]\n"
      "s_cmp_gt_u32 s51, s48\n"
      "ds_write_addtid_b32 v32 offset:35840\n"
      "v_mfma_f32_16x16x32_bf16 v[20:23], a[24:27], a[124:127], v[20:23]\n"
      "s_cselect_b32 s53, s47, s51\n"
      "ds_write_addtid_b32 v33 offset:36096\n"
      "v_mfma_f32_16x16x32_bf16 v[20:23], a[28:31], a[136:139], v[20:23]\n"
      "s_mul_i32 vcc_lo, vcc_lo, 2\n"
      "v_mfma_f32_16x16x32_bf16 v[20:23], a[32:35], a[148:151], v[20:23]\n"
      "v_mfma_f32_16x16x32_bf16 v[24:27], a[12:15], a[92:95], 0\n"
      "s_waitcnt vmcnt(19) lgkmcnt(0)\n"
      "s_barrier\n"
      "v_mfma_f32_16x16x32_bf16 v[24:27], a[16:19], a[104:107], v[24:27]\n"
      "s_add_u32 m0, s58, 13280\n"
      "ds_read_b128 a[0:3], v7 offset:0\n"
      "v_mfma_f32_16x16x32_bf16 v[24:27], a[20:23], a[116:119], v[24:27]\n"
      "ds_read_b128 a[4:7], v7 offset:256\n"
      "v_mfma_f32_16x16x32_bf16 v[24:27], a[24:27], a[128:131], v[24:27]\n"
      "ds_read_b128 a[8:11], v7 offset:512\n"
      "v_mfma_f32_16x16x32_bf16 v[24:27], a[28:31], a[140:143], v[24:27]\n"
      "ds_read_b128 a[36:39], v2 offset:0\n"
      "v_mfma_f32_16x16x32_bf16 v[24:27], a[32:35], a[152:155], v[24:27]\n"
      "ds_read_b128 a[40:43], v3 offset:0\n"
      "v_mfma_f32_16x16x32_bf16 v[68:71], a[84:87], a[60:63], 0\n"
      "ds_read_b128 a[44:47], v4 offset:0\n"
      "s_nop 0\n"
      "v_mfma_f32_16x16x32_bf16 v[28:31], a[12:15], a[96:99], 0\n"
      "ds_read2_b64 a[64:67], v11 offset0:0 offset1:64\n"
      "s_nop 0\n"
      "v_mfma_f32_16x16x32_bf16 v[28:31], a[16:19], a[108:111], v[28:31]\n"
      "buffer_load_dwordx4 v16, s[32:35], s53 offen lds\n"
      "v_mfma_f32_16x16x32_bf16 v[28:31], a[20:23], a[120:123], v[28:31]\n"
      "s_mov_b32 m0, s45\n"
      "ds_read2_b64 a[68:71], v11 offset0:1 offset1:65\n"
      "v_cvt_pk_bf16_f32 v85, v68, v69\n"
      "v_mfma_f32_16x16x32_bf16 v[28:31], a[24:27], a[132:135], v[28:31]\n"
      "ds_read_b128 a[12:15], v6 offset:768\n"
      "v_cvt_pk_bf16_f32 v91, v70, v71\n"
      "v_mfma_f32_16x16x32_bf16 v[28:31], a[28:31], a[144:147], v[28:31]\n"
      "ds_read_b128 a[16:19], v6 offset:1024\n"
      "v_mfma_f32_16x16x32_bf16 v[28:31], a[32:35], a[156:159], v[28:31]\n"
      "s_waitcnt lgkmcnt(6)\n"
      "ds_read_b128 a[20:23], v6 offset:1280\n"
      "v_mfma_f32_16x16x32_bf16 v[20:23], a[0:3], a[160:163], v[20:23]\n"
      "buffer_store_dwordx2 v[84:85], v15, s[36:39], s52 offen sc1\n"
      "v_mfma_f32_16x16x32_bf16 v[20:23], a[4:7], a[172:175], v[20:23]\n"
      "ds_read_b128 a[24:27], v6 offset:5376\n"
      "v_mfma_f32_16x16x32_bf16 v[20:23], a[8:11], a[184:187], v[20:23]\n"
      "ds_read_b128 a[28:31], v6 offset:5632\n"
      "s_nop 0\n"
      "v_mfma_f32_16x16x32_bf16 v[36:39], a[36:39], a[72:75], 0\n"
      "s_waitcnt lgkmcnt(6)\n"
      "ds_read_b128 a[32:35], v6 offset:5888\n"
      "s_nop 0\n"
      "v_mfma_f32_16x16x32_bf16 v[40:43], a[40:43], a[76:79], 0\n"
      "ds_read_b128 a[48:51], v2 offset:64\n"
      "v_cvt_pk_bf16_f32 v20, v20, v21\n"
      "v_mfma_f32_16x16x32_bf16 v[44:47], a[44:47], a[80:83], v[36:39]\n"
      "buffer_store_dwordx2 v[90:91], v15, s[36:39], s52 offen offset:1536 sc1\n"
      "v_cvt_pk_bf16_f32 v21, v22, v23\n"
      "v_mfma_f32_16x16x32_bf16 v[24:27], a[0:3], a[164:167], v[24:27]\n"
      "s_add_u32 s52, s52, 196608\n"
      "ds_write_addtid_b32 v20 offset:57360\n"
      "v_fma_f32 v48, s44, v36, v40\n"
      "v_mfma_f32_16x16x32_bf16 v[24:27], a[4:7], a[176:179], v[24:27]\n"
      "ds_write_addtid_b32 v21 offset:57616\n"
      "v_fma_f32 v49, s44, v37, v41\n"
      "v_mfma_f32_16x16x32_bf16 v[24:27], a[8:11], a[188:191], v[24:27]\n"
      "ds_read_b128 a[52:55], v3 offset:64\n"
      "v_cvt_pk_bf16_f32 v48, v48, v49\n"
      "v_mfma_f32_16x16x32_bf16 v[72:75], a[84:87], a[64:67], 0\n"
      "ds_read_b128 a[56:59], v4 offset:64\n"
      "v_fma_f32 v50, s44, v38, v42\n"
      "v_mfma_f32_16x16x32_bf16 v[28:31], a[0:3], a[168:171], v[28:31]\n"
      "ds_read_b128 a[0:3], v7 offset:768\n"
      "v_fma_f32 v51, s44, v39, v43\n"
      "v_mfma_f32_16x16x32_bf16 v[28:31], a[4:7], a[180:183], v[28:31]\n"
      "ds_read_b128 a[4:7], v7 offset:1024\n"
      "v_cvt_pk_bf16_f32 v24, v24, v25\n"
      "v_mfma_f32_16x16x32_bf16 v[28:31], a[8:11], a[192:195], v[28:31]\n"
      "s_waitcnt lgkmcnt(6)\n"
      "ds_read_b128 a[8:11], v7 offset:1280\n"
      "v_cvt_pk_bf16_f32 v25, v26, v27\n"
      "v_mfma_f32_16x16x32_bf16 v[76:79], a[84:87], a[68:71], 0\n"
      "ds_write_addtid_b32 v24 offset:45064\n"
      "ds_write_addtid_b32 v25 offset:45320\n"
      "v_mfma_f32_16x16x32_bf16 v[32:35], a[12:15], v[92:95], 0\n"
      "s_mov_b32 m0, s18\n"
      "ds_read2_b64 a[60:63], v11 offset0:2 offset1:66\n"
      "v_cvt_pk_bf16_f32 v49, v50, v51\n"
      "v_mfma_f32_16x16x32_bf16 v[32:35], a[16:19], v[148:151], v[32:35]\n"
      "ds_write_addtid_b32 v48 offset:5376\n"
      "v_fma_f32 v40, s44, v36, -v40\n"
      "v_mfma_f32_16x16x32_bf16 v[32:35], a[20:23], v[204:207], v[32:35]\n"
      "ds_write_addtid_b32 v49 offset:42304\n"
      "v_fma_f32 v41, s44, v37, -v41\n"
      "v_mfma_f32_16x16x32_bf16 v[32:35], a[24:27], v[112:115], v[32:35]\n"
      "v_fma_f32 v42, s44, v38, -v42\n"
      "v_fma_f32 v43, s44, v39, -v43\n"
      "v_mfma_f32_16x16x32_bf16 v[32:35], a[28:31], a[208:211], v[32:35]\n"
      "v_cvt_pk_bf16_f32 v40, v40, v41\n"
      "v_cvt_pk_bf16_f32 v41, v42, v43\n"
      "v_mfma_f32_16x16x32_bf16 v[32:35], a[32:35], v[220:223], v[32:35]\n"
      "s_waitcnt lgkmcnt(6)\n"
      "ds_write_addtid_b32 v40 offset:6144\n"
      "v_cvt_pk_bf16_f32 v44, v44, v45\n"
      "v_mfma_f32_16x16x32_bf16 v[32:35], a[0:3], a[212:215], v[32:35]\n"
      "ds_write_addtid_b32 v41 offset:43072\n"
      "v_cvt_pk_bf16_f32 v45, v46, v47\n"
      "v_mfma_f32_16x16x32_bf16 v[32:35], a[4:7], v[184:187], v[32:35]\n"
      "s_waitcnt lgkmcnt(7)\n"
      "ds_write_addtid_b32 v44 offset:4608\n"
      "ds_write_addtid_b32 v45 offset:41536\n"
      "v_mfma_f32_16x16x32_bf16 v[32:35], a[8:11], v[240:243], v[32:35]\n"
      "s_mov_b32 m0, s45\n"
      "ds_read_b128 a[36:39], v2 offset:128\n"
      "v_cvt_pk_bf16_f32 v28, v28, v29\n"
      "v_mfma_f32_16x16x32_bf16 v[20:23], a[12:15], v[100:103], 0\n"
      "ds_write_addtid_b32 v28 offset:32768\n"
      "v_cvt_pk_bf16_f32 v29, v30, v31\n"
      "v_mfma_f32_16x16x32_bf16 v[20:23], a[16:19], a[200:203], v[20:23]\n"
      "ds_write_addtid_b32 v29 offset:33024\n"
      "v_cvt_pk_bf16_f32 v32, v32, v33\n"
      "v_mfma_f32_16x16x32_bf16 v[20:23], a[20:23], v[208:211], v[20:23]\n"
      "s_add_u32 m0, s58, 13280\n"
      "ds_read_b128 a[44:47], v4 offset:128\n"
      "v_cvt_pk_bf16_f32 v33, v34, v35\n"
      "v_mfma_f32_16x16x32_bf16 v[20:23], a[24:27], a[204:207], v[20:23]\n"
      "buffer_load_dwordx4 v17, s[32:35], s53 offen offset:1024 lds\n"
      "v_cvt_pk_bf16_f32 v80, v72, v73\n"
      "v_mfma_f32_16x16x32_bf16 v[20:23], a[28:31], v[172:175], v[20:23]\n"
      "s_mov_b32 m0, s45\n"
      "ds_read2_b64 a[64:67], v11 offset0:3 offset1:67\n"
      "v_cvt_pk_bf16_f32 v81, v76, v77\n"
      "v_mfma_f32_16x16x32_bf16 v[20:23], a[32:35], v[228:231], v[20:23]\n"
      "ds_write_addtid_b32 v32 offset:58384\n"
      "v_cvt_pk_bf16_f32 v86, v74, v75\n"
      "v_mfma_f32_16x16x32_bf16 v[20:23], a[0:3], v[136:139], v[20:23]\n"
      "ds_write_addtid_b32 v33 offset:58640\n"
      "v_cvt_pk_bf16_f32 v87, v78, v79\n"
      "v_mfma_f32_16x16x32_bf16 v[20:23], a[4:7], v[192:195], v[20:23]\n"
      "ds_read_b128 a[40:43], v3 offset:128\n"
      "v_mfma_f32_16x16x32_bf16 v[20:23], a[8:11], v[244:247], v[20:23]\n"
      "global_load_dword v8, v9, s[54:55]\n"
      "s_nop 0\n"
      "v_mfma_f32_16x16x32_bf16 v[52:55], a[48:51], a[72:75], 0\n"
      "ds_read2_b64 a[68:71], v13 offset0:0 offset1:64\n"
      "s_nop 0\n"
      "v_mfma_f32_16x16x32_bf16 v[56:59], a[52:55], a[76:79], 0\n"
      "s_nop 0\n"
      "v_mfma_f32_16x16x32_bf16 v[60:63], a[56:59], a[80:83], v[52:55]\n"
      "s_waitcnt lgkmcnt(5)\n"
      "v_cvt_pk_bf16_f32 v20, v20, v21\n"
      "v_cvt_pk_bf16_f32 v21, v22, v23\n"
      "v_mfma_f32_16x16x32_bf16 v[68:71], a[84:87], a[60:63], 0\n"
      "ds_write_addtid_b32 v20 offset:46088\n"
      "v_mfma_f32_16x16x32_bf16 v[24:27], a[12:15], a[196:199], 0\n"
      "ds_read_b128 a[12:15], v6 offset:1536\n"
      "v_fma_f32 v64, s44, v52, v56\n"
      "v_mfma_f32_16x16x32_bf16 v[24:27], a[16:19], v[160:163], v[24:27]\n"
      "ds_read_b128 a[16:19], v6 offset:1792\n"
      "v_fma_f32 v65, s44, v53, v57\n"
      "v_mfma_f32_16x16x32_bf16 v[24:27], a[20:23], v[216:219], v[24:27]\n"
      "ds_read_b128 a[20:23], v6 offset:2048\n"
      "v_cvt_pk_bf16_f32 v64, v64, v65\n"
      "v_mfma_f32_16x16x32_bf16 v[24:27], a[24:27], v[124:127], v[24:27]\n"
      "ds_read_b128 a[24:27], v6 offset:6144\n"
      "v_fma_f32 v66, s44, v54, v58\n"
      "v_mfma_f32_16x16x32_bf16 v[24:27], a[28:31], v[180:183], v[24:27]\n"
      "ds_read_b128 a[28:31], v6 offset:6400\n"
      "v_fma_f32 v67, s44, v55, v59\n"
      "v_mfma_f32_16x16x32_bf16 v[24:27], a[32:35], v[232:235], v[24:27]\n"
      "ds_read_b128 a[32:35], v6 offset:6656\n"
      "v_cvt_pk_bf16_f32 v65, v66, v67\n"
      "v_mfma_f32_16x16x32_bf16 v[24:27], a[0:3], a[216:219], v[24:27]\n"
      "ds_read_b128 a[0:3], v7 offset:1536\n"
      "v_fma_f32 v56, s44, v52, -v56\n"
      "v_mfma_f32_16x16x32_bf16 v[24:27], a[4:7], v[196:199], v[24:27]\n"
      "ds_read_b128 a[4:7], v7 offset:1792\n"
      "v_fma_f32 v57, s44, v53, -v57\n"
      "v_mfma_f32_16x16x32_bf16 v[24:27], a[8:11], v[252:255], v[24:27]\n"
      "ds_read_b128 a[8:11], v7 offset:2048\n"
      "v_cvt_pk_bf16_f32 v56, v56, v57\n"
      "v_mfma_f32_16x16x32_bf16 v[36:39], a[36:39], a[72:75], 0\n"
      "ds_write_addtid_b32 v21 offset:46344\n"
      "v_fma_f32 v58, s44, v54, -v58\n"
      "v_mfma_f32_16x16x32_bf16 v[44:47], a[44:47], a[80:83], v[36:39]\n"
      "s_waitcnt lgkmcnt(7)\n"
      "v_fma_f32 v59, s44, v55, -v59\n"
      "v_cvt_pk_bf16_f32 v24, v24, v25\n"
      "v_mfma_f32_16x16x32_bf16 v[72:75], a[84:87], a[64:67], 0\n"
      "s_add_u32 m0, s58, 13280\n"
      "v_cvt_pk_bf16_f32 v25, v26, v27\n"
      "v_cvt_pk_bf16_f32 v57, v58, v59\n"
      "v_mfma_f32_16x16x32_bf16 v[28:31], a[12:15], v[96:99], 0\n"
      "buffer_load_dwordx4 v18, s[32:35], s53 offen offset:2048 lds\n"
      "v_cvt_pk_bf16_f32 v60, v60, v61\n"
      "v_mfma_f32_16x16x32_bf16 v[28:31], a[16:19], v[152:155], v[28:31]\n"
      "s_mov_b32 m0, s45\n"
      "v_cvt_pk_bf16_f32 v82, v68, v69\n"
      "v_cvt_pk_bf16_f32 v83, v72, v73\n"
      "v_mfma_f32_16x16x32_bf16 v[28:31], a[20:23], a[220:223], v[28:31]\n"
      "s_waitcnt lgkmcnt(3)\n"
      "ds_write_addtid_b32 v24 offset:33792\n"
      "ds_write_addtid_b32 v25 offset:34048\n"
      "v_mfma_f32_16x16x32_bf16 v[28:31], a[24:27], v[116:119], v[28:31]\n"
      "s_mov_b32 m0, s18\n"
      "v_cvt_pk_bf16_f32 v61, v62, v63\n"
      "v_cvt_pk_bf16_f32 v88, v70, v71\n"
      "v_mfma_f32_16x16x32_bf16 v[28:31], a[28:31], v[168:171], v[28:31]\n"
      "ds_write_addtid_b32 v64 offset:5632\n"
      "ds_write_addtid_b32 v65 offset:42560\n"
      "v_mfma_f32_16x16x32_bf16 v[28:31], a[32:35], v[224:227], v[28:31]\n"
      "ds_write_addtid_b32 v56 offset:6400\n"
      "ds_write_addtid_b32 v57 offset:43328\n"
      "v_mfma_f32_16x16x32_bf16 v[28:31], a[0:3], v[132:135], v[28:31]\n"
      "s_waitcnt lgkmcnt(7)\n"
      "ds_write_addtid_b32 v60 offset:4864\n"
      "ds_write_addtid_b32 v61 offset:41792\n"
      "v_mfma_f32_16x16x32_bf16 v[28:31], a[4:7], v[188:191], v[28:31]\n"
      "buffer_store_dwordx4 v[80:83], v14, s[36:39], s52 offen sc1\n"
      "v_cvt_pk_bf16_f32 v89, v74, v75\n"
      "v_mfma_f32_16x16x32_bf16 v[28:31], a[8:11], a[240:243], v[28:31]\n"
      "s_mov_b32 m0, s45\n"
      "ds_read2_b64 a[60:63], v13 offset0:1 offset1:65\n"
      "v_cvt_pk_bf16_f32 v44, v44, v45\n"
      "v_mfma_f32_16x16x32_bf16 v[40:43], a[40:43], a[76:79], 0\n"
      "v_cvt_pk_bf16_f32 v45, v46, v47\n"
      "s_nop 0\n"
      "v_mfma_f32_16x16x32_bf16 v[20:23], a[12:15], v[104:107], 0\n"
      "v_mfma_f32_16x16x32_bf16 v[20:23], a[16:19], v[156:159], v[20:23]\n"
      "v_cvt_pk_bf16_f32 v28, v28, v29\n"
      "v_cvt_pk_bf16_f32 v29, v30, v31\n"
      "v_mfma_f32_16x16x32_bf16 v[20:23], a[20:23], v[212:215], v[20:23]\n"
      "buffer_store_dwordx4 v[86:89], v14, s[36:39], s52 offen offset:1536 sc1\n"
      "v_fma_f32 v48, s44, v36, v40\n"
      "v_mfma_f32_16x16x32_bf16 v[20:23], a[24:27], v[120:123], v[20:23]\n"
      "ds_write_addtid_b32 v28 offset:59408\n"
      "ds_write_addtid_b32 v29 offset:59664\n"
      "v_mfma_f32_16x16x32_bf16 v[20:23], a[28:31], v[176:179], v[20:23]\n"
      "s_mov_b32 m0, s18\n"
      "v_fma_f32 v49, s44, v37, v41\n"
      "v_cvt_pk_bf16_f32 v48, v48, v49\n"
      "v_mfma_f32_16x16x32_bf16 v[20:23], a[32:35], a[232:235], v[20:23]\n"
      "s_sub_u32 s18, s19, s18\n"
      "ds_write_addtid_b32 v48 offset:5888\n"
      "v_fma_f32 v50, s44, v38, v42\n"
      "v_mfma_f32_16x16x32_bf16 v[20:23], a[0:3], v[140:143], v[20:23]\n"
      "v_fma_f32 v51, s44, v39, v43\n"
      "v_cvt_pk_bf16_f32 v49, v50, v51\n"
      "v_mfma_f32_16x16x32_bf16 v[20:23], a[4:7], a[236:239], v[20:23]\n"
      "ds_write_addtid_b32 v49 offset:42816\n"
      "v_fma_f32 v40, s44, v36, -v40\n"
      "v_mfma_f32_16x16x32_bf16 v[20:23], a[8:11], v[248:251], v[20:23]\n"
      "v_fma_f32 v41, s44, v37, -v41\n"
      "v_cvt_pk_bf16_f32 v40, v40, v41\n"
      "v_mfma_f32_16x16x32_bf16 v[76:79], a[84:87], a[68:71], 0\n"
      "ds_write_addtid_b32 v40 offset:6656\n"
      "v_fma_f32 v42, s44, v38, -v42\n"
      "v_mfma_f32_16x16x32_bf16 v[32:35], a[12:15], v[108:111], 0\n"
      "ds_write_addtid_b32 v44 offset:5120\n"
      "v_fma_f32 v43, s44, v39, -v43\n"
      "v_mfma_f32_16x16x32_bf16 v[32:35], a[16:19], v[164:167], v[32:35]\n"
      "ds_write_addtid_b32 v45 offset:42048\n"
      "v_cvt_pk_bf16_f32 v41, v42, v43\n"
      "v_mfma_f32_16x16x32_bf16 v[32:35], a[20:23], a[224:227], v[32:35]\n"
      "ds_write_addtid_b32 v41 offset:43584\n"
      "v_cvt_pk_bf16_f32 v20, v20, v21\n"
      "v_mfma_f32_16x16x32_bf16 v[32:35], a[24:27], v[128:131], v[32:35]\n"
      "s_mov_b32 m0, s45\n"
      "v_cvt_pk_bf16_f32 v21, v22, v23\n"
      "v_cvt_pk_bf16_f32 v84, v76, v77\n"
      "v_mfma_f32_16x16x32_bf16 v[32:35], a[28:31], a[228:231], v[32:35]\n"
      "ds_write_addtid_b32 v20 offset:47112\n"
      "v_cvt_pk_bf16_f32 v90, v78, v79\n"
      "v_mfma_f32_16x16x32_bf16 v[32:35], a[32:35], v[236:239], v[32:35]\n"
      "ds_write_addtid_b32 v21 offset:47368\n"
      "v_mfma_f32_16x16x32_bf16 v[32:35], a[0:3], v[144:147], v[32:35]\n"
      "v_mfma_f32_16x16x32_bf16 v[32:35], a[4:7], v[200:203], v[32:35]\n"
      "v_mfma_f32_16x16x32_bf16 v[32:35], a[8:11], a[244:247], v[32:35]\n"
      "s_nop 1\n"
      "s_cbranch_vccnz Lconv_loop0\n"
      "Lconv_t0:\n"
      "s_nop 1\n"
      "v_mfma_f32_16x16x32_bf16 v[20:23], a[24:27], v[96:99], 0\n"
      "s_nop 1\n"
      "v_mfma_f32_16x16x32_bf16 v[20:23], a[28:31], v[152:155], v[20:23]\n"
      "v_cvt_pk_bf16_f32 v32, v32, v33\n"
      "v_cvt_pk_bf16_f32 v33, v34, v35\n"
      "v_mfma_f32_16x16x32_bf16 v[20:23], a[32:35], a[220:223], v[20:23]\n"
      "ds_write_addtid_b32 v32 offset:34816\n"
      "v_mfma_f32_16x16x32_bf16 v[20:23], a[0:3], v[116:119], v[20:23]\n"
      "ds_write_addtid_b32 v33 offset:35072\n"
      "v_mfma_f32_16x16x32_bf16 v[20:23], a[4:7], v[168:171], v[20:23]\n"
      "v_mfma_f32_16x16x32_bf16 v[20:23], a[8:11], v[224:227], v[20:23]\n"
      "v_mfma_f32_16x16x32_bf16 v[24:27], a[24:27], v[104:107], 0\n"
      "s_waitcnt lgkmcnt(0)\n"
      "s_barrier\n"
      "v_mfma_f32_16x16x32_bf16 v[24:27], a[28:31], v[156:159], v[24:27]\n"
      "ds_read2_b64 a[64:67], v10 offset0:0 offset1:64\n"
      "v_mfma_f32_16x16x32_bf16 v[24:27], a[32:35], v[212:215], v[24:27]\n"
      "ds_read_b128 a[12:15], v7 offset:6144\n"
      "v_mfma_f32_16x16x32_bf16 v[24:27], a[0:3], v[120:123], v[24:27]\n"
      "ds_read2_b64 a[68:71], v10 offset0:1 offset1:65\n"
      "v_mfma_f32_16x16x32_bf16 v[24:27], a[4:7], v[176:179], v[24:27]\n"
      "ds_read_b128 a[16:19], v7 offset:6400\n"
      "v_mfma_f32_16x16x32_bf16 v[24:27], a[8:11], a[232:235], v[24:27]\n"
      "ds_read_b128 a[20:23], v7 offset:6656\n"
      "v_mfma_f32_16x16x32_bf16 v[68:71], a[84:87], a[60:63], 0\n"
      "ds_read2_b64 a[60:63], v10 offset0:2 offset1:66\n"
      "s_nop 0\n"
      "v_mfma_f32_16x16x32_bf16 v[28:31], a[24:27], v[108:111], 0\n"
      "s_waitcnt lgkmcnt(5)\n"
      "ds_read_b128 a[24:27], v6 offset:5376\n"
      "s_nop 0\n"
      "v_mfma_f32_16x16x32_bf16 v[72:75], a[84:87], a[64:67], 0\n"
      "ds_read2_b64 a[64:67], v10 offset0:3 offset1:67\n"
      "v_cvt_pk_bf16_f32 v85, v68, v69\n"
      "v_mfma_f32_16x16x32_bf16 v[28:31], a[28:31], v[164:167], v[28:31]\n"
      "buffer_store_dwordx2 v[84:85], v15, s[36:39], s52 offen sc1\n"
      "v_cvt_pk_bf16_f32 v91, v70, v71\n"
      "v_mfma_f32_16x16x32_bf16 v[28:31], a[32:35], a[224:227], v[28:31]\n"
      "ds_read_b128 a[28:31], v6 offset:5632\n"
      "v_mfma_f32_16x16x32_bf16 v[28:31], a[0:3], v[128:131], v[28:31]\n"
      "ds_read_b128 a[32:35], v6 offset:5888\n"
      "v_cvt_pk_bf16_f32 v80, v72, v73\n"
      "v_mfma_f32_16x16x32_bf16 v[28:31], a[4:7], a[228:231], v[28:31]\n"
      "ds_read_b128 a[0:3], v7 offset:768\n"
      "v_cvt_pk_bf16_f32 v86, v74, v75\n"
      "v_mfma_f32_16x16x32_bf16 v[28:31], a[8:11], v[236:239], v[28:31]\n"
      "s_waitcnt lgkmcnt(6)\n"
      "ds_read_b128 a[4:7], v7 offset:1024\n"
      "v_mfma_f32_16x16x32_bf16 v[20:23], a[12:15], v[132:135], v[20:23]\n"
      "buffer_store_dwordx2 v[90:91], v15, s[36:39], s52 offen offset:1536 sc1\n"
      "v_mfma_f32_16x16x32_bf16 v[76:79], a[84:87], a[68:71], 0\n"
      "s_waitcnt lgkmcnt(5)\n"
      "ds_read2_b64 a[68:71], v12 offset0:0 offset1:64\n"
      "s_nop 0\n"
      "v_mfma_f32_16x16x32_bf16 v[68:71], a[84:87], a[60:63], 0\n"
      "s_add_u32 s52, s52, 196608\n"
      "ds_read2_b64 a[60:63], v12 offset0:1 offset1:65\n"
      "s_nop 0\n"
      "v_mfma_f32_16x16x32_bf16 v[20:23], a[16:19], v[188:191], v[20:23]\n"
      "ds_read_b128 a[8:11], v7 offset:1280\n"
      "v_cvt_pk_bf16_f32 v81, v76, v77\n"
      "v_mfma_f32_16x16x32_bf16 v[20:23], a[20:23], a[240:243], v[20:23]\n"
      "v_cvt_pk_bf16_f32 v87, v78, v79\n"
      "v_cvt_pk_bf16_f32 v82, v68, v69\n"
      "v_mfma_f32_16x16x32_bf16 v[24:27], a[12:15], v[140:143], v[24:27]\n"
      "s_waitcnt lgkmcnt(5)\n"
      "v_cvt_pk_bf16_f32 v88, v70, v71\n"
      "s_nop 0\n"
      "v_mfma_f32_16x16x32_bf16 v[72:75], a[84:87], a[64:67], 0\n"
      "v_mfma_f32_16x16x32_bf16 v[24:27], a[16:19], a[236:239], v[24:27]\n"
      "v_cvt_pk_bf16_f32 v20, v20, v21\n"
      "v_cvt_pk_bf16_f32 v21, v22, v23\n"
      "v_mfma_f32_16x16x32_bf16 v[24:27], a[20:23], v[248:251], v[24:27]\n"
      "ds_write_addtid_b32 v20 offset:62480\n"
      "s_nop 0\n"
      "v_mfma_f32_16x16x32_bf16 v[28:31], a[12:15], v[144:147], v[28:31]\n"
      "s_waitcnt lgkmcnt(3)\n"
      "ds_read_b128 a[12:15], v7 offset:5376\n"
      "v_cvt_pk_bf16_f32 v83, v72, v73\n"
      "v_mfma_f32_16x16x32_bf16 v[76:79], a[84:87], a[68:71], 0\n"
      "buffer_store_dwordx4 v[80:83], v14, s[36:39], s52 offen sc1\n"
      "v_cvt_pk_bf16_f32 v89, v74, v75\n"
      "v_mfma_f32_16x16x32_bf16 v[28:31], a[16:19], v[200:203], v[28:31]\n"
      "ds_read_b128 a[16:19], v7 offset:5632\n"
      "v_cvt_pk_bf16_f32 v24, v24, v25\n"
      "v_mfma_f32_16x16x32_bf16 v[28:31], a[20:23], a[244:247], v[28:31]\n"
      "ds_read_b128 a[20:23], v7 offset:5888\n"
      "v_cvt_pk_bf16_f32 v25, v26, v27\n"
      "v_mfma_f32_16x16x32_bf16 v[32:35], a[24:27], v[92:95], 0\n"
      "s_waitcnt lgkmcnt(4)\n"
      "ds_write_addtid_b32 v21 offset:62736\n"
      "v_cvt_pk_bf16_f32 v84, v76, v77\n"
      "v_mfma_f32_16x16x32_bf16 v[68:71], a[84:87], a[60:63], 0\n"
      "ds_write_addtid_b32 v24 offset:50184\n"
      "v_cvt_pk_bf16_f32 v90, v78, v79\n"
      "v_mfma_f32_16x16x32_bf16 v[32:35], a[28:31], v[148:151], v[32:35]\n"
      "buffer_store_dwordx4 v[86:89], v14, s[36:39], s52 offen offset:1536 sc1\n"
      "v_cvt_pk_bf16_f32 v28, v28, v29\n"
      "v_mfma_f32_16x16x32_bf16 v[32:35], a[32:35], v[204:207], v[32:35]\n"
      "ds_write_addtid_b32 v25 offset:50440\n"
      "v_cvt_pk_bf16_f32 v29, v30, v31\n"
      "v_mfma_f32_16x16x32_bf16 v[32:35], a[0:3], v[112:115], v[32:35]\n"
      "v_cvt_pk_bf16_f32 v85, v68, v69\n"
      "v_cvt_pk_bf16_f32 v91, v70, v71\n"
      "v_mfma_f32_16x16x32_bf16 v[32:35], a[4:7], a[208:211], v[32:35]\n"
      "ds_write_addtid_b32 v28 offset:37888\n"
      "v_mfma_f32_16x16x32_bf16 v[32:35], a[8:11], v[220:223], v[32:35]\n"
      "s_waitcnt lgkmcnt(5)\n"
      "ds_write_addtid_b32 v29 offset:38144\n"
      "v_mfma_f32_16x16x32_bf16 v[32:35], a[12:15], a[212:215], v[32:35]\n"
      "buffer_store_dwordx2 v[84:85], v15, s[36:39], s52 offen sc1\n"
      "v_mfma_f32_16x16x32_bf16 v[32:35], a[16:19], v[184:187], v[32:35]\n"
      "s_waitcnt lgkmcnt(5)\n"
      "v_mfma_f32_16x16x32_bf16 v[32:35], a[20:23], v[240:243], v[32:35]\n"
      "s_nop 1\n"
      "v_mfma_f32_16x16x32_bf16 v[20:23], a[24:27], v[100:103], 0\n"
      "s_nop 1\n"
      "v_mfma_f32_16x16x32_bf16 v[20:23], a[28:31], a[200:203], v[20:23]\n"
      "s_nop 0\n"
      "v_mfma_f32_16x16x32_bf16 v[20:23], a[32:35], v[208:211], v[20:23]\n"
      "buffer_store_dwordx2 v[90:91], v15, s[36:39], s52 offen offset:1536 sc1\n"
      "v_cvt_pk_bf16_f32 v32, v32, v33\n"
      "v_mfma_f32_16x16x32_bf16 v[20:23], a[0:3], a[204:207], v[20:23]\n"
      "ds_write_addtid_b32 v32 offset:61456\n"
      "v_cvt_pk_bf16_f32 v33, v34, v35\n"
      "v_mfma_f32_16x16x32_bf16 v[20:23], a[4:7], v[172:175], v[20:23]\n"
      "ds_write_addtid_b32 v33 offset:61712\n"
      "v_mfma_f32_16x16x32_bf16 v[20:23], a[8:11], v[228:231], v[20:23]\n"
      "v_mfma_f32_16x16x32_bf16 v[20:23], a[12:15], v[136:139], v[20:23]\n"
      "v_mfma_f32_16x16x32_bf16 v[20:23], a[16:19], v[192:195], v[20:23]\n"
      "v_mfma_f32_16x16x32_bf16 v[20:23], a[20:23], v[244:247], v[20:23]\n"
      "s_nop 1\n"
      "v_mfma_f32_16x16x32_bf16 v[24:27], a[24:27], a[196:199], 0\n"
      "ds_read_b128 a[24:27], v7 offset:4608\n"
      "s_nop 0\n"
      "v_mfma_f32_16x16x32_bf16 v[24:27], a[28:31], v[160:163], v[24:27]\n"
      "ds_read_b128 a[28:31], v7 offset:4864\n"
      "v_mfma_f32_16x16x32_bf16 v[24:27], a[32:35], v[216:219], v[24:27]\n"
      "ds_read_b128 a[32:35], v7 offset:5120\n"
      "v_cvt_pk_bf16_f32 v20, v20, v21\n"
      "v_mfma_f32_16x16x32_bf16 v[24:27], a[0:3], v[124:127], v[24:27]\n"
      "ds_read_b128 a[0:3], v6 offset:4608\n"
      "v_cvt_pk_bf16_f32 v21, v22, v23\n"
      "v_mfma_f32_16x16x32_bf16 v[24:27], a[4:7], v[180:183], v[24:27]\n"
      "ds_read_b128 a[4:7], v6 offset:4864\n"
      "v_mfma_f32_16x16x32_bf16 v[24:27], a[8:11], v[232:235], v[24:27]\n"
      "ds_read_b128 a[8:11], v6 offset:5120\n"
      "v_mfma_f32_16x16x32_bf16 v[24:27], a[12:15], a[216:219], v[24:27]\n"
      "ds_read_b128 a[12:15], v7 offset:0\n"
      "v_mfma_f32_16x16x32_bf16 v[24:27], a[16:19], v[196:199], v[24:27]\n"
      "ds_read_b128 a[16:19], v7 offset:256\n"
      "v_mfma_f32_16x16x32_bf16 v[24:27], a[20:23], v[252:255], v[24:27]\n"
      "s_waitcnt lgkmcnt(7)\n"
      "ds_read_b128 a[20:23], v7 offset:512\n"
      "s_nop 0\n"
      "v_mfma_f32_16x16x32_bf16 v[28:31], a[24:27], a[160:163], 0\n"
      "s_waitcnt lgkmcnt(7)\n"
      "ds_write_addtid_b32 v20 offset:49160\n"
      "ds_write_addtid_b32 v21 offset:49416\n"
      "v_mfma_f32_16x16x32_bf16 v[28:31], a[28:31], a[172:175], v[28:31]\n"
      "s_waitcnt lgkmcnt(8)\n"
      "v_cvt_pk_bf16_f32 v24, v24, v25\n"
      "v_cvt_pk_bf16_f32 v25, v26, v27\n"
      "v_mfma_f32_16x16x32_bf16 v[28:31], a[32:35], a[184:187], v[28:31]\n"
      "s_waitcnt lgkmcnt(7)\n"
      "ds_write_addtid_b32 v24 offset:36864\n"
      "v_mfma_f32_16x16x32_bf16 v[28:31], a[0:3], a[88:91], v[28:31]\n"
      "s_waitcnt lgkmcnt(7)\n"
      "ds_write_addtid_b32 v25 offset:37120\n"
      "v_mfma_f32_16x16x32_bf16 v[28:31], a[4:7], a[100:103], v[28:31]\n"
      "s_waitcnt lgkmcnt(7)\n"
      "v_mfma_f32_16x16x32_bf16 v[28:31], a[8:11], a[112:115], v[28:31]\n"
      "s_waitcnt lgkmcnt(6)\n"
      "v_mfma_f32_16x16x32_bf16 v[28:31], a[12:15], a[124:127], v[28:31]\n"
      "s_waitcnt lgkmcnt(5)\n"
      "v_mfma_f32_16x16x32_bf16 v[28:31], a[16:19], a[136:139], v[28:31]\n"
      "s_waitcnt lgkmcnt(4)\n"
      "v_mfma_f32_16x16x32_bf16 v[28:31], a[20:23], a[148:151], v[28:31]\n"
      "s_nop 1\n"
      "v_mfma_f32_16x16x32_bf16 v[20:23], a[24:27], a[164:167], 0\n"
      "s_nop 1\n"
      "v_mfma_f32_16x16x32_bf16 v[20:23], a[28:31], a[176:179], v[20:23]\n"
      "s_nop 0\n"
      "v_mfma_f32_16x16x32_bf16 v[20:23], a[32:35], a[188:191], v[20:23]\n"
      "v_cvt_pk_bf16_f32 v28, v28, v29\n"
      "v_cvt_pk_bf16_f32 v29, v30, v31\n"
      "v_mfma_f32_16x16x32_bf16 v[20:23], a[0:3], a[92:95], v[20:23]\n"
      "ds_write_addtid_b32 v28 offset:60432\n"
      "v_mfma_f32_16x16x32_bf16 v[20:23], a[4:7], a[104:107], v[20:23]\n"
      "ds_write_addtid_b32 v29 offset:60688\n"
      "v_mfma_f32_16x16x32_bf16 v[20:23], a[8:11], a[116:119], v[20:23]\n"
      "v_mfma_f32_16x16x32_bf16 v[20:23], a[12:15], a[128:131], v[20:23]\n"
      "v_mfma_f32_16x16x32_bf16 v[20:23], a[16:19], a[140:143], v[20:23]\n"
      "v_mfma_f32_16x16x32_bf16 v[20:23], a[20:23], a[152:155], v[20:23]\n"
      "s_nop 1\n"
      "v_mfma_f32_16x16x32_bf16 v[32:35], a[24:27], a[168:171], 0\n"
      "s_nop 1\n"
      "v_mfma_f32_16x16x32_bf16 v[32:35], a[28:31], a[180:183], v[32:35]\n"
      "s_nop 0\n"
      "v_mfma_f32_16x16x32_bf16 v[32:35], a[32:35], a[192:195], v[32:35]\n"
      "v_cvt_pk_bf16_f32 v20, v20, v21\n"
      "v_cvt_pk_bf16_f32 v21, v22, v23\n"
      "v_mfma_f32_16x16x32_bf16 v[32:35], a[0:3], a[96:99], v[32:35]\n"
      "ds_write_addtid_b32 v20 offset:48136\n"
      "v_mfma_f32_16x16x32_bf16 v[32:35], a[4:7], a[108:111], v[32:35]\n"
      "ds_write_addtid_b32 v21 offset:48392\n"
      "v_mfma_f32_16x16x32_bf16 v[32:35], a[8:11], a[120:123], v[32:35]\n"
      "v_mfma_f32_16x16x32_bf16 v[32:35], a[12:15], a[132:135], v[32:35]\n"
      "v_mfma_f32_16x16x32_bf16 v[32:35], a[16:19], a[144:147], v[32:35]\n"
      "v_mfma_f32_16x16x32_bf16 v[32:35], a[20:23], a[156:159], v[32:35]\n"
      "s_nop 1\n"
      "s_nop 5\n"
      "v_cvt_pk_bf16_f32 v32, v32, v33\n"
      "v_cvt_pk_bf16_f32 v33, v34, v35\n"
      "ds_write_addtid_b32 v32 offset:35840\n"
      "ds_write_addtid_b32 v33 offset:36096\n"
      "s_waitcnt lgkmcnt(0)\n"
      "s_barrier\n"
      "ds_read2_b64 a[0:3], v11 offset0:0 offset1:64\n"
      "ds_read2_b64 a[4:7], v11 offset0:1 offset1:65\n"
      "ds_read2_b64 a[8:11], v11 offset0:2 offset1:66\n"
      "ds_read2_b64 a[12:15], v11 offset0:3 offset1:67\n"
      "ds_read2_b64 a[16:19], v13 offset0:0 offset1:64\n"
      "ds_read2_b64 a[20:23], v13 offset0:1 offset1:65\n"
      "s_waitcnt lgkmcnt(5)\n"
      "v_mfma_f32_16x16x32_bf16 v[72:75], a[84:87], a[0:3], 0\n"
      "s_waitcnt lgkmcnt(4)\n"
      "v_mfma_f32_16x16x32_bf16 v[76:79], a[84:87], a[4:7], 0\n"
      "s_waitcnt lgkmcnt(3)\n"
      "v_mfma_f32_16x16x32_bf16 v[68:71], a[84:87], a[8:11], 0\n"
      "s_waitcnt lgkmcnt(2)\n"
      "s_nop 2\n"
      "v_cvt_pk_bf16_f32 v80, v72, v73\n"
      "v_cvt_pk_bf16_f32 v86, v74, v75\n"
      "v_mfma_f32_16x16x32_bf16 v[72:75], a[84:87], a[12:15], 0\n"
      "s_waitcnt lgkmcnt(1)\n"
      "v_cvt_pk_bf16_f32 v81, v76, v77\n"
      "v_cvt_pk_bf16_f32 v87, v78, v79\n"
      "v_cvt_pk_bf16_f32 v82, v68, v69\n"
      "v_cvt_pk_bf16_f32 v88, v70, v71\n"
      "v_mfma_f32_16x16x32_bf16 v[76:79], a[84:87], a[16:19], 0\n"
      "s_waitcnt lgkmcnt(0)\n"
      "v_mfma_f32_16x16x32_bf16 v[68:71], a[84:87], a[20:23], 0\n"
      "s_add_u32 s52, s52, 196608\n"
      "v_cvt_pk_bf16_f32 v83, v72, v73\n"
      "v_cvt_pk_bf16_f32 v89, v74, v75\n"
      "buffer_store_dwordx4 v[80:83], v14, s[36:39], s52 offen sc1\n"
      "buffer_store_dwordx4 v[86:89], v14, s[36:39], s52 offen offset:1536 sc1\n"
      "s_nop 0\n"
      "v_cvt_pk_bf16_f32 v84, v76, v77\n"
      "v_cvt_pk_bf16_f32 v90, v78, v79\n"
      "v_cvt_pk_bf16_f32 v85, v68, v69\n"
      "v_cvt_pk_bf16_f32 v91, v70, v71\n"
      "buffer_store_dwordx2 v[84:85], v15, s[36:39], s52 offen sc1\n"
      "buffer_store_dwordx2 v[90:91], v15, s[36:39], s52 offen offset:1536 sc1\n"
      "Lconv_u0:\n"
      "s_waitcnt vmcnt(0)\n"
      "s_endpgm\n"
      ".p2align 12\n"
      "Lconv_pa1:\n"
      "s_cbranch_execz Lconv_r0\n"
      "s_nop 0\n"
      "Lconv_loop1:\n"
      "v_mfma_f32_16x16x32_bf16 v[20:23], a[24:27], v[96:99], 0\n"
      "s_add_u32 s51, s51, 196608\n"
      "s_nop 1\n"
      "v_mfma_f32_16x16x32_bf16 v[20:23], a[28:31], v[152:155], v[20:23]\n"
      "s_sub_u32 s58, s93, s58\n"
      "v_cvt_pk_bf16_f32 v32, v32, v33\n"
      "v_cvt_pk_bf16_f32 v33, v34, v35\n"
      "v_mfma_f32_16x16x32_bf16 v[20:23], a[32:35], a[220:223], v[20:23]\n"
      "s_sub_u32 s59, s94, s59\n"
      "v_mfma_f32_16x16x32_bf16 v[20:23], a[0:3], v[116:119], v[20:23]\n"
      "s_sub_u32 s47, s51, 3342336\n"
      "v_mfma_f32_16x16x32_bf16 v[20:23], a[4:7], v[168:171], v[20:23]\n"
      "s_cmp_gt_u32 s51, s48\n"
      "ds_write_addtid_b32 v32 offset:34816\n"
      "v_mfma_f32_16x16x32_bf16 v[20:23], a[8:11], v[224:227], v[20:23]\n"
      "s_cselect_b32 s53, s47, s51\n"
      "v_mfma_f32_16x16x32_bf16 v[24:27], a[24:27], v[104:107], 0\n"
      "s_add_u32 s52, s52, 196608\n"
      "ds_write_addtid_b32 v33 offset:35072\n"
      "v_mfma_f32_16x16x32_bf16 v[24:27], a[28:31], v[156:159], v[24:27]\n"
      "v_mfma_f32_16x16x32_bf16 v[24:27], a[32:35], v[212:215], v[24:27]\n"
      "v_mfma_f32_16x16x32_bf16 v[24:27], a[0:3], v[120:123], v[24:27]\n"
      "v_mfma_f32_16x16x32_bf16 v[24:27], a[4:7], v[176:179], v[24:27]\n"
      "s_waitcnt vmcnt(15) lgkmcnt(0)\n"
      "s_barrier\n"
      "v_mfma_f32_16x16x32_bf16 v[24:27], a[8:11], a[232:235], v[24:27]\n"
      "s_mov_b32 m0, s58\n"
      "ds_read_b128 a[48:51], v2 offset:13280\n"
      "v_mfma_f32_16x16x32_bf16 v[28:31], a[24:27], v[108:111], 0\n"
      "ds_read_b128 a[56:59], v4 offset:13280\n"
      "v_mfma_f32_16x16x32_bf16 v[28:31], a[28:31], v[164:167], v[28:31]\n"
      "ds_read_b128 a[12:15], v7 offset:6144\n"
      "v_mfma_f32_16x16x32_bf16 v[28:31], a[32:35], a[224:227], v[28:31]\n"
      "ds_read_b128 a[16:19], v7 offset:6400\n"
      "v_mfma_f32_16x16x32_bf16 v[28:31], a[0:3], v[128:131], v[28:31]\n"
      "ds_read_b128 a[20:23], v7 offset:6656\n"
      "v_mfma_f32_16x16x32_bf16 v[28:31], a[4:7], a[228:231], v[28:31]\n"
      "ds_read_b128 a[52:55], v3 offset:13280\n"
      "v_mfma_f32_16x16x32_bf16 v[28:31], a[8:11], v[236:239], v[28:31]\n"
      "s_waitcnt lgkmcnt(5)\n"
      "ds_read2_b64 a[60:63], v10 offset0:0 offset1:64\n"
      "v_mfma_f32_16x16x32_bf16 v[52:55], a[48:51], a[72:75], 0\n"
      "s_waitcnt lgkmcnt(5)\n"
      "ds_read2_b64 a[64:67], v10 offset0:1 offset1:65\n"
      "v_mfma_f32_16x16x32_bf16 v[60:63], a[56:59], a[80:83], v[52:55]\n"
      "s_waitcnt lgkmcnt(5)\n"
      "ds_read_b128 a[24:27], v6 offset:5376\n"
      "v_mfma_f32_16x16x32_bf16 v[20:23], a[12:15], v[132:135], v[20:23]\n"
      "s_waitcnt lgkmcnt(5)\n"
      "ds_read_b128 a[28:31], v6 offset:5632\n"
      "v_mfma_f32_16x16x32_bf16 v[20:23], a[16:19], v[188:191], v[20:23]\n"
      "s_waitcnt lgkmcnt(5)\n"
      "ds_read_b128 a[32:35], v6 offset:5888\n"
      "v_cvt_pk_bf16_f32 v60, v60, v61\n"
      "v_mfma_f32_16x16x32_bf16 v[20:23], a[20:23], a[240:243], v[20:23]\n"
      "s_waitcnt lgkmcnt(5)\n"
      "ds_read_b128 a[0:3], v7 offset:768\n"
      "v_cvt_pk_bf16_f32 v61, v62, v63\n"
      "v_mfma_f32_16x16x32_bf16 v[56:59], a[52:55], a[76:79], 0\n"
      "ds_read_b128 a[4:7], v7 offset:1024\n"
      "s_nop 0\n"
      "v_mfma_f32_16x16x32_bf16 v[24:27], a[12:15], v[140:143], v[24:27]\n"
      "ds_read_b128 a[8:11], v7 offset:1280\n"
      "v_cvt_pk_bf16_f32 v20, v20, v21\n"
      "v_mfma_f32_16x16x32_bf16 v[24:27], a[16:19], a[236:239], v[24:27]\n"
      "buffer_load_dwordx4 v16, s[32:35], s53 offen lds\n"
      "v_cvt_pk_bf16_f32 v21, v22, v23\n"
      "v_mfma_f32_16x16x32_bf16 v[24:27], a[20:23], v[248:251], v[24:27]\n"
      "s_waitcnt lgkmcnt(6)\n"
      "ds_read_b128 a[36:39], v2 offset:13344\n"
      "v_fma_f32 v64, s44, v52, v56\n"
      "v_mfma_f32_16x16x32_bf16 v[68:71], a[84:87], a[60:63], 0\n"
      "s_mov_b32 m0, s45\n"
      "ds_read_b128 a[40:43], v3 offset:13344\n"
      "v_fma_f32 v65, s44, v53, v57\n"
      "v_mfma_f32_16x16x32_bf16 v[28:31], a[12:15], v[144:147], v[28:31]\n"
      "s_mov_b32 m0, s58\n"
      "ds_read_b128 a[12:15], v7 offset:5376\n"
      "v_cvt_pk_bf16_f32 v24, v24, v25\n"
      "v_mfma_f32_16x16x32_bf16 v[28:31], a[16:19], v[200:203], v[28:31]\n"
      "ds_read_b128 a[16:19], v7 offset:5632\n"
      "v_cvt_pk_bf16_f32 v25, v26, v27\n"
      "v_mfma_f32_16x16x32_bf16 v[28:31], a[20:23], a[244:247], v[28:31]\n"
      "ds_read_b128 a[20:23], v7 offset:5888\n"
      "v_cvt_pk_bf16_f32 v64, v64, v65\n"
      "v_mfma_f32_16x16x32_bf16 v[72:75], a[84:87], a[64:67], 0\n"
      "s_waitcnt lgkmcnt(6)\n"
      "ds_read_b128 a[44:47], v4 offset:13344\n"
      "v_fma_f32 v66, s44, v54, v58\n"
      "v_mfma_f32_16x16x32_bf16 v[32:35], a[24:27], v[92:95], 0\n"
      "ds_read2_b64 a[68:71], v10 offset0:2 offset1:66\n"
      "v_cvt_pk_bf16_f32 v28, v28, v29\n"
      "v_mfma_f32_16x16x32_bf16 v[32:35], a[28:31], v[148:151], v[32:35]\n"
      "ds_read2_b64 a[60:63], v10 offset0:3 offset1:67\n"
      "v_cvt_pk_bf16_f32 v29, v30, v31\n"
      "v_mfma_f32_16x16x32_bf16 v[32:35], a[32:35], v[204:207], v[32:35]\n"
      "buffer_load_dwordx4 v17, s[32:35], s53 offen offset:1024 lds\n"
      "v_fma_f32 v67, s44, v55, v59\n"
      "v_mfma_f32_16x16x32_bf16 v[32:35], a[0:3], v[112:115], v[32:35]\n"
      "s_mov_b32 m0, s45\n"
      "v_cvt_pk_bf16_f32 v65, v66, v67\n"
      "v_fma_f32 v56, s44, v52, -v56\n"
      "v_mfma_f32_16x16x32_bf16 v[32:35], a[4:7], a[208:211], v[32:35]\n"
      "s_waitcnt lgkmcnt(5)\n"
      "ds_write_addtid_b32 v20 offset:62480\n"
      "v_fma_f32 v57, s44, v53, -v57\n"
      "v_mfma_f32_16x16x32_bf16 v[32:35], a[8:11], v[220:223], v[32:35]\n"
      "ds_write_addtid_b32 v21 offset:62736\n"
      "v_cvt_pk_bf16_f32 v56, v56, v57\n"
      "v_mfma_f32_16x16x32_bf16 v[32:35], a[12:15], a[212:215], v[32:35]\n"
      "s_waitcnt lgkmcnt(5)\n"
      "ds_write_addtid_b32 v24 offset:50184\n"
      "v_fma_f32 v58, s44, v54, -v58\n"
      "v_mfma_f32_16x16x32_bf16 v[32:35], a[16:19], v[184:187], v[32:35]\n"
      "ds_write_addtid_b32 v25 offset:50440\n"
      "v_fma_f32 v59, s44, v55, -v59\n"
      "v_mfma_f32_16x16x32_bf16 v[32:35], a[20:23], v[240:243], v[32:35]\n"
      "ds_write_addtid_b32 v28 offset:37888\n"
      "v_cvt_pk_bf16_f32 v57, v58, v59\n"
      "v_mfma_f32_16x16x32_bf16 v[36:39], a[36:39], a[72:75], 0\n"
      "ds_write_addtid_b32 v29 offset:38144\n"
      "v_cvt_pk_bf16_f32 v80, v68, v69\n"
      "v_mfma_f32_16x16x32_bf16 v[40:43], a[40:43], a[76:79], 0\n"
      "s_waitcnt lgkmcnt(6)\n"
      "ds_read_b128 a[48:51], v2 offset:13408\n"
      "v_cvt_pk_bf16_f32 v32, v32, v33\n"
      "v_mfma_f32_16x16x32_bf16 v[44:47], a[44:47], a[80:83], v[36:39]\n"
      "s_mov_b32 m0, s18\n"
      "ds_read_b128 a[56:59], v4 offset:13408\n"
      "v_cvt_pk_bf16_f32 v86, v70, v71\n"
      "v_mfma_f32_16x16x32_bf16 v[76:79], a[84:87], a[68:71], 0\n"
      "ds_write_addtid_b32 v64 offset:768\n"
      "v_cvt_pk_bf16_f32 v33, v34, v35\n"
      "v_mfma_f32_16x16x32_bf16 v[68:71], a[84:87], a[60:63], 0\n"
      "ds_write_addtid_b32 v65 offset:37696\n"
      "v_cvt_pk_bf16_f32 v81, v72, v73\n"
      "v_mfma_f32_16x16x32_bf16 v[20:23], a[24:27], v[100:103], 0\n"
      "ds_write_addtid_b32 v56 offset:1536\n"
      "v_fma_f32 v48, s44, v36, v40\n"
      "v_mfma_f32_16x16x32_bf16 v[20:23], a[28:31], a[200:203], v[20:23]\n"
      "ds_write_addtid_b32 v57 offset:38464\n"
      "v_cvt_pk_bf16_f32 v82, v76, v77\n"
      "v_mfma_f32_16x16x32_bf16 v[20:23], a[32:35], v[208:211], v[20:23]\n"
      "ds_write_addtid_b32 v60 offset:0\n"
      "v_cvt_pk_bf16_f32 v83, v68, v69\n"
      "v_mfma_f32_16x16x32_bf16 v[20:23], a[0:3], a[204:207], v[20:23]\n"
      "ds_write_addtid_b32 v61 offset:36928\n"
      "v_fma_f32 v49, s44, v37, v41\n"
      "v_mfma_f32_16x16x32_bf16 v[20:23], a[4:7], v[172:175], v[20:23]\n"
      "s_mov_b32 m0, s45\n"
      "ds_read_b128 a[52:55], v3 offset:13408\n"
      "v_cvt_pk_bf16_f32 v48, v48, v49\n"
      "v_mfma_f32_16x16x32_bf16 v[20:23], a[8:11], v[228:231], v[20:23]\n"
      "s_mov_b32 m0, s58\n"
      "v_cvt_pk_bf16_f32 v87, v74, v75\n"
      "v_fma_f32 v50, s44, v38, v42\n"
      "v_mfma_f32_16x16x32_bf16 v[20:23], a[12:15], v[136:139], v[20:23]\n"
      "buffer_load_dwordx4 v18, s[32:35], s53 offen offset:2048 lds\n"
      "v_fma_f32 v51, s44, v39, v43\n"
      "v_mfma_f32_16x16x32_bf16 v[20:23], a[16:19], v[192:195], v[20:23]\n"
      "s_mov_b32 m0, s45\n"
      "ds_read2_b64 a[64:67], v12 offset0:0 offset1:64\n"
      "v_cvt_pk_bf16_f32 v49, v50, v51\n"
      "v_mfma_f32_16x16x32_bf16 v[20:23], a[20:23], v[244:247], v[20:23]\n"
      "s_waitcnt lgkmcnt(8)\n"
      "ds_write_addtid_b32 v32 offset:61456\n"
      "v_cvt_pk_bf16_f32 v88, v78, v79\n"
      "v_mfma_f32_16x16x32_bf16 v[52:55], a[48:51], a[72:75], 0\n"
      "ds_write_addtid_b32 v33 offset:61712\n"
      "v_cvt_pk_bf16_f32 v89, v70, v71\n"
      "v_mfma_f32_16x16x32_bf16 v[60:63], a[56:59], a[80:83], v[52:55]\n"
      "buffer_store_dwordx4 v[80:83], v14, s[36:39], s52 offen sc1\n"
      "v_fma_f32 v40, s44, v36, -v40\n"
      "v_mfma_f32_16x16x32_bf16 v[24:27], a[24:27], a[196:199], 0\n"
      "s_mov_b32 m0, s18\n"
      "ds_read_b128 a[24:27], v7 offset:4608\n"
      "v_fma_f32 v41, s44, v37, -v41\n"
      "v_mfma_f32_16x16x32_bf16 v[24:27], a[28:31], v[160:163], v[24:27]\n"
      "ds_read_b128 a[28:31], v7 offset:4864\n"
      "v_cvt_pk_bf16_f32 v40, v40, v41\n"
      "v_mfma_f32_16x16x32_bf16 v[24:27], a[32:35], v[216:219], v[24:27]\n"
      "ds_read_b128 a[32:35], v7 offset:5120\n"
      "v_fma_f32 v42, s44, v38, -v42\n"
      "v_mfma_f32_16x16x32_bf16 v[24:27], a[0:3], v[124:127], v[24:27]\n"
      "ds_read_b128 a[0:3], v6 offset:4608\n"
      "v_fma_f32 v43, s44, v39, -v43\n"
      "v_mfma_f32_16x16x32_bf16 v[24:27], a[4:7], v[180:183], v[24:27]\n"
      "ds_read_b128 a[4:7], v6 offset:4864\n"
      "v_cvt_pk_bf16_f32 v41, v42, v43\n"
      "v_mfma_f32_16x16x32_bf16 v[24:27], a[8:11], v[232:235], v[24:27]\n"
      "ds_read_b128 a[8:11], v6 offset:5120\n"
      "v_cvt_pk_bf16_f32 v44, v44, v45\n"
      "v_mfma_f32_16x16x32_bf16 v[24:27], a[12:15], a[216:219], v[24:27]\n"
      "ds_read_b128 a[12:15], v7 offset:0\n"
      "v_cvt_pk_bf16_f32 v45, v46, v47\n"
      "v_mfma_f32_16x16x32_bf16 v[24:27], a[16:19], v[196:199], v[24:27]\n"
      "ds_read_b128 a[16:19], v7 offset:256\n"
      "v_cvt_pk_bf16_f32 v20, v20, v21\n"
      "v_mfma_f32_16x16x32_bf16 v[24:27], a[20:23], v[252:255], v[24:27]\n"
      "s_waitcnt lgkmcnt(7)\n"
      "ds_read_b128 a[20:23], v7 offset:512\n"
      "v_cvt_pk_bf16_f32 v21, v22, v23\n"
      "v_mfma_f32_16x16x32_bf16 v[56:59], a[52:55], a[76:79], 0\n"
      "ds_write_addtid_b32 v48 offset:1024\n"
      "v_cvt_pk_bf16_f32 v60, v60, v61\n"
      "v_mfma_f32_16x16x32_bf16 v[28:31], a[24:27], a[160:163], 0\n"
      "s_waitcnt lgkmcnt(7)\n"
      "ds_write_addtid_b32 v49 offset:37952\n"
      "v_cvt_pk_bf16_f32 v24, v24, v25\n"
      "v_mfma_f32_16x16x32_bf16 v[28:31], a[28:31], a[172:175], v[28:31]\n"
      "buffer_store_dwordx4 v[86:89], v14, s[36:39], s52 offen offset:1536 sc1\n"
      "v_cvt_pk_bf16_f32 v25, v26, v27\n"
      "v_mfma_f32_16x16x32_bf16 v[28:31], a[32:35], a[184:187], v[28:31]\n"
      "s_waitcnt lgkmcnt(6)\n"
      "ds_write_addtid_b32 v40 offset:1792\n"
      "v_fma_f32 v64, s44, v52, v56\n"
      "v_mfma_f32_16x16x32_bf16 v[28:31], a[0:3], a[88:91], v[28:31]\n"
      "ds_write_addtid_b32 v41 offset:38720\n"
      "v_fma_f32 v65, s44, v53, v57\n"
      "v_mfma_f32_16x16x32_bf16 v[28:31], a[4:7], a[100:103], v[28:31]\n"
      "s_waitcnt lgkmcnt(6)\n"
      "ds_write_addtid_b32 v44 offset:256\n"
      "v_cvt_pk_bf16_f32 v64, v64, v65\n"
      "v_mfma_f32_16x16x32_bf16 v[28:31], a[8:11], a[112:115], v[28:31]\n"
      "ds_write_addtid_b32 v45 offset:37184\n"
      "v_fma_f32 v66, s44, v54, v58\n"
      "v_mfma_f32_16x16x32_bf16 v[28:31], a[12:15], a[124:127], v[28:31]\n"
      "s_waitcnt lgkmcnt(6)\n"
      "ds_read2_b64 a[68:71], v12 offset0:1 offset1:65\n"
      "v_fma_f32 v67, s44, v55, v59\n"
      "v_mfma_f32_16x16x32_bf16 v[28:31], a[16:19], a[136:139], v[28:31]\n"
      "s_mov_b32 m0, s45\n"
      "v_cvt_pk_bf16_f32 v65, v66, v67\n"
      "v_fma_f32 v56, s44, v52, -v56\n"
      "v_mfma_f32_16x16x32_bf16 v[28:31], a[20:23], a[148:151], v[28:31]\n"
      "ds_write_addtid_b32 v20 offset:49160\n"
      "v_fma_f32 v57, s44, v53, -v57\n"
      "v_mfma_f32_16x16x32_bf16 v[72:75], a[84:87], a[64:67], 0\n"
      "ds_write_addtid_b32 v21 offset:49416\n"
      "v_cvt_pk_bf16_f32 v56, v56, v57\n"
      "v_mfma_f32_16x16x32_bf16 v[20:23], a[24:27], a[164:167], 0\n"
      "ds_write_addtid_b32 v24 offset:36864\n"
      "v_fma_f32 v58, s44, v54, -v58\n"
      "v_mfma_f32_16x16x32_bf16 v[20:23], a[28:31], a[176:179], v[20:23]\n"
      "ds_write_addtid_b32 v25 offset:37120\n"
      "v_cvt_pk_bf16_f32 v28, v28, v29\n"
      "v_mfma_f32_16x16x32_bf16 v[20:23], a[32:35], a[188:191], v[20:23]\n"
      "ds_write_addtid_b32 v28 offset:60432\n"
      "v_cvt_pk_bf16_f32 v29, v30, v31\n"
      "v_mfma_f32_16x16x32_bf16 v[20:23], a[0:3], a[92:95], v[20:23]\n"
      "ds_write_addtid_b32 v29 offset:60688\n"
      "v_fma_f32 v59, s44, v55, -v59\n"
      "v_mfma_f32_16x16x32_bf16 v[20:23], a[4:7], a[104:107], v[20:23]\n"
      "s_mov_b32 m0, s18\n"
      "v_cvt_pk_bf16_f32 v57, v58, v59\n"
      "v_cvt_pk_bf16_f32 v61, v62, v63\n"
      "v_mfma_f32_16x16x32_bf16 v[20:23], a[8:11], a[116:119], v[20:23]\n"
      "ds_write_addtid_b32 v64 offset:1280\n"
      "v_add_u32_e64 v2, s91, v2\n"
      "v_mfma_f32_16x16x32_bf16 v[20:23], a[12:15], a[128:131], v[20:23]\n"
      "ds_write_addtid_b32 v65 offset:38208\n"
      "v_add_u32_e64 v4, s91, v4\n"
      "v_mfma_f32_16x16x32_bf16 v[20:23], a[16:19], a[140:143], v[20:23]\n"
      "ds_write_addtid_b32 v56 offset:2048\n"
      "v_cvt_pk_bf16_f32 v84, v72, v73\n"
      "v_mfma_f32_16x16x32_bf16 v[20:23], a[20:23], a[152:155], v[20:23]\n"
      "s_waitcnt lgkmcnt(9)\n"
      "ds_write_addtid_b32 v57 offset:38976\n"
      "v_swap_b32 v6, v7\n"
      "v_mfma_f32_16x16x32_bf16 v[76:79], a[84:87], a[68:71], 0\n"
      "ds_write_addtid_b32 v60 offset:512\n"
      "v_cvt_pk_bf16_f32 v90, v74, v75\n"
      "v_mfma_f32_16x16x32_bf16 v[32:35], a[24:27], a[168:171], 0\n"
      "ds_write_addtid_b32 v61 offset:37440\n"
      "v_cvt_pk_bf16_f32 v20, v20, v21\n"
      "v_mfma_f32_16x16x32_bf16 v[32:35], a[28:31], a[180:183], v[32:35]\n"
      "s_mov_b32 m0, s45\n"
      "v_cvt_pk_bf16_f32 v21, v22, v23\n"
      "v_cvt_pk_bf16_f32 v85, v76, v77\n"
      "v_mfma_f32_16x16x32_bf16 v[32:35], a[32:35], a[192:195], v[32:35]\n"
      "buffer_store_dwordx2 v[84:85], v15, s[36:39], s52 offen sc1\n"
      "v_cvt_pk_bf16_f32 v91, v78, v79\n"
      "v_mfma_f32_16x16x32_bf16 v[32:35], a[0:3], a[96:99], v[32:35]\n"
      "v_add_u32_e64 v3, s91, v3\n"
      "v_mfma_f32_16x16x32_bf16 v[32:35], a[4:7], a[108:111], v[32:35]\n"
      "s_sub_u32 s91, 0, s91\n"
      "ds_write_addtid_b32 v20 offset:48136\n"
      "v_mfma_f32_16x16x32_bf16 v[32:35], a[8:11], a[120:123], v[32:35]\n"
      "ds_write_addtid_b32 v21 offset:48392\n"
      "v_mfma_f32_16x16x32_bf16 v[32:35], a[12:15], a[132:135], v[32:35]\n"
      "buffer_store_dwordx2 v[90:91], v15, s[36:39], s52 offen offset:1536 sc1\n"
      "v_mfma_f32_16x16x32_bf16 v[32:35], a[16:19], a[144:147], v[32:35]\n"
      "v_mfma_f32_16x16x32_bf16 v[32:35], a[20:23], a[156:159], v[32:35]\n"
      "s_nop 1\n"
      "v_mfma_f32_16x16x32_bf16 v[20:23], a[12:15], a[88:91], 0\n"
      "s_add_u32 s51, s51, 196608\n"
      "s_nop 1\n"
      "v_mfma_f32_16x16x32_bf16 v[20:23], a[16:19], a[100:103], v[20:23]\n"
      "s_sub_u32 s47, s51, 3342336\n"
      "v_cvt_pk_bf16_f32 v32, v32, v33\n"
      "v_cvt_pk_bf16_f32 v33, v34, v35\n"
      "v_mfma_f32_16x16x32_bf16 v[20:23], a[20:23], a[112:115], v[20:23]\n"
      "s_cmp_gt_u32 s51, s48\n"
      "v_mfma_f32_16x16x32_bf16 v[20:23], a[24:27], a[124:127], v[20:23]\n"
      "s_cselect_b32 s53, s47, s51\n"
      "ds_write_addtid_b32 v32 offset:35840\n"
      "v_mfma_f32_16x16x32_bf16 v[20:23], a[28:31], a[136:139], v[20:23]\n"
      "s_mul_i32 vcc_lo, vcc_lo, 2\n"
      "v_mfma_f32_16x16x32_bf16 v[20:23], a[32:35], a[148:151], v[20:23]\n"
      "ds_write_addtid_b32 v33 offset:36096\n"
      "v_mfma_f32_16x16x32_bf16 v[24:27], a[12:15], a[92:95], 0\n"
      "v_mfma_f32_16x16x32_bf16 v[24:27], a[16:19], a[104:107], v[24:27]\n"
      "v_mfma_f32_16x16x32_bf16 v[24:27], a[20:23], a[116:119], v[24:27]\n"
      "v_mfma_f32_16x16x32_bf16 v[24:27], a[24:27], a[128:131], v[24:27]\n"
      "v_mfma_f32_16x16x32_bf16 v[24:27], a[28:31], a[140:143], v[24:27]\n"
      "s_waitcnt vmcnt(19) lgkmcnt(0)\n"
      "s_barrier\n"
      "v_mfma_f32_16x16x32_bf16 v[24:27], a[32:35], a[152:155], v[24:27]\n"
      "s_add_u32 m0, s58, 13280\n"
      "ds_read_b128 a[36:39], v2 offset:0\n"
      "v_mfma_f32_16x16x32_bf16 v[28:31], a[12:15], a[96:99], 0\n"
      "ds_read_b128 a[44:47], v4 offset:0\n"
      "v_mfma_f32_16x16x32_bf16 v[28:31], a[16:19], a[108:111], v[28:31]\n"
      "ds_read_b128 a[0:3], v7 offset:0\n"
      "v_mfma_f32_16x16x32_bf16 v[28:31], a[20:23], a[120:123], v[28:31]\n"
      "ds_read_b128 a[4:7], v7 offset:256\n"
      "v_mfma_f32_16x16x32_bf16 v[28:31], a[24:27], a[132:135], v[28:31]\n"
      "ds_read_b128 a[8:11], v7 offset:512\n"
      "v_mfma_f32_16x16x32_bf16 v[28:31], a[28:31], a[144:147], v[28:31]\n"
      "ds_read_b128 a[40:43], v3 offset:0\n"
      "v_mfma_f32_16x16x32_bf16 v[28:31], a[32:35], a[156:159], v[28:31]\n"
      "s_waitcnt lgkmcnt(5)\n"
      "ds_read2_b64 a[60:63], v11 offset0:0 offset1:64\n"
      "v_mfma_f32_16x16x32_bf16 v[36:39], a[36:39], a[72:75], 0\n"
      "s_waitcnt lgkmcnt(5)\n"
      "ds_read2_b64 a[64:67], v11 offset0:1 offset1:65\n"
      "v_mfma_f32_16x16x32_bf16 v[44:47], a[44:47], a[80:83], v[36:39]\n"
      "s_waitcnt lgkmcnt(5)\n"
      "ds_read_b128 a[12:15], v6 offset:768\n"
      "v_mfma_f32_16x16x32_bf16 v[20:23], a[0:3], a[160:163], v[20:23]\n"
      "s_waitcnt lgkmcnt(5)\n"
      "ds_read_b128 a[16:19], v6 offset:1024\n"
      "v_mfma_f32_16x16x32_bf16 v[20:23], a[4:7], a[172:175], v[20:23]\n"
      "s_waitcnt lgkmcnt(5)\n"
      "ds_read_b128 a[20:23], v6 offset:1280\n"
      "v_cvt_pk_bf16_f32 v44, v44, v45\n"
      "v_mfma_f32_16x16x32_bf16 v[20:23], a[8:11], a[184:187], v[20:23]\n"
      "s_waitcnt lgkmcnt(5)\n"
      "ds_read_b128 a[24:27], v6 offset:5376\n"
      "v_cvt_pk_bf16_f32 v45, v46, v47\n"
      "v_mfma_f32_16x16x32_bf16 v[40:43], a[40:43], a[76:79], 0\n"
      "ds_read_b128 a[28:31], v6 offset:5632\n"
      "s_nop 0\n"
      "v_mfma_f32_16x16x32_bf16 v[24:27], a[0:3], a[164:167], v[24:27]\n"
      "ds_read_b128 a[32:35], v6 offset:5888\n"
      "v_cvt_pk_bf16_f32 v20, v20, v21\n"
      "v_mfma_f32_16x16x32_bf16 v[24:27], a[4:7], a[176:179], v[24:27]\n"
      "buffer_load_dwordx4 v16, s[32:35], s53 offen lds\n"
      "v_cvt_pk_bf16_f32 v21, v22, v23\n"
      "v_mfma_f32_16x16x32_bf16 v[24:27], a[8:11], a[188:191], v[24:27]\n"
      "s_waitcnt lgkmcnt(6)\n"
      "ds_read_b128 a[48:51], v2 offset:64\n"
      "v_fma_f32 v48, s44, v36, v40\n"
      "v_mfma_f32_16x16x32_bf16 v[68:71], a[84:87], a[60:63], 0\n"
      "s_mov_b32 m0, s45\n"
      "ds_read_b128 a[52:55], v3 offset:64\n"
      "v_fma_f32 v49, s44, v37, v41\n"
      "v_mfma_f32_16x16x32_bf16 v[28:31], a[0:3], a[168:171], v[28:31]\n"
      "s_add_u32 m0, s58, 13280\n"
      "ds_read_b128 a[0:3], v7 offset:768\n"
      "v_cvt_pk_bf16_f32 v24, v24, v25\n"
      "v_mfma_f32_16x16x32_bf16 v[28:31], a[4:7], a[180:183], v[28:31]\n"
      "s_add_u32 s52, s52, 196608\n"
      "ds_read_b128 a[4:7], v7 offset:1024\n"
      "v_cvt_pk_bf16_f32 v25, v26, v27\n"
      "v_mfma_f32_16x16x32_bf16 v[28:31], a[8:11], a[192:195], v[28:31]\n"
      "ds_read_b128 a[8:11], v7 offset:1280\n"
      "v_cvt_pk_bf16_f32 v48, v48, v49\n"
      "v_mfma_f32_16x16x32_bf16 v[72:75], a[84:87], a[64:67], 0\n"
      "s_waitcnt lgkmcnt(6)\n"
      "ds_read_b128 a[56:59], v4 offset:64\n"
      "v_fma_f32 v50, s44, v38, v42\n"
      "v_mfma_f32_16x16x32_bf16 v[32:35], a[12:15], v[92:95], 0\n"
      "ds_read2_b64 a[68:71], v11 offset0:2 offset1:66\n"
      "v_cvt_pk_bf16_f32 v28, v28, v29\n"
      "v_mfma_f32_16x16x32_bf16 v[32:35], a[16:19], v[148:151], v[32:35]\n"
      "ds_read2_b64 a[60:63], v11 offset0:3 offset1:67\n"
      "v_cvt_pk_bf16_f32 v29, v30, v31\n"
      "v_mfma_f32_16x16x32_bf16 v[32:35], a[20:23], v[204:207], v[32:35]\n"
      "buffer_load_dwordx4 v17, s[32:35], s53 offen offset:1024 lds\n"
      "v_fma_f32 v51, s44, v39, v43\n"
      "v_mfma_f32_16x16x32_bf16 v[32:35], a[24:27], v[112:115], v[32:35]\n"
      "s_mov_b32 m0, s45\n"
      "v_cvt_pk_bf16_f32 v49, v50, v51\n"
      "v_fma_f32 v40, s44, v36, -v40\n"
      "v_mfma_f32_16x16x32_bf16 v[32:35], a[28:31], a[208:211], v[32:35]\n"
      "s_waitcnt lgkmcnt(5)\n"
      "ds_write_addtid_b32 v20 offset:57360\n"
      "v_fma_f32 v41, s44, v37, -v41\n"
      "v_mfma_f32_16x16x32_bf16 v[32:35], a[32:35], v[220:223], v[32:35]\n"
      "ds_write_addtid_b32 v21 offset:57616\n"
      "v_cvt_pk_bf16_f32 v40, v40, v41\n"
      "v_mfma_f32_16x16x32_bf16 v[32:35], a[0:3], a[212:215], v[32:35]\n"
      "s_waitcnt lgkmcnt(5)\n"
      "ds_write_addtid_b32 v24 offset:45064\n"
      "v_fma_f32 v42, s44, v38, -v42\n"
      "v_mfma_f32_16x16x32_bf16 v[32:35], a[4:7], v[184:187], v[32:35]\n"
      "ds_write_addtid_b32 v25 offset:45320\n"
      "v_fma_f32 v43, s44, v39, -v43\n"
      "v_mfma_f32_16x16x32_bf16 v[32:35], a[8:11], v[240:243], v[32:35]\n"
      "ds_write_addtid_b32 v28 offset:32768\n"
      "v_cvt_pk_bf16_f32 v41, v42, v43\n"
      "v_mfma_f32_16x16x32_bf16 v[52:55], a[48:51], a[72:75], 0\n"
      "ds_write_addtid_b32 v29 offset:33024\n"
      "v_cvt_pk_bf16_f32 v80, v68, v69\n"
      "v_mfma_f32_16x16x32_bf16 v[56:59], a[52:55], a[76:79], 0\n"
      "s_waitcnt lgkmcnt(6)\n"
      "ds_read_b128 a[36:39], v2 offset:128\n"
      "v_cvt_pk_bf16_f32 v32, v32, v33\n"
      "v_mfma_f32_16x16x32_bf16 v[60:63], a[56:59], a[80:83], v[52:55]\n"
      "s_mov_b32 m0, s18\n"
      "ds_read_b128 a[44:47], v4 offset:128\n"
      "v_cvt_pk_bf16_f32 v86, v70, v71\n"
      "v_mfma_f32_16x16x32_bf16 v[76:79], a[84:87], a[68:71], 0\n"
      "ds_write_addtid_b32 v48 offset:5376\n"
      "v_cvt_pk_bf16_f32 v33, v34, v35\n"
      "v_mfma_f32_16x16x32_bf16 v[68:71], a[84:87], a[60:63], 0\n"
      "ds_write_addtid_b32 v49 offset:42304\n"
      "v_cvt_pk_bf16_f32 v81, v72, v73\n"
      "v_mfma_f32_16x16x32_bf16 v[20:23], a[12:15], v[100:103], 0\n"
      "ds_write_addtid_b32 v40 offset:6144\n"
      "v_fma_f32 v64, s44, v52, v56\n"
      "v_mfma_f32_16x16x32_bf16 v[20:23], a[16:19], a[200:203], v[20:23]\n"
      "ds_write_addtid_b32 v41 offset:43072\n"
      "v_cvt_pk_bf16_f32 v82, v76, v77\n"
      "v_mfma_f32_16x16x32_bf16 v[20:23], a[20:23], v[208:211], v[20:23]\n"
      "ds_write_addtid_b32 v44 offset:4608\n"
      "v_cvt_pk_bf16_f32 v83, v68, v69\n"
      "v_mfma_f32_16x16x32_bf16 v[20:23], a[24:27], a[204:207], v[20:23]\n"
      "ds_write_addtid_b32 v45 offset:41536\n"
      "v_fma_f32 v65, s44, v53, v57\n"
      "v_mfma_f32_16x16x32_bf16 v[20:23], a[28:31], v[172:175], v[20:23]\n"
      "s_mov_b32 m0, s45\n"
      "ds_read_b128 a[40:43], v3 offset:128\n"
      "v_cvt_pk_bf16_f32 v64, v64, v65\n"
      "v_mfma_f32_16x16x32_bf16 v[20:23], a[32:35], v[228:231], v[20:23]\n"
      "s_add_u32 m0, s58, 13280\n"
      "v_cvt_pk_bf16_f32 v87, v74, v75\n"
      "v_fma_f32 v66, s44, v54, v58\n"
      "v_mfma_f32_16x16x32_bf16 v[20:23], a[0:3], v[136:139], v[20:23]\n"
      "buffer_load_dwordx4 v18, s[32:35], s53 offen offset:2048 lds\n"
      "v_fma_f32 v67, s44, v55, v59\n"
      "v_mfma_f32_16x16x32_bf16 v[20:23], a[4:7], v[192:195], v[20:23]\n"
      "s_mov_b32 m0, s45\n"
      "ds_read2_b64 a[64:67], v13 offset0:0 offset1:64\n"
      "v_cvt_pk_bf16_f32 v65, v66, v67\n"
      "v_mfma_f32_16x16x32_bf16 v[20:23], a[8:11], v[244:247], v[20:23]\n"
      "s_waitcnt lgkmcnt(8)\n"
      "ds_write_addtid_b32 v32 offset:58384\n"
      "v_cvt_pk_bf16_f32 v88, v78, v79\n"
      "v_mfma_f32_16x16x32_bf16 v[36:39], a[36:39], a[72:75], 0\n"
      "ds_write_addtid_b32 v33 offset:58640\n"
      "v_cvt_pk_bf16_f32 v89, v70, v71\n"
      "v_mfma_f32_16x16x32_bf16 v[44:47], a[44:47], a[80:83], v[36:39]\n"
      "global_load_dword v8, v9, s[54:55]\n"
      "v_fma_f32 v56, s44, v52, -v56\n"
      "v_mfma_f32_16x16x32_bf16 v[24:27], a[12:15], a[196:199], 0\n"
      "s_mov_b32 m0, s18\n"
      "ds_read_b128 a[12:15], v6 offset:1536\n"
      "v_fma_f32 v57, s44, v53, -v57\n"
      "v_mfma_f32_16x16x32_bf16 v[24:27], a[16:19], v[160:163], v[24:27]\n"
      "ds_read_b128 a[16:19], v6 offset:1792\n"
      "v_cvt_pk_bf16_f32 v56, v56, v57\n"
      "v_mfma_f32_16x16x32_bf16 v[24:27], a[20:23], v[216:219], v[24:27]\n"
      "ds_read_b128 a[20:23], v6 offset:2048\n"
      "v_fma_f32 v58, s44, v54, -v58\n"
      "v_mfma_f32_16x16x32_bf16 v[24:27], a[24:27], v[124:127], v[24:27]\n"
      "ds_read_b128 a[24:27], v6 offset:6144\n"
      "v_fma_f32 v59, s44, v55, -v59\n"
      "v_mfma_f32_16x16x32_bf16 v[24:27], a[28:31], v[180:183], v[24:27]\n"
      "ds_read_b128 a[28:31], v6 offset:6400\n"
      "v_cvt_pk_bf16_f32 v57, v58, v59\n"
      "v_mfma_f32_16x16x32_bf16 v[24:27], a[32:35], v[232:235], v[24:27]\n"
      "ds_read_b128 a[32:35], v6 offset:6656\n"
      "v_cvt_pk_bf16_f32 v60, v60, v61\n"
      "v_mfma_f32_16x16x32_bf16 v[24:27], a[0:3], a[216:219], v[24:27]\n"
      "ds_read_b128 a[0:3], v7 offset:1536\n"
      "v_cvt_pk_bf16_f32 v61, v62, v63\n"
      "v_mfma_f32_16x16x32_bf16 v[24:27], a[4:7], v[196:199], v[24:27]\n"
      "ds_read_b128 a[4:7], v7 offset:1792\n"
      "v_cvt_pk_bf16_f32 v20, v20, v21\n"
      "v_mfma_f32_16x16x32_bf16 v[24:27], a[8:11], v[252:255], v[24:27]\n"
      "s_waitcnt lgkmcnt(7)\n"
      "ds_read_b128 a[8:11], v7 offset:2048\n"
      "v_cvt_pk_bf16_f32 v21, v22, v23\n"
      "v_mfma_f32_16x16x32_bf16 v[40:43], a[40:43], a[76:79], 0\n"
      "buffer_store_dwordx4 v[80:83], v14, s[36:39], s52 offen sc1\n"
      "v_cvt_pk_bf16_f32 v44, v44, v45\n"
      "v_mfma_f32_16x16x32_bf16 v[28:31], a[12:15], v[96:99], 0\n"
      "s_waitcnt lgkmcnt(6)\n"
      "ds_write_addtid_b32 v64 offset:5632\n"
      "v_cvt_pk_bf16_f32 v24, v24, v25\n"
      "v_mfma_f32_16x16x32_bf16 v[28:31], a[16:19], v[152:155], v[28:31]\n"
      "buffer_store_dwordx4 v[86:89], v14, s[36:39], s52 offen offset:1536 sc1\n"
      "v_cvt_pk_bf16_f32 v25, v26, v27\n"
      "v_mfma_f32_16x16x32_bf16 v[28:31], a[20:23], a[220:223], v[28:31]\n"
      "s_waitcnt lgkmcnt(5)\n"
      "ds_write_addtid_b32 v65 offset:42560\n"
      "v_fma_f32 v48, s44, v36, v40\n"
      "v_mfma_f32_16x16x32_bf16 v[28:31], a[24:27], v[116:119], v[28:31]\n"
      "ds_write_addtid_b32 v56 offset:6400\n"
      "v_fma_f32 v49, s44, v37, v41\n"
      "v_mfma_f32_16x16x32_bf16 v[28:31], a[28:31], v[168:171], v[28:31]\n"
      "s_waitcnt lgkmcnt(5)\n"
      "ds_write_addtid_b32 v57 offset:43328\n"
      "v_cvt_pk_bf16_f32 v48, v48, v49\n"
      "v_mfma_f32_16x16x32_bf16 v[28:31], a[32:35], v[224:227], v[28:31]\n"
      "ds_write_addtid_b32 v60 offset:4864\n"
      "v_fma_f32 v50, s44, v38, v42\n"
      "v_mfma_f32_16x16x32_bf16 v[28:31], a[0:3], v[132:135], v[28:31]\n"
      "s_waitcnt lgkmcnt(5)\n"
      "ds_write_addtid_b32 v61 offset:41792\n"
      "v_fma_f32 v51, s44, v39, v43\n"
      "v_mfma_f32_16x16x32_bf16 v[28:31], a[4:7], v[188:191], v[28:31]\n"
      "s_mov_b32 m0, s45\n"
      "ds_read2_b64 a[68:71], v13 offset0:1 offset1:65\n"
      "v_cvt_pk_bf16_f32 v49, v50, v51\n"
      "v_mfma_f32_16x16x32_bf16 v[28:31], a[8:11], a[240:243], v[28:31]\n"
      "ds_write_addtid_b32 v20 offset:46088\n"
      "v_fma_f32 v40, s44, v36, -v40\n"
      "v_mfma_f32_16x16x32_bf16 v[72:75], a[84:87], a[64:67], 0\n"
      "ds_write_addtid_b32 v21 offset:46344\n"
      "v_fma_f32 v41, s44, v37, -v41\n"
      "v_mfma_f32_16x16x32_bf16 v[20:23], a[12:15], v[104:107], 0\n"
      "ds_write_addtid_b32 v24 offset:33792\n"
      "v_cvt_pk_bf16_f32 v40, v40, v41\n"
      "v_mfma_f32_16x16x32_bf16 v[20:23], a[16:19], v[156:159], v[20:23]\n"
      "ds_write_addtid_b32 v25 offset:34048\n"
      "v_cvt_pk_bf16_f32 v28, v28, v29\n"
      "v_mfma_f32_16x16x32_bf16 v[20:23], a[20:23], v[212:215], v[20:23]\n"
      "ds_write_addtid_b32 v28 offset:59408\n"
      "v_cvt_pk_bf16_f32 v29, v30, v31\n"
      "v_mfma_f32_16x16x32_bf16 v[20:23], a[24:27], v[120:123], v[20:23]\n"
      "ds_write_addtid_b32 v29 offset:59664\n"
      "v_fma_f32 v42, s44, v38, -v42\n"
      "v_mfma_f32_16x16x32_bf16 v[20:23], a[28:31], v[176:179], v[20:23]\n"
      "s_mov_b32 m0, s18\n"
      "v_fma_f32 v43, s44, v39, -v43\n"
      "v_cvt_pk_bf16_f32 v41, v42, v43\n"
      "v_mfma_f32_16x16x32_bf16 v[20:23], a[32:35], a[232:235], v[20:23]\n"
      "s_sub_u32 s18, s19, s18\n"
      "ds_write_addtid_b32 v48 offset:5888\n"
      "v_cvt_pk_bf16_f32 v45, v46, v47\n"
      "v_mfma_f32_16x16x32_bf16 v[20:23], a[0:3], v[140:143], v[20:23]\n"
      "ds_write_addtid_b32 v49 offset:42816\n"
      "v_cvt_pk_bf16_f32 v84, v72, v73\n"
      "v_mfma_f32_16x16x32_bf16 v[20:23], a[4:7], a[236:239], v[20:23]\n"
      "ds_write_addtid_b32 v40 offset:6656\n"
      "v_cvt_pk_bf16_f32 v90, v74, v75\n"
      "v_mfma_f32_16x16x32_bf16 v[20:23], a[8:11], v[248:251], v[20:23]\n"
      "s_waitcnt lgkmcnt(9)\n"
      "ds_write_addtid_b32 v41 offset:43584\n"
      "s_nop 0\n"
      "v_mfma_f32_16x16x32_bf16 v[76:79], a[84:87], a[68:71], 0\n"
      "ds_write_addtid_b32 v44 offset:5120\n"
      "s_nop 0\n"
      "v_mfma_f32_16x16x32_bf16 v[32:35], a[12:15], v[108:111], 0\n"
      "ds_write_addtid_b32 v45 offset:42048\n"
      "v_cvt_pk_bf16_f32 v20, v20, v21\n"
      "v_mfma_f32_16x16x32_bf16 v[32:35], a[16:19], v[164:167], v[32:35]\n"
      "s_mov_b32 m0, s45\n"
      "v_cvt_pk_bf16_f32 v21, v22, v23\n"
      "v_cvt_pk_bf16_f32 v85, v76, v77\n"
      "v_mfma_f32_16x16x32_bf16 v[32:35], a[20:23], a[224:227], v[32:35]\n"
      "buffer_store_dwordx2 v[84:85], v15, s[36:39], s52 offen sc1\n"
      "v_cvt_pk_bf16_f32 v91, v78, v79\n"
      "v_mfma_f32_16x16x32_bf16 v[32:35], a[24:27], v[128:131], v[32:35]\n"
      "v_mfma_f32_16x16x32_bf16 v[32:35], a[28:31], a[228:231], v[32:35]\n"
      "ds_write_addtid_b32 v20 offset:47112\n"
      "v_mfma_f32_16x16x32_bf16 v[32:35], a[32:35], v[236:239], v[32:35]\n"
      "ds_write_addtid_b32 v21 offset:47368\n"
      "v_mfma_f32_16x16x32_bf16 v[32:35], a[0:3], v[144:147], v[32:35]\n"
      "buffer_store_dwordx2 v[90:91], v15, s[36:39], s52 offen offset:1536 sc1\n"
      "v_mfma_f32_16x16x32_bf16 v[32:35], a[4:7], v[200:203], v[32:35]\n"
      "v_mfma_f32_16x16x32_bf16 v[32:35], a[8:11], a[244:247], v[32:35]\n"
      "s_nop 1\n"
      "s_cbranch_vccnz Lconv_loop1\n"
      "Lconv_t1:\n"
      "s_add_u32 s52, s52, 196608\n"
      "s_nop 1\n"
      "v_mfma_f32_16x16x32_bf16 v[20:23], a[24:27], v[96:99], 0\n"
      "s_nop 0\n"
      "v_mfma_f32_16x16x32_bf16 v[20:23], a[28:31], v[152:155], v[20:23]\n"
      "v_cvt_pk_bf16_f32 v32, v32, v33\n"
      "v_cvt_pk_bf16_f32 v33, v34, v35\n"
      "v_mfma_f32_16x16x32_bf16 v[20:23], a[32:35], a[220:223], v[20:23]\n"
      "ds_write_addtid_b32 v32 offset:34816\n"
      "v_mfma_f32_16x16x32_bf16 v[20:23], a[0:3], v[116:119], v[20:23]\n"
      "ds_write_addtid_b32 v33 offset:35072\n"
      "v_mfma_f32_16x16x32_bf16 v[20:23], a[4:7], v[168:171], v[20:23]\n"
      "v_mfma_f32_16x16x32_bf16 v[20:23], a[8:11], v[224:227], v[20:23]\n"
      "v_mfma_f32_16x16x32_bf16 v[24:27], a[24:27], v[104:107], 0\n"
      "v_mfma_f32_16x16x32_bf16 v[24:27], a[28:31], v[156:159], v[24:27]\n"
      "v_mfma_f32_16x16x32_bf16 v[24:27], a[32:35], v[212:215], v[24:27]\n"
      "v_mfma_f32_16x16x32_bf16 v[24:27], a[0:3], v[120:123], v[24:27]\n"
      "v_mfma_f32_16x16x32_bf16 v[24:27], a[4:7], v[176:179], v[24:27]\n"
      "s_waitcnt lgkmcnt(0)\n"
      "s_barrier\n"
      "ds_read2_b64 a[60:63], v10 offset0:0 offset1:64\n"
      "v_mfma_f32_16x16x32_bf16 v[24:27], a[8:11], a[232:235], v[24:27]\n"
      "s_waitcnt lgkmcnt(0)\n"
      "ds_read2_b64 a[64:67], v10 offset0:1 offset1:65\n"
      "v_mfma_f32_16x16x32_bf16 v[68:71], a[84:87], a[60:63], 0\n"
      "ds_read2_b64 a[68:71], v10 offset0:2 offset1:66\n"
      "v_mfma_f32_16x16x32_bf16 v[28:31], a[24:27], v[108:111], 0\n"
      "ds_read_b128 a[12:15], v7 offset:6144\n"
      "v_mfma_f32_16x16x32_bf16 v[28:31], a[28:31], v[164:167], v[28:31]\n"
      "ds_read_b128 a[16:19], v7 offset:6400\n"
      "v_mfma_f32_16x16x32_bf16 v[28:31], a[32:35], a[224:227], v[28:31]\n"
      "ds_read_b128 a[20:23], v7 offset:6656\n"
      "v_mfma_f32_16x16x32_bf16 v[28:31], a[0:3], v[128:131], v[28:31]\n"
      "ds_read2_b64 a[60:63], v10 offset0:3 offset1:67\n"
      "v_cvt_pk_bf16_f32 v80, v68, v69\n"
      "v_mfma_f32_16x16x32_bf16 v[28:31], a[4:7], a[228:231], v[28:31]\n"
      "ds_read_b128 a[24:27], v6 offset:5376\n"
      "v_cvt_pk_bf16_f32 v86, v70, v71\n"
      "v_mfma_f32_16x16x32_bf16 v[28:31], a[8:11], v[236:239], v[28:31]\n"
      "s_waitcnt lgkmcnt(6)\n"
      "ds_read_b128 a[28:31], v6 offset:5632\n"
      "v_mfma_f32_16x16x32_bf16 v[72:75], a[84:87], a[64:67], 0\n"
      "s_waitcnt lgkmcnt(6)\n"
      "ds_read2_b64 a[64:67], v12 offset0:0 offset1:64\n"
      "v_mfma_f32_16x16x32_bf16 v[76:79], a[84:87], a[68:71], 0\n"
      "s_waitcnt lgkmcnt(6)\n"
      "ds_read2_b64 a[68:71], v12 offset0:1 offset1:65\n"
      "v_mfma_f32_16x16x32_bf16 v[20:23], a[12:15], v[132:135], v[20:23]\n"
      "s_waitcnt lgkmcnt(6)\n"
      "ds_read_b128 a[32:35], v6 offset:5888\n"
      "v_cvt_pk_bf16_f32 v81, v72, v73\n"
      "v_mfma_f32_16x16x32_bf16 v[20:23], a[16:19], v[188:191], v[20:23]\n"
      "s_waitcnt lgkmcnt(6)\n"
      "ds_read_b128 a[0:3], v7 offset:768\n"
      "v_cvt_pk_bf16_f32 v87, v74, v75\n"
      "v_mfma_f32_16x16x32_bf16 v[20:23], a[20:23], a[240:243], v[20:23]\n"
      "s_waitcnt lgkmcnt(6)\n"
      "ds_read_b128 a[4:7], v7 offset:1024\n"
      "v_cvt_pk_bf16_f32 v82, v76, v77\n"
      "v_mfma_f32_16x16x32_bf16 v[68:71], a[84:87], a[60:63], 0\n"
      "ds_read_b128 a[8:11], v7 offset:1280\n"
      "v_cvt_pk_bf16_f32 v88, v78, v79\n"
      "v_mfma_f32_16x16x32_bf16 v[24:27], a[12:15], v[140:143], v[24:27]\n"
      "v_mfma_f32_16x16x32_bf16 v[24:27], a[16:19], a[236:239], v[24:27]\n"
      "v_cvt_pk_bf16_f32 v20, v20, v21\n"
      "v_cvt_pk_bf16_f32 v21, v22, v23\n"
      "v_mfma_f32_16x16x32_bf16 v[24:27], a[20:23], v[248:251], v[24:27]\n"
      "s_waitcnt lgkmcnt(5)\n"
      "ds_write_addtid_b32 v20 offset:62480\n"
      "v_cvt_pk_bf16_f32 v83, v68, v69\n"
      "v_mfma_f32_16x16x32_bf16 v[72:75], a[84:87], a[64:67], 0\n"
      "ds_write_addtid_b32 v21 offset:62736\n"
      "v_cvt_pk_bf16_f32 v89, v70, v71\n"
      "v_mfma_f32_16x16x32_bf16 v[28:31], a[12:15], v[144:147], v[28:31]\n"
      "ds_read_b128 a[12:15], v7 offset:5376\n"
      "v_cvt_pk_bf16_f32 v24, v24, v25\n"
      "v_mfma_f32_16x16x32_bf16 v[28:31], a[16:19], v[200:203], v[28:31]\n"
      "ds_read_b128 a[16:19], v7 offset:5632\n"
      "v_cvt_pk_bf16_f32 v25, v26, v27\n"
      "v_mfma_f32_16x16x32_bf16 v[28:31], a[20:23], a[244:247], v[28:31]\n"
      "s_waitcnt lgkmcnt(5)\n"
      "ds_read_b128 a[20:23], v7 offset:5888\n"
      "v_cvt_pk_bf16_f32 v84, v72, v73\n"
      "v_mfma_f32_16x16x32_bf16 v[76:79], a[84:87], a[68:71], 0\n"
      "buffer_store_dwordx4 v[80:83], v14, s[36:39], s52 offen sc1\n"
      "v_cvt_pk_bf16_f32 v90, v74, v75\n"
      "v_mfma_f32_16x16x32_bf16 v[32:35], a[24:27], v[92:95], 0\n"
      "ds_write_addtid_b32 v24 offset:50184\n"
      "v_cvt_pk_bf16_f32 v28, v28, v29\n"
      "v_mfma_f32_16x16x32_bf16 v[32:35], a[28:31], v[148:151], v[32:35]\n"
      "buffer_store_dwordx4 v[86:89], v14, s[36:39], s52 offen offset:1536 sc1\n"
      "v_cvt_pk_bf16_f32 v29, v30, v31\n"
      "v_mfma_f32_16x16x32_bf16 v[32:35], a[32:35], v[204:207], v[32:35]\n"
      "ds_write_addtid_b32 v25 offset:50440\n"
      "v_cvt_pk_bf16_f32 v85, v76, v77\n"
      "v_mfma_f32_16x16x32_bf16 v[32:35], a[0:3], v[112:115], v[32:35]\n"
      "ds_write_addtid_b32 v28 offset:37888\n"
      "v_cvt_pk_bf16_f32 v91, v78, v79\n"
      "v_mfma_f32_16x16x32_bf16 v[32:35], a[4:7], a[208:211], v[32:35]\n"
      "s_waitcnt lgkmcnt(5)\n"
      "v_mfma_f32_16x16x32_bf16 v[32:35], a[8:11], v[220:223], v[32:35]\n"
      "ds_write_addtid_b32 v29 offset:38144\n"
      "v_mfma_f32_16x16x32_bf16 v[32:35], a[12:15], a[212:215], v[32:35]\n"
      "s_waitcnt lgkmcnt(4)\n"
      "v_mfma_f32_16x16x32_bf16 v[32:35], a[16:19], v[184:187], v[32:35]\n"
      "buffer_store_dwordx2 v[84:85], v15, s[36:39], s52 offen sc1\n"
      "v_mfma_f32_16x16x32_bf16 v[32:35], a[20:23], v[240:243], v[32:35]\n"
      "s_nop 1\n"
      "v_mfma_f32_16x16x32_bf16 v[20:23], a[24:27], v[100:103], 0\n"
      "buffer_store_dwordx2 v[90:91], v15, s[36:39], s52 offen offset:1536 sc1\n"
      "s_nop 0\n"
      "v_mfma_f32_16x16x32_bf16 v[20:23], a[28:31], a[200:203], v[20:23]\n"
      "s_nop 0\n"
      "v_mfma_f32_16x16x32_bf16 v[20:23], a[32:35], v[208:211], v[20:23]\n"
      "v_cvt_pk_bf16_f32 v32, v32, v33\n"
      "v_cvt_pk_bf16_f32 v33, v34, v35\n"
      "v_mfma_f32_16x16x32_bf16 v[20:23], a[0:3], a[204:207], v[20:23]\n"
      "ds_write_addtid_b32 v32 offset:61456\n"
      "v_mfma_f32_16x16x32_bf16 v[20:23], a[4:7], v[172:175], v[20:23]\n"
      "ds_write_addtid_b32 v33 offset:61712\n"
      "v_mfma_f32_16x16x32_bf16 v[20:23], a[8:11], v[228:231], v[20:23]\n"
      "v_mfma_f32_16x16x32_bf16 v[20:23], a[12:15], v[136:139], v[20:23]\n"
      "v_mfma_f32_16x16x32_bf16 v[20:23], a[16:19], v[192:195], v[20:23]\n"
      "v_mfma_f32_16x16x32_bf16 v[20:23], a[20:23], v[244:247], v[20:23]\n"
      "s_nop 1\n"
      "v_mfma_f32_16x16x32_bf16 v[24:27], a[24:27], a[196:199], 0\n"
      "ds_read_b128 a[24:27], v7 offset:4608\n"
      "s_nop 0\n"
      "v_mfma_f32_16x16x32_bf16 v[24:27], a[28:31], v[160:163], v[24:27]\n"
      "ds_read_b128 a[28:31], v7 offset:4864\n"
      "v_mfma_f32_16x16x32_bf16 v[24:27], a[32:35], v[216:219], v[24:27]\n"
      "ds_read_b128 a[32:35], v7 offset:5120\n"
      "v_cvt_pk_bf16_f32 v20, v20, v21\n"
      "v_mfma_f32_16x16x32_bf16 v[24:27], a[0:3], v[124:127], v[24:27]\n"
      "ds_read_b128 a[0:3], v6 offset:4608\n"
      "v_cvt_pk_bf16_f32 v21, v22, v23\n"
      "v_mfma_f32_16x16x32_bf16 v[24:27], a[4:7], v[180:183], v[24:27]\n"
      "ds_read_b128 a[4:7], v6 offset:4864\n"
      "v_mfma_f32_16x16x32_bf16 v[24:27], a[8:11], v[232:235], v[24:27]\n"
      "ds_read_b128 a[8:11], v6 offset:5120\n"
      "v_mfma_f32_16x16x32_bf16 v[24:27], a[12:15], a[216:219], v[24:27]\n"
      "ds_read_b128 a[12:15], v7 offset:0\n"
      "v_mfma_f32_16x16x32_bf16 v[24:27], a[16:19], v[196:199], v[24:27]\n"
      "ds_read_b128 a[16:19], v7 offset:256\n"
      "v_mfma_f32_16x16x32_bf16 v[24:27], a[20:23], v[252:255], v[24:27]\n"
      "s_waitcnt lgkmcnt(7)\n"
      "ds_read_b128 a[20:23], v7 offset:512\n"
      "s_nop 0\n"
      "v_mfma_f32_16x16x32_bf16 v[28:31], a[24:27], a[160:163], 0\n"
      "s_waitcnt lgkmcnt(7)\n"
      "ds_write_addtid_b32 v20 offset:49160\n"
      "s_nop 0\n"
      "v_mfma_f32_16x16x32_bf16 v[28:31], a[28:31], a[172:175], v[28:31]\n"
      "s_waitcnt lgkmcnt(7)\n"
      "ds_write_addtid_b32 v21 offset:49416\n"
      "v_cvt_pk_bf16_f32 v24, v24, v25\n"
      "v_mfma_f32_16x16x32_bf16 v[28:31], a[32:35], a[184:187], v[28:31]\n"
      "s_waitcnt lgkmcnt(7)\n"
      "v_cvt_pk_bf16_f32 v25, v26, v27\n"
      "v_mfma_f32_16x16x32_bf16 v[28:31], a[0:3], a[88:91], v[28:31]\n"
      "s_waitcnt lgkmcnt(6)\n"
      "ds_write_addtid_b32 v24 offset:36864\n"
      "v_mfma_f32_16x16x32_bf16 v[28:31], a[4:7], a[100:103], v[28:31]\n"
      "s_waitcnt lgkmcnt(6)\n"
      "v_mfma_f32_16x16x32_bf16 v[28:31], a[8:11], a[112:115], v[28:31]\n"
      "s_waitcnt lgkmcnt(5)\n"
      "ds_write_addtid_b32 v25 offset:37120\n"
      "v_mfma_f32_16x16x32_bf16 v[28:31], a[12:15], a[124:127], v[28:31]\n"
      "s_waitcnt lgkmcnt(5)\n"
      "v_mfma_f32_16x16x32_bf16 v[28:31], a[16:19], a[136:139], v[28:31]\n"
      "s_waitcnt lgkmcnt(4)\n"
      "v_mfma_f32_16x16x32_bf16 v[28:31], a[20:23], a[148:151], v[28:31]\n"
      "s_nop 1\n"
      "v_mfma_f32_16x16x32_bf16 v[20:23], a[24:27], a[164:167], 0\n"
      "s_nop 1\n"
      "v_mfma_f32_16x16x32_bf16 v[20:23], a[28:31], a[176:179], v[20:23]\n"
      "s_nop 0\n"
      "v_mfma_f32_16x16x32_bf16 v[20:23], a[32:35], a[188:191], v[20:23]\n"
      "v_cvt_pk_bf16_f32 v28, v28, v29\n"
      "v_cvt_pk_bf16_f32 v29, v30, v31\n"
      "v_mfma_f32_16x16x32_bf16 v[20:23], a[0:3], a[92:95], v[20:23]\n"
      "ds_write_addtid_b32 v28 offset:60432\n"
      "v_mfma_f32_16x16x32_bf16 v[20:23], a[4:7], a[104:107], v[20:23]\n"
      "ds_write_addtid_b32 v29 offset:60688\n"
      "v_mfma_f32_16x16x32_bf16 v[20:23], a[8:11], a[116:119], v[20:23]\n"
      "v_mfma_f32_16x16x32_bf16 v[20:23], a[12:15], a[128:131], v[20:23]\n"
      "v_mfma_f32_16x16x32_bf16 v[20:23], a[16:19], a[140:143], v[20:23]\n"
      "v_mfma_f32_16x16x32_bf16 v[20:23], a[20:23], a[152:155], v[20:23]\n"
      "s_nop 1\n"
      "v_mfma_f32_16x16x32_bf16 v[32:35], a[24:27], a[168:171], 0\n"
      "s_nop 1\n"
      "v_mfma_f32_16x16x32_bf16 v[32:35], a[28:31], a[180:183], v[32:35]\n"
      "s_nop 0\n"
      "v_mfma_f32_16x16x32_bf16 v[32:35], a[32:35], a[192:195], v[32:35]\n"
      "v_cvt_pk_bf16_f32 v20, v20, v21\n"
      "v_cvt_pk_bf16_f32 v21, v22, v23\n"
      "v_mfma_f32_16x16x32_bf16 v[32:35], a[0:3], a[96:99], v[32:35]\n"
      "ds_write_addtid_b32 v20 offset:48136\n"
      "v_mfma_f32_16x16x32_bf16 v[32:35], a[4:7], a[108:111], v[32:35]\n"
      "ds_write_addtid_b32 v21 offset:48392\n"
      "v_mfma_f32_16x16x32_bf16 v[32:35], a[8:11], a[120:123], v[32:35]\n"
      "v_mfma_f32_16x16x32_bf16 v[32:35], a[12:15], a[132:135], v[32:35]\n"
      "v_mfma_f32_16x16x32_bf16 v[32:35], a[16:19], a[144:147], v[32:35]\n"
      "v_mfma_f32_16x16x32_bf16 v[32:35], a[20:23], a[156:159], v[32:35]\n"
      "s_nop 1\n"
      "s_nop 5\n"
      "v_cvt_pk_bf16_f32 v32, v32, v33\n"
      "v_cvt_pk_bf16_f32 v33, v34, v35\n"
      "ds_write_addtid_b32 v32 offset:35840\n"
      "ds_write_addtid_b32 v33 offset:36096\n"
      "s_waitcnt lgkmcnt(0)\n"
      "s_barrier\n"
      "ds_read2_b64 a[0:3], v11 offset0:0 offset1:64\n"
      "ds_read2_b64 a[4:7], v11 offset0:1 offset1:65\n"
      "ds_read2_b64 a[8:11], v11 offset0:2 offset1:66\n"
      "ds_read2_b64 a[12:15], v11 offset0:3 offset1:67\n"
      "ds_read2_b64 a[16:19], v13 offset0:0 offset1:64\n"
      "ds_read2_b64 a[20:23], v13 offset0:1 offset1:65\n"
      "s_waitcnt lgkmcnt(5)\n"
      "v_mfma_f32_16x16x32_bf16 v[68:71], a[84:87], a[0:3], 0\n"
      "s_waitcnt lgkmcnt(4)\n"
      "v_mfma_f32_16x16x32_bf16 v[72:75], a[84:87], a[4:7], 0\n"
      "s_waitcnt lgkmcnt(3)\n"
      "v_mfma_f32_16x16x32_bf16 v[76:79], a[84:87], a[8:11], 0\n"
      "s_waitcnt lgkmcnt(2)\n"
      "s_nop 2\n"
      "v_cvt_pk_bf16_f32 v80, v68, v69\n"
      "v_cvt_pk_bf16_f32 v86, v70, v71\n"
      "v_mfma_f32_16x16x32_bf16 v[68:71], a[84:87], a[12:15], 0\n"
      "s_waitcnt lgkmcnt(1)\n"
      "v_cvt_pk_bf16_f32 v81, v72, v73\n"
      "v_cvt_pk_bf16_f32 v87, v74, v75\n"
      "v_cvt_pk_bf16_f32 v82, v76, v77\n"
      "v_cvt_pk_bf16_f32 v88, v78, v79\n"
      "v_mfma_f32_16x16x32_bf16 v[72:75], a[84:87], a[16:19], 0\n"
      "s_waitcnt lgkmcnt(0)\n"
      "v_mfma_f32_16x16x32_bf16 v[76:79], a[84:87], a[20:23], 0\n"
      "s_add_u32 s52, s52, 196608\n"
      "v_cvt_pk_bf16_f32 v83, v68, v69\n"
      "v_cvt_pk_bf16_f32 v89, v70, v71\n"
      "buffer_store_dwordx4 v[80:83], v14, s[36:39], s52 offen sc1\n"
      "buffer_store_dwordx4 v[86:89], v14, s[36:39], s52 offen offset:1536 sc1\n"
      "s_nop 0\n"
      "v_cvt_pk_bf16_f32 v84, v72, v73\n"
      "v_cvt_pk_bf16_f32 v90, v74, v75\n"
      "v_cvt_pk_bf16_f32 v85, v76, v77\n"
      "v_cvt_pk_bf16_f32 v91, v78, v79\n"
      "buffer_store_dwordx2 v[84:85], v15, s[36:39], s52 offen sc1\n"
      "buffer_store_dwordx2 v[90:91], v15, s[36:39], s52 offen offset:1536 sc1\n"
      "Lconv_u1:\n"
      "Lconv_tail:\n"
      "s_waitcnt vmcnt(0) lgkmcnt(0)\n"
      "Lconv_end:\n"
      :
      : "v"(threadIdx.x), "s"(blockIdx.x), "s"(__builtin_amdgcn_kernarg_segment_ptr())
      : "v1", "v2", "v3", "v4", "v5", "v6", "v7", "v8", "v9", "v10", "v11", "v12", "v13", "v14", "v15", "v16", "v17", "v18", "v19", "v20", "v21", "v22", "v23", "v24", "v25", "v26", "v27", "v28", "v29", "v30", "v31", "v32", "v33", "v34", "v35", "v36", "v37", "v38", "v39", "v40", "v41", "v42", "v43", "v44", "v45", "v46", "v47", "v48", "v49", "v50", "v51", "v52", "v53", "v54", "v55", "v56", "v57", "v58", "v59", "v60", "v61", "v62", "v63", "v64", "v65", "v66", "v67", "v68", "v69", "v70", "v71", "v72", "v73", "v74", "v75", "v76", "v77", "v78", "v79", "v80", "v81", "v82", "v83", "v84", "v85", "v86", "v87", "v88", "v89", "v90", "v91", "v92", "v93", "v94", "v95", "v96", "v97", "v98", "v99", "v100", "v101", "v102", "v103", "v104", "v105", "v106", "v107", "v108", "v109", "v110", "v111", "v112", "v113", "v114", "v115", "v116", "v117", "v118", "v119", "v120", "v121", "v122", "v123", "v124", "v125", "v126", "v127", "v128", "v129", "v130", "v131", "v132", "v133", "v134", "v135", "v136", "v137", "v138", "v139", "v140", "v141", "v142", "v143", "v144", "v145", "v146", "v147", "v148", "v149", "v150", "v151", "v152", "v153", "v154", "v155", "v156", "v157", "v158", "v159", "v160", "v161", "v162", "v163", "v164", "v165", "v166", "v167", "v168", "v169", "v170", "v171", "v172", "v173", "v174", "v175", "v176", "v177", "v178", "v179", "v180", "v181", "v182", "v183", "v184", "v185", "v186", "v187", "v188", "v189", "v190", "v191", "v192", "v193", "v194", "v195", "v196", "v197", "v198", "v199", "v200", "v201", "v202", "v203", "v204", "v205", "v206", "v207", "v208", "v209", "v210", "v211", "v212", "v213", "v214", "v215", "v216", "v217", "v218", "v219", "v220", "v221", "v222", "v223", "v224", "v225", "v226", "v227", "v228", "v229", "v230", "v231", "v232", "v233", "v234", "v235", "v236", "v237", "v238", "v239", "v240", "v241", "v242", "v243", "v244", "v245", "v246", "v247", "v248", "v249", "v250", "v251", "v252", "v253", "v254", "v255", "a0", "a1", "a2", "a3", "a4", "a5", "a6", "a7", "a8", "a9", "a10", "a11", "a12", "a13", "a14", "a15", "a16", "a17", "a18", "a19", "a20", "a21", "a22", "a23", "a24", "a25", "a26", "a27", "a28", "a29", "a30", "a31", "a32", "a33", "a34", "a35", "a36", "a37", "a38", "a39", "a40", "a41", "a42", "a43", "a44", "a45", "a46", "a47", "a48", "a49", "a50", "a51", "a52", "a53", "a54", "a55", "a56", "a57", "a58", "a59", "a60", "a61", "a62", "a63", "a64", "a65", "a66", "a67", "a68", "a69", "a70", "a71", "a72", "a73", "a74", "a75", "a76", "a77", "a78", "a79", "a80", "a81", "a82", "a83", "a84", "a85", "a86", "a87", "a88", "a89", "a90", "a91", "a92", "a93", "a94", "a95", "a96", "a97", "a98", "a99", "a100", "a101", "a102", "a103", "a104", "a105", "a106", "a107", "a108", "a109", "a110", "a111", "a112", "a113", "a114", "a115", "a116", "a117", "a118", "a119", "a120", "a121", "a122", "a123", "a124", "a125", "a126", "a127", "a128", "a129", "a130", "a131", "a132", "a133", "a134", "a135", "a136", "a137", "a138", "a139", "a140", "a141", "a142", "a143", "a144", "a145", "a146", "a147", "a148", "a149", "a150", "a151", "a152", "a153", "a154", "a155", "a156", "a157", "a158", "a159", "a160", "a161", "a162", "a163", "a164", "a165", "a166", "a167", "a168", "a169", "a170", "a171", "a172", "a173", "a174", "a175", "a176", "a177", "a178", "a179", "a180", "a181", "a182", "a183", "a184", "a185", "a186", "a187", "a188", "a189", "a190", "a191", "a192", "a193", "a194", "a195", "a196", "a197", "a198", "a199", "a200", "a201", "a202", "a203", "a204", "a205", "a206", "a207", "a208", "a209", "a210", "a211", "a212", "a213", "a214", "a215", "a216", "a217", "a218", "a219", "a220", "a221", "a222", "a223", "a224", "a225", "a226", "a227", "a228", "a229", "a230", "a231", "a232", "a233", "a234", "a235", "a236", "a237", "a238", "a239", "a240", "a241", "a242", "a243", "a244", "a245", "a246", "a247", "a248", "a249", "a250", "a251", "a252", "a253", "a254", "a255", "s12", "s13", "s14", "s15", "s16", "s17", "s18", "s19", "s20", "s21", "s22", "s23", "s24", "s25", "s26", "s27", "s28", "s29", "s30", "s31", "s34", "s35", "s36", "s37", "s38", "s39", "s40", "s41", "s42", "s43", "s44", "s45", "s46", "s47", "s48", "s49", "s50", "s51", "s52", "s53", "s54", "s55", "s56", "s57", "s58", "s59", "s60", "s61", "s62", "s63", "s64", "s65", "s66", "s67", "s68", "s69", "s70", "s71", "s72", "s73", "s74", "s75", "s76", "s77", "s78", "s79", "s80", "s81", "s82", "s83", "s84", "s85", "s86", "s87", "s88", "s89", "s90", "s91", "s92", "s93", "s94", "s95", "s96", "s97", "s98", "s99", "scc", "vcc", "memory");
}

extern "C" void kernel_launch(void* const* d_in, const int* in_sizes, int n_in, void* d_out,
                              int out_size, void* d_ws, size_t ws_size, hipStream_t stream) {
  conv_kernel<<<256, 256, 0, stream>>>((const char*)d_in[0], (const char*)d_in[1], (char*)d_out, (char*)d_ws);
}
```

```cpp
#include <hip/hip_runtime.h>
#include <hip/hip_bf16.h>
#include <stdint.h>

__global__ void __launch_bounds__(256, 1) __attribute__((aligned(4096)))
conv_kernel(const char* __restrict__ x, const char* __restrict__ w, char* __restrict__ out, char* __restrict__ dbg) {
  __shared__ __attribute__((aligned(1024))) char smem[163840];
  asm volatile("" :: "v"((unsigned)(uintptr_t)smem) : "memory");
  asm volatile(
      "s_load_dwordx8 s[20:27], %2, 0x0\n"
      "v_readfirstlane_b32 s40, %0\n"
      "v_and_b32 v5, 63, %0\n"
      "v_lshlrev_b32 v8, 4, v5\n"
      "s_lshr_b32 s40, s40, 6\n"
      "s_lshl_b32 s43, s40, 3\n"
      "s_or_b32 s43, s43, 0x80000\n"
      "v_bitop3_b32 v9, v8, v5, 32 bitop3:0x78\n"
      "v_xor_b32 v89, 32, v8\n"
      "s_mul_i32 s92, s40, 3072\n"
      "s_lshr_b32 s14, %1, 7\n"
      "s_sub_u32 s14, s14, 1\n"
      "s_lshr_b32 s41, s40, 1\n"
      "s_and_b32 s42, s40, 1\n"
      "s_mov_b32 s30, 165888\n"
      "s_mov_b32 s31, 0x20000\n"
      "s_lshr_b32 s49, %1, 3\n"
      "s_lshl2_add_u32 s49, s49, s40\n"
      "v_lshlrev_b32 v36, 7, v5\n"
      "s_mul_i32 s50, s49, 384\n"
      "v_min_u32 v37, 256, v36\n"
      "v_add_u32 v37, s50, v37\n"
      "v_min_u32 v66, 1280, v36\n"
      "s_mul_i32 s96, s49, 1408\n"
      "s_getpc_b64 s[54:55]\n"
      "Lconv_start:\n"
      "v_min_u32 v37, Lconv_end-Lconv_start-4, v37\n"
      "s_mov_b32 m0, 162816\n"
      "s_nop 0\n"
      "global_load_lds_dword v37, s[54:55]\n"
      "s_bfe_u32 s99, %1, 0x30003\n"
      "s_mul_i32 s49, s40, 192\n"
      "v_add_u32 v36, s49, v5\n"
      "v_add_u32 v38, 128, v36\n"
      "v_mul_u32_u24 v37, 669, v36\n"
      "v_lshrrev_b32 v37, 16, v37\n"
      "v_mad_i32_i24 v37, v37, -2, v38\n"
      "v_lshlrev_b32 v16, 4, v37\n"
      "v_add_u32 v36, 64, v36\n"
      "v_mul_u32_u24 v37, 669, v36\n"
      "v_lshrrev_b32 v37, 16, v37\n"
      "v_mad_i32_i24 v37, v37, -2, v38\n"
      "v_lshlrev_b32 v17, 4, v37\n"
      "v_add_u32 v36, 64, v36\n"
      "v_mul_u32_u24 v37, 669, v36\n"
      "v_lshrrev_b32 v37, 16, v37\n"
      "v_mad_i32_i24 v37, v37, -2, v38\n"
      "v_lshlrev_b32 v18, 4, v37\n"
      "s_or_b32 s50, s99, s40\n"
      "s_cmp_eq_u32 s50, 0\n"
      "s_cselect_b32 s16, 0xffffff, 0\n"
      "s_mov_b32 s17, 0\n"
      "s_cmp_eq_u32 s99, 7\n"
      "s_cselect_b32 s19, 0xffffff00, 0\n"
      "s_mov_b32 s18, 0\n"
      "v_add_u32 v19, 14080, v8\n"
      "v_cndmask_b32 v16, v16, 2.0, s[16:17]\n"
      "v_cndmask_b32 v19, v19, 2.0, s[18:19]\n"
      "v_and_b32 v36, 16, v8\n"
      "v_mov_b32 v52, 0xffff\n"
      "v_lshlrev_b32 v52, v36, v52\n"
      "s_mov_b32 s16, 0x3000003\n"
      "s_mov_b32 s17, 0x3000003\n"
      "v_cndmask_b32 v48, 0, v52, s[16:17]\n"
      "s_lshl_b64 s[16:17], s[16:17], 2\n"
      "v_cndmask_b32 v49, 0, v52, s[16:17]\n"
      "s_lshl_b64 s[16:17], s[16:17], 2\n"
      "v_cndmask_b32 v50, 0, v52, s[16:17]\n"
      "s_lshl_b64 s[16:17], s[16:17], 2\n"
      "v_cndmask_b32 v51, 0, v52, s[16:17]\n"
      "v_bfe_u32 v53, v5, 5, 1\n"
      "v_xor_b32 v53, s41, v53\n"
      "v_sub_u32 v53, 0, v53\n"
      "v_and_b32 v36, 0x1000100, v53\n"
      "v_xor_b32 v36, 0x3fa83fa8, v36\n"
      "v_and_b32 v68, v36, v48\n"
      "v_and_b32 v69, v36, v49\n"
      "v_and_b32 v70, v36, v50\n"
      "v_and_b32 v71, v36, v51\n"
      "v_xor_b32 v72, 0x80008000, v68\n"
      "v_xor_b32 v73, 0x80008000, v69\n"
      "v_xor_b32 v74, 0x80008000, v70\n"
      "v_xor_b32 v75, 0x80008000, v71\n"
      "v_bfe_i32 v36, v5, 5, 1\n"
      "v_and_b32 v36, 0x3f283f28, v36\n"
      "v_xor_b32 v37, 0x3f283f28, v36\n"
      "v_and_b32 v76, v37, v48\n"
      "v_and_b32 v77, v37, v49\n"
      "v_and_b32 v78, v37, v50\n"
      "v_and_b32 v79, v37, v51\n"
      "v_and_b32 v80, v36, v48\n"
      "v_and_b32 v81, v36, v49\n"
      "v_and_b32 v82, v36, v50\n"
      "v_and_b32 v83, v36, v51\n"
      "s_mul_i32 s50, s42, 96\n"
      "v_bfe_u32 v36, v5, 1, 3\n"
      "v_and_b32 v37, 1, v5\n"
      "v_lshl_add_u32 v36, v37, 3, v36\n"
      "v_mul_u32_u24 v36, 192, v36\n"
      "v_add_u32 v36, 46144, v36\n"
      "v_lshlrev_b32 v37, 5, v37\n"
      "v_and_b32 v38, 16, v5\n"
      "v_add_u32 v38, s50, v38\n"
      "v_and_b32 v39, 32, v5\n"
      "v_mul_u32_u24 v40, 192, v39\n"
      "v_add_u32 v40, v40, v36\n"
      "v_xad_u32 v84, v38, v37, v40\n"
      "v_add_u32 v41, 32, v38\n"
      "v_xad_u32 v85, v41, v37, v40\n"
      "v_add_u32 v41, v39, v38\n"
      "v_xad_u32 v86, v41, v37, v36\n"
      "s_mul_i32 s49, s41, 6144\n"
      "s_add_u32 s49, s49, 46144\n"
      "v_lshrrev_b32 v36, 3, v5\n"
      "v_and_b32 v38, 4, v5\n"
      "v_lshl_add_u32 v36, v38, 1, v36\n"
      "v_mul_u32_u24 v36, 192, v36\n"
      "v_lshlrev_b32 v37, 3, v5\n"
      "v_and_b32 v38, 8, v37\n"
      "v_add3_u32 v36, v36, v38, s49\n"
      "v_and_b32 v38, 32, v37\n"
      "v_and_b32 v37, 16, v37\n"
      "v_add_u32 v37, s50, v37\n"
      "v_xad_u32 v87, v37, v38, v36\n"
      "v_add_u32 v39, 32, v37\n"
      "v_xad_u32 v88, v39, v38, v36\n"
      "s_waitcnt lgkmcnt(0)\n"
      "s_mov_b32 s28, s22\n"
      "s_and_b32 s29, s23, 0xffff\n"
      "buffer_load_dword v66, s[28:31], s96 offen lds\n"
      "s_add_u32 m0, s92, 73824\n"
      "s_and_b32 s97, 0x3c2c24, s14\n"
      "s_xor_b32 s97, 0x120c0600, s97\n"
      "s_bfe_u32 s97, s97, s43\n"
      "s_mul_i32 s97, s97, 3072\n"
      "buffer_load_dwordx4 v8, s[28:31], s97 offen offset:0 lds\n"
      "buffer_load_dwordx4 v9, s[28:31], s97 offen offset:1024 lds\n"
      "buffer_load_dwordx4 v89, s[28:31], s97 offen offset:2048 lds\n"
      "s_add_u32 m0, s92, 86112\n"
      "s_and_b32 s97, 0x2c240000, s14\n"
      "s_xor_b32 s97, 0x2a241e18, s97\n"
      "s_bfe_u32 s97, s97, s43\n"
      "s_mul_i32 s97, s97, 3072\n"
      "buffer_load_dwordx4 v8, s[28:31], s97 offen offset:0 lds\n"
      "buffer_load_dwordx4 v9, s[28:31], s97 offen offset:1024 lds\n"
      "buffer_load_dwordx4 v89, s[28:31], s97 offen offset:2048 lds\n"
      "s_and_b32 s53, %1, 7\n"
      "s_lshl_b32 s53, s53, 6\n"
      "s_bfe_u32 s50, %1, 0x10006\n"
      "s_add_u32 s53, s53, s50\n"
      "s_lshl1_add_u32 s50, s14, 32\n"
      "s_add_u32 s53, s53, s50\n"
      "s_mul_i32 s13, s53, 98304\n"
      "s_or_b32 s12, s14, 1\n"
      "s_mul_i32 s12, s12, 196608\n"
      "s_lshl_b32 s50, s12, 1\n"
      "s_sub_u32 s51, s13, s50\n"
      "s_mul_i32 s99, s99, 12288\n"
      "s_sub_u32 s50, s99, 2432\n"
      "s_ashr_i32 s49, s50, 31\n"
      "s_add_u32 s32, s20, s50\n"
      "s_addc_u32 s33, s21, s49\n"
      "s_and_b32 s33, s33, 0xffff\n"
      "s_sub_u32 s34, 50331648, s50\n"
      "s_mov_b32 s35, 0x20000\n"
      "s_add_u32 s46, s92, 110720\n"
      "v_lshrrev_b32 v36, 1, v5\n"
      "v_and_b32 v37, 4, v36\n"
      "v_and_or_b32 v37, v5, 2, v37\n"
      "v_mul_u32_u24 v37, 1568, v37\n"
      "v_xor_b32 v36, v36, v5\n"
      "v_and_b32 v36, 4, v36\n"
      "v_mad_u32_u24 v37, v36, 48, v37\n"
      "s_mul_i32 s50, s42, 1568\n"
      "s_add_u32 s50, s50, 110720\n"
      "v_and_b32 v36, 16, v5\n"
      "v_lshl_add_u32 v37, v36, 1, v37\n"
      "v_and_b32 v36, 16, v8\n"
      "v_add3_u32 v37, v37, v36, s50\n"
      "v_and_b32 v36, 32, v5\n"
      "v_mad_u32_u24 v38, v36, 24, v37\n"
      "v_mad_u32_u24 v39, v36, 25, v37\n"
      "s_mul_i32 s17, s42, 0xcc00cc00\n"
      "s_mov_b32 s16, 0\n"
      "v_mov_b32 v36, 768\n"
      "v_mov_b32 v37, 736\n"
      "s_mov_b64 exec, 0\n"
      "s_cmp_eq_u32 s40, 0\n"
      "s_cbranch_scc1 Lconv_p1\n"
      "s_cmp_eq_u32 s40, 1\n"
      "s_cbranch_scc1 Lconv_p2\n"
      "s_cmp_eq_u32 s40, 2\n"
      "s_cbranch_scc1 Lconv_pa0\n"
      "s_cmp_eq_u32 s40, 3\n"
      "s_cbranch_scc1 Lconv_pa1\n"
      "Lconv_r0:\n"
      "s_mov_b64 exec, -1\n"
      "s_add_u32 m0, s92, 98400\n"
      "s_and_b32 s97, 0x3c2c243c, s14\n"
      "s_xor_b32 s97, 0xd070130, s97\n"
      "s_bfe_u32 s97, s97, s43\n"
      "s_mul_i32 s97, s97, 3072\n"
      "buffer_load_dwordx4 v8, s[28:31], s97 offen offset:0 lds\n"
      "buffer_load_dwordx4 v9, s[28:31], s97 offen offset:1024 lds\n"
      "buffer_load_dwordx4 v89, s[28:31], s97 offen offset:2048 lds\n"
      "v_cndmask_b32 v36, v36, v37, s[16:17]\n"
      "v_add_u32 v39, v39, v36\n"
      "s_cmp_eq_u32 s41, 0\n"
      "s_cselect_b64 vcc, -1, 0\n"
      "v_add_u32 v40, 384, v38\n"
      "v_add_u32 v41, 384, v39\n"
      "v_cndmask_b32 v2, v40, v39, vcc\n"
      "v_cndmask_b32 v3, v39, v40, vcc\n"
      "v_cndmask_b32 v4, v41, v38, vcc\n"
      "v_and_b32 v36, 3, v5\n"
      "v_mul_u32_u24 v36, 18464, v36\n"
      "v_bfe_u32 v37, v5, 2, 2\n"
      "v_lshrrev_b32 v38, 1, v37\n"
      "v_xor_b32 v37, v37, v38\n"
      "v_lshl_add_u32 v36, v37, 6, v36\n"
      "s_mul_i32 s50, s41, 2304\n"
      "v_and_b32 v37, 48, v5\n"
      "v_add3_u32 v6, v36, v37, s50\n"
      "v_add_u32 v7, 9216, v6\n"
      "s_mov_b32 s16, 0xc0003\n"
      "s_mov_b32 s17, 0xc0003\n"
      "v_cndmask_b32 v48, 0, v52, s[16:17]\n"
      "s_lshl_b64 s[16:17], s[16:17], 4\n"
      "v_cndmask_b32 v49, 0, v52, s[16:17]\n"
      "s_lshl_b64 s[16:17], s[16:17], 4\n"
      "v_cndmask_b32 v50, 0, v52, s[16:17]\n"
      "s_lshl_b64 s[16:17], s[16:17], 4\n"
      "v_cndmask_b32 v51, 0, v52, s[16:17]\n"
      "v_and_b32 v36, 0xff00ff00, v53\n"
      "v_xor_b32 v36, 0xc080c080, v36\n"
      "v_and_b32 v38, v36, v48\n"
      "v_and_b32 v39, v36, v49\n"
      "v_and_b32 v40, v36, v50\n"
      "v_and_b32 v41, v36, v51\n"
      "v_accvgpr_write_b32 a72, v38\n"
      "v_accvgpr_write_b32 a73, v39\n"
      "v_accvgpr_write_b32 a74, v40\n"
      "v_accvgpr_write_b32 a75, v41\n"
      "v_and_b32 v36, 0xff00ff00, v53\n"
      "v_xor_b32 v36, 0xc05dc05d, v36\n"
      "v_and_b32 v38, v36, v48\n"
      "v_and_b32 v39, v36, v49\n"
      "v_and_b32 v40, v36, v50\n"
      "v_and_b32 v41, v36, v51\n"
      "v_accvgpr_write_b32 a76, v38\n"
      "v_accvgpr_write_b32 a77, v39\n"
      "v_accvgpr_write_b32 a78, v40\n"
      "v_accvgpr_write_b32 a79, v41\n"
      "v_and_b32 v36, 0x81008100, v53\n"
      "v_xor_b32 v36, 0x3f803f80, v36\n"
      "v_and_b32 v38, v36, v48\n"
      "v_and_b32 v39, v36, v49\n"
      "v_and_b32 v40, v36, v50\n"
      "v_and_b32 v41, v36, v51\n"
      "v_accvgpr_write_b32 a80, v38\n"
      "v_accvgpr_write_b32 a81, v39\n"
      "v_accvgpr_write_b32 a82, v40\n"
      "v_accvgpr_write_b32 a83, v41\n"
      "s_mov_b32 s44, 0x3fdd0000\n"
      "v_lshlrev_b32 v36, 5, v5\n"
      "v_and_b32 v36, 0x180, v36\n"
      "v_lshlrev_b32 v37, 3, v5\n"
      "v_and_b32 v37, 16, v37\n"
      "v_sub_u32 v38, 0x3e71, v36\n"
      "v_add_u32 v39, 0x3cf1, v36\n"
      "v_mov_b32 v40, 0x3f80\n"
      "s_mov_b64 vcc, 0xffff\n"
      "s_mov_b32 s16, 0xffff0000\n"
      "s_mov_b32 s17, 0xf0f0\n"
      "v_xor_b32 v41, 0x8000, v38\n"
      "v_cndmask_b32 v38, v38, v41, s[16:17]\n"
      "v_cndmask_b32 v38, v38, v40, vcc\n"
      "v_lshlrev_b32 v38, v37, v38\n"
      "v_xor_b32 v41, 0x8000, v39\n"
      "s_xor_b32 s17, s17, 0xffff\n"
      "v_cndmask_b32 v39, v39, v41, s[16:17]\n"
      "v_cndmask_b32 v39, v39, v40, vcc\n"
      "v_lshlrev_b32 v39, v37, v39\n"
      "s_mov_b32 s18, 0x55550005\n"
      "s_mov_b32 s19, 0x5555\n"
      "v_cndmask_b32 v42, 0, v38, s[18:19]\n"
      "s_lshl_b64 s[16:17], s[18:19], 1\n"
      "v_cndmask_b32 v43, 0, v38, s[16:17]\n"
      "v_accvgpr_write_b32 a84, v42\n"
      "v_accvgpr_write_b32 a85, v43\n"
      "s_mov_b32 s18, 0x55555000\n"
      "v_cndmask_b32 v42, 0, v39, s[18:19]\n"
      "s_lshl_b64 s[16:17], s[18:19], 1\n"
      "v_cndmask_b32 v43, 0, v39, s[16:17]\n"
      "v_accvgpr_write_b32 a86, v42\n"
      "v_accvgpr_write_b32 a87, v43\n"
      "v_lshrrev_b32 v36, 4, v5\n"
      "v_min_u32 v37, 2, v36\n"
      "v_lshlrev_b32 v37, 10, v37\n"
      "s_lshl_b32 s50, s41, 7\n"
      "s_lshl_b32 s49, s42, 8\n"
      "s_add_u32 s50, s50, s49\n"
      "s_add_u32 s50, s50, 98416\n"
      "v_and_b32 v38, 8, v5\n"
      "v_lshl_add_u32 v37, v38, 3, v37\n"
      "v_add_u32 v37, s50, v37\n"
      "s_movk_i32 s50, -12296\n"
      "s_mov_b32 s49, 43040\n"
      "v_mov_b32 v42, s49\n"
      "v_bfe_u32 v39, v5, 1, 2\n"
      "v_lshrrev_b32 v40, 2, v38\n"
      "v_add_u32 v39, v39, v40\n"
      "v_lshlrev_b32 v40, 5, v5\n"
      "v_and_b32 v40, 32, v40\n"
      "v_cmp_lt_u32 vcc, 2, v39\n"
      "v_mad_i32_i24 v39, v39, s50, v37\n"
      "v_add_u32 v39, v39, v40\n"
      "v_cndmask_b32 v41, 0, v42, vcc\n"
      "v_add_u32 v10, v39, v41\n"
      "v_add_u32 v11, 3072, v10\n"
      "v_bfe_u32 v39, v5, 2, 1\n"
      "v_lshrrev_b32 v40, 1, v38\n"
      "v_sub_u32 v39, v39, v40\n"
      "v_add_u32 v39, 4, v39\n"
      "v_and_b32 v40, 48, v8\n"
      "v_cmp_lt_u32 vcc, 2, v39\n"
      "v_mad_i32_i24 v39, v39, s50, v37\n"
      "v_add_u32 v39, v39, v40\n"
      "v_cndmask_b32 v41, 0, v42, vcc\n"
      "v_add_u32 v12, v39, v41\n"
      "v_add_u32 v13, 3072, v12\n"
      "v_mul_u32_u24 v36, 384, v36\n"
      "v_and_b32 v37, 15, v5\n"
      "v_lshl_add_u32 v39, v37, 3, v36\n"
      "v_add_u32 v15, 128, v39\n"
      "v_add_lshl_u32 v37, v37, v38, 4\n"
      "v_add_u32 v14, v37, v36\n"
      "s_add_u32 s50, s99, s92\n"
      "s_add_u32 s36, s24, s50\n"
      "s_addc_u32 s37, s25, 0\n"
      "s_and_b32 s37, s37, 0xffff\n"
      "s_sub_u32 s38, 50331648, s50\n"
      "s_mov_b32 s39, 0x20000\n"
      "s_mul_i32 s48, s12, 17\n"
      "s_mul_i32 s45, s42, 6152\n"
      "s_lshl_b32 s50, s41, 9\n"
      "s_add_u32 s45, s45, s50\n"
      "s_add_u32 s45, s45, 41056\n"
      "s_mov_b32 s98, 0\n"
      "s_mov_b32 s91, 26560\n"
      "s_mul_i32 s18, s42, 18464\n"
      "s_mul_i32 s50, s41, 2304\n"
      "s_add_u32 s18, s18, s50\n"
      "s_lshl1_add_u32 s19, s18, 9216\n"
      "s_add_u32 s58, s46, 26560\n"
      "s_lshl1_add_u32 s93, s46, 26560\n"
      "s_mul_i32 s59, s42, -13280\n"
      "s_add_u32 s59, s59, 136256\n"
      "s_lshl1_add_u32 s94, s59, 26560\n"
      "s_mul_i32 s50, s42, 26560\n"
      "s_add_u32 s59, s59, s50\n"
      "s_mul_i32 s95, s42, s12\n"
      "s_sub_u32 s95, s12, s95\n"
      "s_mov_b32 s52, 2.0\n"
      "s_cmp_lt_u32 s40, 3\n"
      "s_cselect_b32 s97, 27680, 0\n"
      "s_add_u32 s97, s97, s92\n"
      "s_add_u32 m0, s97, 36928\n"
      "s_and_b32 s97, 0x24000000, s14\n"
      "s_xor_b32 s97, 0x251f1913, s97\n"
      "s_bfe_u32 s97, s97, s43\n"
      "s_mul_i32 s97, s97, 3072\n"
      "buffer_load_dwordx4 v8, s[28:31], s97 offen offset:0 lds\n"
      "buffer_load_dwordx4 v9, s[28:31], s97 offen offset:1024 lds\n"
      "buffer_load_dwordx4 v89, s[28:31], s97 offen offset:2048 lds\n"
      "s_cmp_lt_u32 s40, 2\n"
      "s_cselect_b32 s97, 27680, 0\n"
      "s_add_u32 s97, s97, s92\n"
      "s_add_u32 m0, s97, 21536\n"
      "s_and_b32 s97, 0x24243c2c, s14\n"
      "s_xor_b32 s97, 0x802312b, s97\n"
      "s_bfe_u32 s97, s97, s43\n"
      "s_mul_i32 s97, s97, 3072\n"
      "buffer_load_dwordx4 v8, s[28:31], s97 offen offset:0 lds\n"
      "buffer_load_dwordx4 v9, s[28:31], s97 offen offset:1024 lds\n"
      "buffer_load_dwordx4 v89, s[28:31], s97 offen offset:2048 lds\n"
      "s_cmp_lt_u32 s40, 1\n"
      "s_cselect_b32 s97, 27680, 0\n"
      "s_add_u32 s97, s97, s92\n"
      "s_add_u32 m0, s97, 6144\n"
      "s_and_b32 s97, 0x3c, s14\n"
      "s_xor_b32 s97, 0x201a140e, s97\n"
      "s_bfe_u32 s97, s97, s43\n"
      "s_mul_i32 s97, s97, 3072\n"
      "buffer_load_dwordx4 v8, s[28:31], s97 offen offset:0 lds\n"
      "buffer_load_dwordx4 v9, s[28:31], s97 offen offset:1024 lds\n"
      "buffer_load_dwordx4 v89, s[28:31], s97 offen offset:2048 lds\n"
      "s_sub_u32 vcc_lo, 0, s41\n"
      "s_mov_b32 vcc_hi, 0\n"
      ".p2align 3\n"
      "s_waitcnt vmcnt(12)\n"
      "s_barrier\n"
      "s_cmp_lt_u32 s40, 3\n"
      "s_cselect_b32 s97, -9248, 0\n"
      "ds_read_b128 a[0:3], v86 offset:30752\n"
      "ds_read_b128 a[4:7], v84 offset:27680\n"
      "ds_read_b128 a[8:11], v85 offset:27680\n"
      "ds_read_b128 a[12:15], v86 offset:30816\n"
      "s_add_u32 s97, s97, s92\n"
      "s_add_u32 m0, s97, 9248\n"
      "s_and_b32 s97, 0x243c2424, s14\n"
      "s_xor_b32 s97, 0x3322c26, s97\n"
      "s_bfe_u32 s97, s97, s43\n"
      "s_mul_i32 s97, s97, 3072\n"
      "s_waitcnt lgkmcnt(3)\n"
      "v_mfma_f32_16x16x32_bf16 v[20:23], a[0:3], v[76:79], 0\n"
      "s_waitcnt lgkmcnt(2)\n"
      "ds_read_b128 a[16:19], v84 offset:27744\n"
      "ds_read_b128 a[20:23], v86 offset:39968\n"
      "buffer_load_dwordx4 v8, s[28:31], s97 offen offset:0 lds\n"
      "v_mfma_f32_16x16x32_bf16 v[24:27], a[4:7], v[68:71], v[20:23]\n"
      "ds_read_b128 a[24:27], v84 offset:36896\n"
      "ds_read_b128 a[28:31], v85 offset:36896\n"
      "v_mfma_f32_16x16x32_bf16 v[20:23], a[4:7], v[72:75], v[20:23]\n"
      "ds_read_b64_tr_b16 a[88:89], v87 offset:27680\n"
      "ds_read_b64_tr_b16 a[92:93], v88 offset:27680\n"
      "buffer_load_dwordx4 v9, s[28:31], s97 offen offset:1024 lds\n"
      "v_mfma_f32_16x16x32_bf16 v[28:31], a[0:3], v[80:83], 0\n"
      "s_waitcnt lgkmcnt(7)\n"
      "ds_read_b64_tr_b16 a[96:97], v87 offset:27744\n"
      "ds_read_b64_tr_b16 a[124:125], v87 offset:36896\n"
      "v_mfma_f32_16x16x32_bf16 v[32:35], a[8:11], v[68:71], v[28:31]\n"
      "ds_read_b64_tr_b16 a[128:129], v88 offset:36896\n"
      "ds_read_b128 a[32:35], v86 offset:40032\n"
      "v_mfma_f32_16x16x32_bf16 v[28:31], a[8:11], v[72:75], v[28:31]\n"
      "s_waitcnt lgkmcnt(10)\n"
      "ds_read_b128 a[0:3], v84 offset:36960\n"
      "ds_read_b64_tr_b16 a[132:133], v87 offset:36960\n"
      "buffer_load_dwordx4 v89, s[28:31], s97 offen offset:2048 lds\n"
      "v_mfma_f32_16x16x32_bf16 v[54:57], a[12:15], v[76:79], 0\n"
      "s_waitcnt lgkmcnt(10)\n"
      "s_cmp_lt_u32 s40, 2\n"
      "s_cselect_b32 s97, -9248, 0\n"
      "s_add_u32 s97, s97, s92\n"
      "s_add_u32 m0, s97, 30784\n"
      "s_and_b32 s97, 0x3c24, s14\n"
      "s_xor_b32 s97, 0x1b150f09, s97\n"
      "s_bfe_u32 s97, s97, s43\n"
      "s_mul_i32 s97, s97, 3072\n"
      "v_cvt_pk_bf16_f32 v96, v20, v21\n"
      "v_cvt_pk_bf16_f32 v97, v22, v23\n"
      "v_mfma_f32_16x16x32_bf16 v[58:61], a[16:19], v[68:71], v[54:57]\n"
      "v_cvt_pk_bf16_f32 v92, v24, v25\n"
      "v_cvt_pk_bf16_f32 v93, v26, v27\n"
      "v_mfma_f32_16x16x32_bf16 v[54:57], a[16:19], v[72:75], v[54:57]\n"
      "buffer_load_dwordx4 v8, s[28:31], s97 offen offset:0 lds\n"
      "v_cvt_pk_bf16_f32 v104, v28, v29\n"
      "v_cvt_pk_bf16_f32 v105, v30, v31\n"
      "v_mfma_f32_16x16x32_bf16 v[62:65], a[20:23], v[76:79], 0\n"
      "s_waitcnt lgkmcnt(4)\n"
      "v_cvt_pk_bf16_f32 v100, v32, v33\n"
      "v_cvt_pk_bf16_f32 v101, v34, v35\n"
      "v_mfma_f32_16x16x32_bf16 v[20:23], a[24:27], v[68:71], v[62:65]\n"
      "v_cvt_pk_bf16_f32 v108, v54, v55\n"
      "v_cvt_pk_bf16_f32 v109, v56, v57\n"
      "v_mfma_f32_16x16x32_bf16 v[62:65], a[24:27], v[72:75], v[62:65]\n"
      "s_waitcnt lgkmcnt(0)\n"
      "s_waitcnt vmcnt(13)\n"
      "s_cbranch_vccnz Lconv_h1\n"
      "s_barrier\n"
      "Lconv_h1:\n"
      "buffer_load_dwordx4 v9, s[28:31], s97 offen offset:1024 lds\n"
      "v_mfma_f32_16x16x32_bf16 v[24:27], a[20:23], v[80:83], 0\n"
      "v_cvt_pk_bf16_f32 v58, v58, v59\n"
      "v_cvt_pk_bf16_f32 v60, v60, v61\n"
      "v_mfma_f32_16x16x32_bf16 v[28:31], a[28:31], v[68:71], v[24:27]\n"
      "s_cbranch_vccz Lconv_h2\n"
      "s_barrier\n"
      "Lconv_h2:\n"
      "ds_read_b128 a[4:7], v86 offset:49184\n"
      "ds_read_b128 a[8:11], v84 offset:46112\n"
      "ds_read_b128 a[12:15], v85 offset:46112\n"
      "ds_read_b128 a[16:19], v86 offset:49248\n"
      "v_mfma_f32_16x16x32_bf16 v[24:27], a[28:31], v[72:75], v[24:27]\n"
      "ds_read_b128 a[20:23], v84 offset:46176\n"
      "ds_read_b128 a[24:27], v86 offset:58400\n"
      "buffer_load_dwordx4 v89, s[28:31], s97 offen offset:2048 lds\n"
      "v_accvgpr_write_b32 a196, v58\n"
      "v_mfma_f32_16x16x32_bf16 v[32:35], a[32:35], v[76:79], 0\n"
      "s_cmp_lt_u32 s40, 1\n"
      "s_cselect_b32 s97, -9248, 0\n"
      "ds_read_b128 a[28:31], v84 offset:55328\n"
      "ds_read_b128 a[32:35], v85 offset:55328\n"
      "s_add_u32 s97, s97, s92\n"
      "s_add_u32 m0, s97, 52320\n"
      "s_and_b32 s97, 0x3c242400, s14\n"
      "s_xor_b32 s97, 0x332d2721, s97\n"
      "s_bfe_u32 s97, s97, s43\n"
      "s_mul_i32 s97, s97, 3072\n"
      "v_accvgpr_write_b32 a197, v60\n"
      "v_cvt_pk_bf16_f32 v116, v62, v63\n"
      "v_mfma_f32_16x16x32_bf16 v[54:57], a[0:3], v[68:71], v[32:35]\n"
      "ds_read_b64_tr_b16 a[160:161], v87 offset:46112\n"
      "ds_read_b64_tr_b16 a[164:165], v88 offset:46112\n"
      "buffer_load_dwordx4 v8, s[28:31], s97 offen offset:0 lds\n"
      "v_cvt_pk_bf16_f32 v117, v64, v65\n"
      "v_mfma_f32_16x16x32_bf16 v[32:35], a[0:3], v[72:75], v[32:35]\n"
      "s_waitcnt lgkmcnt(9)\n"
      "ds_read_b64_tr_b16 a[168:169], v87 offset:46176\n"
      "ds_read_b64_tr_b16 a[90:91], v87 offset:55328\n"
      "v_cvt_pk_bf16_f32 v112, v20, v21\n"
      "v_cvt_pk_bf16_f32 v113, v22, v23\n"
      "v_mfma_f32_16x16x32_bf16 v[58:61], a[4:7], v[76:79], 0\n"
      "s_waitcnt lgkmcnt(10)\n"
      "ds_read_b64_tr_b16 a[94:95], v88 offset:55328\n"
      "ds_read_b128 a[0:3], v86 offset:58464\n"
      "v_cvt_pk_bf16_f32 v120, v24, v25\n"
      "v_cvt_pk_bf16_f32 v121, v26, v27\n"
      "v_mfma_f32_16x16x32_bf16 v[62:65], a[8:11], v[68:71], v[58:61]\n"
      "ds_read_b64_tr_b16 a[98:99], v87 offset:55392\n"
      "buffer_load_dwordx4 v9, s[28:31], s97 offen offset:1024 lds\n"
      "v_cvt_pk_bf16_f32 v28, v28, v29\n"
      "v_cvt_pk_bf16_f32 v30, v30, v31\n"
      "v_mfma_f32_16x16x32_bf16 v[58:61], a[8:11], v[72:75], v[58:61]\n"
      "v_accvgpr_write_b32 a204, v28\n"
      "v_accvgpr_write_b32 a205, v30\n"
      "v_cvt_pk_bf16_f32 v128, v32, v33\n"
      "v_cvt_pk_bf16_f32 v129, v34, v35\n"
      "v_mfma_f32_16x16x32_bf16 v[20:23], a[4:7], v[80:83], 0\n"
      "s_waitcnt lgkmcnt(7)\n"
      "ds_read_b128 a[4:7], v84 offset:55392\n"
      "v_cvt_pk_bf16_f32 v124, v54, v55\n"
      "v_mfma_f32_16x16x32_bf16 v[24:27], a[12:15], v[68:71], v[20:23]\n"
      "buffer_load_dwordx4 v89, s[28:31], s97 offen offset:2048 lds\n"
      "v_cvt_pk_bf16_f32 v125, v56, v57\n"
      "v_mfma_f32_16x16x32_bf16 v[20:23], a[12:15], v[72:75], v[20:23]\n"
      "s_add_u32 m0, s46, 13280\n"
      "s_add_u32 s51, s51, s12\n"
      "s_nop 0\n"
      "v_cvt_pk_bf16_f32 v132, v58, v59\n"
      "v_cvt_pk_bf16_f32 v133, v60, v61\n"
      "v_mfma_f32_16x16x32_bf16 v[28:31], a[16:19], v[76:79], 0\n"
      "v_cvt_pk_bf16_f32 v62, v62, v63\n"
      "v_cvt_pk_bf16_f32 v64, v64, v65\n"
      "v_mfma_f32_16x16x32_bf16 v[32:35], a[20:23], v[68:71], v[28:31]\n"
      "buffer_load_dwordx4 v16, s[32:35], s51 offen lds\n"
      "v_accvgpr_write_b32 a212, v62\n"
      "v_accvgpr_write_b32 a213, v64\n"
      "v_mfma_f32_16x16x32_bf16 v[28:31], a[20:23], v[72:75], v[28:31]\n"
      "s_add_u32 m0, s46, 13280\n"
      "v_cvt_pk_bf16_f32 v140, v20, v21\n"
      "v_cvt_pk_bf16_f32 v141, v22, v23\n"
      "v_mfma_f32_16x16x32_bf16 v[54:57], a[24:27], v[76:79], 0\n"
      "v_cvt_pk_bf16_f32 v136, v24, v25\n"
      "v_cvt_pk_bf16_f32 v137, v26, v27\n"
      "v_mfma_f32_16x16x32_bf16 v[58:61], a[28:31], v[68:71], v[54:57]\n"
      "buffer_load_dwordx4 v17, s[32:35], s51 offen offset:1024 lds\n"
      "v_cvt_pk_bf16_f32 v144, v28, v29\n"
      "v_mfma_f32_16x16x32_bf16 v[54:57], a[28:31], v[72:75], v[54:57]\n"
      "s_waitcnt lgkmcnt(0)\n"
      "s_waitcnt vmcnt(11)\n"
      "s_cbranch_vccnz Lconv_h3\n"
      "s_barrier\n"
      "Lconv_h3:\n"
      "s_add_u32 m0, s46, 13280\n"
      "v_cvt_pk_bf16_f32 v145, v30, v31\n"
      "v_mfma_f32_16x16x32_bf16 v[62:65], a[24:27], v[80:83], 0\n"
      "buffer_load_dwordx4 v18, s[32:35], s51 offen offset:2048 lds\n"
      "v_cvt_pk_bf16_f32 v32, v32, v33\n"
      "v_mfma_f32_16x16x32_bf16 v[20:23], a[32:35], v[68:71], v[62:65]\n"
      "s_cbranch_vccz Lconv_h4\n"
      "s_barrier\n"
      "Lconv_h4:\n"
      "s_add_u32 m0, s46, 26560\n"
      "s_add_u32 s51, s51, s12\n"
      "s_nop 0\n"
      "ds_read_b128 a[8:11], v86 offset:21536\n"
      "ds_read_b128 a[12:15], v84 offset:18464\n"
      "ds_read_b128 a[16:19], v85 offset:18464\n"
      "ds_read_b128 a[20:23], v86 offset:21600\n"
      "v_mfma_f32_16x16x32_bf16 v[62:65], a[32:35], v[72:75], v[62:65]\n"
      "ds_read_b128 a[24:27], v84 offset:18528\n"
      "ds_read_b128 a[28:31], v86 offset:3072\n"
      "v_cvt_pk_bf16_f32 v34, v34, v35\n"
      "v_accvgpr_write_b32 a216, v32\n"
      "v_mfma_f32_16x16x32_bf16 v[24:27], a[0:3], v[76:79], 0\n"
      "ds_read_b128 a[32:35], v84 offset:0\n"
      "ds_read_b128 a[0:3], v85 offset:0\n"
      "buffer_load_dwordx4 v16, s[32:35], s51 offen lds\n"
      "v_accvgpr_write_b32 a217, v34\n"
      "v_mfma_f32_16x16x32_bf16 v[28:31], a[4:7], v[68:71], v[24:27]\n"
      "s_add_u32 m0, s46, 26560\n"
      "ds_read_b64_tr_b16 a[126:127], v87 offset:18464\n"
      "ds_read_b64_tr_b16 a[130:131], v88 offset:18464\n"
      "v_cvt_pk_bf16_f32 v98, v54, v55\n"
      "v_cvt_pk_bf16_f32 v99, v56, v57\n"
      "v_mfma_f32_16x16x32_bf16 v[24:27], a[4:7], v[72:75], v[24:27]\n"
      "s_waitcnt lgkmcnt(9)\n"
      "ds_read_b128 a[4:7], v86 offset:3136\n"
      "ds_read_b64_tr_b16 a[134:135], v87 offset:18528\n"
      "v_cvt_pk_bf16_f32 v94, v58, v59\n"
      "v_cvt_pk_bf16_f32 v95, v60, v61\n"
      "v_mfma_f32_16x16x32_bf16 v[32:35], a[8:11], v[76:79], 0\n"
      "s_waitcnt lgkmcnt(10)\n"
      "ds_read_b64_tr_b16 a[162:163], v87 offset:0\n"
      "ds_read_b64_tr_b16 a[166:167], v88 offset:0\n"
      "buffer_load_dwordx4 v17, s[32:35], s51 offen offset:1024 lds\n"
      "v_cvt_pk_bf16_f32 v106, v62, v63\n"
      "v_mfma_f32_16x16x32_bf16 v[54:57], a[12:15], v[68:71], v[32:35]\n"
      "s_add_u32 m0, s46, 26560\n"
      "s_sub_u32 s96, s51, s95\n"
      "ds_read_b64_tr_b16 a[170:171], v87 offset:64\n"
      "v_cvt_pk_bf16_f32 v107, v64, v65\n"
      "v_cvt_pk_bf16_f32 v102, v20, v21\n"
      "v_cvt_pk_bf16_f32 v103, v22, v23\n"
      "v_mfma_f32_16x16x32_bf16 v[32:35], a[12:15], v[72:75], v[32:35]\n"
      "v_cvt_pk_bf16_f32 v110, v24, v25\n"
      "v_cvt_pk_bf16_f32 v111, v26, v27\n"
      "v_cvt_pk_bf16_f32 v28, v28, v29\n"
      "v_cvt_pk_bf16_f32 v30, v30, v31\n"
      "v_mfma_f32_16x16x32_bf16 v[58:61], a[8:11], v[80:83], 0\n"
      "s_waitcnt lgkmcnt(7)\n"
      "ds_read_b128 a[8:11], v84 offset:64\n"
      "buffer_load_dwordx4 v18, s[32:35], s51 offen offset:2048 lds\n"
      "v_accvgpr_write_b32 a198, v28\n"
      "v_accvgpr_write_b32 a199, v30\n"
      "v_mfma_f32_16x16x32_bf16 v[62:65], a[16:19], v[68:71], v[58:61]\n"
      "s_mov_b32 m0, s59\n"
      "s_nop 0\n"
      "v_cvt_pk_bf16_f32 v118, v32, v33\n"
      "v_cvt_pk_bf16_f32 v119, v34, v35\n"
      "v_add_u32 v86, -46144, v86\n"
      "v_cvt_pk_bf16_f32 v114, v54, v55\n"
      "v_mfma_f32_16x16x32_bf16 v[58:61], a[16:19], v[72:75], v[58:61]\n"
      "Lconv_p1:\n"
      "s_cbranch_execz Lconv_r0\n"
      "s_nop 0\n"
      "ds_read_b128 a[12:15], v86 offset:30752\n"
      "v_cvt_pk_bf16_f32 v115, v56, v57\n"
      "v_add_u32 v84, -46144, v84\n"
      "v_cvt_pk_bf16_f32 v62, v62, v63\n"
      "v_mfma_f32_16x16x32_bf16 v[20:23], a[20:23], v[76:79], 0\n"
      "ds_read_b128 a[16:19], v84 offset:27680\n"
      "buffer_load_dwordx4 v19, s[32:35], s96 offen lds\n"
      "v_mfma_f32_16x16x32_bf16 v[24:27], a[24:27], v[68:71], v[20:23]\n"
      "s_add_u32 m0, s92, 73824\n"
      "s_and_b32 s97, 0x24242c, s14\n"
      "s_xor_b32 s97, 0x16100a04, s97\n"
      "s_bfe_u32 s97, s97, s43\n"
      "s_nop 0\n"
      "s_mul_i32 s97, s97, 3072\n"
      "v_cvt_pk_bf16_f32 v122, v58, v59\n"
      "v_cvt_pk_bf16_f32 v123, v60, v61\n"
      "v_mfma_f32_16x16x32_bf16 v[20:23], a[24:27], v[72:75], v[20:23]\n"
      "ds_read_b128 a[24:27], v86 offset:30816\n"
      "buffer_load_dwordx4 v8, s[28:31], s97 offen offset:0 lds\n"
      "v_cvt_pk_bf16_f32 v64, v64, v65\n"
      "v_accvgpr_write_b32 a206, v62\n"
      "v_mfma_f32_16x16x32_bf16 v[28:31], a[28:31], v[76:79], 0\n"
      "v_accvgpr_write_b32 a207, v64\n"
      "v_add_u32 v85, -46144, v85\n"
      "v_mfma_f32_16x16x32_bf16 v[32:35], a[32:35], v[68:71], v[28:31]\n"
      "ds_read_b128 a[20:23], v85 offset:27680\n"
      "v_cvt_pk_bf16_f32 v130, v20, v21\n"
      "v_cvt_pk_bf16_f32 v131, v22, v23\n"
      "v_mfma_f32_16x16x32_bf16 v[28:31], a[32:35], v[72:75], v[28:31]\n"
      "buffer_load_dwordx4 v9, s[28:31], s97 offen offset:1024 lds\n"
      "v_add_u32 v87, -46144, v87\n"
      "v_add_u32 v88, -46144, v88\n"
      "v_mfma_f32_16x16x32_bf16 v[54:57], a[28:31], v[80:83], 0\n"
      "ds_read_b64_tr_b16 a[100:101], v87 offset:27680\n"
      "ds_read_b64_tr_b16 a[104:105], v88 offset:27680\n"
      "v_mfma_f32_16x16x32_bf16 v[58:61], a[0:3], v[68:71], v[54:57]\n"
      "ds_read_b128 a[28:31], v84 offset:27744\n"
      "ds_read_b64_tr_b16 a[108:109], v87 offset:27744\n"
      "v_cvt_pk_bf16_f32 v126, v24, v25\n"
      "v_cvt_pk_bf16_f32 v127, v26, v27\n"
      "v_mfma_f32_16x16x32_bf16 v[54:57], a[0:3], v[72:75], v[54:57]\n"
      "s_waitcnt lgkmcnt(5)\n"
      "buffer_load_dwordx4 v89, s[28:31], s97 offen offset:2048 lds\n"
      "v_cvt_pk_bf16_f32 v134, v28, v29\n"
      "v_cvt_pk_bf16_f32 v135, v30, v31\n"
      "v_mfma_f32_16x16x32_bf16 v[62:65], a[4:7], v[76:79], 0\n"
      "s_add_u32 m0, s92, 86112\n"
      "s_and_b32 s97, 0x242c0000, s14\n"
      "s_xor_b32 s97, 0x2e28221c, s97\n"
      "s_bfe_u32 s97, s97, s43\n"
      "s_mul_i32 s97, s97, 3072\n"
      "v_cvt_pk_bf16_f32 v32, v32, v33\n"
      "v_cvt_pk_bf16_f32 v34, v34, v35\n"
      "v_mfma_f32_16x16x32_bf16 v[20:23], a[8:11], v[68:71], v[62:65]\n"
      "v_accvgpr_write_b32 a214, v32\n"
      "v_accvgpr_write_b32 a215, v34\n"
      "v_mfma_f32_16x16x32_bf16 v[62:65], a[8:11], v[72:75], v[62:65]\n"
      "buffer_load_dwordx4 v8, s[28:31], s97 offen offset:0 lds\n"
      "v_cvt_pk_bf16_f32 v142, v54, v55\n"
      "v_cvt_pk_bf16_f32 v143, v56, v57\n"
      "v_mfma_f32_16x16x32_bf16 v[24:27], a[12:15], v[76:79], 0\n"
      "v_cvt_pk_bf16_f32 v138, v58, v59\n"
      "v_cvt_pk_bf16_f32 v139, v60, v61\n"
      "v_mfma_f32_16x16x32_bf16 v[28:31], a[16:19], v[68:71], v[24:27]\n"
      "v_cvt_pk_bf16_f32 v20, v20, v21\n"
      "v_cvt_pk_bf16_f32 v146, v62, v63\n"
      "v_mfma_f32_16x16x32_bf16 v[24:27], a[16:19], v[72:75], v[24:27]\n"
      "s_waitcnt lgkmcnt(0)\n"
      "s_waitcnt vmcnt(11)\n"
      "s_cbranch_vccnz Lconv_h5\n"
      "s_barrier\n"
      "Lconv_h5:\n"
      "buffer_load_dwordx4 v9, s[28:31], s97 offen offset:1024 lds\n"
      "v_mfma_f32_16x16x32_bf16 v[32:35], a[12:15], v[80:83], 0\n"
      "v_cvt_pk_bf16_f32 v147, v64, v65\n"
      "v_cvt_pk_bf16_f32 v22, v22, v23\n"
      "v_mfma_f32_16x16x32_bf16 v[54:57], a[20:23], v[68:71], v[32:35]\n"
      "s_cbranch_vccz Lconv_h6\n"
      "s_barrier\n"
      "Lconv_h6:\n"
      "ds_read_b128 a[32:35], v86 offset:12288\n"
      "ds_read_b128 a[0:3], v84 offset:9216\n"
      "ds_read_b128 a[4:7], v85 offset:9216\n"
      "ds_read_b128 a[8:11], v86 offset:12352\n"
      "buffer_load_dwordx4 v89, s[28:31], s97 offen offset:2048 lds\n"
      "v_mfma_f32_16x16x32_bf16 v[32:35], a[20:23], v[72:75], v[32:35]\n"
      "s_add_u32 m0, s92, 98400\n"
      "s_and_b32 s97, 0x24242c24, s14\n"
      "ds_read_b128 a[12:15], v84 offset:9280\n"
      "ds_read_b128 a[16:19], v86 offset:3072\n"
      "s_xor_b32 s97, 0x110b0534, s97\n"
      "s_bfe_u32 s97, s97, s43\n"
      "s_mul_i32 s97, s97, 3072\n"
      "v_accvgpr_write_b32 a218, v20\n"
      "v_accvgpr_write_b32 a219, v22\n"
      "v_mfma_f32_16x16x32_bf16 v[58:61], a[24:27], v[76:79], 0\n"
      "ds_read_b128 a[20:23], v84 offset:0\n"
      "ds_read_b128 a[24:27], v85 offset:0\n"
      "v_cvt_pk_bf16_f32 v152, v24, v25\n"
      "v_cvt_pk_bf16_f32 v153, v26, v27\n"
      "v_mfma_f32_16x16x32_bf16 v[62:65], a[28:31], v[68:71], v[58:61]\n"
      "ds_read_b64_tr_b16 a[136:137], v87 offset:9216\n"
      "ds_read_b64_tr_b16 a[140:141], v88 offset:9216\n"
      "buffer_load_dwordx4 v8, s[28:31], s97 offen offset:0 lds\n"
      "v_cvt_pk_bf16_f32 v148, v28, v29\n"
      "v_mfma_f32_16x16x32_bf16 v[58:61], a[28:31], v[72:75], v[58:61]\n"
      "s_waitcnt lgkmcnt(9)\n"
      "ds_read_b128 a[28:31], v86 offset:3136\n"
      "ds_read_b64_tr_b16 a[144:145], v87 offset:9280\n"
      "v_cvt_pk_bf16_f32 v149, v30, v31\n"
      "v_cvt_pk_bf16_f32 v156, v32, v33\n"
      "v_mfma_f32_16x16x32_bf16 v[20:23], a[32:35], v[76:79], 0\n"
      "s_waitcnt lgkmcnt(10)\n"
      "ds_read_b64_tr_b16 a[172:173], v87 offset:0\n"
      "ds_read_b64_tr_b16 a[176:177], v88 offset:0\n"
      "v_cvt_pk_bf16_f32 v157, v34, v35\n"
      "v_mfma_f32_16x16x32_bf16 v[24:27], a[0:3], v[68:71], v[20:23]\n"
      "ds_read_b64_tr_b16 a[180:181], v87 offset:64\n"
      "ds_read_b64_tr_b16 a[102:103], v87 offset:18464\n"
      "buffer_load_dwordx4 v9, s[28:31], s97 offen offset:1024 lds\n"
      "v_cvt_pk_bf16_f32 v54, v54, v55\n"
      "v_mfma_f32_16x16x32_bf16 v[20:23], a[0:3], v[72:75], v[20:23]\n"
      "ds_read_b128 a[0:3], v86 offset:21536\n"
      "ds_read_b64_tr_b16 a[106:107], v88 offset:18464\n"
      "v_cvt_pk_bf16_f32 v56, v56, v57\n"
      "v_accvgpr_write_b32 a200, v54\n"
      "v_mfma_f32_16x16x32_bf16 v[28:31], a[32:35], v[80:83], 0\n"
      "s_waitcnt lgkmcnt(10)\n"
      "ds_read_b128 a[32:35], v84 offset:64\n"
      "ds_read_b64_tr_b16 a[110:111], v87 offset:18528\n"
      "v_accvgpr_write_b32 a201, v56\n"
      "v_cvt_pk_bf16_f32 v164, v58, v59\n"
      "v_mfma_f32_16x16x32_bf16 v[32:35], a[4:7], v[68:71], v[28:31]\n"
      "ds_read_b64_tr_b16 a[138:139], v87 offset:36928\n"
      "ds_read_b64_tr_b16 a[142:143], v88 offset:36928\n"
      "buffer_load_dwordx4 v89, s[28:31], s97 offen offset:2048 lds\n"
      "v_cvt_pk_bf16_f32 v165, v60, v61\n"
      "v_mfma_f32_16x16x32_bf16 v[28:31], a[4:7], v[72:75], v[28:31]\n"
      "s_cmp_lt_u32 s40, 3\n"
      "s_cselect_b32 s97, 27680, 0\n"
      "ds_read_b128 a[4:7], v84 offset:18464\n"
      "ds_read_b64_tr_b16 a[146:147], v87 offset:36992\n"
      "s_add_u32 s97, s97, s92\n"
      "s_add_u32 m0, s97, 36928\n"
      "s_and_b32 s97, 0x2c000000, s14\n"
      "s_xor_b32 s97, 0x29231d17, s97\n"
      "s_bfe_u32 s97, s97, s43\n"
      "s_nop 0\n"
      "s_mul_i32 s97, s97, 3072\n"
      "v_cvt_pk_bf16_f32 v160, v62, v63\n"
      "v_cvt_pk_bf16_f32 v161, v64, v65\n"
      "v_mfma_f32_16x16x32_bf16 v[54:57], a[8:11], v[76:79], 0\n"
      "ds_read_b128 a[8:11], v85 offset:18464\n"
      "ds_read_b64_tr_b16 a[174:175], v87 offset:55392\n"
      "v_cvt_pk_bf16_f32 v168, v20, v21\n"
      "v_cvt_pk_bf16_f32 v169, v22, v23\n"
      "v_mfma_f32_16x16x32_bf16 v[58:61], a[12:15], v[68:71], v[54:57]\n"
      "ds_read_b64_tr_b16 a[178:179], v88 offset:55392\n"
      "ds_read_b64_tr_b16 a[182:183], v87 offset:55456\n"
      "buffer_load_dwordx4 v8, s[28:31], s97 offen offset:0 lds\n"
      "v_cvt_pk_bf16_f32 v24, v24, v25\n"
      "v_mfma_f32_16x16x32_bf16 v[54:57], a[12:15], v[72:75], v[54:57]\n"
      "ds_read_b128 a[12:15], v86 offset:21600\n"
      "v_cvt_pk_bf16_f32 v26, v26, v27\n"
      "v_accvgpr_write_b32 a208, v24\n"
      "v_accvgpr_write_b32 a209, v26\n"
      "v_mfma_f32_16x16x32_bf16 v[62:65], a[16:19], v[76:79], 0\n"
      "buffer_load_dwordx4 v9, s[28:31], s97 offen offset:1024 lds\n"
      "v_cvt_pk_bf16_f32 v176, v28, v29\n"
      "v_cvt_pk_bf16_f32 v177, v30, v31\n"
      "v_cvt_pk_bf16_f32 v172, v32, v33\n"
      "v_mfma_f32_16x16x32_bf16 v[20:23], a[20:23], v[68:71], v[62:65]\n"
      "v_cvt_pk_bf16_f32 v173, v34, v35\n"
      "v_cvt_pk_bf16_f32 v54, v54, v55\n"
      "v_cvt_pk_bf16_f32 v56, v56, v57\n"
      "v_mfma_f32_16x16x32_bf16 v[62:65], a[20:23], v[72:75], v[62:65]\n"
      "ds_read_b128 a[20:23], v86 offset:40000\n"
      "v_accvgpr_write_b32 a228, v54\n"
      "v_accvgpr_write_b32 a229, v56\n"
      "v_mfma_f32_16x16x32_bf16 v[24:27], a[16:19], v[80:83], 0\n"
      "ds_read_b128 a[16:19], v84 offset:18528\n"
      "buffer_load_dwordx4 v89, s[28:31], s97 offen offset:2048 lds\n"
      "v_mfma_f32_16x16x32_bf16 v[28:31], a[24:27], v[68:71], v[24:27]\n"
      "s_cmp_lt_u32 s40, 2\n"
      "s_cselect_b32 s97, 6144, 0\n"
      "s_add_u32 s97, s97, s92\n"
      "s_add_u32 m0, s97, 43072\n"
      "s_and_b32 s97, 0x24242424, s14\n"
      "s_xor_b32 s97, 0x352f352f, s97\n"
      "s_bfe_u32 s97, s97, s43\n"
      "s_mul_i32 s97, s97, 3072\n"
      "v_cvt_pk_bf16_f32 v180, v58, v59\n"
      "v_cvt_pk_bf16_f32 v181, v60, v61\n"
      "v_mfma_f32_16x16x32_bf16 v[24:27], a[24:27], v[72:75], v[24:27]\n"
      "s_waitcnt lgkmcnt(2)\n"
      "ds_read_b128 a[24:27], v84 offset:36928\n"
      "v_cvt_pk_bf16_f32 v188, v62, v63\n"
      "v_cvt_pk_bf16_f32 v189, v64, v65\n"
      "v_cvt_pk_bf16_f32 v184, v20, v21\n"
      "v_mfma_f32_16x16x32_bf16 v[32:35], a[28:31], v[76:79], 0\n"
      "ds_read_b128 a[28:31], v85 offset:36928\n"
      "buffer_load_dwordx4 v8, s[28:31], s97 offen offset:0 lds\n"
      "v_cvt_pk_bf16_f32 v185, v22, v23\n"
      "v_mfma_f32_16x16x32_bf16 v[54:57], a[32:35], v[68:71], v[32:35]\n"
      "v_cvt_pk_bf16_f32 v24, v24, v25\n"
      "v_cvt_pk_bf16_f32 v26, v26, v27\n"
      "v_mfma_f32_16x16x32_bf16 v[32:35], a[32:35], v[72:75], v[32:35]\n"
      "ds_read_b128 a[32:35], v86 offset:40064\n"
      "v_accvgpr_write_b32 a236, v24\n"
      "v_accvgpr_write_b32 a237, v26\n"
      "v_mfma_f32_16x16x32_bf16 v[58:61], a[0:3], v[76:79], 0\n"
      "buffer_load_dwordx4 v9, s[28:31], s97 offen offset:1024 lds\n"
      "v_cvt_pk_bf16_f32 v192, v28, v29\n"
      "v_mfma_f32_16x16x32_bf16 v[62:65], a[4:7], v[68:71], v[58:61]\n"
      "v_cvt_pk_bf16_f32 v193, v30, v31\n"
      "v_cvt_pk_bf16_f32 v200, v32, v33\n"
      "v_mfma_f32_16x16x32_bf16 v[58:61], a[4:7], v[72:75], v[58:61]\n"
      "ds_read_b128 a[4:7], v86 offset:58464\n"
      "v_cvt_pk_bf16_f32 v201, v34, v35\n"
      "v_mfma_f32_16x16x32_bf16 v[20:23], a[0:3], v[80:83], 0\n"
      "ds_read_b128 a[0:3], v84 offset:36992\n"
      "buffer_load_dwordx4 v89, s[28:31], s97 offen offset:2048 lds\n"
      "v_mfma_f32_16x16x32_bf16 v[24:27], a[8:11], v[68:71], v[20:23]\n"
      "s_add_u32 m0, s46, 39840\n"
      "s_add_u32 s51, s51, s12\n"
      "v_cvt_pk_bf16_f32 v196, v54, v55\n"
      "v_cvt_pk_bf16_f32 v197, v56, v57\n"
      "v_cvt_pk_bf16_f32 v154, v58, v59\n"
      "v_cvt_pk_bf16_f32 v155, v60, v61\n"
      "v_mfma_f32_16x16x32_bf16 v[20:23], a[8:11], v[72:75], v[20:23]\n"
      "ds_read_b128 a[8:11], v84 offset:55392\n"
      "buffer_load_dwordx4 v16, s[32:35], s51 offen lds\n"
      "v_cvt_pk_bf16_f32 v150, v62, v63\n"
      "v_cvt_pk_bf16_f32 v151, v64, v65\n"
      "v_mfma_f32_16x16x32_bf16 v[28:31], a[12:15], v[76:79], 0\n"
      "s_waitcnt lgkmcnt(3)\n"
      "s_add_u32 m0, s46, 39840\n"
      "ds_read_b128 a[12:15], v85 offset:55392\n"
      "v_cvt_pk_bf16_f32 v158, v20, v21\n"
      "v_mfma_f32_16x16x32_bf16 v[32:35], a[16:19], v[68:71], v[28:31]\n"
      "v_cvt_pk_bf16_f32 v159, v22, v23\n"
      "v_cvt_pk_bf16_f32 v24, v24, v25\n"
      "v_mfma_f32_16x16x32_bf16 v[28:31], a[16:19], v[72:75], v[28:31]\n"
      "ds_read_b128 a[16:19], v86 offset:58528\n"
      "buffer_load_dwordx4 v17, s[32:35], s51 offen offset:1024 lds\n"
      "v_cvt_pk_bf16_f32 v26, v26, v27\n"
      "v_accvgpr_write_b32 a202, v24\n"
      "v_mfma_f32_16x16x32_bf16 v[54:57], a[20:23], v[76:79], 0\n"
      "s_add_u32 m0, s46, 39840\n"
      "v_accvgpr_write_b32 a203, v26\n"
      "v_cvt_pk_bf16_f32 v162, v32, v33\n"
      "v_mfma_f32_16x16x32_bf16 v[58:61], a[24:27], v[68:71], v[54:57]\n"
      "v_cvt_pk_bf16_f32 v166, v28, v29\n"
      "v_cvt_pk_bf16_f32 v167, v30, v31\n"
      "v_cvt_pk_bf16_f32 v163, v34, v35\n"
      "v_mfma_f32_16x16x32_bf16 v[54:57], a[24:27], v[72:75], v[54:57]\n"
      "buffer_load_dwordx4 v18, s[32:35], s51 offen offset:2048 lds\n"
      "v_add_u32 v86, 46144, v86\n"
      "v_add_u32 v85, 46144, v85\n"
      "v_mfma_f32_16x16x32_bf16 v[62:65], a[20:23], v[80:83], 0\n"
      "s_add_u32 m0, s46, 0\n"
      "s_add_u32 s51, s51, s12\n"
      "ds_read_b128 a[20:23], v84 offset:55456\n"
      "v_cvt_pk_bf16_f32 v58, v58, v59\n"
      "v_mfma_f32_16x16x32_bf16 v[20:23], a[28:31], v[68:71], v[62:65]\n"
      "v_cvt_pk_bf16_f32 v170, v54, v55\n"
      "v_cvt_pk_bf16_f32 v171, v56, v57\n"
      "v_mfma_f32_16x16x32_bf16 v[62:65], a[28:31], v[72:75], v[62:65]\n"
      "buffer_load_dwordx4 v16, s[32:35], s51 offen lds\n"
      "v_cvt_pk_bf16_f32 v60, v60, v61\n"
      "v_accvgpr_write_b32 a210, v58\n"
      "v_mfma_f32_16x16x32_bf16 v[24:27], a[32:35], v[76:79], 0\n"
      "s_waitcnt lgkmcnt(1)\n"
      "s_add_u32 m0, s46, 0\n"
      "v_accvgpr_write_b32 a211, v60\n"
      "v_cvt_pk_bf16_f32 v174, v20, v21\n"
      "v_mfma_f32_16x16x32_bf16 v[28:31], a[0:3], v[68:71], v[24:27]\n"
      "v_cvt_pk_bf16_f32 v178, v62, v63\n"
      "v_cvt_pk_bf16_f32 v179, v64, v65\n"
      "v_mfma_f32_16x16x32_bf16 v[24:27], a[0:3], v[72:75], v[24:27]\n"
      "buffer_load_dwordx4 v17, s[32:35], s51 offen offset:1024 lds\n"
      "v_cvt_pk_bf16_f32 v175, v22, v23\n"
      "v_add_u32 v84, 46144, v84\n"
      "v_mfma_f32_16x16x32_bf16 v[32:35], a[4:7], v[76:79], 0\n"
      "s_add_u32 m0, s46, 0\n"
      "v_cvt_pk_bf16_f32 v182, v28, v29\n"
      "v_cvt_pk_bf16_f32 v183, v30, v31\n"
      "v_mfma_f32_16x16x32_bf16 v[54:57], a[8:11], v[68:71], v[32:35]\n"
      "buffer_load_dwordx4 v18, s[32:35], s51 offen offset:2048 lds\n"
      "v_cvt_pk_bf16_f32 v24, v24, v25\n"
      "v_mfma_f32_16x16x32_bf16 v[32:35], a[8:11], v[72:75], v[32:35]\n"
      "s_waitcnt lgkmcnt(0)\n"
      "s_waitcnt vmcnt(12)\n"
      "s_cbranch_vccnz Lconv_h7\n"
      "s_barrier\n"
      "Lconv_h7:\n"
      "s_sub_u32 m0, s94, s59\n"
      "s_sub_u32 s96, s51, s95\n"
      "v_cvt_pk_bf16_f32 v26, v26, v27\n"
      "v_mfma_f32_16x16x32_bf16 v[58:61], a[4:7], v[80:83], 0\n"
      "v_accvgpr_write_b32 a230, v24\n"
      "v_accvgpr_write_b32 a231, v26\n"
      "v_mfma_f32_16x16x32_bf16 v[62:65], a[12:15], v[68:71], v[58:61]\n"
      "s_cbranch_vccz Lconv_h8\n"
      "s_barrier\n"
      "Lconv_h8:\n"
      "ds_read_b128 a[48:51], v2 offset:26560\n"
      "ds_read_b128 a[52:55], v3 offset:26560\n"
      "ds_read_b128 a[56:59], v4 offset:26560\n"
      "ds_read_b128 a[36:39], v2 offset:13280\n"
      "buffer_load_dwordx4 v19, s[32:35], s96 offen lds\n"
      "v_mfma_f32_16x16x32_bf16 v[58:61], a[12:15], v[72:75], v[58:61]\n"
      "s_mov_b32 m0, s18\n"
      "ds_read_b128 a[40:43], v3 offset:13280\n"
      "ds_read_b128 a[44:47], v4 offset:13280\n"
      "v_cvt_pk_bf16_f32 v186, v54, v55\n"
      "v_cvt_pk_bf16_f32 v187, v56, v57\n"
      "v_mfma_f32_16x16x32_bf16 v[20:23], a[16:19], v[76:79], 0\n"
      "ds_read_b128 a[24:27], v86 offset:30752\n"
      "ds_read_b128 a[28:31], v84 offset:27680\n"
      "v_cvt_pk_bf16_f32 v58, v58, v59\n"
      "v_accvgpr_write_b32 a238, v58\n"
      "v_mfma_f32_16x16x32_bf16 v[24:27], a[20:23], v[68:71], v[20:23]\n"
      "ds_read_b128 a[32:35], v85 offset:27680\n"
      "ds_read_b128 a[0:3], v86 offset:30816\n"
      "v_cvt_pk_bf16_f32 v194, v62, v63\n"
      "v_cvt_pk_bf16_f32 v60, v60, v61\n"
      "v_mfma_f32_16x16x32_bf16 v[20:23], a[20:23], v[72:75], v[20:23]\n"
      "s_waitcnt lgkmcnt(9)\n"
      "ds_read_b128 a[4:7], v84 offset:27744\n"
      "ds_read_b128 a[8:11], v86 offset:39968\n"
      "v_accvgpr_write_b32 a239, v60\n"
      "v_cvt_pk_bf16_f32 v190, v32, v33\n"
      "v_mfma_f32_16x16x32_bf16 v[52:55], a[48:51], a[72:75], 0\n"
      "s_waitcnt lgkmcnt(10)\n"
      "ds_read_b128 a[48:51], v2 offset:13344\n"
      "ds_read_b128 a[12:15], v84 offset:36896\n"
      "v_mfma_f32_16x16x32_bf16 v[56:59], a[52:55], a[76:79], 0\n"
      "s_waitcnt lgkmcnt(10)\n"
      "ds_read_b128 a[52:55], v3 offset:13344\n"
      "ds_read_b128 a[16:19], v85 offset:36896\n"
      "v_cvt_pk_bf16_f32 v191, v34, v35\n"
      "v_cvt_pk_bf16_f32 v195, v64, v65\n"
      "v_mfma_f32_16x16x32_bf16 v[60:63], a[56:59], a[80:83], v[52:55]\n"
      "ds_read_b128 a[56:59], v4 offset:13344\n"
      "ds_read_b128 a[20:23], v86 offset:40032\n"
      "v_fma_f32 v64, s44, v52, v56\n"
      "v_fma_f32 v65, s44, v53, v57\n"
      "v_mfma_f32_16x16x32_bf16 v[36:39], a[36:39], a[72:75], 0\n"
      "s_waitcnt lgkmcnt(10)\n"
      "ds_read_b128 a[36:39], v2 offset:26624\n"
      "v_fma_f32 v66, s44, v54, v58\n"
      "v_fma_f32 v67, s44, v55, v59\n"
      "v_fma_f32 v56, s44, v52, -v56\n"
      "v_mfma_f32_16x16x32_bf16 v[40:43], a[40:43], a[76:79], 0\n"
      "ds_read_b128 a[40:43], v3 offset:26624\n"
      "v_fma_f32 v57, s44, v53, -v57\n"
      "v_fma_f32 v58, s44, v54, -v58\n"
      "v_fma_f32 v59, s44, v55, -v59\n"
      "v_mfma_f32_16x16x32_bf16 v[44:47], a[44:47], a[80:83], v[36:39]\n"
      "ds_read_b128 a[44:47], v4 offset:26624\n"
      "v_cvt_pk_bf16_f32 v56, v56, v57\n"
      "v_cvt_pk_bf16_f32 v57, v58, v59\n"
      "v_cvt_pk_bf16_f32 v60, v60, v61\n"
      "v_mfma_f32_16x16x32_bf16 v[28:31], a[24:27], v[76:79], 0\n"
      "ds_write_addtid_b32 v56 offset:6144\n"
      "ds_write_addtid_b32 v57 offset:43072\n"
      "ds_write_addtid_b32 v60 offset:4608\n"
      "v_cvt_pk_bf16_f32 v61, v62, v63\n"
      "v_mfma_f32_16x16x32_bf16 v[32:35], a[28:31], v[68:71], v[28:31]\n"
      "ds_write_addtid_b32 v61 offset:41536\n"
      "v_cvt_pk_bf16_f32 v64, v64, v65\n"
      "v_cvt_pk_bf16_f32 v65, v66, v67\n"
      "v_cvt_pk_bf16_f32 v202, v20, v21\n"
      "v_mfma_f32_16x16x32_bf16 v[28:31], a[28:31], v[72:75], v[28:31]\n"
      "ds_write_addtid_b32 v64 offset:5376\n"
      "ds_write_addtid_b32 v65 offset:42304\n"
      "ds_read_b128 a[28:31], v86 offset:49184\n"
      "s_mov_b32 m0, s18\n"
      "v_cvt_pk_bf16_f32 v203, v22, v23\n"
      "v_mfma_f32_16x16x32_bf16 v[54:57], a[24:27], v[80:83], 0\n"
      "s_waitcnt lgkmcnt(9)\n"
      "ds_read_b128 a[24:27], v84 offset:36960\n"
      "v_fma_f32 v48, s44, v36, v40\n"
      "v_fma_f32 v49, s44, v37, v41\n"
      "v_fma_f32 v50, s44, v38, v42\n"
      "v_mfma_f32_16x16x32_bf16 v[58:61], a[32:35], v[68:71], v[54:57]\n"
      "v_fma_f32 v51, s44, v39, v43\n"
      "v_fma_f32 v40, s44, v36, -v40\n"
      "v_fma_f32 v41, s44, v37, -v41\n"
      "v_fma_f32 v42, s44, v38, -v42\n"
      "v_mfma_f32_16x16x32_bf16 v[54:57], a[32:35], v[72:75], v[54:57]\n"
      "ds_read_b128 a[32:35], v84 offset:46112\n"
      "v_fma_f32 v43, s44, v39, -v43\n"
      "v_cvt_pk_bf16_f32 v40, v40, v41\n"
      "v_cvt_pk_bf16_f32 v41, v42, v43\n"
      "v_mfma_f32_16x16x32_bf16 v[62:65], a[0:3], v[76:79], 0\n"
      "ds_write_addtid_b32 v40 offset:1536\n"
      "ds_write_addtid_b32 v41 offset:38464\n"
      "ds_read_b128 a[0:3], v85 offset:46112\n"
      "v_cvt_pk_bf16_f32 v44, v44, v45\n"
      "v_mfma_f32_16x16x32_bf16 v[20:23], a[4:7], v[68:71], v[62:65]\n"
      "ds_write_addtid_b32 v44 offset:0\n"
      "v_cvt_pk_bf16_f32 v45, v46, v47\n"
      "v_cvt_pk_bf16_f32 v212, v54, v55\n"
      "v_cvt_pk_bf16_f32 v208, v58, v59\n"
      "v_mfma_f32_16x16x32_bf16 v[62:65], a[4:7], v[72:75], v[62:65]\n"
      "ds_write_addtid_b32 v45 offset:36928\n"
      "ds_read_b128 a[4:7], v86 offset:49248\n"
      "v_cvt_pk_bf16_f32 v213, v56, v57\n"
      "v_cvt_pk_bf16_f32 v209, v60, v61\n"
      "v_mfma_f32_16x16x32_bf16 v[36:39], a[36:39], a[72:75], 0\n"
      "s_waitcnt lgkmcnt(7)\n"
      "ds_read_b128 a[36:39], v2 offset:13408\n"
      "v_cvt_pk_bf16_f32 v198, v24, v25\n"
      "v_cvt_pk_bf16_f32 v62, v62, v63\n"
      "v_accvgpr_write_b32 a224, v62\n"
      "v_mfma_f32_16x16x32_bf16 v[40:43], a[40:43], a[76:79], 0\n"
      "ds_read_b128 a[40:43], v3 offset:13408\n"
      "v_cvt_pk_bf16_f32 v199, v26, v27\n"
      "v_cvt_pk_bf16_f32 v48, v48, v49\n"
      "v_cvt_pk_bf16_f32 v49, v50, v51\n"
      "v_mfma_f32_16x16x32_bf16 v[44:47], a[44:47], a[80:83], v[36:39]\n"
      "ds_write_addtid_b32 v48 offset:768\n"
      "ds_write_addtid_b32 v49 offset:37696\n"
      "ds_read_b128 a[44:47], v4 offset:13408\n"
      "s_mov_b32 m0, s18\n"
      "v_cvt_pk_bf16_f32 v28, v28, v29\n"
      "v_mfma_f32_16x16x32_bf16 v[52:55], a[48:51], a[72:75], 0\n"
      "ds_read_b128 a[48:51], v2 offset:26688\n"
      "v_cvt_pk_bf16_f32 v30, v30, v31\n"
      "v_accvgpr_write_b32 a220, v28\n"
      "v_accvgpr_write_b32 a221, v30\n"
      "v_mfma_f32_16x16x32_bf16 v[56:59], a[52:55], a[76:79], 0\n"
      "ds_read_b128 a[52:55], v3 offset:26688\n"
      "v_fma_f32 v48, s44, v36, v40\n"
      "v_fma_f32 v49, s44, v37, v41\n"
      "v_fma_f32 v50, s44, v38, v42\n"
      "v_mfma_f32_16x16x32_bf16 v[60:63], a[56:59], a[80:83], v[52:55]\n"
      "ds_read_b128 a[56:59], v4 offset:26688\n"
      "v_fma_f32 v51, s44, v39, v43\n"
      "v_fma_f32 v40, s44, v36, -v40\n"
      "v_fma_f32 v41, s44, v37, -v41\n"
      "v_mfma_f32_16x16x32_bf16 v[24:27], a[8:11], v[76:79], 0\n"
      "v_fma_f32 v42, s44, v38, -v42\n"
      "v_fma_f32 v43, s44, v39, -v43\n"
      "v_cvt_pk_bf16_f32 v48, v48, v49\n"
      "v_cvt_pk_bf16_f32 v49, v50, v51\n"
      "v_mfma_f32_16x16x32_bf16 v[28:31], a[12:15], v[68:71], v[24:27]\n"
      "ds_write_addtid_b32 v48 offset:5632\n"
      "ds_write_addtid_b32 v49 offset:42560\n"
      "v_cvt_pk_bf16_f32 v40, v40, v41\n"
      "v_cvt_pk_bf16_f32 v41, v42, v43\n"
      "v_mfma_f32_16x16x32_bf16 v[24:27], a[12:15], v[72:75], v[24:27]\n"
      "ds_write_addtid_b32 v40 offset:6400\n"
      "ds_write_addtid_b32 v41 offset:43328\n"
      "ds_read_b128 a[12:15], v86 offset:58400\n"
      "v_cvt_pk_bf16_f32 v44, v44, v45\n"
      "v_cvt_pk_bf16_f32 v204, v32, v33\n"
      "v_cvt_pk_bf16_f32 v205, v34, v35\n"
      "v_mfma_f32_16x16x32_bf16 v[32:35], a[8:11], v[80:83], 0\n"
      "ds_write_addtid_b32 v44 offset:4864\n"
      "ds_read_b128 a[8:11], v84 offset:46176\n"
      "v_cvt_pk_bf16_f32 v45, v46, v47\n"
      "v_cvt_pk_bf16_f32 v64, v64, v65\n"
      "v_accvgpr_write_b32 a225, v64\n"
      "ds_write_addtid_b32 v45 offset:41792\n"
      "v_fma_f32 v64, s44, v52, v56\n"
      "v_fma_f32 v65, s44, v53, v57\n"
      "v_fma_f32 v66, s44, v54, v58\n"
      "v_fma_f32 v67, s44, v55, v59\n"
      "v_fma_f32 v56, s44, v52, -v56\n"
      "v_fma_f32 v57, s44, v53, -v57\n"
      "v_fma_f32 v58, s44, v54, -v58\n"
      "v_fma_f32 v59, s44, v55, -v59\n"
      "v_cvt_pk_bf16_f32 v56, v56, v57\n"
      "v_cvt_pk_bf16_f32 v57, v58, v59\n"
      "s_mov_b32 m0, s18\n"
      "s_nop 0\n"
      "ds_write_addtid_b32 v56 offset:1792\n"
      "ds_write_addtid_b32 v57 offset:38720\n"
      "v_mfma_f32_16x16x32_bf16 v[54:57], a[16:19], v[68:71], v[32:35]\n"
      "v_cvt_pk_bf16_f32 v60, v60, v61\n"
      "v_cvt_pk_bf16_f32 v61, v62, v63\n"
      "v_cvt_pk_bf16_f32 v64, v64, v65\n"
      "v_cvt_pk_bf16_f32 v65, v66, v67\n"
      "v_mfma_f32_16x16x32_bf16 v[32:35], a[16:19], v[72:75], v[32:35]\n"
      "ds_write_addtid_b32 v60 offset:256\n"
      "ds_write_addtid_b32 v61 offset:37184\n"
      "ds_write_addtid_b32 v64 offset:1024\n"
      "ds_write_addtid_b32 v65 offset:37952\n"
      "s_mov_b32 m0, s18\n"
      "v_mfma_f32_16x16x32_bf16 v[58:61], a[20:23], v[76:79], 0\n"
      "ds_read_b128 a[16:19], v84 offset:55328\n"
      "ds_read_b128 a[20:23], v85 offset:55328\n"
      "v_cvt_pk_bf16_f32 v228, v54, v55\n"
      "v_cvt_pk_bf16_f32 v229, v56, v57\n"
      "v_mfma_f32_16x16x32_bf16 v[62:65], a[24:27], v[68:71], v[58:61]\n"
      "v_cvt_pk_bf16_f32 v216, v20, v21\n"
      "v_cvt_pk_bf16_f32 v217, v22, v23\n"
      "v_cvt_pk_bf16_f32 v224, v24, v25\n"
      "v_cvt_pk_bf16_f32 v225, v26, v27\n"
      "v_mfma_f32_16x16x32_bf16 v[58:61], a[24:27], v[72:75], v[58:61]\n"
      "s_waitcnt lgkmcnt(11)\n"
      "ds_read_b128 a[24:27], v86 offset:58464\n"
      "v_cvt_pk_bf16_f32 v220, v28, v29\n"
      "v_cvt_pk_bf16_f32 v232, v62, v63\n"
      "v_cvt_pk_bf16_f32 v221, v30, v31\n"
      "v_mfma_f32_16x16x32_bf16 v[52:55], a[48:51], a[72:75], 0\n"
      "v_cvt_pk_bf16_f32 v32, v32, v33\n"
      "v_cvt_pk_bf16_f32 v34, v34, v35\n"
      "v_cvt_pk_bf16_f32 v236, v58, v59\n"
      "v_cvt_pk_bf16_f32 v237, v60, v61\n"
      "v_mfma_f32_16x16x32_bf16 v[56:59], a[52:55], a[76:79], 0\n"
      "v_accvgpr_write_b32 a232, v32\n"
      "v_accvgpr_write_b32 a233, v34\n"
      "v_cvt_pk_bf16_f32 v233, v64, v65\n"
      "v_add_u32 v87, 46144, v87\n"
      "v_mfma_f32_16x16x32_bf16 v[60:63], a[56:59], a[80:83], v[52:55]\n"
      "ds_read_b64_tr_b16 a[112:113], v87 offset:27680\n"
      "ds_read_b64_tr_b16 a[120:121], v87 offset:27744\n"
      "v_add_u32 v88, 46144, v88\n"
      "v_fma_f32 v64, s44, v52, v56\n"
      "v_mfma_f32_16x16x32_bf16 v[36:39], a[36:39], a[72:75], 0\n"
      "ds_read_b64_tr_b16 a[116:117], v88 offset:27680\n"
      "ds_read_b64_tr_b16 a[148:149], v87 offset:36896\n"
      "v_fma_f32 v65, s44, v53, v57\n"
      "v_fma_f32 v66, s44, v54, v58\n"
      "v_mfma_f32_16x16x32_bf16 v[40:43], a[40:43], a[76:79], 0\n"
      "ds_read_b64_tr_b16 a[152:153], v88 offset:36896\n"
      "ds_read_b64_tr_b16 a[156:157], v87 offset:36960\n"
      "v_fma_f32 v67, s44, v55, v59\n"
      "v_fma_f32 v56, s44, v52, -v56\n"
      "v_mfma_f32_16x16x32_bf16 v[44:47], a[44:47], a[80:83], v[36:39]\n"
      "ds_read_b64_tr_b16 a[184:185], v87 offset:46112\n"
      "ds_read_b64_tr_b16 a[188:189], v88 offset:46112\n"
      "v_fma_f32 v57, s44, v53, -v57\n"
      "v_fma_f32 v58, s44, v54, -v58\n"
      "v_mfma_f32_16x16x32_bf16 v[20:23], a[28:31], v[76:79], 0\n"
      "Lconv_p2:\n"
      "s_cbranch_execz Lconv_r0\n"
      "s_nop 0\n"
      "ds_read_b64_tr_b16 a[192:193], v87 offset:46176\n"
      "ds_read_b64_tr_b16 a[114:115], v87 offset:55328\n"
      "v_fma_f32 v59, s44, v55, -v59\n"
      "v_cvt_pk_bf16_f32 v56, v56, v57\n"
      "v_mfma_f32_16x16x32_bf16 v[24:27], a[32:35], v[68:71], v[20:23]\n"
      "ds_write_addtid_b32 v56 offset:6656\n"
      "ds_read_b64_tr_b16 a[118:119], v88 offset:55328\n"
      "v_cvt_pk_bf16_f32 v57, v58, v59\n"
      "v_cvt_pk_bf16_f32 v60, v60, v61\n"
      "v_mfma_f32_16x16x32_bf16 v[20:23], a[32:35], v[72:75], v[20:23]\n"
      "ds_write_addtid_b32 v57 offset:43584\n"
      "ds_write_addtid_b32 v60 offset:5120\n"
      "ds_read_b64_tr_b16 a[122:123], v87 offset:55392\n"
      "v_cvt_pk_bf16_f32 v61, v62, v63\n"
      "v_mfma_f32_16x16x32_bf16 v[28:31], a[28:31], v[80:83], 0\n"
      "ds_write_addtid_b32 v61 offset:42048\n"
      "ds_read_b128 a[28:31], v84 offset:55392\n"
      "v_cvt_pk_bf16_f32 v64, v64, v65\n"
      "v_cvt_pk_bf16_f32 v65, v66, v67\n"
      "v_mfma_f32_16x16x32_bf16 v[32:35], a[0:3], v[68:71], v[28:31]\n"
      "ds_write_addtid_b32 v64 offset:5888\n"
      "ds_write_addtid_b32 v65 offset:42816\n"
      "s_mov_b32 m0, s18\n"
      "v_fma_f32 v48, s44, v36, v40\n"
      "v_fma_f32 v49, s44, v37, v41\n"
      "v_mfma_f32_16x16x32_bf16 v[28:31], a[0:3], v[72:75], v[28:31]\n"
      "v_fma_f32 v50, s44, v38, v42\n"
      "v_fma_f32 v51, s44, v39, v43\n"
      "v_fma_f32 v40, s44, v36, -v40\n"
      "v_fma_f32 v41, s44, v37, -v41\n"
      "v_mfma_f32_16x16x32_bf16 v[54:57], a[4:7], v[76:79], 0\n"
      "s_waitcnt lgkmcnt(9)\n"
      "v_fma_f32 v42, s44, v38, -v42\n"
      "v_fma_f32 v43, s44, v39, -v43\n"
      "v_cvt_pk_bf16_f32 v48, v48, v49\n"
      "v_cvt_pk_bf16_f32 v49, v50, v51\n"
      "v_mfma_f32_16x16x32_bf16 v[58:61], a[8:11], v[68:71], v[54:57]\n"
      "ds_write_addtid_b32 v48 offset:1280\n"
      "ds_write_addtid_b32 v49 offset:38208\n"
      "v_cvt_pk_bf16_f32 v40, v40, v41\n"
      "v_cvt_pk_bf16_f32 v41, v42, v43\n"
      "v_mfma_f32_16x16x32_bf16 v[54:57], a[8:11], v[72:75], v[54:57]\n"
      "ds_write_addtid_b32 v40 offset:2048\n"
      "ds_write_addtid_b32 v41 offset:38976\n"
      "v_cvt_pk_bf16_f32 v44, v44, v45\n"
      "v_cvt_pk_bf16_f32 v45, v46, v47\n"
      "v_mfma_f32_16x16x32_bf16 v[62:65], a[12:15], v[76:79], 0\n"
      "ds_write_addtid_b32 v44 offset:512\n"
      "ds_write_addtid_b32 v45 offset:37440\n"
      "s_add_u32 m0, s46, 13280\n"
      "s_add_u32 s51, s51, s12\n"
      "v_cvt_pk_bf16_f32 v20, v20, v21\n"
      "v_cvt_pk_bf16_f32 v22, v22, v23\n"
      "v_accvgpr_write_b32 a240, v20\n"
      "v_accvgpr_write_b32 a241, v22\n"
      "v_mfma_f32_16x16x32_bf16 v[20:23], a[16:19], v[68:71], v[62:65]\n"
      "v_cvt_pk_bf16_f32 v240, v24, v25\n"
      "v_cvt_pk_bf16_f32 v241, v26, v27\n"
      "v_cvt_pk_bf16_f32 v248, v28, v29\n"
      "v_cvt_pk_bf16_f32 v249, v30, v31\n"
      "v_mfma_f32_16x16x32_bf16 v[62:65], a[16:19], v[72:75], v[62:65]\n"
      "s_waitcnt lgkmcnt(0)\n"
      "s_waitcnt vmcnt(7)\n"
      "s_cbranch_vccnz Lconv_h9\n"
      "s_barrier\n"
      "Lconv_h9:\n"
      "v_cvt_pk_bf16_f32 v244, v32, v33\n"
      "v_cvt_pk_bf16_f32 v245, v34, v35\n"
      "v_cvt_pk_bf16_f32 v54, v54, v55\n"
      "v_mfma_f32_16x16x32_bf16 v[24:27], a[12:15], v[80:83], 0\n"
      "v_cvt_pk_bf16_f32 v56, v56, v57\n"
      "v_accvgpr_write_b32 a244, v54\n"
      "v_accvgpr_write_b32 a245, v56\n"
      "v_mfma_f32_16x16x32_bf16 v[28:31], a[20:23], v[68:71], v[24:27]\n"
      "s_cbranch_vccz Lconv_h10\n"
      "s_barrier\n"
      "Lconv_h10:\n"
      "s_nop 0\n"
      "ds_read_b128 a[32:35], v86 offset:21536\n"
      "ds_read_b128 a[0:3], v84 offset:18464\n"
      "ds_read_b128 a[4:7], v85 offset:18464\n"
      "ds_read_b128 a[8:11], v86 offset:21600\n"
      "v_mfma_f32_16x16x32_bf16 v[24:27], a[20:23], v[72:75], v[24:27]\n"
      "ds_read_b128 a[12:15], v84 offset:18528\n"
      "ds_read_b128 a[16:19], v86 offset:3072\n"
      "v_cvt_pk_bf16_f32 v252, v58, v59\n"
      "v_cvt_pk_bf16_f32 v253, v60, v61\n"
      "v_mfma_f32_16x16x32_bf16 v[32:35], a[24:27], v[76:79], 0\n"
      "ds_read_b128 a[20:23], v84 offset:0\n"
      "ds_read_b128 a[24:27], v85 offset:0\n"
      "v_cvt_pk_bf16_f32 v62, v62, v63\n"
      "v_cvt_pk_bf16_f32 v64, v64, v65\n"
      "v_mfma_f32_16x16x32_bf16 v[54:57], a[28:31], v[68:71], v[32:35]\n"
      "ds_read_b64_tr_b16 a[150:151], v87 offset:18464\n"
      "ds_read_b64_tr_b16 a[154:155], v88 offset:18464\n"
      "v_accvgpr_write_b32 a222, v62\n"
      "v_accvgpr_write_b32 a223, v64\n"
      "v_mfma_f32_16x16x32_bf16 v[32:35], a[28:31], v[72:75], v[32:35]\n"
      "s_waitcnt lgkmcnt(9)\n"
      "ds_read_b64_tr_b16 a[158:159], v87 offset:18528\n"
      "ds_read_b64_tr_b16 a[186:187], v87 offset:0\n"
      "buffer_load_dwordx4 v16, s[32:35], s51 offen lds\n"
      "v_cvt_pk_bf16_f32 v206, v20, v21\n"
      "v_mfma_f32_16x16x32_bf16 v[58:61], a[32:35], v[76:79], 0\n"
      "s_waitcnt lgkmcnt(10)\n"
      "s_add_u32 m0, s46, 13280\n"
      "ds_read_b64_tr_b16 a[190:191], v88 offset:0\n"
      "ds_read_b128 a[28:31], v86 offset:3136\n"
      "v_cvt_pk_bf16_f32 v207, v22, v23\n"
      "v_cvt_pk_bf16_f32 v214, v24, v25\n"
      "v_mfma_f32_16x16x32_bf16 v[62:65], a[0:3], v[68:71], v[58:61]\n"
      "ds_read_b64_tr_b16 a[194:195], v87 offset:64\n"
      "buffer_load_dwordx4 v17, s[32:35], s51 offen offset:1024 lds\n"
      "v_cvt_pk_bf16_f32 v215, v26, v27\n"
      "v_cvt_pk_bf16_f32 v210, v28, v29\n"
      "v_mfma_f32_16x16x32_bf16 v[58:61], a[0:3], v[72:75], v[58:61]\n"
      "s_add_u32 m0, s46, 13280\n"
      "v_cvt_pk_bf16_f32 v211, v30, v31\n"
      "v_cvt_pk_bf16_f32 v32, v32, v33\n"
      "v_cvt_pk_bf16_f32 v34, v34, v35\n"
      "v_mfma_f32_16x16x32_bf16 v[20:23], a[32:35], v[80:83], 0\n"
      "s_waitcnt lgkmcnt(7)\n"
      "ds_read_b128 a[32:35], v84 offset:64\n"
      "v_accvgpr_write_b32 a226, v32\n"
      "v_mfma_f32_16x16x32_bf16 v[24:27], a[4:7], v[68:71], v[20:23]\n"
      "buffer_load_dwordx4 v18, s[32:35], s51 offen offset:2048 lds\n"
      "v_accvgpr_write_b32 a227, v34\n"
      "v_mfma_f32_16x16x32_bf16 v[20:23], a[4:7], v[72:75], v[20:23]\n"
      "s_add_u32 m0, s46, 26560\n"
      "s_add_u32 s51, s51, s12\n"
      "v_cvt_pk_bf16_f32 v218, v54, v55\n"
      "v_cvt_pk_bf16_f32 v219, v56, v57\n"
      "v_cvt_pk_bf16_f32 v226, v58, v59\n"
      "v_cvt_pk_bf16_f32 v227, v60, v61\n"
      "v_mfma_f32_16x16x32_bf16 v[28:31], a[8:11], v[76:79], 0\n"
      "v_cvt_pk_bf16_f32 v222, v62, v63\n"
      "v_cvt_pk_bf16_f32 v223, v64, v65\n"
      "v_mfma_f32_16x16x32_bf16 v[32:35], a[12:15], v[68:71], v[28:31]\n"
      "buffer_load_dwordx4 v16, s[32:35], s51 offen lds\n"
      "v_cvt_pk_bf16_f32 v20, v20, v21\n"
      "v_mfma_f32_16x16x32_bf16 v[28:31], a[12:15], v[72:75], v[28:31]\n"
      "s_add_u32 m0, s46, 26560\n"
      "ds_read_b128 a[12:15], v6 offset:0\n"
      "v_cvt_pk_bf16_f32 v22, v22, v23\n"
      "v_mfma_f32_16x16x32_bf16 v[54:57], a[16:19], v[76:79], 0\n"
      "v_accvgpr_write_b32 a234, v20\n"
      "v_accvgpr_write_b32 a235, v22\n"
      "v_mfma_f32_16x16x32_bf16 v[58:61], a[20:23], v[68:71], v[54:57]\n"
      "buffer_load_dwordx4 v17, s[32:35], s51 offen offset:1024 lds\n"
      "v_cvt_pk_bf16_f32 v230, v24, v25\n"
      "v_mfma_f32_16x16x32_bf16 v[54:57], a[20:23], v[72:75], v[54:57]\n"
      "s_waitcnt lgkmcnt(0)\n"
      "s_waitcnt vmcnt(5)\n"
      "s_cbranch_vccnz Lconv_h11\n"
      "s_barrier\n"
      "Lconv_h11:\n"
      "s_add_u32 m0, s46, 26560\n"
      "s_sub_u32 s96, s51, s95\n"
      "ds_read_b128 a[20:23], v6 offset:512\n"
      "buffer_load_dwordx4 v18, s[32:35], s51 offen offset:2048 lds\n"
      "v_cvt_pk_bf16_f32 v231, v26, v27\n"
      "v_mfma_f32_16x16x32_bf16 v[62:65], a[16:19], v[80:83], 0\n"
      "s_mov_b32 m0, s59\n"
      "ds_read_b128 a[16:19], v6 offset:256\n"
      "buffer_load_dwordx4 v19, s[32:35], s96 offen lds\n"
      "v_cvt_pk_bf16_f32 v238, v28, v29\n"
      "v_cvt_pk_bf16_f32 v239, v30, v31\n"
      "v_mfma_f32_16x16x32_bf16 v[20:23], a[24:27], v[68:71], v[62:65]\n"
      "s_cbranch_vccz Lconv_h12\n"
      "s_barrier\n"
      "Lconv_h12:\n"
      "v_cvt_pk_bf16_f32 v234, v32, v33\n"
      "v_cvt_pk_bf16_f32 v235, v34, v35\n"
      "v_mfma_f32_16x16x32_bf16 v[62:65], a[24:27], v[72:75], v[62:65]\n"
      "ds_read_b128 a[24:27], v6 offset:4608\n"
      "v_cvt_pk_bf16_f32 v242, v58, v59\n"
      "v_cvt_pk_bf16_f32 v243, v60, v61\n"
      "v_mfma_f32_16x16x32_bf16 v[24:27], a[28:31], v[76:79], 0\n"
      "ds_read_b128 a[28:31], v6 offset:4864\n"
      "v_cvt_pk_bf16_f32 v54, v54, v55\n"
      "v_mfma_f32_16x16x32_bf16 v[28:31], a[32:35], v[68:71], v[24:27]\n"
      "v_cvt_pk_bf16_f32 v56, v56, v57\n"
      "v_accvgpr_write_b32 a242, v54\n"
      "v_mfma_f32_16x16x32_bf16 v[24:27], a[32:35], v[72:75], v[24:27]\n"
      "ds_read_b128 a[32:35], v6 offset:5120\n"
      "v_accvgpr_write_b32 a243, v56\n"
      "v_cvt_pk_bf16_f32 v246, v20, v21\n"
      "v_cvt_pk_bf16_f32 v247, v22, v23\n"
      "v_cvt_pk_bf16_f32 v250, v62, v63\n"
      "v_cvt_pk_bf16_f32 v251, v64, v65\n"
      "v_cvt_pk_bf16_f32 v254, v28, v29\n"
      "v_cvt_pk_bf16_f32 v255, v30, v31\n"
      "v_cvt_pk_bf16_f32 v24, v24, v25\n"
      "v_cvt_pk_bf16_f32 v26, v26, v27\n"
      "v_accvgpr_write_b32 a246, v24\n"
      "v_accvgpr_write_b32 a247, v26\n"
      "s_mov_b32 m0, s18\n"
      "ds_read_b128 a[48:51], v2 offset:39840\n"
      "v_lshlrev_b32 v9, 7, v5\n"
      "v_mfma_f32_16x16x32_bf16 v[68:71], a[12:15], a[88:91], 0\n"
      "s_waitcnt lgkmcnt(4)\n"
      "ds_read_b128 a[52:55], v3 offset:39840\n"
      "v_min_u32 v9, Lconv_u0-Lconv_t0, v9\n"
      "v_mfma_f32_16x16x32_bf16 v[68:71], a[16:19], a[100:103], v[68:71]\n"
      "s_mul_i32 s97, s41, Lconv_t1-Lconv_t0\n"
      "ds_read_b128 a[56:59], v4 offset:39840\n"
      "v_add_u32 v9, Lconv_t0-Lconv_start, v9\n"
      "v_mfma_f32_16x16x32_bf16 v[68:71], a[20:23], a[112:115], v[68:71]\n"
      "s_waitcnt lgkmcnt(5)\n"
      "ds_read_b128 a[36:39], v2 offset:39904\n"
      "v_add_u32 v9, s97, v9\n"
      "v_mfma_f32_16x16x32_bf16 v[68:71], a[24:27], a[124:127], v[68:71]\n"
      "s_waitcnt lgkmcnt(5)\n"
      "ds_read_b128 a[40:43], v3 offset:39904\n"
      "v_mfma_f32_16x16x32_bf16 v[68:71], a[28:31], a[136:139], v[68:71]\n"
      "s_waitcnt lgkmcnt(4)\n"
      "ds_read_b128 a[44:47], v4 offset:39904\n"
      "v_mfma_f32_16x16x32_bf16 v[52:55], a[48:51], a[72:75], 0\n"
      "ds_read_b128 a[0:3], v6 offset:768\n"
      "v_mfma_f32_16x16x32_bf16 v[68:71], a[32:35], a[148:151], v[68:71]\n"
      "ds_read_b128 a[48:51], v2 offset:39968\n"
      "v_mfma_f32_16x16x32_bf16 v[72:75], a[12:15], a[92:95], 0\n"
      "s_waitcnt lgkmcnt(6)\n"
      "ds_read_b128 a[4:7], v6 offset:1024\n"
      "v_mfma_f32_16x16x32_bf16 v[56:59], a[52:55], a[76:79], 0\n"
      "ds_read_b128 a[8:11], v6 offset:1280\n"
      "s_nop 0\n"
      "v_mfma_f32_16x16x32_bf16 v[72:75], a[16:19], a[104:107], v[72:75]\n"
      "ds_read_b128 a[52:55], v3 offset:39968\n"
      "s_nop 0\n"
      "v_mfma_f32_16x16x32_bf16 v[72:75], a[20:23], a[116:119], v[72:75]\n"
      "s_waitcnt lgkmcnt(7)\n"
      "ds_read_b128 a[60:63], v6 offset:1536\n"
      "v_fma_f32 v64, s44, v52, v56\n"
      "v_mfma_f32_16x16x32_bf16 v[60:63], a[56:59], a[80:83], v[52:55]\n"
      "ds_read_b128 a[56:59], v4 offset:39968\n"
      "v_fma_f32 v65, s44, v53, v57\n"
      "v_mfma_f32_16x16x32_bf16 v[72:75], a[24:27], a[128:131], v[72:75]\n"
      "v_cvt_pk_bf16_f32 v64, v64, v65\n"
      "v_fma_f32 v66, s44, v54, v58\n"
      "v_mfma_f32_16x16x32_bf16 v[72:75], a[28:31], a[140:143], v[72:75]\n"
      "ds_write_addtid_b32 v64 offset:9984\n"
      "v_fma_f32 v67, s44, v55, v59\n"
      "v_mfma_f32_16x16x32_bf16 v[72:75], a[32:35], a[152:155], v[72:75]\n"
      "v_cvt_pk_bf16_f32 v65, v66, v67\n"
      "v_fma_f32 v56, s44, v52, -v56\n"
      "v_mfma_f32_16x16x32_bf16 v[36:39], a[36:39], a[72:75], 0\n"
      "ds_write_addtid_b32 v65 offset:46912\n"
      "v_fma_f32 v57, s44, v53, -v57\n"
      "v_mfma_f32_16x16x32_bf16 v[76:79], a[12:15], a[96:99], 0\n"
      "ds_read_b128 a[12:15], v6 offset:5376\n"
      "v_cvt_pk_bf16_f32 v56, v56, v57\n"
      "v_mfma_f32_16x16x32_bf16 v[76:79], a[16:19], a[108:111], v[76:79]\n"
      "s_waitcnt lgkmcnt(5)\n"
      "ds_read_b128 a[16:19], v6 offset:5632\n"
      "v_fma_f32 v58, s44, v54, -v58\n"
      "v_mfma_f32_16x16x32_bf16 v[40:43], a[40:43], a[76:79], 0\n"
      "ds_write_addtid_b32 v56 offset:10752\n"
      "v_fma_f32 v59, s44, v55, -v59\n"
      "v_mfma_f32_16x16x32_bf16 v[76:79], a[20:23], a[120:123], v[76:79]\n"
      "ds_read_b128 a[20:23], v6 offset:5888\n"
      "v_cvt_pk_bf16_f32 v57, v58, v59\n"
      "v_mfma_f32_16x16x32_bf16 v[76:79], a[24:27], a[132:135], v[76:79]\n"
      "ds_write_addtid_b32 v57 offset:47680\n"
      "v_cvt_pk_bf16_f32 v60, v60, v61\n"
      "v_mfma_f32_16x16x32_bf16 v[44:47], a[44:47], a[80:83], v[36:39]\n"
      "ds_write_addtid_b32 v60 offset:9216\n"
      "v_cvt_pk_bf16_f32 v61, v62, v63\n"
      "v_mfma_f32_16x16x32_bf16 v[76:79], a[28:31], a[144:147], v[76:79]\n"
      "ds_write_addtid_b32 v61 offset:46144\n"
      "v_fma_f32 v48, s44, v36, v40\n"
      "v_mfma_f32_16x16x32_bf16 v[76:79], a[32:35], a[156:159], v[76:79]\n"
      "ds_read_b128 a[36:39], v2 offset:0\n"
      "v_fma_f32 v49, s44, v37, v41\n"
      "v_mfma_f32_16x16x32_bf16 v[80:83], a[0:3], v[92:95], 0\n"
      "v_cvt_pk_bf16_f32 v48, v48, v49\n"
      "v_fma_f32 v50, s44, v38, v42\n"
      "v_mfma_f32_16x16x32_bf16 v[52:55], a[48:51], a[72:75], 0\n"
      "ds_write_addtid_b32 v48 offset:10240\n"
      "v_fma_f32 v51, s44, v39, v43\n"
      "v_mfma_f32_16x16x32_bf16 v[80:83], a[4:7], v[148:151], v[80:83]\n"
      "ds_read_b128 a[40:43], v3 offset:0\n"
      "v_cvt_pk_bf16_f32 v49, v50, v51\n"
      "v_mfma_f32_16x16x32_bf16 v[80:83], a[8:11], v[204:207], v[80:83]\n"
      "ds_write_addtid_b32 v49 offset:47168\n"
      "v_fma_f32 v40, s44, v36, -v40\n"
      "v_mfma_f32_16x16x32_bf16 v[56:59], a[52:55], a[76:79], 0\n"
      "s_waitcnt lgkmcnt(7)\n"
      "v_fma_f32 v41, s44, v37, -v41\n"
      "v_cvt_pk_bf16_f32 v40, v40, v41\n"
      "v_mfma_f32_16x16x32_bf16 v[80:83], a[12:15], v[112:115], v[80:83]\n"
      "ds_write_addtid_b32 v40 offset:11008\n"
      "v_fma_f32 v42, s44, v38, -v42\n"
      "v_mfma_f32_16x16x32_bf16 v[80:83], a[16:19], a[208:211], v[80:83]\n"
      "v_fma_f32 v43, s44, v39, -v43\n"
      "v_cvt_pk_bf16_f32 v41, v42, v43\n"
      "v_mfma_f32_16x16x32_bf16 v[60:63], a[56:59], a[80:83], v[52:55]\n"
      "ds_write_addtid_b32 v41 offset:47936\n"
      "v_cvt_pk_bf16_f32 v44, v44, v45\n"
      "v_mfma_f32_16x16x32_bf16 v[80:83], a[20:23], v[220:223], v[80:83]\n"
      "ds_write_addtid_b32 v44 offset:9472\n"
      "v_cvt_pk_bf16_f32 v45, v46, v47\n"
      "v_mfma_f32_16x16x32_bf16 v[84:87], a[0:3], v[100:103], 0\n"
      "ds_read_b128 a[44:47], v4 offset:0\n"
      "v_fma_f32 v64, s44, v52, v56\n"
      "v_mfma_f32_16x16x32_bf16 v[84:87], a[4:7], a[200:203], v[84:87]\n"
      "ds_write_addtid_b32 v45 offset:46400\n"
      "v_fma_f32 v65, s44, v53, v57\n"
      "v_mfma_f32_16x16x32_bf16 v[84:87], a[8:11], v[208:211], v[84:87]\n"
      "ds_read_b128 a[64:67], v6 offset:1792\n"
      "v_cvt_pk_bf16_f32 v64, v64, v65\n"
      "v_mfma_f32_16x16x32_bf16 v[84:87], a[12:15], a[204:207], v[84:87]\n"
      "ds_read_b128 a[68:71], v6 offset:2048\n"
      "v_fma_f32 v66, s44, v54, v58\n"
      "v_mfma_f32_16x16x32_bf16 v[84:87], a[16:19], v[172:175], v[84:87]\n"
      "ds_read_b128 a[24:27], v6 offset:6144\n"
      "v_fma_f32 v67, s44, v55, v59\n"
      "v_mfma_f32_16x16x32_bf16 v[84:87], a[20:23], v[228:231], v[84:87]\n"
      "ds_read_b128 a[48:51], v2 offset:64\n"
      "v_cvt_pk_bf16_f32 v65, v66, v67\n"
      "v_mfma_f32_16x16x32_bf16 v[24:27], a[0:3], a[196:199], 0\n"
      "ds_write_addtid_b32 v64 offset:10496\n"
      "v_fma_f32 v56, s44, v52, -v56\n"
      "v_mfma_f32_16x16x32_bf16 v[24:27], a[4:7], v[160:163], v[24:27]\n"
      "s_waitcnt lgkmcnt(11)\n"
      "ds_read_b128 a[28:31], v6 offset:6400\n"
      "v_fma_f32 v57, s44, v53, -v57\n"
      "v_mfma_f32_16x16x32_bf16 v[36:39], a[36:39], a[72:75], 0\n"
      "ds_write_addtid_b32 v65 offset:47424\n"
      "v_cvt_pk_bf16_f32 v56, v56, v57\n"
      "v_mfma_f32_16x16x32_bf16 v[24:27], a[8:11], v[216:219], v[24:27]\n"
      "ds_read_b128 a[32:35], v6 offset:6656\n"
      "v_fma_f32 v58, s44, v54, -v58\n"
      "v_mfma_f32_16x16x32_bf16 v[24:27], a[12:15], v[124:127], v[24:27]\n"
      "ds_write_addtid_b32 v56 offset:11264\n"
      "v_fma_f32 v59, s44, v55, -v59\n"
      "v_mfma_f32_16x16x32_bf16 v[40:43], a[40:43], a[76:79], 0\n"
      "ds_read_b128 a[52:55], v3 offset:64\n"
      "v_cvt_pk_bf16_f32 v57, v58, v59\n"
      "v_mfma_f32_16x16x32_bf16 v[24:27], a[16:19], v[180:183], v[24:27]\n"
      "ds_write_addtid_b32 v57 offset:48192\n"
      "v_cvt_pk_bf16_f32 v60, v60, v61\n"
      "v_mfma_f32_16x16x32_bf16 v[24:27], a[20:23], v[232:235], v[24:27]\n"
      "s_waitcnt lgkmcnt(7)\n"
      "ds_write_addtid_b32 v60 offset:9728\n"
      "v_cvt_pk_bf16_f32 v61, v62, v63\n"
      "v_mfma_f32_16x16x32_bf16 v[44:47], a[44:47], a[80:83], v[36:39]\n"
      "ds_write_addtid_b32 v61 offset:46656\n"
      "v_fma_f32 v48, s44, v36, v40\n"
      "v_mfma_f32_16x16x32_bf16 v[28:31], a[60:63], v[96:99], 0\n"
      "ds_read_b128 a[56:59], v4 offset:64\n"
      "v_fma_f32 v49, s44, v37, v41\n"
      "v_mfma_f32_16x16x32_bf16 v[28:31], a[64:67], v[152:155], v[28:31]\n"
      "ds_read_b128 a[36:39], v2 offset:128\n"
      "v_cvt_pk_bf16_f32 v48, v48, v49\n"
      "v_mfma_f32_16x16x32_bf16 v[28:31], a[68:71], a[220:223], v[28:31]\n"
      "ds_read_b128 a[40:43], v3 offset:128\n"
      "v_fma_f32 v50, s44, v38, v42\n"
      "v_mfma_f32_16x16x32_bf16 v[28:31], a[24:27], v[116:119], v[28:31]\n"
      "s_waitcnt lgkmcnt(0)\n"
      "s_cbranch_vccnz Lconv_h13\n"
      "s_barrier\n"
      "Lconv_h13:\n"
      "v_fma_f32 v51, s44, v39, v43\n"
      "v_mfma_f32_16x16x32_bf16 v[52:55], a[48:51], a[72:75], 0\n"
      "ds_read_b128 a[44:47], v4 offset:128\n"
      "v_cvt_pk_bf16_f32 v49, v50, v51\n"
      "v_mfma_f32_16x16x32_bf16 v[28:31], a[28:31], v[168:171], v[28:31]\n"
      "ds_write_addtid_b32 v48 offset:14592\n"
      "v_fma_f32 v40, s44, v36, -v40\n"
      "v_mfma_f32_16x16x32_bf16 v[28:31], a[32:35], v[224:227], v[28:31]\n"
      "ds_write_addtid_b32 v49 offset:51520\n"
      "v_fma_f32 v41, s44, v37, -v41\n"
      "v_mfma_f32_16x16x32_bf16 v[56:59], a[52:55], a[76:79], 0\n"
      "s_cbranch_vccz Lconv_h14\n"
      "s_barrier\n"
      "Lconv_h14:\n"
      "v_cvt_pk_bf16_f32 v40, v40, v41\n"
      "v_mfma_f32_16x16x32_bf16 v[20:23], a[60:63], v[104:107], 0\n"
      "s_mov_b64 vcc, 0x2000000\n"
      "ds_write_addtid_b32 v40 offset:15360\n"
      "v_fma_f32 v42, s44, v38, -v42\n"
      "v_mfma_f32_16x16x32_bf16 v[20:23], a[64:67], v[156:159], v[20:23]\n"
      "ds_read_b128 a[0:3], v7 offset:0\n"
      "v_fma_f32 v43, s44, v39, -v43\n"
      "v_mfma_f32_16x16x32_bf16 v[60:63], a[56:59], a[80:83], v[52:55]\n"
      "v_cvt_pk_bf16_f32 v41, v42, v43\n"
      "v_cvt_pk_bf16_f32 v44, v44, v45\n"
      "v_mfma_f32_16x16x32_bf16 v[20:23], a[68:71], v[212:215], v[20:23]\n"
      "ds_write_addtid_b32 v41 offset:52288\n"
      "v_cvt_pk_bf16_f32 v45, v46, v47\n"
      "v_mfma_f32_16x16x32_bf16 v[20:23], a[24:27], v[120:123], v[20:23]\n"
      "ds_write_addtid_b32 v44 offset:13824\n"
      "ds_write_addtid_b32 v45 offset:50752\n"
      "v_mfma_f32_16x16x32_bf16 v[20:23], a[28:31], v[176:179], v[20:23]\n"
      "ds_read_b128 a[4:7], v7 offset:256\n"
      "v_fma_f32 v64, s44, v52, v56\n"
      "v_mfma_f32_16x16x32_bf16 v[36:39], a[36:39], a[72:75], 0\n"
      "ds_read_b128 a[8:11], v7 offset:512\n"
      "v_fma_f32 v65, s44, v53, v57\n"
      "v_mfma_f32_16x16x32_bf16 v[20:23], a[32:35], a[232:235], v[20:23]\n"
      "v_fma_f32 v66, s44, v54, v58\n"
      "v_fma_f32 v67, s44, v55, v59\n"
      "v_mfma_f32_16x16x32_bf16 v[32:35], a[60:63], v[108:111], 0\n"
      "v_fma_f32 v56, s44, v52, -v56\n"
      "v_fma_f32 v57, s44, v53, -v57\n"
      "v_mfma_f32_16x16x32_bf16 v[40:43], a[40:43], a[76:79], 0\n"
      "v_fma_f32 v58, s44, v54, -v58\n"
      "v_fma_f32 v59, s44, v55, -v59\n"
      "v_mfma_f32_16x16x32_bf16 v[32:35], a[64:67], v[164:167], v[32:35]\n"
      "v_cvt_pk_bf16_f32 v64, v64, v65\n"
      "v_cvt_pk_bf16_f32 v65, v66, v67\n"
      "v_mfma_f32_16x16x32_bf16 v[32:35], a[68:71], a[224:227], v[32:35]\n"
      "s_waitcnt lgkmcnt(5)\n"
      "v_cvt_pk_bf16_f32 v56, v56, v57\n"
      "v_cvt_pk_bf16_f32 v57, v58, v59\n"
      "v_mfma_f32_16x16x32_bf16 v[44:47], a[44:47], a[80:83], v[36:39]\n"
      "ds_write_addtid_b32 v64 offset:14848\n"
      "v_cvt_pk_bf16_f32 v60, v60, v61\n"
      "v_mfma_f32_16x16x32_bf16 v[32:35], a[24:27], v[128:131], v[32:35]\n"
      "ds_write_addtid_b32 v65 offset:51776\n"
      "v_cvt_pk_bf16_f32 v61, v62, v63\n"
      "v_mfma_f32_16x16x32_bf16 v[32:35], a[28:31], a[228:231], v[32:35]\n"
      "ds_write_addtid_b32 v56 offset:15616\n"
      "ds_write_addtid_b32 v57 offset:52544\n"
      "v_mfma_f32_16x16x32_bf16 v[32:35], a[32:35], v[236:239], v[32:35]\n"
      "ds_write_addtid_b32 v60 offset:14080\n"
      "ds_write_addtid_b32 v61 offset:51008\n"
      "v_mfma_f32_16x16x32_bf16 v[68:71], a[0:3], a[160:163], v[68:71]\n"
      "s_waitcnt lgkmcnt(6)\n"
      "ds_read_b128 a[12:15], v7 offset:768\n"
      "v_fma_f32 v48, s44, v36, v40\n"
      "v_mfma_f32_16x16x32_bf16 v[68:71], a[4:7], a[172:175], v[68:71]\n"
      "s_add_u32 m0, s46, 39840\n"
      "ds_read_b128 a[16:19], v7 offset:1024\n"
      "v_fma_f32 v49, s44, v37, v41\n"
      "v_mfma_f32_16x16x32_bf16 v[68:71], a[8:11], a[184:187], v[68:71]\n"
      "s_add_u32 s51, s51, s12\n"
      "ds_read_b128 a[20:23], v7 offset:1280\n"
      "v_cvt_pk_bf16_f32 v48, v48, v49\n"
      "v_mfma_f32_16x16x32_bf16 v[72:75], a[0:3], a[164:167], v[72:75]\n"
      "buffer_load_dwordx4 v16, s[32:35], s51 offen lds\n"
      "v_fma_f32 v50, s44, v38, v42\n"
      "v_mfma_f32_16x16x32_bf16 v[72:75], a[4:7], a[176:179], v[72:75]\n"
      "v_fma_f32 v51, s44, v39, v43\n"
      "v_cvt_pk_bf16_f32 v49, v50, v51\n"
      "v_mfma_f32_16x16x32_bf16 v[72:75], a[8:11], a[188:191], v[72:75]\n"
      "v_fma_f32 v40, s44, v36, -v40\n"
      "v_fma_f32 v41, s44, v37, -v41\n"
      "v_mfma_f32_16x16x32_bf16 v[76:79], a[0:3], a[168:171], v[76:79]\n"
      "buffer_load_dwordx4 v17, s[32:35], s51 offen offset:1024 lds\n"
      "v_cvt_pk_bf16_f32 v40, v40, v41\n"
      "v_mfma_f32_16x16x32_bf16 v[76:79], a[4:7], a[180:183], v[76:79]\n"
      "ds_read_b128 a[0:3], v7 offset:1536\n"
      "v_fma_f32 v42, s44, v38, -v42\n"
      "v_mfma_f32_16x16x32_bf16 v[76:79], a[8:11], a[192:195], v[76:79]\n"
      "s_waitcnt lgkmcnt(3)\n"
      "ds_read_b128 a[4:7], v7 offset:1792\n"
      "v_fma_f32 v43, s44, v39, -v43\n"
      "v_mfma_f32_16x16x32_bf16 v[80:83], a[12:15], a[212:215], v[80:83]\n"
      "s_waitcnt lgkmcnt(3)\n"
      "ds_read_b128 a[8:11], v7 offset:2048\n"
      "v_cvt_pk_bf16_f32 v41, v42, v43\n"
      "v_mfma_f32_16x16x32_bf16 v[80:83], a[16:19], v[184:187], v[80:83]\n"
      "s_waitcnt lgkmcnt(3)\n"
      "v_cvt_pk_bf16_f32 v44, v44, v45\n"
      "v_cvt_pk_bf16_f32 v45, v46, v47\n"
      "v_mfma_f32_16x16x32_bf16 v[80:83], a[20:23], v[240:243], v[80:83]\n"
      "buffer_load_dwordx4 v18, s[32:35], s51 offen offset:2048 lds\n"
      "v_cvt_pk_bf16_f32 v68, v68, v69\n"
      "v_mfma_f32_16x16x32_bf16 v[84:87], a[12:15], v[136:139], v[84:87]\n"
      "s_mov_b32 m0, s18\n"
      "v_cvt_pk_bf16_f32 v69, v70, v71\n"
      "v_cvt_pk_bf16_f32 v72, v72, v73\n"
      "v_mfma_f32_16x16x32_bf16 v[84:87], a[16:19], v[192:195], v[84:87]\n"
      "ds_write_addtid_b32 v48 offset:15104\n"
      "ds_write_addtid_b32 v49 offset:52032\n"
      "v_mfma_f32_16x16x32_bf16 v[84:87], a[20:23], v[244:247], v[84:87]\n"
      "ds_write_addtid_b32 v40 offset:15872\n"
      "ds_write_addtid_b32 v41 offset:52800\n"
      "v_mfma_f32_16x16x32_bf16 v[24:27], a[12:15], a[216:219], v[24:27]\n"
      "ds_write_addtid_b32 v44 offset:14336\n"
      "ds_write_addtid_b32 v45 offset:51264\n"
      "v_mfma_f32_16x16x32_bf16 v[24:27], a[16:19], v[196:199], v[24:27]\n"
      "s_mov_b32 m0, s45\n"
      "v_cvt_pk_bf16_f32 v73, v74, v75\n"
      "v_cvt_pk_bf16_f32 v76, v76, v77\n"
      "v_mfma_f32_16x16x32_bf16 v[24:27], a[20:23], v[252:255], v[24:27]\n"
      "s_waitcnt lgkmcnt(6)\n"
      "ds_write_addtid_b32 v68 offset:57360\n"
      "ds_write_addtid_b32 v69 offset:57616\n"
      "v_mfma_f32_16x16x32_bf16 v[28:31], a[0:3], v[132:135], v[28:31]\n"
      "ds_write_addtid_b32 v72 offset:45064\n"
      "ds_write_addtid_b32 v73 offset:45320\n"
      "v_mfma_f32_16x16x32_bf16 v[28:31], a[4:7], v[188:191], v[28:31]\n"
      "ds_write_addtid_b32 v76 offset:32768\n"
      "v_cvt_pk_bf16_f32 v77, v78, v79\n"
      "v_mfma_f32_16x16x32_bf16 v[28:31], a[8:11], a[240:243], v[28:31]\n"
      "ds_write_addtid_b32 v77 offset:33024\n"
      "v_cvt_pk_bf16_f32 v80, v80, v81\n"
      "v_mfma_f32_16x16x32_bf16 v[20:23], a[0:3], v[140:143], v[20:23]\n"
      "ds_write_addtid_b32 v80 offset:58384\n"
      "v_cvt_pk_bf16_f32 v81, v82, v83\n"
      "v_mfma_f32_16x16x32_bf16 v[20:23], a[4:7], a[236:239], v[20:23]\n"
      "ds_write_addtid_b32 v81 offset:58640\n"
      "v_cvt_pk_bf16_f32 v84, v84, v85\n"
      "v_mfma_f32_16x16x32_bf16 v[20:23], a[8:11], v[248:251], v[20:23]\n"
      "ds_write_addtid_b32 v84 offset:46088\n"
      "v_cvt_pk_bf16_f32 v85, v86, v87\n"
      "v_mfma_f32_16x16x32_bf16 v[32:35], a[0:3], v[144:147], v[32:35]\n"
      "ds_write_addtid_b32 v85 offset:46344\n"
      "v_cvt_pk_bf16_f32 v24, v24, v25\n"
      "v_mfma_f32_16x16x32_bf16 v[32:35], a[4:7], v[200:203], v[32:35]\n"
      "ds_write_addtid_b32 v24 offset:33792\n"
      "v_cvt_pk_bf16_f32 v25, v26, v27\n"
      "v_mfma_f32_16x16x32_bf16 v[32:35], a[8:11], a[244:247], v[32:35]\n"
      "ds_write_addtid_b32 v25 offset:34048\n"
      "v_cvt_pk_bf16_f32 v28, v28, v29\n"
      "v_cvt_pk_bf16_f32 v29, v30, v31\n"
      "ds_write_addtid_b32 v28 offset:59408\n"
      "ds_write_addtid_b32 v29 offset:59664\n"
      "v_cvt_pk_bf16_f32 v20, v20, v21\n"
      "v_cvt_pk_bf16_f32 v21, v22, v23\n"
      "ds_write_addtid_b32 v20 offset:47112\n"
      "ds_write_addtid_b32 v21 offset:47368\n"
      "s_waitcnt vmcnt(3)\n"
      "s_waitcnt lgkmcnt(0)\n"
      "s_cmp_eq_u32 s41, 1\n"
      "s_cbranch_scc1 Lconv_s1\n"
      "Lconv_s0:\n"
      "s_branch Lconv_loop0\n"
      "Lconv_s1:\n"
      "s_branch Lconv_loop1\n"
      ".p2align 12\n"
      "Lconv_pa0:\n"
      "s_cbranch_execz Lconv_r0\n"
      "s_nop 0\n"
      "Lconv_loop0:\n"
      "v_mfma_f32_16x16x32_bf16 v[20:23], a[24:27], v[96:99], 0\n"
      "s_add_u32 s51, s51, s12\n"
      "s_nop 1\n"
      "v_mfma_f32_16x16x32_bf16 v[20:23], a[28:31], v[152:155], v[20:23]\n"
      "s_sub_u32 s58, s93, s58\n"
      "v_cvt_pk_bf16_f32 v32, v32, v33\n"
      "v_cvt_pk_bf16_f32 v33, v34, v35\n"
      "v_mfma_f32_16x16x32_bf16 v[20:23], a[32:35], a[220:223], v[20:23]\n"
      "s_sub_u32 s59, s94, s59\n"
      "ds_write_addtid_b32 v32 offset:34816\n"
      "v_mfma_f32_16x16x32_bf16 v[20:23], a[0:3], v[116:119], v[20:23]\n"
      "s_sub_u32 s47, s51, s48\n"
      "ds_write_addtid_b32 v33 offset:35072\n"
      "v_mfma_f32_16x16x32_bf16 v[20:23], a[4:7], v[168:171], v[20:23]\n"
      "s_cmp_ge_u32 vcc_lo, 0x80000000\n"
      "v_mfma_f32_16x16x32_bf16 v[20:23], a[8:11], v[224:227], v[20:23]\n"
      "s_cselect_b32 s53, s47, s51\n"
      "v_mfma_f32_16x16x32_bf16 v[24:27], a[24:27], v[104:107], 0\n"
      "s_waitcnt vmcnt(8) lgkmcnt(0)\n"
      "s_barrier\n"
      "v_mfma_f32_16x16x32_bf16 v[24:27], a[28:31], v[156:159], v[24:27]\n"
      "s_mov_b32 m0, s58\n"
      "ds_read_b128 a[12:15], v7 offset:6144\n"
      "v_mfma_f32_16x16x32_bf16 v[24:27], a[32:35], v[212:215], v[24:27]\n"
      "ds_read_b128 a[16:19], v7 offset:6400\n"
      "v_mfma_f32_16x16x32_bf16 v[24:27], a[0:3], v[120:123], v[24:27]\n"
      "ds_read_b128 a[20:23], v7 offset:6656\n"
      "v_mfma_f32_16x16x32_bf16 v[24:27], a[4:7], v[176:179], v[24:27]\n"
      "ds_read_b128 a[48:51], v2 offset:13280\n"
      "v_mfma_f32_16x16x32_bf16 v[24:27], a[8:11], a[232:235], v[24:27]\n"
      "ds_read_b128 a[52:55], v3 offset:13280\n"
      "v_mfma_f32_16x16x32_bf16 v[68:71], a[84:87], a[60:63], 0\n"
      "ds_read_b128 a[56:59], v4 offset:13280\n"
      "s_nop 0\n"
      "v_mfma_f32_16x16x32_bf16 v[28:31], a[24:27], v[108:111], 0\n"
      "ds_read2_b64 a[64:67], v10 offset0:0 offset1:64\n"
      "s_nop 0\n"
      "v_mfma_f32_16x16x32_bf16 v[28:31], a[28:31], v[164:167], v[28:31]\n"
      "buffer_load_dwordx4 v16, s[32:35], s53 offen lds\n"
      "v_mfma_f32_16x16x32_bf16 v[28:31], a[32:35], a[224:227], v[28:31]\n"
      "s_mov_b32 m0, s45\n"
      "ds_read2_b64 a[68:71], v10 offset0:1 offset1:65\n"
      "v_cvt_pk_bf16_f32 v85, v68, v69\n"
      "v_mfma_f32_16x16x32_bf16 v[28:31], a[0:3], v[128:131], v[28:31]\n"
      "ds_read_b128 a[24:27], v6 offset:5376\n"
      "v_cvt_pk_bf16_f32 v91, v70, v71\n"
      "v_mfma_f32_16x16x32_bf16 v[28:31], a[4:7], a[228:231], v[28:31]\n"
      "ds_read_b128 a[28:31], v6 offset:5632\n"
      "v_mfma_f32_16x16x32_bf16 v[28:31], a[8:11], v[236:239], v[28:31]\n"
      "s_waitcnt lgkmcnt(6)\n"
      "ds_read_b128 a[32:35], v6 offset:5888\n"
      "v_mfma_f32_16x16x32_bf16 v[20:23], a[12:15], v[132:135], v[20:23]\n"
      "buffer_store_dwordx2 v[84:85], v15, s[36:39], s52 offen sc1\n"
      "v_mfma_f32_16x16x32_bf16 v[20:23], a[16:19], v[188:191], v[20:23]\n"
      "ds_read_b128 a[0:3], v7 offset:768\n"
      "v_mfma_f32_16x16x32_bf16 v[20:23], a[20:23], a[240:243], v[20:23]\n"
      "ds_read_b128 a[4:7], v7 offset:1024\n"
      "s_nop 0\n"
      "v_mfma_f32_16x16x32_bf16 v[52:55], a[48:51], a[72:75], 0\n"
      "s_waitcnt lgkmcnt(6)\n"
      "ds_read_b128 a[8:11], v7 offset:1280\n"
      "s_nop 0\n"
      "v_mfma_f32_16x16x32_bf16 v[56:59], a[52:55], a[76:79], 0\n"
      "ds_read_b128 a[36:39], v2 offset:13344\n"
      "v_cvt_pk_bf16_f32 v20, v20, v21\n"
      "v_mfma_f32_16x16x32_bf16 v[60:63], a[56:59], a[80:83], v[52:55]\n"
      "buffer_store_dwordx2 v[90:91], v15, s[36:39], s52 offen offset:1536 sc1\n"
      "v_cvt_pk_bf16_f32 v21, v22, v23\n"
      "v_mfma_f32_16x16x32_bf16 v[24:27], a[12:15], v[140:143], v[24:27]\n"
      "s_mov_b32 s52, s13\n"
      "ds_write_addtid_b32 v20 offset:62480\n"
      "v_fma_f32 v64, s44, v52, v56\n"
      "v_mfma_f32_16x16x32_bf16 v[24:27], a[16:19], a[236:239], v[24:27]\n"
      "s_add_u32 s13, s13, s12\n"
      "ds_write_addtid_b32 v21 offset:62736\n"
      "v_fma_f32 v65, s44, v53, v57\n"
      "v_mfma_f32_16x16x32_bf16 v[24:27], a[20:23], v[248:251], v[24:27]\n"
      "s_sub_u32 s96, s53, s95\n"
      "ds_read_b128 a[40:43], v3 offset:13344\n"
      "v_cvt_pk_bf16_f32 v64, v64, v65\n"
      "v_mfma_f32_16x16x32_bf16 v[72:75], a[84:87], a[64:67], 0\n"
      "ds_read_b128 a[44:47], v4 offset:13344\n"
      "v_fma_f32 v66, s44, v54, v58\n"
      "v_mfma_f32_16x16x32_bf16 v[28:31], a[12:15], v[144:147], v[28:31]\n"
      "ds_read_b128 a[12:15], v7 offset:5376\n"
      "v_cvt_pk_bf16_f32 v24, v24, v25\n"
      "v_mfma_f32_16x16x32_bf16 v[28:31], a[16:19], v[200:203], v[28:31]\n"
      "ds_read_b128 a[16:19], v7 offset:5632\n"
      "v_cvt_pk_bf16_f32 v25, v26, v27\n"
      "v_mfma_f32_16x16x32_bf16 v[28:31], a[20:23], a[244:247], v[28:31]\n"
      "s_waitcnt lgkmcnt(6)\n"
      "ds_read_b128 a[20:23], v7 offset:5888\n"
      "v_fma_f32 v67, s44, v55, v59\n"
      "v_mfma_f32_16x16x32_bf16 v[76:79], a[84:87], a[68:71], 0\n"
      "ds_write_addtid_b32 v24 offset:50184\n"
      "ds_write_addtid_b32 v25 offset:50440\n"
      "v_mfma_f32_16x16x32_bf16 v[32:35], a[24:27], v[92:95], 0\n"
      "s_mov_b32 m0, s18\n"
      "ds_read2_b64 a[60:63], v10 offset0:2 offset1:66\n"
      "v_cvt_pk_bf16_f32 v65, v66, v67\n"
      "v_mfma_f32_16x16x32_bf16 v[32:35], a[28:31], v[148:151], v[32:35]\n"
      "ds_write_addtid_b32 v64 offset:768\n"
      "v_fma_f32 v56, s44, v52, -v56\n"
      "v_mfma_f32_16x16x32_bf16 v[32:35], a[32:35], v[204:207], v[32:35]\n"
      "ds_write_addtid_b32 v65 offset:37696\n"
      "v_fma_f32 v57, s44, v53, -v57\n"
      "v_mfma_f32_16x16x32_bf16 v[32:35], a[0:3], v[112:115], v[32:35]\n"
      "v_fma_f32 v58, s44, v54, -v58\n"
      "v_fma_f32 v59, s44, v55, -v59\n"
      "v_mfma_f32_16x16x32_bf16 v[32:35], a[4:7], a[208:211], v[32:35]\n"
      "v_cvt_pk_bf16_f32 v56, v56, v57\n"
      "v_cvt_pk_bf16_f32 v57, v58, v59\n"
      "v_mfma_f32_16x16x32_bf16 v[32:35], a[8:11], v[220:223], v[32:35]\n"
      "s_waitcnt lgkmcnt(6)\n"
      "ds_write_addtid_b32 v56 offset:1536\n"
      "v_cvt_pk_bf16_f32 v60, v60, v61\n"
      "v_mfma_f32_16x16x32_bf16 v[32:35], a[12:15], a[212:215], v[32:35]\n"
      "ds_write_addtid_b32 v57 offset:38464\n"
      "v_cvt_pk_bf16_f32 v61, v62, v63\n"
      "v_mfma_f32_16x16x32_bf16 v[32:35], a[16:19], v[184:187], v[32:35]\n"
      "s_waitcnt lgkmcnt(7)\n"
      "ds_write_addtid_b32 v60 offset:0\n"
      "ds_write_addtid_b32 v61 offset:36928\n"
      "v_mfma_f32_16x16x32_bf16 v[32:35], a[20:23], v[240:243], v[32:35]\n"
      "s_mov_b32 m0, s45\n"
      "ds_read_b128 a[48:51], v2 offset:13408\n"
      "v_cvt_pk_bf16_f32 v28, v28, v29\n"
      "v_mfma_f32_16x16x32_bf16 v[20:23], a[24:27], v[100:103], 0\n"
      "ds_write_addtid_b32 v28 offset:37888\n"
      "v_cvt_pk_bf16_f32 v29, v30, v31\n"
      "v_mfma_f32_16x16x32_bf16 v[20:23], a[28:31], a[200:203], v[20:23]\n"
      "ds_write_addtid_b32 v29 offset:38144\n"
      "v_cvt_pk_bf16_f32 v32, v32, v33\n"
      "v_mfma_f32_16x16x32_bf16 v[20:23], a[32:35], v[208:211], v[20:23]\n"
      "s_mov_b32 m0, s58\n"
      "ds_read_b128 a[56:59], v4 offset:13408\n"
      "v_cvt_pk_bf16_f32 v33, v34, v35\n"
      "v_mfma_f32_16x16x32_bf16 v[20:23], a[0:3], a[204:207], v[20:23]\n"
      "buffer_load_dwordx4 v17, s[32:35], s53 offen offset:1024 lds\n"
      "v_cvt_pk_bf16_f32 v80, v72, v73\n"
      "v_mfma_f32_16x16x32_bf16 v[20:23], a[4:7], v[172:175], v[20:23]\n"
      "s_mov_b32 m0, s45\n"
      "ds_read2_b64 a[64:67], v10 offset0:3 offset1:67\n"
      "v_cvt_pk_bf16_f32 v81, v76, v77\n"
      "v_mfma_f32_16x16x32_bf16 v[20:23], a[8:11], v[228:231], v[20:23]\n"
      "ds_write_addtid_b32 v32 offset:61456\n"
      "v_cvt_pk_bf16_f32 v86, v74, v75\n"
      "v_mfma_f32_16x16x32_bf16 v[20:23], a[12:15], v[136:139], v[20:23]\n"
      "ds_write_addtid_b32 v33 offset:61712\n"
      "v_cvt_pk_bf16_f32 v87, v78, v79\n"
      "v_mfma_f32_16x16x32_bf16 v[20:23], a[16:19], v[192:195], v[20:23]\n"
      "ds_read_b128 a[52:55], v3 offset:13408\n"
      "v_add_u32_e64 v2, s91, v2\n"
      "v_mfma_f32_16x16x32_bf16 v[20:23], a[20:23], v[244:247], v[20:23]\n"
      "ds_read2_b64 a[68:71], v12 offset0:0 offset1:64\n"
      "v_add_u32_e64 v3, s91, v3\n"
      "v_mfma_f32_16x16x32_bf16 v[36:39], a[36:39], a[72:75], 0\n"
      "v_add_u32_e32 v4, s91, v4\n"
      "s_nop 0\n"
      "v_mfma_f32_16x16x32_bf16 v[40:43], a[40:43], a[76:79], 0\n"
      "s_sub_u32 s91, 0, s91\n"
      "v_mfma_f32_16x16x32_bf16 v[44:47], a[44:47], a[80:83], v[36:39]\n"
      "s_waitcnt lgkmcnt(5)\n"
      "v_cvt_pk_bf16_f32 v20, v20, v21\n"
      "v_cvt_pk_bf16_f32 v21, v22, v23\n"
      "v_mfma_f32_16x16x32_bf16 v[68:71], a[84:87], a[60:63], 0\n"
      "ds_write_addtid_b32 v20 offset:49160\n"
      "v_mfma_f32_16x16x32_bf16 v[24:27], a[24:27], a[196:199], 0\n"
      "ds_read_b128 a[24:27], v7 offset:4608\n"
      "v_fma_f32 v48, s44, v36, v40\n"
      "v_mfma_f32_16x16x32_bf16 v[24:27], a[28:31], v[160:163], v[24:27]\n"
      "ds_read_b128 a[28:31], v7 offset:4864\n"
      "v_fma_f32 v49, s44, v37, v41\n"
      "v_mfma_f32_16x16x32_bf16 v[24:27], a[32:35], v[216:219], v[24:27]\n"
      "ds_read_b128 a[32:35], v7 offset:5120\n"
      "v_cvt_pk_bf16_f32 v48, v48, v49\n"
      "v_mfma_f32_16x16x32_bf16 v[24:27], a[0:3], v[124:127], v[24:27]\n"
      "ds_read_b128 a[0:3], v6 offset:4608\n"
      "v_fma_f32 v50, s44, v38, v42\n"
      "v_mfma_f32_16x16x32_bf16 v[24:27], a[4:7], v[180:183], v[24:27]\n"
      "ds_read_b128 a[4:7], v6 offset:4864\n"
      "v_fma_f32 v51, s44, v39, v43\n"
      "v_mfma_f32_16x16x32_bf16 v[24:27], a[8:11], v[232:235], v[24:27]\n"
      "ds_read_b128 a[8:11], v6 offset:5120\n"
      "v_cvt_pk_bf16_f32 v49, v50, v51\n"
      "v_mfma_f32_16x16x32_bf16 v[24:27], a[12:15], a[216:219], v[24:27]\n"
      "ds_read_b128 a[12:15], v7 offset:0\n"
      "v_fma_f32 v40, s44, v36, -v40\n"
      "v_mfma_f32_16x16x32_bf16 v[24:27], a[16:19], v[196:199], v[24:27]\n"
      "ds_read_b128 a[16:19], v7 offset:256\n"
      "v_fma_f32 v41, s44, v37, -v41\n"
      "v_mfma_f32_16x16x32_bf16 v[24:27], a[20:23], v[252:255], v[24:27]\n"
      "ds_read_b128 a[20:23], v7 offset:512\n"
      "v_cvt_pk_bf16_f32 v40, v40, v41\n"
      "v_mfma_f32_16x16x32_bf16 v[52:55], a[48:51], a[72:75], 0\n"
      "ds_write_addtid_b32 v21 offset:49416\n"
      "v_fma_f32 v42, s44, v38, -v42\n"
      "v_mfma_f32_16x16x32_bf16 v[60:63], a[56:59], a[80:83], v[52:55]\n"
      "s_waitcnt lgkmcnt(7)\n"
      "v_fma_f32 v43, s44, v39, -v43\n"
      "v_cvt_pk_bf16_f32 v24, v24, v25\n"
      "v_mfma_f32_16x16x32_bf16 v[72:75], a[84:87], a[64:67], 0\n"
      "s_mov_b32 m0, s58\n"
      "v_cvt_pk_bf16_f32 v25, v26, v27\n"
      "v_cvt_pk_bf16_f32 v41, v42, v43\n"
      "v_mfma_f32_16x16x32_bf16 v[28:31], a[24:27], a[160:163], 0\n"
      "buffer_load_dwordx4 v18, s[32:35], s53 offen offset:2048 lds\n"
      "v_cvt_pk_bf16_f32 v44, v44, v45\n"
      "v_mfma_f32_16x16x32_bf16 v[28:31], a[28:31], a[172:175], v[28:31]\n"
      "s_mov_b32 m0, s45\n"
      "v_cvt_pk_bf16_f32 v82, v68, v69\n"
      "v_cvt_pk_bf16_f32 v83, v72, v73\n"
      "v_mfma_f32_16x16x32_bf16 v[28:31], a[32:35], a[184:187], v[28:31]\n"
      "s_waitcnt lgkmcnt(3)\n"
      "ds_write_addtid_b32 v24 offset:36864\n"
      "ds_write_addtid_b32 v25 offset:37120\n"
      "v_mfma_f32_16x16x32_bf16 v[28:31], a[0:3], a[88:91], v[28:31]\n"
      "s_mov_b32 m0, s18\n"
      "v_cvt_pk_bf16_f32 v45, v46, v47\n"
      "v_cvt_pk_bf16_f32 v88, v70, v71\n"
      "v_mfma_f32_16x16x32_bf16 v[28:31], a[4:7], a[100:103], v[28:31]\n"
      "ds_write_addtid_b32 v48 offset:1024\n"
      "ds_write_addtid_b32 v49 offset:37952\n"
      "v_mfma_f32_16x16x32_bf16 v[28:31], a[8:11], a[112:115], v[28:31]\n"
      "ds_write_addtid_b32 v40 offset:1792\n"
      "ds_write_addtid_b32 v41 offset:38720\n"
      "v_mfma_f32_16x16x32_bf16 v[28:31], a[12:15], a[124:127], v[28:31]\n"
      "s_waitcnt lgkmcnt(7)\n"
      "ds_write_addtid_b32 v44 offset:256\n"
      "ds_write_addtid_b32 v45 offset:37184\n"
      "v_mfma_f32_16x16x32_bf16 v[28:31], a[16:19], a[136:139], v[28:31]\n"
      "buffer_store_dwordx4 v[80:83], v14, s[36:39], s52 offen sc1\n"
      "v_cvt_pk_bf16_f32 v89, v74, v75\n"
      "v_mfma_f32_16x16x32_bf16 v[28:31], a[20:23], a[148:151], v[28:31]\n"
      "s_mov_b32 m0, s45\n"
      "ds_read2_b64 a[60:63], v12 offset0:1 offset1:65\n"
      "v_cvt_pk_bf16_f32 v60, v60, v61\n"
      "v_mfma_f32_16x16x32_bf16 v[56:59], a[52:55], a[76:79], 0\n"
      "v_cvt_pk_bf16_f32 v61, v62, v63\n"
      "v_swap_b32 v6, v7\n"
      "v_mfma_f32_16x16x32_bf16 v[20:23], a[24:27], a[164:167], 0\n"
      "v_mfma_f32_16x16x32_bf16 v[20:23], a[28:31], a[176:179], v[20:23]\n"
      "v_cvt_pk_bf16_f32 v28, v28, v29\n"
      "v_cvt_pk_bf16_f32 v29, v30, v31\n"
      "v_mfma_f32_16x16x32_bf16 v[20:23], a[32:35], a[188:191], v[20:23]\n"
      "buffer_store_dwordx4 v[86:89], v14, s[36:39], s52 offen offset:1536 sc1\n"
      "v_fma_f32 v64, s44, v52, v56\n"
      "v_mfma_f32_16x16x32_bf16 v[20:23], a[0:3], a[92:95], v[20:23]\n"
      "ds_write_addtid_b32 v28 offset:60432\n"
      "ds_write_addtid_b32 v29 offset:60688\n"
      "v_mfma_f32_16x16x32_bf16 v[20:23], a[4:7], a[104:107], v[20:23]\n"
      "s_mov_b32 m0, s18\n"
      "v_fma_f32 v65, s44, v53, v57\n"
      "v_cvt_pk_bf16_f32 v64, v64, v65\n"
      "v_mfma_f32_16x16x32_bf16 v[20:23], a[8:11], a[116:119], v[20:23]\n"
      "ds_write_addtid_b32 v64 offset:1280\n"
      "v_fma_f32 v66, s44, v54, v58\n"
      "v_mfma_f32_16x16x32_bf16 v[20:23], a[12:15], a[128:131], v[20:23]\n"
      "v_fma_f32 v67, s44, v55, v59\n"
      "v_cvt_pk_bf16_f32 v65, v66, v67\n"
      "v_mfma_f32_16x16x32_bf16 v[20:23], a[16:19], a[140:143], v[20:23]\n"
      "ds_write_addtid_b32 v65 offset:38208\n"
      "v_fma_f32 v56, s44, v52, -v56\n"
      "v_mfma_f32_16x16x32_bf16 v[20:23], a[20:23], a[152:155], v[20:23]\n"
      "v_fma_f32 v57, s44, v53, -v57\n"
      "v_cvt_pk_bf16_f32 v56, v56, v57\n"
      "v_mfma_f32_16x16x32_bf16 v[76:79], a[84:87], a[68:71], 0\n"
      "ds_write_addtid_b32 v56 offset:2048\n"
      "v_fma_f32 v58, s44, v54, -v58\n"
      "v_mfma_f32_16x16x32_bf16 v[32:35], a[24:27], a[168:171], 0\n"
      "ds_write_addtid_b32 v60 offset:512\n"
      "v_fma_f32 v59, s44, v55, -v59\n"
      "v_mfma_f32_16x16x32_bf16 v[32:35], a[28:31], a[180:183], v[32:35]\n"
      "ds_write_addtid_b32 v61 offset:37440\n"
      "v_cvt_pk_bf16_f32 v57, v58, v59\n"
      "v_mfma_f32_16x16x32_bf16 v[32:35], a[32:35], a[192:195], v[32:35]\n"
      "ds_write_addtid_b32 v57 offset:38976\n"
      "v_cvt_pk_bf16_f32 v20, v20, v21\n"
      "v_mfma_f32_16x16x32_bf16 v[32:35], a[0:3], a[96:99], v[32:35]\n"
      "s_mov_b32 m0, s45\n"
      "v_cvt_pk_bf16_f32 v21, v22, v23\n"
      "v_cvt_pk_bf16_f32 v84, v76, v77\n"
      "v_mfma_f32_16x16x32_bf16 v[32:35], a[4:7], a[108:111], v[32:35]\n"
      "s_mov_b32 m0, s59\n"
      "v_cvt_pk_bf16_f32 v90, v78, v79\n"
      "v_mfma_f32_16x16x32_bf16 v[32:35], a[8:11], a[120:123], v[32:35]\n"
      "buffer_load_dwordx4 v19, s[32:35], s96 offen lds\n"
      "v_mfma_f32_16x16x32_bf16 v[32:35], a[12:15], a[132:135], v[32:35]\n"
      "s_mov_b32 m0, s45\n"
      "v_mfma_f32_16x16x32_bf16 v[32:35], a[16:19], a[144:147], v[32:35]\n"
      "ds_write_addtid_b32 v20 offset:48136\n"
      "ds_write_addtid_b32 v21 offset:48392\n"
      "v_mfma_f32_16x16x32_bf16 v[32:35], a[20:23], a[156:159], v[32:35]\n"
      "s_nop 1\n"
      "v_mfma_f32_16x16x32_bf16 v[20:23], a[12:15], a[88:91], 0\n"
      "s_add_u32 s51, s51, s12\n"
      "s_nop 1\n"
      "v_mfma_f32_16x16x32_bf16 v[20:23], a[16:19], a[100:103], v[20:23]\n"
      "s_sub_u32 s47, s51, s48\n"
      "v_cvt_pk_bf16_f32 v32, v32, v33\n"
      "v_cvt_pk_bf16_f32 v33, v34, v35\n"
      "v_mfma_f32_16x16x32_bf16 v[20:23], a[20:23], a[112:115], v[20:23]\n"
      "s_cmp_ge_u32 vcc_lo, 0x40000000\n"
      "ds_write_addtid_b32 v32 offset:35840\n"
      "v_mfma_f32_16x16x32_bf16 v[20:23], a[24:27], a[124:127], v[20:23]\n"
      "s_cselect_b32 s53, s47, s51\n"
      "ds_write_addtid_b32 v33 offset:36096\n"
      "v_mfma_f32_16x16x32_bf16 v[20:23], a[28:31], a[136:139], v[20:23]\n"
      "s_mul_i32 vcc_lo, vcc_lo, 2\n"
      "v_mfma_f32_16x16x32_bf16 v[20:23], a[32:35], a[148:151], v[20:23]\n"
      "v_mfma_f32_16x16x32_bf16 v[24:27], a[12:15], a[92:95], 0\n"
      "s_waitcnt vmcnt(19) lgkmcnt(0)\n"
      "s_barrier\n"
      "v_mfma_f32_16x16x32_bf16 v[24:27], a[16:19], a[104:107], v[24:27]\n"
      "s_add_u32 m0, s58, 13280\n"
      "ds_read_b128 a[0:3], v7 offset:0\n"
      "v_mfma_f32_16x16x32_bf16 v[24:27], a[20:23], a[116:119], v[24:27]\n"
      "ds_read_b128 a[4:7], v7 offset:256\n"
      "v_mfma_f32_16x16x32_bf16 v[24:27], a[24:27], a[128:131], v[24:27]\n"
      "ds_read_b128 a[8:11], v7 offset:512\n"
      "v_mfma_f32_16x16x32_bf16 v[24:27], a[28:31], a[140:143], v[24:27]\n"
      "ds_read_b128 a[36:39], v2 offset:0\n"
      "v_mfma_f32_16x16x32_bf16 v[24:27], a[32:35], a[152:155], v[24:27]\n"
      "ds_read_b128 a[40:43], v3 offset:0\n"
      "v_mfma_f32_16x16x32_bf16 v[68:71], a[84:87], a[60:63], 0\n"
      "ds_read_b128 a[44:47], v4 offset:0\n"
      "s_nop 0\n"
      "v_mfma_f32_16x16x32_bf16 v[28:31], a[12:15], a[96:99], 0\n"
      "ds_read2_b64 a[64:67], v11 offset0:0 offset1:64\n"
      "s_nop 0\n"
      "v_mfma_f32_16x16x32_bf16 v[28:31], a[16:19], a[108:111], v[28:31]\n"
      "buffer_load_dwordx4 v16, s[32:35], s53 offen lds\n"
      "v_mfma_f32_16x16x32_bf16 v[28:31], a[20:23], a[120:123], v[28:31]\n"
      "s_mov_b32 m0, s45\n"
      "ds_read2_b64 a[68:71], v11 offset0:1 offset1:65\n"
      "v_cvt_pk_bf16_f32 v85, v68, v69\n"
      "v_mfma_f32_16x16x32_bf16 v[28:31], a[24:27], a[132:135], v[28:31]\n"
      "ds_read_b128 a[12:15], v6 offset:768\n"
      "v_cvt_pk_bf16_f32 v91, v70, v71\n"
      "v_mfma_f32_16x16x32_bf16 v[28:31], a[28:31], a[144:147], v[28:31]\n"
      "ds_read_b128 a[16:19], v6 offset:1024\n"
      "v_mfma_f32_16x16x32_bf16 v[28:31], a[32:35], a[156:159], v[28:31]\n"
      "s_waitcnt lgkmcnt(6)\n"
      "ds_read_b128 a[20:23], v6 offset:1280\n"
      "v_mfma_f32_16x16x32_bf16 v[20:23], a[0:3], a[160:163], v[20:23]\n"
      "buffer_store_dwordx2 v[84:85], v15, s[36:39], s52 offen sc1\n"
      "v_mfma_f32_16x16x32_bf16 v[20:23], a[4:7], a[172:175], v[20:23]\n"
      "ds_read_b128 a[24:27], v6 offset:5376\n"
      "v_mfma_f32_16x16x32_bf16 v[20:23], a[8:11], a[184:187], v[20:23]\n"
      "ds_read_b128 a[28:31], v6 offset:5632\n"
      "s_nop 0\n"
      "v_mfma_f32_16x16x32_bf16 v[36:39], a[36:39], a[72:75], 0\n"
      "s_waitcnt lgkmcnt(6)\n"
      "ds_read_b128 a[32:35], v6 offset:5888\n"
      "s_nop 0\n"
      "v_mfma_f32_16x16x32_bf16 v[40:43], a[40:43], a[76:79], 0\n"
      "ds_read_b128 a[48:51], v2 offset:64\n"
      "v_cvt_pk_bf16_f32 v20, v20, v21\n"
      "v_mfma_f32_16x16x32_bf16 v[44:47], a[44:47], a[80:83], v[36:39]\n"
      "buffer_store_dwordx2 v[90:91], v15, s[36:39], s52 offen offset:1536 sc1\n"
      "v_cvt_pk_bf16_f32 v21, v22, v23\n"
      "v_mfma_f32_16x16x32_bf16 v[24:27], a[0:3], a[164:167], v[24:27]\n"
      "s_mov_b32 s52, s13\n"
      "ds_write_addtid_b32 v20 offset:57360\n"
      "v_fma_f32 v48, s44, v36, v40\n"
      "v_mfma_f32_16x16x32_bf16 v[24:27], a[4:7], a[176:179], v[24:27]\n"
      "s_add_u32 s13, s13, s12\n"
      "ds_write_addtid_b32 v21 offset:57616\n"
      "v_fma_f32 v49, s44, v37, v41\n"
      "v_mfma_f32_16x16x32_bf16 v[24:27], a[8:11], a[188:191], v[24:27]\n"
      "ds_read_b128 a[52:55], v3 offset:64\n"
      "v_cvt_pk_bf16_f32 v48, v48, v49\n"
      "v_mfma_f32_16x16x32_bf16 v[72:75], a[84:87], a[64:67], 0\n"
      "ds_read_b128 a[56:59], v4 offset:64\n"
      "v_fma_f32 v50, s44, v38, v42\n"
      "v_mfma_f32_16x16x32_bf16 v[28:31], a[0:3], a[168:171], v[28:31]\n"
      "ds_read_b128 a[0:3], v7 offset:768\n"
      "v_fma_f32 v51, s44, v39, v43\n"
      "v_mfma_f32_16x16x32_bf16 v[28:31], a[4:7], a[180:183], v[28:31]\n"
      "ds_read_b128 a[4:7], v7 offset:1024\n"
      "v_cvt_pk_bf16_f32 v24, v24, v25\n"
      "v_mfma_f32_16x16x32_bf16 v[28:31], a[8:11], a[192:195], v[28:31]\n"
      "s_waitcnt lgkmcnt(6)\n"
      "ds_read_b128 a[8:11], v7 offset:1280\n"
      "v_cvt_pk_bf16_f32 v25, v26, v27\n"
      "v_mfma_f32_16x16x32_bf16 v[76:79], a[84:87], a[68:71], 0\n"
      "ds_write_addtid_b32 v24 offset:45064\n"
      "ds_write_addtid_b32 v25 offset:45320\n"
      "v_mfma_f32_16x16x32_bf16 v[32:35], a[12:15], v[92:95], 0\n"
      "s_mov_b32 m0, s18\n"
      "ds_read2_b64 a[60:63], v11 offset0:2 offset1:66\n"
      "v_cvt_pk_bf16_f32 v49, v50, v51\n"
      "v_mfma_f32_16x16x32_bf16 v[32:35], a[16:19], v[148:151], v[32:35]\n"
      "ds_write_addtid_b32 v48 offset:5376\n"
      "v_fma_f32 v40, s44, v36, -v40\n"
      "v_mfma_f32_16x16x32_bf16 v[32:35], a[20:23], v[204:207], v[32:35]\n"
      "ds_write_addtid_b32 v49 offset:42304\n"
      "v_fma_f32 v41, s44, v37, -v41\n"
      "v_mfma_f32_16x16x32_bf16 v[32:35], a[24:27], v[112:115], v[32:35]\n"
      "v_fma_f32 v42, s44, v38, -v42\n"
      "v_fma_f32 v43, s44, v39, -v43\n"
      "v_mfma_f32_16x16x32_bf16 v[32:35], a[28:31], a[208:211], v[32:35]\n"
      "v_cvt_pk_bf16_f32 v40, v40, v41\n"
      "v_cvt_pk_bf16_f32 v41, v42, v43\n"
      "v_mfma_f32_16x16x32_bf16 v[32:35], a[32:35], v[220:223], v[32:35]\n"
      "s_waitcnt lgkmcnt(6)\n"
      "ds_write_addtid_b32 v40 offset:6144\n"
      "v_cvt_pk_bf16_f32 v44, v44, v45\n"
      "v_mfma_f32_16x16x32_bf16 v[32:35], a[0:3], a[212:215], v[32:35]\n"
      "ds_write_addtid_b32 v41 offset:43072\n"
      "v_cvt_pk_bf16_f32 v45, v46, v47\n"
      "v_mfma_f32_16x16x32_bf16 v[32:35], a[4:7], v[184:187], v[32:35]\n"
      "s_waitcnt lgkmcnt(7)\n"
      "ds_write_addtid_b32 v44 offset:4608\n"
      "ds_write_addtid_b32 v45 offset:41536\n"
      "v_mfma_f32_16x16x32_bf16 v[32:35], a[8:11], v[240:243], v[32:35]\n"
      "s_mov_b32 m0, s45\n"
      "ds_read_b128 a[36:39], v2 offset:128\n"
      "v_cvt_pk_bf16_f32 v28, v28, v29\n"
      "v_mfma_f32_16x16x32_bf16 v[20:23], a[12:15], v[100:103], 0\n"
      "ds_write_addtid_b32 v28 offset:32768\n"
      "v_cvt_pk_bf16_f32 v29, v30, v31\n"
      "v_mfma_f32_16x16x32_bf16 v[20:23], a[16:19], a[200:203], v[20:23]\n"
      "ds_write_addtid_b32 v29 offset:33024\n"
      "v_cvt_pk_bf16_f32 v32, v32, v33\n"
      "v_mfma_f32_16x16x32_bf16 v[20:23], a[20:23], v[208:211], v[20:23]\n"
      "s_add_u32 m0, s58, 13280\n"
      "ds_read_b128 a[44:47], v4 offset:128\n"
      "v_cvt_pk_bf16_f32 v33, v34, v35\n"
      "v_mfma_f32_16x16x32_bf16 v[20:23], a[24:27], a[204:207], v[20:23]\n"
      "buffer_load_dwordx4 v17, s[32:35], s53 offen offset:1024 lds\n"
      "v_cvt_pk_bf16_f32 v80, v72, v73\n"
      "v_mfma_f32_16x16x32_bf16 v[20:23], a[28:31], v[172:175], v[20:23]\n"
      "s_mov_b32 m0, s45\n"
      "ds_read2_b64 a[64:67], v11 offset0:3 offset1:67\n"
      "v_cvt_pk_bf16_f32 v81, v76, v77\n"
      "v_mfma_f32_16x16x32_bf16 v[20:23], a[32:35], v[228:231], v[20:23]\n"
      "ds_write_addtid_b32 v32 offset:58384\n"
      "v_cvt_pk_bf16_f32 v86, v74, v75\n"
      "v_mfma_f32_16x16x32_bf16 v[20:23], a[0:3], v[136:139], v[20:23]\n"
      "ds_write_addtid_b32 v33 offset:58640\n"
      "v_cvt_pk_bf16_f32 v87, v78, v79\n"
      "v_mfma_f32_16x16x32_bf16 v[20:23], a[4:7], v[192:195], v[20:23]\n"
      "ds_read_b128 a[40:43], v3 offset:128\n"
      "v_mfma_f32_16x16x32_bf16 v[20:23], a[8:11], v[244:247], v[20:23]\n"
      "global_load_dword v8, v9, s[54:55]\n"
      "s_nop 0\n"
      "v_mfma_f32_16x16x32_bf16 v[52:55], a[48:51], a[72:75], 0\n"
      "ds_read2_b64 a[68:71], v13 offset0:0 offset1:64\n"
      "s_nop 0\n"
      "v_mfma_f32_16x16x32_bf16 v[56:59], a[52:55], a[76:79], 0\n"
      "s_nop 0\n"
      "v_mfma_f32_16x16x32_bf16 v[60:63], a[56:59], a[80:83], v[52:55]\n"
      "s_waitcnt lgkmcnt(5)\n"
      "v_cvt_pk_bf16_f32 v20, v20, v21\n"
      "v_cvt_pk_bf16_f32 v21, v22, v23\n"
      "v_mfma_f32_16x16x32_bf16 v[68:71], a[84:87], a[60:63], 0\n"
      "ds_write_addtid_b32 v20 offset:46088\n"
      "v_mfma_f32_16x16x32_bf16 v[24:27], a[12:15], a[196:199], 0\n"
      "ds_read_b128 a[12:15], v6 offset:1536\n"
      "v_fma_f32 v64, s44, v52, v56\n"
      "v_mfma_f32_16x16x32_bf16 v[24:27], a[16:19], v[160:163], v[24:27]\n"
      "ds_read_b128 a[16:19], v6 offset:1792\n"
      "v_fma_f32 v65, s44, v53, v57\n"
      "v_mfma_f32_16x16x32_bf16 v[24:27], a[20:23], v[216:219], v[24:27]\n"
      "ds_read_b128 a[20:23], v6 offset:2048\n"
      "v_cvt_pk_bf16_f32 v64, v64, v65\n"
      "v_mfma_f32_16x16x32_bf16 v[24:27], a[24:27], v[124:127], v[24:27]\n"
      "ds_read_b128 a[24:27], v6 offset:6144\n"
      "v_fma_f32 v66, s44, v54, v58\n"
      "v_mfma_f32_16x16x32_bf16 v[24:27], a[28:31], v[180:183], v[24:27]\n"
      "ds_read_b128 a[28:31], v6 offset:6400\n"
      "v_fma_f32 v67, s44, v55, v59\n"
      "v_mfma_f32_16x16x32_bf16 v[24:27], a[32:35], v[232:235], v[24:27]\n"
      "ds_read_b128 a[32:35], v6 offset:6656\n"
      "v_cvt_pk_bf16_f32 v65, v66, v67\n"
      "v_mfma_f32_16x16x32_bf16 v[24:27], a[0:3], a[216:219], v[24:27]\n"
      "ds_read_b128 a[0:3], v7 offset:1536\n"
      "v_fma_f32 v56, s44, v52, -v56\n"
      "v_mfma_f32_16x16x32_bf16 v[24:27], a[4:7], v[196:199], v[24:27]\n"
      "ds_read_b128 a[4:7], v7 offset:1792\n"
      "v_fma_f32 v57, s44, v53, -v57\n"
      "v_mfma_f32_16x16x32_bf16 v[24:27], a[8:11], v[252:255], v[24:27]\n"
      "ds_read_b128 a[8:11], v7 offset:2048\n"
      "v_cvt_pk_bf16_f32 v56, v56, v57\n"
      "v_mfma_f32_16x16x32_bf16 v[36:39], a[36:39], a[72:75], 0\n"
      "ds_write_addtid_b32 v21 offset:46344\n"
      "v_fma_f32 v58, s44, v54, -v58\n"
      "v_mfma_f32_16x16x32_bf16 v[44:47], a[44:47], a[80:83], v[36:39]\n"
      "s_waitcnt lgkmcnt(7)\n"
      "v_fma_f32 v59, s44, v55, -v59\n"
      "v_cvt_pk_bf16_f32 v24, v24, v25\n"
      "v_mfma_f32_16x16x32_bf16 v[72:75], a[84:87], a[64:67], 0\n"
      "s_add_u32 m0, s58, 13280\n"
      "v_cvt_pk_bf16_f32 v25, v26, v27\n"
      "v_cvt_pk_bf16_f32 v57, v58, v59\n"
      "v_mfma_f32_16x16x32_bf16 v[28:31], a[12:15], v[96:99], 0\n"
      "buffer_load_dwordx4 v18, s[32:35], s53 offen offset:2048 lds\n"
      "v_cvt_pk_bf16_f32 v60, v60, v61\n"
      "v_mfma_f32_16x16x32_bf16 v[28:31], a[16:19], v[152:155], v[28:31]\n"
      "s_mov_b32 m0, s45\n"
      "v_cvt_pk_bf16_f32 v82, v68, v69\n"
      "v_cvt_pk_bf16_f32 v83, v72, v73\n"
      "v_mfma_f32_16x16x32_bf16 v[28:31], a[20:23], a[220:223], v[28:31]\n"
      "s_waitcnt lgkmcnt(3)\n"
      "ds_write_addtid_b32 v24 offset:33792\n"
      "ds_write_addtid_b32 v25 offset:34048\n"
      "v_mfma_f32_16x16x32_bf16 v[28:31], a[24:27], v[116:119], v[28:31]\n"
      "s_mov_b32 m0, s18\n"
      "v_cvt_pk_bf16_f32 v61, v62, v63\n"
      "v_cvt_pk_bf16_f32 v88, v70, v71\n"
      "v_mfma_f32_16x16x32_bf16 v[28:31], a[28:31], v[168:171], v[28:31]\n"
      "ds_write_addtid_b32 v64 offset:5632\n"
      "ds_write_addtid_b32 v65 offset:42560\n"
      "v_mfma_f32_16x16x32_bf16 v[28:31], a[32:35], v[224:227], v[28:31]\n"
      "ds_write_addtid_b32 v56 offset:6400\n"
      "ds_write_addtid_b32 v57 offset:43328\n"
      "v_mfma_f32_16x16x32_bf16 v[28:31], a[0:3], v[132:135], v[28:31]\n"
      "s_waitcnt lgkmcnt(7)\n"
      "ds_write_addtid_b32 v60 offset:4864\n"
      "ds_write_addtid_b32 v61 offset:41792\n"
      "v_mfma_f32_16x16x32_bf16 v[28:31], a[4:7], v[188:191], v[28:31]\n"
      "buffer_store_dwordx4 v[80:83], v14, s[36:39], s52 offen sc1\n"
      "v_cvt_pk_bf16_f32 v89, v74, v75\n"
      "v_mfma_f32_16x16x32_bf16 v[28:31], a[8:11], a[240:243], v[28:31]\n"
      "s_mov_b32 m0, s45\n"
      "ds_read2_b64 a[60:63], v13 offset0:1 offset1:65\n"
      "v_cvt_pk_bf16_f32 v44, v44, v45\n"
      "v_mfma_f32_16x16x32_bf16 v[40:43], a[40:43], a[76:79], 0\n"
      "v_cvt_pk_bf16_f32 v45, v46, v47\n"
      "s_nop 0\n"
      "v_mfma_f32_16x16x32_bf16 v[20:23], a[12:15], v[104:107], 0\n"
      "v_mfma_f32_16x16x32_bf16 v[20:23], a[16:19], v[156:159], v[20:23]\n"
      "v_cvt_pk_bf16_f32 v28, v28, v29\n"
      "v_cvt_pk_bf16_f32 v29, v30, v31\n"
      "v_mfma_f32_16x16x32_bf16 v[20:23], a[20:23], v[212:215], v[20:23]\n"
      "buffer_store_dwordx4 v[86:89], v14, s[36:39], s52 offen offset:1536 sc1\n"
      "v_fma_f32 v48, s44, v36, v40\n"
      "v_mfma_f32_16x16x32_bf16 v[20:23], a[24:27], v[120:123], v[20:23]\n"
      "ds_write_addtid_b32 v28 offset:59408\n"
      "ds_write_addtid_b32 v29 offset:59664\n"
      "v_mfma_f32_16x16x32_bf16 v[20:23], a[28:31], v[176:179], v[20:23]\n"
      "s_mov_b32 m0, s18\n"
      "v_fma_f32 v49, s44, v37, v41\n"
      "v_cvt_pk_bf16_f32 v48, v48, v49\n"
      "v_mfma_f32_16x16x32_bf16 v[20:23], a[32:35], a[232:235], v[20:23]\n"
      "s_sub_u32 s18, s19, s18\n"
      "ds_write_addtid_b32 v48 offset:5888\n"
      "v_fma_f32 v50, s44, v38, v42\n"
      "v_mfma_f32_16x16x32_bf16 v[20:23], a[0:3], v[140:143], v[20:23]\n"
      "v_fma_f32 v51, s44, v39, v43\n"
      "v_cvt_pk_bf16_f32 v49, v50, v51\n"
      "v_mfma_f32_16x16x32_bf16 v[20:23], a[4:7], a[236:239], v[20:23]\n"
      "ds_write_addtid_b32 v49 offset:42816\n"
      "v_fma_f32 v40, s44, v36, -v40\n"
      "v_mfma_f32_16x16x32_bf16 v[20:23], a[8:11], v[248:251], v[20:23]\n"
      "v_fma_f32 v41, s44, v37, -v41\n"
      "v_cvt_pk_bf16_f32 v40, v40, v41\n"
      "v_mfma_f32_16x16x32_bf16 v[76:79], a[84:87], a[68:71], 0\n"
      "ds_write_addtid_b32 v40 offset:6656\n"
      "v_fma_f32 v42, s44, v38, -v42\n"
      "v_mfma_f32_16x16x32_bf16 v[32:35], a[12:15], v[108:111], 0\n"
      "ds_write_addtid_b32 v44 offset:5120\n"
      "v_fma_f32 v43, s44, v39, -v43\n"
      "v_mfma_f32_16x16x32_bf16 v[32:35], a[16:19], v[164:167], v[32:35]\n"
      "ds_write_addtid_b32 v45 offset:42048\n"
      "v_cvt_pk_bf16_f32 v41, v42, v43\n"
      "v_mfma_f32_16x16x32_bf16 v[32:35], a[20:23], a[224:227], v[32:35]\n"
      "ds_write_addtid_b32 v41 offset:43584\n"
      "v_cvt_pk_bf16_f32 v20, v20, v21\n"
      "v_mfma_f32_16x16x32_bf16 v[32:35], a[24:27], v[128:131], v[32:35]\n"
      "s_mov_b32 m0, s45\n"
      "v_cvt_pk_bf16_f32 v21, v22, v23\n"
      "v_cvt_pk_bf16_f32 v84, v76, v77\n"
      "v_mfma_f32_16x16x32_bf16 v[32:35], a[28:31], a[228:231], v[32:35]\n"
      "ds_write_addtid_b32 v20 offset:47112\n"
      "v_cvt_pk_bf16_f32 v90, v78, v79\n"
      "v_mfma_f32_16x16x32_bf16 v[32:35], a[32:35], v[236:239], v[32:35]\n"
      "ds_write_addtid_b32 v21 offset:47368\n"
      "v_mfma_f32_16x16x32_bf16 v[32:35], a[0:3], v[144:147], v[32:35]\n"
      "v_mfma_f32_16x16x32_bf16 v[32:35], a[4:7], v[200:203], v[32:35]\n"
      "v_mfma_f32_16x16x32_bf16 v[32:35], a[8:11], a[244:247], v[32:35]\n"
      "s_nop 1\n"
      "s_cbranch_vccnz Lconv_loop0\n"
      "Lconv_t0:\n"
      "s_nop 1\n"
      "v_mfma_f32_16x16x32_bf16 v[20:23], a[24:27], v[96:99], 0\n"
      "s_nop 1\n"
      "v_mfma_f32_16x16x32_bf16 v[20:23], a[28:31], v[152:155], v[20:23]\n"
      "v_cvt_pk_bf16_f32 v32, v32, v33\n"
      "v_cvt_pk_bf16_f32 v33, v34, v35\n"
      "v_mfma_f32_16x16x32_bf16 v[20:23], a[32:35], a[220:223], v[20:23]\n"
      "ds_write_addtid_b32 v32 offset:34816\n"
      "v_mfma_f32_16x16x32_bf16 v[20:23], a[0:3], v[116:119], v[20:23]\n"
      "ds_write_addtid_b32 v33 offset:35072\n"
      "v_mfma_f32_16x16x32_bf16 v[20:23], a[4:7], v[168:171], v[20:23]\n"
      "v_mfma_f32_16x16x32_bf16 v[20:23], a[8:11], v[224:227], v[20:23]\n"
      "v_mfma_f32_16x16x32_bf16 v[24:27], a[24:27], v[104:107], 0\n"
      "s_waitcnt lgkmcnt(0)\n"
      "s_barrier\n"
      "v_mfma_f32_16x16x32_bf16 v[24:27], a[28:31], v[156:159], v[24:27]\n"
      "ds_read2_b64 a[64:67], v10 offset0:0 offset1:64\n"
      "v_mfma_f32_16x16x32_bf16 v[24:27], a[32:35], v[212:215], v[24:27]\n"
      "ds_read_b128 a[12:15], v7 offset:6144\n"
      "v_mfma_f32_16x16x32_bf16 v[24:27], a[0:3], v[120:123], v[24:27]\n"
      "ds_read2_b64 a[68:71], v10 offset0:1 offset1:65\n"
      "v_mfma_f32_16x16x32_bf16 v[24:27], a[4:7], v[176:179], v[24:27]\n"
      "ds_read_b128 a[16:19], v7 offset:6400\n"
      "v_mfma_f32_16x16x32_bf16 v[24:27], a[8:11], a[232:235], v[24:27]\n"
      "ds_read_b128 a[20:23], v7 offset:6656\n"
      "v_mfma_f32_16x16x32_bf16 v[68:71], a[84:87], a[60:63], 0\n"
      "ds_read2_b64 a[60:63], v10 offset0:2 offset1:66\n"
      "s_nop 0\n"
      "v_mfma_f32_16x16x32_bf16 v[28:31], a[24:27], v[108:111], 0\n"
      "s_waitcnt lgkmcnt(5)\n"
      "ds_read_b128 a[24:27], v6 offset:5376\n"
      "s_nop 0\n"
      "v_mfma_f32_16x16x32_bf16 v[72:75], a[84:87], a[64:67], 0\n"
      "ds_read2_b64 a[64:67], v10 offset0:3 offset1:67\n"
      "v_cvt_pk_bf16_f32 v85, v68, v69\n"
      "v_mfma_f32_16x16x32_bf16 v[28:31], a[28:31], v[164:167], v[28:31]\n"
      "buffer_store_dwordx2 v[84:85], v15, s[36:39], s52 offen sc1\n"
      "v_cvt_pk_bf16_f32 v91, v70, v71\n"
      "v_mfma_f32_16x16x32_bf16 v[28:31], a[32:35], a[224:227], v[28:31]\n"
      "ds_read_b128 a[28:31], v6 offset:5632\n"
      "v_mfma_f32_16x16x32_bf16 v[28:31], a[0:3], v[128:131], v[28:31]\n"
      "ds_read_b128 a[32:35], v6 offset:5888\n"
      "v_cvt_pk_bf16_f32 v80, v72, v73\n"
      "v_mfma_f32_16x16x32_bf16 v[28:31], a[4:7], a[228:231], v[28:31]\n"
      "ds_read_b128 a[0:3], v7 offset:768\n"
      "v_cvt_pk_bf16_f32 v86, v74, v75\n"
      "v_mfma_f32_16x16x32_bf16 v[28:31], a[8:11], v[236:239], v[28:31]\n"
      "s_waitcnt lgkmcnt(6)\n"
      "ds_read_b128 a[4:7], v7 offset:1024\n"
      "v_mfma_f32_16x16x32_bf16 v[20:23], a[12:15], v[132:135], v[20:23]\n"
      "buffer_store_dwordx2 v[90:91], v15, s[36:39], s52 offen offset:1536 sc1\n"
      "v_mfma_f32_16x16x32_bf16 v[76:79], a[84:87], a[68:71], 0\n"
      "s_waitcnt lgkmcnt(5)\n"
      "ds_read2_b64 a[68:71], v12 offset0:0 offset1:64\n"
      "s_nop 0\n"
      "v_mfma_f32_16x16x32_bf16 v[68:71], a[84:87], a[60:63], 0\n"
      "s_mov_b32 s52, s13\n"
      "ds_read2_b64 a[60:63], v12 offset0:1 offset1:65\n"
      "s_nop 0\n"
      "v_mfma_f32_16x16x32_bf16 v[20:23], a[16:19], v[188:191], v[20:23]\n"
      "s_add_u32 s13, s13, s12\n"
      "ds_read_b128 a[8:11], v7 offset:1280\n"
      "v_cvt_pk_bf16_f32 v81, v76, v77\n"
      "v_mfma_f32_16x16x32_bf16 v[20:23], a[20:23], a[240:243], v[20:23]\n"
      "v_cvt_pk_bf16_f32 v82, v68, v69\n"
      "v_cvt_pk_bf16_f32 v87, v78, v79\n"
      "v_mfma_f32_16x16x32_bf16 v[24:27], a[12:15], v[140:143], v[24:27]\n"
      "s_waitcnt lgkmcnt(5)\n"
      "v_cvt_pk_bf16_f32 v88, v70, v71\n"
      "s_nop 0\n"
      "v_mfma_f32_16x16x32_bf16 v[72:75], a[84:87], a[64:67], 0\n"
      "v_mfma_f32_16x16x32_bf16 v[24:27], a[16:19], a[236:239], v[24:27]\n"
      "v_cvt_pk_bf16_f32 v20, v20, v21\n"
      "v_cvt_pk_bf16_f32 v21, v22, v23\n"
      "v_mfma_f32_16x16x32_bf16 v[24:27], a[20:23], v[248:251], v[24:27]\n"
      "ds_write_addtid_b32 v20 offset:62480\n"
      "s_nop 0\n"
      "v_mfma_f32_16x16x32_bf16 v[28:31], a[12:15], v[144:147], v[28:31]\n"
      "s_waitcnt lgkmcnt(3)\n"
      "ds_read_b128 a[12:15], v7 offset:5376\n"
      "v_cvt_pk_bf16_f32 v83, v72, v73\n"
      "v_mfma_f32_16x16x32_bf16 v[76:79], a[84:87], a[68:71], 0\n"
      "buffer_store_dwordx4 v[80:83], v14, s[36:39], s52 offen sc1\n"
      "v_cvt_pk_bf16_f32 v89, v74, v75\n"
      "v_mfma_f32_16x16x32_bf16 v[28:31], a[16:19], v[200:203], v[28:31]\n"
      "ds_read_b128 a[16:19], v7 offset:5632\n"
      "v_cvt_pk_bf16_f32 v24, v24, v25\n"
      "v_mfma_f32_16x16x32_bf16 v[28:31], a[20:23], a[244:247], v[28:31]\n"
      "ds_read_b128 a[20:23], v7 offset:5888\n"
      "v_cvt_pk_bf16_f32 v25, v26, v27\n"
      "v_mfma_f32_16x16x32_bf16 v[32:35], a[24:27], v[92:95], 0\n"
      "s_waitcnt lgkmcnt(4)\n"
      "ds_write_addtid_b32 v21 offset:62736\n"
      "v_cvt_pk_bf16_f32 v84, v76, v77\n"
      "v_mfma_f32_16x16x32_bf16 v[68:71], a[84:87], a[60:63], 0\n"
      "ds_write_addtid_b32 v24 offset:50184\n"
      "v_cvt_pk_bf16_f32 v90, v78, v79\n"
      "v_mfma_f32_16x16x32_bf16 v[32:35], a[28:31], v[148:151], v[32:35]\n"
      "buffer_store_dwordx4 v[86:89], v14, s[36:39], s52 offen offset:1536 sc1\n"
      "v_cvt_pk_bf16_f32 v28, v28, v29\n"
      "v_mfma_f32_16x16x32_bf16 v[32:35], a[32:35], v[204:207], v[32:35]\n"
      "ds_write_addtid_b32 v25 offset:50440\n"
      "v_cvt_pk_bf16_f32 v29, v30, v31\n"
      "v_mfma_f32_16x16x32_bf16 v[32:35], a[0:3], v[112:115], v[32:35]\n"
      "v_cvt_pk_bf16_f32 v85, v68, v69\n"
      "v_cvt_pk_bf16_f32 v91, v70, v71\n"
      "v_mfma_f32_16x16x32_bf16 v[32:35], a[4:7], a[208:211], v[32:35]\n"
      "ds_write_addtid_b32 v28 offset:37888\n"
      "v_mfma_f32_16x16x32_bf16 v[32:35], a[8:11], v[220:223], v[32:35]\n"
      "s_waitcnt lgkmcnt(5)\n"
      "ds_write_addtid_b32 v29 offset:38144\n"
      "v_mfma_f32_16x16x32_bf16 v[32:35], a[12:15], a[212:215], v[32:35]\n"
      "buffer_store_dwordx2 v[84:85], v15, s[36:39], s52 offen sc1\n"
      "v_mfma_f32_16x16x32_bf16 v[32:35], a[16:19], v[184:187], v[32:35]\n"
      "s_waitcnt lgkmcnt(5)\n"
      "v_mfma_f32_16x16x32_bf16 v[32:35], a[20:23], v[240:243], v[32:35]\n"
      "s_nop 1\n"
      "v_mfma_f32_16x16x32_bf16 v[20:23], a[24:27], v[100:103], 0\n"
      "s_nop 1\n"
      "v_mfma_f32_16x16x32_bf16 v[20:23], a[28:31], a[200:203], v[20:23]\n"
      "s_nop 0\n"
      "v_mfma_f32_16x16x32_bf16 v[20:23], a[32:35], v[208:211], v[20:23]\n"
      "buffer_store_dwordx2 v[90:91], v15, s[36:39], s52 offen offset:1536 sc1\n"
      "v_cvt_pk_bf16_f32 v32, v32, v33\n"
      "v_mfma_f32_16x16x32_bf16 v[20:23], a[0:3], a[204:207], v[20:23]\n"
      "ds_write_addtid_b32 v32 offset:61456\n"
      "v_cvt_pk_bf16_f32 v33, v34, v35\n"
      "v_mfma_f32_16x16x32_bf16 v[20:23], a[4:7], v[172:175], v[20:23]\n"
      "ds_write_addtid_b32 v33 offset:61712\n"
      "v_mfma_f32_16x16x32_bf16 v[20:23], a[8:11], v[228:231], v[20:23]\n"
      "v_mfma_f32_16x16x32_bf16 v[20:23], a[12:15], v[136:139], v[20:23]\n"
      "v_mfma_f32_16x16x32_bf16 v[20:23], a[16:19], v[192:195], v[20:23]\n"
      "v_mfma_f32_16x16x32_bf16 v[20:23], a[20:23], v[244:247], v[20:23]\n"
      "s_nop 1\n"
      "v_mfma_f32_16x16x32_bf16 v[24:27], a[24:27], a[196:199], 0\n"
      "ds_read_b128 a[24:27], v7 offset:4608\n"
      "s_nop 0\n"
      "v_mfma_f32_16x16x32_bf16 v[24:27], a[28:31], v[160:163], v[24:27]\n"
      "ds_read_b128 a[28:31], v7 offset:4864\n"
      "v_mfma_f32_16x16x32_bf16 v[24:27], a[32:35], v[216:219], v[24:27]\n"
      "ds_read_b128 a[32:35], v7 offset:5120\n"
      "v_cvt_pk_bf16_f32 v20, v20, v21\n"
      "v_mfma_f32_16x16x32_bf16 v[24:27], a[0:3], v[124:127], v[24:27]\n"
      "ds_read_b128 a[0:3], v6 offset:4608\n"
      "v_cvt_pk_bf16_f32 v21, v22, v23\n"
      "v_mfma_f32_16x16x32_bf16 v[24:27], a[4:7], v[180:183], v[24:27]\n"
      "ds_read_b128 a[4:7], v6 offset:4864\n"
      "v_mfma_f32_16x16x32_bf16 v[24:27], a[8:11], v[232:235], v[24:27]\n"
      "ds_read_b128 a[8:11], v6 offset:5120\n"
      "v_mfma_f32_16x16x32_bf16 v[24:27], a[12:15], a[216:219], v[24:27]\n"
      "ds_read_b128 a[12:15], v7 offset:0\n"
      "v_mfma_f32_16x16x32_bf16 v[24:27], a[16:19], v[196:199], v[24:27]\n"
      "ds_read_b128 a[16:19], v7 offset:256\n"
      "v_mfma_f32_16x16x32_bf16 v[24:27], a[20:23], v[252:255], v[24:27]\n"
      "s_waitcnt lgkmcnt(7)\n"
      "ds_read_b128 a[20:23], v7 offset:512\n"
      "s_nop 0\n"
      "v_mfma_f32_16x16x32_bf16 v[28:31], a[24:27], a[160:163], 0\n"
      "s_waitcnt lgkmcnt(7)\n"
      "ds_write_addtid_b32 v20 offset:49160\n"
      "ds_write_addtid_b32 v21 offset:49416\n"
      "v_mfma_f32_16x16x32_bf16 v[28:31], a[28:31], a[172:175], v[28:31]\n"
      "s_waitcnt lgkmcnt(8)\n"
      "v_cvt_pk_bf16_f32 v24, v24, v25\n"
      "v_cvt_pk_bf16_f32 v25, v26, v27\n"
      "v_mfma_f32_16x16x32_bf16 v[28:31], a[32:35], a[184:187], v[28:31]\n"
      "s_waitcnt lgkmcnt(7)\n"
      "ds_write_addtid_b32 v24 offset:36864\n"
      "v_mfma_f32_16x16x32_bf16 v[28:31], a[0:3], a[88:91], v[28:31]\n"
      "s_waitcnt lgkmcnt(7)\n"
      "ds_write_addtid_b32 v25 offset:37120\n"
      "v_mfma_f32_16x16x32_bf16 v[28:31], a[4:7], a[100:103], v[28:31]\n"
      "s_waitcnt lgkmcnt(7)\n"
      "v_mfma_f32_16x16x32_bf16 v[28:31], a[8:11], a[112:115], v[28:31]\n"
      "s_waitcnt lgkmcnt(6)\n"
      "v_mfma_f32_16x16x32_bf16 v[28:31], a[12:15], a[124:127], v[28:31]\n"
      "s_waitcnt lgkmcnt(5)\n"
      "v_mfma_f32_16x16x32_bf16 v[28:31], a[16:19], a[136:139], v[28:31]\n"
      "s_waitcnt lgkmcnt(4)\n"
      "v_mfma_f32_16x16x32_bf16 v[28:31], a[20:23], a[148:151], v[28:31]\n"
      "s_nop 1\n"
      "v_mfma_f32_16x16x32_bf16 v[20:23], a[24:27], a[164:167], 0\n"
      "s_nop 1\n"
      "v_mfma_f32_16x16x32_bf16 v[20:23], a[28:31], a[176:179], v[20:23]\n"
      "s_nop 0\n"
      "v_mfma_f32_16x16x32_bf16 v[20:23], a[32:35], a[188:191], v[20:23]\n"
      "v_cvt_pk_bf16_f32 v28, v28, v29\n"
      "v_cvt_pk_bf16_f32 v29, v30, v31\n"
      "v_mfma_f32_16x16x32_bf16 v[20:23], a[0:3], a[92:95], v[20:23]\n"
      "ds_write_addtid_b32 v28 offset:60432\n"
      "v_mfma_f32_16x16x32_bf16 v[20:23], a[4:7], a[104:107], v[20:23]\n"
      "ds_write_addtid_b32 v29 offset:60688\n"
      "v_mfma_f32_16x16x32_bf16 v[20:23], a[8:11], a[116:119], v[20:23]\n"
      "v_mfma_f32_16x16x32_bf16 v[20:23], a[12:15], a[128:131], v[20:23]\n"
      "v_mfma_f32_16x16x32_bf16 v[20:23], a[16:19], a[140:143], v[20:23]\n"
      "v_mfma_f32_16x16x32_bf16 v[20:23], a[20:23], a[152:155], v[20:23]\n"
      "s_nop 1\n"
      "v_mfma_f32_16x16x32_bf16 v[32:35], a[24:27], a[168:171], 0\n"
      "s_nop 1\n"
      "v_mfma_f32_16x16x32_bf16 v[32:35], a[28:31], a[180:183], v[32:35]\n"
      "s_nop 0\n"
      "v_mfma_f32_16x16x32_bf16 v[32:35], a[32:35], a[192:195], v[32:35]\n"
      "v_cvt_pk_bf16_f32 v20, v20, v21\n"
      "v_cvt_pk_bf16_f32 v21, v22, v23\n"
      "v_mfma_f32_16x16x32_bf16 v[32:35], a[0:3], a[96:99], v[32:35]\n"
      "ds_write_addtid_b32 v20 offset:48136\n"
      "v_mfma_f32_16x16x32_bf16 v[32:35], a[4:7], a[108:111], v[32:35]\n"
      "ds_write_addtid_b32 v21 offset:48392\n"
      "v_mfma_f32_16x16x32_bf16 v[32:35], a[8:11], a[120:123], v[32:35]\n"
      "v_mfma_f32_16x16x32_bf16 v[32:35], a[12:15], a[132:135], v[32:35]\n"
      "v_mfma_f32_16x16x32_bf16 v[32:35], a[16:19], a[144:147], v[32:35]\n"
      "v_mfma_f32_16x16x32_bf16 v[32:35], a[20:23], a[156:159], v[32:35]\n"
      "s_nop 1\n"
      "s_nop 5\n"
      "v_cvt_pk_bf16_f32 v32, v32, v33\n"
      "v_cvt_pk_bf16_f32 v33, v34, v35\n"
      "ds_write_addtid_b32 v32 offset:35840\n"
      "ds_write_addtid_b32 v33 offset:36096\n"
      "s_waitcnt lgkmcnt(0)\n"
      "s_barrier\n"
      "ds_read2_b64 a[0:3], v11 offset0:0 offset1:64\n"
      "ds_read2_b64 a[4:7], v11 offset0:1 offset1:65\n"
      "ds_read2_b64 a[8:11], v11 offset0:2 offset1:66\n"
      "ds_read2_b64 a[12:15], v11 offset0:3 offset1:67\n"
      "ds_read2_b64 a[16:19], v13 offset0:0 offset1:64\n"
      "ds_read2_b64 a[20:23], v13 offset0:1 offset1:65\n"
      "s_waitcnt lgkmcnt(5)\n"
      "v_mfma_f32_16x16x32_bf16 v[72:75], a[84:87], a[0:3], 0\n"
      "s_waitcnt lgkmcnt(4)\n"
      "v_mfma_f32_16x16x32_bf16 v[76:79], a[84:87], a[4:7], 0\n"
      "s_waitcnt lgkmcnt(3)\n"
      "v_mfma_f32_16x16x32_bf16 v[68:71], a[84:87], a[8:11], 0\n"
      "s_waitcnt lgkmcnt(2)\n"
      "s_nop 2\n"
      "v_cvt_pk_bf16_f32 v80, v72, v73\n"
      "v_cvt_pk_bf16_f32 v86, v74, v75\n"
      "v_mfma_f32_16x16x32_bf16 v[72:75], a[84:87], a[12:15], 0\n"
      "s_waitcnt lgkmcnt(1)\n"
      "v_cvt_pk_bf16_f32 v81, v76, v77\n"
      "v_cvt_pk_bf16_f32 v87, v78, v79\n"
      "v_cvt_pk_bf16_f32 v82, v68, v69\n"
      "v_cvt_pk_bf16_f32 v88, v70, v71\n"
      "v_mfma_f32_16x16x32_bf16 v[76:79], a[84:87], a[16:19], 0\n"
      "s_waitcnt lgkmcnt(0)\n"
      "v_mfma_f32_16x16x32_bf16 v[68:71], a[84:87], a[20:23], 0\n"
      "s_mov_b32 s52, s13\n"
      "v_cvt_pk_bf16_f32 v83, v72, v73\n"
      "v_cvt_pk_bf16_f32 v89, v74, v75\n"
      "s_add_u32 s13, s13, s12\n"
      "buffer_store_dwordx4 v[80:83], v14, s[36:39], s52 offen sc1\n"
      "buffer_store_dwordx4 v[86:89], v14, s[36:39], s52 offen offset:1536 sc1\n"
      "v_cvt_pk_bf16_f32 v84, v76, v77\n"
      "v_cvt_pk_bf16_f32 v90, v78, v79\n"
      "v_cvt_pk_bf16_f32 v85, v68, v69\n"
      "v_cvt_pk_bf16_f32 v91, v70, v71\n"
      "buffer_store_dwordx2 v[84:85], v15, s[36:39], s52 offen sc1\n"
      "buffer_store_dwordx2 v[90:91], v15, s[36:39], s52 offen offset:1536 sc1\n"
      "Lconv_u0:\n"
      "s_waitcnt vmcnt(0)\n"
      "s_endpgm\n"
      ".p2align 12\n"
      "Lconv_pa1:\n"
      "s_cbranch_execz Lconv_r0\n"
      "s_nop 0\n"
      "Lconv_loop1:\n"
      "v_mfma_f32_16x16x32_bf16 v[20:23], a[24:27], v[96:99], 0\n"
      "s_add_u32 s51, s51, s12\n"
      "s_nop 1\n"
      "v_mfma_f32_16x16x32_bf16 v[20:23], a[28:31], v[152:155], v[20:23]\n"
      "s_sub_u32 s58, s93, s58\n"
      "v_cvt_pk_bf16_f32 v32, v32, v33\n"
      "v_cvt_pk_bf16_f32 v33, v34, v35\n"
      "v_mfma_f32_16x16x32_bf16 v[20:23], a[32:35], a[220:223], v[20:23]\n"
      "s_sub_u32 s59, s94, s59\n"
      "v_mfma_f32_16x16x32_bf16 v[20:23], a[0:3], v[116:119], v[20:23]\n"
      "s_sub_u32 s47, s51, s48\n"
      "v_mfma_f32_16x16x32_bf16 v[20:23], a[4:7], v[168:171], v[20:23]\n"
      "s_cmp_ge_u32 vcc_lo, 0x80000000\n"
      "ds_write_addtid_b32 v32 offset:34816\n"
      "v_mfma_f32_16x16x32_bf16 v[20:23], a[8:11], v[224:227], v[20:23]\n"
      "s_cselect_b32 s53, s47, s51\n"
      "v_mfma_f32_16x16x32_bf16 v[24:27], a[24:27], v[104:107], 0\n"
      "s_mov_b32 s52, s13\n"
      "ds_write_addtid_b32 v33 offset:35072\n"
      "v_mfma_f32_16x16x32_bf16 v[24:27], a[28:31], v[156:159], v[24:27]\n"
      "s_add_u32 s13, s13, s12\n"
      "v_mfma_f32_16x16x32_bf16 v[24:27], a[32:35], v[212:215], v[24:27]\n"
      "v_mfma_f32_16x16x32_bf16 v[24:27], a[0:3], v[120:123], v[24:27]\n"
      "v_mfma_f32_16x16x32_bf16 v[24:27], a[4:7], v[176:179], v[24:27]\n"
      "s_waitcnt vmcnt(15) lgkmcnt(0)\n"
      "s_barrier\n"
      "v_mfma_f32_16x16x32_bf16 v[24:27], a[8:11], a[232:235], v[24:27]\n"
      "s_mov_b32 m0, s58\n"
      "ds_read_b128 a[48:51], v2 offset:13280\n"
      "v_mfma_f32_16x16x32_bf16 v[28:31], a[24:27], v[108:111], 0\n"
      "ds_read_b128 a[56:59], v4 offset:13280\n"
      "v_mfma_f32_16x16x32_bf16 v[28:31], a[28:31], v[164:167], v[28:31]\n"
      "ds_read_b128 a[12:15], v7 offset:6144\n"
      "v_mfma_f32_16x16x32_bf16 v[28:31], a[32:35], a[224:227], v[28:31]\n"
      "ds_read_b128 a[16:19], v7 offset:6400\n"
      "v_mfma_f32_16x16x32_bf16 v[28:31], a[0:3], v[128:131], v[28:31]\n"
      "ds_read_b128 a[20:23], v7 offset:6656\n"
      "v_mfma_f32_16x16x32_bf16 v[28:31], a[4:7], a[228:231], v[28:31]\n"
      "ds_read_b128 a[52:55], v3 offset:13280\n"
      "v_mfma_f32_16x16x32_bf16 v[28:31], a[8:11], v[236:239], v[28:31]\n"
      "s_waitcnt lgkmcnt(5)\n"
      "ds_read2_b64 a[60:63], v10 offset0:0 offset1:64\n"
      "v_mfma_f32_16x16x32_bf16 v[52:55], a[48:51], a[72:75], 0\n"
      "s_waitcnt lgkmcnt(5)\n"
      "ds_read2_b64 a[64:67], v10 offset0:1 offset1:65\n"
      "v_mfma_f32_16x16x32_bf16 v[60:63], a[56:59], a[80:83], v[52:55]\n"
      "s_waitcnt lgkmcnt(5)\n"
      "ds_read_b128 a[24:27], v6 offset:5376\n"
      "v_mfma_f32_16x16x32_bf16 v[20:23], a[12:15], v[132:135], v[20:23]\n"
      "s_waitcnt lgkmcnt(5)\n"
      "ds_read_b128 a[28:31], v6 offset:5632\n"
      "v_mfma_f32_16x16x32_bf16 v[20:23], a[16:19], v[188:191], v[20:23]\n"
      "s_waitcnt lgkmcnt(5)\n"
      "ds_read_b128 a[32:35], v6 offset:5888\n"
      "v_cvt_pk_bf16_f32 v60, v60, v61\n"
      "v_mfma_f32_16x16x32_bf16 v[20:23], a[20:23], a[240:243], v[20:23]\n"
      "s_waitcnt lgkmcnt(5)\n"
      "ds_read_b128 a[0:3], v7 offset:768\n"
      "v_cvt_pk_bf16_f32 v61, v62, v63\n"
      "v_mfma_f32_16x16x32_bf16 v[56:59], a[52:55], a[76:79], 0\n"
      "ds_read_b128 a[4:7], v7 offset:1024\n"
      "s_nop 0\n"
      "v_mfma_f32_16x16x32_bf16 v[24:27], a[12:15], v[140:143], v[24:27]\n"
      "ds_read_b128 a[8:11], v7 offset:1280\n"
      "v_cvt_pk_bf16_f32 v20, v20, v21\n"
      "v_mfma_f32_16x16x32_bf16 v[24:27], a[16:19], a[236:239], v[24:27]\n"
      "buffer_load_dwordx4 v16, s[32:35], s53 offen lds\n"
      "v_cvt_pk_bf16_f32 v21, v22, v23\n"
      "v_mfma_f32_16x16x32_bf16 v[24:27], a[20:23], v[248:251], v[24:27]\n"
      "s_waitcnt lgkmcnt(6)\n"
      "ds_read_b128 a[36:39], v2 offset:13344\n"
      "v_fma_f32 v64, s44, v52, v56\n"
      "v_mfma_f32_16x16x32_bf16 v[68:71], a[84:87], a[60:63], 0\n"
      "s_mov_b32 m0, s45\n"
      "ds_read_b128 a[40:43], v3 offset:13344\n"
      "v_fma_f32 v65, s44, v53, v57\n"
      "v_mfma_f32_16x16x32_bf16 v[28:31], a[12:15], v[144:147], v[28:31]\n"
      "s_mov_b32 m0, s58\n"
      "ds_read_b128 a[12:15], v7 offset:5376\n"
      "v_cvt_pk_bf16_f32 v24, v24, v25\n"
      "v_mfma_f32_16x16x32_bf16 v[28:31], a[16:19], v[200:203], v[28:31]\n"
      "ds_read_b128 a[16:19], v7 offset:5632\n"
      "v_cvt_pk_bf16_f32 v25, v26, v27\n"
      "v_mfma_f32_16x16x32_bf16 v[28:31], a[20:23], a[244:247], v[28:31]\n"
      "ds_read_b128 a[20:23], v7 offset:5888\n"
      "v_cvt_pk_bf16_f32 v64, v64, v65\n"
      "v_mfma_f32_16x16x32_bf16 v[72:75], a[84:87], a[64:67], 0\n"
      "s_waitcnt lgkmcnt(6)\n"
      "ds_read_b128 a[44:47], v4 offset:13344\n"
      "v_fma_f32 v66, s44, v54, v58\n"
      "v_mfma_f32_16x16x32_bf16 v[32:35], a[24:27], v[92:95], 0\n"
      "ds_read2_b64 a[68:71], v10 offset0:2 offset1:66\n"
      "v_cvt_pk_bf16_f32 v28, v28, v29\n"
      "v_mfma_f32_16x16x32_bf16 v[32:35], a[28:31], v[148:151], v[32:35]\n"
      "ds_read2_b64 a[60:63], v10 offset0:3 offset1:67\n"
      "v_cvt_pk_bf16_f32 v29, v30, v31\n"
      "v_mfma_f32_16x16x32_bf16 v[32:35], a[32:35], v[204:207], v[32:35]\n"
      "buffer_load_dwordx4 v17, s[32:35], s53 offen offset:1024 lds\n"
      "v_fma_f32 v67, s44, v55, v59\n"
      "v_mfma_f32_16x16x32_bf16 v[32:35], a[0:3], v[112:115], v[32:35]\n"
      "s_mov_b32 m0, s45\n"
      "v_cvt_pk_bf16_f32 v65, v66, v67\n"
      "v_fma_f32 v56, s44, v52, -v56\n"
      "v_mfma_f32_16x16x32_bf16 v[32:35], a[4:7], a[208:211], v[32:35]\n"
      "s_waitcnt lgkmcnt(5)\n"
      "ds_write_addtid_b32 v20 offset:62480\n"
      "v_fma_f32 v57, s44, v53, -v57\n"
      "v_mfma_f32_16x16x32_bf16 v[32:35], a[8:11], v[220:223], v[32:35]\n"
      "ds_write_addtid_b32 v21 offset:62736\n"
      "v_cvt_pk_bf16_f32 v56, v56, v57\n"
      "v_mfma_f32_16x16x32_bf16 v[32:35], a[12:15], a[212:215], v[32:35]\n"
      "s_waitcnt lgkmcnt(5)\n"
      "ds_write_addtid_b32 v24 offset:50184\n"
      "v_fma_f32 v58, s44, v54, -v58\n"
      "v_mfma_f32_16x16x32_bf16 v[32:35], a[16:19], v[184:187], v[32:35]\n"
      "ds_write_addtid_b32 v25 offset:50440\n"
      "v_fma_f32 v59, s44, v55, -v59\n"
      "v_mfma_f32_16x16x32_bf16 v[32:35], a[20:23], v[240:243], v[32:35]\n"
      "ds_write_addtid_b32 v28 offset:37888\n"
      "v_cvt_pk_bf16_f32 v57, v58, v59\n"
      "v_mfma_f32_16x16x32_bf16 v[36:39], a[36:39], a[72:75], 0\n"
      "ds_write_addtid_b32 v29 offset:38144\n"
      "v_cvt_pk_bf16_f32 v80, v68, v69\n"
      "v_mfma_f32_16x16x32_bf16 v[40:43], a[40:43], a[76:79], 0\n"
      "s_waitcnt lgkmcnt(6)\n"
      "ds_read_b128 a[48:51], v2 offset:13408\n"
      "v_cvt_pk_bf16_f32 v32, v32, v33\n"
      "v_mfma_f32_16x16x32_bf16 v[44:47], a[44:47], a[80:83], v[36:39]\n"
      "s_mov_b32 m0, s18\n"
      "ds_read_b128 a[56:59], v4 offset:13408\n"
      "v_cvt_pk_bf16_f32 v86, v70, v71\n"
      "v_mfma_f32_16x16x32_bf16 v[76:79], a[84:87], a[68:71], 0\n"
      "ds_write_addtid_b32 v64 offset:768\n"
      "v_cvt_pk_bf16_f32 v33, v34, v35\n"
      "v_mfma_f32_16x16x32_bf16 v[68:71], a[84:87], a[60:63], 0\n"
      "ds_write_addtid_b32 v65 offset:37696\n"
      "v_cvt_pk_bf16_f32 v81, v72, v73\n"
      "v_mfma_f32_16x16x32_bf16 v[20:23], a[24:27], v[100:103], 0\n"
      "ds_write_addtid_b32 v56 offset:1536\n"
      "v_fma_f32 v48, s44, v36, v40\n"
      "v_mfma_f32_16x16x32_bf16 v[20:23], a[28:31], a[200:203], v[20:23]\n"
      "ds_write_addtid_b32 v57 offset:38464\n"
      "v_cvt_pk_bf16_f32 v82, v76, v77\n"
      "v_mfma_f32_16x16x32_bf16 v[20:23], a[32:35], v[208:211], v[20:23]\n"
      "ds_write_addtid_b32 v60 offset:0\n"
      "v_cvt_pk_bf16_f32 v83, v68, v69\n"
      "v_mfma_f32_16x16x32_bf16 v[20:23], a[0:3], a[204:207], v[20:23]\n"
      "ds_write_addtid_b32 v61 offset:36928\n"
      "v_fma_f32 v49, s44, v37, v41\n"
      "v_mfma_f32_16x16x32_bf16 v[20:23], a[4:7], v[172:175], v[20:23]\n"
      "s_mov_b32 m0, s45\n"
      "ds_read_b128 a[52:55], v3 offset:13408\n"
      "v_cvt_pk_bf16_f32 v48, v48, v49\n"
      "v_mfma_f32_16x16x32_bf16 v[20:23], a[8:11], v[228:231], v[20:23]\n"
      "s_mov_b32 m0, s58\n"
      "v_cvt_pk_bf16_f32 v87, v74, v75\n"
      "v_fma_f32 v50, s44, v38, v42\n"
      "v_mfma_f32_16x16x32_bf16 v[20:23], a[12:15], v[136:139], v[20:23]\n"
      "buffer_load_dwordx4 v18, s[32:35], s53 offen offset:2048 lds\n"
      "v_fma_f32 v51, s44, v39, v43\n"
      "v_mfma_f32_16x16x32_bf16 v[20:23], a[16:19], v[192:195], v[20:23]\n"
      "s_mov_b32 m0, s45\n"
      "ds_read2_b64 a[64:67], v12 offset0:0 offset1:64\n"
      "v_cvt_pk_bf16_f32 v49, v50, v51\n"
      "v_mfma_f32_16x16x32_bf16 v[20:23], a[20:23], v[244:247], v[20:23]\n"
      "s_waitcnt lgkmcnt(8)\n"
      "ds_write_addtid_b32 v32 offset:61456\n"
      "v_cvt_pk_bf16_f32 v88, v78, v79\n"
      "v_mfma_f32_16x16x32_bf16 v[52:55], a[48:51], a[72:75], 0\n"
      "ds_write_addtid_b32 v33 offset:61712\n"
      "v_cvt_pk_bf16_f32 v89, v70, v71\n"
      "v_mfma_f32_16x16x32_bf16 v[60:63], a[56:59], a[80:83], v[52:55]\n"
      "buffer_store_dwordx4 v[80:83], v14, s[36:39], s52 offen sc1\n"
      "v_fma_f32 v40, s44, v36, -v40\n"
      "v_mfma_f32_16x16x32_bf16 v[24:27], a[24:27], a[196:199], 0\n"
      "s_mov_b32 m0, s18\n"
      "ds_read_b128 a[24:27], v7 offset:4608\n"
      "v_fma_f32 v41, s44, v37, -v41\n"
      "v_mfma_f32_16x16x32_bf16 v[24:27], a[28:31], v[160:163], v[24:27]\n"
      "ds_read_b128 a[28:31], v7 offset:4864\n"
      "v_cvt_pk_bf16_f32 v40, v40, v41\n"
      "v_mfma_f32_16x16x32_bf16 v[24:27], a[32:35], v[216:219], v[24:27]\n"
      "ds_read_b128 a[32:35], v7 offset:5120\n"
      "v_fma_f32 v42, s44, v38, -v42\n"
      "v_mfma_f32_16x16x32_bf16 v[24:27], a[0:3], v[124:127], v[24:27]\n"
      "ds_read_b128 a[0:3], v6 offset:4608\n"
      "v_fma_f32 v43, s44, v39, -v43\n"
      "v_mfma_f32_16x16x32_bf16 v[24:27], a[4:7], v[180:183], v[24:27]\n"
      "ds_read_b128 a[4:7], v6 offset:4864\n"
      "v_cvt_pk_bf16_f32 v41, v42, v43\n"
      "v_mfma_f32_16x16x32_bf16 v[24:27], a[8:11], v[232:235], v[24:27]\n"
      "ds_read_b128 a[8:11], v6 offset:5120\n"
      "v_cvt_pk_bf16_f32 v44, v44, v45\n"
      "v_mfma_f32_16x16x32_bf16 v[24:27], a[12:15], a[216:219], v[24:27]\n"
      "ds_read_b128 a[12:15], v7 offset:0\n"
      "v_cvt_pk_bf16_f32 v45, v46, v47\n"
      "v_mfma_f32_16x16x32_bf16 v[24:27], a[16:19], v[196:199], v[24:27]\n"
      "ds_read_b128 a[16:19], v7 offset:256\n"
      "v_cvt_pk_bf16_f32 v20, v20, v21\n"
      "v_mfma_f32_16x16x32_bf16 v[24:27], a[20:23], v[252:255], v[24:27]\n"
      "s_waitcnt lgkmcnt(7)\n"
      "ds_read_b128 a[20:23], v7 offset:512\n"
      "v_cvt_pk_bf16_f32 v21, v22, v23\n"
      "v_mfma_f32_16x16x32_bf16 v[56:59], a[52:55], a[76:79], 0\n"
      "ds_write_addtid_b32 v48 offset:1024\n"
      "v_cvt_pk_bf16_f32 v60, v60, v61\n"
      "v_mfma_f32_16x16x32_bf16 v[28:31], a[24:27], a[160:163], 0\n"
      "s_waitcnt lgkmcnt(7)\n"
      "ds_write_addtid_b32 v49 offset:37952\n"
      "v_cvt_pk_bf16_f32 v24, v24, v25\n"
      "v_mfma_f32_16x16x32_bf16 v[28:31], a[28:31], a[172:175], v[28:31]\n"
      "buffer_store_dwordx4 v[86:89], v14, s[36:39], s52 offen offset:1536 sc1\n"
      "v_cvt_pk_bf16_f32 v25, v26, v27\n"
      "v_mfma_f32_16x16x32_bf16 v[28:31], a[32:35], a[184:187], v[28:31]\n"
      "s_waitcnt lgkmcnt(6)\n"
      "ds_write_addtid_b32 v40 offset:1792\n"
      "v_fma_f32 v64, s44, v52, v56\n"
      "v_mfma_f32_16x16x32_bf16 v[28:31], a[0:3], a[88:91], v[28:31]\n"
      "ds_write_addtid_b32 v41 offset:38720\n"
      "v_fma_f32 v65, s44, v53, v57\n"
      "v_mfma_f32_16x16x32_bf16 v[28:31], a[4:7], a[100:103], v[28:31]\n"
      "s_waitcnt lgkmcnt(6)\n"
      "ds_write_addtid_b32 v44 offset:256\n"
      "v_cvt_pk_bf16_f32 v64, v64, v65\n"
      "v_mfma_f32_16x16x32_bf16 v[28:31], a[8:11], a[112:115], v[28:31]\n"
      "ds_write_addtid_b32 v45 offset:37184\n"
      "v_fma_f32 v66, s44, v54, v58\n"
      "v_mfma_f32_16x16x32_bf16 v[28:31], a[12:15], a[124:127], v[28:31]\n"
      "s_waitcnt lgkmcnt(6)\n"
      "ds_read2_b64 a[68:71], v12 offset0:1 offset1:65\n"
      "v_fma_f32 v67, s44, v55, v59\n"
      "v_mfma_f32_16x16x32_bf16 v[28:31], a[16:19], a[136:139], v[28:31]\n"
      "s_mov_b32 m0, s45\n"
      "v_cvt_pk_bf16_f32 v65, v66, v67\n"
      "v_fma_f32 v56, s44, v52, -v56\n"
      "v_mfma_f32_16x16x32_bf16 v[28:31], a[20:23], a[148:151], v[28:31]\n"
      "ds_write_addtid_b32 v20 offset:49160\n"
      "v_fma_f32 v57, s44, v53, -v57\n"
      "v_mfma_f32_16x16x32_bf16 v[72:75], a[84:87], a[64:67], 0\n"
      "ds_write_addtid_b32 v21 offset:49416\n"
      "v_cvt_pk_bf16_f32 v56, v56, v57\n"
      "v_mfma_f32_16x16x32_bf16 v[20:23], a[24:27], a[164:167], 0\n"
      "ds_write_addtid_b32 v24 offset:36864\n"
      "v_fma_f32 v58, s44, v54, -v58\n"
      "v_mfma_f32_16x16x32_bf16 v[20:23], a[28:31], a[176:179], v[20:23]\n"
      "ds_write_addtid_b32 v25 offset:37120\n"
      "v_cvt_pk_bf16_f32 v28, v28, v29\n"
      "v_mfma_f32_16x16x32_bf16 v[20:23], a[32:35], a[188:191], v[20:23]\n"
      "ds_write_addtid_b32 v28 offset:60432\n"
      "v_cvt_pk_bf16_f32 v29, v30, v31\n"
      "v_mfma_f32_16x16x32_bf16 v[20:23], a[0:3], a[92:95], v[20:23]\n"
      "ds_write_addtid_b32 v29 offset:60688\n"
      "v_fma_f32 v59, s44, v55, -v59\n"
      "v_mfma_f32_16x16x32_bf16 v[20:23], a[4:7], a[104:107], v[20:23]\n"
      "s_mov_b32 m0, s18\n"
      "v_cvt_pk_bf16_f32 v57, v58, v59\n"
      "v_cvt_pk_bf16_f32 v61, v62, v63\n"
      "v_mfma_f32_16x16x32_bf16 v[20:23], a[8:11], a[116:119], v[20:23]\n"
      "ds_write_addtid_b32 v64 offset:1280\n"
      "v_add_u32_e32 v2, s91, v2\n"
      "v_mfma_f32_16x16x32_bf16 v[20:23], a[12:15], a[128:131], v[20:23]\n"
      "ds_write_addtid_b32 v65 offset:38208\n"
      "v_add_u32_e64 v4, s91, v4\n"
      "v_mfma_f32_16x16x32_bf16 v[20:23], a[16:19], a[140:143], v[20:23]\n"
      "ds_write_addtid_b32 v56 offset:2048\n"
      "v_cvt_pk_bf16_f32 v84, v72, v73\n"
      "v_mfma_f32_16x16x32_bf16 v[20:23], a[20:23], a[152:155], v[20:23]\n"
      "s_waitcnt lgkmcnt(9)\n"
      "ds_write_addtid_b32 v57 offset:38976\n"
      "v_swap_b32 v6, v7\n"
      "v_mfma_f32_16x16x32_bf16 v[76:79], a[84:87], a[68:71], 0\n"
      "ds_write_addtid_b32 v60 offset:512\n"
      "v_cvt_pk_bf16_f32 v90, v74, v75\n"
      "v_mfma_f32_16x16x32_bf16 v[32:35], a[24:27], a[168:171], 0\n"
      "ds_write_addtid_b32 v61 offset:37440\n"
      "v_cvt_pk_bf16_f32 v20, v20, v21\n"
      "v_mfma_f32_16x16x32_bf16 v[32:35], a[28:31], a[180:183], v[32:35]\n"
      "s_mov_b32 m0, s45\n"
      "v_cvt_pk_bf16_f32 v21, v22, v23\n"
      "v_cvt_pk_bf16_f32 v85, v76, v77\n"
      "v_mfma_f32_16x16x32_bf16 v[32:35], a[32:35], a[192:195], v[32:35]\n"
      "buffer_store_dwordx2 v[84:85], v15, s[36:39], s52 offen sc1\n"
      "v_cvt_pk_bf16_f32 v91, v78, v79\n"
      "v_mfma_f32_16x16x32_bf16 v[32:35], a[0:3], a[96:99], v[32:35]\n"
      "v_add_u32_e32 v3, s91, v3\n"
      "v_mfma_f32_16x16x32_bf16 v[32:35], a[4:7], a[108:111], v[32:35]\n"
      "s_sub_u32 s91, 0, s91\n"
      "ds_write_addtid_b32 v20 offset:48136\n"
      "v_mfma_f32_16x16x32_bf16 v[32:35], a[8:11], a[120:123], v[32:35]\n"
      "ds_write_addtid_b32 v21 offset:48392\n"
      "v_mfma_f32_16x16x32_bf16 v[32:35], a[12:15], a[132:135], v[32:35]\n"
      "buffer_store_dwordx2 v[90:91], v15, s[36:39], s52 offen offset:1536 sc1\n"
      "v_mfma_f32_16x16x32_bf16 v[32:35], a[16:19], a[144:147], v[32:35]\n"
      "v_mfma_f32_16x16x32_bf16 v[32:35], a[20:23], a[156:159], v[32:35]\n"
      "s_nop 1\n"
      "v_mfma_f32_16x16x32_bf16 v[20:23], a[12:15], a[88:91], 0\n"
      "s_add_u32 s51, s51, s12\n"
      "s_nop 1\n"
      "v_mfma_f32_16x16x32_bf16 v[20:23], a[16:19], a[100:103], v[20:23]\n"
      "s_sub_u32 s47, s51, s48\n"
      "v_cvt_pk_bf16_f32 v32, v32, v33\n"
      "v_cvt_pk_bf16_f32 v33, v34, v35\n"
      "v_mfma_f32_16x16x32_bf16 v[20:23], a[20:23], a[112:115], v[20:23]\n"
      "s_cmp_ge_u32 vcc_lo, 0x40000000\n"
      "v_mfma_f32_16x16x32_bf16 v[20:23], a[24:27], a[124:127], v[20:23]\n"
      "s_cselect_b32 s53, s47, s51\n"
      "ds_write_addtid_b32 v32 offset:35840\n"
      "v_mfma_f32_16x16x32_bf16 v[20:23], a[28:31], a[136:139], v[20:23]\n"
      "s_mov_b32 s52, s13\n"
      "v_mfma_f32_16x16x32_bf16 v[20:23], a[32:35], a[148:151], v[20:23]\n"
      "s_mul_i32 vcc_lo, vcc_lo, 2\n"
      "ds_write_addtid_b32 v33 offset:36096\n"
      "v_mfma_f32_16x16x32_bf16 v[24:27], a[12:15], a[92:95], 0\n"
      "v_mfma_f32_16x16x32_bf16 v[24:27], a[16:19], a[104:107], v[24:27]\n"
      "v_mfma_f32_16x16x32_bf16 v[24:27], a[20:23], a[116:119], v[24:27]\n"
      "v_mfma_f32_16x16x32_bf16 v[24:27], a[24:27], a[128:131], v[24:27]\n"
      "v_mfma_f32_16x16x32_bf16 v[24:27], a[28:31], a[140:143], v[24:27]\n"
      "s_waitcnt vmcnt(19) lgkmcnt(0)\n"
      "s_barrier\n"
      "v_mfma_f32_16x16x32_bf16 v[24:27], a[32:35], a[152:155], v[24:27]\n"
      "s_add_u32 m0, s58, 13280\n"
      "ds_read_b128 a[36:39], v2 offset:0\n"
      "v_mfma_f32_16x16x32_bf16 v[28:31], a[12:15], a[96:99], 0\n"
      "ds_read_b128 a[44:47], v4 offset:0\n"
      "v_mfma_f32_16x16x32_bf16 v[28:31], a[16:19], a[108:111], v[28:31]\n"
      "ds_read_b128 a[0:3], v7 offset:0\n"
      "v_mfma_f32_16x16x32_bf16 v[28:31], a[20:23], a[120:123], v[28:31]\n"
      "ds_read_b128 a[4:7], v7 offset:256\n"
      "v_mfma_f32_16x16x32_bf16 v[28:31], a[24:27], a[132:135], v[28:31]\n"
      "ds_read_b128 a[8:11], v7 offset:512\n"
      "v_mfma_f32_16x16x32_bf16 v[28:31], a[28:31], a[144:147], v[28:31]\n"
      "ds_read_b128 a[40:43], v3 offset:0\n"
      "v_mfma_f32_16x16x32_bf16 v[28:31], a[32:35], a[156:159], v[28:31]\n"
      "s_waitcnt lgkmcnt(5)\n"
      "ds_read2_b64 a[60:63], v11 offset0:0 offset1:64\n"
      "v_mfma_f32_16x16x32_bf16 v[36:39], a[36:39], a[72:75], 0\n"
      "s_waitcnt lgkmcnt(5)\n"
      "ds_read2_b64 a[64:67], v11 offset0:1 offset1:65\n"
      "v_mfma_f32_16x16x32_bf16 v[44:47], a[44:47], a[80:83], v[36:39]\n"
      "s_waitcnt lgkmcnt(5)\n"
      "ds_read_b128 a[12:15], v6 offset:768\n"
      "v_mfma_f32_16x16x32_bf16 v[20:23], a[0:3], a[160:163], v[20:23]\n"
      "s_waitcnt lgkmcnt(5)\n"
      "ds_read_b128 a[16:19], v6 offset:1024\n"
      "v_mfma_f32_16x16x32_bf16 v[20:23], a[4:7], a[172:175], v[20:23]\n"
      "s_waitcnt lgkmcnt(5)\n"
      "ds_read_b128 a[20:23], v6 offset:1280\n"
      "v_cvt_pk_bf16_f32 v44, v44, v45\n"
      "v_mfma_f32_16x16x32_bf16 v[20:23], a[8:11], a[184:187], v[20:23]\n"
      "s_waitcnt lgkmcnt(5)\n"
      "ds_read_b128 a[24:27], v6 offset:5376\n"
      "v_cvt_pk_bf16_f32 v45, v46, v47\n"
      "v_mfma_f32_16x16x32_bf16 v[40:43], a[40:43], a[76:79], 0\n"
      "ds_read_b128 a[28:31], v6 offset:5632\n"
      "s_nop 0\n"
      "v_mfma_f32_16x16x32_bf16 v[24:27], a[0:3], a[164:167], v[24:27]\n"
      "ds_read_b128 a[32:35], v6 offset:5888\n"
      "v_cvt_pk_bf16_f32 v20, v20, v21\n"
      "v_mfma_f32_16x16x32_bf16 v[24:27], a[4:7], a[176:179], v[24:27]\n"
      "buffer_load_dwordx4 v16, s[32:35], s53 offen lds\n"
      "v_cvt_pk_bf16_f32 v21, v22, v23\n"
      "v_mfma_f32_16x16x32_bf16 v[24:27], a[8:11], a[188:191], v[24:27]\n"
      "s_waitcnt lgkmcnt(6)\n"
      "ds_read_b128 a[48:51], v2 offset:64\n"
      "v_fma_f32 v48, s44, v36, v40\n"
      "v_mfma_f32_16x16x32_bf16 v[68:71], a[84:87], a[60:63], 0\n"
      "s_mov_b32 m0, s45\n"
      "ds_read_b128 a[52:55], v3 offset:64\n"
      "v_fma_f32 v49, s44, v37, v41\n"
      "v_mfma_f32_16x16x32_bf16 v[28:31], a[0:3], a[168:171], v[28:31]\n"
      "s_add_u32 m0, s58, 13280\n"
      "ds_read_b128 a[0:3], v7 offset:768\n"
      "v_cvt_pk_bf16_f32 v24, v24, v25\n"
      "v_mfma_f32_16x16x32_bf16 v[28:31], a[4:7], a[180:183], v[28:31]\n"
      "s_add_u32 s13, s13, s12\n"
      "ds_read_b128 a[4:7], v7 offset:1024\n"
      "v_cvt_pk_bf16_f32 v25, v26, v27\n"
      "v_mfma_f32_16x16x32_bf16 v[28:31], a[8:11], a[192:195], v[28:31]\n"
      "ds_read_b128 a[8:11], v7 offset:1280\n"
      "v_cvt_pk_bf16_f32 v48, v48, v49\n"
      "v_mfma_f32_16x16x32_bf16 v[72:75], a[84:87], a[64:67], 0\n"
      "s_waitcnt lgkmcnt(6)\n"
      "ds_read_b128 a[56:59], v4 offset:64\n"
      "v_fma_f32 v50, s44, v38, v42\n"
      "v_mfma_f32_16x16x32_bf16 v[32:35], a[12:15], v[92:95], 0\n"
      "ds_read2_b64 a[68:71], v11 offset0:2 offset1:66\n"
      "v_cvt_pk_bf16_f32 v28, v28, v29\n"
      "v_mfma_f32_16x16x32_bf16 v[32:35], a[16:19], v[148:151], v[32:35]\n"
      "ds_read2_b64 a[60:63], v11 offset0:3 offset1:67\n"
      "v_cvt_pk_bf16_f32 v29, v30, v31\n"
      "v_mfma_f32_16x16x32_bf16 v[32:35], a[20:23], v[204:207], v[32:35]\n"
      "buffer_load_dwordx4 v17, s[32:35], s53 offen offset:1024 lds\n"
      "v_fma_f32 v51, s44, v39, v43\n"
      "v_mfma_f32_16x16x32_bf16 v[32:35], a[24:27], v[112:115], v[32:35]\n"
      "s_mov_b32 m0, s45\n"
      "v_cvt_pk_bf16_f32 v49, v50, v51\n"
      "v_fma_f32 v40, s44, v36, -v40\n"
      "v_mfma_f32_16x16x32_bf16 v[32:35], a[28:31], a[208:211], v[32:35]\n"
      "s_waitcnt lgkmcnt(5)\n"
      "ds_write_addtid_b32 v20 offset:57360\n"
      "v_fma_f32 v41, s44, v37, -v41\n"
      "v_mfma_f32_16x16x32_bf16 v[32:35], a[32:35], v[220:223], v[32:35]\n"
      "ds_write_addtid_b32 v21 offset:57616\n"
      "v_cvt_pk_bf16_f32 v40, v40, v41\n"
      "v_mfma_f32_16x16x32_bf16 v[32:35], a[0:3], a[212:215], v[32:35]\n"
      "s_waitcnt lgkmcnt(5)\n"
      "ds_write_addtid_b32 v24 offset:45064\n"
      "v_fma_f32 v42, s44, v38, -v42\n"
      "v_mfma_f32_16x16x32_bf16 v[32:35], a[4:7], v[184:187], v[32:35]\n"
      "ds_write_addtid_b32 v25 offset:45320\n"
      "v_fma_f32 v43, s44, v39, -v43\n"
      "v_mfma_f32_16x16x32_bf16 v[32:35], a[8:11], v[240:243], v[32:35]\n"
      "ds_write_addtid_b32 v28 offset:32768\n"
      "v_cvt_pk_bf16_f32 v41, v42, v43\n"
      "v_mfma_f32_16x16x32_bf16 v[52:55], a[48:51], a[72:75], 0\n"
      "ds_write_addtid_b32 v29 offset:33024\n"
      "v_cvt_pk_bf16_f32 v80, v68, v69\n"
      "v_mfma_f32_16x16x32_bf16 v[56:59], a[52:55], a[76:79], 0\n"
      "s_waitcnt lgkmcnt(6)\n"
      "ds_read_b128 a[36:39], v2 offset:128\n"
      "v_cvt_pk_bf16_f32 v32, v32, v33\n"
      "v_mfma_f32_16x16x32_bf16 v[60:63], a[56:59], a[80:83], v[52:55]\n"
      "s_mov_b32 m0, s18\n"
      "ds_read_b128 a[44:47], v4 offset:128\n"
      "v_cvt_pk_bf16_f32 v86, v70, v71\n"
      "v_mfma_f32_16x16x32_bf16 v[76:79], a[84:87], a[68:71], 0\n"
      "ds_write_addtid_b32 v48 offset:5376\n"
      "v_cvt_pk_bf16_f32 v33, v34, v35\n"
      "v_mfma_f32_16x16x32_bf16 v[68:71], a[84:87], a[60:63], 0\n"
      "ds_write_addtid_b32 v49 offset:42304\n"
      "v_cvt_pk_bf16_f32 v81, v72, v73\n"
      "v_mfma_f32_16x16x32_bf16 v[20:23], a[12:15], v[100:103], 0\n"
      "ds_write_addtid_b32 v40 offset:6144\n"
      "v_fma_f32 v64, s44, v52, v56\n"
      "v_mfma_f32_16x16x32_bf16 v[20:23], a[16:19], a[200:203], v[20:23]\n"
      "ds_write_addtid_b32 v41 offset:43072\n"
      "v_cvt_pk_bf16_f32 v82, v76, v77\n"
      "v_mfma_f32_16x16x32_bf16 v[20:23], a[20:23], v[208:211], v[20:23]\n"
      "ds_write_addtid_b32 v44 offset:4608\n"
      "v_cvt_pk_bf16_f32 v83, v68, v69\n"
      "v_mfma_f32_16x16x32_bf16 v[20:23], a[24:27], a[204:207], v[20:23]\n"
      "ds_write_addtid_b32 v45 offset:41536\n"
      "v_fma_f32 v65, s44, v53, v57\n"
      "v_mfma_f32_16x16x32_bf16 v[20:23], a[28:31], v[172:175], v[20:23]\n"
      "s_mov_b32 m0, s45\n"
      "ds_read_b128 a[40:43], v3 offset:128\n"
      "v_cvt_pk_bf16_f32 v64, v64, v65\n"
      "v_mfma_f32_16x16x32_bf16 v[20:23], a[32:35], v[228:231], v[20:23]\n"
      "s_add_u32 m0, s58, 13280\n"
      "v_cvt_pk_bf16_f32 v87, v74, v75\n"
      "v_fma_f32 v66, s44, v54, v58\n"
      "v_mfma_f32_16x16x32_bf16 v[20:23], a[0:3], v[136:139], v[20:23]\n"
      "buffer_load_dwordx4 v18, s[32:35], s53 offen offset:2048 lds\n"
      "v_fma_f32 v67, s44, v55, v59\n"
      "v_mfma_f32_16x16x32_bf16 v[20:23], a[4:7], v[192:195], v[20:23]\n"
      "s_mov_b32 m0, s45\n"
      "ds_read2_b64 a[64:67], v13 offset0:0 offset1:64\n"
      "v_cvt_pk_bf16_f32 v65, v66, v67\n"
      "v_mfma_f32_16x16x32_bf16 v[20:23], a[8:11], v[244:247], v[20:23]\n"
      "s_waitcnt lgkmcnt(8)\n"
      "ds_write_addtid_b32 v32 offset:58384\n"
      "v_cvt_pk_bf16_f32 v88, v78, v79\n"
      "v_mfma_f32_16x16x32_bf16 v[36:39], a[36:39], a[72:75], 0\n"
      "ds_write_addtid_b32 v33 offset:58640\n"
      "v_cvt_pk_bf16_f32 v89, v70, v71\n"
      "v_mfma_f32_16x16x32_bf16 v[44:47], a[44:47], a[80:83], v[36:39]\n"
      "global_load_dword v8, v9, s[54:55]\n"
      "v_fma_f32 v56, s44, v52, -v56\n"
      "v_mfma_f32_16x16x32_bf16 v[24:27], a[12:15], a[196:199], 0\n"
      "s_mov_b32 m0, s18\n"
      "ds_read_b128 a[12:15], v6 offset:1536\n"
      "v_fma_f32 v57, s44, v53, -v57\n"
      "v_mfma_f32_16x16x32_bf16 v[24:27], a[16:19], v[160:163], v[24:27]\n"
      "ds_read_b128 a[16:19], v6 offset:1792\n"
      "v_cvt_pk_bf16_f32 v56, v56, v57\n"
      "v_mfma_f32_16x16x32_bf16 v[24:27], a[20:23], v[216:219], v[24:27]\n"
      "ds_read_b128 a[20:23], v6 offset:2048\n"
      "v_fma_f32 v58, s44, v54, -v58\n"
      "v_mfma_f32_16x16x32_bf16 v[24:27], a[24:27], v[124:127], v[24:27]\n"
      "ds_read_b128 a[24:27], v6 offset:6144\n"
      "v_fma_f32 v59, s44, v55, -v59\n"
      "v_mfma_f32_16x16x32_bf16 v[24:27], a[28:31], v[180:183], v[24:27]\n"
      "ds_read_b128 a[28:31], v6 offset:6400\n"
      "v_cvt_pk_bf16_f32 v57, v58, v59\n"
      "v_mfma_f32_16x16x32_bf16 v[24:27], a[32:35], v[232:235], v[24:27]\n"
      "ds_read_b128 a[32:35], v6 offset:6656\n"
      "v_cvt_pk_bf16_f32 v60, v60, v61\n"
      "v_mfma_f32_16x16x32_bf16 v[24:27], a[0:3], a[216:219], v[24:27]\n"
      "ds_read_b128 a[0:3], v7 offset:1536\n"
      "v_cvt_pk_bf16_f32 v61, v62, v63\n"
      "v_mfma_f32_16x16x32_bf16 v[24:27], a[4:7], v[196:199], v[24:27]\n"
      "ds_read_b128 a[4:7], v7 offset:1792\n"
      "v_cvt_pk_bf16_f32 v20, v20, v21\n"
      "v_mfma_f32_16x16x32_bf16 v[24:27], a[8:11], v[252:255], v[24:27]\n"
      "s_waitcnt lgkmcnt(7)\n"
      "ds_read_b128 a[8:11], v7 offset:2048\n"
      "v_cvt_pk_bf16_f32 v21, v22, v23\n"
      "v_mfma_f32_16x16x32_bf16 v[40:43], a[40:43], a[76:79], 0\n"
      "buffer_store_dwordx4 v[80:83], v14, s[36:39], s52 offen sc1\n"
      "v_cvt_pk_bf16_f32 v44, v44, v45\n"
      "v_mfma_f32_16x16x32_bf16 v[28:31], a[12:15], v[96:99], 0\n"
      "s_waitcnt lgkmcnt(6)\n"
      "ds_write_addtid_b32 v64 offset:5632\n"
      "v_cvt_pk_bf16_f32 v24, v24, v25\n"
      "v_mfma_f32_16x16x32_bf16 v[28:31], a[16:19], v[152:155], v[28:31]\n"
      "buffer_store_dwordx4 v[86:89], v14, s[36:39], s52 offen offset:1536 sc1\n"
      "v_cvt_pk_bf16_f32 v25, v26, v27\n"
      "v_mfma_f32_16x16x32_bf16 v[28:31], a[20:23], a[220:223], v[28:31]\n"
      "s_waitcnt lgkmcnt(5)\n"
      "ds_write_addtid_b32 v65 offset:42560\n"
      "v_fma_f32 v48, s44, v36, v40\n"
      "v_mfma_f32_16x16x32_bf16 v[28:31], a[24:27], v[116:119], v[28:31]\n"
      "ds_write_addtid_b32 v56 offset:6400\n"
      "v_fma_f32 v49, s44, v37, v41\n"
      "v_mfma_f32_16x16x32_bf16 v[28:31], a[28:31], v[168:171], v[28:31]\n"
      "s_waitcnt lgkmcnt(5)\n"
      "ds_write_addtid_b32 v57 offset:43328\n"
      "v_cvt_pk_bf16_f32 v48, v48, v49\n"
      "v_mfma_f32_16x16x32_bf16 v[28:31], a[32:35], v[224:227], v[28:31]\n"
      "ds_write_addtid_b32 v60 offset:4864\n"
      "v_fma_f32 v50, s44, v38, v42\n"
      "v_mfma_f32_16x16x32_bf16 v[28:31], a[0:3], v[132:135], v[28:31]\n"
      "s_waitcnt lgkmcnt(5)\n"
      "ds_write_addtid_b32 v61 offset:41792\n"
      "v_fma_f32 v51, s44, v39, v43\n"
      "v_mfma_f32_16x16x32_bf16 v[28:31], a[4:7], v[188:191], v[28:31]\n"
      "s_mov_b32 m0, s45\n"
      "ds_read2_b64 a[68:71], v13 offset0:1 offset1:65\n"
      "v_cvt_pk_bf16_f32 v49, v50, v51\n"
      "v_mfma_f32_16x16x32_bf16 v[28:31], a[8:11], a[240:243], v[28:31]\n"
      "ds_write_addtid_b32 v20 offset:46088\n"
      "v_fma_f32 v40, s44, v36, -v40\n"
      "v_mfma_f32_16x16x32_bf16 v[72:75], a[84:87], a[64:67], 0\n"
      "ds_write_addtid_b32 v21 offset:46344\n"
      "v_fma_f32 v41, s44, v37, -v41\n"
      "v_mfma_f32_16x16x32_bf16 v[20:23], a[12:15], v[104:107], 0\n"
      "ds_write_addtid_b32 v24 offset:33792\n"
      "v_cvt_pk_bf16_f32 v40, v40, v41\n"
      "v_mfma_f32_16x16x32_bf16 v[20:23], a[16:19], v[156:159], v[20:23]\n"
      "ds_write_addtid_b32 v25 offset:34048\n"
      "v_cvt_pk_bf16_f32 v28, v28, v29\n"
      "v_mfma_f32_16x16x32_bf16 v[20:23], a[20:23], v[212:215], v[20:23]\n"
      "ds_write_addtid_b32 v28 offset:59408\n"
      "v_cvt_pk_bf16_f32 v29, v30, v31\n"
      "v_mfma_f32_16x16x32_bf16 v[20:23], a[24:27], v[120:123], v[20:23]\n"
      "ds_write_addtid_b32 v29 offset:59664\n"
      "v_fma_f32 v42, s44, v38, -v42\n"
      "v_mfma_f32_16x16x32_bf16 v[20:23], a[28:31], v[176:179], v[20:23]\n"
      "s_mov_b32 m0, s18\n"
      "v_fma_f32 v43, s44, v39, -v43\n"
      "v_cvt_pk_bf16_f32 v41, v42, v43\n"
      "v_mfma_f32_16x16x32_bf16 v[20:23], a[32:35], a[232:235], v[20:23]\n"
      "s_sub_u32 s18, s19, s18\n"
      "ds_write_addtid_b32 v48 offset:5888\n"
      "v_cvt_pk_bf16_f32 v45, v46, v47\n"
      "v_mfma_f32_16x16x32_bf16 v[20:23], a[0:3], v[140:143], v[20:23]\n"
      "ds_write_addtid_b32 v49 offset:42816\n"
      "v_cvt_pk_bf16_f32 v84, v72, v73\n"
      "v_mfma_f32_16x16x32_bf16 v[20:23], a[4:7], a[236:239], v[20:23]\n"
      "ds_write_addtid_b32 v40 offset:6656\n"
      "v_cvt_pk_bf16_f32 v90, v74, v75\n"
      "v_mfma_f32_16x16x32_bf16 v[20:23], a[8:11], v[248:251], v[20:23]\n"
      "s_waitcnt lgkmcnt(9)\n"
      "ds_write_addtid_b32 v41 offset:43584\n"
      "s_nop 0\n"
      "v_mfma_f32_16x16x32_bf16 v[76:79], a[84:87], a[68:71], 0\n"
      "ds_write_addtid_b32 v44 offset:5120\n"
      "s_nop 0\n"
      "v_mfma_f32_16x16x32_bf16 v[32:35], a[12:15], v[108:111], 0\n"
      "ds_write_addtid_b32 v45 offset:42048\n"
      "v_cvt_pk_bf16_f32 v20, v20, v21\n"
      "v_mfma_f32_16x16x32_bf16 v[32:35], a[16:19], v[164:167], v[32:35]\n"
      "s_mov_b32 m0, s45\n"
      "v_cvt_pk_bf16_f32 v21, v22, v23\n"
      "v_cvt_pk_bf16_f32 v85, v76, v77\n"
      "v_mfma_f32_16x16x32_bf16 v[32:35], a[20:23], a[224:227], v[32:35]\n"
      "buffer_store_dwordx2 v[84:85], v15, s[36:39], s52 offen sc1\n"
      "v_cvt_pk_bf16_f32 v91, v78, v79\n"
      "v_mfma_f32_16x16x32_bf16 v[32:35], a[24:27], v[128:131], v[32:35]\n"
      "v_mfma_f32_16x16x32_bf16 v[32:35], a[28:31], a[228:231], v[32:35]\n"
      "ds_write_addtid_b32 v20 offset:47112\n"
      "v_mfma_f32_16x16x32_bf16 v[32:35], a[32:35], v[236:239], v[32:35]\n"
      "ds_write_addtid_b32 v21 offset:47368\n"
      "v_mfma_f32_16x16x32_bf16 v[32:35], a[0:3], v[144:147], v[32:35]\n"
      "buffer_store_dwordx2 v[90:91], v15, s[36:39], s52 offen offset:1536 sc1\n"
      "v_mfma_f32_16x16x32_bf16 v[32:35], a[4:7], v[200:203], v[32:35]\n"
      "v_mfma_f32_16x16x32_bf16 v[32:35], a[8:11], a[244:247], v[32:35]\n"
      "s_nop 1\n"
      "s_cbranch_vccnz Lconv_loop1\n"
      "Lconv_t1:\n"
      "s_mov_b32 s52, s13\n"
      "s_nop 1\n"
      "v_mfma_f32_16x16x32_bf16 v[20:23], a[24:27], v[96:99], 0\n"
      "s_add_u32 s13, s13, s12\n"
      "v_mfma_f32_16x16x32_bf16 v[20:23], a[28:31], v[152:155], v[20:23]\n"
      "v_cvt_pk_bf16_f32 v32, v32, v33\n"
      "v_cvt_pk_bf16_f32 v33, v34, v35\n"
      "v_mfma_f32_16x16x32_bf16 v[20:23], a[32:35], a[220:223], v[20:23]\n"
      "ds_write_addtid_b32 v32 offset:34816\n"
      "v_mfma_f32_16x16x32_bf16 v[20:23], a[0:3], v[116:119], v[20:23]\n"
      "ds_write_addtid_b32 v33 offset:35072\n"
      "v_mfma_f32_16x16x32_bf16 v[20:23], a[4:7], v[168:171], v[20:23]\n"
      "v_mfma_f32_16x16x32_bf16 v[20:23], a[8:11], v[224:227], v[20:23]\n"
      "v_mfma_f32_16x16x32_bf16 v[24:27], a[24:27], v[104:107], 0\n"
      "v_mfma_f32_16x16x32_bf16 v[24:27], a[28:31], v[156:159], v[24:27]\n"
      "v_mfma_f32_16x16x32_bf16 v[24:27], a[32:35], v[212:215], v[24:27]\n"
      "v_mfma_f32_16x16x32_bf16 v[24:27], a[0:3], v[120:123], v[24:27]\n"
      "v_mfma_f32_16x16x32_bf16 v[24:27], a[4:7], v[176:179], v[24:27]\n"
      "s_waitcnt lgkmcnt(0)\n"
      "s_barrier\n"
      "ds_read2_b64 a[60:63], v10 offset0:0 offset1:64\n"
      "v_mfma_f32_16x16x32_bf16 v[24:27], a[8:11], a[232:235], v[24:27]\n"
      "s_waitcnt lgkmcnt(0)\n"
      "ds_read2_b64 a[64:67], v10 offset0:1 offset1:65\n"
      "v_mfma_f32_16x16x32_bf16 v[68:71], a[84:87], a[60:63], 0\n"
      "ds_read2_b64 a[68:71], v10 offset0:2 offset1:66\n"
      "v_mfma_f32_16x16x32_bf16 v[28:31], a[24:27], v[108:111], 0\n"
      "ds_read_b128 a[12:15], v7 offset:6144\n"
      "v_mfma_f32_16x16x32_bf16 v[28:31], a[28:31], v[164:167], v[28:31]\n"
      "ds_read_b128 a[16:19], v7 offset:6400\n"
      "v_mfma_f32_16x16x32_bf16 v[28:31], a[32:35], a[224:227], v[28:31]\n"
      "ds_read_b128 a[20:23], v7 offset:6656\n"
      "v_mfma_f32_16x16x32_bf16 v[28:31], a[0:3], v[128:131], v[28:31]\n"
      "ds_read2_b64 a[60:63], v10 offset0:3 offset1:67\n"
      "v_cvt_pk_bf16_f32 v80, v68, v69\n"
      "v_mfma_f32_16x16x32_bf16 v[28:31], a[4:7], a[228:231], v[28:31]\n"
      "ds_read_b128 a[24:27], v6 offset:5376\n"
      "v_cvt_pk_bf16_f32 v86, v70, v71\n"
      "v_mfma_f32_16x16x32_bf16 v[28:31], a[8:11], v[236:239], v[28:31]\n"
      "s_waitcnt lgkmcnt(6)\n"
      "ds_read_b128 a[28:31], v6 offset:5632\n"
      "v_mfma_f32_16x16x32_bf16 v[72:75], a[84:87], a[64:67], 0\n"
      "s_waitcnt lgkmcnt(6)\n"
      "ds_read2_b64 a[64:67], v12 offset0:0 offset1:64\n"
      "v_mfma_f32_16x16x32_bf16 v[76:79], a[84:87], a[68:71], 0\n"
      "s_waitcnt lgkmcnt(6)\n"
      "ds_read2_b64 a[68:71], v12 offset0:1 offset1:65\n"
      "v_mfma_f32_16x16x32_bf16 v[20:23], a[12:15], v[132:135], v[20:23]\n"
      "s_waitcnt lgkmcnt(6)\n"
      "ds_read_b128 a[32:35], v6 offset:5888\n"
      "v_cvt_pk_bf16_f32 v81, v72, v73\n"
      "v_mfma_f32_16x16x32_bf16 v[20:23], a[16:19], v[188:191], v[20:23]\n"
      "s_waitcnt lgkmcnt(6)\n"
      "ds_read_b128 a[0:3], v7 offset:768\n"
      "v_cvt_pk_bf16_f32 v87, v74, v75\n"
      "v_mfma_f32_16x16x32_bf16 v[20:23], a[20:23], a[240:243], v[20:23]\n"
      "s_waitcnt lgkmcnt(6)\n"
      "ds_read_b128 a[4:7], v7 offset:1024\n"
      "v_cvt_pk_bf16_f32 v82, v76, v77\n"
      "v_mfma_f32_16x16x32_bf16 v[68:71], a[84:87], a[60:63], 0\n"
      "ds_read_b128 a[8:11], v7 offset:1280\n"
      "v_cvt_pk_bf16_f32 v88, v78, v79\n"
      "v_mfma_f32_16x16x32_bf16 v[24:27], a[12:15], v[140:143], v[24:27]\n"
      "v_mfma_f32_16x16x32_bf16 v[24:27], a[16:19], a[236:239], v[24:27]\n"
      "v_cvt_pk_bf16_f32 v20, v20, v21\n"
      "v_cvt_pk_bf16_f32 v21, v22, v23\n"
      "v_mfma_f32_16x16x32_bf16 v[24:27], a[20:23], v[248:251], v[24:27]\n"
      "s_waitcnt lgkmcnt(5)\n"
      "ds_write_addtid_b32 v20 offset:62480\n"
      "v_cvt_pk_bf16_f32 v83, v68, v69\n"
      "v_mfma_f32_16x16x32_bf16 v[72:75], a[84:87], a[64:67], 0\n"
      "ds_write_addtid_b32 v21 offset:62736\n"
      "v_cvt_pk_bf16_f32 v89, v70, v71\n"
      "v_mfma_f32_16x16x32_bf16 v[28:31], a[12:15], v[144:147], v[28:31]\n"
      "ds_read_b128 a[12:15], v7 offset:5376\n"
      "v_cvt_pk_bf16_f32 v24, v24, v25\n"
      "v_mfma_f32_16x16x32_bf16 v[28:31], a[16:19], v[200:203], v[28:31]\n"
      "ds_read_b128 a[16:19], v7 offset:5632\n"
      "v_cvt_pk_bf16_f32 v25, v26, v27\n"
      "v_mfma_f32_16x16x32_bf16 v[28:31], a[20:23], a[244:247], v[28:31]\n"
      "s_waitcnt lgkmcnt(5)\n"
      "ds_read_b128 a[20:23], v7 offset:5888\n"
      "v_cvt_pk_bf16_f32 v84, v72, v73\n"
      "v_mfma_f32_16x16x32_bf16 v[76:79], a[84:87], a[68:71], 0\n"
      "buffer_store_dwordx4 v[80:83], v14, s[36:39], s52 offen sc1\n"
      "v_cvt_pk_bf16_f32 v90, v74, v75\n"
      "v_mfma_f32_16x16x32_bf16 v[32:35], a[24:27], v[92:95], 0\n"
      "ds_write_addtid_b32 v24 offset:50184\n"
      "v_cvt_pk_bf16_f32 v28, v28, v29\n"
      "v_mfma_f32_16x16x32_bf16 v[32:35], a[28:31], v[148:151], v[32:35]\n"
      "buffer_store_dwordx4 v[86:89], v14, s[36:39], s52 offen offset:1536 sc1\n"
      "v_cvt_pk_bf16_f32 v29, v30, v31\n"
      "v_mfma_f32_16x16x32_bf16 v[32:35], a[32:35], v[204:207], v[32:35]\n"
      "ds_write_addtid_b32 v25 offset:50440\n"
      "v_cvt_pk_bf16_f32 v85, v76, v77\n"
      "v_mfma_f32_16x16x32_bf16 v[32:35], a[0:3], v[112:115], v[32:35]\n"
      "ds_write_addtid_b32 v28 offset:37888\n"
      "v_cvt_pk_bf16_f32 v91, v78, v79\n"
      "v_mfma_f32_16x16x32_bf16 v[32:35], a[4:7], a[208:211], v[32:35]\n"
      "s_waitcnt lgkmcnt(5)\n"
      "v_mfma_f32_16x16x32_bf16 v[32:35], a[8:11], v[220:223], v[32:35]\n"
      "ds_write_addtid_b32 v29 offset:38144\n"
      "v_mfma_f32_16x16x32_bf16 v[32:35], a[12:15], a[212:215], v[32:35]\n"
      "s_waitcnt lgkmcnt(4)\n"
      "v_mfma_f32_16x16x32_bf16 v[32:35], a[16:19], v[184:187], v[32:35]\n"
      "buffer_store_dwordx2 v[84:85], v15, s[36:39], s52 offen sc1\n"
      "v_mfma_f32_16x16x32_bf16 v[32:35], a[20:23], v[240:243], v[32:35]\n"
      "s_nop 1\n"
      "v_mfma_f32_16x16x32_bf16 v[20:23], a[24:27], v[100:103], 0\n"
      "buffer_store_dwordx2 v[90:91], v15, s[36:39], s52 offen offset:1536 sc1\n"
      "s_nop 0\n"
      "v_mfma_f32_16x16x32_bf16 v[20:23], a[28:31], a[200:203], v[20:23]\n"
      "s_nop 0\n"
      "v_mfma_f32_16x16x32_bf16 v[20:23], a[32:35], v[208:211], v[20:23]\n"
      "v_cvt_pk_bf16_f32 v32, v32, v33\n"
      "v_cvt_pk_bf16_f32 v33, v34, v35\n"
      "v_mfma_f32_16x16x32_bf16 v[20:23], a[0:3], a[204:207], v[20:23]\n"
      "ds_write_addtid_b32 v32 offset:61456\n"
      "v_mfma_f32_16x16x32_bf16 v[20:23], a[4:7], v[172:175], v[20:23]\n"
      "ds_write_addtid_b32 v33 offset:61712\n"
      "v_mfma_f32_16x16x32_bf16 v[20:23], a[8:11], v[228:231], v[20:23]\n"
      "v_mfma_f32_16x16x32_bf16 v[20:23], a[12:15], v[136:139], v[20:23]\n"
      "v_mfma_f32_16x16x32_bf16 v[20:23], a[16:19], v[192:195], v[20:23]\n"
      "v_mfma_f32_16x16x32_bf16 v[20:23], a[20:23], v[244:247], v[20:23]\n"
      "s_nop 1\n"
      "v_mfma_f32_16x16x32_bf16 v[24:27], a[24:27], a[196:199], 0\n"
      "ds_read_b128 a[24:27], v7 offset:4608\n"
      "s_nop 0\n"
      "v_mfma_f32_16x16x32_bf16 v[24:27], a[28:31], v[160:163], v[24:27]\n"
      "ds_read_b128 a[28:31], v7 offset:4864\n"
      "v_mfma_f32_16x16x32_bf16 v[24:27], a[32:35], v[216:219], v[24:27]\n"
      "ds_read_b128 a[32:35], v7 offset:5120\n"
      "v_cvt_pk_bf16_f32 v20, v20, v21\n"
      "v_mfma_f32_16x16x32_bf16 v[24:27], a[0:3], v[124:127], v[24:27]\n"
      "ds_read_b128 a[0:3], v6 offset:4608\n"
      "v_cvt_pk_bf16_f32 v21, v22, v23\n"
      "v_mfma_f32_16x16x32_bf16 v[24:27], a[4:7], v[180:183], v[24:27]\n"
      "ds_read_b128 a[4:7], v6 offset:4864\n"
      "v_mfma_f32_16x16x32_bf16 v[24:27], a[8:11], v[232:235], v[24:27]\n"
      "ds_read_b128 a[8:11], v6 offset:5120\n"
      "v_mfma_f32_16x16x32_bf16 v[24:27], a[12:15], a[216:219], v[24:27]\n"
      "ds_read_b128 a[12:15], v7 offset:0\n"
      "v_mfma_f32_16x16x32_bf16 v[24:27], a[16:19], v[196:199], v[24:27]\n"
      "ds_read_b128 a[16:19], v7 offset:256\n"
      "v_mfma_f32_16x16x32_bf16 v[24:27], a[20:23], v[252:255], v[24:27]\n"
      "s_waitcnt lgkmcnt(7)\n"
      "ds_read_b128 a[20:23], v7 offset:512\n"
      "s_nop 0\n"
      "v_mfma_f32_16x16x32_bf16 v[28:31], a[24:27], a[160:163], 0\n"
      "s_waitcnt lgkmcnt(7)\n"
      "ds_write_addtid_b32 v20 offset:49160\n"
      "s_nop 0\n"
      "v_mfma_f32_16x16x32_bf16 v[28:31], a[28:31], a[172:175], v[28:31]\n"
      "s_waitcnt lgkmcnt(7)\n"
      "ds_write_addtid_b32 v21 offset:49416\n"
      "v_cvt_pk_bf16_f32 v24, v24, v25\n"
      "v_mfma_f32_16x16x32_bf16 v[28:31], a[32:35], a[184:187], v[28:31]\n"
      "s_waitcnt lgkmcnt(7)\n"
      "v_cvt_pk_bf16_f32 v25, v26, v27\n"
      "v_mfma_f32_16x16x32_bf16 v[28:31], a[0:3], a[88:91], v[28:31]\n"
      "s_waitcnt lgkmcnt(6)\n"
      "ds_write_addtid_b32 v24 offset:36864\n"
      "v_mfma_f32_16x16x32_bf16 v[28:31], a[4:7], a[100:103], v[28:31]\n"
      "s_waitcnt lgkmcnt(6)\n"
      "v_mfma_f32_16x16x32_bf16 v[28:31], a[8:11], a[112:115], v[28:31]\n"
      "s_waitcnt lgkmcnt(5)\n"
      "ds_write_addtid_b32 v25 offset:37120\n"
      "v_mfma_f32_16x16x32_bf16 v[28:31], a[12:15], a[124:127], v[28:31]\n"
      "s_waitcnt lgkmcnt(5)\n"
      "v_mfma_f32_16x16x32_bf16 v[28:31], a[16:19], a[136:139], v[28:31]\n"
      "s_waitcnt lgkmcnt(4)\n"
      "v_mfma_f32_16x16x32_bf16 v[28:31], a[20:23], a[148:151], v[28:31]\n"
      "s_nop 1\n"
      "v_mfma_f32_16x16x32_bf16 v[20:23], a[24:27], a[164:167], 0\n"
      "s_nop 1\n"
      "v_mfma_f32_16x16x32_bf16 v[20:23], a[28:31], a[176:179], v[20:23]\n"
      "s_nop 0\n"
      "v_mfma_f32_16x16x32_bf16 v[20:23], a[32:35], a[188:191], v[20:23]\n"
      "v_cvt_pk_bf16_f32 v28, v28, v29\n"
      "v_cvt_pk_bf16_f32 v29, v30, v31\n"
      "v_mfma_f32_16x16x32_bf16 v[20:23], a[0:3], a[92:95], v[20:23]\n"
      "ds_write_addtid_b32 v28 offset:60432\n"
      "v_mfma_f32_16x16x32_bf16 v[20:23], a[4:7], a[104:107], v[20:23]\n"
      "ds_write_addtid_b32 v29 offset:60688\n"
      "v_mfma_f32_16x16x32_bf16 v[20:23], a[8:11], a[116:119], v[20:23]\n"
      "v_mfma_f32_16x16x32_bf16 v[20:23], a[12:15], a[128:131], v[20:23]\n"
      "v_mfma_f32_16x16x32_bf16 v[20:23], a[16:19], a[140:143], v[20:23]\n"
      "v_mfma_f32_16x16x32_bf16 v[20:23], a[20:23], a[152:155], v[20:23]\n"
      "s_nop 1\n"
      "v_mfma_f32_16x16x32_bf16 v[32:35], a[24:27], a[168:171], 0\n"
      "s_nop 1\n"
      "v_mfma_f32_16x16x32_bf16 v[32:35], a[28:31], a[180:183], v[32:35]\n"
      "s_nop 0\n"
      "v_mfma_f32_16x16x32_bf16 v[32:35], a[32:35], a[192:195], v[32:35]\n"
      "v_cvt_pk_bf16_f32 v20, v20, v21\n"
      "v_cvt_pk_bf16_f32 v21, v22, v23\n"
      "v_mfma_f32_16x16x32_bf16 v[32:35], a[0:3], a[96:99], v[32:35]\n"
      "ds_write_addtid_b32 v20 offset:48136\n"
      "v_mfma_f32_16x16x32_bf16 v[32:35], a[4:7], a[108:111], v[32:35]\n"
      "ds_write_addtid_b32 v21 offset:48392\n"
      "v_mfma_f32_16x16x32_bf16 v[32:35], a[8:11], a[120:123], v[32:35]\n"
      "v_mfma_f32_16x16x32_bf16 v[32:35], a[12:15], a[132:135], v[32:35]\n"
      "v_mfma_f32_16x16x32_bf16 v[32:35], a[16:19], a[144:147], v[32:35]\n"
      "v_mfma_f32_16x16x32_bf16 v[32:35], a[20:23], a[156:159], v[32:35]\n"
      "s_nop 1\n"
      "s_nop 5\n"
      "v_cvt_pk_bf16_f32 v32, v32, v33\n"
      "v_cvt_pk_bf16_f32 v33, v34, v35\n"
      "ds_write_addtid_b32 v32 offset:35840\n"
      "ds_write_addtid_b32 v33 offset:36096\n"
      "s_waitcnt lgkmcnt(0)\n"
      "s_barrier\n"
      "ds_read2_b64 a[0:3], v11 offset0:0 offset1:64\n"
      "ds_read2_b64 a[4:7], v11 offset0:1 offset1:65\n"
      "ds_read2_b64 a[8:11], v11 offset0:2 offset1:66\n"
      "ds_read2_b64 a[12:15], v11 offset0:3 offset1:67\n"
      "ds_read2_b64 a[16:19], v13 offset0:0 offset1:64\n"
      "ds_read2_b64 a[20:23], v13 offset0:1 offset1:65\n"
      "s_waitcnt lgkmcnt(5)\n"
      "v_mfma_f32_16x16x32_bf16 v[68:71], a[84:87], a[0:3], 0\n"
      "s_waitcnt lgkmcnt(4)\n"
      "v_mfma_f32_16x16x32_bf16 v[72:75], a[84:87], a[4:7], 0\n"
      "s_waitcnt lgkmcnt(3)\n"
      "v_mfma_f32_16x16x32_bf16 v[76:79], a[84:87], a[8:11], 0\n"
      "s_waitcnt lgkmcnt(2)\n"
      "s_nop 2\n"
      "v_cvt_pk_bf16_f32 v80, v68, v69\n"
      "v_cvt_pk_bf16_f32 v86, v70, v71\n"
      "v_mfma_f32_16x16x32_bf16 v[68:71], a[84:87], a[12:15], 0\n"
      "s_waitcnt lgkmcnt(1)\n"
      "v_cvt_pk_bf16_f32 v81, v72, v73\n"
      "v_cvt_pk_bf16_f32 v87, v74, v75\n"
      "v_cvt_pk_bf16_f32 v82, v76, v77\n"
      "v_cvt_pk_bf16_f32 v88, v78, v79\n"
      "v_mfma_f32_16x16x32_bf16 v[72:75], a[84:87], a[16:19], 0\n"
      "s_waitcnt lgkmcnt(0)\n"
      "v_mfma_f32_16x16x32_bf16 v[76:79], a[84:87], a[20:23], 0\n"
      "s_mov_b32 s52, s13\n"
      "v_cvt_pk_bf16_f32 v83, v68, v69\n"
      "v_cvt_pk_bf16_f32 v89, v70, v71\n"
      "s_add_u32 s13, s13, s12\n"
      "buffer_store_dwordx4 v[80:83], v14, s[36:39], s52 offen sc1\n"
      "buffer_store_dwordx4 v[86:89], v14, s[36:39], s52 offen offset:1536 sc1\n"
      "v_cvt_pk_bf16_f32 v84, v72, v73\n"
      "v_cvt_pk_bf16_f32 v90, v74, v75\n"
      "v_cvt_pk_bf16_f32 v85, v76, v77\n"
      "v_cvt_pk_bf16_f32 v91, v78, v79\n"
      "buffer_store_dwordx2 v[84:85], v15, s[36:39], s52 offen sc1\n"
      "buffer_store_dwordx2 v[90:91], v15, s[36:39], s52 offen offset:1536 sc1\n"
      "Lconv_u1:\n"
      "Lconv_tail:\n"
      "s_waitcnt vmcnt(0) lgkmcnt(0)\n"
      "Lconv_end:\n"
      :
      : "v"(threadIdx.x), "s"(blockIdx.x), "s"(__builtin_amdgcn_kernarg_segment_ptr())
      : "v1", "v2", "v3", "v4", "v5", "v6", "v7", "v8", "v9", "v10", "v11", "v12", "v13", "v14", "v15", "v16", "v17", "v18", "v19", "v20", "v21", "v22", "v23", "v24", "v25", "v26", "v27", "v28", "v29", "v30", "v31", "v32", "v33", "v34", "v35", "v36", "v37", "v38", "v39", "v40", "v41", "v42", "v43", "v44", "v45", "v46", "v47", "v48", "v49", "v50", "v51", "v52", "v53", "v54", "v55", "v56", "v57", "v58", "v59", "v60", "v61", "v62", "v63", "v64", "v65", "v66", "v67", "v68", "v69", "v70", "v71", "v72", "v73", "v74", "v75", "v76", "v77", "v78", "v79", "v80", "v81", "v82", "v83", "v84", "v85", "v86", "v87", "v88", "v89", "v90", "v91", "v92", "v93", "v94", "v95", "v96", "v97", "v98", "v99", "v100", "v101", "v102", "v103", "v104", "v105", "v106", "v107", "v108", "v109", "v110", "v111", "v112", "v113", "v114", "v115", "v116", "v117", "v118", "v119", "v120", "v121", "v122", "v123", "v124", "v125", "v126", "v127", "v128", "v129", "v130", "v131", "v132", "v133", "v134", "v135", "v136", "v137", "v138", "v139", "v140", "v141", "v142", "v143", "v144", "v145", "v146", "v147", "v148", "v149", "v150", "v151", "v152", "v153", "v154", "v155", "v156", "v157", "v158", "v159", "v160", "v161", "v162", "v163", "v164", "v165", "v166", "v167", "v168", "v169", "v170", "v171", "v172", "v173", "v174", "v175", "v176", "v177", "v178", "v179", "v180", "v181", "v182", "v183", "v184", "v185", "v186", "v187", "v188", "v189", "v190", "v191", "v192", "v193", "v194", "v195", "v196", "v197", "v198", "v199", "v200", "v201", "v202", "v203", "v204", "v205", "v206", "v207", "v208", "v209", "v210", "v211", "v212", "v213", "v214", "v215", "v216", "v217", "v218", "v219", "v220", "v221", "v222", "v223", "v224", "v225", "v226", "v227", "v228", "v229", "v230", "v231", "v232", "v233", "v234", "v235", "v236", "v237", "v238", "v239", "v240", "v241", "v242", "v243", "v244", "v245", "v246", "v247", "v248", "v249", "v250", "v251", "v252", "v253", "v254", "v255", "a0", "a1", "a2", "a3", "a4", "a5", "a6", "a7", "a8", "a9", "a10", "a11", "a12", "a13", "a14", "a15", "a16", "a17", "a18", "a19", "a20", "a21", "a22", "a23", "a24", "a25", "a26", "a27", "a28", "a29", "a30", "a31", "a32", "a33", "a34", "a35", "a36", "a37", "a38", "a39", "a40", "a41", "a42", "a43", "a44", "a45", "a46", "a47", "a48", "a49", "a50", "a51", "a52", "a53", "a54", "a55", "a56", "a57", "a58", "a59", "a60", "a61", "a62", "a63", "a64", "a65", "a66", "a67", "a68", "a69", "a70", "a71", "a72", "a73", "a74", "a75", "a76", "a77", "a78", "a79", "a80", "a81", "a82", "a83", "a84", "a85", "a86", "a87", "a88", "a89", "a90", "a91", "a92", "a93", "a94", "a95", "a96", "a97", "a98", "a99", "a100", "a101", "a102", "a103", "a104", "a105", "a106", "a107", "a108", "a109", "a110", "a111", "a112", "a113", "a114", "a115", "a116", "a117", "a118", "a119", "a120", "a121", "a122", "a123", "a124", "a125", "a126", "a127", "a128", "a129", "a130", "a131", "a132", "a133", "a134", "a135", "a136", "a137", "a138", "a139", "a140", "a141", "a142", "a143", "a144", "a145", "a146", "a147", "a148", "a149", "a150", "a151", "a152", "a153", "a154", "a155", "a156", "a157", "a158", "a159", "a160", "a161", "a162", "a163", "a164", "a165", "a166", "a167", "a168", "a169", "a170", "a171", "a172", "a173", "a174", "a175", "a176", "a177", "a178", "a179", "a180", "a181", "a182", "a183", "a184", "a185", "a186", "a187", "a188", "a189", "a190", "a191", "a192", "a193", "a194", "a195", "a196", "a197", "a198", "a199", "a200", "a201", "a202", "a203", "a204", "a205", "a206", "a207", "a208", "a209", "a210", "a211", "a212", "a213", "a214", "a215", "a216", "a217", "a218", "a219", "a220", "a221", "a222", "a223", "a224", "a225", "a226", "a227", "a228", "a229", "a230", "a231", "a232", "a233", "a234", "a235", "a236", "a237", "a238", "a239", "a240", "a241", "a242", "a243", "a244", "a245", "a246", "a247", "a248", "a249", "a250", "a251", "a252", "a253", "a254", "a255", "s12", "s13", "s14", "s15", "s16", "s17", "s18", "s19", "s20", "s21", "s22", "s23", "s24", "s25", "s26", "s27", "s28", "s29", "s30", "s31", "s34", "s35", "s36", "s37", "s38", "s39", "s40", "s41", "s42", "s43", "s44", "s45", "s46", "s47", "s48", "s49", "s50", "s51", "s52", "s53", "s54", "s55", "s56", "s57", "s58", "s59", "s60", "s61", "s62", "s63", "s64", "s65", "s66", "s67", "s68", "s69", "s70", "s71", "s72", "s73", "s74", "s75", "s76", "s77", "s78", "s79", "s80", "s81", "s82", "s83", "s84", "s85", "s86", "s87", "s88", "s89", "s90", "s91", "s92", "s93", "s94", "s95", "s96", "s97", "s98", "s99", "scc", "vcc", "memory");
}

extern "C" void kernel_launch(void* const* d_in, const int* in_sizes, int n_in, void* d_out,
                              int out_size, void* d_ws, size_t ws_size, hipStream_t stream) {
  conv_kernel<<<256, 256, 0, stream>>>((const char*)d_in[0], (const char*)d_in[1], (char*)d_out, (char*)d_ws);
}
```

```cpp
#include <hip/hip_runtime.h>
#include <hip/hip_bf16.h>
#include <stdint.h>

__global__ void __launch_bounds__(256, 1) __attribute__((aligned(4096)))
conv_kernel(const char* __restrict__ x, const char* __restrict__ w, char* __restrict__ out, char* __restrict__ dbg) {
  __shared__ __attribute__((aligned(1024))) char smem[163840];
  asm volatile("" :: "v"((unsigned)(uintptr_t)smem) : "memory");
  asm volatile(
      "s_load_dwordx8 s[20:27], %2, 0x0\n"
      "v_readfirstlane_b32 s40, %0\n"
      "v_and_b32 v5, 63, %0\n"
      "v_lshlrev_b32 v8, 4, v5\n"
      "s_lshr_b32 s40, s40, 6\n"
      "s_lshl_b32 s43, s40, 3\n"
      "s_or_b32 s43, s43, 0x80000\n"
      "v_bitop3_b32 v9, v8, v5, 32 bitop3:0x78\n"
      "v_xor_b32 v89, 32, v8\n"
      "s_mul_i32 s92, s40, 3072\n"
      "s_lshr_b32 s14, %1, 7\n"
      "s_sub_u32 s14, s14, 1\n"
      "s_lshr_b32 s41, s40, 1\n"
      "s_and_b32 s42, s40, 1\n"
      "s_mov_b32 s30, 165888\n"
      "s_mov_b32 s31, 0x20000\n"
      "s_lshr_b32 s49, %1, 3\n"
      "s_lshl2_add_u32 s49, s49, s40\n"
      "v_lshlrev_b32 v36, 7, v5\n"
      "s_mul_i32 s50, s49, 384\n"
      "v_min_u32 v37, 256, v36\n"
      "v_add_u32 v37, s50, v37\n"
      "v_min_u32 v66, 1280, v36\n"
      "s_mul_i32 s96, s49, 1408\n"
      "s_getpc_b64 s[54:55]\n"
      "Lconv_start:\n"
      "v_min_u32 v37, Lconv_end-Lconv_start-4, v37\n"
      "s_mov_b32 m0, 162816\n"
      "s_nop 0\n"
      "global_load_lds_dword v37, s[54:55]\n"
      "s_bfe_u32 s99, %1, 0x30003\n"
      "s_mul_i32 s49, s40, 192\n"
      "v_add_u32 v36, s49, v5\n"
      "v_add_u32 v38, 128, v36\n"
      "v_mul_u32_u24 v37, 669, v36\n"
      "v_lshrrev_b32 v37, 16, v37\n"
      "v_mad_i32_i24 v37, v37, -2, v38\n"
      "v_lshlrev_b32 v16, 4, v37\n"
      "v_add_u32 v36, 64, v36\n"
      "v_mul_u32_u24 v37, 669, v36\n"
      "v_lshrrev_b32 v37, 16, v37\n"
      "v_mad_i32_i24 v37, v37, -2, v38\n"
      "v_lshlrev_b32 v17, 4, v37\n"
      "v_add_u32 v36, 64, v36\n"
      "v_mul_u32_u24 v37, 669, v36\n"
      "v_lshrrev_b32 v37, 16, v37\n"
      "v_mad_i32_i24 v37, v37, -2, v38\n"
      "v_lshlrev_b32 v18, 4, v37\n"
      "s_or_b32 s50, s99, s40\n"
      "s_cmp_eq_u32 s50, 0\n"
      "s_cselect_b32 s16, 0xffffff, 0\n"
      "s_mov_b32 s17, 0\n"
      "s_cmp_eq_u32 s99, 7\n"
      "s_cselect_b32 s19, 0xffffff00, 0\n"
      "s_mov_b32 s18, 0\n"
      "v_add_u32 v19, 14080, v8\n"
      "v_cndmask_b32 v16, v16, 2.0, s[16:17]\n"
      "v_cndmask_b32 v19, v19, 2.0, s[18:19]\n"
      "v_and_b32 v36, 16, v8\n"
      "v_mov_b32 v52, 0xffff\n"
      "v_lshlrev_b32 v52, v36, v52\n"
      "s_mov_b32 s16, 0x3000003\n"
      "s_mov_b32 s17, 0x3000003\n"
      "v_cndmask_b32 v48, 0, v52, s[16:17]\n"
      "s_lshl_b64 s[16:17], s[16:17], 2\n"
      "v_cndmask_b32 v49, 0, v52, s[16:17]\n"
      "s_lshl_b64 s[16:17], s[16:17], 2\n"
      "v_cndmask_b32 v50, 0, v52, s[16:17]\n"
      "s_lshl_b64 s[16:17], s[16:17], 2\n"
      "v_cndmask_b32 v51, 0, v52, s[16:17]\n"
      "v_bfe_u32 v53, v5, 5, 1\n"
      "v_xor_b32 v53, s41, v53\n"
      "v_sub_u32 v53, 0, v53\n"
      "v_and_b32 v36, 0x1000100, v53\n"
      "v_xor_b32 v36, 0x3fa83fa8, v36\n"
      "v_and_b32 v68, v36, v48\n"
      "v_and_b32 v69, v36, v49\n"
      "v_and_b32 v70, v36, v50\n"
      "v_and_b32 v71, v36, v51\n"
      "v_xor_b32 v72, 0x80008000, v68\n"
      "v_xor_b32 v73, 0x80008000, v69\n"
      "v_xor_b32 v74, 0x80008000, v70\n"
      "v_xor_b32 v75, 0x80008000, v71\n"
      "v_bfe_i32 v36, v5, 5, 1\n"
      "v_and_b32 v36, 0x3f283f28, v36\n"
      "v_xor_b32 v37, 0x3f283f28, v36\n"
      "v_and_b32 v76, v37, v48\n"
      "v_and_b32 v77, v37, v49\n"
      "v_and_b32 v78, v37, v50\n"
      "v_and_b32 v79, v37, v51\n"
      "v_and_b32 v80, v36, v48\n"
      "v_and_b32 v81, v36, v49\n"
      "v_and_b32 v82, v36, v50\n"
      "v_and_b32 v83, v36, v51\n"
      "s_mul_i32 s50, s42, 96\n"
      "v_bfe_u32 v36, v5, 1, 3\n"
      "v_and_b32 v37, 1, v5\n"
      "v_lshl_add_u32 v36, v37, 3, v36\n"
      "v_mul_u32_u24 v36, 192, v36\n"
      "v_add_u32 v36, 46144, v36\n"
      "v_lshlrev_b32 v37, 5, v37\n"
      "v_and_b32 v38, 16, v5\n"
      "v_add_u32 v38, s50, v38\n"
      "v_and_b32 v39, 32, v5\n"
      "v_mul_u32_u24 v40, 192, v39\n"
      "v_add_u32 v40, v40, v36\n"
      "v_xad_u32 v84, v38, v37, v40\n"
      "v_add_u32 v41, 32, v38\n"
      "v_xad_u32 v85, v41, v37, v40\n"
      "v_add_u32 v41, v39, v38\n"
      "v_xad_u32 v86, v41, v37, v36\n"
      "s_mul_i32 s49, s41, 6144\n"
      "s_add_u32 s49, s49, 46144\n"
      "v_lshrrev_b32 v36, 3, v5\n"
      "v_and_b32 v38, 4, v5\n"
      "v_lshl_add_u32 v36, v38, 1, v36\n"
      "v_mul_u32_u24 v36, 192, v36\n"
      "v_lshlrev_b32 v37, 3, v5\n"
      "v_and_b32 v38, 8, v37\n"
      "v_add3_u32 v36, v36, v38, s49\n"
      "v_and_b32 v38, 32, v37\n"
      "v_and_b32 v37, 16, v37\n"
      "v_add_u32 v37, s50, v37\n"
      "v_xad_u32 v87, v37, v38, v36\n"
      "v_add_u32 v39, 32, v37\n"
      "v_xad_u32 v88, v39, v38, v36\n"
      "s_waitcnt lgkmcnt(0)\n"
      "s_mov_b32 s28, s22\n"
      "s_and_b32 s29, s23, 0xffff\n"
      "buffer_load_dword v66, s[28:31], s96 offen lds\n"
      "s_add_u32 m0, s92, 73824\n"
      "s_and_b32 s97, 0x3c2c24, s14\n"
      "s_xor_b32 s97, 0x120c0600, s97\n"
      "s_bfe_u32 s97, s97, s43\n"
      "s_mul_i32 s97, s97, 3072\n"
      "buffer_load_dwordx4 v8, s[28:31], s97 offen offset:0 lds\n"
      "buffer_load_dwordx4 v9, s[28:31], s97 offen offset:1024 lds\n"
      "buffer_load_dwordx4 v89, s[28:31], s97 offen offset:2048 lds\n"
      "s_add_u32 m0, s92, 86112\n"
      "s_and_b32 s97, 0x2c240000, s14\n"
      "s_xor_b32 s97, 0x2a241e18, s97\n"
      "s_bfe_u32 s97, s97, s43\n"
      "s_mul_i32 s97, s97, 3072\n"
      "buffer_load_dwordx4 v8, s[28:31], s97 offen offset:0 lds\n"
      "buffer_load_dwordx4 v9, s[28:31], s97 offen offset:1024 lds\n"
      "buffer_load_dwordx4 v89, s[28:31], s97 offen offset:2048 lds\n"
      "s_and_b32 s53, %1, 7\n"
      "s_lshl_b32 s53, s53, 6\n"
      "s_bfe_u32 s50, %1, 0x10006\n"
      "s_add_u32 s53, s53, s50\n"
      "s_lshl1_add_u32 s50, s14, 32\n"
      "s_add_u32 s53, s53, s50\n"
      "s_mul_i32 s13, s53, 98304\n"
      "s_or_b32 s12, s14, 1\n"
      "s_mul_i32 s12, s12, 196608\n"
      "s_lshl_b32 s50, s12, 1\n"
      "s_sub_u32 s51, s13, s50\n"
      "s_mul_i32 s99, s99, 12288\n"
      "s_sub_u32 s50, s99, 2432\n"
      "s_ashr_i32 s49, s50, 31\n"
      "s_add_u32 s32, s20, s50\n"
      "s_addc_u32 s33, s21, s49\n"
      "s_and_b32 s33, s33, 0xffff\n"
      "s_sub_u32 s34, 50331648, s50\n"
      "s_mov_b32 s35, 0x20000\n"
      "s_add_u32 s46, s92, 110720\n"
      "v_lshrrev_b32 v36, 1, v5\n"
      "v_and_b32 v37, 4, v36\n"
      "v_and_or_b32 v37, v5, 2, v37\n"
      "v_mul_u32_u24 v37, 1568, v37\n"
      "v_xor_b32 v36, v36, v5\n"
      "v_and_b32 v36, 4, v36\n"
      "v_mad_u32_u24 v37, v36, 48, v37\n"
      "s_mul_i32 s50, s42, 1568\n"
      "s_add_u32 s50, s50, 110720\n"
      "v_and_b32 v36, 16, v5\n"
      "v_lshl_add_u32 v37, v36, 1, v37\n"
      "v_and_b32 v36, 16, v8\n"
      "v_add3_u32 v37, v37, v36, s50\n"
      "v_and_b32 v36, 32, v5\n"
      "v_mad_u32_u24 v38, v36, 24, v37\n"
      "v_mad_u32_u24 v39, v36, 25, v37\n"
      "s_mul_i32 s17, s42, 0xcc00cc00\n"
      "s_mov_b32 s16, 0\n"
      "v_mov_b32 v36, 768\n"
      "v_mov_b32 v37, 736\n"
      "s_mov_b64 exec, 0\n"
      "s_cmp_eq_u32 s40, 0\n"
      "s_cbranch_scc1 Lconv_p1\n"
      "s_cmp_eq_u32 s40, 1\n"
      "s_cbranch_scc1 Lconv_p2\n"
      "s_cmp_eq_u32 s40, 2\n"
      "s_cbranch_scc1 Lconv_pa0\n"
      "s_cmp_eq_u32 s40, 3\n"
      "s_cbranch_scc1 Lconv_pa1\n"
      "Lconv_r0:\n"
      "s_mov_b64 exec, -1\n"
      "s_add_u32 m0, s92, 98400\n"
      "s_and_b32 s97, 0x3c2c243c, s14\n"
      "s_xor_b32 s97, 0xd070130, s97\n"
      "s_bfe_u32 s97, s97, s43\n"
      "s_mul_i32 s97, s97, 3072\n"
      "buffer_load_dwordx4 v8, s[28:31], s97 offen offset:0 lds\n"
      "buffer_load_dwordx4 v9, s[28:31], s97 offen offset:1024 lds\n"
      "buffer_load_dwordx4 v89, s[28:31], s97 offen offset:2048 lds\n"
      "v_cndmask_b32 v36, v36, v37, s[16:17]\n"
      "v_add_u32 v39, v39, v36\n"
      "s_cmp_eq_u32 s41, 0\n"
      "s_cselect_b64 vcc, -1, 0\n"
      "v_add_u32 v40, 384, v38\n"
      "v_add_u32 v41, 384, v39\n"
      "v_cndmask_b32 v2, v40, v39, vcc\n"
      "v_cndmask_b32 v3, v39, v40, vcc\n"
      "v_cndmask_b32 v4, v41, v38, vcc\n"
      "v_and_b32 v36, 3, v5\n"
      "v_mul_u32_u24 v36, 18464, v36\n"
      "v_bfe_u32 v37, v5, 2, 2\n"
      "v_lshrrev_b32 v38, 1, v37\n"
      "v_xor_b32 v37, v37, v38\n"
      "v_lshl_add_u32 v36, v37, 6, v36\n"
      "s_mul_i32 s50, s41, 2304\n"
      "v_and_b32 v37, 48, v5\n"
      "v_add3_u32 v6, v36, v37, s50\n"
      "v_add_u32 v7, 9216, v6\n"
      "s_mov_b32 s16, 0xc0003\n"
      "s_mov_b32 s17, 0xc0003\n"
      "v_cndmask_b32 v48, 0, v52, s[16:17]\n"
      "s_lshl_b64 s[16:17], s[16:17], 4\n"
      "v_cndmask_b32 v49, 0, v52, s[16:17]\n"
      "s_lshl_b64 s[16:17], s[16:17], 4\n"
      "v_cndmask_b32 v50, 0, v52, s[16:17]\n"
      "s_lshl_b64 s[16:17], s[16:17], 4\n"
      "v_cndmask_b32 v51, 0, v52, s[16:17]\n"
      "v_and_b32 v36, 0xff00ff00, v53\n"
      "v_xor_b32 v36, 0xc080c080, v36\n"
      "v_and_b32 v38, v36, v48\n"
      "v_and_b32 v39, v36, v49\n"
      "v_and_b32 v40, v36, v50\n"
      "v_and_b32 v41, v36, v51\n"
      "v_accvgpr_write_b32 a72, v38\n"
      "v_accvgpr_write_b32 a73, v39\n"
      "v_accvgpr_write_b32 a74, v40\n"
      "v_accvgpr_write_b32 a75, v41\n"
      "v_and_b32 v36, 0xff00ff00, v53\n"
      "v_xor_b32 v36, 0xc05dc05d, v36\n"
      "v_and_b32 v38, v36, v48\n"
      "v_and_b32 v39, v36, v49\n"
      "v_and_b32 v40, v36, v50\n"
      "v_and_b32 v41, v36, v51\n"
      "v_accvgpr_write_b32 a76, v38\n"
      "v_accvgpr_write_b32 a77, v39\n"
      "v_accvgpr_write_b32 a78, v40\n"
      "v_accvgpr_write_b32 a79, v41\n"
      "v_and_b32 v36, 0x81008100, v53\n"
      "v_xor_b32 v36, 0x3f803f80, v36\n"
      "v_and_b32 v38, v36, v48\n"
      "v_and_b32 v39, v36, v49\n"
      "v_and_b32 v40, v36, v50\n"
      "v_and_b32 v41, v36, v51\n"
      "v_accvgpr_write_b32 a80, v38\n"
      "v_accvgpr_write_b32 a81, v39\n"
      "v_accvgpr_write_b32 a82, v40\n"
      "v_accvgpr_write_b32 a83, v41\n"
      "s_mov_b32 s44, 0x3fdd0000\n"
      "v_lshlrev_b32 v36, 5, v5\n"
      "v_and_b32 v36, 0x180, v36\n"
      "v_lshlrev_b32 v37, 3, v5\n"
      "v_and_b32 v37, 16, v37\n"
      "v_sub_u32 v38, 0x3e71, v36\n"
      "v_add_u32 v39, 0x3cf1, v36\n"
      "v_mov_b32 v40, 0x3f80\n"
      "s_mov_b64 vcc, 0xffff\n"
      "s_mov_b32 s16, 0xffff0000\n"
      "s_mov_b32 s17, 0xf0f0\n"
      "v_xor_b32 v41, 0x8000, v38\n"
      "v_cndmask_b32 v38, v38, v41, s[16:17]\n"
      "v_cndmask_b32 v38, v38, v40, vcc\n"
      "v_lshlrev_b32 v38, v37, v38\n"
      "v_xor_b32 v41, 0x8000, v39\n"
      "s_xor_b32 s17, s17, 0xffff\n"
      "v_cndmask_b32 v39, v39, v41, s[16:17]\n"
      "v_cndmask_b32 v39, v39, v40, vcc\n"
      "v_lshlrev_b32 v39, v37, v39\n"
      "s_mov_b32 s18, 0x55550005\n"
      "s_mov_b32 s19, 0x5555\n"
      "v_cndmask_b32 v42, 0, v38, s[18:19]\n"
      "s_lshl_b64 s[16:17], s[18:19], 1\n"
      "v_cndmask_b32 v43, 0, v38, s[16:17]\n"
      "v_accvgpr_write_b32 a84, v42\n"
      "v_accvgpr_write_b32 a85, v43\n"
      "s_mov_b32 s18, 0x55555000\n"
      "v_cndmask_b32 v42, 0, v39, s[18:19]\n"
      "s_lshl_b64 s[16:17], s[18:19], 1\n"
      "v_cndmask_b32 v43, 0, v39, s[16:17]\n"
      "v_accvgpr_write_b32 a86, v42\n"
      "v_accvgpr_write_b32 a87, v43\n"
      "v_lshrrev_b32 v36, 4, v5\n"
      "v_min_u32 v37, 2, v36\n"
      "v_lshlrev_b32 v37, 10, v37\n"
      "s_lshl_b32 s50, s41, 7\n"
      "s_lshl_b32 s49, s42, 8\n"
      "s_add_u32 s50, s50, s49\n"
      "s_add_u32 s50, s50, 98416\n"
      "v_and_b32 v38, 8, v5\n"
      "v_lshl_add_u32 v37, v38, 3, v37\n"
      "v_add_u32 v37, s50, v37\n"
      "s_movk_i32 s50, -12296\n"
      "s_mov_b32 s49, 43040\n"
      "v_mov_b32 v42, s49\n"
      "v_bfe_u32 v39, v5, 1, 2\n"
      "v_lshrrev_b32 v40, 2, v38\n"
      "v_add_u32 v39, v39, v40\n"
      "v_lshlrev_b32 v40, 5, v5\n"
      "v_and_b32 v40, 32, v40\n"
      "v_cmp_lt_u32 vcc, 2, v39\n"
      "v_mad_i32_i24 v39, v39, s50, v37\n"
      "v_add_u32 v39, v39, v40\n"
      "v_cndmask_b32 v41, 0, v42, vcc\n"
      "v_add_u32 v10, v39, v41\n"
      "v_add_u32 v11, 3072, v10\n"
      "v_bfe_u32 v39, v5, 2, 1\n"
      "v_lshrrev_b32 v40, 1, v38\n"
      "v_sub_u32 v39, v39, v40\n"
      "v_add_u32 v39, 4, v39\n"
      "v_and_b32 v40, 48, v8\n"
      "v_cmp_lt_u32 vcc, 2, v39\n"
      "v_mad_i32_i24 v39, v39, s50, v37\n"
      "v_add_u32 v39, v39, v40\n"
      "v_cndmask_b32 v41, 0, v42, vcc\n"
      "v_add_u32 v12, v39, v41\n"
      "v_add_u32 v13, 3072, v12\n"
      "v_mul_u32_u24 v36, 384, v36\n"
      "v_and_b32 v37, 15, v5\n"
      "v_lshl_add_u32 v39, v37, 3, v36\n"
      "v_add_u32 v15, 128, v39\n"
      "v_add_lshl_u32 v37, v37, v38, 4\n"
      "v_add_u32 v14, v37, v36\n"
      "s_add_u32 s50, s99, s92\n"
      "s_add_u32 s36, s24, s50\n"
      "s_addc_u32 s37, s25, 0\n"
      "s_and_b32 s37, s37, 0xffff\n"
      "s_sub_u32 s38, 50331648, s50\n"
      "s_mov_b32 s39, 0x20000\n"
      "s_mul_i32 s48, s12, 17\n"
      "s_mul_i32 s45, s42, 6152\n"
      "s_lshl_b32 s50, s41, 9\n"
      "s_add_u32 s45, s45, s50\n"
      "s_add_u32 s45, s45, 41056\n"
      "s_mov_b32 s98, 0\n"
      "s_mov_b32 s91, 26560\n"
      "s_mul_i32 s18, s42, 18464\n"
      "s_mul_i32 s50, s41, 2304\n"
      "s_add_u32 s18, s18, s50\n"
      "s_lshl1_add_u32 s19, s18, 9216\n"
      "s_add_u32 s58, s46, 26560\n"
      "s_lshl1_add_u32 s93, s46, 26560\n"
      "s_mul_i32 s59, s42, -13280\n"
      "s_add_u32 s59, s59, 136256\n"
      "s_lshl1_add_u32 s94, s59, 26560\n"
      "s_mul_i32 s50, s42, 26560\n"
      "s_add_u32 s59, s59, s50\n"
      "s_mul_i32 s15, s42, s12\n"
      "s_sub_u32 s95, s12, s15\n"
      "s_mov_b32 s52, 2.0\n"
      "s_cmp_lt_u32 s40, 3\n"
      "s_cselect_b32 s97, 27680, 0\n"
      "s_add_u32 s97, s97, s92\n"
      "s_add_u32 m0, s97, 36928\n"
      "s_and_b32 s97, 0x24000000, s14\n"
      "s_xor_b32 s97, 0x251f1913, s97\n"
      "s_bfe_u32 s97, s97, s43\n"
      "s_mul_i32 s97, s97, 3072\n"
      "buffer_load_dwordx4 v8, s[28:31], s97 offen offset:0 lds\n"
      "buffer_load_dwordx4 v9, s[28:31], s97 offen offset:1024 lds\n"
      "buffer_load_dwordx4 v89, s[28:31], s97 offen offset:2048 lds\n"
      "s_cmp_lt_u32 s40, 2\n"
      "s_cselect_b32 s97, 27680, 0\n"
      "s_add_u32 s97, s97, s92\n"
      "s_add_u32 m0, s97, 21536\n"
      "s_and_b32 s97, 0x24243c2c, s14\n"
      "s_xor_b32 s97, 0x802312b, s97\n"
      "s_bfe_u32 s97, s97, s43\n"
      "s_mul_i32 s97, s97, 3072\n"
      "buffer_load_dwordx4 v8, s[28:31], s97 offen offset:0 lds\n"
      "buffer_load_dwordx4 v9, s[28:31], s97 offen offset:1024 lds\n"
      "buffer_load_dwordx4 v89, s[28:31], s97 offen offset:2048 lds\n"
      "s_cmp_lt_u32 s40, 1\n"
      "s_cselect_b32 s97, 27680, 0\n"
      "s_add_u32 s97, s97, s92\n"
      "s_add_u32 m0, s97, 6144\n"
      "s_and_b32 s97, 0x3c, s14\n"
      "s_xor_b32 s97, 0x201a140e, s97\n"
      "s_bfe_u32 s97, s97, s43\n"
      "s_mul_i32 s97, s97, 3072\n"
      "buffer_load_dwordx4 v8, s[28:31], s97 offen offset:0 lds\n"
      "buffer_load_dwordx4 v9, s[28:31], s97 offen offset:1024 lds\n"
      "buffer_load_dwordx4 v89, s[28:31], s97 offen offset:2048 lds\n"
      "s_sub_u32 vcc_lo, 0, s41\n"
      "s_mov_b32 vcc_hi, 0\n"
      ".p2align 3\n"
      "s_waitcnt vmcnt(12)\n"
      "s_barrier\n"
      "s_cmp_lt_u32 s40, 3\n"
      "s_cselect_b32 s97, -9248, 0\n"
      "ds_read_b128 a[0:3], v86 offset:30752\n"
      "ds_read_b128 a[4:7], v84 offset:27680\n"
      "ds_read_b128 a[8:11], v85 offset:27680\n"
      "ds_read_b128 a[12:15], v86 offset:30816\n"
      "s_add_u32 s97, s97, s92\n"
      "s_add_u32 m0, s97, 9248\n"
      "s_and_b32 s97, 0x243c2424, s14\n"
      "s_xor_b32 s97, 0x3322c26, s97\n"
      "s_bfe_u32 s97, s97, s43\n"
      "s_mul_i32 s97, s97, 3072\n"
      "s_waitcnt lgkmcnt(3)\n"
      "v_mfma_f32_16x16x32_bf16 v[20:23], a[0:3], v[76:79], 0\n"
      "s_waitcnt lgkmcnt(2)\n"
      "ds_read_b128 a[16:19], v84 offset:27744\n"
      "ds_read_b128 a[20:23], v86 offset:39968\n"
      "buffer_load_dwordx4 v8, s[28:31], s97 offen offset:0 lds\n"
      "v_mfma_f32_16x16x32_bf16 v[24:27], a[4:7], v[68:71], v[20:23]\n"
      "ds_read_b128 a[24:27], v84 offset:36896\n"
      "ds_read_b128 a[28:31], v85 offset:36896\n"
      "v_mfma_f32_16x16x32_bf16 v[20:23], a[4:7], v[72:75], v[20:23]\n"
      "ds_read_b64_tr_b16 a[88:89], v87 offset:27680\n"
      "ds_read_b64_tr_b16 a[92:93], v88 offset:27680\n"
      "buffer_load_dwordx4 v9, s[28:31], s97 offen offset:1024 lds\n"
      "v_mfma_f32_16x16x32_bf16 v[28:31], a[0:3], v[80:83], 0\n"
      "s_waitcnt lgkmcnt(7)\n"
      "ds_read_b64_tr_b16 a[96:97], v87 offset:27744\n"
      "ds_read_b64_tr_b16 a[124:125], v87 offset:36896\n"
      "v_mfma_f32_16x16x32_bf16 v[32:35], a[8:11], v[68:71], v[28:31]\n"
      "ds_read_b64_tr_b16 a[128:129], v88 offset:36896\n"
      "ds_read_b128 a[32:35], v86 offset:40032\n"
      "v_mfma_f32_16x16x32_bf16 v[28:31], a[8:11], v[72:75], v[28:31]\n"
      "s_waitcnt lgkmcnt(10)\n"
      "ds_read_b128 a[0:3], v84 offset:36960\n"
      "ds_read_b64_tr_b16 a[132:133], v87 offset:36960\n"
      "buffer_load_dwordx4 v89, s[28:31], s97 offen offset:2048 lds\n"
      "v_mfma_f32_16x16x32_bf16 v[54:57], a[12:15], v[76:79], 0\n"
      "s_waitcnt lgkmcnt(10)\n"
      "s_cmp_lt_u32 s40, 2\n"
      "s_cselect_b32 s97, -9248, 0\n"
      "s_add_u32 s97, s97, s92\n"
      "s_add_u32 m0, s97, 30784\n"
      "s_and_b32 s97, 0x3c24, s14\n"
      "s_xor_b32 s97, 0x1b150f09, s97\n"
      "s_bfe_u32 s97, s97, s43\n"
      "s_mul_i32 s97, s97, 3072\n"
      "v_cvt_pk_bf16_f32 v96, v20, v21\n"
      "v_cvt_pk_bf16_f32 v97, v22, v23\n"
      "v_mfma_f32_16x16x32_bf16 v[58:61], a[16:19], v[68:71], v[54:57]\n"
      "v_cvt_pk_bf16_f32 v92, v24, v25\n"
      "v_cvt_pk_bf16_f32 v93, v26, v27\n"
      "v_mfma_f32_16x16x32_bf16 v[54:57], a[16:19], v[72:75], v[54:57]\n"
      "buffer_load_dwordx4 v8, s[28:31], s97 offen offset:0 lds\n"
      "v_cvt_pk_bf16_f32 v104, v28, v29\n"
      "v_cvt_pk_bf16_f32 v105, v30, v31\n"
      "v_mfma_f32_16x16x32_bf16 v[62:65], a[20:23], v[76:79], 0\n"
      "s_waitcnt lgkmcnt(4)\n"
      "v_cvt_pk_bf16_f32 v100, v32, v33\n"
      "v_cvt_pk_bf16_f32 v101, v34, v35\n"
      "v_mfma_f32_16x16x32_bf16 v[20:23], a[24:27], v[68:71], v[62:65]\n"
      "v_cvt_pk_bf16_f32 v108, v54, v55\n"
      "v_cvt_pk_bf16_f32 v109, v56, v57\n"
      "v_mfma_f32_16x16x32_bf16 v[62:65], a[24:27], v[72:75], v[62:65]\n"
      "s_waitcnt lgkmcnt(0)\n"
      "s_waitcnt vmcnt(13)\n"
      "s_cbranch_vccnz Lconv_h1\n"
      "s_barrier\n"
      "Lconv_h1:\n"
      "buffer_load_dwordx4 v9, s[28:31], s97 offen offset:1024 lds\n"
      "v_mfma_f32_16x16x32_bf16 v[24:27], a[20:23], v[80:83], 0\n"
      "v_cvt_pk_bf16_f32 v58, v58, v59\n"
      "v_cvt_pk_bf16_f32 v60, v60, v61\n"
      "v_mfma_f32_16x16x32_bf16 v[28:31], a[28:31], v[68:71], v[24:27]\n"
      "s_cbranch_vccz Lconv_h2\n"
      "s_barrier\n"
      "Lconv_h2:\n"
      "ds_read_b128 a[4:7], v86 offset:49184\n"
      "ds_read_b128 a[8:11], v84 offset:46112\n"
      "ds_read_b128 a[12:15], v85 offset:46112\n"
      "ds_read_b128 a[16:19], v86 offset:49248\n"
      "v_mfma_f32_16x16x32_bf16 v[24:27], a[28:31], v[72:75], v[24:27]\n"
      "ds_read_b128 a[20:23], v84 offset:46176\n"
      "ds_read_b128 a[24:27], v86 offset:58400\n"
      "buffer_load_dwordx4 v89, s[28:31], s97 offen offset:2048 lds\n"
      "v_accvgpr_write_b32 a196, v58\n"
      "v_mfma_f32_16x16x32_bf16 v[32:35], a[32:35], v[76:79], 0\n"
      "s_cmp_lt_u32 s40, 1\n"
      "s_cselect_b32 s97, -9248, 0\n"
      "ds_read_b128 a[28:31], v84 offset:55328\n"
      "ds_read_b128 a[32:35], v85 offset:55328\n"
      "s_add_u32 s97, s97, s92\n"
      "s_add_u32 m0, s97, 52320\n"
      "s_and_b32 s97, 0x3c242400, s14\n"
      "s_xor_b32 s97, 0x332d2721, s97\n"
      "s_bfe_u32 s97, s97, s43\n"
      "s_mul_i32 s97, s97, 3072\n"
      "v_accvgpr_write_b32 a197, v60\n"
      "v_cvt_pk_bf16_f32 v116, v62, v63\n"
      "v_mfma_f32_16x16x32_bf16 v[54:57], a[0:3], v[68:71], v[32:35]\n"
      "ds_read_b64_tr_b16 a[160:161], v87 offset:46112\n"
      "ds_read_b64_tr_b16 a[164:165], v88 offset:46112\n"
      "buffer_load_dwordx4 v8, s[28:31], s97 offen offset:0 lds\n"
      "v_cvt_pk_bf16_f32 v117, v64, v65\n"
      "v_mfma_f32_16x16x32_bf16 v[32:35], a[0:3], v[72:75], v[32:35]\n"
      "s_waitcnt lgkmcnt(9)\n"
      "ds_read_b64_tr_b16 a[168:169], v87 offset:46176\n"
      "ds_read_b64_tr_b16 a[90:91], v87 offset:55328\n"
      "v_cvt_pk_bf16_f32 v112, v20, v21\n"
      "v_cvt_pk_bf16_f32 v113, v22, v23\n"
      "v_mfma_f32_16x16x32_bf16 v[58:61], a[4:7], v[76:79], 0\n"
      "s_waitcnt lgkmcnt(10)\n"
      "ds_read_b64_tr_b16 a[94:95], v88 offset:55328\n"
      "ds_read_b128 a[0:3], v86 offset:58464\n"
      "v_cvt_pk_bf16_f32 v120, v24, v25\n"
      "v_cvt_pk_bf16_f32 v121, v26, v27\n"
      "v_mfma_f32_16x16x32_bf16 v[62:65], a[8:11], v[68:71], v[58:61]\n"
      "ds_read_b64_tr_b16 a[98:99], v87 offset:55392\n"
      "buffer_load_dwordx4 v9, s[28:31], s97 offen offset:1024 lds\n"
      "v_cvt_pk_bf16_f32 v28, v28, v29\n"
      "v_cvt_pk_bf16_f32 v30, v30, v31\n"
      "v_mfma_f32_16x16x32_bf16 v[58:61], a[8:11], v[72:75], v[58:61]\n"
      "v_accvgpr_write_b32 a204, v28\n"
      "v_accvgpr_write_b32 a205, v30\n"
      "v_cvt_pk_bf16_f32 v128, v32, v33\n"
      "v_cvt_pk_bf16_f32 v129, v34, v35\n"
      "v_mfma_f32_16x16x32_bf16 v[20:23], a[4:7], v[80:83], 0\n"
      "s_waitcnt lgkmcnt(7)\n"
      "ds_read_b128 a[4:7], v84 offset:55392\n"
      "v_cvt_pk_bf16_f32 v124, v54, v55\n"
      "v_mfma_f32_16x16x32_bf16 v[24:27], a[12:15], v[68:71], v[20:23]\n"
      "buffer_load_dwordx4 v89, s[28:31], s97 offen offset:2048 lds\n"
      "v_cvt_pk_bf16_f32 v125, v56, v57\n"
      "v_mfma_f32_16x16x32_bf16 v[20:23], a[12:15], v[72:75], v[20:23]\n"
      "s_add_u32 m0, s46, 13280\n"
      "s_add_u32 s51, s51, s12\n"
      "s_nop 0\n"
      "v_cvt_pk_bf16_f32 v132, v58, v59\n"
      "v_cvt_pk_bf16_f32 v133, v60, v61\n"
      "v_mfma_f32_16x16x32_bf16 v[28:31], a[16:19], v[76:79], 0\n"
      "v_cvt_pk_bf16_f32 v62, v62, v63\n"
      "v_cvt_pk_bf16_f32 v64, v64, v65\n"
      "v_mfma_f32_16x16x32_bf16 v[32:35], a[20:23], v[68:71], v[28:31]\n"
      "buffer_load_dwordx4 v16, s[32:35], s51 offen lds\n"
      "v_accvgpr_write_b32 a212, v62\n"
      "v_accvgpr_write_b32 a213, v64\n"
      "v_mfma_f32_16x16x32_bf16 v[28:31], a[20:23], v[72:75], v[28:31]\n"
      "s_add_u32 m0, s46, 13280\n"
      "v_cvt_pk_bf16_f32 v140, v20, v21\n"
      "v_cvt_pk_bf16_f32 v141, v22, v23\n"
      "v_mfma_f32_16x16x32_bf16 v[54:57], a[24:27], v[76:79], 0\n"
      "buffer_load_dwordx4 v17, s[32:35], s51 offen offset:1024 lds\n"
      "v_cvt_pk_bf16_f32 v136, v24, v25\n"
      "v_mfma_f32_16x16x32_bf16 v[58:61], a[28:31], v[68:71], v[54:57]\n"
      "s_add_u32 m0, s46, 13280\n"
      "s_add_u32 s96, s51, s15\n"
      "buffer_load_dwordx4 v18, s[32:35], s51 offen offset:2048 lds\n"
      "v_cvt_pk_bf16_f32 v137, v26, v27\n"
      "v_mfma_f32_16x16x32_bf16 v[54:57], a[28:31], v[72:75], v[54:57]\n"
      "s_waitcnt lgkmcnt(0)\n"
      "s_waitcnt vmcnt(12)\n"
      "s_cbranch_vccnz Lconv_h3\n"
      "s_barrier\n"
      "Lconv_h3:\n"
      "s_mov_b32 m0, s59\n"
      "v_cvt_pk_bf16_f32 v144, v28, v29\n"
      "v_cvt_pk_bf16_f32 v145, v30, v31\n"
      "v_mfma_f32_16x16x32_bf16 v[62:65], a[24:27], v[80:83], 0\n"
      "buffer_load_dwordx4 v19, s[32:35], s96 offen lds\n"
      "v_cvt_pk_bf16_f32 v32, v32, v33\n"
      "v_mfma_f32_16x16x32_bf16 v[20:23], a[32:35], v[68:71], v[62:65]\n"
      "s_cbranch_vccz Lconv_h4\n"
      "s_barrier\n"
      "Lconv_h4:\n"
      "s_add_u32 m0, s46, 26560\n"
      "s_add_u32 s51, s51, s12\n"
      "s_nop 0\n"
      "ds_read_b128 a[8:11], v86 offset:21536\n"
      "ds_read_b128 a[12:15], v84 offset:18464\n"
      "ds_read_b128 a[16:19], v85 offset:18464\n"
      "ds_read_b128 a[20:23], v86 offset:21600\n"
      "buffer_load_dwordx4 v16, s[32:35], s51 offen lds\n"
      "v_mfma_f32_16x16x32_bf16 v[62:65], a[32:35], v[72:75], v[62:65]\n"
      "s_add_u32 m0, s46, 26560\n"
      "ds_read_b128 a[24:27], v84 offset:18528\n"
      "ds_read_b128 a[28:31], v86 offset:3072\n"
      "buffer_load_dwordx4 v17, s[32:35], s51 offen offset:1024 lds\n"
      "v_cvt_pk_bf16_f32 v34, v34, v35\n"
      "v_mfma_f32_16x16x32_bf16 v[24:27], a[0:3], v[76:79], 0\n"
      "s_add_u32 m0, s46, 26560\n"
      "ds_read_b128 a[32:35], v84 offset:0\n"
      "ds_read_b128 a[0:3], v85 offset:0\n"
      "buffer_load_dwordx4 v18, s[32:35], s51 offen offset:2048 lds\n"
      "v_accvgpr_write_b32 a216, v32\n"
      "v_mfma_f32_16x16x32_bf16 v[28:31], a[4:7], v[68:71], v[24:27]\n"
      "s_add_u32 m0, s92, 73824\n"
      "s_and_b32 s97, 0x24242c, s14\n"
      "ds_read_b64_tr_b16 a[126:127], v87 offset:18464\n"
      "ds_read_b64_tr_b16 a[130:131], v88 offset:18464\n"
      "s_xor_b32 s97, 0x16100a04, s97\n"
      "s_bfe_u32 s97, s97, s43\n"
      "s_mul_i32 s97, s97, 3072\n"
      "buffer_load_dwordx4 v8, s[28:31], s97 offen offset:0 lds\n"
      "v_accvgpr_write_b32 a217, v34\n"
      "v_mfma_f32_16x16x32_bf16 v[24:27], a[4:7], v[72:75], v[24:27]\n"
      "s_waitcnt lgkmcnt(9)\n"
      "ds_read_b128 a[4:7], v86 offset:3136\n"
      "ds_read_b64_tr_b16 a[134:135], v87 offset:18528\n"
      "v_cvt_pk_bf16_f32 v98, v54, v55\n"
      "v_cvt_pk_bf16_f32 v99, v56, v57\n"
      "v_mfma_f32_16x16x32_bf16 v[32:35], a[8:11], v[76:79], 0\n"
      "s_waitcnt lgkmcnt(10)\n"
      "ds_read_b64_tr_b16 a[162:163], v87 offset:0\n"
      "ds_read_b64_tr_b16 a[166:167], v88 offset:0\n"
      "v_cvt_pk_bf16_f32 v94, v58, v59\n"
      "v_cvt_pk_bf16_f32 v95, v60, v61\n"
      "v_mfma_f32_16x16x32_bf16 v[54:57], a[12:15], v[68:71], v[32:35]\n"
      "ds_read_b64_tr_b16 a[170:171], v87 offset:64\n"
      "v_cvt_pk_bf16_f32 v106, v62, v63\n"
      "v_cvt_pk_bf16_f32 v107, v64, v65\n"
      "v_cvt_pk_bf16_f32 v102, v20, v21\n"
      "v_mfma_f32_16x16x32_bf16 v[32:35], a[12:15], v[72:75], v[32:35]\n"
      "buffer_load_dwordx4 v9, s[28:31], s97 offen offset:1024 lds\n"
      "v_cvt_pk_bf16_f32 v103, v22, v23\n"
      "v_cvt_pk_bf16_f32 v110, v24, v25\n"
      "v_cvt_pk_bf16_f32 v111, v26, v27\n"
      "v_mfma_f32_16x16x32_bf16 v[58:61], a[8:11], v[80:83], 0\n"
      "s_waitcnt lgkmcnt(7)\n"
      "ds_read_b128 a[8:11], v84 offset:64\n"
      "v_cvt_pk_bf16_f32 v28, v28, v29\n"
      "v_cvt_pk_bf16_f32 v30, v30, v31\n"
      "v_accvgpr_write_b32 a198, v28\n"
      "v_mfma_f32_16x16x32_bf16 v[62:65], a[16:19], v[68:71], v[58:61]\n"
      "Lconv_p1:\n"
      "s_cbranch_execz Lconv_r0\n"
      "s_nop 0\n"
      "v_accvgpr_write_b32 a199, v30\n"
      "v_cvt_pk_bf16_f32 v118, v32, v33\n"
      "v_cvt_pk_bf16_f32 v119, v34, v35\n"
      "v_add_u32 v86, -46144, v86\n"
      "v_mfma_f32_16x16x32_bf16 v[58:61], a[16:19], v[72:75], v[58:61]\n"
      "ds_read_b128 a[12:15], v86 offset:30752\n"
      "v_cvt_pk_bf16_f32 v114, v54, v55\n"
      "v_cvt_pk_bf16_f32 v115, v56, v57\n"
      "v_add_u32 v84, -46144, v84\n"
      "v_mfma_f32_16x16x32_bf16 v[20:23], a[20:23], v[76:79], 0\n"
      "ds_read_b128 a[16:19], v84 offset:27680\n"
      "buffer_load_dwordx4 v89, s[28:31], s97 offen offset:2048 lds\n"
      "v_cvt_pk_bf16_f32 v62, v62, v63\n"
      "v_mfma_f32_16x16x32_bf16 v[24:27], a[24:27], v[68:71], v[20:23]\n"
      "s_add_u32 m0, s92, 86112\n"
      "s_and_b32 s97, 0x242c0000, s14\n"
      "s_xor_b32 s97, 0x2e28221c, s97\n"
      "s_bfe_u32 s97, s97, s43\n"
      "s_nop 0\n"
      "s_mul_i32 s97, s97, 3072\n"
      "v_cvt_pk_bf16_f32 v122, v58, v59\n"
      "v_cvt_pk_bf16_f32 v123, v60, v61\n"
      "v_mfma_f32_16x16x32_bf16 v[20:23], a[24:27], v[72:75], v[20:23]\n"
      "ds_read_b128 a[24:27], v86 offset:30816\n"
      "v_cvt_pk_bf16_f32 v64, v64, v65\n"
      "v_accvgpr_write_b32 a206, v62\n"
      "v_mfma_f32_16x16x32_bf16 v[28:31], a[28:31], v[76:79], 0\n"
      "v_accvgpr_write_b32 a207, v64\n"
      "v_add_u32 v85, -46144, v85\n"
      "v_mfma_f32_16x16x32_bf16 v[32:35], a[32:35], v[68:71], v[28:31]\n"
      "ds_read_b128 a[20:23], v85 offset:27680\n"
      "v_cvt_pk_bf16_f32 v130, v20, v21\n"
      "v_cvt_pk_bf16_f32 v131, v22, v23\n"
      "v_mfma_f32_16x16x32_bf16 v[28:31], a[32:35], v[72:75], v[28:31]\n"
      "buffer_load_dwordx4 v8, s[28:31], s97 offen offset:0 lds\n"
      "v_add_u32 v87, -46144, v87\n"
      "v_add_u32 v88, -46144, v88\n"
      "v_mfma_f32_16x16x32_bf16 v[54:57], a[28:31], v[80:83], 0\n"
      "ds_read_b64_tr_b16 a[100:101], v87 offset:27680\n"
      "ds_read_b64_tr_b16 a[104:105], v88 offset:27680\n"
      "v_mfma_f32_16x16x32_bf16 v[58:61], a[0:3], v[68:71], v[54:57]\n"
      "ds_read_b128 a[28:31], v84 offset:27744\n"
      "ds_read_b64_tr_b16 a[108:109], v87 offset:27744\n"
      "v_cvt_pk_bf16_f32 v126, v24, v25\n"
      "v_cvt_pk_bf16_f32 v127, v26, v27\n"
      "v_mfma_f32_16x16x32_bf16 v[54:57], a[0:3], v[72:75], v[54:57]\n"
      "s_waitcnt lgkmcnt(5)\n"
      "s_nop 0\n"
      "v_cvt_pk_bf16_f32 v134, v28, v29\n"
      "v_cvt_pk_bf16_f32 v135, v30, v31\n"
      "v_mfma_f32_16x16x32_bf16 v[62:65], a[4:7], v[76:79], 0\n"
      "buffer_load_dwordx4 v9, s[28:31], s97 offen offset:1024 lds\n"
      "v_cvt_pk_bf16_f32 v32, v32, v33\n"
      "v_mfma_f32_16x16x32_bf16 v[20:23], a[8:11], v[68:71], v[62:65]\n"
      "v_cvt_pk_bf16_f32 v34, v34, v35\n"
      "v_accvgpr_write_b32 a214, v32\n"
      "v_accvgpr_write_b32 a215, v34\n"
      "v_mfma_f32_16x16x32_bf16 v[62:65], a[8:11], v[72:75], v[62:65]\n"
      "v_cvt_pk_bf16_f32 v142, v54, v55\n"
      "v_cvt_pk_bf16_f32 v143, v56, v57\n"
      "v_mfma_f32_16x16x32_bf16 v[24:27], a[12:15], v[76:79], 0\n"
      "v_cvt_pk_bf16_f32 v138, v58, v59\n"
      "v_cvt_pk_bf16_f32 v139, v60, v61\n"
      "v_mfma_f32_16x16x32_bf16 v[28:31], a[16:19], v[68:71], v[24:27]\n"
      "v_cvt_pk_bf16_f32 v20, v20, v21\n"
      "v_cvt_pk_bf16_f32 v22, v22, v23\n"
      "v_mfma_f32_16x16x32_bf16 v[24:27], a[16:19], v[72:75], v[24:27]\n"
      "s_waitcnt lgkmcnt(0)\n"
      "s_waitcnt vmcnt(12)\n"
      "s_cbranch_vccnz Lconv_h5\n"
      "s_barrier\n"
      "Lconv_h5:\n"
      "buffer_load_dwordx4 v89, s[28:31], s97 offen offset:2048 lds\n"
      "v_mfma_f32_16x16x32_bf16 v[32:35], a[12:15], v[80:83], 0\n"
      "s_add_u32 m0, s92, 98400\n"
      "s_and_b32 s97, 0x24242c24, s14\n"
      "s_xor_b32 s97, 0x110b0534, s97\n"
      "s_bfe_u32 s97, s97, s43\n"
      "s_mul_i32 s97, s97, 3072\n"
      "v_cvt_pk_bf16_f32 v146, v62, v63\n"
      "v_cvt_pk_bf16_f32 v147, v64, v65\n"
      "v_mfma_f32_16x16x32_bf16 v[54:57], a[20:23], v[68:71], v[32:35]\n"
      "s_cbranch_vccz Lconv_h6\n"
      "s_barrier\n"
      "Lconv_h6:\n"
      "s_nop 0\n"
      "ds_read_b128 a[32:35], v86 offset:12288\n"
      "ds_read_b128 a[0:3], v84 offset:9216\n"
      "ds_read_b128 a[4:7], v85 offset:9216\n"
      "ds_read_b128 a[8:11], v86 offset:12352\n"
      "v_mfma_f32_16x16x32_bf16 v[32:35], a[20:23], v[72:75], v[32:35]\n"
      "ds_read_b128 a[12:15], v84 offset:9280\n"
      "ds_read_b128 a[16:19], v86 offset:3072\n"
      "v_accvgpr_write_b32 a218, v20\n"
      "v_accvgpr_write_b32 a219, v22\n"
      "v_mfma_f32_16x16x32_bf16 v[58:61], a[24:27], v[76:79], 0\n"
      "ds_read_b128 a[20:23], v84 offset:0\n"
      "ds_read_b128 a[24:27], v85 offset:0\n"
      "buffer_load_dwordx4 v8, s[28:31], s97 offen offset:0 lds\n"
      "v_cvt_pk_bf16_f32 v152, v24, v25\n"
      "v_mfma_f32_16x16x32_bf16 v[62:65], a[28:31], v[68:71], v[58:61]\n"
      "ds_read_b64_tr_b16 a[136:137], v87 offset:9216\n"
      "ds_read_b64_tr_b16 a[140:141], v88 offset:9216\n"
      "v_cvt_pk_bf16_f32 v153, v26, v27\n"
      "v_cvt_pk_bf16_f32 v148, v28, v29\n"
      "v_mfma_f32_16x16x32_bf16 v[58:61], a[28:31], v[72:75], v[58:61]\n"
      "s_waitcnt lgkmcnt(9)\n"
      "ds_read_b128 a[28:31], v86 offset:3136\n"
      "ds_read_b64_tr_b16 a[144:145], v87 offset:9280\n"
      "v_cvt_pk_bf16_f32 v149, v30, v31\n"
      "v_cvt_pk_bf16_f32 v156, v32, v33\n"
      "v_mfma_f32_16x16x32_bf16 v[20:23], a[32:35], v[76:79], 0\n"
      "s_waitcnt lgkmcnt(10)\n"
      "ds_read_b64_tr_b16 a[172:173], v87 offset:0\n"
      "ds_read_b64_tr_b16 a[176:177], v88 offset:0\n"
      "v_cvt_pk_bf16_f32 v157, v34, v35\n"
      "v_mfma_f32_16x16x32_bf16 v[24:27], a[0:3], v[68:71], v[20:23]\n"
      "ds_read_b64_tr_b16 a[180:181], v87 offset:64\n"
      "ds_read_b64_tr_b16 a[102:103], v87 offset:18464\n"
      "v_cvt_pk_bf16_f32 v54, v54, v55\n"
      "v_cvt_pk_bf16_f32 v56, v56, v57\n"
      "v_mfma_f32_16x16x32_bf16 v[20:23], a[0:3], v[72:75], v[20:23]\n"
      "ds_read_b128 a[0:3], v86 offset:21536\n"
      "ds_read_b64_tr_b16 a[106:107], v88 offset:18464\n"
      "buffer_load_dwordx4 v9, s[28:31], s97 offen offset:1024 lds\n"
      "v_accvgpr_write_b32 a200, v54\n"
      "v_mfma_f32_16x16x32_bf16 v[28:31], a[32:35], v[80:83], 0\n"
      "s_waitcnt lgkmcnt(10)\n"
      "s_nop 0\n"
      "ds_read_b128 a[32:35], v84 offset:64\n"
      "ds_read_b64_tr_b16 a[110:111], v87 offset:18528\n"
      "v_accvgpr_write_b32 a201, v56\n"
      "v_cvt_pk_bf16_f32 v164, v58, v59\n"
      "v_mfma_f32_16x16x32_bf16 v[32:35], a[4:7], v[68:71], v[28:31]\n"
      "ds_read_b64_tr_b16 a[138:139], v87 offset:36928\n"
      "ds_read_b64_tr_b16 a[142:143], v88 offset:36928\n"
      "v_cvt_pk_bf16_f32 v165, v60, v61\n"
      "v_cvt_pk_bf16_f32 v160, v62, v63\n"
      "v_mfma_f32_16x16x32_bf16 v[28:31], a[4:7], v[72:75], v[28:31]\n"
      "ds_read_b128 a[4:7], v84 offset:18464\n"
      "ds_read_b64_tr_b16 a[146:147], v87 offset:36992\n"
      "v_cvt_pk_bf16_f32 v161, v64, v65\n"
      "v_cvt_pk_bf16_f32 v168, v20, v21\n"
      "v_mfma_f32_16x16x32_bf16 v[54:57], a[8:11], v[76:79], 0\n"
      "ds_read_b128 a[8:11], v85 offset:18464\n"
      "ds_read_b64_tr_b16 a[174:175], v87 offset:55392\n"
      "buffer_load_dwordx4 v89, s[28:31], s97 offen offset:2048 lds\n"
      "v_cvt_pk_bf16_f32 v169, v22, v23\n"
      "v_mfma_f32_16x16x32_bf16 v[58:61], a[12:15], v[68:71], v[54:57]\n"
      "s_cmp_lt_u32 s40, 3\n"
      "s_cselect_b32 s97, 27680, 0\n"
      "ds_read_b64_tr_b16 a[178:179], v88 offset:55392\n"
      "ds_read_b64_tr_b16 a[182:183], v87 offset:55456\n"
      "s_add_u32 s97, s97, s92\n"
      "s_add_u32 m0, s97, 36928\n"
      "s_and_b32 s97, 0x2c000000, s14\n"
      "s_xor_b32 s97, 0x29231d17, s97\n"
      "s_bfe_u32 s97, s97, s43\n"
      "s_nop 0\n"
      "s_mul_i32 s97, s97, 3072\n"
      "v_cvt_pk_bf16_f32 v24, v24, v25\n"
      "v_cvt_pk_bf16_f32 v26, v26, v27\n"
      "v_mfma_f32_16x16x32_bf16 v[54:57], a[12:15], v[72:75], v[54:57]\n"
      "ds_read_b128 a[12:15], v86 offset:21600\n"
      "v_accvgpr_write_b32 a208, v24\n"
      "v_accvgpr_write_b32 a209, v26\n"
      "v_cvt_pk_bf16_f32 v176, v28, v29\n"
      "v_mfma_f32_16x16x32_bf16 v[62:65], a[16:19], v[76:79], 0\n"
      "v_cvt_pk_bf16_f32 v177, v30, v31\n"
      "v_cvt_pk_bf16_f32 v172, v32, v33\n"
      "v_cvt_pk_bf16_f32 v173, v34, v35\n"
      "v_mfma_f32_16x16x32_bf16 v[20:23], a[20:23], v[68:71], v[62:65]\n"
      "v_cvt_pk_bf16_f32 v54, v54, v55\n"
      "v_cvt_pk_bf16_f32 v56, v56, v57\n"
      "v_mfma_f32_16x16x32_bf16 v[62:65], a[20:23], v[72:75], v[62:65]\n"
      "ds_read_b128 a[20:23], v86 offset:40000\n"
      "buffer_load_dwordx4 v8, s[28:31], s97 offen offset:0 lds\n"
      "v_accvgpr_write_b32 a228, v54\n"
      "v_accvgpr_write_b32 a229, v56\n"
      "v_mfma_f32_16x16x32_bf16 v[24:27], a[16:19], v[80:83], 0\n"
      "ds_read_b128 a[16:19], v84 offset:18528\n"
      "v_cvt_pk_bf16_f32 v180, v58, v59\n"
      "v_mfma_f32_16x16x32_bf16 v[28:31], a[24:27], v[68:71], v[24:27]\n"
      "v_cvt_pk_bf16_f32 v181, v60, v61\n"
      "v_cvt_pk_bf16_f32 v188, v62, v63\n"
      "v_mfma_f32_16x16x32_bf16 v[24:27], a[24:27], v[72:75], v[24:27]\n"
      "s_waitcnt lgkmcnt(2)\n"
      "s_nop 0\n"
      "ds_read_b128 a[24:27], v84 offset:36928\n"
      "v_cvt_pk_bf16_f32 v189, v64, v65\n"
      "v_cvt_pk_bf16_f32 v184, v20, v21\n"
      "v_cvt_pk_bf16_f32 v185, v22, v23\n"
      "v_mfma_f32_16x16x32_bf16 v[32:35], a[28:31], v[76:79], 0\n"
      "ds_read_b128 a[28:31], v85 offset:36928\n"
      "buffer_load_dwordx4 v9, s[28:31], s97 offen offset:1024 lds\n"
      "v_mfma_f32_16x16x32_bf16 v[54:57], a[32:35], v[68:71], v[32:35]\n"
      "v_cvt_pk_bf16_f32 v24, v24, v25\n"
      "v_cvt_pk_bf16_f32 v26, v26, v27\n"
      "v_mfma_f32_16x16x32_bf16 v[32:35], a[32:35], v[72:75], v[32:35]\n"
      "ds_read_b128 a[32:35], v86 offset:40064\n"
      "v_accvgpr_write_b32 a236, v24\n"
      "v_accvgpr_write_b32 a237, v26\n"
      "v_mfma_f32_16x16x32_bf16 v[58:61], a[0:3], v[76:79], 0\n"
      "v_cvt_pk_bf16_f32 v192, v28, v29\n"
      "v_cvt_pk_bf16_f32 v193, v30, v31\n"
      "v_mfma_f32_16x16x32_bf16 v[62:65], a[4:7], v[68:71], v[58:61]\n"
      "v_cvt_pk_bf16_f32 v196, v54, v55\n"
      "v_cvt_pk_bf16_f32 v200, v32, v33\n"
      "v_mfma_f32_16x16x32_bf16 v[58:61], a[4:7], v[72:75], v[58:61]\n"
      "ds_read_b128 a[4:7], v86 offset:58464\n"
      "buffer_load_dwordx4 v89, s[28:31], s97 offen offset:2048 lds\n"
      "v_mfma_f32_16x16x32_bf16 v[20:23], a[0:3], v[80:83], 0\n"
      "s_cmp_lt_u32 s40, 2\n"
      "s_cselect_b32 s97, 6144, 0\n"
      "ds_read_b128 a[0:3], v84 offset:36992\n"
      "s_add_u32 s97, s97, s92\n"
      "s_add_u32 m0, s97, 43072\n"
      "s_and_b32 s97, 0x24242424, s14\n"
      "s_xor_b32 s97, 0x352f352f, s97\n"
      "s_bfe_u32 s97, s97, s43\n"
      "s_mul_i32 s97, s97, 3072\n"
      "v_cvt_pk_bf16_f32 v201, v34, v35\n"
      "v_mfma_f32_16x16x32_bf16 v[24:27], a[8:11], v[68:71], v[20:23]\n"
      "v_cvt_pk_bf16_f32 v197, v56, v57\n"
      "v_cvt_pk_bf16_f32 v154, v58, v59\n"
      "v_cvt_pk_bf16_f32 v155, v60, v61\n"
      "v_mfma_f32_16x16x32_bf16 v[20:23], a[8:11], v[72:75], v[20:23]\n"
      "ds_read_b128 a[8:11], v84 offset:55392\n"
      "v_cvt_pk_bf16_f32 v150, v62, v63\n"
      "v_cvt_pk_bf16_f32 v151, v64, v65\n"
      "v_mfma_f32_16x16x32_bf16 v[28:31], a[12:15], v[76:79], 0\n"
      "s_waitcnt lgkmcnt(3)\n"
      "ds_read_b128 a[12:15], v85 offset:55392\n"
      "buffer_load_dwordx4 v8, s[28:31], s97 offen offset:0 lds\n"
      "v_cvt_pk_bf16_f32 v24, v24, v25\n"
      "v_mfma_f32_16x16x32_bf16 v[32:35], a[16:19], v[68:71], v[28:31]\n"
      "v_cvt_pk_bf16_f32 v158, v20, v21\n"
      "v_cvt_pk_bf16_f32 v159, v22, v23\n"
      "v_mfma_f32_16x16x32_bf16 v[28:31], a[16:19], v[72:75], v[28:31]\n"
      "ds_read_b128 a[16:19], v86 offset:58528\n"
      "v_cvt_pk_bf16_f32 v26, v26, v27\n"
      "v_accvgpr_write_b32 a202, v24\n"
      "v_accvgpr_write_b32 a203, v26\n"
      "v_mfma_f32_16x16x32_bf16 v[54:57], a[20:23], v[76:79], 0\n"
      "v_cvt_pk_bf16_f32 v162, v32, v33\n"
      "v_cvt_pk_bf16_f32 v163, v34, v35\n"
      "v_mfma_f32_16x16x32_bf16 v[58:61], a[24:27], v[68:71], v[54:57]\n"
      "v_cvt_pk_bf16_f32 v166, v28, v29\n"
      "v_cvt_pk_bf16_f32 v167, v30, v31\n"
      "v_mfma_f32_16x16x32_bf16 v[54:57], a[24:27], v[72:75], v[54:57]\n"
      "buffer_load_dwordx4 v9, s[28:31], s97 offen offset:1024 lds\n"
      "v_add_u32 v86, 46144, v86\n"
      "v_add_u32 v85, 46144, v85\n"
      "v_mfma_f32_16x16x32_bf16 v[62:65], a[20:23], v[80:83], 0\n"
      "ds_read_b128 a[20:23], v84 offset:55456\n"
      "v_cvt_pk_bf16_f32 v58, v58, v59\n"
      "v_mfma_f32_16x16x32_bf16 v[20:23], a[28:31], v[68:71], v[62:65]\n"
      "v_cvt_pk_bf16_f32 v60, v60, v61\n"
      "v_cvt_pk_bf16_f32 v170, v54, v55\n"
      "v_mfma_f32_16x16x32_bf16 v[62:65], a[28:31], v[72:75], v[62:65]\n"
      "v_cvt_pk_bf16_f32 v171, v56, v57\n"
      "v_accvgpr_write_b32 a210, v58\n"
      "v_mfma_f32_16x16x32_bf16 v[24:27], a[32:35], v[76:79], 0\n"
      "s_waitcnt lgkmcnt(1)\n"
      "buffer_load_dwordx4 v89, s[28:31], s97 offen offset:2048 lds\n"
      "v_accvgpr_write_b32 a211, v60\n"
      "v_mfma_f32_16x16x32_bf16 v[28:31], a[0:3], v[68:71], v[24:27]\n"
      "s_add_u32 m0, s46, 39840\n"
      "s_add_u32 s51, s51, s12\n"
      "v_cvt_pk_bf16_f32 v178, v62, v63\n"
      "v_cvt_pk_bf16_f32 v179, v64, v65\n"
      "v_mfma_f32_16x16x32_bf16 v[24:27], a[0:3], v[72:75], v[24:27]\n"
      "v_cvt_pk_bf16_f32 v174, v20, v21\n"
      "v_cvt_pk_bf16_f32 v175, v22, v23\n"
      "v_add_u32 v84, 46144, v84\n"
      "v_mfma_f32_16x16x32_bf16 v[32:35], a[4:7], v[76:79], 0\n"
      "v_cvt_pk_bf16_f32 v182, v28, v29\n"
      "v_cvt_pk_bf16_f32 v183, v30, v31\n"
      "v_mfma_f32_16x16x32_bf16 v[54:57], a[8:11], v[68:71], v[32:35]\n"
      "v_add_u32 v87, 46144, v87\n"
      "v_cvt_pk_bf16_f32 v24, v24, v25\n"
      "v_mfma_f32_16x16x32_bf16 v[32:35], a[8:11], v[72:75], v[32:35]\n"
      "s_waitcnt lgkmcnt(0)\n"
      "s_waitcnt vmcnt(6)\n"
      "s_cbranch_vccnz Lconv_h7\n"
      "s_barrier\n"
      "Lconv_h7:\n"
      "buffer_load_dwordx4 v16, s[32:35], s51 offen lds\n"
      "v_mfma_f32_16x16x32_bf16 v[58:61], a[4:7], v[80:83], 0\n"
      "s_add_u32 m0, s46, 39840\n"
      "v_cvt_pk_bf16_f32 v26, v26, v27\n"
      "v_accvgpr_write_b32 a230, v24\n"
      "v_mfma_f32_16x16x32_bf16 v[62:65], a[12:15], v[68:71], v[58:61]\n"
      "s_cbranch_vccz Lconv_h8\n"
      "s_barrier\n"
      "Lconv_h8:\n"
      "ds_read_b128 a[48:51], v2 offset:26560\n"
      "ds_read_b128 a[52:55], v3 offset:26560\n"
      "ds_read_b128 a[56:59], v4 offset:26560\n"
      "ds_read_b128 a[36:39], v2 offset:13280\n"
      "buffer_load_dwordx4 v17, s[32:35], s51 offen offset:1024 lds\n"
      "v_mfma_f32_16x16x32_bf16 v[58:61], a[12:15], v[72:75], v[58:61]\n"
      "s_add_u32 m0, s46, 39840\n"
      "ds_read_b128 a[40:43], v3 offset:13280\n"
      "ds_read_b128 a[44:47], v4 offset:13280\n"
      "buffer_load_dwordx4 v18, s[32:35], s51 offen offset:2048 lds\n"
      "v_accvgpr_write_b32 a231, v26\n"
      "v_mfma_f32_16x16x32_bf16 v[20:23], a[16:19], v[76:79], 0\n"
      "s_sub_u32 m0, s94, s59\n"
      "s_add_u32 s96, s51, s15\n"
      "ds_read_b128 a[24:27], v86 offset:30752\n"
      "ds_read_b128 a[28:31], v84 offset:27680\n"
      "buffer_load_dwordx4 v19, s[32:35], s96 offen lds\n"
      "v_cvt_pk_bf16_f32 v186, v54, v55\n"
      "v_mfma_f32_16x16x32_bf16 v[24:27], a[20:23], v[68:71], v[20:23]\n"
      "s_add_u32 m0, s46, 0\n"
      "s_add_u32 s51, s51, s12\n"
      "ds_read_b128 a[32:35], v85 offset:27680\n"
      "ds_read_b128 a[0:3], v86 offset:30816\n"
      "buffer_load_dwordx4 v16, s[32:35], s51 offen lds\n"
      "v_cvt_pk_bf16_f32 v187, v56, v57\n"
      "v_mfma_f32_16x16x32_bf16 v[20:23], a[20:23], v[72:75], v[20:23]\n"
      "s_waitcnt lgkmcnt(9)\n"
      "s_add_u32 m0, s46, 0\n"
      "ds_read_b128 a[4:7], v84 offset:27744\n"
      "ds_read_b128 a[8:11], v86 offset:39968\n"
      "buffer_load_dwordx4 v17, s[32:35], s51 offen offset:1024 lds\n"
      "v_cvt_pk_bf16_f32 v58, v58, v59\n"
      "v_mfma_f32_16x16x32_bf16 v[52:55], a[48:51], a[72:75], 0\n"
      "s_waitcnt lgkmcnt(10)\n"
      "s_add_u32 m0, s46, 0\n"
      "ds_read_b128 a[48:51], v2 offset:13344\n"
      "ds_read_b128 a[12:15], v84 offset:36896\n"
      "buffer_load_dwordx4 v18, s[32:35], s51 offen offset:2048 lds\n"
      "v_accvgpr_write_b32 a238, v58\n"
      "v_mfma_f32_16x16x32_bf16 v[56:59], a[52:55], a[76:79], 0\n"
      "s_waitcnt lgkmcnt(10)\n"
      "s_mov_b32 m0, s18\n"
      "ds_read_b128 a[52:55], v3 offset:13344\n"
      "ds_read_b128 a[16:19], v85 offset:36896\n"
      "v_cvt_pk_bf16_f32 v194, v62, v63\n"
      "v_cvt_pk_bf16_f32 v60, v60, v61\n"
      "v_accvgpr_write_b32 a239, v60\n"
      "v_mfma_f32_16x16x32_bf16 v[60:63], a[56:59], a[80:83], v[52:55]\n"
      "ds_read_b128 a[56:59], v4 offset:13344\n"
      "ds_read_b128 a[20:23], v86 offset:40032\n"
      "v_cvt_pk_bf16_f32 v190, v32, v33\n"
      "v_cvt_pk_bf16_f32 v191, v34, v35\n"
      "v_mfma_f32_16x16x32_bf16 v[36:39], a[36:39], a[72:75], 0\n"
      "s_waitcnt lgkmcnt(10)\n"
      "s_nop 0\n"
      "ds_read_b128 a[36:39], v2 offset:26624\n"
      "ds_read_b64_tr_b16 a[112:113], v87 offset:27680\n"
      "v_cvt_pk_bf16_f32 v195, v64, v65\n"
      "v_fma_f32 v64, s44, v52, v56\n"
      "v_mfma_f32_16x16x32_bf16 v[40:43], a[40:43], a[76:79], 0\n"
      "ds_read_b128 a[40:43], v3 offset:26624\n"
      "ds_read_b64_tr_b16 a[120:121], v87 offset:27744\n"
      "v_fma_f32 v65, s44, v53, v57\n"
      "v_fma_f32 v66, s44, v54, v58\n"
      "v_mfma_f32_16x16x32_bf16 v[44:47], a[44:47], a[80:83], v[36:39]\n"
      "ds_read_b128 a[44:47], v4 offset:26624\n"
      "ds_read_b64_tr_b16 a[148:149], v87 offset:36896\n"
      "v_fma_f32 v67, s44, v55, v59\n"
      "v_fma_f32 v56, s44, v52, -v56\n"
      "v_mfma_f32_16x16x32_bf16 v[28:31], a[24:27], v[76:79], 0\n"
      "ds_read_b64_tr_b16 a[156:157], v87 offset:36960\n"
      "ds_read_b64_tr_b16 a[184:185], v87 offset:46112\n"
      "v_fma_f32 v57, s44, v53, -v57\n"
      "v_fma_f32 v58, s44, v54, -v58\n"
      "v_mfma_f32_16x16x32_bf16 v[32:35], a[28:31], v[68:71], v[28:31]\n"
      "ds_read_b64_tr_b16 a[192:193], v87 offset:46176\n"
      "ds_read_b64_tr_b16 a[114:115], v87 offset:55328\n"
      "v_fma_f32 v59, s44, v55, -v59\n"
      "v_cvt_pk_bf16_f32 v56, v56, v57\n"
      "v_mfma_f32_16x16x32_bf16 v[28:31], a[28:31], v[72:75], v[28:31]\n"
      "ds_write_addtid_b32 v56 offset:6144\n"
      "ds_read_b128 a[28:31], v86 offset:49184\n"
      "v_cvt_pk_bf16_f32 v57, v58, v59\n"
      "v_cvt_pk_bf16_f32 v60, v60, v61\n"
      "ds_write_addtid_b32 v57 offset:43072\n"
      "v_mfma_f32_16x16x32_bf16 v[54:57], a[24:27], v[80:83], 0\n"
      "s_waitcnt lgkmcnt(11)\n"
      "ds_write_addtid_b32 v60 offset:4608\n"
      "ds_read_b128 a[24:27], v84 offset:36960\n"
      "v_cvt_pk_bf16_f32 v61, v62, v63\n"
      "v_cvt_pk_bf16_f32 v64, v64, v65\n"
      "ds_write_addtid_b32 v61 offset:41536\n"
      "v_mfma_f32_16x16x32_bf16 v[58:61], a[32:35], v[68:71], v[54:57]\n"
      "ds_write_addtid_b32 v64 offset:5376\n"
      "ds_read_b64_tr_b16 a[122:123], v87 offset:55392\n"
      "v_cvt_pk_bf16_f32 v65, v66, v67\n"
      "v_cvt_pk_bf16_f32 v202, v20, v21\n"
      "v_mfma_f32_16x16x32_bf16 v[54:57], a[32:35], v[72:75], v[54:57]\n"
      "ds_write_addtid_b32 v65 offset:42304\n"
      "ds_read_b128 a[32:35], v84 offset:46112\n"
      "s_mov_b32 m0, s18\n"
      "v_cvt_pk_bf16_f32 v203, v22, v23\n"
      "v_fma_f32 v48, s44, v36, v40\n"
      "v_mfma_f32_16x16x32_bf16 v[62:65], a[0:3], v[76:79], 0\n"
      "ds_read_b128 a[0:3], v85 offset:46112\n"
      "v_fma_f32 v49, s44, v37, v41\n"
      "v_fma_f32 v50, s44, v38, v42\n"
      "v_fma_f32 v51, s44, v39, v43\n"
      "v_mfma_f32_16x16x32_bf16 v[20:23], a[4:7], v[68:71], v[62:65]\n"
      "v_fma_f32 v40, s44, v36, -v40\n"
      "v_fma_f32 v41, s44, v37, -v41\n"
      "v_fma_f32 v42, s44, v38, -v42\n"
      "v_fma_f32 v43, s44, v39, -v43\n"
      "v_mfma_f32_16x16x32_bf16 v[62:65], a[4:7], v[72:75], v[62:65]\n"
      "ds_read_b128 a[4:7], v86 offset:49248\n"
      "v_cvt_pk_bf16_f32 v40, v40, v41\n"
      "v_cvt_pk_bf16_f32 v41, v42, v43\n"
      "v_cvt_pk_bf16_f32 v44, v44, v45\n"
      "v_mfma_f32_16x16x32_bf16 v[36:39], a[36:39], a[72:75], 0\n"
      "s_waitcnt lgkmcnt(7)\n"
      "s_nop 0\n"
      "ds_write_addtid_b32 v40 offset:1536\n"
      "ds_write_addtid_b32 v41 offset:38464\n"
      "ds_write_addtid_b32 v44 offset:0\n"
      "v_cvt_pk_bf16_f32 v45, v46, v47\n"
      "v_mfma_f32_16x16x32_bf16 v[40:43], a[40:43], a[76:79], 0\n"
      "ds_write_addtid_b32 v45 offset:36928\n"
      "ds_read_b128 a[36:39], v2 offset:13408\n"
      "v_cvt_pk_bf16_f32 v212, v54, v55\n"
      "v_cvt_pk_bf16_f32 v208, v58, v59\n"
      "v_mfma_f32_16x16x32_bf16 v[44:47], a[44:47], a[80:83], v[36:39]\n"
      "ds_read_b128 a[40:43], v3 offset:13408\n"
      "ds_read_b128 a[44:47], v4 offset:13408\n"
      "v_cvt_pk_bf16_f32 v213, v56, v57\n"
      "v_cvt_pk_bf16_f32 v209, v60, v61\n"
      "v_mfma_f32_16x16x32_bf16 v[52:55], a[48:51], a[72:75], 0\n"
      "ds_read_b128 a[48:51], v2 offset:26688\n"
      "v_cvt_pk_bf16_f32 v62, v62, v63\n"
      "v_accvgpr_write_b32 a224, v62\n"
      "v_cvt_pk_bf16_f32 v198, v24, v25\n"
      "v_mfma_f32_16x16x32_bf16 v[56:59], a[52:55], a[76:79], 0\n"
      "ds_read_b128 a[52:55], v3 offset:26688\n"
      "v_cvt_pk_bf16_f32 v199, v26, v27\n"
      "v_cvt_pk_bf16_f32 v48, v48, v49\n"
      "v_cvt_pk_bf16_f32 v49, v50, v51\n"
      "v_mfma_f32_16x16x32_bf16 v[60:63], a[56:59], a[80:83], v[52:55]\n"
      "ds_write_addtid_b32 v48 offset:768\n"
      "ds_write_addtid_b32 v49 offset:37696\n"
      "ds_read_b128 a[56:59], v4 offset:26688\n"
      "s_mov_b32 m0, s18\n"
      "s_nop 0\n"
      "v_cvt_pk_bf16_f32 v28, v28, v29\n"
      "v_mfma_f32_16x16x32_bf16 v[24:27], a[8:11], v[76:79], 0\n"
      "v_cvt_pk_bf16_f32 v30, v30, v31\n"
      "v_accvgpr_write_b32 a220, v28\n"
      "v_accvgpr_write_b32 a221, v30\n"
      "v_fma_f32 v48, s44, v36, v40\n"
      "v_mfma_f32_16x16x32_bf16 v[28:31], a[12:15], v[68:71], v[24:27]\n"
      "v_fma_f32 v49, s44, v37, v41\n"
      "v_fma_f32 v50, s44, v38, v42\n"
      "v_fma_f32 v51, s44, v39, v43\n"
      "v_fma_f32 v40, s44, v36, -v40\n"
      "v_mfma_f32_16x16x32_bf16 v[24:27], a[12:15], v[72:75], v[24:27]\n"
      "ds_read_b128 a[12:15], v86 offset:58400\n"
      "v_fma_f32 v41, s44, v37, -v41\n"
      "v_fma_f32 v42, s44, v38, -v42\n"
      "v_fma_f32 v43, s44, v39, -v43\n"
      "v_cvt_pk_bf16_f32 v204, v32, v33\n"
      "v_cvt_pk_bf16_f32 v205, v34, v35\n"
      "v_mfma_f32_16x16x32_bf16 v[32:35], a[8:11], v[80:83], 0\n"
      "ds_read_b128 a[8:11], v84 offset:46176\n"
      "v_cvt_pk_bf16_f32 v48, v48, v49\n"
      "v_cvt_pk_bf16_f32 v49, v50, v51\n"
      "v_cvt_pk_bf16_f32 v40, v40, v41\n"
      "v_cvt_pk_bf16_f32 v64, v64, v65\n"
      "v_accvgpr_write_b32 a225, v64\n"
      "v_cvt_pk_bf16_f32 v41, v42, v43\n"
      "v_cvt_pk_bf16_f32 v44, v44, v45\n"
      "v_cvt_pk_bf16_f32 v45, v46, v47\n"
      "ds_write_addtid_b32 v48 offset:5632\n"
      "ds_write_addtid_b32 v49 offset:42560\n"
      "ds_write_addtid_b32 v40 offset:6400\n"
      "ds_write_addtid_b32 v41 offset:43328\n"
      "ds_write_addtid_b32 v44 offset:4864\n"
      "ds_write_addtid_b32 v45 offset:41792\n"
      "v_fma_f32 v64, s44, v52, v56\n"
      "v_fma_f32 v65, s44, v53, v57\n"
      "v_fma_f32 v66, s44, v54, v58\n"
      "v_fma_f32 v67, s44, v55, v59\n"
      "v_fma_f32 v56, s44, v52, -v56\n"
      "v_fma_f32 v57, s44, v53, -v57\n"
      "v_fma_f32 v58, s44, v54, -v58\n"
      "v_fma_f32 v59, s44, v55, -v59\n"
      "v_cvt_pk_bf16_f32 v56, v56, v57\n"
      "v_cvt_pk_bf16_f32 v57, v58, v59\n"
      "s_mov_b32 m0, s18\n"
      "s_nop 0\n"
      "ds_write_addtid_b32 v56 offset:1792\n"
      "ds_write_addtid_b32 v57 offset:38720\n"
      "v_mfma_f32_16x16x32_bf16 v[54:57], a[16:19], v[68:71], v[32:35]\n"
      "v_cvt_pk_bf16_f32 v60, v60, v61\n"
      "v_cvt_pk_bf16_f32 v61, v62, v63\n"
      "v_cvt_pk_bf16_f32 v64, v64, v65\n"
      "v_cvt_pk_bf16_f32 v65, v66, v67\n"
      "v_mfma_f32_16x16x32_bf16 v[32:35], a[16:19], v[72:75], v[32:35]\n"
      "ds_write_addtid_b32 v60 offset:256\n"
      "ds_write_addtid_b32 v61 offset:37184\n"
      "ds_write_addtid_b32 v64 offset:1024\n"
      "ds_write_addtid_b32 v65 offset:37952\n"
      "s_mov_b32 m0, s18\n"
      "v_mfma_f32_16x16x32_bf16 v[58:61], a[20:23], v[76:79], 0\n"
      "ds_read_b128 a[16:19], v84 offset:55328\n"
      "ds_read_b128 a[20:23], v85 offset:55328\n"
      "v_cvt_pk_bf16_f32 v228, v54, v55\n"
      "v_cvt_pk_bf16_f32 v229, v56, v57\n"
      "v_mfma_f32_16x16x32_bf16 v[62:65], a[24:27], v[68:71], v[58:61]\n"
      "v_cvt_pk_bf16_f32 v216, v20, v21\n"
      "v_cvt_pk_bf16_f32 v217, v22, v23\n"
      "v_cvt_pk_bf16_f32 v224, v24, v25\n"
      "v_cvt_pk_bf16_f32 v225, v26, v27\n"
      "v_mfma_f32_16x16x32_bf16 v[58:61], a[24:27], v[72:75], v[58:61]\n"
      "s_waitcnt lgkmcnt(14)\n"
      "ds_read_b128 a[24:27], v86 offset:58464\n"
      "v_cvt_pk_bf16_f32 v220, v28, v29\n"
      "v_cvt_pk_bf16_f32 v232, v62, v63\n"
      "v_cvt_pk_bf16_f32 v221, v30, v31\n"
      "v_mfma_f32_16x16x32_bf16 v[52:55], a[48:51], a[72:75], 0\n"
      "v_cvt_pk_bf16_f32 v32, v32, v33\n"
      "v_cvt_pk_bf16_f32 v34, v34, v35\n"
      "v_cvt_pk_bf16_f32 v236, v58, v59\n"
      "v_cvt_pk_bf16_f32 v237, v60, v61\n"
      "v_mfma_f32_16x16x32_bf16 v[56:59], a[52:55], a[76:79], 0\n"
      "v_accvgpr_write_b32 a232, v32\n"
      "v_accvgpr_write_b32 a233, v34\n"
      "v_cvt_pk_bf16_f32 v233, v64, v65\n"
      "v_add_u32 v88, 46144, v88\n"
      "v_mfma_f32_16x16x32_bf16 v[60:63], a[56:59], a[80:83], v[52:55]\n"
      "ds_read_b64_tr_b16 a[116:117], v88 offset:27680\n"
      "ds_read_b64_tr_b16 a[152:153], v88 offset:36896\n"
      "v_mfma_f32_16x16x32_bf16 v[36:39], a[36:39], a[72:75], 0\n"
      "ds_read_b64_tr_b16 a[188:189], v88 offset:46112\n"
      "ds_read_b64_tr_b16 a[118:119], v88 offset:55328\n"
      "v_fma_f32 v64, s44, v52, v56\n"
      "v_fma_f32 v65, s44, v53, v57\n"
      "v_mfma_f32_16x16x32_bf16 v[40:43], a[40:43], a[76:79], 0\n"
      "Lconv_p2:\n"
      "s_cbranch_execz Lconv_r0\n"
      "s_nop 0\n"
      "v_fma_f32 v66, s44, v54, v58\n"
      "v_fma_f32 v67, s44, v55, v59\n"
      "v_fma_f32 v56, s44, v52, -v56\n"
      "v_fma_f32 v57, s44, v53, -v57\n"
      "v_mfma_f32_16x16x32_bf16 v[44:47], a[44:47], a[80:83], v[36:39]\n"
      "v_fma_f32 v58, s44, v54, -v58\n"
      "v_fma_f32 v59, s44, v55, -v59\n"
      "v_cvt_pk_bf16_f32 v56, v56, v57\n"
      "v_cvt_pk_bf16_f32 v57, v58, v59\n"
      "v_mfma_f32_16x16x32_bf16 v[20:23], a[28:31], v[76:79], 0\n"
      "ds_write_addtid_b32 v56 offset:6656\n"
      "ds_write_addtid_b32 v57 offset:43584\n"
      "v_cvt_pk_bf16_f32 v60, v60, v61\n"
      "v_cvt_pk_bf16_f32 v61, v62, v63\n"
      "v_mfma_f32_16x16x32_bf16 v[24:27], a[32:35], v[68:71], v[20:23]\n"
      "ds_write_addtid_b32 v60 offset:5120\n"
      "ds_write_addtid_b32 v61 offset:42048\n"
      "v_cvt_pk_bf16_f32 v64, v64, v65\n"
      "v_cvt_pk_bf16_f32 v65, v66, v67\n"
      "v_mfma_f32_16x16x32_bf16 v[20:23], a[32:35], v[72:75], v[20:23]\n"
      "ds_write_addtid_b32 v64 offset:5888\n"
      "ds_write_addtid_b32 v65 offset:42816\n"
      "s_mov_b32 m0, s18\n"
      "s_nop 0\n"
      "v_fma_f32 v48, s44, v36, v40\n"
      "v_fma_f32 v49, s44, v37, v41\n"
      "v_mfma_f32_16x16x32_bf16 v[28:31], a[28:31], v[80:83], 0\n"
      "ds_read_b128 a[28:31], v84 offset:55392\n"
      "v_fma_f32 v50, s44, v38, v42\n"
      "v_fma_f32 v51, s44, v39, v43\n"
      "v_fma_f32 v40, s44, v36, -v40\n"
      "v_mfma_f32_16x16x32_bf16 v[32:35], a[0:3], v[68:71], v[28:31]\n"
      "v_fma_f32 v41, s44, v37, -v41\n"
      "v_fma_f32 v42, s44, v38, -v42\n"
      "v_fma_f32 v43, s44, v39, -v43\n"
      "v_cvt_pk_bf16_f32 v48, v48, v49\n"
      "v_mfma_f32_16x16x32_bf16 v[28:31], a[0:3], v[72:75], v[28:31]\n"
      "ds_write_addtid_b32 v48 offset:1280\n"
      "v_cvt_pk_bf16_f32 v49, v50, v51\n"
      "v_cvt_pk_bf16_f32 v40, v40, v41\n"
      "v_cvt_pk_bf16_f32 v41, v42, v43\n"
      "v_mfma_f32_16x16x32_bf16 v[54:57], a[4:7], v[76:79], 0\n"
      "ds_write_addtid_b32 v49 offset:38208\n"
      "ds_write_addtid_b32 v40 offset:2048\n"
      "ds_write_addtid_b32 v41 offset:38976\n"
      "v_cvt_pk_bf16_f32 v44, v44, v45\n"
      "v_mfma_f32_16x16x32_bf16 v[58:61], a[8:11], v[68:71], v[54:57]\n"
      "ds_write_addtid_b32 v44 offset:512\n"
      "v_cvt_pk_bf16_f32 v45, v46, v47\n"
      "v_cvt_pk_bf16_f32 v20, v20, v21\n"
      "v_cvt_pk_bf16_f32 v22, v22, v23\n"
      "v_mfma_f32_16x16x32_bf16 v[54:57], a[8:11], v[72:75], v[54:57]\n"
      "ds_write_addtid_b32 v45 offset:37440\n"
      "s_add_u32 m0, s46, 13280\n"
      "s_add_u32 s51, s51, s12\n"
      "v_accvgpr_write_b32 a240, v20\n"
      "v_accvgpr_write_b32 a241, v22\n"
      "v_cvt_pk_bf16_f32 v240, v24, v25\n"
      "v_mfma_f32_16x16x32_bf16 v[62:65], a[12:15], v[76:79], 0\n"
      "s_waitcnt lgkmcnt(6)\n"
      "v_cvt_pk_bf16_f32 v241, v26, v27\n"
      "v_cvt_pk_bf16_f32 v248, v28, v29\n"
      "v_cvt_pk_bf16_f32 v249, v30, v31\n"
      "v_mfma_f32_16x16x32_bf16 v[20:23], a[16:19], v[68:71], v[62:65]\n"
      "v_cvt_pk_bf16_f32 v244, v32, v33\n"
      "v_cvt_pk_bf16_f32 v245, v34, v35\n"
      "v_mfma_f32_16x16x32_bf16 v[62:65], a[16:19], v[72:75], v[62:65]\n"
      "s_waitcnt lgkmcnt(0)\n"
      "s_waitcnt vmcnt(7)\n"
      "s_cbranch_vccnz Lconv_h9\n"
      "s_barrier\n"
      "Lconv_h9:\n"
      "v_cvt_pk_bf16_f32 v54, v54, v55\n"
      "v_cvt_pk_bf16_f32 v56, v56, v57\n"
      "v_accvgpr_write_b32 a244, v54\n"
      "v_mfma_f32_16x16x32_bf16 v[24:27], a[12:15], v[80:83], 0\n"
      "v_accvgpr_write_b32 a245, v56\n"
      "v_cvt_pk_bf16_f32 v252, v58, v59\n"
      "v_mfma_f32_16x16x32_bf16 v[28:31], a[20:23], v[68:71], v[24:27]\n"
      "s_cbranch_vccz Lconv_h10\n"
      "s_barrier\n"
      "Lconv_h10:\n"
      "ds_read_b128 a[32:35], v86 offset:21536\n"
      "ds_read_b128 a[0:3], v84 offset:18464\n"
      "ds_read_b128 a[4:7], v85 offset:18464\n"
      "ds_read_b128 a[8:11], v86 offset:21600\n"
      "v_mfma_f32_16x16x32_bf16 v[24:27], a[20:23], v[72:75], v[24:27]\n"
      "ds_read_b128 a[12:15], v84 offset:18528\n"
      "ds_read_b128 a[16:19], v86 offset:3072\n"
      "v_cvt_pk_bf16_f32 v253, v60, v61\n"
      "v_cvt_pk_bf16_f32 v62, v62, v63\n"
      "v_mfma_f32_16x16x32_bf16 v[32:35], a[24:27], v[76:79], 0\n"
      "ds_read_b128 a[20:23], v84 offset:0\n"
      "ds_read_b128 a[24:27], v85 offset:0\n"
      "v_cvt_pk_bf16_f32 v64, v64, v65\n"
      "v_mfma_f32_16x16x32_bf16 v[54:57], a[28:31], v[68:71], v[32:35]\n"
      "ds_read_b64_tr_b16 a[150:151], v87 offset:18464\n"
      "ds_read_b64_tr_b16 a[154:155], v88 offset:18464\n"
      "v_accvgpr_write_b32 a222, v62\n"
      "v_accvgpr_write_b32 a223, v64\n"
      "v_mfma_f32_16x16x32_bf16 v[32:35], a[28:31], v[72:75], v[32:35]\n"
      "s_waitcnt lgkmcnt(9)\n"
      "ds_read_b64_tr_b16 a[158:159], v87 offset:18528\n"
      "ds_read_b64_tr_b16 a[186:187], v87 offset:0\n"
      "buffer_load_dwordx4 v16, s[32:35], s51 offen lds\n"
      "v_cvt_pk_bf16_f32 v206, v20, v21\n"
      "v_mfma_f32_16x16x32_bf16 v[58:61], a[32:35], v[76:79], 0\n"
      "s_waitcnt lgkmcnt(10)\n"
      "s_add_u32 m0, s46, 13280\n"
      "ds_read_b64_tr_b16 a[190:191], v88 offset:0\n"
      "ds_read_b128 a[28:31], v86 offset:3136\n"
      "buffer_load_dwordx4 v17, s[32:35], s51 offen offset:1024 lds\n"
      "v_cvt_pk_bf16_f32 v207, v22, v23\n"
      "v_mfma_f32_16x16x32_bf16 v[62:65], a[0:3], v[68:71], v[58:61]\n"
      "s_add_u32 m0, s46, 13280\n"
      "s_add_u32 s96, s51, s15\n"
      "ds_read_b64_tr_b16 a[194:195], v87 offset:64\n"
      "buffer_load_dwordx4 v18, s[32:35], s51 offen offset:2048 lds\n"
      "v_cvt_pk_bf16_f32 v214, v24, v25\n"
      "v_cvt_pk_bf16_f32 v215, v26, v27\n"
      "v_mfma_f32_16x16x32_bf16 v[58:61], a[0:3], v[72:75], v[58:61]\n"
      "s_mov_b32 m0, s59\n"
      "v_cvt_pk_bf16_f32 v210, v28, v29\n"
      "v_cvt_pk_bf16_f32 v211, v30, v31\n"
      "v_cvt_pk_bf16_f32 v32, v32, v33\n"
      "v_cvt_pk_bf16_f32 v34, v34, v35\n"
      "v_mfma_f32_16x16x32_bf16 v[20:23], a[32:35], v[80:83], 0\n"
      "s_waitcnt lgkmcnt(7)\n"
      "ds_read_b128 a[32:35], v84 offset:64\n"
      "buffer_load_dwordx4 v19, s[32:35], s96 offen lds\n"
      "v_mfma_f32_16x16x32_bf16 v[24:27], a[4:7], v[68:71], v[20:23]\n"
      "s_add_u32 m0, s46, 26560\n"
      "s_add_u32 s51, s51, s12\n"
      "buffer_load_dwordx4 v16, s[32:35], s51 offen lds\n"
      "v_accvgpr_write_b32 a226, v32\n"
      "v_accvgpr_write_b32 a227, v34\n"
      "v_mfma_f32_16x16x32_bf16 v[20:23], a[4:7], v[72:75], v[20:23]\n"
      "s_add_u32 m0, s46, 26560\n"
      "v_cvt_pk_bf16_f32 v218, v54, v55\n"
      "v_cvt_pk_bf16_f32 v219, v56, v57\n"
      "v_cvt_pk_bf16_f32 v226, v58, v59\n"
      "v_cvt_pk_bf16_f32 v227, v60, v61\n"
      "v_mfma_f32_16x16x32_bf16 v[28:31], a[8:11], v[76:79], 0\n"
      "buffer_load_dwordx4 v17, s[32:35], s51 offen offset:1024 lds\n"
      "v_cvt_pk_bf16_f32 v222, v62, v63\n"
      "v_mfma_f32_16x16x32_bf16 v[32:35], a[12:15], v[68:71], v[28:31]\n"
      "s_add_u32 m0, s46, 26560\n"
      "v_cvt_pk_bf16_f32 v223, v64, v65\n"
      "v_cvt_pk_bf16_f32 v20, v20, v21\n"
      "v_mfma_f32_16x16x32_bf16 v[28:31], a[12:15], v[72:75], v[28:31]\n"
      "ds_read_b128 a[12:15], v6 offset:0\n"
      "buffer_load_dwordx4 v18, s[32:35], s51 offen offset:2048 lds\n"
      "v_cvt_pk_bf16_f32 v22, v22, v23\n"
      "v_mfma_f32_16x16x32_bf16 v[54:57], a[16:19], v[76:79], 0\n"
      "v_accvgpr_write_b32 a234, v20\n"
      "v_accvgpr_write_b32 a235, v22\n"
      "v_mfma_f32_16x16x32_bf16 v[58:61], a[20:23], v[68:71], v[54:57]\n"
      "v_cvt_pk_bf16_f32 v230, v24, v25\n"
      "v_cvt_pk_bf16_f32 v231, v26, v27\n"
      "v_mfma_f32_16x16x32_bf16 v[54:57], a[20:23], v[72:75], v[54:57]\n"
      "s_waitcnt lgkmcnt(0)\n"
      "s_waitcnt vmcnt(7)\n"
      "s_cbranch_vccnz Lconv_h11\n"
      "s_barrier\n"
      "Lconv_h11:\n"
      "ds_read_b128 a[20:23], v6 offset:512\n"
      "v_cvt_pk_bf16_f32 v238, v28, v29\n"
      "v_cvt_pk_bf16_f32 v239, v30, v31\n"
      "v_mfma_f32_16x16x32_bf16 v[62:65], a[16:19], v[80:83], 0\n"
      "ds_read_b128 a[16:19], v6 offset:256\n"
      "v_cvt_pk_bf16_f32 v234, v32, v33\n"
      "v_mfma_f32_16x16x32_bf16 v[20:23], a[24:27], v[68:71], v[62:65]\n"
      "s_cbranch_vccz Lconv_h12\n"
      "s_barrier\n"
      "Lconv_h12:\n"
      "v_cvt_pk_bf16_f32 v235, v34, v35\n"
      "v_mfma_f32_16x16x32_bf16 v[62:65], a[24:27], v[72:75], v[62:65]\n"
      "ds_read_b128 a[24:27], v6 offset:4608\n"
      "v_cvt_pk_bf16_f32 v242, v58, v59\n"
      "v_cvt_pk_bf16_f32 v243, v60, v61\n"
      "v_mfma_f32_16x16x32_bf16 v[24:27], a[28:31], v[76:79], 0\n"
      "ds_read_b128 a[28:31], v6 offset:4864\n"
      "v_cvt_pk_bf16_f32 v54, v54, v55\n"
      "v_mfma_f32_16x16x32_bf16 v[28:31], a[32:35], v[68:71], v[24:27]\n"
      "v_cvt_pk_bf16_f32 v56, v56, v57\n"
      "v_accvgpr_write_b32 a242, v54\n"
      "v_mfma_f32_16x16x32_bf16 v[24:27], a[32:35], v[72:75], v[24:27]\n"
      "ds_read_b128 a[32:35], v6 offset:5120\n"
      "v_accvgpr_write_b32 a243, v56\n"
      "v_cvt_pk_bf16_f32 v246, v20, v21\n"
      "v_cvt_pk_bf16_f32 v247, v22, v23\n"
      "v_cvt_pk_bf16_f32 v250, v62, v63\n"
      "v_cvt_pk_bf16_f32 v251, v64, v65\n"
      "v_cvt_pk_bf16_f32 v254, v28, v29\n"
      "v_cvt_pk_bf16_f32 v255, v30, v31\n"
      "v_cvt_pk_bf16_f32 v24, v24, v25\n"
      "v_cvt_pk_bf16_f32 v26, v26, v27\n"
      "v_accvgpr_write_b32 a246, v24\n"
      "v_accvgpr_write_b32 a247, v26\n"
      "s_mov_b32 m0, s18\n"
      "ds_read_b128 a[48:51], v2 offset:39840\n"
      "v_lshlrev_b32 v9, 7, v5\n"
      "v_mfma_f32_16x16x32_bf16 v[68:71], a[12:15], a[88:91], 0\n"
      "s_waitcnt lgkmcnt(4)\n"
      "ds_read_b128 a[52:55], v3 offset:39840\n"
      "v_min_u32 v9, Lconv_u0-Lconv_t0, v9\n"
      "v_mfma_f32_16x16x32_bf16 v[68:71], a[16:19], a[100:103], v[68:71]\n"
      "s_mul_i32 s97, s41, Lconv_t1-Lconv_t0\n"
      "ds_read_b128 a[56:59], v4 offset:39840\n"
      "v_add_u32 v9, Lconv_t0-Lconv_start, v9\n"
      "v_mfma_f32_16x16x32_bf16 v[68:71], a[20:23], a[112:115], v[68:71]\n"
      "s_waitcnt lgkmcnt(5)\n"
      "ds_read_b128 a[36:39], v2 offset:39904\n"
      "v_add_u32 v9, s97, v9\n"
      "v_mfma_f32_16x16x32_bf16 v[68:71], a[24:27], a[124:127], v[68:71]\n"
      "s_waitcnt lgkmcnt(5)\n"
      "ds_read_b128 a[40:43], v3 offset:39904\n"
      "v_mfma_f32_16x16x32_bf16 v[68:71], a[28:31], a[136:139], v[68:71]\n"
      "s_waitcnt lgkmcnt(4)\n"
      "ds_read_b128 a[44:47], v4 offset:39904\n"
      "v_mfma_f32_16x16x32_bf16 v[52:55], a[48:51], a[72:75], 0\n"
      "ds_read_b128 a[0:3], v6 offset:768\n"
      "v_mfma_f32_16x16x32_bf16 v[68:71], a[32:35], a[148:151], v[68:71]\n"
      "ds_read_b128 a[48:51], v2 offset:39968\n"
      "v_mfma_f32_16x16x32_bf16 v[72:75], a[12:15], a[92:95], 0\n"
      "s_waitcnt lgkmcnt(6)\n"
      "ds_read_b128 a[4:7], v6 offset:1024\n"
      "v_mfma_f32_16x16x32_bf16 v[56:59], a[52:55], a[76:79], 0\n"
      "ds_read_b128 a[8:11], v6 offset:1280\n"
      "s_nop 0\n"
      "v_mfma_f32_16x16x32_bf16 v[72:75], a[16:19], a[104:107], v[72:75]\n"
      "ds_read_b128 a[52:55], v3 offset:39968\n"
      "s_nop 0\n"
      "v_mfma_f32_16x16x32_bf16 v[72:75], a[20:23], a[116:119], v[72:75]\n"
      "s_waitcnt lgkmcnt(7)\n"
      "ds_read_b128 a[60:63], v6 offset:1536\n"
      "v_fma_f32 v64, s44, v52, v56\n"
      "v_mfma_f32_16x16x32_bf16 v[60:63], a[56:59], a[80:83], v[52:55]\n"
      "ds_read_b128 a[56:59], v4 offset:39968\n"
      "v_fma_f32 v65, s44, v53, v57\n"
      "v_mfma_f32_16x16x32_bf16 v[72:75], a[24:27], a[128:131], v[72:75]\n"
      "v_cvt_pk_bf16_f32 v64, v64, v65\n"
      "v_fma_f32 v66, s44, v54, v58\n"
      "v_mfma_f32_16x16x32_bf16 v[72:75], a[28:31], a[140:143], v[72:75]\n"
      "ds_write_addtid_b32 v64 offset:9984\n"
      "v_fma_f32 v67, s44, v55, v59\n"
      "v_mfma_f32_16x16x32_bf16 v[72:75], a[32:35], a[152:155], v[72:75]\n"
      "v_cvt_pk_bf16_f32 v65, v66, v67\n"
      "v_fma_f32 v56, s44, v52, -v56\n"
      "v_mfma_f32_16x16x32_bf16 v[36:39], a[36:39], a[72:75], 0\n"
      "ds_write_addtid_b32 v65 offset:46912\n"
      "v_fma_f32 v57, s44, v53, -v57\n"
      "v_mfma_f32_16x16x32_bf16 v[76:79], a[12:15], a[96:99], 0\n"
      "ds_read_b128 a[12:15], v6 offset:5376\n"
      "v_cvt_pk_bf16_f32 v56, v56, v57\n"
      "v_mfma_f32_16x16x32_bf16 v[76:79], a[16:19], a[108:111], v[76:79]\n"
      "s_waitcnt lgkmcnt(5)\n"
      "ds_read_b128 a[16:19], v6 offset:5632\n"
      "v_fma_f32 v58, s44, v54, -v58\n"
      "v_mfma_f32_16x16x32_bf16 v[40:43], a[40:43], a[76:79], 0\n"
      "ds_write_addtid_b32 v56 offset:10752\n"
      "v_fma_f32 v59, s44, v55, -v59\n"
      "v_mfma_f32_16x16x32_bf16 v[76:79], a[20:23], a[120:123], v[76:79]\n"
      "ds_read_b128 a[20:23], v6 offset:5888\n"
      "v_cvt_pk_bf16_f32 v57, v58, v59\n"
      "v_mfma_f32_16x16x32_bf16 v[76:79], a[24:27], a[132:135], v[76:79]\n"
      "ds_write_addtid_b32 v57 offset:47680\n"
      "v_cvt_pk_bf16_f32 v60, v60, v61\n"
      "v_mfma_f32_16x16x32_bf16 v[44:47], a[44:47], a[80:83], v[36:39]\n"
      "ds_write_addtid_b32 v60 offset:9216\n"
      "v_cvt_pk_bf16_f32 v61, v62, v63\n"
      "v_mfma_f32_16x16x32_bf16 v[76:79], a[28:31], a[144:147], v[76:79]\n"
      "ds_write_addtid_b32 v61 offset:46144\n"
      "v_fma_f32 v48, s44, v36, v40\n"
      "v_mfma_f32_16x16x32_bf16 v[76:79], a[32:35], a[156:159], v[76:79]\n"
      "ds_read_b128 a[36:39], v2 offset:0\n"
      "v_fma_f32 v49, s44, v37, v41\n"
      "v_mfma_f32_16x16x32_bf16 v[80:83], a[0:3], v[92:95], 0\n"
      "v_cvt_pk_bf16_f32 v48, v48, v49\n"
      "v_fma_f32 v50, s44, v38, v42\n"
      "v_mfma_f32_16x16x32_bf16 v[52:55], a[48:51], a[72:75], 0\n"
      "ds_write_addtid_b32 v48 offset:10240\n"
      "v_fma_f32 v51, s44, v39, v43\n"
      "v_mfma_f32_16x16x32_bf16 v[80:83], a[4:7], v[148:151], v[80:83]\n"
      "ds_read_b128 a[40:43], v3 offset:0\n"
      "v_cvt_pk_bf16_f32 v49, v50, v51\n"
      "v_mfma_f32_16x16x32_bf16 v[80:83], a[8:11], v[204:207], v[80:83]\n"
      "ds_write_addtid_b32 v49 offset:47168\n"
      "v_fma_f32 v40, s44, v36, -v40\n"
      "v_mfma_f32_16x16x32_bf16 v[56:59], a[52:55], a[76:79], 0\n"
      "s_waitcnt lgkmcnt(7)\n"
      "v_fma_f32 v41, s44, v37, -v41\n"
      "v_cvt_pk_bf16_f32 v40, v40, v41\n"
      "v_mfma_f32_16x16x32_bf16 v[80:83], a[12:15], v[112:115], v[80:83]\n"
      "ds_write_addtid_b32 v40 offset:11008\n"
      "v_fma_f32 v42, s44, v38, -v42\n"
      "v_mfma_f32_16x16x32_bf16 v[80:83], a[16:19], a[208:211], v[80:83]\n"
      "v_fma_f32 v43, s44, v39, -v43\n"
      "v_cvt_pk_bf16_f32 v41, v42, v43\n"
      "v_mfma_f32_16x16x32_bf16 v[60:63], a[56:59], a[80:83], v[52:55]\n"
      "ds_write_addtid_b32 v41 offset:47936\n"
      "v_cvt_pk_bf16_f32 v44, v44, v45\n"
      "v_mfma_f32_16x16x32_bf16 v[80:83], a[20:23], v[220:223], v[80:83]\n"
      "ds_write_addtid_b32 v44 offset:9472\n"
      "v_cvt_pk_bf16_f32 v45, v46, v47\n"
      "v_mfma_f32_16x16x32_bf16 v[84:87], a[0:3], v[100:103], 0\n"
      "ds_read_b128 a[44:47], v4 offset:0\n"
      "v_fma_f32 v64, s44, v52, v56\n"
      "v_mfma_f32_16x16x32_bf16 v[84:87], a[4:7], a[200:203], v[84:87]\n"
      "ds_write_addtid_b32 v45 offset:46400\n"
      "v_fma_f32 v65, s44, v53, v57\n"
      "v_mfma_f32_16x16x32_bf16 v[84:87], a[8:11], v[208:211], v[84:87]\n"
      "ds_read_b128 a[64:67], v6 offset:1792\n"
      "v_cvt_pk_bf16_f32 v64, v64, v65\n"
      "v_mfma_f32_16x16x32_bf16 v[84:87], a[12:15], a[204:207], v[84:87]\n"
      "ds_read_b128 a[68:71], v6 offset:2048\n"
      "v_fma_f32 v66, s44, v54, v58\n"
      "v_mfma_f32_16x16x32_bf16 v[84:87], a[16:19], v[172:175], v[84:87]\n"
      "ds_read_b128 a[24:27], v6 offset:6144\n"
      "v_fma_f32 v67, s44, v55, v59\n"
      "v_mfma_f32_16x16x32_bf16 v[84:87], a[20:23], v[228:231], v[84:87]\n"
      "ds_read_b128 a[48:51], v2 offset:64\n"
      "v_cvt_pk_bf16_f32 v65, v66, v67\n"
      "v_mfma_f32_16x16x32_bf16 v[24:27], a[0:3], a[196:199], 0\n"
      "ds_write_addtid_b32 v64 offset:10496\n"
      "v_fma_f32 v56, s44, v52, -v56\n"
      "v_mfma_f32_16x16x32_bf16 v[24:27], a[4:7], v[160:163], v[24:27]\n"
      "s_waitcnt lgkmcnt(11)\n"
      "ds_read_b128 a[28:31], v6 offset:6400\n"
      "v_fma_f32 v57, s44, v53, -v57\n"
      "v_mfma_f32_16x16x32_bf16 v[36:39], a[36:39], a[72:75], 0\n"
      "ds_write_addtid_b32 v65 offset:47424\n"
      "v_cvt_pk_bf16_f32 v56, v56, v57\n"
      "v_mfma_f32_16x16x32_bf16 v[24:27], a[8:11], v[216:219], v[24:27]\n"
      "ds_read_b128 a[32:35], v6 offset:6656\n"
      "v_fma_f32 v58, s44, v54, -v58\n"
      "v_mfma_f32_16x16x32_bf16 v[24:27], a[12:15], v[124:127], v[24:27]\n"
      "ds_write_addtid_b32 v56 offset:11264\n"
      "v_fma_f32 v59, s44, v55, -v59\n"
      "v_mfma_f32_16x16x32_bf16 v[40:43], a[40:43], a[76:79], 0\n"
      "ds_read_b128 a[52:55], v3 offset:64\n"
      "v_cvt_pk_bf16_f32 v57, v58, v59\n"
      "v_mfma_f32_16x16x32_bf16 v[24:27], a[16:19], v[180:183], v[24:27]\n"
      "ds_write_addtid_b32 v57 offset:48192\n"
      "v_cvt_pk_bf16_f32 v60, v60, v61\n"
      "v_mfma_f32_16x16x32_bf16 v[24:27], a[20:23], v[232:235], v[24:27]\n"
      "s_waitcnt lgkmcnt(7)\n"
      "ds_write_addtid_b32 v60 offset:9728\n"
      "v_cvt_pk_bf16_f32 v61, v62, v63\n"
      "v_mfma_f32_16x16x32_bf16 v[44:47], a[44:47], a[80:83], v[36:39]\n"
      "ds_write_addtid_b32 v61 offset:46656\n"
      "v_fma_f32 v48, s44, v36, v40\n"
      "v_mfma_f32_16x16x32_bf16 v[28:31], a[60:63], v[96:99], 0\n"
      "ds_read_b128 a[56:59], v4 offset:64\n"
      "v_fma_f32 v49, s44, v37, v41\n"
      "v_mfma_f32_16x16x32_bf16 v[28:31], a[64:67], v[152:155], v[28:31]\n"
      "ds_read_b128 a[36:39], v2 offset:128\n"
      "v_cvt_pk_bf16_f32 v48, v48, v49\n"
      "v_mfma_f32_16x16x32_bf16 v[28:31], a[68:71], a[220:223], v[28:31]\n"
      "ds_read_b128 a[40:43], v3 offset:128\n"
      "v_fma_f32 v50, s44, v38, v42\n"
      "v_mfma_f32_16x16x32_bf16 v[28:31], a[24:27], v[116:119], v[28:31]\n"
      "s_waitcnt lgkmcnt(0)\n"
      "s_cbranch_vccnz Lconv_h13\n"
      "s_barrier\n"
      "Lconv_h13:\n"
      "v_fma_f32 v51, s44, v39, v43\n"
      "v_mfma_f32_16x16x32_bf16 v[52:55], a[48:51], a[72:75], 0\n"
      "ds_read_b128 a[44:47], v4 offset:128\n"
      "v_cvt_pk_bf16_f32 v49, v50, v51\n"
      "v_mfma_f32_16x16x32_bf16 v[28:31], a[28:31], v[168:171], v[28:31]\n"
      "ds_write_addtid_b32 v48 offset:14592\n"
      "v_fma_f32 v40, s44, v36, -v40\n"
      "v_mfma_f32_16x16x32_bf16 v[28:31], a[32:35], v[224:227], v[28:31]\n"
      "ds_write_addtid_b32 v49 offset:51520\n"
      "v_fma_f32 v41, s44, v37, -v41\n"
      "v_mfma_f32_16x16x32_bf16 v[56:59], a[52:55], a[76:79], 0\n"
      "s_cbranch_vccz Lconv_h14\n"
      "s_barrier\n"
      "Lconv_h14:\n"
      "v_cvt_pk_bf16_f32 v40, v40, v41\n"
      "v_mfma_f32_16x16x32_bf16 v[20:23], a[60:63], v[104:107], 0\n"
      "s_mov_b64 vcc, 0x2000000\n"
      "ds_write_addtid_b32 v40 offset:15360\n"
      "v_fma_f32 v42, s44, v38, -v42\n"
      "v_mfma_f32_16x16x32_bf16 v[20:23], a[64:67], v[156:159], v[20:23]\n"
      "ds_read_b128 a[0:3], v7 offset:0\n"
      "v_fma_f32 v43, s44, v39, -v43\n"
      "v_mfma_f32_16x16x32_bf16 v[60:63], a[56:59], a[80:83], v[52:55]\n"
      "v_cvt_pk_bf16_f32 v41, v42, v43\n"
      "v_cvt_pk_bf16_f32 v44, v44, v45\n"
      "v_mfma_f32_16x16x32_bf16 v[20:23], a[68:71], v[212:215], v[20:23]\n"
      "ds_write_addtid_b32 v41 offset:52288\n"
      "v_cvt_pk_bf16_f32 v45, v46, v47\n"
      "v_mfma_f32_16x16x32_bf16 v[20:23], a[24:27], v[120:123], v[20:23]\n"
      "ds_write_addtid_b32 v44 offset:13824\n"
      "ds_write_addtid_b32 v45 offset:50752\n"
      "v_mfma_f32_16x16x32_bf16 v[20:23], a[28:31], v[176:179], v[20:23]\n"
      "ds_read_b128 a[4:7], v7 offset:256\n"
      "v_fma_f32 v64, s44, v52, v56\n"
      "v_mfma_f32_16x16x32_bf16 v[36:39], a[36:39], a[72:75], 0\n"
      "ds_read_b128 a[8:11], v7 offset:512\n"
      "v_fma_f32 v65, s44, v53, v57\n"
      "v_mfma_f32_16x16x32_bf16 v[20:23], a[32:35], a[232:235], v[20:23]\n"
      "v_fma_f32 v66, s44, v54, v58\n"
      "v_fma_f32 v67, s44, v55, v59\n"
      "v_mfma_f32_16x16x32_bf16 v[32:35], a[60:63], v[108:111], 0\n"
      "v_fma_f32 v56, s44, v52, -v56\n"
      "v_fma_f32 v57, s44, v53, -v57\n"
      "v_mfma_f32_16x16x32_bf16 v[40:43], a[40:43], a[76:79], 0\n"
      "v_fma_f32 v58, s44, v54, -v58\n"
      "v_fma_f32 v59, s44, v55, -v59\n"
      "v_mfma_f32_16x16x32_bf16 v[32:35], a[64:67], v[164:167], v[32:35]\n"
      "v_cvt_pk_bf16_f32 v64, v64, v65\n"
      "v_cvt_pk_bf16_f32 v65, v66, v67\n"
      "v_mfma_f32_16x16x32_bf16 v[32:35], a[68:71], a[224:227], v[32:35]\n"
      "s_waitcnt lgkmcnt(5)\n"
      "v_cvt_pk_bf16_f32 v56, v56, v57\n"
      "v_cvt_pk_bf16_f32 v57, v58, v59\n"
      "v_mfma_f32_16x16x32_bf16 v[44:47], a[44:47], a[80:83], v[36:39]\n"
      "ds_write_addtid_b32 v64 offset:14848\n"
      "v_cvt_pk_bf16_f32 v60, v60, v61\n"
      "v_mfma_f32_16x16x32_bf16 v[32:35], a[24:27], v[128:131], v[32:35]\n"
      "ds_write_addtid_b32 v65 offset:51776\n"
      "v_cvt_pk_bf16_f32 v61, v62, v63\n"
      "v_mfma_f32_16x16x32_bf16 v[32:35], a[28:31], a[228:231], v[32:35]\n"
      "ds_write_addtid_b32 v56 offset:15616\n"
      "ds_write_addtid_b32 v57 offset:52544\n"
      "v_mfma_f32_16x16x32_bf16 v[32:35], a[32:35], v[236:239], v[32:35]\n"
      "ds_write_addtid_b32 v60 offset:14080\n"
      "ds_write_addtid_b32 v61 offset:51008\n"
      "v_mfma_f32_16x16x32_bf16 v[68:71], a[0:3], a[160:163], v[68:71]\n"
      "s_waitcnt lgkmcnt(6)\n"
      "ds_read_b128 a[12:15], v7 offset:768\n"
      "v_fma_f32 v48, s44, v36, v40\n"
      "v_mfma_f32_16x16x32_bf16 v[68:71], a[4:7], a[172:175], v[68:71]\n"
      "s_add_u32 m0, s46, 39840\n"
      "ds_read_b128 a[16:19], v7 offset:1024\n"
      "v_fma_f32 v49, s44, v37, v41\n"
      "v_mfma_f32_16x16x32_bf16 v[68:71], a[8:11], a[184:187], v[68:71]\n"
      "s_add_u32 s51, s51, s12\n"
      "ds_read_b128 a[20:23], v7 offset:1280\n"
      "v_cvt_pk_bf16_f32 v48, v48, v49\n"
      "v_mfma_f32_16x16x32_bf16 v[72:75], a[0:3], a[164:167], v[72:75]\n"
      "buffer_load_dwordx4 v16, s[32:35], s51 offen lds\n"
      "v_fma_f32 v50, s44, v38, v42\n"
      "v_mfma_f32_16x16x32_bf16 v[72:75], a[4:7], a[176:179], v[72:75]\n"
      "v_fma_f32 v51, s44, v39, v43\n"
      "v_cvt_pk_bf16_f32 v49, v50, v51\n"
      "v_mfma_f32_16x16x32_bf16 v[72:75], a[8:11], a[188:191], v[72:75]\n"
      "v_fma_f32 v40, s44, v36, -v40\n"
      "v_fma_f32 v41, s44, v37, -v41\n"
      "v_mfma_f32_16x16x32_bf16 v[76:79], a[0:3], a[168:171], v[76:79]\n"
      "buffer_load_dwordx4 v17, s[32:35], s51 offen offset:1024 lds\n"
      "v_cvt_pk_bf16_f32 v40, v40, v41\n"
      "v_mfma_f32_16x16x32_bf16 v[76:79], a[4:7], a[180:183], v[76:79]\n"
      "ds_read_b128 a[0:3], v7 offset:1536\n"
      "v_fma_f32 v42, s44, v38, -v42\n"
      "v_mfma_f32_16x16x32_bf16 v[76:79], a[8:11], a[192:195], v[76:79]\n"
      "s_waitcnt lgkmcnt(3)\n"
      "ds_read_b128 a[4:7], v7 offset:1792\n"
      "v_fma_f32 v43, s44, v39, -v43\n"
      "v_mfma_f32_16x16x32_bf16 v[80:83], a[12:15], a[212:215], v[80:83]\n"
      "s_waitcnt lgkmcnt(3)\n"
      "ds_read_b128 a[8:11], v7 offset:2048\n"
      "v_cvt_pk_bf16_f32 v41, v42, v43\n"
      "v_mfma_f32_16x16x32_bf16 v[80:83], a[16:19], v[184:187], v[80:83]\n"
      "s_waitcnt lgkmcnt(3)\n"
      "v_cvt_pk_bf16_f32 v44, v44, v45\n"
      "v_cvt_pk_bf16_f32 v45, v46, v47\n"
      "v_mfma_f32_16x16x32_bf16 v[80:83], a[20:23], v[240:243], v[80:83]\n"
      "buffer_load_dwordx4 v18, s[32:35], s51 offen offset:2048 lds\n"
      "v_cvt_pk_bf16_f32 v68, v68, v69\n"
      "v_mfma_f32_16x16x32_bf16 v[84:87], a[12:15], v[136:139], v[84:87]\n"
      "s_mov_b32 m0, s18\n"
      "v_cvt_pk_bf16_f32 v69, v70, v71\n"
      "v_cvt_pk_bf16_f32 v72, v72, v73\n"
      "v_mfma_f32_16x16x32_bf16 v[84:87], a[16:19], v[192:195], v[84:87]\n"
      "ds_write_addtid_b32 v48 offset:15104\n"
      "ds_write_addtid_b32 v49 offset:52032\n"
      "v_mfma_f32_16x16x32_bf16 v[84:87], a[20:23], v[244:247], v[84:87]\n"
      "ds_write_addtid_b32 v40 offset:15872\n"
      "ds_write_addtid_b32 v41 offset:52800\n"
      "v_mfma_f32_16x16x32_bf16 v[24:27], a[12:15], a[216:219], v[24:27]\n"
      "ds_write_addtid_b32 v44 offset:14336\n"
      "ds_write_addtid_b32 v45 offset:51264\n"
      "v_mfma_f32_16x16x32_bf16 v[24:27], a[16:19], v[196:199], v[24:27]\n"
      "s_mov_b32 m0, s45\n"
      "v_cvt_pk_bf16_f32 v73, v74, v75\n"
      "v_cvt_pk_bf16_f32 v76, v76, v77\n"
      "v_mfma_f32_16x16x32_bf16 v[24:27], a[20:23], v[252:255], v[24:27]\n"
      "s_waitcnt lgkmcnt(6)\n"
      "ds_write_addtid_b32 v68 offset:57360\n"
      "ds_write_addtid_b32 v69 offset:57616\n"
      "v_mfma_f32_16x16x32_bf16 v[28:31], a[0:3], v[132:135], v[28:31]\n"
      "ds_write_addtid_b32 v72 offset:45064\n"
      "ds_write_addtid_b32 v73 offset:45320\n"
      "v_mfma_f32_16x16x32_bf16 v[28:31], a[4:7], v[188:191], v[28:31]\n"
      "ds_write_addtid_b32 v76 offset:32768\n"
      "v_cvt_pk_bf16_f32 v77, v78, v79\n"
      "v_mfma_f32_16x16x32_bf16 v[28:31], a[8:11], a[240:243], v[28:31]\n"
      "ds_write_addtid_b32 v77 offset:33024\n"
      "v_cvt_pk_bf16_f32 v80, v80, v81\n"
      "v_mfma_f32_16x16x32_bf16 v[20:23], a[0:3], v[140:143], v[20:23]\n"
      "ds_write_addtid_b32 v80 offset:58384\n"
      "v_cvt_pk_bf16_f32 v81, v82, v83\n"
      "v_mfma_f32_16x16x32_bf16 v[20:23], a[4:7], a[236:239], v[20:23]\n"
      "ds_write_addtid_b32 v81 offset:58640\n"
      "v_cvt_pk_bf16_f32 v84, v84, v85\n"
      "v_mfma_f32_16x16x32_bf16 v[20:23], a[8:11], v[248:251], v[20:23]\n"
      "ds_write_addtid_b32 v84 offset:46088\n"
      "v_cvt_pk_bf16_f32 v85, v86, v87\n"
      "v_mfma_f32_16x16x32_bf16 v[32:35], a[0:3], v[144:147], v[32:35]\n"
      "ds_write_addtid_b32 v85 offset:46344\n"
      "v_cvt_pk_bf16_f32 v24, v24, v25\n"
      "v_mfma_f32_16x16x32_bf16 v[32:35], a[4:7], v[200:203], v[32:35]\n"
      "ds_write_addtid_b32 v24 offset:33792\n"
      "v_cvt_pk_bf16_f32 v25, v26, v27\n"
      "v_mfma_f32_16x16x32_bf16 v[32:35], a[8:11], a[244:247], v[32:35]\n"
      "ds_write_addtid_b32 v25 offset:34048\n"
      "v_cvt_pk_bf16_f32 v28, v28, v29\n"
      "v_cvt_pk_bf16_f32 v29, v30, v31\n"
      "ds_write_addtid_b32 v28 offset:59408\n"
      "ds_write_addtid_b32 v29 offset:59664\n"
      "v_cvt_pk_bf16_f32 v20, v20, v21\n"
      "v_cvt_pk_bf16_f32 v21, v22, v23\n"
      "ds_write_addtid_b32 v20 offset:47112\n"
      "ds_write_addtid_b32 v21 offset:47368\n"
      "s_waitcnt vmcnt(3)\n"
      "s_waitcnt lgkmcnt(0)\n"
      "s_cmp_eq_u32 s41, 1\n"
      "s_cbranch_scc1 Lconv_s1\n"
      "Lconv_s0:\n"
      "s_branch Lconv_loop0\n"
      "Lconv_s1:\n"
      "s_branch Lconv_loop1\n"
      ".p2align 12\n"
      "Lconv_pa0:\n"
      "s_cbranch_execz Lconv_r0\n"
      "s_nop 0\n"
      "Lconv_loop0:\n"
      "v_mfma_f32_16x16x32_bf16 v[20:23], a[24:27], v[96:99], 0\n"
      "s_add_u32 s51, s51, s12\n"
      "s_nop 1\n"
      "v_mfma_f32_16x16x32_bf16 v[20:23], a[28:31], v[152:155], v[20:23]\n"
      "s_sub_u32 s58, s93, s58\n"
      "v_cvt_pk_bf16_f32 v32, v32, v33\n"
      "v_cvt_pk_bf16_f32 v33, v34, v35\n"
      "v_mfma_f32_16x16x32_bf16 v[20:23], a[32:35], a[220:223], v[20:23]\n"
      "s_sub_u32 s59, s94, s59\n"
      "ds_write_addtid_b32 v32 offset:34816\n"
      "v_mfma_f32_16x16x32_bf16 v[20:23], a[0:3], v[116:119], v[20:23]\n"
      "s_sub_u32 s47, s51, s48\n"
      "ds_write_addtid_b32 v33 offset:35072\n"
      "v_mfma_f32_16x16x32_bf16 v[20:23], a[4:7], v[168:171], v[20:23]\n"
      "s_cmp_ge_u32 vcc_lo, 0x80000000\n"
      "v_mfma_f32_16x16x32_bf16 v[20:23], a[8:11], v[224:227], v[20:23]\n"
      "s_cselect_b32 s53, s47, s51\n"
      "v_mfma_f32_16x16x32_bf16 v[24:27], a[24:27], v[104:107], 0\n"
      "s_waitcnt vmcnt(8) lgkmcnt(0)\n"
      "s_barrier\n"
      "v_mfma_f32_16x16x32_bf16 v[24:27], a[28:31], v[156:159], v[24:27]\n"
      "s_mov_b32 m0, s58\n"
      "ds_read_b128 a[12:15], v7 offset:6144\n"
      "v_mfma_f32_16x16x32_bf16 v[24:27], a[32:35], v[212:215], v[24:27]\n"
      "ds_read_b128 a[16:19], v7 offset:6400\n"
      "v_mfma_f32_16x16x32_bf16 v[24:27], a[0:3], v[120:123], v[24:27]\n"
      "ds_read_b128 a[20:23], v7 offset:6656\n"
      "v_mfma_f32_16x16x32_bf16 v[24:27], a[4:7], v[176:179], v[24:27]\n"
      "ds_read_b128 a[48:51], v2 offset:13280\n"
      "v_mfma_f32_16x16x32_bf16 v[24:27], a[8:11], a[232:235], v[24:27]\n"
      "ds_read_b128 a[52:55], v3 offset:13280\n"
      "v_mfma_f32_16x16x32_bf16 v[68:71], a[84:87], a[60:63], 0\n"
      "ds_read_b128 a[56:59], v4 offset:13280\n"
      "s_nop 0\n"
      "v_mfma_f32_16x16x32_bf16 v[28:31], a[24:27], v[108:111], 0\n"
      "ds_read2_b64 a[64:67], v10 offset0:0 offset1:64\n"
      "s_nop 0\n"
      "v_mfma_f32_16x16x32_bf16 v[28:31], a[28:31], v[164:167], v[28:31]\n"
      "buffer_load_dwordx4 v16, s[32:35], s53 offen lds\n"
      "v_mfma_f32_16x16x32_bf16 v[28:31], a[32:35], a[224:227], v[28:31]\n"
      "s_mov_b32 m0, s45\n"
      "ds_read2_b64 a[68:71], v10 offset0:1 offset1:65\n"
      "v_cvt_pk_bf16_f32 v85, v68, v69\n"
      "v_mfma_f32_16x16x32_bf16 v[28:31], a[0:3], v[128:131], v[28:31]\n"
      "ds_read_b128 a[24:27], v6 offset:5376\n"
      "v_cvt_pk_bf16_f32 v91, v70, v71\n"
      "v_mfma_f32_16x16x32_bf16 v[28:31], a[4:7], a[228:231], v[28:31]\n"
      "ds_read_b128 a[28:31], v6 offset:5632\n"
      "v_mfma_f32_16x16x32_bf16 v[28:31], a[8:11], v[236:239], v[28:31]\n"
      "s_waitcnt lgkmcnt(6)\n"
      "ds_read_b128 a[32:35], v6 offset:5888\n"
      "v_mfma_f32_16x16x32_bf16 v[20:23], a[12:15], v[132:135], v[20:23]\n"
      "buffer_store_dwordx2 v[84:85], v15, s[36:39], s52 offen sc1\n"
      "v_mfma_f32_16x16x32_bf16 v[20:23], a[16:19], v[188:191], v[20:23]\n"
      "ds_read_b128 a[0:3], v7 offset:768\n"
      "v_mfma_f32_16x16x32_bf16 v[20:23], a[20:23], a[240:243], v[20:23]\n"
      "ds_read_b128 a[4:7], v7 offset:1024\n"
      "s_nop 0\n"
      "v_mfma_f32_16x16x32_bf16 v[52:55], a[48:51], a[72:75], 0\n"
      "s_waitcnt lgkmcnt(6)\n"
      "ds_read_b128 a[8:11], v7 offset:1280\n"
      "s_nop 0\n"
      "v_mfma_f32_16x16x32_bf16 v[56:59], a[52:55], a[76:79], 0\n"
      "ds_read_b128 a[36:39], v2 offset:13344\n"
      "v_cvt_pk_bf16_f32 v20, v20, v21\n"
      "v_mfma_f32_16x16x32_bf16 v[60:63], a[56:59], a[80:83], v[52:55]\n"
      "buffer_store_dwordx2 v[90:91], v15, s[36:39], s52 offen offset:1536 sc1\n"
      "v_cvt_pk_bf16_f32 v21, v22, v23\n"
      "v_mfma_f32_16x16x32_bf16 v[24:27], a[12:15], v[140:143], v[24:27]\n"
      "s_mov_b32 s52, s13\n"
      "ds_write_addtid_b32 v20 offset:62480\n"
      "v_fma_f32 v64, s44, v52, v56\n"
      "v_mfma_f32_16x16x32_bf16 v[24:27], a[16:19], a[236:239], v[24:27]\n"
      "s_add_u32 s13, s13, s12\n"
      "ds_write_addtid_b32 v21 offset:62736\n"
      "v_fma_f32 v65, s44, v53, v57\n"
      "v_mfma_f32_16x16x32_bf16 v[24:27], a[20:23], v[248:251], v[24:27]\n"
      "s_sub_u32 s96, s53, s95\n"
      "ds_read_b128 a[40:43], v3 offset:13344\n"
      "v_cvt_pk_bf16_f32 v64, v64, v65\n"
      "v_mfma_f32_16x16x32_bf16 v[72:75], a[84:87], a[64:67], 0\n"
      "ds_read_b128 a[44:47], v4 offset:13344\n"
      "v_fma_f32 v66, s44, v54, v58\n"
      "v_mfma_f32_16x16x32_bf16 v[28:31], a[12:15], v[144:147], v[28:31]\n"
      "ds_read_b128 a[12:15], v7 offset:5376\n"
      "v_cvt_pk_bf16_f32 v24, v24, v25\n"
      "v_mfma_f32_16x16x32_bf16 v[28:31], a[16:19], v[200:203], v[28:31]\n"
      "ds_read_b128 a[16:19], v7 offset:5632\n"
      "v_cvt_pk_bf16_f32 v25, v26, v27\n"
      "v_mfma_f32_16x16x32_bf16 v[28:31], a[20:23], a[244:247], v[28:31]\n"
      "s_waitcnt lgkmcnt(6)\n"
      "ds_read_b128 a[20:23], v7 offset:5888\n"
      "v_fma_f32 v67, s44, v55, v59\n"
      "v_mfma_f32_16x16x32_bf16 v[76:79], a[84:87], a[68:71], 0\n"
      "ds_write_addtid_b32 v24 offset:50184\n"
      "v_fma_f32 v56, s44, v52, -v56\n"
      "v_mfma_f32_16x16x32_bf16 v[32:35], a[24:27], v[92:95], 0\n"
      "ds_write_addtid_b32 v25 offset:50440\n"
      "v_fma_f32 v57, s44, v53, -v57\n"
      "v_mfma_f32_16x16x32_bf16 v[32:35], a[28:31], v[148:151], v[32:35]\n"
      "s_mov_b32 m0, s18\n"
      "ds_read2_b64 a[60:63], v10 offset0:2 offset1:66\n"
      "v_fma_f32 v58, s44, v54, -v58\n"
      "v_mfma_f32_16x16x32_bf16 v[32:35], a[32:35], v[204:207], v[32:35]\n"
      "ds_write_addtid_b32 v64 offset:768\n"
      "v_fma_f32 v59, s44, v55, -v59\n"
      "v_mfma_f32_16x16x32_bf16 v[32:35], a[0:3], v[112:115], v[32:35]\n"
      "v_cvt_pk_bf16_f32 v65, v66, v67\n"
      "v_cvt_pk_bf16_f32 v56, v56, v57\n"
      "v_mfma_f32_16x16x32_bf16 v[32:35], a[4:7], a[208:211], v[32:35]\n"
      "v_cvt_pk_bf16_f32 v57, v58, v59\n"
      "v_cvt_pk_bf16_f32 v60, v60, v61\n"
      "v_mfma_f32_16x16x32_bf16 v[32:35], a[8:11], v[220:223], v[32:35]\n"
      "s_waitcnt lgkmcnt(5)\n"
      "ds_write_addtid_b32 v65 offset:37696\n"
      "v_cvt_pk_bf16_f32 v61, v62, v63\n"
      "v_mfma_f32_16x16x32_bf16 v[32:35], a[12:15], a[212:215], v[32:35]\n"
      "ds_write_addtid_b32 v56 offset:1536\n"
      "v_cvt_pk_bf16_f32 v28, v28, v29\n"
      "v_mfma_f32_16x16x32_bf16 v[32:35], a[16:19], v[184:187], v[32:35]\n"
      "s_waitcnt lgkmcnt(6)\n"
      "ds_write_addtid_b32 v57 offset:38464\n"
      "v_cvt_pk_bf16_f32 v29, v30, v31\n"
      "v_mfma_f32_16x16x32_bf16 v[32:35], a[20:23], v[240:243], v[32:35]\n"
      "ds_write_addtid_b32 v60 offset:0\n"
      "v_cvt_pk_bf16_f32 v80, v72, v73\n"
      "v_mfma_f32_16x16x32_bf16 v[20:23], a[24:27], v[100:103], 0\n"
      "ds_write_addtid_b32 v61 offset:36928\n"
      "v_cvt_pk_bf16_f32 v81, v76, v77\n"
      "v_mfma_f32_16x16x32_bf16 v[20:23], a[28:31], a[200:203], v[20:23]\n"
      "s_mov_b32 m0, s45\n"
      "ds_read_b128 a[48:51], v2 offset:13408\n"
      "v_cvt_pk_bf16_f32 v32, v32, v33\n"
      "v_mfma_f32_16x16x32_bf16 v[20:23], a[32:35], v[208:211], v[20:23]\n"
      "ds_write_addtid_b32 v28 offset:37888\n"
      "v_cvt_pk_bf16_f32 v33, v34, v35\n"
      "v_mfma_f32_16x16x32_bf16 v[20:23], a[0:3], a[204:207], v[20:23]\n"
      "ds_write_addtid_b32 v29 offset:38144\n"
      "v_cvt_pk_bf16_f32 v86, v74, v75\n"
      "v_mfma_f32_16x16x32_bf16 v[20:23], a[4:7], v[172:175], v[20:23]\n"
      "s_mov_b32 m0, s58\n"
      "ds_read_b128 a[56:59], v4 offset:13408\n"
      "v_cvt_pk_bf16_f32 v87, v78, v79\n"
      "v_mfma_f32_16x16x32_bf16 v[20:23], a[8:11], v[228:231], v[20:23]\n"
      "buffer_load_dwordx4 v17, s[32:35], s53 offen offset:1024 lds\n"
      "v_add_u32_e32 v2, s91, v2\n"
      "v_mfma_f32_16x16x32_bf16 v[20:23], a[12:15], v[136:139], v[20:23]\n"
      "s_mov_b32 m0, s45\n"
      "ds_read2_b64 a[64:67], v10 offset0:3 offset1:67\n"
      "v_add_u32_e32 v4, s91, v4\n"
      "v_mfma_f32_16x16x32_bf16 v[20:23], a[16:19], v[192:195], v[20:23]\n"
      "ds_write_addtid_b32 v32 offset:61456\n"
      "v_mfma_f32_16x16x32_bf16 v[20:23], a[20:23], v[244:247], v[20:23]\n"
      "ds_write_addtid_b32 v33 offset:61712\n"
      "s_nop 0\n"
      "v_mfma_f32_16x16x32_bf16 v[36:39], a[36:39], a[72:75], 0\n"
      "ds_read_b128 a[52:55], v3 offset:13408\n"
      "v_add_u32_e32 v3, s91, v3\n"
      "v_mfma_f32_16x16x32_bf16 v[40:43], a[40:43], a[76:79], 0\n"
      "s_sub_u32 s91, 0, s91\n"
      "ds_read2_b64 a[68:71], v12 offset0:0 offset1:64\n"
      "v_cvt_pk_bf16_f32 v20, v20, v21\n"
      "v_mfma_f32_16x16x32_bf16 v[44:47], a[44:47], a[80:83], v[36:39]\n"
      "s_waitcnt lgkmcnt(8)\n"
      "ds_write_addtid_b32 v20 offset:49160\n"
      "v_cvt_pk_bf16_f32 v21, v22, v23\n"
      "v_mfma_f32_16x16x32_bf16 v[68:71], a[84:87], a[60:63], 0\n"
      "ds_write_addtid_b32 v21 offset:49416\n"
      "v_fma_f32 v48, s44, v36, v40\n"
      "v_mfma_f32_16x16x32_bf16 v[24:27], a[24:27], a[196:199], 0\n"
      "s_mov_b32 m0, s58\n"
      "ds_read_b128 a[24:27], v7 offset:4608\n"
      "v_fma_f32 v49, s44, v37, v41\n"
      "v_mfma_f32_16x16x32_bf16 v[24:27], a[28:31], v[160:163], v[24:27]\n"
      "ds_read_b128 a[28:31], v7 offset:4864\n"
      "v_cvt_pk_bf16_f32 v48, v48, v49\n"
      "v_mfma_f32_16x16x32_bf16 v[24:27], a[32:35], v[216:219], v[24:27]\n"
      "ds_read_b128 a[32:35], v7 offset:5120\n"
      "v_fma_f32 v50, s44, v38, v42\n"
      "v_mfma_f32_16x16x32_bf16 v[24:27], a[0:3], v[124:127], v[24:27]\n"
      "ds_read_b128 a[0:3], v6 offset:4608\n"
      "v_fma_f32 v51, s44, v39, v43\n"
      "v_mfma_f32_16x16x32_bf16 v[24:27], a[4:7], v[180:183], v[24:27]\n"
      "ds_read_b128 a[4:7], v6 offset:4864\n"
      "v_cvt_pk_bf16_f32 v49, v50, v51\n"
      "v_mfma_f32_16x16x32_bf16 v[24:27], a[8:11], v[232:235], v[24:27]\n"
      "ds_read_b128 a[8:11], v6 offset:5120\n"
      "v_fma_f32 v40, s44, v36, -v40\n"
      "v_mfma_f32_16x16x32_bf16 v[24:27], a[12:15], a[216:219], v[24:27]\n"
      "ds_read_b128 a[12:15], v7 offset:0\n"
      "v_fma_f32 v41, s44, v37, -v41\n"
      "v_mfma_f32_16x16x32_bf16 v[24:27], a[16:19], v[196:199], v[24:27]\n"
      "ds_read_b128 a[16:19], v7 offset:256\n"
      "v_cvt_pk_bf16_f32 v40, v40, v41\n"
      "v_mfma_f32_16x16x32_bf16 v[24:27], a[20:23], v[252:255], v[24:27]\n"
      "ds_read_b128 a[20:23], v7 offset:512\n"
      "v_fma_f32 v42, s44, v38, -v42\n"
      "v_mfma_f32_16x16x32_bf16 v[52:55], a[48:51], a[72:75], 0\n"
      "s_waitcnt lgkmcnt(7)\n"
      "v_fma_f32 v43, s44, v39, -v43\n"
      "v_cvt_pk_bf16_f32 v41, v42, v43\n"
      "v_mfma_f32_16x16x32_bf16 v[60:63], a[56:59], a[80:83], v[52:55]\n"
      "buffer_load_dwordx4 v18, s[32:35], s53 offen offset:2048 lds\n"
      "v_cvt_pk_bf16_f32 v24, v24, v25\n"
      "v_mfma_f32_16x16x32_bf16 v[72:75], a[84:87], a[64:67], 0\n"
      "s_mov_b32 m0, s45\n"
      "v_cvt_pk_bf16_f32 v25, v26, v27\n"
      "v_cvt_pk_bf16_f32 v44, v44, v45\n"
      "v_mfma_f32_16x16x32_bf16 v[28:31], a[24:27], a[160:163], 0\n"
      "ds_write_addtid_b32 v24 offset:36864\n"
      "v_cvt_pk_bf16_f32 v82, v68, v69\n"
      "v_mfma_f32_16x16x32_bf16 v[28:31], a[28:31], a[172:175], v[28:31]\n"
      "s_waitcnt lgkmcnt(4)\n"
      "ds_write_addtid_b32 v25 offset:37120\n"
      "v_cvt_pk_bf16_f32 v83, v72, v73\n"
      "v_mfma_f32_16x16x32_bf16 v[28:31], a[32:35], a[184:187], v[28:31]\n"
      "s_mov_b32 m0, s18\n"
      "v_cvt_pk_bf16_f32 v45, v46, v47\n"
      "v_cvt_pk_bf16_f32 v88, v70, v71\n"
      "v_mfma_f32_16x16x32_bf16 v[28:31], a[0:3], a[88:91], v[28:31]\n"
      "ds_write_addtid_b32 v48 offset:1024\n"
      "v_cvt_pk_bf16_f32 v89, v74, v75\n"
      "v_mfma_f32_16x16x32_bf16 v[28:31], a[4:7], a[100:103], v[28:31]\n"
      "ds_write_addtid_b32 v49 offset:37952\n"
      "v_cvt_pk_bf16_f32 v60, v60, v61\n"
      "v_mfma_f32_16x16x32_bf16 v[28:31], a[8:11], a[112:115], v[28:31]\n"
      "s_waitcnt lgkmcnt(4)\n"
      "ds_write_addtid_b32 v40 offset:1792\n"
      "v_cvt_pk_bf16_f32 v61, v62, v63\n"
      "v_mfma_f32_16x16x32_bf16 v[28:31], a[12:15], a[124:127], v[28:31]\n"
      "ds_write_addtid_b32 v41 offset:38720\n"
      "v_swap_b32 v6, v7\n"
      "v_mfma_f32_16x16x32_bf16 v[28:31], a[16:19], a[136:139], v[28:31]\n"
      "ds_write_addtid_b32 v44 offset:256\n"
      "v_mfma_f32_16x16x32_bf16 v[28:31], a[20:23], a[148:151], v[28:31]\n"
      "buffer_store_dwordx4 v[80:83], v14, s[36:39], s52 offen sc1\n"
      "s_nop 0\n"
      "v_mfma_f32_16x16x32_bf16 v[56:59], a[52:55], a[76:79], 0\n"
      "ds_write_addtid_b32 v45 offset:37184\n"
      "s_nop 0\n"
      "v_mfma_f32_16x16x32_bf16 v[20:23], a[24:27], a[164:167], 0\n"
      "s_mov_b32 m0, s45\n"
      "ds_read2_b64 a[60:63], v12 offset0:1 offset1:65\n"
      "v_cvt_pk_bf16_f32 v28, v28, v29\n"
      "v_mfma_f32_16x16x32_bf16 v[20:23], a[28:31], a[176:179], v[20:23]\n"
      "ds_write_addtid_b32 v28 offset:60432\n"
      "v_cvt_pk_bf16_f32 v29, v30, v31\n"
      "v_mfma_f32_16x16x32_bf16 v[20:23], a[32:35], a[188:191], v[20:23]\n"
      "ds_write_addtid_b32 v29 offset:60688\n"
      "v_fma_f32 v64, s44, v52, v56\n"
      "v_mfma_f32_16x16x32_bf16 v[20:23], a[0:3], a[92:95], v[20:23]\n"
      "buffer_store_dwordx4 v[86:89], v14, s[36:39], s52 offen offset:1536 sc1\n"
      "v_fma_f32 v65, s44, v53, v57\n"
      "v_mfma_f32_16x16x32_bf16 v[20:23], a[4:7], a[104:107], v[20:23]\n"
      "s_mov_b32 m0, s18\n"
      "v_cvt_pk_bf16_f32 v64, v64, v65\n"
      "v_fma_f32 v66, s44, v54, v58\n"
      "v_mfma_f32_16x16x32_bf16 v[20:23], a[8:11], a[116:119], v[20:23]\n"
      "ds_write_addtid_b32 v64 offset:1280\n"
      "v_fma_f32 v67, s44, v55, v59\n"
      "v_mfma_f32_16x16x32_bf16 v[20:23], a[12:15], a[128:131], v[20:23]\n"
      "v_cvt_pk_bf16_f32 v65, v66, v67\n"
      "v_fma_f32 v56, s44, v52, -v56\n"
      "v_mfma_f32_16x16x32_bf16 v[20:23], a[16:19], a[140:143], v[20:23]\n"
      "ds_write_addtid_b32 v65 offset:38208\n"
      "v_fma_f32 v57, s44, v53, -v57\n"
      "v_mfma_f32_16x16x32_bf16 v[20:23], a[20:23], a[152:155], v[20:23]\n"
      "v_cvt_pk_bf16_f32 v56, v56, v57\n"
      "v_fma_f32 v58, s44, v54, -v58\n"
      "v_mfma_f32_16x16x32_bf16 v[76:79], a[84:87], a[68:71], 0\n"
      "ds_write_addtid_b32 v56 offset:2048\n"
      "v_fma_f32 v59, s44, v55, -v59\n"
      "v_mfma_f32_16x16x32_bf16 v[32:35], a[24:27], a[168:171], 0\n"
      "ds_write_addtid_b32 v60 offset:512\n"
      "v_cvt_pk_bf16_f32 v57, v58, v59\n"
      "v_mfma_f32_16x16x32_bf16 v[32:35], a[28:31], a[180:183], v[32:35]\n"
      "ds_write_addtid_b32 v57 offset:38976\n"
      "v_cvt_pk_bf16_f32 v20, v20, v21\n"
      "v_mfma_f32_16x16x32_bf16 v[32:35], a[32:35], a[192:195], v[32:35]\n"
      "ds_write_addtid_b32 v61 offset:37440\n"
      "v_cvt_pk_bf16_f32 v21, v22, v23\n"
      "v_mfma_f32_16x16x32_bf16 v[32:35], a[0:3], a[96:99], v[32:35]\n"
      "s_mov_b32 m0, s45\n"
      "v_cvt_pk_bf16_f32 v84, v76, v77\n"
      "v_cvt_pk_bf16_f32 v90, v78, v79\n"
      "v_mfma_f32_16x16x32_bf16 v[32:35], a[4:7], a[108:111], v[32:35]\n"
      "s_mov_b32 m0, s59\n"
      "v_mfma_f32_16x16x32_bf16 v[32:35], a[8:11], a[120:123], v[32:35]\n"
      "buffer_load_dwordx4 v19, s[32:35], s96 offen lds\n"
      "v_mfma_f32_16x16x32_bf16 v[32:35], a[12:15], a[132:135], v[32:35]\n"
      "s_mov_b32 m0, s45\n"
      "v_mfma_f32_16x16x32_bf16 v[32:35], a[16:19], a[144:147], v[32:35]\n"
      "ds_write_addtid_b32 v20 offset:48136\n"
      "v_mfma_f32_16x16x32_bf16 v[32:35], a[20:23], a[156:159], v[32:35]\n"
      "ds_write_addtid_b32 v21 offset:48392\n"
      "s_nop 0\n"
      "v_mfma_f32_16x16x32_bf16 v[20:23], a[12:15], a[88:91], 0\n"
      "s_add_u32 s51, s51, s12\n"
      "s_nop 1\n"
      "v_mfma_f32_16x16x32_bf16 v[20:23], a[16:19], a[100:103], v[20:23]\n"
      "s_sub_u32 s47, s51, s48\n"
      "v_cvt_pk_bf16_f32 v32, v32, v33\n"
      "v_cvt_pk_bf16_f32 v33, v34, v35\n"
      "v_mfma_f32_16x16x32_bf16 v[20:23], a[20:23], a[112:115], v[20:23]\n"
      "s_cmp_ge_u32 vcc_lo, 0x40000000\n"
      "ds_write_addtid_b32 v32 offset:35840\n"
      "v_mfma_f32_16x16x32_bf16 v[20:23], a[24:27], a[124:127], v[20:23]\n"
      "s_cselect_b32 s53, s47, s51\n"
      "ds_write_addtid_b32 v33 offset:36096\n"
      "v_mfma_f32_16x16x32_bf16 v[20:23], a[28:31], a[136:139], v[20:23]\n"
      "s_mul_i32 vcc_lo, vcc_lo, 2\n"
      "v_mfma_f32_16x16x32_bf16 v[20:23], a[32:35], a[148:151], v[20:23]\n"
      "v_mfma_f32_16x16x32_bf16 v[24:27], a[12:15], a[92:95], 0\n"
      "s_waitcnt vmcnt(19) lgkmcnt(0)\n"
      "s_barrier\n"
      "v_mfma_f32_16x16x32_bf16 v[24:27], a[16:19], a[104:107], v[24:27]\n"
      "s_add_u32 m0, s58, 13280\n"
      "ds_read_b128 a[0:3], v7 offset:0\n"
      "v_mfma_f32_16x16x32_bf16 v[24:27], a[20:23], a[116:119], v[24:27]\n"
      "ds_read_b128 a[4:7], v7 offset:256\n"
      "v_mfma_f32_16x16x32_bf16 v[24:27], a[24:27], a[128:131], v[24:27]\n"
      "ds_read_b128 a[8:11], v7 offset:512\n"
      "v_mfma_f32_16x16x32_bf16 v[24:27], a[28:31], a[140:143], v[24:27]\n"
      "ds_read_b128 a[36:39], v2 offset:0\n"
      "v_mfma_f32_16x16x32_bf16 v[24:27], a[32:35], a[152:155], v[24:27]\n"
      "ds_read_b128 a[40:43], v3 offset:0\n"
      "v_mfma_f32_16x16x32_bf16 v[68:71], a[84:87], a[60:63], 0\n"
      "ds_read_b128 a[44:47], v4 offset:0\n"
      "s_nop 0\n"
      "v_mfma_f32_16x16x32_bf16 v[28:31], a[12:15], a[96:99], 0\n"
      "ds_read2_b64 a[64:67], v11 offset0:0 offset1:64\n"
      "s_nop 0\n"
      "v_mfma_f32_16x16x32_bf16 v[28:31], a[16:19], a[108:111], v[28:31]\n"
      "buffer_load_dwordx4 v16, s[32:35], s53 offen lds\n"
      "v_mfma_f32_16x16x32_bf16 v[28:31], a[20:23], a[120:123], v[28:31]\n"
      "s_mov_b32 m0, s45\n"
      "ds_read2_b64 a[68:71], v11 offset0:1 offset1:65\n"
      "v_cvt_pk_bf16_f32 v85, v68, v69\n"
      "v_mfma_f32_16x16x32_bf16 v[28:31], a[24:27], a[132:135], v[28:31]\n"
      "ds_read_b128 a[12:15], v6 offset:768\n"
      "v_cvt_pk_bf16_f32 v91, v70, v71\n"
      "v_mfma_f32_16x16x32_bf16 v[28:31], a[28:31], a[144:147], v[28:31]\n"
      "ds_read_b128 a[16:19], v6 offset:1024\n"
      "v_mfma_f32_16x16x32_bf16 v[28:31], a[32:35], a[156:159], v[28:31]\n"
      "s_waitcnt lgkmcnt(6)\n"
      "ds_read_b128 a[20:23], v6 offset:1280\n"
      "v_mfma_f32_16x16x32_bf16 v[20:23], a[0:3], a[160:163], v[20:23]\n"
      "buffer_store_dwordx2 v[84:85], v15, s[36:39], s52 offen sc1\n"
      "v_mfma_f32_16x16x32_bf16 v[20:23], a[4:7], a[172:175], v[20:23]\n"
      "ds_read_b128 a[24:27], v6 offset:5376\n"
      "v_mfma_f32_16x16x32_bf16 v[20:23], a[8:11], a[184:187], v[20:23]\n"
      "ds_read_b128 a[28:31], v6 offset:5632\n"
      "s_nop 0\n"
      "v_mfma_f32_16x16x32_bf16 v[36:39], a[36:39], a[72:75], 0\n"
      "s_waitcnt lgkmcnt(6)\n"
      "ds_read_b128 a[32:35], v6 offset:5888\n"
      "s_nop 0\n"
      "v_mfma_f32_16x16x32_bf16 v[40:43], a[40:43], a[76:79], 0\n"
      "ds_read_b128 a[48:51], v2 offset:64\n"
      "v_cvt_pk_bf16_f32 v20, v20, v21\n"
      "v_mfma_f32_16x16x32_bf16 v[44:47], a[44:47], a[80:83], v[36:39]\n"
      "buffer_store_dwordx2 v[90:91], v15, s[36:39], s52 offen offset:1536 sc1\n"
      "v_cvt_pk_bf16_f32 v21, v22, v23\n"
      "v_mfma_f32_16x16x32_bf16 v[24:27], a[0:3], a[164:167], v[24:27]\n"
      "s_mov_b32 s52, s13\n"
      "ds_write_addtid_b32 v20 offset:57360\n"
      "v_fma_f32 v48, s44, v36, v40\n"
      "v_mfma_f32_16x16x32_bf16 v[24:27], a[4:7], a[176:179], v[24:27]\n"
      "s_add_u32 s13, s13, s12\n"
      "ds_write_addtid_b32 v21 offset:57616\n"
      "v_fma_f32 v49, s44, v37, v41\n"
      "v_mfma_f32_16x16x32_bf16 v[24:27], a[8:11], a[188:191], v[24:27]\n"
      "ds_read_b128 a[52:55], v3 offset:64\n"
      "v_cvt_pk_bf16_f32 v48, v48, v49\n"
      "v_mfma_f32_16x16x32_bf16 v[72:75], a[84:87], a[64:67], 0\n"
      "ds_read_b128 a[56:59], v4 offset:64\n"
      "v_fma_f32 v50, s44, v38, v42\n"
      "v_mfma_f32_16x16x32_bf16 v[28:31], a[0:3], a[168:171], v[28:31]\n"
      "ds_read_b128 a[0:3], v7 offset:768\n"
      "v_fma_f32 v51, s44, v39, v43\n"
      "v_mfma_f32_16x16x32_bf16 v[28:31], a[4:7], a[180:183], v[28:31]\n"
      "ds_read_b128 a[4:7], v7 offset:1024\n"
      "v_cvt_pk_bf16_f32 v24, v24, v25\n"
      "v_mfma_f32_16x16x32_bf16 v[28:31], a[8:11], a[192:195], v[28:31]\n"
      "s_waitcnt lgkmcnt(6)\n"
      "ds_read_b128 a[8:11], v7 offset:1280\n"
      "v_cvt_pk_bf16_f32 v25, v26, v27\n"
      "v_mfma_f32_16x16x32_bf16 v[76:79], a[84:87], a[68:71], 0\n"
      "ds_write_addtid_b32 v24 offset:45064\n"
      "v_fma_f32 v40, s44, v36, -v40\n"
      "v_mfma_f32_16x16x32_bf16 v[32:35], a[12:15], v[92:95], 0\n"
      "ds_write_addtid_b32 v25 offset:45320\n"
      "v_fma_f32 v41, s44, v37, -v41\n"
      "v_mfma_f32_16x16x32_bf16 v[32:35], a[16:19], v[148:151], v[32:35]\n"
      "s_mov_b32 m0, s18\n"
      "ds_read2_b64 a[60:63], v11 offset0:2 offset1:66\n"
      "v_fma_f32 v42, s44, v38, -v42\n"
      "v_mfma_f32_16x16x32_bf16 v[32:35], a[20:23], v[204:207], v[32:35]\n"
      "ds_write_addtid_b32 v48 offset:5376\n"
      "v_fma_f32 v43, s44, v39, -v43\n"
      "v_mfma_f32_16x16x32_bf16 v[32:35], a[24:27], v[112:115], v[32:35]\n"
      "v_cvt_pk_bf16_f32 v49, v50, v51\n"
      "v_cvt_pk_bf16_f32 v40, v40, v41\n"
      "v_mfma_f32_16x16x32_bf16 v[32:35], a[28:31], a[208:211], v[32:35]\n"
      "v_cvt_pk_bf16_f32 v41, v42, v43\n"
      "v_cvt_pk_bf16_f32 v44, v44, v45\n"
      "v_mfma_f32_16x16x32_bf16 v[32:35], a[32:35], v[220:223], v[32:35]\n"
      "s_waitcnt lgkmcnt(5)\n"
      "ds_write_addtid_b32 v49 offset:42304\n"
      "v_cvt_pk_bf16_f32 v45, v46, v47\n"
      "v_mfma_f32_16x16x32_bf16 v[32:35], a[0:3], a[212:215], v[32:35]\n"
      "ds_write_addtid_b32 v40 offset:6144\n"
      "v_cvt_pk_bf16_f32 v28, v28, v29\n"
      "v_mfma_f32_16x16x32_bf16 v[32:35], a[4:7], v[184:187], v[32:35]\n"
      "s_waitcnt lgkmcnt(6)\n"
      "ds_write_addtid_b32 v41 offset:43072\n"
      "v_cvt_pk_bf16_f32 v29, v30, v31\n"
      "v_mfma_f32_16x16x32_bf16 v[32:35], a[8:11], v[240:243], v[32:35]\n"
      "ds_write_addtid_b32 v44 offset:4608\n"
      "v_cvt_pk_bf16_f32 v80, v72, v73\n"
      "v_mfma_f32_16x16x32_bf16 v[20:23], a[12:15], v[100:103], 0\n"
      "ds_write_addtid_b32 v45 offset:41536\n"
      "v_cvt_pk_bf16_f32 v81, v76, v77\n"
      "v_mfma_f32_16x16x32_bf16 v[20:23], a[16:19], a[200:203], v[20:23]\n"
      "s_mov_b32 m0, s45\n"
      "ds_read_b128 a[36:39], v2 offset:128\n"
      "v_cvt_pk_bf16_f32 v32, v32, v33\n"
      "v_mfma_f32_16x16x32_bf16 v[20:23], a[20:23], v[208:211], v[20:23]\n"
      "ds_write_addtid_b32 v28 offset:32768\n"
      "v_cvt_pk_bf16_f32 v33, v34, v35\n"
      "v_mfma_f32_16x16x32_bf16 v[20:23], a[24:27], a[204:207], v[20:23]\n"
      "ds_write_addtid_b32 v29 offset:33024\n"
      "v_cvt_pk_bf16_f32 v86, v74, v75\n"
      "v_mfma_f32_16x16x32_bf16 v[20:23], a[28:31], v[172:175], v[20:23]\n"
      "s_add_u32 m0, s58, 13280\n"
      "ds_read_b128 a[44:47], v4 offset:128\n"
      "v_cvt_pk_bf16_f32 v87, v78, v79\n"
      "v_mfma_f32_16x16x32_bf16 v[20:23], a[32:35], v[228:231], v[20:23]\n"
      "buffer_load_dwordx4 v17, s[32:35], s53 offen offset:1024 lds\n"
      "v_mfma_f32_16x16x32_bf16 v[20:23], a[0:3], v[136:139], v[20:23]\n"
      "s_mov_b32 m0, s45\n"
      "ds_read2_b64 a[64:67], v11 offset0:3 offset1:67\n"
      "v_mfma_f32_16x16x32_bf16 v[20:23], a[4:7], v[192:195], v[20:23]\n"
      "ds_write_addtid_b32 v32 offset:58384\n"
      "v_mfma_f32_16x16x32_bf16 v[20:23], a[8:11], v[244:247], v[20:23]\n"
      "ds_write_addtid_b32 v33 offset:58640\n"
      "s_nop 0\n"
      "v_mfma_f32_16x16x32_bf16 v[52:55], a[48:51], a[72:75], 0\n"
      "ds_read_b128 a[40:43], v3 offset:128\n"
      "s_nop 0\n"
      "v_mfma_f32_16x16x32_bf16 v[56:59], a[52:55], a[76:79], 0\n"
      "global_load_dword v8, v9, s[54:55]\n"
      "v_mfma_f32_16x16x32_bf16 v[60:63], a[56:59], a[80:83], v[52:55]\n"
      "s_waitcnt lgkmcnt(7)\n"
      "ds_read2_b64 a[68:71], v13 offset0:0 offset1:64\n"
      "v_cvt_pk_bf16_f32 v20, v20, v21\n"
      "v_mfma_f32_16x16x32_bf16 v[68:71], a[84:87], a[60:63], 0\n"
      "ds_write_addtid_b32 v20 offset:46088\n"
      "v_cvt_pk_bf16_f32 v21, v22, v23\n"
      "v_mfma_f32_16x16x32_bf16 v[24:27], a[12:15], a[196:199], 0\n"
      "ds_read_b128 a[12:15], v6 offset:1536\n"
      "v_fma_f32 v64, s44, v52, v56\n"
      "v_mfma_f32_16x16x32_bf16 v[24:27], a[16:19], v[160:163], v[24:27]\n"
      "ds_read_b128 a[16:19], v6 offset:1792\n"
      "v_fma_f32 v65, s44, v53, v57\n"
      "v_mfma_f32_16x16x32_bf16 v[24:27], a[20:23], v[216:219], v[24:27]\n"
      "ds_read_b128 a[20:23], v6 offset:2048\n"
      "v_cvt_pk_bf16_f32 v64, v64, v65\n"
      "v_mfma_f32_16x16x32_bf16 v[24:27], a[24:27], v[124:127], v[24:27]\n"
      "ds_read_b128 a[24:27], v6 offset:6144\n"
      "v_fma_f32 v66, s44, v54, v58\n"
      "v_mfma_f32_16x16x32_bf16 v[24:27], a[28:31], v[180:183], v[24:27]\n"
      "ds_read_b128 a[28:31], v6 offset:6400\n"
      "v_fma_f32 v67, s44, v55, v59\n"
      "v_mfma_f32_16x16x32_bf16 v[24:27], a[32:35], v[232:235], v[24:27]\n"
      "ds_read_b128 a[32:35], v6 offset:6656\n"
      "v_cvt_pk_bf16_f32 v65, v66, v67\n"
      "v_mfma_f32_16x16x32_bf16 v[24:27], a[0:3], a[216:219], v[24:27]\n"
      "ds_read_b128 a[0:3], v7 offset:1536\n"
      "v_fma_f32 v56, s44, v52, -v56\n"
      "v_mfma_f32_16x16x32_bf16 v[24:27], a[4:7], v[196:199], v[24:27]\n"
      "ds_read_b128 a[4:7], v7 offset:1792\n"
      "v_fma_f32 v57, s44, v53, -v57\n"
      "v_mfma_f32_16x16x32_bf16 v[24:27], a[8:11], v[252:255], v[24:27]\n"
      "ds_read_b128 a[8:11], v7 offset:2048\n"
      "v_cvt_pk_bf16_f32 v56, v56, v57\n"
      "v_mfma_f32_16x16x32_bf16 v[36:39], a[36:39], a[72:75], 0\n"
      "s_waitcnt lgkmcnt(7)\n"
      "ds_write_addtid_b32 v21 offset:46344\n"
      "v_fma_f32 v58, s44, v54, -v58\n"
      "v_mfma_f32_16x16x32_bf16 v[44:47], a[44:47], a[80:83], v[36:39]\n"
      "s_add_u32 m0, s58, 13280\n"
      "v_cvt_pk_bf16_f32 v24, v24, v25\n"
      "v_cvt_pk_bf16_f32 v25, v26, v27\n"
      "v_mfma_f32_16x16x32_bf16 v[72:75], a[84:87], a[64:67], 0\n"
      "buffer_load_dwordx4 v18, s[32:35], s53 offen offset:2048 lds\n"
      "v_fma_f32 v59, s44, v55, -v59\n"
      "v_mfma_f32_16x16x32_bf16 v[28:31], a[12:15], v[96:99], 0\n"
      "s_mov_b32 m0, s45\n"
      "v_cvt_pk_bf16_f32 v57, v58, v59\n"
      "v_cvt_pk_bf16_f32 v60, v60, v61\n"
      "v_mfma_f32_16x16x32_bf16 v[28:31], a[16:19], v[152:155], v[28:31]\n"
      "s_waitcnt lgkmcnt(4)\n"
      "ds_write_addtid_b32 v24 offset:33792\n"
      "v_cvt_pk_bf16_f32 v82, v68, v69\n"
      "v_mfma_f32_16x16x32_bf16 v[28:31], a[20:23], a[220:223], v[28:31]\n"
      "ds_write_addtid_b32 v25 offset:34048\n"
      "v_cvt_pk_bf16_f32 v83, v72, v73\n"
      "v_mfma_f32_16x16x32_bf16 v[28:31], a[24:27], v[116:119], v[28:31]\n"
      "s_mov_b32 m0, s18\n"
      "v_cvt_pk_bf16_f32 v61, v62, v63\n"
      "v_cvt_pk_bf16_f32 v88, v70, v71\n"
      "v_mfma_f32_16x16x32_bf16 v[28:31], a[28:31], v[168:171], v[28:31]\n"
      "ds_write_addtid_b32 v64 offset:5632\n"
      "v_cvt_pk_bf16_f32 v89, v74, v75\n"
      "v_mfma_f32_16x16x32_bf16 v[28:31], a[32:35], v[224:227], v[28:31]\n"
      "s_waitcnt lgkmcnt(4)\n"
      "ds_write_addtid_b32 v65 offset:42560\n"
      "v_cvt_pk_bf16_f32 v44, v44, v45\n"
      "v_mfma_f32_16x16x32_bf16 v[28:31], a[0:3], v[132:135], v[28:31]\n"
      "ds_write_addtid_b32 v56 offset:6400\n"
      "v_cvt_pk_bf16_f32 v45, v46, v47\n"
      "v_mfma_f32_16x16x32_bf16 v[28:31], a[4:7], v[188:191], v[28:31]\n"
      "ds_write_addtid_b32 v57 offset:43328\n"
      "v_mfma_f32_16x16x32_bf16 v[28:31], a[8:11], a[240:243], v[28:31]\n"
      "ds_write_addtid_b32 v60 offset:4864\n"
      "s_nop 0\n"
      "v_mfma_f32_16x16x32_bf16 v[40:43], a[40:43], a[76:79], 0\n"
      "buffer_store_dwordx4 v[80:83], v14, s[36:39], s52 offen sc1\n"
      "s_nop 0\n"
      "v_mfma_f32_16x16x32_bf16 v[20:23], a[12:15], v[104:107], 0\n"
      "ds_write_addtid_b32 v61 offset:41792\n"
      "v_mfma_f32_16x16x32_bf16 v[20:23], a[16:19], v[156:159], v[20:23]\n"
      "s_mov_b32 m0, s45\n"
      "ds_read2_b64 a[60:63], v13 offset0:1 offset1:65\n"
      "v_cvt_pk_bf16_f32 v28, v28, v29\n"
      "v_mfma_f32_16x16x32_bf16 v[20:23], a[20:23], v[212:215], v[20:23]\n"
      "ds_write_addtid_b32 v28 offset:59408\n"
      "v_cvt_pk_bf16_f32 v29, v30, v31\n"
      "v_mfma_f32_16x16x32_bf16 v[20:23], a[24:27], v[120:123], v[20:23]\n"
      "ds_write_addtid_b32 v29 offset:59664\n"
      "v_fma_f32 v48, s44, v36, v40\n"
      "v_mfma_f32_16x16x32_bf16 v[20:23], a[28:31], v[176:179], v[20:23]\n"
      "buffer_store_dwordx4 v[86:89], v14, s[36:39], s52 offen offset:1536 sc1\n"
      "v_fma_f32 v49, s44, v37, v41\n"
      "v_mfma_f32_16x16x32_bf16 v[20:23], a[32:35], a[232:235], v[20:23]\n"
      "s_mov_b32 m0, s18\n"
      "v_cvt_pk_bf16_f32 v48, v48, v49\n"
      "v_fma_f32 v50, s44, v38, v42\n"
      "v_mfma_f32_16x16x32_bf16 v[20:23], a[0:3], v[140:143], v[20:23]\n"
      "s_sub_u32 s18, s19, s18\n"
      "ds_write_addtid_b32 v48 offset:5888\n"
      "v_fma_f32 v51, s44, v39, v43\n"
      "v_mfma_f32_16x16x32_bf16 v[20:23], a[4:7], a[236:239], v[20:23]\n"
      "v_cvt_pk_bf16_f32 v49, v50, v51\n"
      "v_fma_f32 v40, s44, v36, -v40\n"
      "v_mfma_f32_16x16x32_bf16 v[20:23], a[8:11], v[248:251], v[20:23]\n"
      "ds_write_addtid_b32 v49 offset:42816\n"
      "v_fma_f32 v41, s44, v37, -v41\n"
      "v_mfma_f32_16x16x32_bf16 v[76:79], a[84:87], a[68:71], 0\n"
      "v_cvt_pk_bf16_f32 v40, v40, v41\n"
      "v_fma_f32 v42, s44, v38, -v42\n"
      "v_mfma_f32_16x16x32_bf16 v[32:35], a[12:15], v[108:111], 0\n"
      "ds_write_addtid_b32 v40 offset:6656\n"
      "v_fma_f32 v43, s44, v39, -v43\n"
      "v_mfma_f32_16x16x32_bf16 v[32:35], a[16:19], v[164:167], v[32:35]\n"
      "ds_write_addtid_b32 v44 offset:5120\n"
      "v_cvt_pk_bf16_f32 v41, v42, v43\n"
      "v_mfma_f32_16x16x32_bf16 v[32:35], a[20:23], a[224:227], v[32:35]\n"
      "ds_write_addtid_b32 v41 offset:43584\n"
      "v_cvt_pk_bf16_f32 v20, v20, v21\n"
      "v_mfma_f32_16x16x32_bf16 v[32:35], a[24:27], v[128:131], v[32:35]\n"
      "ds_write_addtid_b32 v45 offset:42048\n"
      "v_cvt_pk_bf16_f32 v21, v22, v23\n"
      "v_mfma_f32_16x16x32_bf16 v[32:35], a[28:31], a[228:231], v[32:35]\n"
      "s_mov_b32 m0, s45\n"
      "v_cvt_pk_bf16_f32 v84, v76, v77\n"
      "v_cvt_pk_bf16_f32 v90, v78, v79\n"
      "v_mfma_f32_16x16x32_bf16 v[32:35], a[32:35], v[236:239], v[32:35]\n"
      "ds_write_addtid_b32 v20 offset:47112\n"
      "v_mfma_f32_16x16x32_bf16 v[32:35], a[0:3], v[144:147], v[32:35]\n"
      "ds_write_addtid_b32 v21 offset:47368\n"
      "v_mfma_f32_16x16x32_bf16 v[32:35], a[4:7], v[200:203], v[32:35]\n"
      "v_mfma_f32_16x16x32_bf16 v[32:35], a[8:11], a[244:247], v[32:35]\n"
      "s_nop 1\n"
      "s_cbranch_vccnz Lconv_loop0\n"
      "Lconv_t0:\n"
      "s_nop 1\n"
      "v_mfma_f32_16x16x32_bf16 v[20:23], a[24:27], v[96:99], 0\n"
      "s_nop 1\n"
      "v_mfma_f32_16x16x32_bf16 v[20:23], a[28:31], v[152:155], v[20:23]\n"
      "v_cvt_pk_bf16_f32 v32, v32, v33\n"
      "v_cvt_pk_bf16_f32 v33, v34, v35\n"
      "v_mfma_f32_16x16x32_bf16 v[20:23], a[32:35], a[220:223], v[20:23]\n"
      "ds_write_addtid_b32 v32 offset:34816\n"
      "v_mfma_f32_16x16x32_bf16 v[20:23], a[0:3], v[116:119], v[20:23]\n"
      "ds_write_addtid_b32 v33 offset:35072\n"
      "v_mfma_f32_16x16x32_bf16 v[20:23], a[4:7], v[168:171], v[20:23]\n"
      "v_mfma_f32_16x16x32_bf16 v[20:23], a[8:11], v[224:227], v[20:23]\n"
      "v_mfma_f32_16x16x32_bf16 v[24:27], a[24:27], v[104:107], 0\n"
      "s_waitcnt lgkmcnt(0)\n"
      "s_barrier\n"
      "v_mfma_f32_16x16x32_bf16 v[24:27], a[28:31], v[156:159], v[24:27]\n"
      "ds_read2_b64 a[64:67], v10 offset0:0 offset1:64\n"
      "v_mfma_f32_16x16x32_bf16 v[24:27], a[32:35], v[212:215], v[24:27]\n"
      "ds_read_b128 a[12:15], v7 offset:6144\n"
      "v_mfma_f32_16x16x32_bf16 v[24:27], a[0:3], v[120:123], v[24:27]\n"
      "ds_read2_b64 a[68:71], v10 offset0:1 offset1:65\n"
      "v_mfma_f32_16x16x32_bf16 v[24:27], a[4:7], v[176:179], v[24:27]\n"
      "ds_read_b128 a[16:19], v7 offset:6400\n"
      "v_mfma_f32_16x16x32_bf16 v[24:27], a[8:11], a[232:235], v[24:27]\n"
      "ds_read_b128 a[20:23], v7 offset:6656\n"
      "v_mfma_f32_16x16x32_bf16 v[68:71], a[84:87], a[60:63], 0\n"
      "ds_read2_b64 a[60:63], v10 offset0:2 offset1:66\n"
      "s_nop 0\n"
      "v_mfma_f32_16x16x32_bf16 v[28:31], a[24:27], v[108:111], 0\n"
      "s_waitcnt lgkmcnt(5)\n"
      "ds_read_b128 a[24:27], v6 offset:5376\n"
      "s_nop 0\n"
      "v_mfma_f32_16x16x32_bf16 v[72:75], a[84:87], a[64:67], 0\n"
      "ds_read2_b64 a[64:67], v10 offset0:3 offset1:67\n"
      "v_cvt_pk_bf16_f32 v85, v68, v69\n"
      "v_mfma_f32_16x16x32_bf16 v[28:31], a[28:31], v[164:167], v[28:31]\n"
      "buffer_store_dwordx2 v[84:85], v15, s[36:39], s52 offen sc1\n"
      "v_cvt_pk_bf16_f32 v91, v70, v71\n"
      "v_mfma_f32_16x16x32_bf16 v[28:31], a[32:35], a[224:227], v[28:31]\n"
      "ds_read_b128 a[28:31], v6 offset:5632\n"
      "v_mfma_f32_16x16x32_bf16 v[28:31], a[0:3], v[128:131], v[28:31]\n"
      "ds_read_b128 a[32:35], v6 offset:5888\n"
      "v_cvt_pk_bf16_f32 v80, v72, v73\n"
      "v_mfma_f32_16x16x32_bf16 v[28:31], a[4:7], a[228:231], v[28:31]\n"
      "ds_read_b128 a[0:3], v7 offset:768\n"
      "v_cvt_pk_bf16_f32 v86, v74, v75\n"
      "v_mfma_f32_16x16x32_bf16 v[28:31], a[8:11], v[236:239], v[28:31]\n"
      "s_waitcnt lgkmcnt(6)\n"
      "ds_read_b128 a[4:7], v7 offset:1024\n"
      "v_mfma_f32_16x16x32_bf16 v[20:23], a[12:15], v[132:135], v[20:23]\n"
      "ds_read_b128 a[8:11], v7 offset:1280\n"
      "v_mfma_f32_16x16x32_bf16 v[76:79], a[84:87], a[68:71], 0\n"
      "s_waitcnt lgkmcnt(6)\n"
      "ds_read2_b64 a[68:71], v12 offset0:0 offset1:64\n"
      "s_nop 0\n"
      "v_mfma_f32_16x16x32_bf16 v[68:71], a[84:87], a[60:63], 0\n"
      "ds_read2_b64 a[60:63], v12 offset0:1 offset1:65\n"
      "s_nop 0\n"
      "v_mfma_f32_16x16x32_bf16 v[20:23], a[16:19], v[188:191], v[20:23]\n"
      "buffer_store_dwordx2 v[90:91], v15, s[36:39], s52 offen offset:1536 sc1\n"
      "v_cvt_pk_bf16_f32 v81, v76, v77\n"
      "v_mfma_f32_16x16x32_bf16 v[20:23], a[20:23], a[240:243], v[20:23]\n"
      "s_mov_b32 s52, s13\n"
      "v_cvt_pk_bf16_f32 v87, v78, v79\n"
      "v_cvt_pk_bf16_f32 v82, v68, v69\n"
      "v_mfma_f32_16x16x32_bf16 v[24:27], a[12:15], v[140:143], v[24:27]\n"
      "s_waitcnt lgkmcnt(5)\n"
      "v_cvt_pk_bf16_f32 v88, v70, v71\n"
      "s_nop 0\n"
      "v_mfma_f32_16x16x32_bf16 v[72:75], a[84:87], a[64:67], 0\n"
      "s_add_u32 s13, s13, s12\n"
      "v_cvt_pk_bf16_f32 v20, v20, v21\n"
      "v_cvt_pk_bf16_f32 v21, v22, v23\n"
      "v_mfma_f32_16x16x32_bf16 v[24:27], a[16:19], a[236:239], v[24:27]\n"
      "ds_write_addtid_b32 v20 offset:62480\n"
      "s_nop 0\n"
      "v_mfma_f32_16x16x32_bf16 v[24:27], a[20:23], v[248:251], v[24:27]\n"
      "ds_write_addtid_b32 v21 offset:62736\n"
      "v_cvt_pk_bf16_f32 v83, v72, v73\n"
      "v_mfma_f32_16x16x32_bf16 v[28:31], a[12:15], v[144:147], v[28:31]\n"
      "s_waitcnt lgkmcnt(3)\n"
      "ds_read_b128 a[12:15], v7 offset:5376\n"
      "v_cvt_pk_bf16_f32 v89, v74, v75\n"
      "v_mfma_f32_16x16x32_bf16 v[76:79], a[84:87], a[68:71], 0\n"
      "buffer_store_dwordx4 v[80:83], v14, s[36:39], s52 offen sc1\n"
      "v_cvt_pk_bf16_f32 v24, v24, v25\n"
      "v_mfma_f32_16x16x32_bf16 v[28:31], a[16:19], v[200:203], v[28:31]\n"
      "ds_read_b128 a[16:19], v7 offset:5632\n"
      "v_cvt_pk_bf16_f32 v25, v26, v27\n"
      "v_mfma_f32_16x16x32_bf16 v[28:31], a[20:23], a[244:247], v[28:31]\n"
      "ds_read_b128 a[20:23], v7 offset:5888\n"
      "v_mfma_f32_16x16x32_bf16 v[32:35], a[24:27], v[92:95], 0\n"
      "s_waitcnt lgkmcnt(5)\n"
      "ds_write_addtid_b32 v24 offset:50184\n"
      "v_cvt_pk_bf16_f32 v84, v76, v77\n"
      "v_mfma_f32_16x16x32_bf16 v[68:71], a[84:87], a[60:63], 0\n"
      "ds_write_addtid_b32 v25 offset:50440\n"
      "v_cvt_pk_bf16_f32 v90, v78, v79\n"
      "v_mfma_f32_16x16x32_bf16 v[32:35], a[28:31], v[148:151], v[32:35]\n"
      "buffer_store_dwordx4 v[86:89], v14, s[36:39], s52 offen offset:1536 sc1\n"
      "v_cvt_pk_bf16_f32 v28, v28, v29\n"
      "v_mfma_f32_16x16x32_bf16 v[32:35], a[32:35], v[204:207], v[32:35]\n"
      "ds_write_addtid_b32 v28 offset:37888\n"
      "v_cvt_pk_bf16_f32 v29, v30, v31\n"
      "v_mfma_f32_16x16x32_bf16 v[32:35], a[0:3], v[112:115], v[32:35]\n"
      "v_cvt_pk_bf16_f32 v85, v68, v69\n"
      "v_cvt_pk_bf16_f32 v91, v70, v71\n"
      "v_mfma_f32_16x16x32_bf16 v[32:35], a[4:7], a[208:211], v[32:35]\n"
      "ds_write_addtid_b32 v29 offset:38144\n"
      "v_mfma_f32_16x16x32_bf16 v[32:35], a[8:11], v[220:223], v[32:35]\n"
      "s_waitcnt lgkmcnt(5)\n"
      "v_mfma_f32_16x16x32_bf16 v[32:35], a[12:15], a[212:215], v[32:35]\n"
      "buffer_store_dwordx2 v[84:85], v15, s[36:39], s52 offen sc1\n"
      "v_mfma_f32_16x16x32_bf16 v[32:35], a[16:19], v[184:187], v[32:35]\n"
      "s_waitcnt lgkmcnt(4)\n"
      "v_mfma_f32_16x16x32_bf16 v[32:35], a[20:23], v[240:243], v[32:35]\n"
      "s_nop 1\n"
      "v_mfma_f32_16x16x32_bf16 v[20:23], a[24:27], v[100:103], 0\n"
      "s_nop 1\n"
      "v_mfma_f32_16x16x32_bf16 v[20:23], a[28:31], a[200:203], v[20:23]\n"
      "s_nop 0\n"
      "v_mfma_f32_16x16x32_bf16 v[20:23], a[32:35], v[208:211], v[20:23]\n"
      "buffer_store_dwordx2 v[90:91], v15, s[36:39], s52 offen offset:1536 sc1\n"
      "v_cvt_pk_bf16_f32 v32, v32, v33\n"
      "v_mfma_f32_16x16x32_bf16 v[20:23], a[0:3], a[204:207], v[20:23]\n"
      "ds_write_addtid_b32 v32 offset:61456\n"
      "v_cvt_pk_bf16_f32 v33, v34, v35\n"
      "v_mfma_f32_16x16x32_bf16 v[20:23], a[4:7], v[172:175], v[20:23]\n"
      "ds_write_addtid_b32 v33 offset:61712\n"
      "v_mfma_f32_16x16x32_bf16 v[20:23], a[8:11], v[228:231], v[20:23]\n"
      "v_mfma_f32_16x16x32_bf16 v[20:23], a[12:15], v[136:139], v[20:23]\n"
      "v_mfma_f32_16x16x32_bf16 v[20:23], a[16:19], v[192:195], v[20:23]\n"
      "v_mfma_f32_16x16x32_bf16 v[20:23], a[20:23], v[244:247], v[20:23]\n"
      "s_nop 1\n"
      "v_mfma_f32_16x16x32_bf16 v[24:27], a[24:27], a[196:199], 0\n"
      "ds_read_b128 a[24:27], v7 offset:4608\n"
      "s_nop 0\n"
      "v_mfma_f32_16x16x32_bf16 v[24:27], a[28:31], v[160:163], v[24:27]\n"
      "ds_read_b128 a[28:31], v7 offset:4864\n"
      "v_mfma_f32_16x16x32_bf16 v[24:27], a[32:35], v[216:219], v[24:27]\n"
      "ds_read_b128 a[32:35], v7 offset:5120\n"
      "v_cvt_pk_bf16_f32 v20, v20, v21\n"
      "v_mfma_f32_16x16x32_bf16 v[24:27], a[0:3], v[124:127], v[24:27]\n"
      "ds_read_b128 a[0:3], v6 offset:4608\n"
      "v_cvt_pk_bf16_f32 v21, v22, v23\n"
      "v_mfma_f32_16x16x32_bf16 v[24:27], a[4:7], v[180:183], v[24:27]\n"
      "ds_read_b128 a[4:7], v6 offset:4864\n"
      "v_mfma_f32_16x16x32_bf16 v[24:27], a[8:11], v[232:235], v[24:27]\n"
      "ds_read_b128 a[8:11], v6 offset:5120\n"
      "v_mfma_f32_16x16x32_bf16 v[24:27], a[12:15], a[216:219], v[24:27]\n"
      "ds_read_b128 a[12:15], v7 offset:0\n"
      "v_mfma_f32_16x16x32_bf16 v[24:27], a[16:19], v[196:199], v[24:27]\n"
      "ds_read_b128 a[16:19], v7 offset:256\n"
      "v_mfma_f32_16x16x32_bf16 v[24:27], a[20:23], v[252:255], v[24:27]\n"
      "s_waitcnt lgkmcnt(7)\n"
      "ds_read_b128 a[20:23], v7 offset:512\n"
      "s_nop 0\n"
      "v_mfma_f32_16x16x32_bf16 v[28:31], a[24:27], a[160:163], 0\n"
      "s_waitcnt lgkmcnt(7)\n"
      "ds_write_addtid_b32 v20 offset:49160\n"
      "s_nop 0\n"
      "v_mfma_f32_16x16x32_bf16 v[28:31], a[28:31], a[172:175], v[28:31]\n"
      "s_waitcnt lgkmcnt(7)\n"
      "ds_write_addtid_b32 v21 offset:49416\n"
      "v_cvt_pk_bf16_f32 v24, v24, v25\n"
      "v_mfma_f32_16x16x32_bf16 v[28:31], a[32:35], a[184:187], v[28:31]\n"
      "s_waitcnt lgkmcnt(7)\n"
      "ds_write_addtid_b32 v24 offset:36864\n"
      "v_cvt_pk_bf16_f32 v25, v26, v27\n"
      "v_mfma_f32_16x16x32_bf16 v[28:31], a[0:3], a[88:91], v[28:31]\n"
      "s_waitcnt lgkmcnt(7)\n"
      "ds_write_addtid_b32 v25 offset:37120\n"
      "v_mfma_f32_16x16x32_bf16 v[28:31], a[4:7], a[100:103], v[28:31]\n"
      "s_waitcnt lgkmcnt(7)\n"
      "v_mfma_f32_16x16x32_bf16 v[28:31], a[8:11], a[112:115], v[28:31]\n"
      "s_waitcnt lgkmcnt(6)\n"
      "v_mfma_f32_16x16x32_bf16 v[28:31], a[12:15], a[124:127], v[28:31]\n"
      "s_waitcnt lgkmcnt(5)\n"
      "v_mfma_f32_16x16x32_bf16 v[28:31], a[16:19], a[136:139], v[28:31]\n"
      "s_waitcnt lgkmcnt(4)\n"
      "v_mfma_f32_16x16x32_bf16 v[28:31], a[20:23], a[148:151], v[28:31]\n"
      "s_nop 1\n"
      "v_mfma_f32_16x16x32_bf16 v[20:23], a[24:27], a[164:167], 0\n"
      "s_nop 1\n"
      "v_mfma_f32_16x16x32_bf16 v[20:23], a[28:31], a[176:179], v[20:23]\n"
      "s_nop 0\n"
      "v_mfma_f32_16x16x32_bf16 v[20:23], a[32:35], a[188:191], v[20:23]\n"
      "v_cvt_pk_bf16_f32 v28, v28, v29\n"
      "v_cvt_pk_bf16_f32 v29, v30, v31\n"
      "v_mfma_f32_16x16x32_bf16 v[20:23], a[0:3], a[92:95], v[20:23]\n"
      "ds_write_addtid_b32 v28 offset:60432\n"
      "v_mfma_f32_16x16x32_bf16 v[20:23], a[4:7], a[104:107], v[20:23]\n"
      "ds_write_addtid_b32 v29 offset:60688\n"
      "v_mfma_f32_16x16x32_bf16 v[20:23], a[8:11], a[116:119], v[20:23]\n"
      "v_mfma_f32_16x16x32_bf16 v[20:23], a[12:15], a[128:131], v[20:23]\n"
      "v_mfma_f32_16x16x32_bf16 v[20:23], a[16:19], a[140:143], v[20:23]\n"
      "v_mfma_f32_16x16x32_bf16 v[20:23], a[20:23], a[152:155], v[20:23]\n"
      "s_nop 1\n"
      "v_mfma_f32_16x16x32_bf16 v[32:35], a[24:27], a[168:171], 0\n"
      "s_nop 1\n"
      "v_mfma_f32_16x16x32_bf16 v[32:35], a[28:31], a[180:183], v[32:35]\n"
      "s_nop 0\n"
      "v_mfma_f32_16x16x32_bf16 v[32:35], a[32:35], a[192:195], v[32:35]\n"
      "v_cvt_pk_bf16_f32 v20, v20, v21\n"
      "v_cvt_pk_bf16_f32 v21, v22, v23\n"
      "v_mfma_f32_16x16x32_bf16 v[32:35], a[0:3], a[96:99], v[32:35]\n"
      "ds_write_addtid_b32 v20 offset:48136\n"
      "v_mfma_f32_16x16x32_bf16 v[32:35], a[4:7], a[108:111], v[32:35]\n"
      "ds_write_addtid_b32 v21 offset:48392\n"
      "v_mfma_f32_16x16x32_bf16 v[32:35], a[8:11], a[120:123], v[32:35]\n"
      "v_mfma_f32_16x16x32_bf16 v[32:35], a[12:15], a[132:135], v[32:35]\n"
      "v_mfma_f32_16x16x32_bf16 v[32:35], a[16:19], a[144:147], v[32:35]\n"
      "v_mfma_f32_16x16x32_bf16 v[32:35], a[20:23], a[156:159], v[32:35]\n"
      "s_nop 1\n"
      "s_nop 5\n"
      "v_cvt_pk_bf16_f32 v32, v32, v33\n"
      "v_cvt_pk_bf16_f32 v33, v34, v35\n"
      "ds_write_addtid_b32 v32 offset:35840\n"
      "ds_write_addtid_b32 v33 offset:36096\n"
      "s_waitcnt lgkmcnt(0)\n"
      "s_barrier\n"
      "ds_read2_b64 a[0:3], v11 offset0:0 offset1:64\n"
      "ds_read2_b64 a[4:7], v11 offset0:1 offset1:65\n"
      "ds_read2_b64 a[8:11], v11 offset0:2 offset1:66\n"
      "ds_read2_b64 a[12:15], v11 offset0:3 offset1:67\n"
      "ds_read2_b64 a[16:19], v13 offset0:0 offset1:64\n"
      "ds_read2_b64 a[20:23], v13 offset0:1 offset1:65\n"
      "s_waitcnt lgkmcnt(5)\n"
      "v_mfma_f32_16x16x32_bf16 v[72:75], a[84:87], a[0:3], 0\n"
      "s_waitcnt lgkmcnt(4)\n"
      "v_mfma_f32_16x16x32_bf16 v[76:79], a[84:87], a[4:7], 0\n"
      "s_waitcnt lgkmcnt(3)\n"
      "v_mfma_f32_16x16x32_bf16 v[68:71], a[84:87], a[8:11], 0\n"
      "s_waitcnt lgkmcnt(2)\n"
      "s_nop 2\n"
      "v_cvt_pk_bf16_f32 v80, v72, v73\n"
      "v_cvt_pk_bf16_f32 v86, v74, v75\n"
      "v_mfma_f32_16x16x32_bf16 v[72:75], a[84:87], a[12:15], 0\n"
      "s_waitcnt lgkmcnt(1)\n"
      "v_cvt_pk_bf16_f32 v81, v76, v77\n"
      "v_cvt_pk_bf16_f32 v87, v78, v79\n"
      "v_cvt_pk_bf16_f32 v82, v68, v69\n"
      "v_cvt_pk_bf16_f32 v88, v70, v71\n"
      "v_mfma_f32_16x16x32_bf16 v[76:79], a[84:87], a[16:19], 0\n"
      "s_waitcnt lgkmcnt(0)\n"
      "v_mfma_f32_16x16x32_bf16 v[68:71], a[84:87], a[20:23], 0\n"
      "s_mov_b32 s52, s13\n"
      "v_cvt_pk_bf16_f32 v83, v72, v73\n"
      "v_cvt_pk_bf16_f32 v89, v74, v75\n"
      "s_add_u32 s13, s13, s12\n"
      "buffer_store_dwordx4 v[80:83], v14, s[36:39], s52 offen sc1\n"
      "buffer_store_dwordx4 v[86:89], v14, s[36:39], s52 offen offset:1536 sc1\n"
      "v_cvt_pk_bf16_f32 v84, v76, v77\n"
      "v_cvt_pk_bf16_f32 v90, v78, v79\n"
      "v_cvt_pk_bf16_f32 v85, v68, v69\n"
      "v_cvt_pk_bf16_f32 v91, v70, v71\n"
      "buffer_store_dwordx2 v[84:85], v15, s[36:39], s52 offen sc1\n"
      "buffer_store_dwordx2 v[90:91], v15, s[36:39], s52 offen offset:1536 sc1\n"
      "Lconv_u0:\n"
      "s_waitcnt vmcnt(0)\n"
      "s_endpgm\n"
      ".p2align 12\n"
      "Lconv_pa1:\n"
      "s_cbranch_execz Lconv_r0\n"
      "s_nop 0\n"
      "Lconv_loop1:\n"
      "v_mfma_f32_16x16x32_bf16 v[20:23], a[24:27], v[96:99], 0\n"
      "s_add_u32 s51, s51, s12\n"
      "v_cvt_pk_bf16_f32 v32, v32, v33\n"
      "v_cvt_pk_bf16_f32 v33, v34, v35\n"
      "v_mfma_f32_16x16x32_bf16 v[20:23], a[28:31], v[152:155], v[20:23]\n"
      "s_sub_u32 s58, s93, s58\n"
      "v_cvt_pk_bf16_f32 v84, v72, v73\n"
      "v_cvt_pk_bf16_f32 v90, v74, v75\n"
      "v_mfma_f32_16x16x32_bf16 v[20:23], a[32:35], a[220:223], v[20:23]\n"
      "s_sub_u32 s59, s94, s59\n"
      "v_cvt_pk_bf16_f32 v85, v76, v77\n"
      "v_cvt_pk_bf16_f32 v91, v78, v79\n"
      "v_mfma_f32_16x16x32_bf16 v[20:23], a[0:3], v[116:119], v[20:23]\n"
      "buffer_store_dwordx2 v[84:85], v15, s[36:39], s52 offen sc1\n"
      "v_mfma_f32_16x16x32_bf16 v[20:23], a[4:7], v[168:171], v[20:23]\n"
      "s_sub_u32 s47, s51, s48\n"
      "ds_write_addtid_b32 v32 offset:34816\n"
      "v_mfma_f32_16x16x32_bf16 v[20:23], a[8:11], v[224:227], v[20:23]\n"
      "s_cmp_ge_u32 vcc_lo, 0x80000000\n"
      "ds_write_addtid_b32 v33 offset:35072\n"
      "v_mfma_f32_16x16x32_bf16 v[24:27], a[24:27], v[104:107], 0\n"
      "buffer_store_dwordx2 v[90:91], v15, s[36:39], s52 offen offset:1536 sc1\n"
      "v_mfma_f32_16x16x32_bf16 v[24:27], a[28:31], v[156:159], v[24:27]\n"
      "s_cselect_b32 s53, s47, s51\n"
      "v_mfma_f32_16x16x32_bf16 v[24:27], a[32:35], v[212:215], v[24:27]\n"
      "s_mov_b32 s52, s13\n"
      "v_mfma_f32_16x16x32_bf16 v[24:27], a[0:3], v[120:123], v[24:27]\n"
      "s_add_u32 s13, s13, s12\n"
      "v_mfma_f32_16x16x32_bf16 v[24:27], a[4:7], v[176:179], v[24:27]\n"
      "s_waitcnt vmcnt(17) lgkmcnt(0)\n"
      "s_barrier\n"
      "v_mfma_f32_16x16x32_bf16 v[24:27], a[8:11], a[232:235], v[24:27]\n"
      "s_mov_b32 m0, s58\n"
      "ds_read2_b64 a[60:63], v10 offset0:0 offset1:64\n"
      "v_mfma_f32_16x16x32_bf16 v[28:31], a[24:27], v[108:111], 0\n"
      "ds_read_b128 a[12:15], v7 offset:6144\n"
      "v_mfma_f32_16x16x32_bf16 v[28:31], a[28:31], v[164:167], v[28:31]\n"
      "ds_read_b128 a[16:19], v7 offset:6400\n"
      "v_mfma_f32_16x16x32_bf16 v[28:31], a[32:35], a[224:227], v[28:31]\n"
      "ds_read_b128 a[20:23], v7 offset:6656\n"
      "v_mfma_f32_16x16x32_bf16 v[28:31], a[0:3], v[128:131], v[28:31]\n"
      "ds_read_b128 a[48:51], v2 offset:13280\n"
      "v_mfma_f32_16x16x32_bf16 v[28:31], a[4:7], a[228:231], v[28:31]\n"
      "ds_read_b128 a[56:59], v4 offset:13280\n"
      "v_mfma_f32_16x16x32_bf16 v[28:31], a[8:11], v[236:239], v[28:31]\n"
      "s_waitcnt lgkmcnt(5)\n"
      "ds_read_b128 a[52:55], v3 offset:13280\n"
      "v_mfma_f32_16x16x32_bf16 v[68:71], a[84:87], a[60:63], 0\n"
      "s_waitcnt lgkmcnt(5)\n"
      "ds_read_b128 a[24:27], v6 offset:5376\n"
      "v_mfma_f32_16x16x32_bf16 v[20:23], a[12:15], v[132:135], v[20:23]\n"
      "s_waitcnt lgkmcnt(5)\n"
      "ds_read_b128 a[28:31], v6 offset:5632\n"
      "v_mfma_f32_16x16x32_bf16 v[20:23], a[16:19], v[188:191], v[20:23]\n"
      "s_waitcnt lgkmcnt(5)\n"
      "ds_read_b128 a[32:35], v6 offset:5888\n"
      "v_cvt_pk_bf16_f32 v80, v68, v69\n"
      "v_mfma_f32_16x16x32_bf16 v[20:23], a[20:23], a[240:243], v[20:23]\n"
      "ds_read_b128 a[0:3], v7 offset:768\n"
      "v_cvt_pk_bf16_f32 v86, v70, v71\n"
      "v_mfma_f32_16x16x32_bf16 v[24:27], a[12:15], v[140:143], v[24:27]\n"
      "ds_read_b128 a[4:7], v7 offset:1024\n"
      "s_nop 0\n"
      "v_mfma_f32_16x16x32_bf16 v[24:27], a[16:19], a[236:239], v[24:27]\n"
      "ds_read_b128 a[8:11], v7 offset:1280\n"
      "v_mfma_f32_16x16x32_bf16 v[24:27], a[20:23], v[248:251], v[24:27]\n"
      "s_waitcnt lgkmcnt(7)\n"
      "ds_read2_b64 a[64:67], v10 offset0:1 offset1:65\n"
      "v_cvt_pk_bf16_f32 v20, v20, v21\n"
      "v_mfma_f32_16x16x32_bf16 v[52:55], a[48:51], a[72:75], 0\n"
      "buffer_load_dwordx4 v16, s[32:35], s53 offen lds\n"
      "v_cvt_pk_bf16_f32 v21, v22, v23\n"
      "v_mfma_f32_16x16x32_bf16 v[60:63], a[56:59], a[80:83], v[52:55]\n"
      "s_mov_b32 m0, s45\n"
      "ds_read_b128 a[36:39], v2 offset:13344\n"
      "v_cvt_pk_bf16_f32 v24, v24, v25\n"
      "v_mfma_f32_16x16x32_bf16 v[28:31], a[12:15], v[144:147], v[28:31]\n"
      "ds_read_b128 a[12:15], v7 offset:5376\n"
      "v_cvt_pk_bf16_f32 v25, v26, v27\n"
      "v_mfma_f32_16x16x32_bf16 v[28:31], a[16:19], v[200:203], v[28:31]\n"
      "ds_read_b128 a[16:19], v7 offset:5632\n"
      "v_cvt_pk_bf16_f32 v60, v60, v61\n"
      "v_mfma_f32_16x16x32_bf16 v[28:31], a[20:23], a[244:247], v[28:31]\n"
      "s_waitcnt lgkmcnt(6)\n"
      "ds_read_b128 a[20:23], v7 offset:5888\n"
      "v_cvt_pk_bf16_f32 v61, v62, v63\n"
      "v_mfma_f32_16x16x32_bf16 v[56:59], a[52:55], a[76:79], 0\n"
      "ds_read_b128 a[40:43], v3 offset:13344\n"
      "s_nop 0\n"
      "v_mfma_f32_16x16x32_bf16 v[32:35], a[24:27], v[92:95], 0\n"
      "ds_write_addtid_b32 v20 offset:62480\n"
      "v_cvt_pk_bf16_f32 v28, v28, v29\n"
      "v_mfma_f32_16x16x32_bf16 v[32:35], a[28:31], v[148:151], v[32:35]\n"
      "ds_read_b128 a[44:47], v4 offset:13344\n"
      "v_cvt_pk_bf16_f32 v29, v30, v31\n"
      "v_mfma_f32_16x16x32_bf16 v[32:35], a[32:35], v[204:207], v[32:35]\n"
      "ds_write_addtid_b32 v21 offset:62736\n"
      "v_fma_f32 v64, s44, v52, v56\n"
      "v_mfma_f32_16x16x32_bf16 v[32:35], a[0:3], v[112:115], v[32:35]\n"
      "s_waitcnt lgkmcnt(7)\n"
      "ds_read_b128 a[48:51], v2 offset:13408\n"
      "v_fma_f32 v65, s44, v53, v57\n"
      "v_mfma_f32_16x16x32_bf16 v[32:35], a[4:7], a[208:211], v[32:35]\n"
      "ds_read_b128 a[56:59], v4 offset:13408\n"
      "v_fma_f32 v66, s44, v54, v58\n"
      "v_mfma_f32_16x16x32_bf16 v[32:35], a[8:11], v[220:223], v[32:35]\n"
      "s_waitcnt lgkmcnt(7)\n"
      "ds_write_addtid_b32 v24 offset:50184\n"
      "v_fma_f32 v67, s44, v55, v59\n"
      "v_mfma_f32_16x16x32_bf16 v[32:35], a[12:15], a[212:215], v[32:35]\n"
      "ds_read2_b64 a[68:71], v10 offset0:2 offset1:66\n"
      "v_fma_f32 v56, s44, v52, -v56\n"
      "v_mfma_f32_16x16x32_bf16 v[32:35], a[16:19], v[184:187], v[32:35]\n"
      "s_waitcnt lgkmcnt(7)\n"
      "ds_write_addtid_b32 v25 offset:50440\n"
      "v_fma_f32 v57, s44, v53, -v57\n"
      "v_mfma_f32_16x16x32_bf16 v[32:35], a[20:23], v[240:243], v[32:35]\n"
      "ds_write_addtid_b32 v28 offset:37888\n"
      "v_fma_f32 v58, s44, v54, -v58\n"
      "v_mfma_f32_16x16x32_bf16 v[72:75], a[84:87], a[64:67], 0\n"
      "ds_write_addtid_b32 v29 offset:38144\n"
      "v_fma_f32 v59, s44, v55, -v59\n"
      "v_mfma_f32_16x16x32_bf16 v[20:23], a[24:27], v[100:103], 0\n"
      "s_mov_b32 m0, s18\n"
      "v_cvt_pk_bf16_f32 v64, v64, v65\n"
      "v_cvt_pk_bf16_f32 v65, v66, v67\n"
      "v_mfma_f32_16x16x32_bf16 v[20:23], a[28:31], a[200:203], v[20:23]\n"
      "ds_write_addtid_b32 v64 offset:768\n"
      "v_cvt_pk_bf16_f32 v56, v56, v57\n"
      "v_mfma_f32_16x16x32_bf16 v[20:23], a[32:35], v[208:211], v[20:23]\n"
      "ds_write_addtid_b32 v65 offset:37696\n"
      "v_cvt_pk_bf16_f32 v57, v58, v59\n"
      "v_mfma_f32_16x16x32_bf16 v[20:23], a[0:3], a[204:207], v[20:23]\n"
      "ds_write_addtid_b32 v56 offset:1536\n"
      "v_cvt_pk_bf16_f32 v32, v32, v33\n"
      "v_mfma_f32_16x16x32_bf16 v[20:23], a[4:7], v[172:175], v[20:23]\n"
      "ds_write_addtid_b32 v57 offset:38464\n"
      "v_cvt_pk_bf16_f32 v33, v34, v35\n"
      "v_mfma_f32_16x16x32_bf16 v[20:23], a[8:11], v[228:231], v[20:23]\n"
      "ds_write_addtid_b32 v60 offset:0\n"
      "v_cvt_pk_bf16_f32 v81, v72, v73\n"
      "v_mfma_f32_16x16x32_bf16 v[20:23], a[12:15], v[136:139], v[20:23]\n"
      "ds_write_addtid_b32 v61 offset:36928\n"
      "v_cvt_pk_bf16_f32 v87, v74, v75\n"
      "v_mfma_f32_16x16x32_bf16 v[20:23], a[16:19], v[192:195], v[20:23]\n"
      "s_mov_b32 m0, s45\n"
      "ds_read_b128 a[52:55], v3 offset:13408\n"
      "v_add_u32_e64 v2, s91, v2\n"
      "v_mfma_f32_16x16x32_bf16 v[20:23], a[20:23], v[244:247], v[20:23]\n"
      "s_mov_b32 m0, s58\n"
      "ds_read2_b64 a[60:63], v10 offset0:3 offset1:67\n"
      "v_add_u32_e32 v4, s91, v4\n"
      "v_mfma_f32_16x16x32_bf16 v[36:39], a[36:39], a[72:75], 0\n"
      "buffer_load_dwordx4 v17, s[32:35], s53 offen offset:1024 lds\n"
      "v_add_u32_e64 v3, s91, v3\n"
      "v_mfma_f32_16x16x32_bf16 v[40:43], a[40:43], a[76:79], 0\n"
      "s_waitcnt lgkmcnt(11)\n"
      "ds_read2_b64 a[64:67], v12 offset0:0 offset1:64\n"
      "v_cvt_pk_bf16_f32 v20, v20, v21\n"
      "v_mfma_f32_16x16x32_bf16 v[44:47], a[44:47], a[80:83], v[36:39]\n"
      "s_mov_b32 m0, s45\n"
      "v_cvt_pk_bf16_f32 v21, v22, v23\n"
      "s_nop 0\n"
      "v_mfma_f32_16x16x32_bf16 v[52:55], a[48:51], a[72:75], 0\n"
      "s_sub_u32 s91, 0, s91\n"
      "ds_write_addtid_b32 v32 offset:61456\n"
      "v_fma_f32 v48, s44, v36, v40\n"
      "v_mfma_f32_16x16x32_bf16 v[60:63], a[56:59], a[80:83], v[52:55]\n"
      "ds_write_addtid_b32 v33 offset:61712\n"
      "v_fma_f32 v49, s44, v37, v41\n"
      "v_mfma_f32_16x16x32_bf16 v[76:79], a[84:87], a[68:71], 0\n"
      "ds_write_addtid_b32 v20 offset:49160\n"
      "v_fma_f32 v50, s44, v38, v42\n"
      "v_mfma_f32_16x16x32_bf16 v[24:27], a[24:27], a[196:199], 0\n"
      "ds_read_b128 a[24:27], v7 offset:4608\n"
      "v_fma_f32 v51, s44, v39, v43\n"
      "v_mfma_f32_16x16x32_bf16 v[24:27], a[28:31], v[160:163], v[24:27]\n"
      "ds_read_b128 a[28:31], v7 offset:4864\n"
      "v_fma_f32 v40, s44, v36, -v40\n"
      "v_mfma_f32_16x16x32_bf16 v[24:27], a[32:35], v[216:219], v[24:27]\n"
      "ds_read_b128 a[32:35], v7 offset:5120\n"
      "v_fma_f32 v41, s44, v37, -v41\n"
      "v_mfma_f32_16x16x32_bf16 v[24:27], a[0:3], v[124:127], v[24:27]\n"
      "ds_read_b128 a[0:3], v6 offset:4608\n"
      "v_fma_f32 v42, s44, v38, -v42\n"
      "v_mfma_f32_16x16x32_bf16 v[24:27], a[4:7], v[180:183], v[24:27]\n"
      "ds_read_b128 a[4:7], v6 offset:4864\n"
      "v_fma_f32 v43, s44, v39, -v43\n"
      "v_mfma_f32_16x16x32_bf16 v[24:27], a[8:11], v[232:235], v[24:27]\n"
      "ds_read_b128 a[8:11], v6 offset:5120\n"
      "v_cvt_pk_bf16_f32 v48, v48, v49\n"
      "v_mfma_f32_16x16x32_bf16 v[24:27], a[12:15], a[216:219], v[24:27]\n"
      "ds_read_b128 a[12:15], v7 offset:0\n"
      "v_cvt_pk_bf16_f32 v49, v50, v51\n"
      "v_mfma_f32_16x16x32_bf16 v[24:27], a[16:19], v[196:199], v[24:27]\n"
      "ds_read_b128 a[16:19], v7 offset:256\n"
      "v_cvt_pk_bf16_f32 v40, v40, v41\n"
      "v_mfma_f32_16x16x32_bf16 v[24:27], a[20:23], v[252:255], v[24:27]\n"
      "s_waitcnt lgkmcnt(7)\n"
      "ds_read_b128 a[20:23], v7 offset:512\n"
      "v_cvt_pk_bf16_f32 v41, v42, v43\n"
      "v_mfma_f32_16x16x32_bf16 v[56:59], a[52:55], a[76:79], 0\n"
      "ds_write_addtid_b32 v21 offset:49416\n"
      "v_cvt_pk_bf16_f32 v44, v44, v45\n"
      "v_mfma_f32_16x16x32_bf16 v[28:31], a[24:27], a[160:163], 0\n"
      "s_waitcnt lgkmcnt(7)\n"
      "v_cvt_pk_bf16_f32 v45, v46, v47\n"
      "v_cvt_pk_bf16_f32 v24, v24, v25\n"
      "v_mfma_f32_16x16x32_bf16 v[28:31], a[28:31], a[172:175], v[28:31]\n"
      "s_mov_b32 m0, s18\n"
      "v_cvt_pk_bf16_f32 v25, v26, v27\n"
      "v_fma_f32 v64, s44, v52, v56\n"
      "v_mfma_f32_16x16x32_bf16 v[28:31], a[32:35], a[184:187], v[28:31]\n"
      "s_waitcnt lgkmcnt(5)\n"
      "ds_write_addtid_b32 v48 offset:1024\n"
      "v_fma_f32 v65, s44, v53, v57\n"
      "v_mfma_f32_16x16x32_bf16 v[28:31], a[0:3], a[88:91], v[28:31]\n"
      "ds_write_addtid_b32 v49 offset:37952\n"
      "v_cvt_pk_bf16_f32 v64, v64, v65\n"
      "v_mfma_f32_16x16x32_bf16 v[28:31], a[4:7], a[100:103], v[28:31]\n"
      "s_waitcnt lgkmcnt(5)\n"
      "ds_write_addtid_b32 v40 offset:1792\n"
      "v_fma_f32 v66, s44, v54, v58\n"
      "v_mfma_f32_16x16x32_bf16 v[28:31], a[8:11], a[112:115], v[28:31]\n"
      "ds_write_addtid_b32 v41 offset:38720\n"
      "v_fma_f32 v67, s44, v55, v59\n"
      "v_mfma_f32_16x16x32_bf16 v[28:31], a[12:15], a[124:127], v[28:31]\n"
      "s_waitcnt lgkmcnt(5)\n"
      "ds_write_addtid_b32 v44 offset:256\n"
      "v_cvt_pk_bf16_f32 v65, v66, v67\n"
      "v_mfma_f32_16x16x32_bf16 v[28:31], a[16:19], a[136:139], v[28:31]\n"
      "ds_write_addtid_b32 v45 offset:37184\n"
      "v_fma_f32 v56, s44, v52, -v56\n"
      "v_mfma_f32_16x16x32_bf16 v[28:31], a[20:23], a[148:151], v[28:31]\n"
      "s_mov_b32 m0, s45\n"
      "v_fma_f32 v57, s44, v53, -v57\n"
      "v_cvt_pk_bf16_f32 v56, v56, v57\n"
      "v_mfma_f32_16x16x32_bf16 v[20:23], a[24:27], a[164:167], 0\n"
      "s_mov_b32 m0, s58\n"
      "v_fma_f32 v58, s44, v54, -v58\n"
      "v_fma_f32 v59, s44, v55, -v59\n"
      "v_mfma_f32_16x16x32_bf16 v[20:23], a[28:31], a[176:179], v[20:23]\n"
      "buffer_load_dwordx4 v18, s[32:35], s53 offen offset:2048 lds\n"
      "v_cvt_pk_bf16_f32 v57, v58, v59\n"
      "v_mfma_f32_16x16x32_bf16 v[20:23], a[32:35], a[188:191], v[20:23]\n"
      "s_mov_b32 m0, s45\n"
      "ds_read2_b64 a[68:71], v12 offset0:1 offset1:65\n"
      "v_cvt_pk_bf16_f32 v60, v60, v61\n"
      "v_mfma_f32_16x16x32_bf16 v[20:23], a[0:3], a[92:95], v[20:23]\n"
      "ds_write_addtid_b32 v24 offset:36864\n"
      "v_cvt_pk_bf16_f32 v61, v62, v63\n"
      "v_mfma_f32_16x16x32_bf16 v[20:23], a[4:7], a[104:107], v[20:23]\n"
      "ds_write_addtid_b32 v25 offset:37120\n"
      "v_cvt_pk_bf16_f32 v28, v28, v29\n"
      "v_mfma_f32_16x16x32_bf16 v[20:23], a[8:11], a[116:119], v[20:23]\n"
      "s_mov_b32 m0, s18\n"
      "v_cvt_pk_bf16_f32 v29, v30, v31\n"
      "v_cvt_pk_bf16_f32 v82, v76, v77\n"
      "v_mfma_f32_16x16x32_bf16 v[20:23], a[12:15], a[128:131], v[20:23]\n"
      "ds_write_addtid_b32 v64 offset:1280\n"
      "v_cvt_pk_bf16_f32 v88, v78, v79\n"
      "v_mfma_f32_16x16x32_bf16 v[20:23], a[16:19], a[140:143], v[20:23]\n"
      "ds_write_addtid_b32 v65 offset:38208\n"
      "v_swap_b32 v6, v7\n"
      "v_mfma_f32_16x16x32_bf16 v[20:23], a[20:23], a[152:155], v[20:23]\n"
      "ds_write_addtid_b32 v56 offset:2048\n"
      "s_nop 0\n"
      "v_mfma_f32_16x16x32_bf16 v[68:71], a[84:87], a[60:63], 0\n"
      "ds_write_addtid_b32 v57 offset:38976\n"
      "s_nop 0\n"
      "v_mfma_f32_16x16x32_bf16 v[32:35], a[24:27], a[168:171], 0\n"
      "ds_write_addtid_b32 v60 offset:512\n"
      "v_mfma_f32_16x16x32_bf16 v[32:35], a[28:31], a[180:183], v[32:35]\n"
      "ds_write_addtid_b32 v61 offset:37440\n"
      "v_cvt_pk_bf16_f32 v20, v20, v21\n"
      "v_mfma_f32_16x16x32_bf16 v[32:35], a[32:35], a[192:195], v[32:35]\n"
      "s_mov_b32 m0, s45\n"
      "v_cvt_pk_bf16_f32 v83, v68, v69\n"
      "v_cvt_pk_bf16_f32 v89, v70, v71\n"
      "v_mfma_f32_16x16x32_bf16 v[32:35], a[0:3], a[96:99], v[32:35]\n"
      "ds_write_addtid_b32 v28 offset:60432\n"
      "v_cvt_pk_bf16_f32 v21, v22, v23\n"
      "v_mfma_f32_16x16x32_bf16 v[32:35], a[4:7], a[108:111], v[32:35]\n"
      "ds_write_addtid_b32 v29 offset:60688\n"
      "v_mfma_f32_16x16x32_bf16 v[32:35], a[8:11], a[120:123], v[32:35]\n"
      "buffer_store_dwordx4 v[80:83], v14, s[36:39], s52 offen sc1\n"
      "v_mfma_f32_16x16x32_bf16 v[32:35], a[12:15], a[132:135], v[32:35]\n"
      "ds_write_addtid_b32 v20 offset:48136\n"
      "v_mfma_f32_16x16x32_bf16 v[32:35], a[16:19], a[144:147], v[32:35]\n"
      "ds_write_addtid_b32 v21 offset:48392\n"
      "v_mfma_f32_16x16x32_bf16 v[32:35], a[20:23], a[156:159], v[32:35]\n"
      "buffer_store_dwordx4 v[86:89], v14, s[36:39], s52 offen offset:1536 sc1\n"
      "s_nop 0\n"
      "v_mfma_f32_16x16x32_bf16 v[72:75], a[84:87], a[64:67], 0\n"
      "s_waitcnt lgkmcnt(12)\n"
      "s_nop 1\n"
      "v_mfma_f32_16x16x32_bf16 v[76:79], a[84:87], a[68:71], 0\n"
      "v_mfma_f32_16x16x32_bf16 v[20:23], a[12:15], a[88:91], 0\n"
      "s_add_u32 s51, s51, s12\n"
      "v_cvt_pk_bf16_f32 v32, v32, v33\n"
      "v_cvt_pk_bf16_f32 v33, v34, v35\n"
      "v_mfma_f32_16x16x32_bf16 v[20:23], a[16:19], a[100:103], v[20:23]\n"
      "s_sub_u32 s47, s51, s48\n"
      "v_cvt_pk_bf16_f32 v84, v72, v73\n"
      "v_cvt_pk_bf16_f32 v90, v74, v75\n"
      "v_mfma_f32_16x16x32_bf16 v[20:23], a[20:23], a[112:115], v[20:23]\n"
      "s_cmp_ge_u32 vcc_lo, 0x40000000\n"
      "v_cvt_pk_bf16_f32 v85, v76, v77\n"
      "v_cvt_pk_bf16_f32 v91, v78, v79\n"
      "v_mfma_f32_16x16x32_bf16 v[20:23], a[24:27], a[124:127], v[20:23]\n"
      "buffer_store_dwordx2 v[84:85], v15, s[36:39], s52 offen sc1\n"
      "v_mfma_f32_16x16x32_bf16 v[20:23], a[28:31], a[136:139], v[20:23]\n"
      "s_cselect_b32 s53, s47, s51\n"
      "ds_write_addtid_b32 v32 offset:35840\n"
      "v_mfma_f32_16x16x32_bf16 v[20:23], a[32:35], a[148:151], v[20:23]\n"
      "s_mul_i32 vcc_lo, vcc_lo, 2\n"
      "ds_write_addtid_b32 v33 offset:36096\n"
      "v_mfma_f32_16x16x32_bf16 v[24:27], a[12:15], a[92:95], 0\n"
      "buffer_store_dwordx2 v[90:91], v15, s[36:39], s52 offen offset:1536 sc1\n"
      "v_mfma_f32_16x16x32_bf16 v[24:27], a[16:19], a[104:107], v[24:27]\n"
      "s_mov_b32 s52, s13\n"
      "v_mfma_f32_16x16x32_bf16 v[24:27], a[20:23], a[116:119], v[24:27]\n"
      "v_mfma_f32_16x16x32_bf16 v[24:27], a[24:27], a[128:131], v[24:27]\n"
      "v_mfma_f32_16x16x32_bf16 v[24:27], a[28:31], a[140:143], v[24:27]\n"
      "s_waitcnt vmcnt(19) lgkmcnt(0)\n"
      "s_barrier\n"
      "v_mfma_f32_16x16x32_bf16 v[24:27], a[32:35], a[152:155], v[24:27]\n"
      "s_add_u32 m0, s58, 13280\n"
      "ds_read2_b64 a[60:63], v11 offset0:0 offset1:64\n"
      "v_mfma_f32_16x16x32_bf16 v[28:31], a[12:15], a[96:99], 0\n"
      "s_add_u32 s13, s13, s12\n"
      "ds_read_b128 a[0:3], v7 offset:0\n"
      "v_mfma_f32_16x16x32_bf16 v[28:31], a[16:19], a[108:111], v[28:31]\n"
      "ds_read_b128 a[4:7], v7 offset:256\n"
      "v_mfma_f32_16x16x32_bf16 v[28:31], a[20:23], a[120:123], v[28:31]\n"
      "ds_read_b128 a[8:11], v7 offset:512\n"
      "v_mfma_f32_16x16x32_bf16 v[28:31], a[24:27], a[132:135], v[28:31]\n"
      "ds_read_b128 a[36:39], v2 offset:0\n"
      "v_mfma_f32_16x16x32_bf16 v[28:31], a[28:31], a[144:147], v[28:31]\n"
      "ds_read_b128 a[44:47], v4 offset:0\n"
      "v_mfma_f32_16x16x32_bf16 v[28:31], a[32:35], a[156:159], v[28:31]\n"
      "s_waitcnt lgkmcnt(5)\n"
      "ds_read_b128 a[40:43], v3 offset:0\n"
      "v_mfma_f32_16x16x32_bf16 v[68:71], a[84:87], a[60:63], 0\n"
      "s_waitcnt lgkmcnt(5)\n"
      "ds_read_b128 a[12:15], v6 offset:768\n"
      "v_mfma_f32_16x16x32_bf16 v[20:23], a[0:3], a[160:163], v[20:23]\n"
      "s_waitcnt lgkmcnt(5)\n"
      "ds_read_b128 a[16:19], v6 offset:1024\n"
      "v_mfma_f32_16x16x32_bf16 v[20:23], a[4:7], a[172:175], v[20:23]\n"
      "s_waitcnt lgkmcnt(5)\n"
      "ds_read_b128 a[20:23], v6 offset:1280\n"
      "v_cvt_pk_bf16_f32 v80, v68, v69\n"
      "v_mfma_f32_16x16x32_bf16 v[20:23], a[8:11], a[184:187], v[20:23]\n"
      "ds_read_b128 a[24:27], v6 offset:5376\n"
      "v_cvt_pk_bf16_f32 v86, v70, v71\n"
      "v_mfma_f32_16x16x32_bf16 v[24:27], a[0:3], a[164:167], v[24:27]\n"
      "ds_read_b128 a[28:31], v6 offset:5632\n"
      "s_nop 0\n"
      "v_mfma_f32_16x16x32_bf16 v[24:27], a[4:7], a[176:179], v[24:27]\n"
      "ds_read_b128 a[32:35], v6 offset:5888\n"
      "v_mfma_f32_16x16x32_bf16 v[24:27], a[8:11], a[188:191], v[24:27]\n"
      "s_waitcnt lgkmcnt(7)\n"
      "ds_read2_b64 a[64:67], v11 offset0:1 offset1:65\n"
      "v_cvt_pk_bf16_f32 v20, v20, v21\n"
      "v_mfma_f32_16x16x32_bf16 v[36:39], a[36:39], a[72:75], 0\n"
      "buffer_load_dwordx4 v16, s[32:35], s53 offen lds\n"
      "v_cvt_pk_bf16_f32 v21, v22, v23\n"
      "v_mfma_f32_16x16x32_bf16 v[44:47], a[44:47], a[80:83], v[36:39]\n"
      "s_mov_b32 m0, s45\n"
      "ds_read_b128 a[48:51], v2 offset:64\n"
      "v_cvt_pk_bf16_f32 v24, v24, v25\n"
      "v_mfma_f32_16x16x32_bf16 v[28:31], a[0:3], a[168:171], v[28:31]\n"
      "ds_read_b128 a[0:3], v7 offset:768\n"
      "v_cvt_pk_bf16_f32 v25, v26, v27\n"
      "v_mfma_f32_16x16x32_bf16 v[28:31], a[4:7], a[180:183], v[28:31]\n"
      "ds_read_b128 a[4:7], v7 offset:1024\n"
      "v_cvt_pk_bf16_f32 v44, v44, v45\n"
      "v_mfma_f32_16x16x32_bf16 v[28:31], a[8:11], a[192:195], v[28:31]\n"
      "s_waitcnt lgkmcnt(6)\n"
      "ds_read_b128 a[8:11], v7 offset:1280\n"
      "v_cvt_pk_bf16_f32 v45, v46, v47\n"
      "v_mfma_f32_16x16x32_bf16 v[40:43], a[40:43], a[76:79], 0\n"
      "ds_read_b128 a[52:55], v3 offset:64\n"
      "s_nop 0\n"
      "v_mfma_f32_16x16x32_bf16 v[32:35], a[12:15], v[92:95], 0\n"
      "ds_write_addtid_b32 v20 offset:57360\n"
      "v_cvt_pk_bf16_f32 v28, v28, v29\n"
      "v_mfma_f32_16x16x32_bf16 v[32:35], a[16:19], v[148:151], v[32:35]\n"
      "ds_read_b128 a[56:59], v4 offset:64\n"
      "v_cvt_pk_bf16_f32 v29, v30, v31\n"
      "v_mfma_f32_16x16x32_bf16 v[32:35], a[20:23], v[204:207], v[32:35]\n"
      "ds_write_addtid_b32 v21 offset:57616\n"
      "v_fma_f32 v48, s44, v36, v40\n"
      "v_mfma_f32_16x16x32_bf16 v[32:35], a[24:27], v[112:115], v[32:35]\n"
      "s_waitcnt lgkmcnt(7)\n"
      "ds_read_b128 a[36:39], v2 offset:128\n"
      "v_fma_f32 v49, s44, v37, v41\n"
      "v_mfma_f32_16x16x32_bf16 v[32:35], a[28:31], a[208:211], v[32:35]\n"
      "ds_read_b128 a[44:47], v4 offset:128\n"
      "v_fma_f32 v50, s44, v38, v42\n"
      "v_mfma_f32_16x16x32_bf16 v[32:35], a[32:35], v[220:223], v[32:35]\n"
      "s_waitcnt lgkmcnt(7)\n"
      "ds_write_addtid_b32 v24 offset:45064\n"
      "v_fma_f32 v51, s44, v39, v43\n"
      "v_mfma_f32_16x16x32_bf16 v[32:35], a[0:3], a[212:215], v[32:35]\n"
      "ds_read2_b64 a[68:71], v11 offset0:2 offset1:66\n"
      "v_fma_f32 v40, s44, v36, -v40\n"
      "v_mfma_f32_16x16x32_bf16 v[32:35], a[4:7], v[184:187], v[32:35]\n"
      "s_waitcnt lgkmcnt(7)\n"
      "ds_write_addtid_b32 v25 offset:45320\n"
      "v_fma_f32 v41, s44, v37, -v41\n"
      "v_mfma_f32_16x16x32_bf16 v[32:35], a[8:11], v[240:243], v[32:35]\n"
      "ds_write_addtid_b32 v28 offset:32768\n"
      "v_fma_f32 v42, s44, v38, -v42\n"
      "v_mfma_f32_16x16x32_bf16 v[72:75], a[84:87], a[64:67], 0\n"
      "ds_write_addtid_b32 v29 offset:33024\n"
      "v_fma_f32 v43, s44, v39, -v43\n"
      "v_mfma_f32_16x16x32_bf16 v[20:23], a[12:15], v[100:103], 0\n"
      "s_mov_b32 m0, s18\n"
      "v_cvt_pk_bf16_f32 v48, v48, v49\n"
      "v_cvt_pk_bf16_f32 v49, v50, v51\n"
      "v_mfma_f32_16x16x32_bf16 v[20:23], a[16:19], a[200:203], v[20:23]\n"
      "ds_write_addtid_b32 v48 offset:5376\n"
      "v_cvt_pk_bf16_f32 v40, v40, v41\n"
      "v_mfma_f32_16x16x32_bf16 v[20:23], a[20:23], v[208:211], v[20:23]\n"
      "ds_write_addtid_b32 v49 offset:42304\n"
      "v_cvt_pk_bf16_f32 v41, v42, v43\n"
      "v_mfma_f32_16x16x32_bf16 v[20:23], a[24:27], a[204:207], v[20:23]\n"
      "ds_write_addtid_b32 v40 offset:6144\n"
      "v_cvt_pk_bf16_f32 v32, v32, v33\n"
      "v_mfma_f32_16x16x32_bf16 v[20:23], a[28:31], v[172:175], v[20:23]\n"
      "ds_write_addtid_b32 v41 offset:43072\n"
      "v_cvt_pk_bf16_f32 v33, v34, v35\n"
      "v_mfma_f32_16x16x32_bf16 v[20:23], a[32:35], v[228:231], v[20:23]\n"
      "ds_write_addtid_b32 v44 offset:4608\n"
      "v_cvt_pk_bf16_f32 v81, v72, v73\n"
      "v_mfma_f32_16x16x32_bf16 v[20:23], a[0:3], v[136:139], v[20:23]\n"
      "ds_write_addtid_b32 v45 offset:41536\n"
      "v_cvt_pk_bf16_f32 v87, v74, v75\n"
      "v_mfma_f32_16x16x32_bf16 v[20:23], a[4:7], v[192:195], v[20:23]\n"
      "s_mov_b32 m0, s45\n"
      "ds_read_b128 a[40:43], v3 offset:128\n"
      "v_mfma_f32_16x16x32_bf16 v[20:23], a[8:11], v[244:247], v[20:23]\n"
      "s_add_u32 m0, s58, 13280\n"
      "ds_read2_b64 a[60:63], v11 offset0:3 offset1:67\n"
      "s_nop 0\n"
      "v_mfma_f32_16x16x32_bf16 v[52:55], a[48:51], a[72:75], 0\n"
      "buffer_load_dwordx4 v17, s[32:35], s53 offen offset:1024 lds\n"
      "s_nop 0\n"
      "v_mfma_f32_16x16x32_bf16 v[56:59], a[52:55], a[76:79], 0\n"
      "s_waitcnt lgkmcnt(11)\n"
      "ds_read2_b64 a[64:67], v13 offset0:0 offset1:64\n"
      "v_cvt_pk_bf16_f32 v20, v20, v21\n"
      "v_mfma_f32_16x16x32_bf16 v[60:63], a[56:59], a[80:83], v[52:55]\n"
      "s_mov_b32 m0, s45\n"
      "v_cvt_pk_bf16_f32 v21, v22, v23\n"
      "s_nop 0\n"
      "v_mfma_f32_16x16x32_bf16 v[36:39], a[36:39], a[72:75], 0\n"
      "ds_write_addtid_b32 v32 offset:58384\n"
      "v_fma_f32 v64, s44, v52, v56\n"
      "v_mfma_f32_16x16x32_bf16 v[44:47], a[44:47], a[80:83], v[36:39]\n"
      "ds_write_addtid_b32 v33 offset:58640\n"
      "v_fma_f32 v65, s44, v53, v57\n"
      "v_mfma_f32_16x16x32_bf16 v[76:79], a[84:87], a[68:71], 0\n"
      "global_load_dword v8, v9, s[54:55]\n"
      "v_fma_f32 v66, s44, v54, v58\n"
      "v_mfma_f32_16x16x32_bf16 v[24:27], a[12:15], a[196:199], 0\n"
      "ds_read_b128 a[12:15], v6 offset:1536\n"
      "v_fma_f32 v67, s44, v55, v59\n"
      "v_mfma_f32_16x16x32_bf16 v[24:27], a[16:19], v[160:163], v[24:27]\n"
      "ds_read_b128 a[16:19], v6 offset:1792\n"
      "v_fma_f32 v56, s44, v52, -v56\n"
      "v_mfma_f32_16x16x32_bf16 v[24:27], a[20:23], v[216:219], v[24:27]\n"
      "ds_read_b128 a[20:23], v6 offset:2048\n"
      "v_fma_f32 v57, s44, v53, -v57\n"
      "v_mfma_f32_16x16x32_bf16 v[24:27], a[24:27], v[124:127], v[24:27]\n"
      "ds_read_b128 a[24:27], v6 offset:6144\n"
      "v_fma_f32 v58, s44, v54, -v58\n"
      "v_mfma_f32_16x16x32_bf16 v[24:27], a[28:31], v[180:183], v[24:27]\n"
      "ds_read_b128 a[28:31], v6 offset:6400\n"
      "v_fma_f32 v59, s44, v55, -v59\n"
      "v_mfma_f32_16x16x32_bf16 v[24:27], a[32:35], v[232:235], v[24:27]\n"
      "ds_read_b128 a[32:35], v6 offset:6656\n"
      "v_cvt_pk_bf16_f32 v64, v64, v65\n"
      "v_mfma_f32_16x16x32_bf16 v[24:27], a[0:3], a[216:219], v[24:27]\n"
      "ds_read_b128 a[0:3], v7 offset:1536\n"
      "v_cvt_pk_bf16_f32 v65, v66, v67\n"
      "v_mfma_f32_16x16x32_bf16 v[24:27], a[4:7], v[196:199], v[24:27]\n"
      "ds_read_b128 a[4:7], v7 offset:1792\n"
      "v_cvt_pk_bf16_f32 v56, v56, v57\n"
      "v_mfma_f32_16x16x32_bf16 v[24:27], a[8:11], v[252:255], v[24:27]\n"
      "s_waitcnt lgkmcnt(7)\n"
      "ds_read_b128 a[8:11], v7 offset:2048\n"
      "v_cvt_pk_bf16_f32 v57, v58, v59\n"
      "v_mfma_f32_16x16x32_bf16 v[40:43], a[40:43], a[76:79], 0\n"
      "ds_write_addtid_b32 v20 offset:46088\n"
      "v_cvt_pk_bf16_f32 v60, v60, v61\n"
      "v_mfma_f32_16x16x32_bf16 v[28:31], a[12:15], v[96:99], 0\n"
      "s_waitcnt lgkmcnt(7)\n"
      "ds_write_addtid_b32 v21 offset:46344\n"
      "v_cvt_pk_bf16_f32 v61, v62, v63\n"
      "v_mfma_f32_16x16x32_bf16 v[28:31], a[16:19], v[152:155], v[28:31]\n"
      "s_mov_b32 m0, s18\n"
      "v_cvt_pk_bf16_f32 v24, v24, v25\n"
      "v_cvt_pk_bf16_f32 v25, v26, v27\n"
      "v_mfma_f32_16x16x32_bf16 v[28:31], a[20:23], a[220:223], v[28:31]\n"
      "s_waitcnt lgkmcnt(6)\n"
      "ds_write_addtid_b32 v64 offset:5632\n"
      "v_fma_f32 v48, s44, v36, v40\n"
      "v_mfma_f32_16x16x32_bf16 v[28:31], a[24:27], v[116:119], v[28:31]\n"
      "ds_write_addtid_b32 v65 offset:42560\n"
      "v_fma_f32 v49, s44, v37, v41\n"
      "v_mfma_f32_16x16x32_bf16 v[28:31], a[28:31], v[168:171], v[28:31]\n"
      "s_waitcnt lgkmcnt(6)\n"
      "ds_write_addtid_b32 v56 offset:6400\n"
      "v_cvt_pk_bf16_f32 v48, v48, v49\n"
      "v_mfma_f32_16x16x32_bf16 v[28:31], a[32:35], v[224:227], v[28:31]\n"
      "ds_write_addtid_b32 v57 offset:43328\n"
      "v_fma_f32 v50, s44, v38, v42\n"
      "v_mfma_f32_16x16x32_bf16 v[28:31], a[0:3], v[132:135], v[28:31]\n"
      "s_waitcnt lgkmcnt(6)\n"
      "ds_write_addtid_b32 v60 offset:4864\n"
      "v_fma_f32 v51, s44, v39, v43\n"
      "v_mfma_f32_16x16x32_bf16 v[28:31], a[4:7], v[188:191], v[28:31]\n"
      "ds_write_addtid_b32 v61 offset:41792\n"
      "v_cvt_pk_bf16_f32 v49, v50, v51\n"
      "v_mfma_f32_16x16x32_bf16 v[28:31], a[8:11], a[240:243], v[28:31]\n"
      "s_mov_b32 m0, s45\n"
      "v_fma_f32 v40, s44, v36, -v40\n"
      "v_fma_f32 v41, s44, v37, -v41\n"
      "v_mfma_f32_16x16x32_bf16 v[20:23], a[12:15], v[104:107], 0\n"
      "s_add_u32 m0, s58, 13280\n"
      "v_cvt_pk_bf16_f32 v40, v40, v41\n"
      "v_fma_f32 v42, s44, v38, -v42\n"
      "v_mfma_f32_16x16x32_bf16 v[20:23], a[16:19], v[156:159], v[20:23]\n"
      "buffer_load_dwordx4 v18, s[32:35], s53 offen offset:2048 lds\n"
      "v_fma_f32 v43, s44, v39, -v43\n"
      "v_mfma_f32_16x16x32_bf16 v[20:23], a[20:23], v[212:215], v[20:23]\n"
      "s_mov_b32 m0, s45\n"
      "v_cvt_pk_bf16_f32 v41, v42, v43\n"
      "v_cvt_pk_bf16_f32 v44, v44, v45\n"
      "v_mfma_f32_16x16x32_bf16 v[20:23], a[24:27], v[120:123], v[20:23]\n"
      "ds_write_addtid_b32 v24 offset:33792\n"
      "v_cvt_pk_bf16_f32 v45, v46, v47\n"
      "v_mfma_f32_16x16x32_bf16 v[20:23], a[28:31], v[176:179], v[20:23]\n"
      "ds_write_addtid_b32 v25 offset:34048\n"
      "v_cvt_pk_bf16_f32 v28, v28, v29\n"
      "v_mfma_f32_16x16x32_bf16 v[20:23], a[32:35], a[232:235], v[20:23]\n"
      "s_mov_b32 m0, s18\n"
      "ds_read2_b64 a[68:71], v13 offset0:1 offset1:65\n"
      "v_cvt_pk_bf16_f32 v29, v30, v31\n"
      "v_mfma_f32_16x16x32_bf16 v[20:23], a[0:3], v[140:143], v[20:23]\n"
      "s_sub_u32 s18, s19, s18\n"
      "ds_write_addtid_b32 v48 offset:5888\n"
      "v_cvt_pk_bf16_f32 v82, v76, v77\n"
      "v_mfma_f32_16x16x32_bf16 v[20:23], a[4:7], a[236:239], v[20:23]\n"
      "ds_write_addtid_b32 v49 offset:42816\n"
      "v_cvt_pk_bf16_f32 v88, v78, v79\n"
      "v_mfma_f32_16x16x32_bf16 v[20:23], a[8:11], v[248:251], v[20:23]\n"
      "ds_write_addtid_b32 v40 offset:6656\n"
      "s_nop 0\n"
      "v_mfma_f32_16x16x32_bf16 v[68:71], a[84:87], a[60:63], 0\n"
      "ds_write_addtid_b32 v41 offset:43584\n"
      "s_nop 0\n"
      "v_mfma_f32_16x16x32_bf16 v[32:35], a[12:15], v[108:111], 0\n"
      "ds_write_addtid_b32 v44 offset:5120\n"
      "v_mfma_f32_16x16x32_bf16 v[32:35], a[16:19], v[164:167], v[32:35]\n"
      "ds_write_addtid_b32 v45 offset:42048\n"
      "v_cvt_pk_bf16_f32 v20, v20, v21\n"
      "v_mfma_f32_16x16x32_bf16 v[32:35], a[20:23], a[224:227], v[32:35]\n"
      "s_mov_b32 m0, s45\n"
      "v_cvt_pk_bf16_f32 v83, v68, v69\n"
      "v_cvt_pk_bf16_f32 v89, v70, v71\n"
      "v_mfma_f32_16x16x32_bf16 v[32:35], a[24:27], v[128:131], v[32:35]\n"
      "ds_write_addtid_b32 v28 offset:59408\n"
      "v_cvt_pk_bf16_f32 v21, v22, v23\n"
      "v_mfma_f32_16x16x32_bf16 v[32:35], a[28:31], a[228:231], v[32:35]\n"
      "ds_write_addtid_b32 v29 offset:59664\n"
      "v_mfma_f32_16x16x32_bf16 v[32:35], a[32:35], v[236:239], v[32:35]\n"
      "buffer_store_dwordx4 v[80:83], v14, s[36:39], s52 offen sc1\n"
      "v_mfma_f32_16x16x32_bf16 v[32:35], a[0:3], v[144:147], v[32:35]\n"
      "ds_write_addtid_b32 v20 offset:47112\n"
      "v_mfma_f32_16x16x32_bf16 v[32:35], a[4:7], v[200:203], v[32:35]\n"
      "ds_write_addtid_b32 v21 offset:47368\n"
      "v_mfma_f32_16x16x32_bf16 v[32:35], a[8:11], a[244:247], v[32:35]\n"
      "buffer_store_dwordx4 v[86:89], v14, s[36:39], s52 offen offset:1536 sc1\n"
      "s_nop 0\n"
      "v_mfma_f32_16x16x32_bf16 v[72:75], a[84:87], a[64:67], 0\n"
      "s_waitcnt lgkmcnt(10)\n"
      "s_nop 1\n"
      "v_mfma_f32_16x16x32_bf16 v[76:79], a[84:87], a[68:71], 0\n"
      "s_cbranch_vccnz Lconv_loop1\n"
      "Lconv_t1:\n"
      "v_mfma_f32_16x16x32_bf16 v[20:23], a[24:27], v[96:99], 0\n"
      "v_cvt_pk_bf16_f32 v32, v32, v33\n"
      "v_cvt_pk_bf16_f32 v33, v34, v35\n"
      "v_mfma_f32_16x16x32_bf16 v[20:23], a[28:31], v[152:155], v[20:23]\n"
      "ds_write_addtid_b32 v32 offset:34816\n"
      "v_cvt_pk_bf16_f32 v84, v72, v73\n"
      "v_mfma_f32_16x16x32_bf16 v[20:23], a[32:35], a[220:223], v[20:23]\n"
      "ds_write_addtid_b32 v33 offset:35072\n"
      "v_cvt_pk_bf16_f32 v90, v74, v75\n"
      "v_mfma_f32_16x16x32_bf16 v[20:23], a[0:3], v[116:119], v[20:23]\n"
      "v_cvt_pk_bf16_f32 v85, v76, v77\n"
      "v_cvt_pk_bf16_f32 v91, v78, v79\n"
      "v_mfma_f32_16x16x32_bf16 v[20:23], a[4:7], v[168:171], v[20:23]\n"
      "buffer_store_dwordx2 v[84:85], v15, s[36:39], s52 offen sc1\n"
      "v_mfma_f32_16x16x32_bf16 v[20:23], a[8:11], v[224:227], v[20:23]\n"
      "v_mfma_f32_16x16x32_bf16 v[24:27], a[24:27], v[104:107], 0\n"
      "v_mfma_f32_16x16x32_bf16 v[24:27], a[28:31], v[156:159], v[24:27]\n"
      "buffer_store_dwordx2 v[90:91], v15, s[36:39], s52 offen offset:1536 sc1\n"
      "v_mfma_f32_16x16x32_bf16 v[24:27], a[32:35], v[212:215], v[24:27]\n"
      "s_mov_b32 s52, s13\n"
      "v_mfma_f32_16x16x32_bf16 v[24:27], a[0:3], v[120:123], v[24:27]\n"
      "s_add_u32 s13, s13, s12\n"
      "v_mfma_f32_16x16x32_bf16 v[24:27], a[4:7], v[176:179], v[24:27]\n"
      "s_waitcnt lgkmcnt(0)\n"
      "s_barrier\n"
      "ds_read2_b64 a[60:63], v10 offset0:0 offset1:64\n"
      "v_mfma_f32_16x16x32_bf16 v[24:27], a[8:11], a[232:235], v[24:27]\n"
      "s_waitcnt lgkmcnt(0)\n"
      "ds_read2_b64 a[64:67], v10 offset0:1 offset1:65\n"
      "v_mfma_f32_16x16x32_bf16 v[68:71], a[84:87], a[60:63], 0\n"
      "ds_read2_b64 a[68:71], v10 offset0:2 offset1:66\n"
      "v_mfma_f32_16x16x32_bf16 v[28:31], a[24:27], v[108:111], 0\n"
      "ds_read_b128 a[12:15], v7 offset:6144\n"
      "v_mfma_f32_16x16x32_bf16 v[28:31], a[28:31], v[164:167], v[28:31]\n"
      "ds_read_b128 a[16:19], v7 offset:6400\n"
      "v_mfma_f32_16x16x32_bf16 v[28:31], a[32:35], a[224:227], v[28:31]\n"
      "ds_read_b128 a[20:23], v7 offset:6656\n"
      "v_mfma_f32_16x16x32_bf16 v[28:31], a[0:3], v[128:131], v[28:31]\n"
      "ds_read2_b64 a[60:63], v10 offset0:3 offset1:67\n"
      "v_cvt_pk_bf16_f32 v80, v68, v69\n"
      "v_mfma_f32_16x16x32_bf16 v[28:31], a[4:7], a[228:231], v[28:31]\n"
      "ds_read_b128 a[24:27], v6 offset:5376\n"
      "v_cvt_pk_bf16_f32 v86, v70, v71\n"
      "v_mfma_f32_16x16x32_bf16 v[28:31], a[8:11], v[236:239], v[28:31]\n"
      "s_waitcnt lgkmcnt(6)\n"
      "ds_read_b128 a[28:31], v6 offset:5632\n"
      "v_mfma_f32_16x16x32_bf16 v[72:75], a[84:87], a[64:67], 0\n"
      "s_waitcnt lgkmcnt(6)\n"
      "ds_read2_b64 a[64:67], v12 offset0:0 offset1:64\n"
      "s_nop 0\n"
      "v_mfma_f32_16x16x32_bf16 v[76:79], a[84:87], a[68:71], 0\n"
      "s_waitcnt lgkmcnt(6)\n"
      "ds_read2_b64 a[68:71], v12 offset0:1 offset1:65\n"
      "s_nop 0\n"
      "v_mfma_f32_16x16x32_bf16 v[20:23], a[12:15], v[132:135], v[20:23]\n"
      "s_waitcnt lgkmcnt(6)\n"
      "ds_read_b128 a[32:35], v6 offset:5888\n"
      "v_cvt_pk_bf16_f32 v81, v72, v73\n"
      "v_mfma_f32_16x16x32_bf16 v[20:23], a[16:19], v[188:191], v[20:23]\n"
      "s_waitcnt lgkmcnt(6)\n"
      "ds_read_b128 a[0:3], v7 offset:768\n"
      "v_cvt_pk_bf16_f32 v82, v76, v77\n"
      "v_mfma_f32_16x16x32_bf16 v[20:23], a[20:23], a[240:243], v[20:23]\n"
      "s_waitcnt lgkmcnt(6)\n"
      "ds_read_b128 a[4:7], v7 offset:1024\n"
      "v_cvt_pk_bf16_f32 v87, v74, v75\n"
      "v_mfma_f32_16x16x32_bf16 v[68:71], a[84:87], a[60:63], 0\n"
      "ds_read_b128 a[8:11], v7 offset:1280\n"
      "v_cvt_pk_bf16_f32 v88, v78, v79\n"
      "v_mfma_f32_16x16x32_bf16 v[24:27], a[12:15], v[140:143], v[24:27]\n"
      "v_mfma_f32_16x16x32_bf16 v[24:27], a[16:19], a[236:239], v[24:27]\n"
      "v_cvt_pk_bf16_f32 v20, v20, v21\n"
      "v_cvt_pk_bf16_f32 v21, v22, v23\n"
      "v_mfma_f32_16x16x32_bf16 v[24:27], a[20:23], v[248:251], v[24:27]\n"
      "s_waitcnt lgkmcnt(5)\n"
      "ds_write_addtid_b32 v20 offset:62480\n"
      "v_cvt_pk_bf16_f32 v83, v68, v69\n"
      "v_mfma_f32_16x16x32_bf16 v[72:75], a[84:87], a[64:67], 0\n"
      "buffer_store_dwordx4 v[80:83], v14, s[36:39], s52 offen sc1\n"
      "v_cvt_pk_bf16_f32 v89, v70, v71\n"
      "v_mfma_f32_16x16x32_bf16 v[28:31], a[12:15], v[144:147], v[28:31]\n"
      "ds_read_b128 a[12:15], v7 offset:5376\n"
      "v_cvt_pk_bf16_f32 v24, v24, v25\n"
      "v_mfma_f32_16x16x32_bf16 v[28:31], a[16:19], v[200:203], v[28:31]\n"
      "ds_read_b128 a[16:19], v7 offset:5632\n"
      "v_cvt_pk_bf16_f32 v25, v26, v27\n"
      "v_mfma_f32_16x16x32_bf16 v[28:31], a[20:23], a[244:247], v[28:31]\n"
      "s_waitcnt lgkmcnt(4)\n"
      "ds_read_b128 a[20:23], v7 offset:5888\n"
      "v_cvt_pk_bf16_f32 v84, v72, v73\n"
      "v_mfma_f32_16x16x32_bf16 v[76:79], a[84:87], a[68:71], 0\n"
      "buffer_store_dwordx4 v[86:89], v14, s[36:39], s52 offen offset:1536 sc1\n"
      "v_cvt_pk_bf16_f32 v90, v74, v75\n"
      "v_mfma_f32_16x16x32_bf16 v[32:35], a[24:27], v[92:95], 0\n"
      "ds_write_addtid_b32 v21 offset:62736\n"
      "v_cvt_pk_bf16_f32 v28, v28, v29\n"
      "v_mfma_f32_16x16x32_bf16 v[32:35], a[28:31], v[148:151], v[32:35]\n"
      "ds_write_addtid_b32 v24 offset:50184\n"
      "v_cvt_pk_bf16_f32 v29, v30, v31\n"
      "v_mfma_f32_16x16x32_bf16 v[32:35], a[32:35], v[204:207], v[32:35]\n"
      "ds_write_addtid_b32 v25 offset:50440\n"
      "v_cvt_pk_bf16_f32 v85, v76, v77\n"
      "v_mfma_f32_16x16x32_bf16 v[32:35], a[0:3], v[112:115], v[32:35]\n"
      "buffer_store_dwordx2 v[84:85], v15, s[36:39], s52 offen sc1\n"
      "v_cvt_pk_bf16_f32 v91, v78, v79\n"
      "v_mfma_f32_16x16x32_bf16 v[32:35], a[4:7], a[208:211], v[32:35]\n"
      "s_waitcnt lgkmcnt(5)\n"
      "ds_write_addtid_b32 v28 offset:37888\n"
      "v_mfma_f32_16x16x32_bf16 v[32:35], a[8:11], v[220:223], v[32:35]\n"
      "ds_write_addtid_b32 v29 offset:38144\n"
      "v_mfma_f32_16x16x32_bf16 v[32:35], a[12:15], a[212:215], v[32:35]\n"
      "s_waitcnt lgkmcnt(5)\n"
      "v_mfma_f32_16x16x32_bf16 v[32:35], a[16:19], v[184:187], v[32:35]\n"
      "buffer_store_dwordx2 v[90:91], v15, s[36:39], s52 offen offset:1536 sc1\n"
      "v_mfma_f32_16x16x32_bf16 v[32:35], a[20:23], v[240:243], v[32:35]\n"
      "s_nop 1\n"
      "v_mfma_f32_16x16x32_bf16 v[20:23], a[24:27], v[100:103], 0\n"
      "s_nop 1\n"
      "v_mfma_f32_16x16x32_bf16 v[20:23], a[28:31], a[200:203], v[20:23]\n"
      "s_nop 0\n"
      "v_mfma_f32_16x16x32_bf16 v[20:23], a[32:35], v[208:211], v[20:23]\n"
      "v_cvt_pk_bf16_f32 v32, v32, v33\n"
      "v_cvt_pk_bf16_f32 v33, v34, v35\n"
      "v_mfma_f32_16x16x32_bf16 v[20:23], a[0:3], a[204:207], v[20:23]\n"
      "ds_write_addtid_b32 v32 offset:61456\n"
      "v_mfma_f32_16x16x32_bf16 v[20:23], a[4:7], v[172:175], v[20:23]\n"
      "ds_write_addtid_b32 v33 offset:61712\n"
      "v_mfma_f32_16x16x32_bf16 v[20:23], a[8:11], v[228:231], v[20:23]\n"
      "v_mfma_f32_16x16x32_bf16 v[20:23], a[12:15], v[136:139], v[20:23]\n"
      "v_mfma_f32_16x16x32_bf16 v[20:23], a[16:19], v[192:195], v[20:23]\n"
      "v_mfma_f32_16x16x32_bf16 v[20:23], a[20:23], v[244:247], v[20:23]\n"
      "s_nop 1\n"
      "v_mfma_f32_16x16x32_bf16 v[24:27], a[24:27], a[196:199], 0\n"
      "ds_read_b128 a[24:27], v7 offset:4608\n"
      "s_nop 0\n"
      "v_mfma_f32_16x16x32_bf16 v[24:27], a[28:31], v[160:163], v[24:27]\n"
      "ds_read_b128 a[28:31], v7 offset:4864\n"
      "v_mfma_f32_16x16x32_bf16 v[24:27], a[32:35], v[216:219], v[24:27]\n"
      "ds_read_b128 a[32:35], v7 offset:5120\n"
      "v_cvt_pk_bf16_f32 v20, v20, v21\n"
      "v_mfma_f32_16x16x32_bf16 v[24:27], a[0:3], v[124:127], v[24:27]\n"
      "ds_read_b128 a[0:3], v6 offset:4608\n"
      "v_cvt_pk_bf16_f32 v21, v22, v23\n"
      "v_mfma_f32_16x16x32_bf16 v[24:27], a[4:7], v[180:183], v[24:27]\n"
      "ds_read_b128 a[4:7], v6 offset:4864\n"
      "v_mfma_f32_16x16x32_bf16 v[24:27], a[8:11], v[232:235], v[24:27]\n"
      "ds_read_b128 a[8:11], v6 offset:5120\n"
      "v_mfma_f32_16x16x32_bf16 v[24:27], a[12:15], a[216:219], v[24:27]\n"
      "ds_read_b128 a[12:15], v7 offset:0\n"
      "v_mfma_f32_16x16x32_bf16 v[24:27], a[16:19], v[196:199], v[24:27]\n"
      "ds_read_b128 a[16:19], v7 offset:256\n"
      "v_mfma_f32_16x16x32_bf16 v[24:27], a[20:23], v[252:255], v[24:27]\n"
      "s_waitcnt lgkmcnt(7)\n"
      "ds_read_b128 a[20:23], v7 offset:512\n"
      "s_nop 0\n"
      "v_mfma_f32_16x16x32_bf16 v[28:31], a[24:27], a[160:163], 0\n"
      "s_waitcnt lgkmcnt(7)\n"
      "ds_write_addtid_b32 v20 offset:49160\n"
      "s_nop 0\n"
      "v_mfma_f32_16x16x32_bf16 v[28:31], a[28:31], a[172:175], v[28:31]\n"
      "s_waitcnt lgkmcnt(7)\n"
      "ds_write_addtid_b32 v21 offset:49416\n"
      "v_cvt_pk_bf16_f32 v24, v24, v25\n"
      "v_mfma_f32_16x16x32_bf16 v[28:31], a[32:35], a[184:187], v[28:31]\n"
      "s_waitcnt lgkmcnt(7)\n"
      "ds_write_addtid_b32 v24 offset:36864\n"
      "v_cvt_pk_bf16_f32 v25, v26, v27\n"
      "v_mfma_f32_16x16x32_bf16 v[28:31], a[0:3], a[88:91], v[28:31]\n"
      "s_waitcnt lgkmcnt(7)\n"
      "ds_write_addtid_b32 v25 offset:37120\n"
      "v_mfma_f32_16x16x32_bf16 v[28:31], a[4:7], a[100:103], v[28:31]\n"
      "s_waitcnt lgkmcnt(7)\n"
      "v_mfma_f32_16x16x32_bf16 v[28:31], a[8:11], a[112:115], v[28:31]\n"
      "s_waitcnt lgkmcnt(6)\n"
      "v_mfma_f32_16x16x32_bf16 v[28:31], a[12:15], a[124:127], v[28:31]\n"
      "s_waitcnt lgkmcnt(5)\n"
      "v_mfma_f32_16x16x32_bf16 v[28:31], a[16:19], a[136:139], v[28:31]\n"
      "s_waitcnt lgkmcnt(4)\n"
      "v_mfma_f32_16x16x32_bf16 v[28:31], a[20:23], a[148:151], v[28:31]\n"
      "s_nop 1\n"
      "v_mfma_f32_16x16x32_bf16 v[20:23], a[24:27], a[164:167], 0\n"
      "s_nop 1\n"
      "v_mfma_f32_16x16x32_bf16 v[20:23], a[28:31], a[176:179], v[20:23]\n"
      "s_nop 0\n"
      "v_mfma_f32_16x16x32_bf16 v[20:23], a[32:35], a[188:191], v[20:23]\n"
      "v_cvt_pk_bf16_f32 v28, v28, v29\n"
      "v_cvt_pk_bf16_f32 v29, v30, v31\n"
      "v_mfma_f32_16x16x32_bf16 v[20:23], a[0:3], a[92:95], v[20:23]\n"
      "ds_write_addtid_b32 v28 offset:60432\n"
      "v_mfma_f32_16x16x32_bf16 v[20:23], a[4:7], a[104:107], v[20:23]\n"
      "ds_write_addtid_b32 v29 offset:60688\n"
      "v_mfma_f32_16x16x32_bf16 v[20:23], a[8:11], a[116:119], v[20:23]\n"
      "v_mfma_f32_16x16x32_bf16 v[20:23], a[12:15], a[128:131], v[20:23]\n"
      "v_mfma_f32_16x16x32_bf16 v[20:23], a[16:19], a[140:143], v[20:23]\n"
      "v_mfma_f32_16x16x32_bf16 v[20:23], a[20:23], a[152:155], v[20:23]\n"
      "s_nop 1\n"
      "v_mfma_f32_16x16x32_bf16 v[32:35], a[24:27], a[168:171], 0\n"
      "s_nop 1\n"
      "v_mfma_f32_16x16x32_bf16 v[32:35], a[28:31], a[180:183], v[32:35]\n"
      "s_nop 0\n"
      "v_mfma_f32_16x16x32_bf16 v[32:35], a[32:35], a[192:195], v[32:35]\n"
      "v_cvt_pk_bf16_f32 v20, v20, v21\n"
      "v_cvt_pk_bf16_f32 v21, v22, v23\n"
      "v_mfma_f32_16x16x32_bf16 v[32:35], a[0:3], a[96:99], v[32:35]\n"
      "ds_write_addtid_b32 v20 offset:48136\n"
      "v_mfma_f32_16x16x32_bf16 v[32:35], a[4:7], a[108:111], v[32:35]\n"
      "ds_write_addtid_b32 v21 offset:48392\n"
      "v_mfma_f32_16x16x32_bf16 v[32:35], a[8:11], a[120:123], v[32:35]\n"
      "v_mfma_f32_16x16x32_bf16 v[32:35], a[12:15], a[132:135], v[32:35]\n"
      "v_mfma_f32_16x16x32_bf16 v[32:35], a[16:19], a[144:147], v[32:35]\n"
      "v_mfma_f32_16x16x32_bf16 v[32:35], a[20:23], a[156:159], v[32:35]\n"
      "s_nop 1\n"
      "s_nop 5\n"
      "v_cvt_pk_bf16_f32 v32, v32, v33\n"
      "v_cvt_pk_bf16_f32 v33, v34, v35\n"
      "ds_write_addtid_b32 v32 offset:35840\n"
      "ds_write_addtid_b32 v33 offset:36096\n"
      "s_waitcnt lgkmcnt(0)\n"
      "s_barrier\n"
      "ds_read2_b64 a[0:3], v11 offset0:0 offset1:64\n"
      "ds_read2_b64 a[4:7], v11 offset0:1 offset1:65\n"
      "ds_read2_b64 a[8:11], v11 offset0:2 offset1:66\n"
      "ds_read2_b64 a[12:15], v11 offset0:3 offset1:67\n"
      "ds_read2_b64 a[16:19], v13 offset0:0 offset1:64\n"
      "ds_read2_b64 a[20:23], v13 offset0:1 offset1:65\n"
      "s_waitcnt lgkmcnt(5)\n"
      "v_mfma_f32_16x16x32_bf16 v[68:71], a[84:87], a[0:3], 0\n"
      "s_waitcnt lgkmcnt(4)\n"
      "v_mfma_f32_16x16x32_bf16 v[72:75], a[84:87], a[4:7], 0\n"
      "s_waitcnt lgkmcnt(3)\n"
      "v_mfma_f32_16x16x32_bf16 v[76:79], a[84:87], a[8:11], 0\n"
      "s_waitcnt lgkmcnt(2)\n"
      "s_nop 2\n"
      "v_cvt_pk_bf16_f32 v80, v68, v69\n"
      "v_cvt_pk_bf16_f32 v86, v70, v71\n"
      "v_mfma_f32_16x16x32_bf16 v[68:71], a[84:87], a[12:15], 0\n"
      "s_waitcnt lgkmcnt(1)\n"
      "v_cvt_pk_bf16_f32 v81, v72, v73\n"
      "v_cvt_pk_bf16_f32 v87, v74, v75\n"
      "v_cvt_pk_bf16_f32 v82, v76, v77\n"
      "v_cvt_pk_bf16_f32 v88, v78, v79\n"
      "v_mfma_f32_16x16x32_bf16 v[72:75], a[84:87], a[16:19], 0\n"
      "s_waitcnt lgkmcnt(0)\n"
      "v_mfma_f32_16x16x32_bf16 v[76:79], a[84:87], a[20:23], 0\n"
      "s_mov_b32 s52, s13\n"
      "v_cvt_pk_bf16_f32 v83, v68, v69\n"
      "v_cvt_pk_bf16_f32 v89, v70, v71\n"
      "s_add_u32 s13, s13, s12\n"
      "buffer_store_dwordx4 v[80:83], v14, s[36:39], s52 offen sc1\n"
      "buffer_store_dwordx4 v[86:89], v14, s[36:39], s52 offen offset:1536 sc1\n"
      "v_cvt_pk_bf16_f32 v84, v72, v73\n"
      "v_cvt_pk_bf16_f32 v90, v74, v75\n"
      "v_cvt_pk_bf16_f32 v85, v76, v77\n"
      "v_cvt_pk_bf16_f32 v91, v78, v79\n"
      "buffer_store_dwordx2 v[84:85], v15, s[36:39], s52 offen sc1\n"
      "buffer_store_dwordx2 v[90:91], v15, s[36:39], s52 offen offset:1536 sc1\n"
      "Lconv_u1:\n"
      "Lconv_tail:\n"
      "s_waitcnt vmcnt(0) lgkmcnt(0)\n"
      "Lconv_end:\n"
      :
      : "v"(threadIdx.x), "s"(blockIdx.x), "s"(__builtin_amdgcn_kernarg_segment_ptr())
      : "v1", "v2", "v3", "v4", "v5", "v6", "v7", "v8", "v9", "v10", "v11", "v12", "v13", "v14", "v15", "v16", "v17", "v18", "v19", "v20", "v21", "v22", "v23", "v24", "v25", "v26", "v27", "v28", "v29", "v30", "v31", "v32", "v33", "v34", "v35", "v36", "v37", "v38", "v39", "v40", "v41", "v42", "v43", "v44", "v45", "v46", "v47", "v48", "v49", "v50", "v51", "v52", "v53", "v54", "v55", "v56", "v57", "v58", "v59", "v60", "v61", "v62", "v63", "v64", "v65", "v66", "v67", "v68", "v69", "v70", "v71", "v72", "v73", "v74", "v75", "v76", "v77", "v78", "v79", "v80", "v81", "v82", "v83", "v84", "v85", "v86", "v87", "v88", "v89", "v90", "v91", "v92", "v93", "v94", "v95", "v96", "v97", "v98", "v99", "v100", "v101", "v102", "v103", "v104", "v105", "v106", "v107", "v108", "v109", "v110", "v111", "v112", "v113", "v114", "v115", "v116", "v117", "v118", "v119", "v120", "v121", "v122", "v123", "v124", "v125", "v126", "v127", "v128", "v129", "v130", "v131", "v132", "v133", "v134", "v135", "v136", "v137", "v138", "v139", "v140", "v141", "v142", "v143", "v144", "v145", "v146", "v147", "v148", "v149", "v150", "v151", "v152", "v153", "v154", "v155", "v156", "v157", "v158", "v159", "v160", "v161", "v162", "v163", "v164", "v165", "v166", "v167", "v168", "v169", "v170", "v171", "v172", "v173", "v174", "v175", "v176", "v177", "v178", "v179", "v180", "v181", "v182", "v183", "v184", "v185", "v186", "v187", "v188", "v189", "v190", "v191", "v192", "v193", "v194", "v195", "v196", "v197", "v198", "v199", "v200", "v201", "v202", "v203", "v204", "v205", "v206", "v207", "v208", "v209", "v210", "v211", "v212", "v213", "v214", "v215", "v216", "v217", "v218", "v219", "v220", "v221", "v222", "v223", "v224", "v225", "v226", "v227", "v228", "v229", "v230", "v231", "v232", "v233", "v234", "v235", "v236", "v237", "v238", "v239", "v240", "v241", "v242", "v243", "v244", "v245", "v246", "v247", "v248", "v249", "v250", "v251", "v252", "v253", "v254", "v255", "a0", "a1", "a2", "a3", "a4", "a5", "a6", "a7", "a8", "a9", "a10", "a11", "a12", "a13", "a14", "a15", "a16", "a17", "a18", "a19", "a20", "a21", "a22", "a23", "a24", "a25", "a26", "a27", "a28", "a29", "a30", "a31", "a32", "a33", "a34", "a35", "a36", "a37", "a38", "a39", "a40", "a41", "a42", "a43", "a44", "a45", "a46", "a47", "a48", "a49", "a50", "a51", "a52", "a53", "a54", "a55", "a56", "a57", "a58", "a59", "a60", "a61", "a62", "a63", "a64", "a65", "a66", "a67", "a68", "a69", "a70", "a71", "a72", "a73", "a74", "a75", "a76", "a77", "a78", "a79", "a80", "a81", "a82", "a83", "a84", "a85", "a86", "a87", "a88", "a89", "a90", "a91", "a92", "a93", "a94", "a95", "a96", "a97", "a98", "a99", "a100", "a101", "a102", "a103", "a104", "a105", "a106", "a107", "a108", "a109", "a110", "a111", "a112", "a113", "a114", "a115", "a116", "a117", "a118", "a119", "a120", "a121", "a122", "a123", "a124", "a125", "a126", "a127", "a128", "a129", "a130", "a131", "a132", "a133", "a134", "a135", "a136", "a137", "a138", "a139", "a140", "a141", "a142", "a143", "a144", "a145", "a146", "a147", "a148", "a149", "a150", "a151", "a152", "a153", "a154", "a155", "a156", "a157", "a158", "a159", "a160", "a161", "a162", "a163", "a164", "a165", "a166", "a167", "a168", "a169", "a170", "a171", "a172", "a173", "a174", "a175", "a176", "a177", "a178", "a179", "a180", "a181", "a182", "a183", "a184", "a185", "a186", "a187", "a188", "a189", "a190", "a191", "a192", "a193", "a194", "a195", "a196", "a197", "a198", "a199", "a200", "a201", "a202", "a203", "a204", "a205", "a206", "a207", "a208", "a209", "a210", "a211", "a212", "a213", "a214", "a215", "a216", "a217", "a218", "a219", "a220", "a221", "a222", "a223", "a224", "a225", "a226", "a227", "a228", "a229", "a230", "a231", "a232", "a233", "a234", "a235", "a236", "a237", "a238", "a239", "a240", "a241", "a242", "a243", "a244", "a245", "a246", "a247", "a248", "a249", "a250", "a251", "a252", "a253", "a254", "a255", "s12", "s13", "s14", "s15", "s16", "s17", "s18", "s19", "s20", "s21", "s22", "s23", "s24", "s25", "s26", "s27", "s28", "s29", "s30", "s31", "s34", "s35", "s36", "s37", "s38", "s39", "s40", "s41", "s42", "s43", "s44", "s45", "s46", "s47", "s48", "s49", "s50", "s51", "s52", "s53", "s54", "s55", "s56", "s57", "s58", "s59", "s60", "s61", "s62", "s63", "s64", "s65", "s66", "s67", "s68", "s69", "s70", "s71", "s72", "s73", "s74", "s75", "s76", "s77", "s78", "s79", "s80", "s81", "s82", "s83", "s84", "s85", "s86", "s87", "s88", "s89", "s90", "s91", "s92", "s93", "s94", "s95", "s96", "s97", "s98", "s99", "scc", "vcc", "memory");
}

extern "C" void kernel_launch(void* const* d_in, const int* in_sizes, int n_in, void* d_out,
                              int out_size, void* d_ws, size_t ws_size, hipStream_t stream) {
  conv_kernel<<<256, 256, 0, stream>>>((const char*)d_in[0], (const char*)d_in[1], (char*)d_out, (char*)d_ws);
}
```

```cpp
#include <hip/hip_runtime.h>
#include <hip/hip_bf16.h>
#include <stdint.h>

__global__ void __launch_bounds__(256, 1) __attribute__((aligned(4096)))
conv_kernel(const char* __restrict__ x, const char* __restrict__ w, char* __restrict__ out, char* __restrict__ dbg) {
  __shared__ __attribute__((aligned(1024))) char smem[163840];
  asm volatile("" :: "v"((unsigned)(uintptr_t)smem) : "memory");
  asm volatile(
      "s_load_dwordx8 s[20:27], %2, 0x0\n"
      "v_readfirstlane_b32 s40, %0\n"
      "v_and_b32 v5, 63, %0\n"
      "v_lshlrev_b32 v8, 4, v5\n"
      "s_lshr_b32 s40, s40, 6\n"
      "s_lshl_b32 s43, s40, 3\n"
      "s_or_b32 s43, s43, 0x80000\n"
      "v_bitop3_b32 v9, v8, v5, 32 bitop3:0x78\n"
      "v_xor_b32 v89, 32, v8\n"
      "s_mul_i32 s92, s40, 3072\n"
      "s_lshr_b32 s14, %1, 7\n"
      "s_sub_u32 s14, s14, 1\n"
      "s_lshr_b32 s41, s40, 1\n"
      "s_and_b32 s42, s40, 1\n"
      "s_mov_b32 s30, 165888\n"
      "s_mov_b32 s31, 0x20000\n"
      "s_lshr_b32 s49, %1, 3\n"
      "s_lshl2_add_u32 s49, s49, s40\n"
      "v_lshlrev_b32 v36, 7, v5\n"
      "s_mul_i32 s50, s49, 384\n"
      "v_min_u32 v37, 256, v36\n"
      "v_add_u32 v37, s50, v37\n"
      "v_min_u32 v66, 1280, v36\n"
      "s_mul_i32 s96, s49, 1408\n"
      "s_getpc_b64 s[54:55]\n"
      "Lconv_start:\n"
      "v_min_u32 v37, Lconv_end-Lconv_start-4, v37\n"
      "s_mov_b32 m0, 162816\n"
      "s_nop 0\n"
      "global_load_lds_dword v37, s[54:55]\n"
      "s_bfe_u32 s99, %1, 0x30003\n"
      "s_mul_i32 s49, s40, 192\n"
      "v_add_u32 v36, s49, v5\n"
      "v_add_u32 v38, 128, v36\n"
      "v_mul_u32_u24 v37, 669, v36\n"
      "v_lshrrev_b32 v37, 16, v37\n"
      "v_mad_i32_i24 v37, v37, -2, v38\n"
      "v_lshlrev_b32 v16, 4, v37\n"
      "v_add_u32 v36, 64, v36\n"
      "v_mul_u32_u24 v37, 669, v36\n"
      "v_lshrrev_b32 v37, 16, v37\n"
      "v_mad_i32_i24 v37, v37, -2, v38\n"
      "v_lshlrev_b32 v17, 4, v37\n"
      "v_add_u32 v36, 64, v36\n"
      "v_mul_u32_u24 v37, 669, v36\n"
      "v_lshrrev_b32 v37, 16, v37\n"
      "v_mad_i32_i24 v37, v37, -2, v38\n"
      "v_lshlrev_b32 v18, 4, v37\n"
      "s_or_b32 s50, s99, s40\n"
      "s_cmp_eq_u32 s50, 0\n"
      "s_cselect_b32 s16, 0xffffff, 0\n"
      "s_mov_b32 s17, 0\n"
      "s_cmp_eq_u32 s99, 7\n"
      "s_cselect_b32 s19, 0xffffff00, 0\n"
      "s_mov_b32 s18, 0\n"
      "v_add_u32 v19, 14080, v8\n"
      "v_cndmask_b32 v16, v16, 2.0, s[16:17]\n"
      "v_cndmask_b32 v19, v19, 2.0, s[18:19]\n"
      "v_and_b32 v36, 16, v8\n"
      "v_mov_b32 v52, 0xffff\n"
      "v_lshlrev_b32 v52, v36, v52\n"
      "s_mov_b32 s16, 0x3000003\n"
      "s_mov_b32 s17, 0x3000003\n"
      "v_cndmask_b32 v48, 0, v52, s[16:17]\n"
      "s_lshl_b64 s[16:17], s[16:17], 2\n"
      "v_cndmask_b32 v49, 0, v52, s[16:17]\n"
      "s_lshl_b64 s[16:17], s[16:17], 2\n"
      "v_cndmask_b32 v50, 0, v52, s[16:17]\n"
      "s_lshl_b64 s[16:17], s[16:17], 2\n"
      "v_cndmask_b32 v51, 0, v52, s[16:17]\n"
      "v_bfe_u32 v53, v5, 5, 1\n"
      "v_xor_b32 v53, s41, v53\n"
      "v_sub_u32 v53, 0, v53\n"
      "v_and_b32 v36, 0x1000100, v53\n"
      "v_xor_b32 v36, 0x3fa83fa8, v36\n"
      "v_and_b32 v68, v36, v48\n"
      "v_and_b32 v69, v36, v49\n"
      "v_and_b32 v70, v36, v50\n"
      "v_and_b32 v71, v36, v51\n"
      "v_xor_b32 v72, 0x80008000, v68\n"
      "v_xor_b32 v73, 0x80008000, v69\n"
      "v_xor_b32 v74, 0x80008000, v70\n"
      "v_xor_b32 v75, 0x80008000, v71\n"
      "v_bfe_i32 v36, v5, 5, 1\n"
      "v_and_b32 v36, 0x3f283f28, v36\n"
      "v_xor_b32 v37, 0x3f283f28, v36\n"
      "v_and_b32 v76, v37, v48\n"
      "v_and_b32 v77, v37, v49\n"
      "v_and_b32 v78, v37, v50\n"
      "v_and_b32 v79, v37, v51\n"
      "v_and_b32 v80, v36, v48\n"
      "v_and_b32 v81, v36, v49\n"
      "v_and_b32 v82, v36, v50\n"
      "v_and_b32 v83, v36, v51\n"
      "s_mul_i32 s50, s42, 96\n"
      "v_bfe_u32 v36, v5, 1, 3\n"
      "v_and_b32 v37, 1, v5\n"
      "v_lshl_add_u32 v36, v37, 3, v36\n"
      "v_mul_u32_u24 v36, 192, v36\n"
      "v_add_u32 v36, 46144, v36\n"
      "v_lshlrev_b32 v37, 5, v37\n"
      "v_and_b32 v38, 16, v5\n"
      "v_add_u32 v38, s50, v38\n"
      "v_and_b32 v39, 32, v5\n"
      "v_mul_u32_u24 v40, 192, v39\n"
      "v_add_u32 v40, v40, v36\n"
      "v_xad_u32 v84, v38, v37, v40\n"
      "v_add_u32 v41, 32, v38\n"
      "v_xad_u32 v85, v41, v37, v40\n"
      "v_add_u32 v41, v39, v38\n"
      "v_xad_u32 v86, v41, v37, v36\n"
      "s_mul_i32 s49, s41, 6144\n"
      "s_add_u32 s49, s49, 46144\n"
      "v_lshrrev_b32 v36, 3, v5\n"
      "v_and_b32 v38, 4, v5\n"
      "v_lshl_add_u32 v36, v38, 1, v36\n"
      "v_mul_u32_u24 v36, 192, v36\n"
      "v_lshlrev_b32 v37, 3, v5\n"
      "v_and_b32 v38, 8, v37\n"
      "v_add3_u32 v36, v36, v38, s49\n"
      "v_and_b32 v38, 32, v37\n"
      "v_and_b32 v37, 16, v37\n"
      "v_add_u32 v37, s50, v37\n"
      "v_xad_u32 v87, v37, v38, v36\n"
      "v_add_u32 v39, 32, v37\n"
      "v_xad_u32 v88, v39, v38, v36\n"
      "s_waitcnt lgkmcnt(0)\n"
      "s_mov_b32 s28, s22\n"
      "s_and_b32 s29, s23, 0xffff\n"
      "buffer_load_dword v66, s[28:31], s96 offen lds\n"
      "s_add_u32 m0, s92, 73824\n"
      "s_and_b32 s97, 0x3c2c24, s14\n"
      "s_xor_b32 s97, 0x120c0600, s97\n"
      "s_bfe_u32 s97, s97, s43\n"
      "s_mul_i32 s97, s97, 3072\n"
      "buffer_load_dwordx4 v8, s[28:31], s97 offen offset:0 lds\n"
      "buffer_load_dwordx4 v9, s[28:31], s97 offen offset:1024 lds\n"
      "buffer_load_dwordx4 v89, s[28:31], s97 offen offset:2048 lds\n"
      "s_add_u32 m0, s92, 86112\n"
      "s_and_b32 s97, 0x2c240000, s14\n"
      "s_xor_b32 s97, 0x2a241e18, s97\n"
      "s_bfe_u32 s97, s97, s43\n"
      "s_mul_i32 s97, s97, 3072\n"
      "buffer_load_dwordx4 v8, s[28:31], s97 offen offset:0 lds\n"
      "buffer_load_dwordx4 v9, s[28:31], s97 offen offset:1024 lds\n"
      "buffer_load_dwordx4 v89, s[28:31], s97 offen offset:2048 lds\n"
      "s_and_b32 s53, %1, 7\n"
      "s_lshl_b32 s53, s53, 6\n"
      "s_bfe_u32 s50, %1, 0x10006\n"
      "s_add_u32 s53, s53, s50\n"
      "s_lshl1_add_u32 s50, s14, 32\n"
      "s_add_u32 s53, s53, s50\n"
      "s_mul_i32 s13, s53, 98304\n"
      "s_or_b32 s12, s14, 1\n"
      "s_mul_i32 s12, s12, 196608\n"
      "s_lshl_b32 s50, s12, 1\n"
      "s_sub_u32 s51, s13, s50\n"
      "s_mul_i32 s99, s99, 12288\n"
      "s_sub_u32 s50, s99, 2432\n"
      "s_ashr_i32 s49, s50, 31\n"
      "s_add_u32 s32, s20, s50\n"
      "s_addc_u32 s33, s21, s49\n"
      "s_and_b32 s33, s33, 0xffff\n"
      "s_sub_u32 s34, 50331648, s50\n"
      "s_mov_b32 s35, 0x20000\n"
      "s_add_u32 s46, s92, 110720\n"
      "v_lshrrev_b32 v36, 1, v5\n"
      "v_and_b32 v37, 4, v36\n"
      "v_and_or_b32 v37, v5, 2, v37\n"
      "v_mul_u32_u24 v37, 1568, v37\n"
      "v_xor_b32 v36, v36, v5\n"
      "v_and_b32 v36, 4, v36\n"
      "v_mad_u32_u24 v37, v36, 48, v37\n"
      "s_mul_i32 s50, s42, 1568\n"
      "s_add_u32 s50, s50, 110720\n"
      "v_and_b32 v36, 16, v5\n"
      "v_lshl_add_u32 v37, v36, 1, v37\n"
      "v_and_b32 v36, 16, v8\n"
      "v_add3_u32 v37, v37, v36, s50\n"
      "v_and_b32 v36, 32, v5\n"
      "v_mad_u32_u24 v38, v36, 24, v37\n"
      "v_mad_u32_u24 v39, v36, 25, v37\n"
      "s_mul_i32 s17, s42, 0xcc00cc00\n"
      "s_mov_b32 s16, 0\n"
      "v_mov_b32 v36, 768\n"
      "v_mov_b32 v37, 736\n"
      "s_mov_b64 exec, 0\n"
      "s_cmp_eq_u32 s40, 0\n"
      "s_cbranch_scc1 Lconv_p1\n"
      "s_cmp_eq_u32 s40, 1\n"
      "s_cbranch_scc1 Lconv_p2\n"
      "s_cmp_eq_u32 s40, 2\n"
      "s_cbranch_scc1 Lconv_pa0\n"
      "s_cmp_eq_u32 s40, 3\n"
      "s_cbranch_scc1 Lconv_pa1\n"
      "Lconv_r0:\n"
      "s_mov_b64 exec, -1\n"
      "s_add_u32 m0, s92, 98400\n"
      "s_and_b32 s97, 0x3c2c243c, s14\n"
      "s_xor_b32 s97, 0xd070130, s97\n"
      "s_bfe_u32 s97, s97, s43\n"
      "s_mul_i32 s97, s97, 3072\n"
      "buffer_load_dwordx4 v8, s[28:31], s97 offen offset:0 lds\n"
      "buffer_load_dwordx4 v9, s[28:31], s97 offen offset:1024 lds\n"
      "buffer_load_dwordx4 v89, s[28:31], s97 offen offset:2048 lds\n"
      "v_cndmask_b32 v36, v36, v37, s[16:17]\n"
      "v_add_u32 v39, v39, v36\n"
      "s_cmp_eq_u32 s41, 0\n"
      "s_cselect_b64 vcc, -1, 0\n"
      "v_add_u32 v40, 384, v38\n"
      "v_add_u32 v41, 384, v39\n"
      "v_cndmask_b32 v2, v40, v39, vcc\n"
      "v_cndmask_b32 v3, v39, v40, vcc\n"
      "v_cndmask_b32 v4, v41, v38, vcc\n"
      "v_and_b32 v36, 3, v5\n"
      "v_mul_u32_u24 v36, 18464, v36\n"
      "v_bfe_u32 v37, v5, 2, 2\n"
      "v_lshrrev_b32 v38, 1, v37\n"
      "v_xor_b32 v37, v37, v38\n"
      "v_lshl_add_u32 v36, v37, 6, v36\n"
      "s_mul_i32 s50, s41, 2304\n"
      "v_and_b32 v37, 48, v5\n"
      "v_add3_u32 v6, v36, v37, s50\n"
      "v_add_u32 v7, 9216, v6\n"
      "s_mov_b32 s16, 0xc0003\n"
      "s_mov_b32 s17, 0xc0003\n"
      "v_cndmask_b32 v48, 0, v52, s[16:17]\n"
      "s_lshl_b64 s[16:17], s[16:17], 4\n"
      "v_cndmask_b32 v49, 0, v52, s[16:17]\n"
      "s_lshl_b64 s[16:17], s[16:17], 4\n"
      "v_cndmask_b32 v50, 0, v52, s[16:17]\n"
      "s_lshl_b64 s[16:17], s[16:17], 4\n"
      "v_cndmask_b32 v51, 0, v52, s[16:17]\n"
      "v_and_b32 v36, 0xff00ff00, v53\n"
      "v_xor_b32 v36, 0xc080c080, v36\n"
      "v_and_b32 v38, v36, v48\n"
      "v_and_b32 v39, v36, v49\n"
      "v_and_b32 v40, v36, v50\n"
      "v_and_b32 v41, v36, v51\n"
      "v_accvgpr_write_b32 a72, v38\n"
      "v_accvgpr_write_b32 a73, v39\n"
      "v_accvgpr_write_b32 a74, v40\n"
      "v_accvgpr_write_b32 a75, v41\n"
      "v_and_b32 v36, 0xff00ff00, v53\n"
      "v_xor_b32 v36, 0xc05dc05d, v36\n"
      "v_and_b32 v38, v36, v48\n"
      "v_and_b32 v39, v36, v49\n"
      "v_and_b32 v40, v36, v50\n"
      "v_and_b32 v41, v36, v51\n"
      "v_accvgpr_write_b32 a76, v38\n"
      "v_accvgpr_write_b32 a77, v39\n"
      "v_accvgpr_write_b32 a78, v40\n"
      "v_accvgpr_write_b32 a79, v41\n"
      "v_and_b32 v36, 0x81008100, v53\n"
      "v_xor_b32 v36, 0x3f803f80, v36\n"
      "v_and_b32 v38, v36, v48\n"
      "v_and_b32 v39, v36, v49\n"
      "v_and_b32 v40, v36, v50\n"
      "v_and_b32 v41, v36, v51\n"
      "v_accvgpr_write_b32 a80, v38\n"
      "v_accvgpr_write_b32 a81, v39\n"
      "v_accvgpr_write_b32 a82, v40\n"
      "v_accvgpr_write_b32 a83, v41\n"
      "s_mov_b32 s44, 0x3fdd0000\n"
      "v_lshlrev_b32 v36, 5, v5\n"
      "v_and_b32 v36, 0x180, v36\n"
      "v_lshlrev_b32 v37, 3, v5\n"
      "v_and_b32 v37, 16, v37\n"
      "v_sub_u32 v38, 0x3e71, v36\n"
      "v_add_u32 v39, 0x3cf1, v36\n"
      "v_mov_b32 v40, 0x3f80\n"
      "s_mov_b64 vcc, 0xffff\n"
      "s_mov_b32 s16, 0xffff0000\n"
      "s_mov_b32 s17, 0xf0f0\n"
      "v_xor_b32 v41, 0x8000, v38\n"
      "v_cndmask_b32 v38, v38, v41, s[16:17]\n"
      "v_cndmask_b32 v38, v38, v40, vcc\n"
      "v_lshlrev_b32 v38, v37, v38\n"
      "v_xor_b32 v41, 0x8000, v39\n"
      "s_xor_b32 s17, s17, 0xffff\n"
      "v_cndmask_b32 v39, v39, v41, s[16:17]\n"
      "v_cndmask_b32 v39, v39, v40, vcc\n"
      "v_lshlrev_b32 v39, v37, v39\n"
      "s_mov_b32 s18, 0x55550005\n"
      "s_mov_b32 s19, 0x5555\n"
      "v_cndmask_b32 v42, 0, v38, s[18:19]\n"
      "s_lshl_b64 s[16:17], s[18:19], 1\n"
      "v_cndmask_b32 v43, 0, v38, s[16:17]\n"
      "v_accvgpr_write_b32 a84, v42\n"
      "v_accvgpr_write_b32 a85, v43\n"
      "s_mov_b32 s18, 0x55555000\n"
      "v_cndmask_b32 v42, 0, v39, s[18:19]\n"
      "s_lshl_b64 s[16:17], s[18:19], 1\n"
      "v_cndmask_b32 v43, 0, v39, s[16:17]\n"
      "v_accvgpr_write_b32 a86, v42\n"
      "v_accvgpr_write_b32 a87, v43\n"
      "v_lshrrev_b32 v36, 4, v5\n"
      "v_min_u32 v37, 2, v36\n"
      "v_lshlrev_b32 v37, 10, v37\n"
      "s_lshl_b32 s50, s41, 7\n"
      "s_lshl_b32 s49, s42, 8\n"
      "s_add_u32 s50, s50, s49\n"
      "s_add_u32 s50, s50, 98416\n"
      "v_and_b32 v38, 8, v5\n"
      "v_lshl_add_u32 v37, v38, 3, v37\n"
      "v_add_u32 v37, s50, v37\n"
      "s_movk_i32 s50, -12296\n"
      "s_mov_b32 s49, 43040\n"
      "v_mov_b32 v42, s49\n"
      "v_bfe_u32 v39, v5, 1, 2\n"
      "v_lshrrev_b32 v40, 2, v38\n"
      "v_add_u32 v39, v39, v40\n"
      "v_lshlrev_b32 v40, 5, v5\n"
      "v_and_b32 v40, 32, v40\n"
      "v_cmp_lt_u32 vcc, 2, v39\n"
      "v_mad_i32_i24 v39, v39, s50, v37\n"
      "v_add_u32 v39, v39, v40\n"
      "v_cndmask_b32 v41, 0, v42, vcc\n"
      "v_add_u32 v10, v39, v41\n"
      "v_add_u32 v11, 3072, v10\n"
      "v_bfe_u32 v39, v5, 2, 1\n"
      "v_lshrrev_b32 v40, 1, v38\n"
      "v_sub_u32 v39, v39, v40\n"
      "v_add_u32 v39, 4, v39\n"
      "v_and_b32 v40, 48, v8\n"
      "v_cmp_lt_u32 vcc, 2, v39\n"
      "v_mad_i32_i24 v39, v39, s50, v37\n"
      "v_add_u32 v39, v39, v40\n"
      "v_cndmask_b32 v41, 0, v42, vcc\n"
      "v_add_u32 v12, v39, v41\n"
      "v_add_u32 v13, 3072, v12\n"
      "v_mul_u32_u24 v36, 384, v36\n"
      "v_and_b32 v37, 15, v5\n"
      "v_lshl_add_u32 v39, v37, 3, v36\n"
      "v_add_u32 v15, 128, v39\n"
      "v_add_lshl_u32 v37, v37, v38, 4\n"
      "v_add_u32 v14, v37, v36\n"
      "s_add_u32 s50, s99, s92\n"
      "s_add_u32 s36, s24, s50\n"
      "s_addc_u32 s37, s25, 0\n"
      "s_and_b32 s37, s37, 0xffff\n"
      "s_sub_u32 s38, 50331648, s50\n"
      "s_mov_b32 s39, 0x20000\n"
      "s_mul_i32 s48, s12, 17\n"
      "s_mul_i32 s45, s42, 6152\n"
      "s_lshl_b32 s50, s41, 9\n"
      "s_add_u32 s45, s45, s50\n"
      "s_add_u32 s45, s45, 41056\n"
      "s_mov_b32 s98, 0\n"
      "s_mov_b32 s91, 26560\n"
      "s_mul_i32 s18, s42, 18464\n"
      "s_mul_i32 s50, s41, 2304\n"
      "s_add_u32 s18, s18, s50\n"
      "s_lshl1_add_u32 s19, s18, 9216\n"
      "s_add_u32 s58, s46, 26560\n"
      "s_lshl1_add_u32 s93, s46, 26560\n"
      "s_mul_i32 s59, s42, -13280\n"
      "s_add_u32 s59, s59, 136256\n"
      "s_lshl1_add_u32 s94, s59, 26560\n"
      "s_mul_i32 s50, s42, 26560\n"
      "s_add_u32 s59, s59, s50\n"
      "s_mul_i32 s15, s42, s12\n"
      "s_sub_u32 s95, s12, s15\n"
      "s_mov_b32 s52, 2.0\n"
      "s_cmp_lt_u32 s40, 3\n"
      "s_cselect_b32 s97, 27680, 0\n"
      "s_add_u32 s97, s97, s92\n"
      "s_add_u32 m0, s97, 36928\n"
      "s_and_b32 s97, 0x24000000, s14\n"
      "s_xor_b32 s97, 0x251f1913, s97\n"
      "s_bfe_u32 s97, s97, s43\n"
      "s_mul_i32 s97, s97, 3072\n"
      "buffer_load_dwordx4 v8, s[28:31], s97 offen offset:0 lds\n"
      "buffer_load_dwordx4 v9, s[28:31], s97 offen offset:1024 lds\n"
      "buffer_load_dwordx4 v89, s[28:31], s97 offen offset:2048 lds\n"
      "s_cmp_lt_u32 s40, 2\n"
      "s_cselect_b32 s97, 27680, 0\n"
      "s_add_u32 s97, s97, s92\n"
      "s_add_u32 m0, s97, 21536\n"
      "s_and_b32 s97, 0x24243c2c, s14\n"
      "s_xor_b32 s97, 0x802312b, s97\n"
      "s_bfe_u32 s97, s97, s43\n"
      "s_mul_i32 s97, s97, 3072\n"
      "buffer_load_dwordx4 v8, s[28:31], s97 offen offset:0 lds\n"
      "buffer_load_dwordx4 v9, s[28:31], s97 offen offset:1024 lds\n"
      "buffer_load_dwordx4 v89, s[28:31], s97 offen offset:2048 lds\n"
      "s_cmp_lt_u32 s40, 1\n"
      "s_cselect_b32 s97, 27680, 0\n"
      "s_add_u32 s97, s97, s92\n"
      "s_add_u32 m0, s97, 6144\n"
      "s_and_b32 s97, 0x3c, s14\n"
      "s_xor_b32 s97, 0x201a140e, s97\n"
      "s_bfe_u32 s97, s97, s43\n"
      "s_mul_i32 s97, s97, 3072\n"
      "buffer_load_dwordx4 v8, s[28:31], s97 offen offset:0 lds\n"
      "buffer_load_dwordx4 v9, s[28:31], s97 offen offset:1024 lds\n"
      "buffer_load_dwordx4 v89, s[28:31], s97 offen offset:2048 lds\n"
      "s_sub_u32 vcc_lo, 0, s41\n"
      "s_mov_b32 vcc_hi, 0\n"
      ".p2align 3\n"
      "s_waitcnt vmcnt(12)\n"
      "s_barrier\n"
      "s_cmp_lt_u32 s40, 3\n"
      "s_cselect_b32 s97, -9248, 0\n"
      "ds_read_b128 a[0:3], v86 offset:30752\n"
      "ds_read_b128 a[4:7], v84 offset:27680\n"
      "ds_read_b128 a[8:11], v85 offset:27680\n"
      "ds_read_b128 a[12:15], v86 offset:30816\n"
      "s_add_u32 s97, s97, s92\n"
      "s_add_u32 m0, s97, 9248\n"
      "s_and_b32 s97, 0x243c2424, s14\n"
      "s_xor_b32 s97, 0x3322c26, s97\n"
      "s_bfe_u32 s97, s97, s43\n"
      "s_mul_i32 s97, s97, 3072\n"
      "s_waitcnt lgkmcnt(3)\n"
      "v_mfma_f32_16x16x32_bf16 v[20:23], a[0:3], v[76:79], 0\n"
      "s_waitcnt lgkmcnt(2)\n"
      "ds_read_b128 a[16:19], v84 offset:27744\n"
      "ds_read_b128 a[20:23], v86 offset:39968\n"
      "buffer_load_dwordx4 v8, s[28:31], s97 offen offset:0 lds\n"
      "v_mfma_f32_16x16x32_bf16 v[24:27], a[4:7], v[68:71], v[20:23]\n"
      "ds_read_b128 a[24:27], v84 offset:36896\n"
      "ds_read_b128 a[28:31], v85 offset:36896\n"
      "v_mfma_f32_16x16x32_bf16 v[20:23], a[4:7], v[72:75], v[20:23]\n"
      "ds_read_b64_tr_b16 a[88:89], v87 offset:27680\n"
      "ds_read_b64_tr_b16 a[92:93], v88 offset:27680\n"
      "buffer_load_dwordx4 v9, s[28:31], s97 offen offset:1024 lds\n"
      "v_mfma_f32_16x16x32_bf16 v[28:31], a[0:3], v[80:83], 0\n"
      "s_waitcnt lgkmcnt(7)\n"
      "ds_read_b64_tr_b16 a[96:97], v87 offset:27744\n"
      "ds_read_b64_tr_b16 a[124:125], v87 offset:36896\n"
      "v_mfma_f32_16x16x32_bf16 v[32:35], a[8:11], v[68:71], v[28:31]\n"
      "ds_read_b64_tr_b16 a[128:129], v88 offset:36896\n"
      "ds_read_b128 a[32:35], v86 offset:40032\n"
      "v_mfma_f32_16x16x32_bf16 v[28:31], a[8:11], v[72:75], v[28:31]\n"
      "s_waitcnt lgkmcnt(10)\n"
      "ds_read_b128 a[0:3], v84 offset:36960\n"
      "ds_read_b64_tr_b16 a[132:133], v87 offset:36960\n"
      "buffer_load_dwordx4 v89, s[28:31], s97 offen offset:2048 lds\n"
      "v_mfma_f32_16x16x32_bf16 v[54:57], a[12:15], v[76:79], 0\n"
      "s_waitcnt lgkmcnt(10)\n"
      "s_cmp_lt_u32 s40, 2\n"
      "s_cselect_b32 s97, -9248, 0\n"
      "s_add_u32 s97, s97, s92\n"
      "s_add_u32 m0, s97, 30784\n"
      "s_and_b32 s97, 0x3c24, s14\n"
      "s_xor_b32 s97, 0x1b150f09, s97\n"
      "s_bfe_u32 s97, s97, s43\n"
      "s_mul_i32 s97, s97, 3072\n"
      "v_cvt_pk_bf16_f32 v96, v20, v21\n"
      "v_cvt_pk_bf16_f32 v97, v22, v23\n"
      "v_mfma_f32_16x16x32_bf16 v[58:61], a[16:19], v[68:71], v[54:57]\n"
      "v_cvt_pk_bf16_f32 v92, v24, v25\n"
      "v_cvt_pk_bf16_f32 v93, v26, v27\n"
      "v_mfma_f32_16x16x32_bf16 v[54:57], a[16:19], v[72:75], v[54:57]\n"
      "buffer_load_dwordx4 v8, s[28:31], s97 offen offset:0 lds\n"
      "v_cvt_pk_bf16_f32 v104, v28, v29\n"
      "v_cvt_pk_bf16_f32 v105, v30, v31\n"
      "v_mfma_f32_16x16x32_bf16 v[62:65], a[20:23], v[76:79], 0\n"
      "s_waitcnt lgkmcnt(4)\n"
      "v_cvt_pk_bf16_f32 v100, v32, v33\n"
      "v_cvt_pk_bf16_f32 v101, v34, v35\n"
      "v_mfma_f32_16x16x32_bf16 v[20:23], a[24:27], v[68:71], v[62:65]\n"
      "v_cvt_pk_bf16_f32 v108, v54, v55\n"
      "v_cvt_pk_bf16_f32 v109, v56, v57\n"
      "v_mfma_f32_16x16x32_bf16 v[62:65], a[24:27], v[72:75], v[62:65]\n"
      "s_waitcnt lgkmcnt(0)\n"
      "s_waitcnt vmcnt(13)\n"
      "s_cbranch_vccnz Lconv_h1\n"
      "s_barrier\n"
      "Lconv_h1:\n"
      "buffer_load_dwordx4 v9, s[28:31], s97 offen offset:1024 lds\n"
      "v_mfma_f32_16x16x32_bf16 v[24:27], a[20:23], v[80:83], 0\n"
      "v_cvt_pk_bf16_f32 v58, v58, v59\n"
      "v_cvt_pk_bf16_f32 v60, v60, v61\n"
      "v_mfma_f32_16x16x32_bf16 v[28:31], a[28:31], v[68:71], v[24:27]\n"
      "s_cbranch_vccz Lconv_h2\n"
      "s_barrier\n"
      "Lconv_h2:\n"
      "ds_read_b128 a[4:7], v86 offset:49184\n"
      "ds_read_b128 a[8:11], v84 offset:46112\n"
      "ds_read_b128 a[12:15], v85 offset:46112\n"
      "ds_read_b128 a[16:19], v86 offset:49248\n"
      "v_mfma_f32_16x16x32_bf16 v[24:27], a[28:31], v[72:75], v[24:27]\n"
      "ds_read_b128 a[20:23], v84 offset:46176\n"
      "ds_read_b128 a[24:27], v86 offset:58400\n"
      "buffer_load_dwordx4 v89, s[28:31], s97 offen offset:2048 lds\n"
      "v_accvgpr_write_b32 a196, v58\n"
      "v_accvgpr_write_b32 a197, v60\n"
      "v_mfma_f32_16x16x32_bf16 v[32:35], a[32:35], v[76:79], 0\n"
      "s_cmp_lt_u32 s40, 1\n"
      "s_cselect_b32 s97, -9248, 0\n"
      "ds_read_b128 a[28:31], v84 offset:55328\n"
      "ds_read_b128 a[32:35], v85 offset:55328\n"
      "s_add_u32 s97, s97, s92\n"
      "s_add_u32 m0, s97, 52320\n"
      "s_and_b32 s97, 0x3c242400, s14\n"
      "s_xor_b32 s97, 0x332d2721, s97\n"
      "s_bfe_u32 s97, s97, s43\n"
      "s_mul_i32 s97, s97, 3072\n"
      "v_cvt_pk_bf16_f32 v116, v62, v63\n"
      "v_cvt_pk_bf16_f32 v117, v64, v65\n"
      "v_mfma_f32_16x16x32_bf16 v[54:57], a[0:3], v[68:71], v[32:35]\n"
      "ds_read_b64_tr_b16 a[160:161], v87 offset:46112\n"
      "ds_read_b64_tr_b16 a[164:165], v88 offset:46112\n"
      "buffer_load_dwordx4 v8, s[28:31], s97 offen offset:0 lds\n"
      "v_cvt_pk_bf16_f32 v112, v20, v21\n"
      "v_cvt_pk_bf16_f32 v113, v22, v23\n"
      "v_mfma_f32_16x16x32_bf16 v[32:35], a[0:3], v[72:75], v[32:35]\n"
      "s_waitcnt lgkmcnt(9)\n"
      "ds_read_b64_tr_b16 a[168:169], v87 offset:46176\n"
      "ds_read_b64_tr_b16 a[90:91], v87 offset:55328\n"
      "v_cvt_pk_bf16_f32 v120, v24, v25\n"
      "v_cvt_pk_bf16_f32 v121, v26, v27\n"
      "v_mfma_f32_16x16x32_bf16 v[58:61], a[4:7], v[76:79], 0\n"
      "s_waitcnt lgkmcnt(10)\n"
      "ds_read_b64_tr_b16 a[94:95], v88 offset:55328\n"
      "ds_read_b128 a[0:3], v86 offset:58464\n"
      "v_mfma_f32_16x16x32_bf16 v[62:65], a[8:11], v[68:71], v[58:61]\n"
      "ds_read_b64_tr_b16 a[98:99], v87 offset:55392\n"
      "buffer_load_dwordx4 v9, s[28:31], s97 offen offset:1024 lds\n"
      "v_cvt_pk_bf16_f32 v28, v28, v29\n"
      "v_cvt_pk_bf16_f32 v30, v30, v31\n"
      "v_accvgpr_write_b32 a204, v28\n"
      "v_mfma_f32_16x16x32_bf16 v[58:61], a[8:11], v[72:75], v[58:61]\n"
      "v_accvgpr_write_b32 a205, v30\n"
      "v_cvt_pk_bf16_f32 v128, v32, v33\n"
      "v_cvt_pk_bf16_f32 v129, v34, v35\n"
      "v_mfma_f32_16x16x32_bf16 v[20:23], a[4:7], v[80:83], 0\n"
      "s_waitcnt lgkmcnt(7)\n"
      "ds_read_b128 a[4:7], v84 offset:55392\n"
      "v_cvt_pk_bf16_f32 v124, v54, v55\n"
      "v_mfma_f32_16x16x32_bf16 v[24:27], a[12:15], v[68:71], v[20:23]\n"
      "buffer_load_dwordx4 v89, s[28:31], s97 offen offset:2048 lds\n"
      "v_cvt_pk_bf16_f32 v125, v56, v57\n"
      "v_mfma_f32_16x16x32_bf16 v[20:23], a[12:15], v[72:75], v[20:23]\n"
      "s_add_u32 m0, s46, 13280\n"
      "s_add_u32 s51, s51, s12\n"
      "v_cvt_pk_bf16_f32 v132, v58, v59\n"
      "v_cvt_pk_bf16_f32 v133, v60, v61\n"
      "v_mfma_f32_16x16x32_bf16 v[28:31], a[16:19], v[76:79], 0\n"
      "v_cvt_pk_bf16_f32 v62, v62, v63\n"
      "v_cvt_pk_bf16_f32 v64, v64, v65\n"
      "v_mfma_f32_16x16x32_bf16 v[32:35], a[20:23], v[68:71], v[28:31]\n"
      "buffer_load_dwordx4 v16, s[32:35], s51 offen lds\n"
      "v_accvgpr_write_b32 a212, v62\n"
      "v_accvgpr_write_b32 a213, v64\n"
      "v_mfma_f32_16x16x32_bf16 v[28:31], a[20:23], v[72:75], v[28:31]\n"
      "s_add_u32 m0, s46, 13280\n"
      "v_cvt_pk_bf16_f32 v140, v20, v21\n"
      "v_cvt_pk_bf16_f32 v141, v22, v23\n"
      "v_mfma_f32_16x16x32_bf16 v[54:57], a[24:27], v[76:79], 0\n"
      "buffer_load_dwordx4 v17, s[32:35], s51 offen offset:1024 lds\n"
      "v_cvt_pk_bf16_f32 v136, v24, v25\n"
      "v_mfma_f32_16x16x32_bf16 v[58:61], a[28:31], v[68:71], v[54:57]\n"
      "s_add_u32 m0, s46, 13280\n"
      "s_add_u32 s96, s51, s15\n"
      "buffer_load_dwordx4 v18, s[32:35], s51 offen offset:2048 lds\n"
      "v_cvt_pk_bf16_f32 v137, v26, v27\n"
      "v_mfma_f32_16x16x32_bf16 v[54:57], a[28:31], v[72:75], v[54:57]\n"
      "s_waitcnt lgkmcnt(0)\n"
      "s_waitcnt vmcnt(12)\n"
      "s_cbranch_vccnz Lconv_h3\n"
      "s_barrier\n"
      "Lconv_h3:\n"
      "s_mov_b32 m0, s59\n"
      "v_cvt_pk_bf16_f32 v144, v28, v29\n"
      "v_cvt_pk_bf16_f32 v145, v30, v31\n"
      "v_mfma_f32_16x16x32_bf16 v[62:65], a[24:27], v[80:83], 0\n"
      "buffer_load_dwordx4 v19, s[32:35], s96 offen lds\n"
      "v_cvt_pk_bf16_f32 v32, v32, v33\n"
      "v_mfma_f32_16x16x32_bf16 v[20:23], a[32:35], v[68:71], v[62:65]\n"
      "s_cbranch_vccz Lconv_h4\n"
      "s_barrier\n"
      "Lconv_h4:\n"
      "s_add_u32 m0, s46, 26560\n"
      "s_add_u32 s51, s51, s12\n"
      "ds_read_b128 a[8:11], v86 offset:21536\n"
      "ds_read_b128 a[12:15], v84 offset:18464\n"
      "ds_read_b128 a[16:19], v85 offset:18464\n"
      "ds_read_b128 a[20:23], v86 offset:21600\n"
      "buffer_load_dwordx4 v16, s[32:35], s51 offen lds\n"
      "v_mfma_f32_16x16x32_bf16 v[62:65], a[32:35], v[72:75], v[62:65]\n"
      "s_add_u32 m0, s46, 26560\n"
      "ds_read_b128 a[24:27], v84 offset:18528\n"
      "ds_read_b128 a[28:31], v86 offset:3072\n"
      "buffer_load_dwordx4 v17, s[32:35], s51 offen offset:1024 lds\n"
      "v_cvt_pk_bf16_f32 v34, v34, v35\n"
      "v_accvgpr_write_b32 a216, v32\n"
      "v_mfma_f32_16x16x32_bf16 v[24:27], a[0:3], v[76:79], 0\n"
      "s_add_u32 m0, s46, 26560\n"
      "ds_read_b128 a[32:35], v84 offset:0\n"
      "ds_read_b128 a[0:3], v85 offset:0\n"
      "buffer_load_dwordx4 v18, s[32:35], s51 offen offset:2048 lds\n"
      "v_accvgpr_write_b32 a217, v34\n"
      "v_cvt_pk_bf16_f32 v98, v54, v55\n"
      "v_mfma_f32_16x16x32_bf16 v[28:31], a[4:7], v[68:71], v[24:27]\n"
      "s_add_u32 m0, s92, 73824\n"
      "s_and_b32 s97, 0x24242c, s14\n"
      "ds_read_b64_tr_b16 a[126:127], v87 offset:18464\n"
      "ds_read_b64_tr_b16 a[130:131], v88 offset:18464\n"
      "s_xor_b32 s97, 0x16100a04, s97\n"
      "s_bfe_u32 s97, s97, s43\n"
      "s_mul_i32 s97, s97, 3072\n"
      "buffer_load_dwordx4 v8, s[28:31], s97 offen offset:0 lds\n"
      "v_cvt_pk_bf16_f32 v99, v56, v57\n"
      "v_cvt_pk_bf16_f32 v94, v58, v59\n"
      "v_mfma_f32_16x16x32_bf16 v[24:27], a[4:7], v[72:75], v[24:27]\n"
      "s_waitcnt lgkmcnt(9)\n"
      "ds_read_b128 a[4:7], v86 offset:3136\n"
      "ds_read_b64_tr_b16 a[134:135], v87 offset:18528\n"
      "v_cvt_pk_bf16_f32 v95, v60, v61\n"
      "v_cvt_pk_bf16_f32 v106, v62, v63\n"
      "v_cvt_pk_bf16_f32 v107, v64, v65\n"
      "v_mfma_f32_16x16x32_bf16 v[32:35], a[8:11], v[76:79], 0\n"
      "s_waitcnt lgkmcnt(10)\n"
      "ds_read_b64_tr_b16 a[162:163], v87 offset:0\n"
      "ds_read_b64_tr_b16 a[166:167], v88 offset:0\n"
      "v_cvt_pk_bf16_f32 v102, v20, v21\n"
      "v_cvt_pk_bf16_f32 v103, v22, v23\n"
      "v_cvt_pk_bf16_f32 v110, v24, v25\n"
      "v_mfma_f32_16x16x32_bf16 v[54:57], a[12:15], v[68:71], v[32:35]\n"
      "ds_read_b64_tr_b16 a[170:171], v87 offset:64\n"
      "v_cvt_pk_bf16_f32 v111, v26, v27\n"
      "v_cvt_pk_bf16_f32 v28, v28, v29\n"
      "v_cvt_pk_bf16_f32 v30, v30, v31\n"
      "v_mfma_f32_16x16x32_bf16 v[32:35], a[12:15], v[72:75], v[32:35]\n"
      "buffer_load_dwordx4 v9, s[28:31], s97 offen offset:1024 lds\n"
      "v_accvgpr_write_b32 a198, v28\n"
      "v_accvgpr_write_b32 a199, v30\n"
      "v_mfma_f32_16x16x32_bf16 v[58:61], a[8:11], v[80:83], 0\n"
      "Lconv_p1:\n"
      "s_cbranch_execz Lconv_r0\n"
      "s_nop 0\n"
      "s_waitcnt lgkmcnt(7)\n"
      "ds_read_b128 a[8:11], v84 offset:64\n"
      "v_add_u32 v86, -46144, v86\n"
      "v_mfma_f32_16x16x32_bf16 v[62:65], a[16:19], v[68:71], v[58:61]\n"
      "ds_read_b128 a[12:15], v86 offset:30752\n"
      "v_cvt_pk_bf16_f32 v118, v32, v33\n"
      "v_cvt_pk_bf16_f32 v119, v34, v35\n"
      "v_cvt_pk_bf16_f32 v114, v54, v55\n"
      "v_cvt_pk_bf16_f32 v115, v56, v57\n"
      "v_mfma_f32_16x16x32_bf16 v[58:61], a[16:19], v[72:75], v[58:61]\n"
      "v_add_u32 v84, -46144, v84\n"
      "v_add_u32 v85, -46144, v85\n"
      "v_mfma_f32_16x16x32_bf16 v[20:23], a[20:23], v[76:79], 0\n"
      "ds_read_b128 a[16:19], v84 offset:27680\n"
      "ds_read_b128 a[20:23], v85 offset:27680\n"
      "buffer_load_dwordx4 v89, s[28:31], s97 offen offset:2048 lds\n"
      "v_mfma_f32_16x16x32_bf16 v[24:27], a[24:27], v[68:71], v[20:23]\n"
      "s_add_u32 m0, s92, 86112\n"
      "s_and_b32 s97, 0x242c0000, s14\n"
      "s_xor_b32 s97, 0x2e28221c, s97\n"
      "s_bfe_u32 s97, s97, s43\n"
      "s_mul_i32 s97, s97, 3072\n"
      "v_cvt_pk_bf16_f32 v122, v58, v59\n"
      "v_cvt_pk_bf16_f32 v123, v60, v61\n"
      "v_mfma_f32_16x16x32_bf16 v[20:23], a[24:27], v[72:75], v[20:23]\n"
      "ds_read_b128 a[24:27], v86 offset:30816\n"
      "v_cvt_pk_bf16_f32 v62, v62, v63\n"
      "v_cvt_pk_bf16_f32 v64, v64, v65\n"
      "v_mfma_f32_16x16x32_bf16 v[28:31], a[28:31], v[76:79], 0\n"
      "v_accvgpr_write_b32 a206, v62\n"
      "v_accvgpr_write_b32 a207, v64\n"
      "v_mfma_f32_16x16x32_bf16 v[32:35], a[32:35], v[68:71], v[28:31]\n"
      "v_add_u32 v87, -46144, v87\n"
      "v_cvt_pk_bf16_f32 v130, v20, v21\n"
      "v_mfma_f32_16x16x32_bf16 v[28:31], a[32:35], v[72:75], v[28:31]\n"
      "ds_read_b64_tr_b16 a[100:101], v87 offset:27680\n"
      "ds_read_b64_tr_b16 a[108:109], v87 offset:27744\n"
      "buffer_load_dwordx4 v8, s[28:31], s97 offen offset:0 lds\n"
      "v_cvt_pk_bf16_f32 v131, v22, v23\n"
      "v_add_u32 v88, -46144, v88\n"
      "v_mfma_f32_16x16x32_bf16 v[54:57], a[28:31], v[80:83], 0\n"
      "ds_read_b64_tr_b16 a[104:105], v88 offset:27680\n"
      "ds_read_b128 a[28:31], v84 offset:27744\n"
      "v_mfma_f32_16x16x32_bf16 v[58:61], a[0:3], v[68:71], v[54:57]\n"
      "v_cvt_pk_bf16_f32 v126, v24, v25\n"
      "v_cvt_pk_bf16_f32 v127, v26, v27\n"
      "v_mfma_f32_16x16x32_bf16 v[54:57], a[0:3], v[72:75], v[54:57]\n"
      "s_waitcnt lgkmcnt(4)\n"
      "v_cvt_pk_bf16_f32 v134, v28, v29\n"
      "v_cvt_pk_bf16_f32 v135, v30, v31\n"
      "v_mfma_f32_16x16x32_bf16 v[62:65], a[4:7], v[76:79], 0\n"
      "buffer_load_dwordx4 v9, s[28:31], s97 offen offset:1024 lds\n"
      "v_cvt_pk_bf16_f32 v32, v32, v33\n"
      "v_mfma_f32_16x16x32_bf16 v[20:23], a[8:11], v[68:71], v[62:65]\n"
      "v_cvt_pk_bf16_f32 v34, v34, v35\n"
      "v_accvgpr_write_b32 a214, v32\n"
      "v_accvgpr_write_b32 a215, v34\n"
      "v_mfma_f32_16x16x32_bf16 v[62:65], a[8:11], v[72:75], v[62:65]\n"
      "v_cvt_pk_bf16_f32 v142, v54, v55\n"
      "v_cvt_pk_bf16_f32 v143, v56, v57\n"
      "v_mfma_f32_16x16x32_bf16 v[24:27], a[12:15], v[76:79], 0\n"
      "v_cvt_pk_bf16_f32 v138, v58, v59\n"
      "v_cvt_pk_bf16_f32 v139, v60, v61\n"
      "v_mfma_f32_16x16x32_bf16 v[28:31], a[16:19], v[68:71], v[24:27]\n"
      "v_cvt_pk_bf16_f32 v20, v20, v21\n"
      "v_cvt_pk_bf16_f32 v22, v22, v23\n"
      "v_mfma_f32_16x16x32_bf16 v[24:27], a[16:19], v[72:75], v[24:27]\n"
      "s_waitcnt lgkmcnt(0)\n"
      "s_waitcnt vmcnt(12)\n"
      "s_cbranch_vccnz Lconv_h5\n"
      "s_barrier\n"
      "Lconv_h5:\n"
      "buffer_load_dwordx4 v89, s[28:31], s97 offen offset:2048 lds\n"
      "v_mfma_f32_16x16x32_bf16 v[32:35], a[12:15], v[80:83], 0\n"
      "s_add_u32 m0, s92, 98400\n"
      "s_and_b32 s97, 0x24242c24, s14\n"
      "s_xor_b32 s97, 0x110b0534, s97\n"
      "s_bfe_u32 s97, s97, s43\n"
      "s_mul_i32 s97, s97, 3072\n"
      "v_cvt_pk_bf16_f32 v146, v62, v63\n"
      "v_cvt_pk_bf16_f32 v147, v64, v65\n"
      "v_mfma_f32_16x16x32_bf16 v[54:57], a[20:23], v[68:71], v[32:35]\n"
      "s_cbranch_vccz Lconv_h6\n"
      "s_barrier\n"
      "Lconv_h6:\n"
      "ds_read_b128 a[32:35], v86 offset:12288\n"
      "ds_read_b128 a[0:3], v84 offset:9216\n"
      "ds_read_b128 a[4:7], v85 offset:9216\n"
      "ds_read_b128 a[8:11], v86 offset:12352\n"
      "v_mfma_f32_16x16x32_bf16 v[32:35], a[20:23], v[72:75], v[32:35]\n"
      "ds_read_b128 a[12:15], v84 offset:9280\n"
      "ds_read_b128 a[16:19], v86 offset:3072\n"
      "v_accvgpr_write_b32 a218, v20\n"
      "v_accvgpr_write_b32 a219, v22\n"
      "v_cvt_pk_bf16_f32 v152, v24, v25\n"
      "v_mfma_f32_16x16x32_bf16 v[58:61], a[24:27], v[76:79], 0\n"
      "ds_read_b128 a[20:23], v84 offset:0\n"
      "ds_read_b128 a[24:27], v85 offset:0\n"
      "buffer_load_dwordx4 v8, s[28:31], s97 offen offset:0 lds\n"
      "v_cvt_pk_bf16_f32 v153, v26, v27\n"
      "v_mfma_f32_16x16x32_bf16 v[62:65], a[28:31], v[68:71], v[58:61]\n"
      "ds_read_b64_tr_b16 a[136:137], v87 offset:9216\n"
      "ds_read_b64_tr_b16 a[140:141], v88 offset:9216\n"
      "v_cvt_pk_bf16_f32 v148, v28, v29\n"
      "v_cvt_pk_bf16_f32 v149, v30, v31\n"
      "v_mfma_f32_16x16x32_bf16 v[58:61], a[28:31], v[72:75], v[58:61]\n"
      "s_waitcnt lgkmcnt(9)\n"
      "ds_read_b128 a[28:31], v86 offset:3136\n"
      "ds_read_b64_tr_b16 a[144:145], v87 offset:9280\n"
      "v_cvt_pk_bf16_f32 v156, v32, v33\n"
      "v_cvt_pk_bf16_f32 v157, v34, v35\n"
      "v_mfma_f32_16x16x32_bf16 v[20:23], a[32:35], v[76:79], 0\n"
      "s_waitcnt lgkmcnt(10)\n"
      "ds_read_b64_tr_b16 a[172:173], v87 offset:0\n"
      "ds_read_b64_tr_b16 a[176:177], v88 offset:0\n"
      "v_mfma_f32_16x16x32_bf16 v[24:27], a[0:3], v[68:71], v[20:23]\n"
      "ds_read_b64_tr_b16 a[180:181], v87 offset:64\n"
      "ds_read_b64_tr_b16 a[102:103], v87 offset:18464\n"
      "v_cvt_pk_bf16_f32 v54, v54, v55\n"
      "v_cvt_pk_bf16_f32 v56, v56, v57\n"
      "v_accvgpr_write_b32 a200, v54\n"
      "v_mfma_f32_16x16x32_bf16 v[20:23], a[0:3], v[72:75], v[20:23]\n"
      "ds_read_b128 a[0:3], v86 offset:21536\n"
      "ds_read_b64_tr_b16 a[106:107], v88 offset:18464\n"
      "buffer_load_dwordx4 v9, s[28:31], s97 offen offset:1024 lds\n"
      "v_accvgpr_write_b32 a201, v56\n"
      "v_cvt_pk_bf16_f32 v164, v58, v59\n"
      "v_mfma_f32_16x16x32_bf16 v[28:31], a[32:35], v[80:83], 0\n"
      "s_waitcnt lgkmcnt(10)\n"
      "ds_read_b128 a[32:35], v84 offset:64\n"
      "ds_read_b64_tr_b16 a[110:111], v87 offset:18528\n"
      "v_cvt_pk_bf16_f32 v165, v60, v61\n"
      "v_mfma_f32_16x16x32_bf16 v[32:35], a[4:7], v[68:71], v[28:31]\n"
      "ds_read_b64_tr_b16 a[138:139], v87 offset:36928\n"
      "ds_read_b64_tr_b16 a[142:143], v88 offset:36928\n"
      "v_cvt_pk_bf16_f32 v160, v62, v63\n"
      "v_cvt_pk_bf16_f32 v161, v64, v65\n"
      "v_mfma_f32_16x16x32_bf16 v[28:31], a[4:7], v[72:75], v[28:31]\n"
      "ds_read_b128 a[4:7], v84 offset:18464\n"
      "ds_read_b64_tr_b16 a[146:147], v87 offset:36992\n"
      "v_cvt_pk_bf16_f32 v168, v20, v21\n"
      "v_cvt_pk_bf16_f32 v169, v22, v23\n"
      "v_mfma_f32_16x16x32_bf16 v[54:57], a[8:11], v[76:79], 0\n"
      "ds_read_b128 a[8:11], v85 offset:18464\n"
      "ds_read_b64_tr_b16 a[174:175], v87 offset:55392\n"
      "buffer_load_dwordx4 v89, s[28:31], s97 offen offset:2048 lds\n"
      "v_mfma_f32_16x16x32_bf16 v[58:61], a[12:15], v[68:71], v[54:57]\n"
      "s_cmp_lt_u32 s40, 3\n"
      "s_cselect_b32 s97, 27680, 0\n"
      "ds_read_b64_tr_b16 a[178:179], v88 offset:55392\n"
      "ds_read_b64_tr_b16 a[182:183], v87 offset:55456\n"
      "s_add_u32 s97, s97, s92\n"
      "s_add_u32 m0, s97, 36928\n"
      "s_and_b32 s97, 0x2c000000, s14\n"
      "s_xor_b32 s97, 0x29231d17, s97\n"
      "s_bfe_u32 s97, s97, s43\n"
      "s_mul_i32 s97, s97, 3072\n"
      "v_cvt_pk_bf16_f32 v24, v24, v25\n"
      "v_cvt_pk_bf16_f32 v26, v26, v27\n"
      "v_accvgpr_write_b32 a208, v24\n"
      "v_mfma_f32_16x16x32_bf16 v[54:57], a[12:15], v[72:75], v[54:57]\n"
      "ds_read_b128 a[12:15], v86 offset:21600\n"
      "v_accvgpr_write_b32 a209, v26\n"
      "v_cvt_pk_bf16_f32 v176, v28, v29\n"
      "v_cvt_pk_bf16_f32 v177, v30, v31\n"
      "v_cvt_pk_bf16_f32 v172, v32, v33\n"
      "v_mfma_f32_16x16x32_bf16 v[62:65], a[16:19], v[76:79], 0\n"
      "v_cvt_pk_bf16_f32 v173, v34, v35\n"
      "v_cvt_pk_bf16_f32 v180, v58, v59\n"
      "v_mfma_f32_16x16x32_bf16 v[20:23], a[20:23], v[68:71], v[62:65]\n"
      "v_cvt_pk_bf16_f32 v54, v54, v55\n"
      "v_cvt_pk_bf16_f32 v56, v56, v57\n"
      "v_mfma_f32_16x16x32_bf16 v[62:65], a[20:23], v[72:75], v[62:65]\n"
      "ds_read_b128 a[20:23], v86 offset:40000\n"
      "buffer_load_dwordx4 v8, s[28:31], s97 offen offset:0 lds\n"
      "v_accvgpr_write_b32 a228, v54\n"
      "v_mfma_f32_16x16x32_bf16 v[24:27], a[16:19], v[80:83], 0\n"
      "ds_read_b128 a[16:19], v84 offset:18528\n"
      "v_accvgpr_write_b32 a229, v56\n"
      "v_mfma_f32_16x16x32_bf16 v[28:31], a[24:27], v[68:71], v[24:27]\n"
      "v_cvt_pk_bf16_f32 v181, v60, v61\n"
      "v_cvt_pk_bf16_f32 v188, v62, v63\n"
      "v_mfma_f32_16x16x32_bf16 v[24:27], a[24:27], v[72:75], v[24:27]\n"
      "s_waitcnt lgkmcnt(2)\n"
      "ds_read_b128 a[24:27], v84 offset:36928\n"
      "v_cvt_pk_bf16_f32 v189, v64, v65\n"
      "v_cvt_pk_bf16_f32 v184, v20, v21\n"
      "v_cvt_pk_bf16_f32 v185, v22, v23\n"
      "v_mfma_f32_16x16x32_bf16 v[32:35], a[28:31], v[76:79], 0\n"
      "ds_read_b128 a[28:31], v85 offset:36928\n"
      "buffer_load_dwordx4 v9, s[28:31], s97 offen offset:1024 lds\n"
      "v_mfma_f32_16x16x32_bf16 v[54:57], a[32:35], v[68:71], v[32:35]\n"
      "v_cvt_pk_bf16_f32 v24, v24, v25\n"
      "v_cvt_pk_bf16_f32 v26, v26, v27\n"
      "v_mfma_f32_16x16x32_bf16 v[32:35], a[32:35], v[72:75], v[32:35]\n"
      "ds_read_b128 a[32:35], v86 offset:40064\n"
      "v_accvgpr_write_b32 a236, v24\n"
      "v_accvgpr_write_b32 a237, v26\n"
      "v_mfma_f32_16x16x32_bf16 v[58:61], a[0:3], v[76:79], 0\n"
      "v_cvt_pk_bf16_f32 v192, v28, v29\n"
      "v_cvt_pk_bf16_f32 v193, v30, v31\n"
      "v_mfma_f32_16x16x32_bf16 v[62:65], a[4:7], v[68:71], v[58:61]\n"
      "v_cvt_pk_bf16_f32 v196, v54, v55\n"
      "v_cvt_pk_bf16_f32 v200, v32, v33\n"
      "v_mfma_f32_16x16x32_bf16 v[58:61], a[4:7], v[72:75], v[58:61]\n"
      "ds_read_b128 a[4:7], v86 offset:58464\n"
      "buffer_load_dwordx4 v89, s[28:31], s97 offen offset:2048 lds\n"
      "v_mfma_f32_16x16x32_bf16 v[20:23], a[0:3], v[80:83], 0\n"
      "s_cmp_lt_u32 s40, 2\n"
      "s_cselect_b32 s97, 6144, 0\n"
      "ds_read_b128 a[0:3], v84 offset:36992\n"
      "s_add_u32 s97, s97, s92\n"
      "s_add_u32 m0, s97, 43072\n"
      "s_and_b32 s97, 0x24242424, s14\n"
      "s_xor_b32 s97, 0x352f352f, s97\n"
      "s_bfe_u32 s97, s97, s43\n"
      "s_mul_i32 s97, s97, 3072\n"
      "v_cvt_pk_bf16_f32 v201, v34, v35\n"
      "v_mfma_f32_16x16x32_bf16 v[24:27], a[8:11], v[68:71], v[20:23]\n"
      "v_cvt_pk_bf16_f32 v197, v56, v57\n"
      "v_cvt_pk_bf16_f32 v154, v58, v59\n"
      "v_cvt_pk_bf16_f32 v155, v60, v61\n"
      "v_mfma_f32_16x16x32_bf16 v[20:23], a[8:11], v[72:75], v[20:23]\n"
      "ds_read_b128 a[8:11], v84 offset:55392\n"
      "v_cvt_pk_bf16_f32 v150, v62, v63\n"
      "v_cvt_pk_bf16_f32 v151, v64, v65\n"
      "v_mfma_f32_16x16x32_bf16 v[28:31], a[12:15], v[76:79], 0\n"
      "s_waitcnt lgkmcnt(3)\n"
      "ds_read_b128 a[12:15], v85 offset:55392\n"
      "buffer_load_dwordx4 v8, s[28:31], s97 offen offset:0 lds\n"
      "v_cvt_pk_bf16_f32 v24, v24, v25\n"
      "v_mfma_f32_16x16x32_bf16 v[32:35], a[16:19], v[68:71], v[28:31]\n"
      "v_cvt_pk_bf16_f32 v158, v20, v21\n"
      "v_cvt_pk_bf16_f32 v159, v22, v23\n"
      "v_mfma_f32_16x16x32_bf16 v[28:31], a[16:19], v[72:75], v[28:31]\n"
      "ds_read_b128 a[16:19], v86 offset:58528\n"
      "v_cvt_pk_bf16_f32 v26, v26, v27\n"
      "v_accvgpr_write_b32 a202, v24\n"
      "v_accvgpr_write_b32 a203, v26\n"
      "v_mfma_f32_16x16x32_bf16 v[54:57], a[20:23], v[76:79], 0\n"
      "v_cvt_pk_bf16_f32 v162, v32, v33\n"
      "v_cvt_pk_bf16_f32 v163, v34, v35\n"
      "v_mfma_f32_16x16x32_bf16 v[58:61], a[24:27], v[68:71], v[54:57]\n"
      "v_cvt_pk_bf16_f32 v166, v28, v29\n"
      "v_cvt_pk_bf16_f32 v167, v30, v31\n"
      "v_mfma_f32_16x16x32_bf16 v[54:57], a[24:27], v[72:75], v[54:57]\n"
      "buffer_load_dwordx4 v9, s[28:31], s97 offen offset:1024 lds\n"
      "v_add_u32 v86, 46144, v86\n"
      "v_add_u32 v85, 46144, v85\n"
      "v_mfma_f32_16x16x32_bf16 v[62:65], a[20:23], v[80:83], 0\n"
      "ds_read_b128 a[20:23], v84 offset:55456\n"
      "v_cvt_pk_bf16_f32 v58, v58, v59\n"
      "v_mfma_f32_16x16x32_bf16 v[20:23], a[28:31], v[68:71], v[62:65]\n"
      "v_cvt_pk_bf16_f32 v60, v60, v61\n"
      "v_cvt_pk_bf16_f32 v170, v54, v55\n"
      "v_mfma_f32_16x16x32_bf16 v[62:65], a[28:31], v[72:75], v[62:65]\n"
      "v_cvt_pk_bf16_f32 v171, v56, v57\n"
      "v_accvgpr_write_b32 a210, v58\n"
      "v_mfma_f32_16x16x32_bf16 v[24:27], a[32:35], v[76:79], 0\n"
      "s_waitcnt lgkmcnt(1)\n"
      "buffer_load_dwordx4 v89, s[28:31], s97 offen offset:2048 lds\n"
      "v_accvgpr_write_b32 a211, v60\n"
      "v_mfma_f32_16x16x32_bf16 v[28:31], a[0:3], v[68:71], v[24:27]\n"
      "s_add_u32 m0, s46, 39840\n"
      "s_add_u32 s51, s51, s12\n"
      "v_cvt_pk_bf16_f32 v178, v62, v63\n"
      "v_cvt_pk_bf16_f32 v179, v64, v65\n"
      "v_mfma_f32_16x16x32_bf16 v[24:27], a[0:3], v[72:75], v[24:27]\n"
      "v_cvt_pk_bf16_f32 v174, v20, v21\n"
      "v_cvt_pk_bf16_f32 v175, v22, v23\n"
      "v_add_u32 v84, 46144, v84\n"
      "v_mfma_f32_16x16x32_bf16 v[32:35], a[4:7], v[76:79], 0\n"
      "v_cvt_pk_bf16_f32 v182, v28, v29\n"
      "v_cvt_pk_bf16_f32 v183, v30, v31\n"
      "v_mfma_f32_16x16x32_bf16 v[54:57], a[8:11], v[68:71], v[32:35]\n"
      "v_add_u32 v87, 46144, v87\n"
      "v_cvt_pk_bf16_f32 v24, v24, v25\n"
      "v_mfma_f32_16x16x32_bf16 v[32:35], a[8:11], v[72:75], v[32:35]\n"
      "s_waitcnt lgkmcnt(0)\n"
      "s_waitcnt vmcnt(6)\n"
      "s_cbranch_vccnz Lconv_h7\n"
      "s_barrier\n"
      "Lconv_h7:\n"
      "buffer_load_dwordx4 v16, s[32:35], s51 offen lds\n"
      "v_mfma_f32_16x16x32_bf16 v[58:61], a[4:7], v[80:83], 0\n"
      "s_add_u32 m0, s46, 39840\n"
      "v_cvt_pk_bf16_f32 v26, v26, v27\n"
      "v_accvgpr_write_b32 a230, v24\n"
      "v_mfma_f32_16x16x32_bf16 v[62:65], a[12:15], v[68:71], v[58:61]\n"
      "s_cbranch_vccz Lconv_h8\n"
      "s_barrier\n"
      "Lconv_h8:\n"
      "ds_read_b128 a[48:51], v2 offset:26560\n"
      "ds_read_b128 a[52:55], v3 offset:26560\n"
      "ds_read_b128 a[56:59], v4 offset:26560\n"
      "ds_read_b128 a[36:39], v2 offset:13280\n"
      "buffer_load_dwordx4 v17, s[32:35], s51 offen offset:1024 lds\n"
      "v_mfma_f32_16x16x32_bf16 v[58:61], a[12:15], v[72:75], v[58:61]\n"
      "s_add_u32 m0, s46, 39840\n"
      "ds_read_b128 a[40:43], v3 offset:13280\n"
      "ds_read_b128 a[44:47], v4 offset:13280\n"
      "buffer_load_dwordx4 v18, s[32:35], s51 offen offset:2048 lds\n"
      "v_accvgpr_write_b32 a231, v26\n"
      "v_cvt_pk_bf16_f32 v186, v54, v55\n"
      "v_mfma_f32_16x16x32_bf16 v[20:23], a[16:19], v[76:79], 0\n"
      "s_sub_u32 m0, s94, s59\n"
      "s_add_u32 s96, s51, s15\n"
      "ds_read_b128 a[24:27], v86 offset:30752\n"
      "ds_read_b128 a[28:31], v84 offset:27680\n"
      "buffer_load_dwordx4 v19, s[32:35], s96 offen lds\n"
      "v_cvt_pk_bf16_f32 v187, v56, v57\n"
      "v_cvt_pk_bf16_f32 v58, v58, v59\n"
      "v_mfma_f32_16x16x32_bf16 v[24:27], a[20:23], v[68:71], v[20:23]\n"
      "s_add_u32 m0, s46, 0\n"
      "s_add_u32 s51, s51, s12\n"
      "ds_read_b128 a[32:35], v85 offset:27680\n"
      "ds_read_b128 a[0:3], v86 offset:30816\n"
      "buffer_load_dwordx4 v16, s[32:35], s51 offen lds\n"
      "v_accvgpr_write_b32 a238, v58\n"
      "v_cvt_pk_bf16_f32 v194, v62, v63\n"
      "v_mfma_f32_16x16x32_bf16 v[20:23], a[20:23], v[72:75], v[20:23]\n"
      "s_waitcnt lgkmcnt(9)\n"
      "s_add_u32 m0, s46, 0\n"
      "ds_read_b128 a[4:7], v84 offset:27744\n"
      "ds_read_b128 a[8:11], v86 offset:39968\n"
      "buffer_load_dwordx4 v17, s[32:35], s51 offen offset:1024 lds\n"
      "v_cvt_pk_bf16_f32 v60, v60, v61\n"
      "v_accvgpr_write_b32 a239, v60\n"
      "v_mfma_f32_16x16x32_bf16 v[52:55], a[48:51], a[72:75], 0\n"
      "s_waitcnt lgkmcnt(10)\n"
      "s_add_u32 m0, s46, 0\n"
      "ds_read_b128 a[48:51], v2 offset:13344\n"
      "ds_read_b128 a[12:15], v84 offset:36896\n"
      "buffer_load_dwordx4 v18, s[32:35], s51 offen offset:2048 lds\n"
      "v_mfma_f32_16x16x32_bf16 v[56:59], a[52:55], a[76:79], 0\n"
      "s_waitcnt lgkmcnt(10)\n"
      "s_mov_b32 m0, s18\n"
      "ds_read_b128 a[52:55], v3 offset:13344\n"
      "ds_read_b128 a[16:19], v85 offset:36896\n"
      "v_cvt_pk_bf16_f32 v190, v32, v33\n"
      "v_cvt_pk_bf16_f32 v191, v34, v35\n"
      "v_cvt_pk_bf16_f32 v195, v64, v65\n"
      "v_mfma_f32_16x16x32_bf16 v[60:63], a[56:59], a[80:83], v[52:55]\n"
      "ds_read_b128 a[56:59], v4 offset:13344\n"
      "ds_read_b128 a[20:23], v86 offset:40032\n"
      "v_fma_f32 v64, s44, v52, -v56\n"
      "v_fma_f32 v65, s44, v53, -v57\n"
      "v_fma_f32 v66, s44, v54, -v58\n"
      "v_mfma_f32_16x16x32_bf16 v[36:39], a[36:39], a[72:75], 0\n"
      "s_waitcnt lgkmcnt(10)\n"
      "ds_read_b128 a[36:39], v2 offset:26624\n"
      "ds_read_b64_tr_b16 a[112:113], v87 offset:27680\n"
      "v_fma_f32 v67, s44, v55, -v59\n"
      "v_fmac_f32_e64 v56, s44, v52\n"
      "v_fmac_f32_e64 v57, s44, v53\n"
      "v_mfma_f32_16x16x32_bf16 v[40:43], a[40:43], a[76:79], 0\n"
      "ds_read_b128 a[40:43], v3 offset:26624\n"
      "ds_read_b64_tr_b16 a[120:121], v87 offset:27744\n"
      "v_fmac_f32_e64 v58, s44, v54\n"
      "v_fmac_f32_e64 v59, s44, v55\n"
      "v_cvt_pk_bf16_f32 v56, v56, v57\n"
      "v_mfma_f32_16x16x32_bf16 v[44:47], a[44:47], a[80:83], v[36:39]\n"
      "ds_write_addtid_b32 v56 offset:5376\n"
      "ds_read_b128 a[44:47], v4 offset:26624\n"
      "v_cvt_pk_bf16_f32 v57, v58, v59\n"
      "v_cvt_pk_bf16_f32 v60, v60, v61\n"
      "v_cvt_pk_bf16_f32 v61, v62, v63\n"
      "v_mfma_f32_16x16x32_bf16 v[28:31], a[24:27], v[76:79], 0\n"
      "ds_write_addtid_b32 v57 offset:42304\n"
      "ds_write_addtid_b32 v60 offset:4608\n"
      "ds_write_addtid_b32 v61 offset:41536\n"
      "v_cvt_pk_bf16_f32 v64, v64, v65\n"
      "v_cvt_pk_bf16_f32 v65, v66, v67\n"
      "v_mfma_f32_16x16x32_bf16 v[32:35], a[28:31], v[68:71], v[28:31]\n"
      "ds_write_addtid_b32 v64 offset:6144\n"
      "ds_write_addtid_b32 v65 offset:43072\n"
      "ds_read_b64_tr_b16 a[148:149], v87 offset:36896\n"
      "s_mov_b32 m0, s18\n"
      "v_cvt_pk_bf16_f32 v202, v20, v21\n"
      "v_cvt_pk_bf16_f32 v203, v22, v23\n"
      "v_mfma_f32_16x16x32_bf16 v[28:31], a[28:31], v[72:75], v[28:31]\n"
      "ds_read_b128 a[28:31], v86 offset:49184\n"
      "ds_read_b64_tr_b16 a[156:157], v87 offset:36960\n"
      "v_fma_f32 v48, s44, v36, -v40\n"
      "v_fma_f32 v49, s44, v37, -v41\n"
      "v_fma_f32 v50, s44, v38, -v42\n"
      "v_mfma_f32_16x16x32_bf16 v[54:57], a[24:27], v[80:83], 0\n"
      "s_waitcnt lgkmcnt(12)\n"
      "ds_read_b128 a[24:27], v84 offset:36960\n"
      "ds_read_b64_tr_b16 a[184:185], v87 offset:46112\n"
      "v_fma_f32 v51, s44, v39, -v43\n"
      "v_fmac_f32_e64 v40, s44, v36\n"
      "v_fmac_f32_e64 v41, s44, v37\n"
      "v_mfma_f32_16x16x32_bf16 v[58:61], a[32:35], v[68:71], v[54:57]\n"
      "ds_read_b64_tr_b16 a[192:193], v87 offset:46176\n"
      "ds_read_b64_tr_b16 a[114:115], v87 offset:55328\n"
      "v_fmac_f32_e64 v42, s44, v38\n"
      "v_fmac_f32_e64 v43, s44, v39\n"
      "v_cvt_pk_bf16_f32 v40, v40, v41\n"
      "v_mfma_f32_16x16x32_bf16 v[54:57], a[32:35], v[72:75], v[54:57]\n"
      "ds_write_addtid_b32 v40 offset:768\n"
      "ds_read_b128 a[32:35], v84 offset:46112\n"
      "v_cvt_pk_bf16_f32 v41, v42, v43\n"
      "v_cvt_pk_bf16_f32 v44, v44, v45\n"
      "v_cvt_pk_bf16_f32 v45, v46, v47\n"
      "v_mfma_f32_16x16x32_bf16 v[62:65], a[0:3], v[76:79], 0\n"
      "ds_write_addtid_b32 v41 offset:37696\n"
      "ds_write_addtid_b32 v44 offset:0\n"
      "ds_write_addtid_b32 v45 offset:36928\n"
      "v_cvt_pk_bf16_f32 v212, v54, v55\n"
      "v_cvt_pk_bf16_f32 v208, v58, v59\n"
      "v_mfma_f32_16x16x32_bf16 v[20:23], a[4:7], v[68:71], v[62:65]\n"
      "ds_read_b128 a[0:3], v85 offset:46112\n"
      "ds_read_b64_tr_b16 a[122:123], v87 offset:55392\n"
      "v_cvt_pk_bf16_f32 v213, v56, v57\n"
      "v_cvt_pk_bf16_f32 v209, v60, v61\n"
      "v_cvt_pk_bf16_f32 v198, v24, v25\n"
      "v_mfma_f32_16x16x32_bf16 v[62:65], a[4:7], v[72:75], v[62:65]\n"
      "ds_read_b128 a[4:7], v86 offset:49248\n"
      "v_cvt_pk_bf16_f32 v199, v26, v27\n"
      "v_cvt_pk_bf16_f32 v48, v48, v49\n"
      "v_cvt_pk_bf16_f32 v49, v50, v51\n"
      "v_cvt_pk_bf16_f32 v28, v28, v29\n"
      "v_mfma_f32_16x16x32_bf16 v[36:39], a[36:39], a[72:75], 0\n"
      "s_waitcnt lgkmcnt(10)\n"
      "ds_write_addtid_b32 v48 offset:1536\n"
      "ds_write_addtid_b32 v49 offset:38464\n"
      "ds_read_b128 a[36:39], v2 offset:13408\n"
      "s_mov_b32 m0, s18\n"
      "v_cvt_pk_bf16_f32 v62, v62, v63\n"
      "v_accvgpr_write_b32 a224, v62\n"
      "v_mfma_f32_16x16x32_bf16 v[40:43], a[40:43], a[76:79], 0\n"
      "ds_read_b128 a[40:43], v3 offset:13408\n"
      "v_cvt_pk_bf16_f32 v30, v30, v31\n"
      "v_accvgpr_write_b32 a220, v28\n"
      "v_accvgpr_write_b32 a221, v30\n"
      "v_cvt_pk_bf16_f32 v204, v32, v33\n"
      "v_mfma_f32_16x16x32_bf16 v[44:47], a[44:47], a[80:83], v[36:39]\n"
      "ds_read_b128 a[44:47], v4 offset:13408\n"
      "v_cvt_pk_bf16_f32 v205, v34, v35\n"
      "v_fma_f32 v48, s44, v36, -v40\n"
      "v_fma_f32 v49, s44, v37, -v41\n"
      "v_fma_f32 v50, s44, v38, -v42\n"
      "v_mfma_f32_16x16x32_bf16 v[52:55], a[48:51], a[72:75], 0\n"
      "ds_read_b128 a[48:51], v2 offset:26688\n"
      "v_fma_f32 v51, s44, v39, -v43\n"
      "v_fmac_f32_e64 v40, s44, v36\n"
      "v_fmac_f32_e64 v41, s44, v37\n"
      "v_fmac_f32_e64 v42, s44, v38\n"
      "v_mfma_f32_16x16x32_bf16 v[56:59], a[52:55], a[76:79], 0\n"
      "ds_read_b128 a[52:55], v3 offset:26688\n"
      "v_fmac_f32_e64 v43, s44, v39\n"
      "v_cvt_pk_bf16_f32 v48, v48, v49\n"
      "v_cvt_pk_bf16_f32 v49, v50, v51\n"
      "v_cvt_pk_bf16_f32 v40, v40, v41\n"
      "v_mfma_f32_16x16x32_bf16 v[60:63], a[56:59], a[80:83], v[52:55]\n"
      "ds_write_addtid_b32 v48 offset:6400\n"
      "ds_write_addtid_b32 v49 offset:43328\n"
      "ds_write_addtid_b32 v40 offset:5632\n"
      "v_cvt_pk_bf16_f32 v41, v42, v43\n"
      "v_cvt_pk_bf16_f32 v44, v44, v45\n"
      "v_mfma_f32_16x16x32_bf16 v[24:27], a[8:11], v[76:79], 0\n"
      "ds_write_addtid_b32 v41 offset:42560\n"
      "ds_write_addtid_b32 v44 offset:4864\n"
      "ds_read_b128 a[56:59], v4 offset:26688\n"
      "v_cvt_pk_bf16_f32 v45, v46, v47\n"
      "v_cvt_pk_bf16_f32 v64, v64, v65\n"
      "v_mfma_f32_16x16x32_bf16 v[28:31], a[12:15], v[68:71], v[24:27]\n"
      "ds_write_addtid_b32 v45 offset:41792\n"
      "s_mov_b32 m0, s18\n"
      "v_accvgpr_write_b32 a225, v64\n"
      "v_fma_f32 v64, s44, v52, -v56\n"
      "v_fma_f32 v65, s44, v53, -v57\n"
      "v_fma_f32 v66, s44, v54, -v58\n"
      "v_mfma_f32_16x16x32_bf16 v[24:27], a[12:15], v[72:75], v[24:27]\n"
      "ds_read_b128 a[12:15], v86 offset:58400\n"
      "v_fma_f32 v67, s44, v55, -v59\n"
      "v_fmac_f32_e64 v56, s44, v52\n"
      "v_fmac_f32_e64 v57, s44, v53\n"
      "v_fmac_f32_e64 v58, s44, v54\n"
      "v_mfma_f32_16x16x32_bf16 v[32:35], a[8:11], v[80:83], 0\n"
      "ds_read_b128 a[8:11], v84 offset:46176\n"
      "v_fmac_f32_e64 v59, s44, v55\n"
      "v_cvt_pk_bf16_f32 v56, v56, v57\n"
      "v_cvt_pk_bf16_f32 v57, v58, v59\n"
      "v_cvt_pk_bf16_f32 v60, v60, v61\n"
      "ds_write_addtid_b32 v56 offset:1024\n"
      "ds_write_addtid_b32 v57 offset:37952\n"
      "v_mfma_f32_16x16x32_bf16 v[54:57], a[16:19], v[68:71], v[32:35]\n"
      "ds_write_addtid_b32 v60 offset:256\n"
      "v_cvt_pk_bf16_f32 v61, v62, v63\n"
      "v_cvt_pk_bf16_f32 v64, v64, v65\n"
      "v_cvt_pk_bf16_f32 v65, v66, v67\n"
      "v_cvt_pk_bf16_f32 v216, v20, v21\n"
      "v_mfma_f32_16x16x32_bf16 v[32:35], a[16:19], v[72:75], v[32:35]\n"
      "ds_write_addtid_b32 v61 offset:37184\n"
      "ds_write_addtid_b32 v64 offset:1792\n"
      "ds_write_addtid_b32 v65 offset:38720\n"
      "s_mov_b32 m0, s18\n"
      "v_cvt_pk_bf16_f32 v228, v54, v55\n"
      "v_cvt_pk_bf16_f32 v229, v56, v57\n"
      "v_mfma_f32_16x16x32_bf16 v[58:61], a[20:23], v[76:79], 0\n"
      "ds_read_b128 a[16:19], v84 offset:55328\n"
      "ds_read_b128 a[20:23], v85 offset:55328\n"
      "v_cvt_pk_bf16_f32 v217, v22, v23\n"
      "v_cvt_pk_bf16_f32 v224, v24, v25\n"
      "v_mfma_f32_16x16x32_bf16 v[62:65], a[24:27], v[68:71], v[58:61]\n"
      "v_cvt_pk_bf16_f32 v225, v26, v27\n"
      "v_cvt_pk_bf16_f32 v220, v28, v29\n"
      "v_mfma_f32_16x16x32_bf16 v[58:61], a[24:27], v[72:75], v[58:61]\n"
      "s_waitcnt lgkmcnt(9)\n"
      "ds_read_b128 a[24:27], v86 offset:58464\n"
      "v_cvt_pk_bf16_f32 v221, v30, v31\n"
      "v_cvt_pk_bf16_f32 v32, v32, v33\n"
      "v_mfma_f32_16x16x32_bf16 v[52:55], a[48:51], a[72:75], 0\n"
      "v_cvt_pk_bf16_f32 v232, v62, v63\n"
      "v_cvt_pk_bf16_f32 v34, v34, v35\n"
      "s_nop 0\n"
      "v_cvt_pk_bf16_f32 v236, v58, v59\n"
      "v_mfma_f32_16x16x32_bf16 v[56:59], a[52:55], a[76:79], 0\n"
      "v_cvt_pk_bf16_f32 v237, v60, v61\n"
      "v_accvgpr_write_b32 a232, v32\n"
      "v_accvgpr_write_b32 a233, v34\n"
      "v_cvt_pk_bf16_f32 v233, v64, v65\n"
      "v_add_u32 v88, 46144, v88\n"
      "v_mfma_f32_16x16x32_bf16 v[60:63], a[56:59], a[80:83], v[52:55]\n"
      "ds_read_b64_tr_b16 a[116:117], v88 offset:27680\n"
      "ds_read_b64_tr_b16 a[152:153], v88 offset:36896\n"
      "v_fma_f32 v64, s44, v52, -v56\n"
      "v_fma_f32 v65, s44, v53, -v57\n"
      "v_fma_f32 v66, s44, v54, -v58\n"
      "v_mfma_f32_16x16x32_bf16 v[36:39], a[36:39], a[72:75], 0\n"
      "ds_read_b64_tr_b16 a[188:189], v88 offset:46112\n"
      "ds_read_b64_tr_b16 a[118:119], v88 offset:55328\n"
      "v_fma_f32 v67, s44, v55, -v59\n"
      "v_fmac_f32_e64 v56, s44, v52\n"
      "v_fmac_f32_e64 v57, s44, v53\n"
      "v_mfma_f32_16x16x32_bf16 v[40:43], a[40:43], a[76:79], 0\n"
      "v_fmac_f32_e64 v58, s44, v54\n"
      "v_fmac_f32_e64 v59, s44, v55\n"
      "v_cvt_pk_bf16_f32 v56, v56, v57\n"
      "v_cvt_pk_bf16_f32 v57, v58, v59\n"
      "v_cvt_pk_bf16_f32 v60, v60, v61\n"
      "v_mfma_f32_16x16x32_bf16 v[44:47], a[44:47], a[80:83], v[36:39]\n"
      "Lconv_p2:\n"
      "s_cbranch_execz Lconv_r0\n"
      "s_nop 0\n"
      "ds_write_addtid_b32 v56 offset:5888\n"
      "ds_write_addtid_b32 v57 offset:42816\n"
      "ds_write_addtid_b32 v60 offset:5120\n"
      "v_cvt_pk_bf16_f32 v61, v62, v63\n"
      "v_cvt_pk_bf16_f32 v64, v64, v65\n"
      "v_mfma_f32_16x16x32_bf16 v[20:23], a[28:31], v[76:79], 0\n"
      "ds_write_addtid_b32 v61 offset:42048\n"
      "ds_write_addtid_b32 v64 offset:6656\n"
      "v_cvt_pk_bf16_f32 v65, v66, v67\n"
      "v_fma_f32 v48, s44, v36, -v40\n"
      "v_fma_f32 v49, s44, v37, -v41\n"
      "v_mfma_f32_16x16x32_bf16 v[24:27], a[32:35], v[68:71], v[20:23]\n"
      "ds_write_addtid_b32 v65 offset:43584\n"
      "s_mov_b32 m0, s18\n"
      "v_fma_f32 v50, s44, v38, -v42\n"
      "v_fma_f32 v51, s44, v39, -v43\n"
      "v_fmac_f32_e64 v40, s44, v36\n"
      "v_fmac_f32_e64 v41, s44, v37\n"
      "v_mfma_f32_16x16x32_bf16 v[20:23], a[32:35], v[72:75], v[20:23]\n"
      "v_fmac_f32_e64 v42, s44, v38\n"
      "v_fmac_f32_e64 v43, s44, v39\n"
      "v_cvt_pk_bf16_f32 v48, v48, v49\n"
      "v_cvt_pk_bf16_f32 v49, v50, v51\n"
      "v_cvt_pk_bf16_f32 v40, v40, v41\n"
      "v_mfma_f32_16x16x32_bf16 v[28:31], a[28:31], v[80:83], 0\n"
      "ds_write_addtid_b32 v48 offset:2048\n"
      "ds_write_addtid_b32 v49 offset:38976\n"
      "ds_write_addtid_b32 v40 offset:1280\n"
      "v_cvt_pk_bf16_f32 v41, v42, v43\n"
      "v_mfma_f32_16x16x32_bf16 v[32:35], a[0:3], v[68:71], v[28:31]\n"
      "ds_write_addtid_b32 v41 offset:38208\n"
      "ds_read_b128 a[28:31], v84 offset:55392\n"
      "v_cvt_pk_bf16_f32 v44, v44, v45\n"
      "v_cvt_pk_bf16_f32 v45, v46, v47\n"
      "v_mfma_f32_16x16x32_bf16 v[28:31], a[0:3], v[72:75], v[28:31]\n"
      "ds_write_addtid_b32 v44 offset:512\n"
      "ds_write_addtid_b32 v45 offset:37440\n"
      "s_add_u32 m0, s46, 13280\n"
      "s_add_u32 s51, s51, s12\n"
      "v_cvt_pk_bf16_f32 v20, v20, v21\n"
      "v_cvt_pk_bf16_f32 v22, v22, v23\n"
      "v_mfma_f32_16x16x32_bf16 v[54:57], a[4:7], v[76:79], 0\n"
      "s_waitcnt lgkmcnt(13)\n"
      "v_accvgpr_write_b32 a240, v20\n"
      "v_accvgpr_write_b32 a241, v22\n"
      "v_mfma_f32_16x16x32_bf16 v[58:61], a[8:11], v[68:71], v[54:57]\n"
      "v_cvt_pk_bf16_f32 v240, v24, v25\n"
      "v_cvt_pk_bf16_f32 v241, v26, v27\n"
      "v_mfma_f32_16x16x32_bf16 v[54:57], a[8:11], v[72:75], v[54:57]\n"
      "v_cvt_pk_bf16_f32 v248, v28, v29\n"
      "v_cvt_pk_bf16_f32 v249, v30, v31\n"
      "v_mfma_f32_16x16x32_bf16 v[62:65], a[12:15], v[76:79], 0\n"
      "v_cvt_pk_bf16_f32 v244, v32, v33\n"
      "v_cvt_pk_bf16_f32 v245, v34, v35\n"
      "v_mfma_f32_16x16x32_bf16 v[20:23], a[16:19], v[68:71], v[62:65]\n"
      "v_cvt_pk_bf16_f32 v252, v58, v59\n"
      "v_cvt_pk_bf16_f32 v253, v60, v61\n"
      "v_mfma_f32_16x16x32_bf16 v[62:65], a[16:19], v[72:75], v[62:65]\n"
      "s_waitcnt lgkmcnt(0)\n"
      "s_waitcnt vmcnt(7)\n"
      "s_cbranch_vccnz Lconv_h9\n"
      "s_barrier\n"
      "Lconv_h9:\n"
      "v_cvt_pk_bf16_f32 v54, v54, v55\n"
      "v_cvt_pk_bf16_f32 v56, v56, v57\n"
      "v_mfma_f32_16x16x32_bf16 v[24:27], a[12:15], v[80:83], 0\n"
      "v_accvgpr_write_b32 a244, v54\n"
      "v_accvgpr_write_b32 a245, v56\n"
      "v_mfma_f32_16x16x32_bf16 v[28:31], a[20:23], v[68:71], v[24:27]\n"
      "s_cbranch_vccz Lconv_h10\n"
      "s_barrier\n"
      "Lconv_h10:\n"
      "ds_read_b128 a[32:35], v86 offset:21536\n"
      "ds_read_b128 a[0:3], v84 offset:18464\n"
      "ds_read_b128 a[4:7], v85 offset:18464\n"
      "ds_read_b128 a[8:11], v86 offset:21600\n"
      "v_mfma_f32_16x16x32_bf16 v[24:27], a[20:23], v[72:75], v[24:27]\n"
      "ds_read_b128 a[12:15], v84 offset:18528\n"
      "ds_read_b128 a[16:19], v86 offset:3072\n"
      "v_cvt_pk_bf16_f32 v62, v62, v63\n"
      "v_cvt_pk_bf16_f32 v64, v64, v65\n"
      "v_mfma_f32_16x16x32_bf16 v[32:35], a[24:27], v[76:79], 0\n"
      "ds_read_b128 a[20:23], v84 offset:0\n"
      "ds_read_b128 a[24:27], v85 offset:0\n"
      "v_mfma_f32_16x16x32_bf16 v[54:57], a[28:31], v[68:71], v[32:35]\n"
      "ds_read_b64_tr_b16 a[150:151], v87 offset:18464\n"
      "ds_read_b64_tr_b16 a[154:155], v88 offset:18464\n"
      "v_accvgpr_write_b32 a222, v62\n"
      "v_accvgpr_write_b32 a223, v64\n"
      "v_mfma_f32_16x16x32_bf16 v[32:35], a[28:31], v[72:75], v[32:35]\n"
      "s_waitcnt lgkmcnt(9)\n"
      "ds_read_b64_tr_b16 a[158:159], v87 offset:18528\n"
      "ds_read_b64_tr_b16 a[186:187], v87 offset:0\n"
      "buffer_load_dwordx4 v16, s[32:35], s51 offen lds\n"
      "v_cvt_pk_bf16_f32 v206, v20, v21\n"
      "v_cvt_pk_bf16_f32 v207, v22, v23\n"
      "v_mfma_f32_16x16x32_bf16 v[58:61], a[32:35], v[76:79], 0\n"
      "s_waitcnt lgkmcnt(10)\n"
      "s_add_u32 m0, s46, 13280\n"
      "ds_read_b64_tr_b16 a[190:191], v88 offset:0\n"
      "ds_read_b128 a[28:31], v86 offset:3136\n"
      "buffer_load_dwordx4 v17, s[32:35], s51 offen offset:1024 lds\n"
      "v_cvt_pk_bf16_f32 v214, v24, v25\n"
      "v_cvt_pk_bf16_f32 v215, v26, v27\n"
      "v_mfma_f32_16x16x32_bf16 v[62:65], a[0:3], v[68:71], v[58:61]\n"
      "s_add_u32 m0, s46, 13280\n"
      "s_add_u32 s96, s51, s15\n"
      "ds_read_b64_tr_b16 a[194:195], v87 offset:64\n"
      "buffer_load_dwordx4 v18, s[32:35], s51 offen offset:2048 lds\n"
      "v_cvt_pk_bf16_f32 v210, v28, v29\n"
      "v_cvt_pk_bf16_f32 v211, v30, v31\n"
      "v_mfma_f32_16x16x32_bf16 v[58:61], a[0:3], v[72:75], v[58:61]\n"
      "s_mov_b32 m0, s59\n"
      "v_cvt_pk_bf16_f32 v32, v32, v33\n"
      "v_cvt_pk_bf16_f32 v34, v34, v35\n"
      "v_mfma_f32_16x16x32_bf16 v[20:23], a[32:35], v[80:83], 0\n"
      "s_waitcnt lgkmcnt(7)\n"
      "ds_read_b128 a[32:35], v84 offset:64\n"
      "buffer_load_dwordx4 v19, s[32:35], s96 offen lds\n"
      "v_mfma_f32_16x16x32_bf16 v[24:27], a[4:7], v[68:71], v[20:23]\n"
      "s_add_u32 m0, s46, 26560\n"
      "s_add_u32 s51, s51, s12\n"
      "buffer_load_dwordx4 v16, s[32:35], s51 offen lds\n"
      "v_accvgpr_write_b32 a226, v32\n"
      "v_accvgpr_write_b32 a227, v34\n"
      "v_mfma_f32_16x16x32_bf16 v[20:23], a[4:7], v[72:75], v[20:23]\n"
      "s_add_u32 m0, s46, 26560\n"
      "v_cvt_pk_bf16_f32 v218, v54, v55\n"
      "v_cvt_pk_bf16_f32 v219, v56, v57\n"
      "v_cvt_pk_bf16_f32 v226, v58, v59\n"
      "v_cvt_pk_bf16_f32 v227, v60, v61\n"
      "v_mfma_f32_16x16x32_bf16 v[28:31], a[8:11], v[76:79], 0\n"
      "buffer_load_dwordx4 v17, s[32:35], s51 offen offset:1024 lds\n"
      "v_cvt_pk_bf16_f32 v222, v62, v63\n"
      "v_mfma_f32_16x16x32_bf16 v[32:35], a[12:15], v[68:71], v[28:31]\n"
      "s_add_u32 m0, s46, 26560\n"
      "v_cvt_pk_bf16_f32 v223, v64, v65\n"
      "v_cvt_pk_bf16_f32 v20, v20, v21\n"
      "v_mfma_f32_16x16x32_bf16 v[28:31], a[12:15], v[72:75], v[28:31]\n"
      "ds_read_b128 a[12:15], v6 offset:0\n"
      "buffer_load_dwordx4 v18, s[32:35], s51 offen offset:2048 lds\n"
      "v_cvt_pk_bf16_f32 v22, v22, v23\n"
      "v_mfma_f32_16x16x32_bf16 v[54:57], a[16:19], v[76:79], 0\n"
      "v_accvgpr_write_b32 a234, v20\n"
      "v_accvgpr_write_b32 a235, v22\n"
      "v_mfma_f32_16x16x32_bf16 v[58:61], a[20:23], v[68:71], v[54:57]\n"
      "v_cvt_pk_bf16_f32 v230, v24, v25\n"
      "v_cvt_pk_bf16_f32 v231, v26, v27\n"
      "v_mfma_f32_16x16x32_bf16 v[54:57], a[20:23], v[72:75], v[54:57]\n"
      "s_waitcnt lgkmcnt(0)\n"
      "s_waitcnt vmcnt(7)\n"
      "s_cbranch_vccnz Lconv_h11\n"
      "s_barrier\n"
      "Lconv_h11:\n"
      "ds_read_b128 a[20:23], v6 offset:512\n"
      "v_cvt_pk_bf16_f32 v238, v28, v29\n"
      "v_cvt_pk_bf16_f32 v239, v30, v31\n"
      "v_mfma_f32_16x16x32_bf16 v[62:65], a[16:19], v[80:83], 0\n"
      "ds_read_b128 a[16:19], v6 offset:256\n"
      "v_cvt_pk_bf16_f32 v234, v32, v33\n"
      "v_mfma_f32_16x16x32_bf16 v[20:23], a[24:27], v[68:71], v[62:65]\n"
      "s_cbranch_vccz Lconv_h12\n"
      "s_barrier\n"
      "Lconv_h12:\n"
      "v_cvt_pk_bf16_f32 v235, v34, v35\n"
      "v_mfma_f32_16x16x32_bf16 v[62:65], a[24:27], v[72:75], v[62:65]\n"
      "ds_read_b128 a[24:27], v6 offset:4608\n"
      "v_cvt_pk_bf16_f32 v242, v58, v59\n"
      "v_cvt_pk_bf16_f32 v243, v60, v61\n"
      "v_mfma_f32_16x16x32_bf16 v[24:27], a[28:31], v[76:79], 0\n"
      "ds_read_b128 a[28:31], v6 offset:4864\n"
      "v_cvt_pk_bf16_f32 v54, v54, v55\n"
      "v_mfma_f32_16x16x32_bf16 v[28:31], a[32:35], v[68:71], v[24:27]\n"
      "v_cvt_pk_bf16_f32 v56, v56, v57\n"
      "v_accvgpr_write_b32 a242, v54\n"
      "v_mfma_f32_16x16x32_bf16 v[24:27], a[32:35], v[72:75], v[24:27]\n"
      "ds_read_b128 a[32:35], v6 offset:5120\n"
      "v_accvgpr_write_b32 a243, v56\n"
      "v_cvt_pk_bf16_f32 v246, v20, v21\n"
      "v_cvt_pk_bf16_f32 v247, v22, v23\n"
      "v_cvt_pk_bf16_f32 v250, v62, v63\n"
      "v_cvt_pk_bf16_f32 v251, v64, v65\n"
      "v_cvt_pk_bf16_f32 v254, v28, v29\n"
      "v_cvt_pk_bf16_f32 v255, v30, v31\n"
      "v_cvt_pk_bf16_f32 v24, v24, v25\n"
      "v_cvt_pk_bf16_f32 v26, v26, v27\n"
      "v_accvgpr_write_b32 a246, v24\n"
      "v_accvgpr_write_b32 a247, v26\n"
      "s_mov_b32 m0, s18\n"
      "ds_read_b128 a[48:51], v2 offset:39840\n"
      "v_lshlrev_b32 v9, 7, v5\n"
      "v_mfma_f32_16x16x32_bf16 v[68:71], a[12:15], a[88:91], 0\n"
      "s_waitcnt lgkmcnt(4)\n"
      "ds_read_b128 a[52:55], v3 offset:39840\n"
      "v_min_u32 v9, Lconv_u0-Lconv_t0, v9\n"
      "v_mfma_f32_16x16x32_bf16 v[68:71], a[16:19], a[100:103], v[68:71]\n"
      "s_mul_i32 s97, s41, Lconv_t1-Lconv_t0\n"
      "ds_read_b128 a[56:59], v4 offset:39840\n"
      "v_add_u32 v9, Lconv_t0-Lconv_start, v9\n"
      "v_mfma_f32_16x16x32_bf16 v[68:71], a[20:23], a[112:115], v[68:71]\n"
      "s_waitcnt lgkmcnt(5)\n"
      "ds_read_b128 a[36:39], v2 offset:39904\n"
      "v_add_u32 v9, s97, v9\n"
      "v_mfma_f32_16x16x32_bf16 v[68:71], a[24:27], a[124:127], v[68:71]\n"
      "s_waitcnt lgkmcnt(5)\n"
      "ds_read_b128 a[40:43], v3 offset:39904\n"
      "v_mfma_f32_16x16x32_bf16 v[68:71], a[28:31], a[136:139], v[68:71]\n"
      "s_waitcnt lgkmcnt(4)\n"
      "ds_read_b128 a[44:47], v4 offset:39904\n"
      "v_mfma_f32_16x16x32_bf16 v[52:55], a[48:51], a[72:75], 0\n"
      "ds_read_b128 a[0:3], v6 offset:768\n"
      "v_mfma_f32_16x16x32_bf16 v[68:71], a[32:35], a[148:151], v[68:71]\n"
      "ds_read_b128 a[48:51], v2 offset:39968\n"
      "v_mfma_f32_16x16x32_bf16 v[72:75], a[12:15], a[92:95], 0\n"
      "s_waitcnt lgkmcnt(6)\n"
      "ds_read_b128 a[4:7], v6 offset:1024\n"
      "v_mfma_f32_16x16x32_bf16 v[56:59], a[52:55], a[76:79], 0\n"
      "ds_read_b128 a[8:11], v6 offset:1280\n"
      "s_nop 0\n"
      "v_mfma_f32_16x16x32_bf16 v[72:75], a[16:19], a[104:107], v[72:75]\n"
      "ds_read_b128 a[52:55], v3 offset:39968\n"
      "s_nop 0\n"
      "v_mfma_f32_16x16x32_bf16 v[72:75], a[20:23], a[116:119], v[72:75]\n"
      "s_waitcnt lgkmcnt(7)\n"
      "ds_read_b128 a[60:63], v6 offset:1536\n"
      "v_fma_f32 v64, s44, v52, -v56\n"
      "v_mfma_f32_16x16x32_bf16 v[60:63], a[56:59], a[80:83], v[52:55]\n"
      "ds_read_b128 a[56:59], v4 offset:39968\n"
      "v_fma_f32 v65, s44, v53, -v57\n"
      "v_mfma_f32_16x16x32_bf16 v[72:75], a[24:27], a[128:131], v[72:75]\n"
      "v_cvt_pk_bf16_f32 v64, v64, v65\n"
      "v_fma_f32 v66, s44, v54, -v58\n"
      "v_mfma_f32_16x16x32_bf16 v[72:75], a[28:31], a[140:143], v[72:75]\n"
      "ds_write_addtid_b32 v64 offset:10752\n"
      "v_fma_f32 v67, s44, v55, -v59\n"
      "v_mfma_f32_16x16x32_bf16 v[72:75], a[32:35], a[152:155], v[72:75]\n"
      "v_cvt_pk_bf16_f32 v65, v66, v67\n"
      "v_fmac_f32_e64 v56, s44, v52\n"
      "v_mfma_f32_16x16x32_bf16 v[36:39], a[36:39], a[72:75], 0\n"
      "ds_write_addtid_b32 v65 offset:47680\n"
      "v_fmac_f32_e64 v57, s44, v53\n"
      "v_mfma_f32_16x16x32_bf16 v[76:79], a[12:15], a[96:99], 0\n"
      "ds_read_b128 a[12:15], v6 offset:5376\n"
      "v_cvt_pk_bf16_f32 v56, v56, v57\n"
      "v_mfma_f32_16x16x32_bf16 v[76:79], a[16:19], a[108:111], v[76:79]\n"
      "s_waitcnt lgkmcnt(5)\n"
      "ds_read_b128 a[16:19], v6 offset:5632\n"
      "v_fmac_f32_e64 v58, s44, v54\n"
      "v_mfma_f32_16x16x32_bf16 v[40:43], a[40:43], a[76:79], 0\n"
      "ds_write_addtid_b32 v56 offset:9984\n"
      "v_fmac_f32_e64 v59, s44, v55\n"
      "v_mfma_f32_16x16x32_bf16 v[76:79], a[20:23], a[120:123], v[76:79]\n"
      "ds_read_b128 a[20:23], v6 offset:5888\n"
      "v_cvt_pk_bf16_f32 v57, v58, v59\n"
      "v_mfma_f32_16x16x32_bf16 v[76:79], a[24:27], a[132:135], v[76:79]\n"
      "ds_write_addtid_b32 v57 offset:46912\n"
      "v_cvt_pk_bf16_f32 v60, v60, v61\n"
      "v_mfma_f32_16x16x32_bf16 v[44:47], a[44:47], a[80:83], v[36:39]\n"
      "ds_write_addtid_b32 v60 offset:9216\n"
      "v_cvt_pk_bf16_f32 v61, v62, v63\n"
      "v_mfma_f32_16x16x32_bf16 v[76:79], a[28:31], a[144:147], v[76:79]\n"
      "ds_write_addtid_b32 v61 offset:46144\n"
      "v_fma_f32 v48, s44, v36, -v40\n"
      "v_mfma_f32_16x16x32_bf16 v[76:79], a[32:35], a[156:159], v[76:79]\n"
      "ds_read_b128 a[36:39], v2 offset:0\n"
      "v_fma_f32 v49, s44, v37, -v41\n"
      "v_mfma_f32_16x16x32_bf16 v[80:83], a[0:3], v[92:95], 0\n"
      "v_cvt_pk_bf16_f32 v48, v48, v49\n"
      "v_fma_f32 v50, s44, v38, -v42\n"
      "v_mfma_f32_16x16x32_bf16 v[52:55], a[48:51], a[72:75], 0\n"
      "ds_write_addtid_b32 v48 offset:11008\n"
      "v_fma_f32 v51, s44, v39, -v43\n"
      "v_mfma_f32_16x16x32_bf16 v[80:83], a[4:7], v[148:151], v[80:83]\n"
      "ds_read_b128 a[40:43], v3 offset:0\n"
      "v_cvt_pk_bf16_f32 v49, v50, v51\n"
      "v_mfma_f32_16x16x32_bf16 v[80:83], a[8:11], v[204:207], v[80:83]\n"
      "ds_write_addtid_b32 v49 offset:47936\n"
      "v_fmac_f32_e64 v40, s44, v36\n"
      "v_mfma_f32_16x16x32_bf16 v[56:59], a[52:55], a[76:79], 0\n"
      "s_waitcnt lgkmcnt(7)\n"
      "v_fmac_f32_e64 v41, s44, v37\n"
      "v_cvt_pk_bf16_f32 v40, v40, v41\n"
      "v_mfma_f32_16x16x32_bf16 v[80:83], a[12:15], v[112:115], v[80:83]\n"
      "ds_write_addtid_b32 v40 offset:10240\n"
      "v_fmac_f32_e64 v42, s44, v38\n"
      "v_mfma_f32_16x16x32_bf16 v[80:83], a[16:19], a[208:211], v[80:83]\n"
      "v_fmac_f32_e64 v43, s44, v39\n"
      "v_cvt_pk_bf16_f32 v41, v42, v43\n"
      "v_mfma_f32_16x16x32_bf16 v[60:63], a[56:59], a[80:83], v[52:55]\n"
      "ds_write_addtid_b32 v41 offset:47168\n"
      "v_cvt_pk_bf16_f32 v44, v44, v45\n"
      "v_mfma_f32_16x16x32_bf16 v[80:83], a[20:23], v[220:223], v[80:83]\n"
      "ds_write_addtid_b32 v44 offset:9472\n"
      "v_cvt_pk_bf16_f32 v45, v46, v47\n"
      "v_mfma_f32_16x16x32_bf16 v[84:87], a[0:3], v[100:103], 0\n"
      "ds_read_b128 a[44:47], v4 offset:0\n"
      "v_fma_f32 v64, s44, v52, -v56\n"
      "v_mfma_f32_16x16x32_bf16 v[84:87], a[4:7], a[200:203], v[84:87]\n"
      "ds_write_addtid_b32 v45 offset:46400\n"
      "v_fma_f32 v65, s44, v53, -v57\n"
      "v_mfma_f32_16x16x32_bf16 v[84:87], a[8:11], v[208:211], v[84:87]\n"
      "ds_read_b128 a[64:67], v6 offset:1792\n"
      "v_cvt_pk_bf16_f32 v64, v64, v65\n"
      "v_mfma_f32_16x16x32_bf16 v[84:87], a[12:15], a[204:207], v[84:87]\n"
      "ds_read_b128 a[68:71], v6 offset:2048\n"
      "v_fma_f32 v66, s44, v54, -v58\n"
      "v_mfma_f32_16x16x32_bf16 v[84:87], a[16:19], v[172:175], v[84:87]\n"
      "ds_read_b128 a[24:27], v6 offset:6144\n"
      "v_fma_f32 v67, s44, v55, -v59\n"
      "v_mfma_f32_16x16x32_bf16 v[84:87], a[20:23], v[228:231], v[84:87]\n"
      "ds_read_b128 a[48:51], v2 offset:64\n"
      "v_cvt_pk_bf16_f32 v65, v66, v67\n"
      "v_mfma_f32_16x16x32_bf16 v[24:27], a[0:3], a[196:199], 0\n"
      "ds_write_addtid_b32 v64 offset:11264\n"
      "v_fmac_f32_e64 v56, s44, v52\n"
      "v_mfma_f32_16x16x32_bf16 v[24:27], a[4:7], v[160:163], v[24:27]\n"
      "s_waitcnt lgkmcnt(11)\n"
      "ds_read_b128 a[28:31], v6 offset:6400\n"
      "v_fmac_f32_e64 v57, s44, v53\n"
      "v_mfma_f32_16x16x32_bf16 v[36:39], a[36:39], a[72:75], 0\n"
      "ds_write_addtid_b32 v65 offset:48192\n"
      "v_cvt_pk_bf16_f32 v56, v56, v57\n"
      "v_mfma_f32_16x16x32_bf16 v[24:27], a[8:11], v[216:219], v[24:27]\n"
      "ds_read_b128 a[32:35], v6 offset:6656\n"
      "v_fmac_f32_e64 v58, s44, v54\n"
      "v_mfma_f32_16x16x32_bf16 v[24:27], a[12:15], v[124:127], v[24:27]\n"
      "ds_write_addtid_b32 v56 offset:10496\n"
      "v_fmac_f32_e64 v59, s44, v55\n"
      "v_mfma_f32_16x16x32_bf16 v[40:43], a[40:43], a[76:79], 0\n"
      "ds_read_b128 a[52:55], v3 offset:64\n"
      "v_cvt_pk_bf16_f32 v57, v58, v59\n"
      "v_mfma_f32_16x16x32_bf16 v[24:27], a[16:19], v[180:183], v[24:27]\n"
      "ds_write_addtid_b32 v57 offset:47424\n"
      "v_cvt_pk_bf16_f32 v60, v60, v61\n"
      "v_mfma_f32_16x16x32_bf16 v[24:27], a[20:23], v[232:235], v[24:27]\n"
      "s_waitcnt lgkmcnt(7)\n"
      "ds_write_addtid_b32 v60 offset:9728\n"
      "v_cvt_pk_bf16_f32 v61, v62, v63\n"
      "v_mfma_f32_16x16x32_bf16 v[44:47], a[44:47], a[80:83], v[36:39]\n"
      "ds_write_addtid_b32 v61 offset:46656\n"
      "v_fma_f32 v48, s44, v36, -v40\n"
      "v_mfma_f32_16x16x32_bf16 v[28:31], a[60:63], v[96:99], 0\n"
      "ds_read_b128 a[56:59], v4 offset:64\n"
      "v_fma_f32 v49, s44, v37, -v41\n"
      "v_mfma_f32_16x16x32_bf16 v[28:31], a[64:67], v[152:155], v[28:31]\n"
      "ds_read_b128 a[36:39], v2 offset:128\n"
      "v_cvt_pk_bf16_f32 v48, v48, v49\n"
      "v_mfma_f32_16x16x32_bf16 v[28:31], a[68:71], a[220:223], v[28:31]\n"
      "ds_read_b128 a[40:43], v3 offset:128\n"
      "v_fma_f32 v50, s44, v38, -v42\n"
      "v_mfma_f32_16x16x32_bf16 v[28:31], a[24:27], v[116:119], v[28:31]\n"
      "s_waitcnt lgkmcnt(0)\n"
      "s_cbranch_vccnz Lconv_h13\n"
      "s_barrier\n"
      "Lconv_h13:\n"
      "v_fma_f32 v51, s44, v39, -v43\n"
      "v_mfma_f32_16x16x32_bf16 v[52:55], a[48:51], a[72:75], 0\n"
      "ds_read_b128 a[44:47], v4 offset:128\n"
      "v_cvt_pk_bf16_f32 v49, v50, v51\n"
      "v_mfma_f32_16x16x32_bf16 v[28:31], a[28:31], v[168:171], v[28:31]\n"
      "ds_write_addtid_b32 v48 offset:15360\n"
      "v_fmac_f32_e64 v40, s44, v36\n"
      "v_mfma_f32_16x16x32_bf16 v[28:31], a[32:35], v[224:227], v[28:31]\n"
      "ds_write_addtid_b32 v49 offset:52288\n"
      "v_fmac_f32_e64 v41, s44, v37\n"
      "v_mfma_f32_16x16x32_bf16 v[56:59], a[52:55], a[76:79], 0\n"
      "s_cbranch_vccz Lconv_h14\n"
      "s_barrier\n"
      "Lconv_h14:\n"
      "v_cvt_pk_bf16_f32 v40, v40, v41\n"
      "v_mfma_f32_16x16x32_bf16 v[20:23], a[60:63], v[104:107], 0\n"
      "s_mov_b64 vcc, 0x2000000\n"
      "ds_write_addtid_b32 v40 offset:14592\n"
      "v_fmac_f32_e64 v42, s44, v38\n"
      "v_mfma_f32_16x16x32_bf16 v[20:23], a[64:67], v[156:159], v[20:23]\n"
      "ds_read_b128 a[0:3], v7 offset:0\n"
      "v_fmac_f32_e64 v43, s44, v39\n"
      "v_mfma_f32_16x16x32_bf16 v[60:63], a[56:59], a[80:83], v[52:55]\n"
      "v_cvt_pk_bf16_f32 v41, v42, v43\n"
      "v_cvt_pk_bf16_f32 v44, v44, v45\n"
      "v_mfma_f32_16x16x32_bf16 v[20:23], a[68:71], v[212:215], v[20:23]\n"
      "ds_write_addtid_b32 v41 offset:51520\n"
      "v_cvt_pk_bf16_f32 v45, v46, v47\n"
      "v_mfma_f32_16x16x32_bf16 v[20:23], a[24:27], v[120:123], v[20:23]\n"
      "ds_write_addtid_b32 v44 offset:13824\n"
      "ds_write_addtid_b32 v45 offset:50752\n"
      "v_mfma_f32_16x16x32_bf16 v[20:23], a[28:31], v[176:179], v[20:23]\n"
      "ds_read_b128 a[4:7], v7 offset:256\n"
      "v_fma_f32 v64, s44, v52, -v56\n"
      "v_mfma_f32_16x16x32_bf16 v[36:39], a[36:39], a[72:75], 0\n"
      "ds_read_b128 a[8:11], v7 offset:512\n"
      "v_fma_f32 v65, s44, v53, -v57\n"
      "v_mfma_f32_16x16x32_bf16 v[20:23], a[32:35], a[232:235], v[20:23]\n"
      "v_fma_f32 v66, s44, v54, -v58\n"
      "v_fma_f32 v67, s44, v55, -v59\n"
      "v_mfma_f32_16x16x32_bf16 v[32:35], a[60:63], v[108:111], 0\n"
      "v_fmac_f32_e64 v56, s44, v52\n"
      "v_fmac_f32_e64 v57, s44, v53\n"
      "v_mfma_f32_16x16x32_bf16 v[40:43], a[40:43], a[76:79], 0\n"
      "v_fmac_f32_e64 v58, s44, v54\n"
      "v_fmac_f32_e64 v59, s44, v55\n"
      "v_mfma_f32_16x16x32_bf16 v[32:35], a[64:67], v[164:167], v[32:35]\n"
      "v_cvt_pk_bf16_f32 v64, v64, v65\n"
      "v_cvt_pk_bf16_f32 v65, v66, v67\n"
      "v_mfma_f32_16x16x32_bf16 v[32:35], a[68:71], a[224:227], v[32:35]\n"
      "s_waitcnt lgkmcnt(5)\n"
      "v_cvt_pk_bf16_f32 v56, v56, v57\n"
      "v_cvt_pk_bf16_f32 v57, v58, v59\n"
      "v_mfma_f32_16x16x32_bf16 v[44:47], a[44:47], a[80:83], v[36:39]\n"
      "ds_write_addtid_b32 v64 offset:15616\n"
      "v_cvt_pk_bf16_f32 v60, v60, v61\n"
      "v_mfma_f32_16x16x32_bf16 v[32:35], a[24:27], v[128:131], v[32:35]\n"
      "ds_write_addtid_b32 v65 offset:52544\n"
      "v_cvt_pk_bf16_f32 v61, v62, v63\n"
      "v_mfma_f32_16x16x32_bf16 v[32:35], a[28:31], a[228:231], v[32:35]\n"
      "ds_write_addtid_b32 v56 offset:14848\n"
      "ds_write_addtid_b32 v57 offset:51776\n"
      "v_mfma_f32_16x16x32_bf16 v[32:35], a[32:35], v[236:239], v[32:35]\n"
      "ds_write_addtid_b32 v60 offset:14080\n"
      "ds_write_addtid_b32 v61 offset:51008\n"
      "v_mfma_f32_16x16x32_bf16 v[68:71], a[0:3], a[160:163], v[68:71]\n"
      "s_waitcnt lgkmcnt(6)\n"
      "ds_read_b128 a[12:15], v7 offset:768\n"
      "v_fma_f32 v48, s44, v36, -v40\n"
      "v_mfma_f32_16x16x32_bf16 v[68:71], a[4:7], a[172:175], v[68:71]\n"
      "s_add_u32 m0, s46, 39840\n"
      "ds_read_b128 a[16:19], v7 offset:1024\n"
      "v_fma_f32 v49, s44, v37, -v41\n"
      "v_mfma_f32_16x16x32_bf16 v[68:71], a[8:11], a[184:187], v[68:71]\n"
      "s_add_u32 s51, s51, s12\n"
      "ds_read_b128 a[20:23], v7 offset:1280\n"
      "v_cvt_pk_bf16_f32 v48, v48, v49\n"
      "v_mfma_f32_16x16x32_bf16 v[72:75], a[0:3], a[164:167], v[72:75]\n"
      "buffer_load_dwordx4 v16, s[32:35], s51 offen lds\n"
      "v_fma_f32 v50, s44, v38, -v42\n"
      "v_mfma_f32_16x16x32_bf16 v[72:75], a[4:7], a[176:179], v[72:75]\n"
      "v_fma_f32 v51, s44, v39, -v43\n"
      "v_cvt_pk_bf16_f32 v49, v50, v51\n"
      "v_mfma_f32_16x16x32_bf16 v[72:75], a[8:11], a[188:191], v[72:75]\n"
      "v_fmac_f32_e64 v40, s44, v36\n"
      "v_fmac_f32_e64 v41, s44, v37\n"
      "v_mfma_f32_16x16x32_bf16 v[76:79], a[0:3], a[168:171], v[76:79]\n"
      "buffer_load_dwordx4 v17, s[32:35], s51 offen offset:1024 lds\n"
      "v_cvt_pk_bf16_f32 v40, v40, v41\n"
      "v_mfma_f32_16x16x32_bf16 v[76:79], a[4:7], a[180:183], v[76:79]\n"
      "ds_read_b128 a[0:3], v7 offset:1536\n"
      "v_fmac_f32_e64 v42, s44, v38\n"
      "v_mfma_f32_16x16x32_bf16 v[76:79], a[8:11], a[192:195], v[76:79]\n"
      "s_waitcnt lgkmcnt(3)\n"
      "ds_read_b128 a[4:7], v7 offset:1792\n"
      "v_fmac_f32_e64 v43, s44, v39\n"
      "v_mfma_f32_16x16x32_bf16 v[80:83], a[12:15], a[212:215], v[80:83]\n"
      "s_waitcnt lgkmcnt(3)\n"
      "ds_read_b128 a[8:11], v7 offset:2048\n"
      "v_cvt_pk_bf16_f32 v41, v42, v43\n"
      "v_mfma_f32_16x16x32_bf16 v[80:83], a[16:19], v[184:187], v[80:83]\n"
      "s_waitcnt lgkmcnt(3)\n"
      "v_cvt_pk_bf16_f32 v44, v44, v45\n"
      "v_cvt_pk_bf16_f32 v45, v46, v47\n"
      "v_mfma_f32_16x16x32_bf16 v[80:83], a[20:23], v[240:243], v[80:83]\n"
      "buffer_load_dwordx4 v18, s[32:35], s51 offen offset:2048 lds\n"
      "v_cvt_pk_bf16_f32 v68, v68, v69\n"
      "v_mfma_f32_16x16x32_bf16 v[84:87], a[12:15], v[136:139], v[84:87]\n"
      "s_mov_b32 m0, s18\n"
      "v_cvt_pk_bf16_f32 v69, v70, v71\n"
      "v_cvt_pk_bf16_f32 v72, v72, v73\n"
      "v_mfma_f32_16x16x32_bf16 v[84:87], a[16:19], v[192:195], v[84:87]\n"
      "ds_write_addtid_b32 v48 offset:15872\n"
      "ds_write_addtid_b32 v49 offset:52800\n"
      "v_mfma_f32_16x16x32_bf16 v[84:87], a[20:23], v[244:247], v[84:87]\n"
      "ds_write_addtid_b32 v40 offset:15104\n"
      "ds_write_addtid_b32 v41 offset:52032\n"
      "v_mfma_f32_16x16x32_bf16 v[24:27], a[12:15], a[216:219], v[24:27]\n"
      "ds_write_addtid_b32 v44 offset:14336\n"
      "ds_write_addtid_b32 v45 offset:51264\n"
      "v_mfma_f32_16x16x32_bf16 v[24:27], a[16:19], v[196:199], v[24:27]\n"
      "s_mov_b32 m0, s45\n"
      "v_cvt_pk_bf16_f32 v73, v74, v75\n"
      "v_cvt_pk_bf16_f32 v76, v76, v77\n"
      "v_mfma_f32_16x16x32_bf16 v[24:27], a[20:23], v[252:255], v[24:27]\n"
      "s_waitcnt lgkmcnt(6)\n"
      "ds_write_addtid_b32 v68 offset:57360\n"
      "ds_write_addtid_b32 v69 offset:57616\n"
      "v_mfma_f32_16x16x32_bf16 v[28:31], a[0:3], v[132:135], v[28:31]\n"
      "ds_write_addtid_b32 v72 offset:45064\n"
      "ds_write_addtid_b32 v73 offset:45320\n"
      "v_mfma_f32_16x16x32_bf16 v[28:31], a[4:7], v[188:191], v[28:31]\n"
      "ds_write_addtid_b32 v76 offset:32768\n"
      "v_cvt_pk_bf16_f32 v77, v78, v79\n"
      "v_mfma_f32_16x16x32_bf16 v[28:31], a[8:11], a[240:243], v[28:31]\n"
      "ds_write_addtid_b32 v77 offset:33024\n"
      "v_cvt_pk_bf16_f32 v80, v80, v81\n"
      "v_mfma_f32_16x16x32_bf16 v[20:23], a[0:3], v[140:143], v[20:23]\n"
      "ds_write_addtid_b32 v80 offset:58384\n"
      "v_cvt_pk_bf16_f32 v81, v82, v83\n"
      "v_mfma_f32_16x16x32_bf16 v[20:23], a[4:7], a[236:239], v[20:23]\n"
      "ds_write_addtid_b32 v81 offset:58640\n"
      "v_cvt_pk_bf16_f32 v84, v84, v85\n"
      "v_mfma_f32_16x16x32_bf16 v[20:23], a[8:11], v[248:251], v[20:23]\n"
      "ds_write_addtid_b32 v84 offset:46088\n"
      "v_cvt_pk_bf16_f32 v85, v86, v87\n"
      "v_mfma_f32_16x16x32_bf16 v[32:35], a[0:3], v[144:147], v[32:35]\n"
      "ds_write_addtid_b32 v85 offset:46344\n"
      "v_cvt_pk_bf16_f32 v24, v24, v25\n"
      "v_mfma_f32_16x16x32_bf16 v[32:35], a[4:7], v[200:203], v[32:35]\n"
      "ds_write_addtid_b32 v24 offset:33792\n"
      "v_cvt_pk_bf16_f32 v25, v26, v27\n"
      "v_mfma_f32_16x16x32_bf16 v[32:35], a[8:11], a[244:247], v[32:35]\n"
      "ds_write_addtid_b32 v25 offset:34048\n"
      "v_cvt_pk_bf16_f32 v28, v28, v29\n"
      "v_cvt_pk_bf16_f32 v29, v30, v31\n"
      "ds_write_addtid_b32 v28 offset:59408\n"
      "ds_write_addtid_b32 v29 offset:59664\n"
      "v_cvt_pk_bf16_f32 v20, v20, v21\n"
      "v_cvt_pk_bf16_f32 v21, v22, v23\n"
      "ds_write_addtid_b32 v20 offset:47112\n"
      "ds_write_addtid_b32 v21 offset:47368\n"
      "s_waitcnt vmcnt(3)\n"
      "s_waitcnt lgkmcnt(0)\n"
      "s_cmp_eq_u32 s41, 1\n"
      "s_cbranch_scc1 Lconv_s1\n"
      "Lconv_s0:\n"
      "s_branch Lconv_loop0\n"
      "Lconv_s1:\n"
      "s_branch Lconv_loop1\n"
      ".p2align 12\n"
      "Lconv_pa0:\n"
      "s_cbranch_execz Lconv_r0\n"
      "s_nop 0\n"
      "Lconv_loop0:\n"
      "v_mfma_f32_16x16x32_bf16 v[20:23], a[24:27], v[96:99], 0\n"
      "s_add_u32 s51, s51, s12\n"
      "s_nop 1\n"
      "v_mfma_f32_16x16x32_bf16 v[20:23], a[28:31], v[152:155], v[20:23]\n"
      "s_sub_u32 s58, s93, s58\n"
      "v_cvt_pk_bf16_f32 v32, v32, v33\n"
      "v_cvt_pk_bf16_f32 v33, v34, v35\n"
      "v_mfma_f32_16x16x32_bf16 v[20:23], a[32:35], a[220:223], v[20:23]\n"
      "s_sub_u32 s59, s94, s59\n"
      "ds_write_addtid_b32 v32 offset:34816\n"
      "v_mfma_f32_16x16x32_bf16 v[20:23], a[0:3], v[116:119], v[20:23]\n"
      "s_sub_u32 s47, s51, s48\n"
      "ds_write_addtid_b32 v33 offset:35072\n"
      "v_mfma_f32_16x16x32_bf16 v[20:23], a[4:7], v[168:171], v[20:23]\n"
      "s_cmp_ge_u32 vcc_lo, 0x80000000\n"
      "v_mfma_f32_16x16x32_bf16 v[20:23], a[8:11], v[224:227], v[20:23]\n"
      "s_cselect_b32 s53, s47, s51\n"
      "v_mfma_f32_16x16x32_bf16 v[24:27], a[24:27], v[104:107], 0\n"
      "s_waitcnt vmcnt(8) lgkmcnt(0)\n"
      "s_barrier\n"
      "v_mfma_f32_16x16x32_bf16 v[24:27], a[28:31], v[156:159], v[24:27]\n"
      "s_mov_b32 m0, s58\n"
      "ds_read_b128 a[12:15], v7 offset:6144\n"
      "v_mfma_f32_16x16x32_bf16 v[24:27], a[32:35], v[212:215], v[24:27]\n"
      "ds_read_b128 a[16:19], v7 offset:6400\n"
      "v_mfma_f32_16x16x32_bf16 v[24:27], a[0:3], v[120:123], v[24:27]\n"
      "ds_read_b128 a[20:23], v7 offset:6656\n"
      "v_mfma_f32_16x16x32_bf16 v[24:27], a[4:7], v[176:179], v[24:27]\n"
      "ds_read_b128 a[48:51], v2 offset:13280\n"
      "v_mfma_f32_16x16x32_bf16 v[24:27], a[8:11], a[232:235], v[24:27]\n"
      "ds_read_b128 a[52:55], v3 offset:13280\n"
      "v_mfma_f32_16x16x32_bf16 v[68:71], a[84:87], a[60:63], 0\n"
      "ds_read_b128 a[56:59], v4 offset:13280\n"
      "s_nop 0\n"
      "v_mfma_f32_16x16x32_bf16 v[28:31], a[24:27], v[108:111], 0\n"
      "ds_read2_b64 a[64:67], v10 offset0:0 offset1:64\n"
      "s_nop 0\n"
      "v_mfma_f32_16x16x32_bf16 v[28:31], a[28:31], v[164:167], v[28:31]\n"
      "buffer_load_dwordx4 v16, s[32:35], s53 offen lds\n"
      "v_mfma_f32_16x16x32_bf16 v[28:31], a[32:35], a[224:227], v[28:31]\n"
      "s_mov_b32 m0, s45\n"
      "ds_read2_b64 a[68:71], v10 offset0:1 offset1:65\n"
      "v_cvt_pk_bf16_f32 v85, v68, v69\n"
      "v_mfma_f32_16x16x32_bf16 v[28:31], a[0:3], v[128:131], v[28:31]\n"
      "ds_read_b128 a[24:27], v6 offset:5376\n"
      "v_cvt_pk_bf16_f32 v91, v70, v71\n"
      "v_mfma_f32_16x16x32_bf16 v[28:31], a[4:7], a[228:231], v[28:31]\n"
      "ds_read_b128 a[28:31], v6 offset:5632\n"
      "v_mfma_f32_16x16x32_bf16 v[28:31], a[8:11], v[236:239], v[28:31]\n"
      "s_waitcnt lgkmcnt(6)\n"
      "ds_read_b128 a[32:35], v6 offset:5888\n"
      "v_mfma_f32_16x16x32_bf16 v[20:23], a[12:15], v[132:135], v[20:23]\n"
      "buffer_store_dwordx2 v[84:85], v15, s[36:39], s52 offen sc1\n"
      "v_mfma_f32_16x16x32_bf16 v[20:23], a[16:19], v[188:191], v[20:23]\n"
      "ds_read_b128 a[0:3], v7 offset:768\n"
      "v_mfma_f32_16x16x32_bf16 v[20:23], a[20:23], a[240:243], v[20:23]\n"
      "ds_read_b128 a[4:7], v7 offset:1024\n"
      "s_nop 0\n"
      "v_mfma_f32_16x16x32_bf16 v[52:55], a[48:51], a[72:75], 0\n"
      "s_waitcnt lgkmcnt(6)\n"
      "ds_read_b128 a[8:11], v7 offset:1280\n"
      "s_nop 0\n"
      "v_mfma_f32_16x16x32_bf16 v[56:59], a[52:55], a[76:79], 0\n"
      "ds_read_b128 a[36:39], v2 offset:13344\n"
      "v_cvt_pk_bf16_f32 v20, v20, v21\n"
      "v_mfma_f32_16x16x32_bf16 v[60:63], a[56:59], a[80:83], v[52:55]\n"
      "buffer_store_dwordx2 v[90:91], v15, s[36:39], s52 offen offset:1536 sc1\n"
      "v_cvt_pk_bf16_f32 v21, v22, v23\n"
      "v_mfma_f32_16x16x32_bf16 v[24:27], a[12:15], v[140:143], v[24:27]\n"
      "s_mov_b32 s52, s13\n"
      "ds_write_addtid_b32 v20 offset:62480\n"
      "v_fma_f32 v64, s44, v52, -v56\n"
      "v_mfma_f32_16x16x32_bf16 v[24:27], a[16:19], a[236:239], v[24:27]\n"
      "s_add_u32 s13, s13, s12\n"
      "ds_write_addtid_b32 v21 offset:62736\n"
      "v_fma_f32 v65, s44, v53, -v57\n"
      "v_mfma_f32_16x16x32_bf16 v[24:27], a[20:23], v[248:251], v[24:27]\n"
      "s_sub_u32 s96, s53, s95\n"
      "ds_read_b128 a[40:43], v3 offset:13344\n"
      "v_cvt_pk_bf16_f32 v64, v64, v65\n"
      "v_mfma_f32_16x16x32_bf16 v[72:75], a[84:87], a[64:67], 0\n"
      "ds_read_b128 a[44:47], v4 offset:13344\n"
      "v_fma_f32 v66, s44, v54, -v58\n"
      "v_mfma_f32_16x16x32_bf16 v[28:31], a[12:15], v[144:147], v[28:31]\n"
      "ds_read_b128 a[12:15], v7 offset:5376\n"
      "v_cvt_pk_bf16_f32 v24, v24, v25\n"
      "v_mfma_f32_16x16x32_bf16 v[28:31], a[16:19], v[200:203], v[28:31]\n"
      "ds_read_b128 a[16:19], v7 offset:5632\n"
      "v_cvt_pk_bf16_f32 v25, v26, v27\n"
      "v_mfma_f32_16x16x32_bf16 v[28:31], a[20:23], a[244:247], v[28:31]\n"
      "s_waitcnt lgkmcnt(6)\n"
      "ds_read_b128 a[20:23], v7 offset:5888\n"
      "v_fma_f32 v67, s44, v55, -v59\n"
      "v_mfma_f32_16x16x32_bf16 v[76:79], a[84:87], a[68:71], 0\n"
      "ds_write_addtid_b32 v24 offset:50184\n"
      "v_fmac_f32_e64 v56, s44, v52\n"
      "v_mfma_f32_16x16x32_bf16 v[32:35], a[24:27], v[92:95], 0\n"
      "ds_write_addtid_b32 v25 offset:50440\n"
      "v_fmac_f32_e64 v57, s44, v53\n"
      "v_mfma_f32_16x16x32_bf16 v[32:35], a[28:31], v[148:151], v[32:35]\n"
      "s_mov_b32 m0, s18\n"
      "ds_read2_b64 a[60:63], v10 offset0:2 offset1:66\n"
      "v_fmac_f32_e32 v58, s44, v54\n"
      "v_mfma_f32_16x16x32_bf16 v[32:35], a[32:35], v[204:207], v[32:35]\n"
      "ds_write_addtid_b32 v64 offset:1536\n"
      "v_fmac_f32_e64 v59, s44, v55\n"
      "v_mfma_f32_16x16x32_bf16 v[32:35], a[0:3], v[112:115], v[32:35]\n"
      "v_cvt_pk_bf16_f32 v65, v66, v67\n"
      "v_cvt_pk_bf16_f32 v56, v56, v57\n"
      "v_mfma_f32_16x16x32_bf16 v[32:35], a[4:7], a[208:211], v[32:35]\n"
      "v_cvt_pk_bf16_f32 v57, v58, v59\n"
      "v_cvt_pk_bf16_f32 v60, v60, v61\n"
      "v_mfma_f32_16x16x32_bf16 v[32:35], a[8:11], v[220:223], v[32:35]\n"
      "s_waitcnt lgkmcnt(5)\n"
      "ds_write_addtid_b32 v65 offset:38464\n"
      "v_cvt_pk_bf16_f32 v61, v62, v63\n"
      "v_mfma_f32_16x16x32_bf16 v[32:35], a[12:15], a[212:215], v[32:35]\n"
      "ds_write_addtid_b32 v56 offset:768\n"
      "v_cvt_pk_bf16_f32 v28, v28, v29\n"
      "v_mfma_f32_16x16x32_bf16 v[32:35], a[16:19], v[184:187], v[32:35]\n"
      "s_waitcnt lgkmcnt(6)\n"
      "ds_write_addtid_b32 v57 offset:37696\n"
      "v_cvt_pk_bf16_f32 v29, v30, v31\n"
      "v_mfma_f32_16x16x32_bf16 v[32:35], a[20:23], v[240:243], v[32:35]\n"
      "ds_write_addtid_b32 v60 offset:0\n"
      "v_cvt_pk_bf16_f32 v80, v72, v73\n"
      "v_mfma_f32_16x16x32_bf16 v[20:23], a[24:27], v[100:103], 0\n"
      "ds_write_addtid_b32 v61 offset:36928\n"
      "v_cvt_pk_bf16_f32 v81, v76, v77\n"
      "v_mfma_f32_16x16x32_bf16 v[20:23], a[28:31], a[200:203], v[20:23]\n"
      "s_mov_b32 m0, s45\n"
      "ds_read_b128 a[48:51], v2 offset:13408\n"
      "v_cvt_pk_bf16_f32 v32, v32, v33\n"
      "v_mfma_f32_16x16x32_bf16 v[20:23], a[32:35], v[208:211], v[20:23]\n"
      "ds_write_addtid_b32 v28 offset:37888\n"
      "v_cvt_pk_bf16_f32 v33, v34, v35\n"
      "v_mfma_f32_16x16x32_bf16 v[20:23], a[0:3], a[204:207], v[20:23]\n"
      "ds_write_addtid_b32 v29 offset:38144\n"
      "v_cvt_pk_bf16_f32 v86, v74, v75\n"
      "v_mfma_f32_16x16x32_bf16 v[20:23], a[4:7], v[172:175], v[20:23]\n"
      "s_mov_b32 m0, s58\n"
      "ds_read_b128 a[56:59], v4 offset:13408\n"
      "v_cvt_pk_bf16_f32 v87, v78, v79\n"
      "v_mfma_f32_16x16x32_bf16 v[20:23], a[8:11], v[228:231], v[20:23]\n"
      "buffer_load_dwordx4 v17, s[32:35], s53 offen offset:1024 lds\n"
      "v_add_u32_e32 v2, s91, v2\n"
      "v_mfma_f32_16x16x32_bf16 v[20:23], a[12:15], v[136:139], v[20:23]\n"
      "s_mov_b32 m0, s45\n"
      "ds_read2_b64 a[64:67], v10 offset0:3 offset1:67\n"
      "v_add_u32_e64 v4, s91, v4\n"
      "v_mfma_f32_16x16x32_bf16 v[20:23], a[16:19], v[192:195], v[20:23]\n"
      "ds_write_addtid_b32 v32 offset:61456\n"
      "v_mfma_f32_16x16x32_bf16 v[20:23], a[20:23], v[244:247], v[20:23]\n"
      "ds_write_addtid_b32 v33 offset:61712\n"
      "s_nop 0\n"
      "v_mfma_f32_16x16x32_bf16 v[36:39], a[36:39], a[72:75], 0\n"
      "ds_read_b128 a[52:55], v3 offset:13408\n"
      "v_add_u32_e64 v3, s91, v3\n"
      "v_mfma_f32_16x16x32_bf16 v[40:43], a[40:43], a[76:79], 0\n"
      "s_sub_u32 s91, 0, s91\n"
      "ds_read2_b64 a[68:71], v12 offset0:0 offset1:64\n"
      "v_cvt_pk_bf16_f32 v20, v20, v21\n"
      "v_mfma_f32_16x16x32_bf16 v[44:47], a[44:47], a[80:83], v[36:39]\n"
      "s_waitcnt lgkmcnt(8)\n"
      "ds_write_addtid_b32 v20 offset:49160\n"
      "v_cvt_pk_bf16_f32 v21, v22, v23\n"
      "v_mfma_f32_16x16x32_bf16 v[68:71], a[84:87], a[60:63], 0\n"
      "ds_write_addtid_b32 v21 offset:49416\n"
      "v_fma_f32 v48, s44, v36, -v40\n"
      "v_mfma_f32_16x16x32_bf16 v[24:27], a[24:27], a[196:199], 0\n"
      "s_mov_b32 m0, s58\n"
      "ds_read_b128 a[24:27], v7 offset:4608\n"
      "v_fma_f32 v49, s44, v37, -v41\n"
      "v_mfma_f32_16x16x32_bf16 v[24:27], a[28:31], v[160:163], v[24:27]\n"
      "ds_read_b128 a[28:31], v7 offset:4864\n"
      "v_cvt_pk_bf16_f32 v48, v48, v49\n"
      "v_mfma_f32_16x16x32_bf16 v[24:27], a[32:35], v[216:219], v[24:27]\n"
      "ds_read_b128 a[32:35], v7 offset:5120\n"
      "v_fma_f32 v50, s44, v38, -v42\n"
      "v_mfma_f32_16x16x32_bf16 v[24:27], a[0:3], v[124:127], v[24:27]\n"
      "ds_read_b128 a[0:3], v6 offset:4608\n"
      "v_fma_f32 v51, s44, v39, -v43\n"
      "v_mfma_f32_16x16x32_bf16 v[24:27], a[4:7], v[180:183], v[24:27]\n"
      "ds_read_b128 a[4:7], v6 offset:4864\n"
      "v_cvt_pk_bf16_f32 v49, v50, v51\n"
      "v_mfma_f32_16x16x32_bf16 v[24:27], a[8:11], v[232:235], v[24:27]\n"
      "ds_read_b128 a[8:11], v6 offset:5120\n"
      "v_fmac_f32_e64 v40, s44, v36\n"
      "v_mfma_f32_16x16x32_bf16 v[24:27], a[12:15], a[216:219], v[24:27]\n"
      "ds_read_b128 a[12:15], v7 offset:0\n"
      "v_fmac_f32_e64 v41, s44, v37\n"
      "v_mfma_f32_16x16x32_bf16 v[24:27], a[16:19], v[196:199], v[24:27]\n"
      "ds_read_b128 a[16:19], v7 offset:256\n"
      "v_cvt_pk_bf16_f32 v40, v40, v41\n"
      "v_mfma_f32_16x16x32_bf16 v[24:27], a[20:23], v[252:255], v[24:27]\n"
      "ds_read_b128 a[20:23], v7 offset:512\n"
      "v_fmac_f32_e64 v42, s44, v38\n"
      "v_mfma_f32_16x16x32_bf16 v[52:55], a[48:51], a[72:75], 0\n"
      "s_waitcnt lgkmcnt(7)\n"
      "v_fmac_f32_e64 v43, s44, v39\n"
      "v_cvt_pk_bf16_f32 v41, v42, v43\n"
      "v_mfma_f32_16x16x32_bf16 v[60:63], a[56:59], a[80:83], v[52:55]\n"
      "buffer_load_dwordx4 v18, s[32:35], s53 offen offset:2048 lds\n"
      "v_cvt_pk_bf16_f32 v24, v24, v25\n"
      "v_mfma_f32_16x16x32_bf16 v[72:75], a[84:87], a[64:67], 0\n"
      "s_mov_b32 m0, s45\n"
      "v_cvt_pk_bf16_f32 v25, v26, v27\n"
      "v_cvt_pk_bf16_f32 v44, v44, v45\n"
      "v_mfma_f32_16x16x32_bf16 v[28:31], a[24:27], a[160:163], 0\n"
      "ds_write_addtid_b32 v24 offset:36864\n"
      "v_cvt_pk_bf16_f32 v82, v68, v69\n"
      "v_mfma_f32_16x16x32_bf16 v[28:31], a[28:31], a[172:175], v[28:31]\n"
      "s_waitcnt lgkmcnt(4)\n"
      "ds_write_addtid_b32 v25 offset:37120\n"
      "v_cvt_pk_bf16_f32 v83, v72, v73\n"
      "v_mfma_f32_16x16x32_bf16 v[28:31], a[32:35], a[184:187], v[28:31]\n"
      "s_mov_b32 m0, s18\n"
      "v_cvt_pk_bf16_f32 v45, v46, v47\n"
      "v_cvt_pk_bf16_f32 v88, v70, v71\n"
      "v_mfma_f32_16x16x32_bf16 v[28:31], a[0:3], a[88:91], v[28:31]\n"
      "ds_write_addtid_b32 v48 offset:1792\n"
      "v_cvt_pk_bf16_f32 v89, v74, v75\n"
      "v_mfma_f32_16x16x32_bf16 v[28:31], a[4:7], a[100:103], v[28:31]\n"
      "ds_write_addtid_b32 v49 offset:38720\n"
      "v_cvt_pk_bf16_f32 v60, v60, v61\n"
      "v_mfma_f32_16x16x32_bf16 v[28:31], a[8:11], a[112:115], v[28:31]\n"
      "s_waitcnt lgkmcnt(4)\n"
      "ds_write_addtid_b32 v40 offset:1024\n"
      "v_cvt_pk_bf16_f32 v61, v62, v63\n"
      "v_mfma_f32_16x16x32_bf16 v[28:31], a[12:15], a[124:127], v[28:31]\n"
      "ds_write_addtid_b32 v41 offset:37952\n"
      "v_swap_b32 v6, v7\n"
      "v_mfma_f32_16x16x32_bf16 v[28:31], a[16:19], a[136:139], v[28:31]\n"
      "ds_write_addtid_b32 v44 offset:256\n"
      "v_mfma_f32_16x16x32_bf16 v[28:31], a[20:23], a[148:151], v[28:31]\n"
      "buffer_store_dwordx4 v[80:83], v14, s[36:39], s52 offen sc1\n"
      "s_nop 0\n"
      "v_mfma_f32_16x16x32_bf16 v[56:59], a[52:55], a[76:79], 0\n"
      "ds_write_addtid_b32 v45 offset:37184\n"
      "s_nop 0\n"
      "v_mfma_f32_16x16x32_bf16 v[20:23], a[24:27], a[164:167], 0\n"
      "s_mov_b32 m0, s45\n"
      "ds_read2_b64 a[60:63], v12 offset0:1 offset1:65\n"
      "v_cvt_pk_bf16_f32 v28, v28, v29\n"
      "v_mfma_f32_16x16x32_bf16 v[20:23], a[28:31], a[176:179], v[20:23]\n"
      "ds_write_addtid_b32 v28 offset:60432\n"
      "v_cvt_pk_bf16_f32 v29, v30, v31\n"
      "v_mfma_f32_16x16x32_bf16 v[20:23], a[32:35], a[188:191], v[20:23]\n"
      "ds_write_addtid_b32 v29 offset:60688\n"
      "v_fma_f32 v64, s44, v52, -v56\n"
      "v_mfma_f32_16x16x32_bf16 v[20:23], a[0:3], a[92:95], v[20:23]\n"
      "buffer_store_dwordx4 v[86:89], v14, s[36:39], s52 offen offset:1536 sc1\n"
      "v_fma_f32 v65, s44, v53, -v57\n"
      "v_mfma_f32_16x16x32_bf16 v[20:23], a[4:7], a[104:107], v[20:23]\n"
      "s_mov_b32 m0, s18\n"
      "v_cvt_pk_bf16_f32 v64, v64, v65\n"
      "v_fma_f32 v66, s44, v54, -v58\n"
      "v_mfma_f32_16x16x32_bf16 v[20:23], a[8:11], a[116:119], v[20:23]\n"
      "ds_write_addtid_b32 v64 offset:2048\n"
      "v_fma_f32 v67, s44, v55, -v59\n"
      "v_mfma_f32_16x16x32_bf16 v[20:23], a[12:15], a[128:131], v[20:23]\n"
      "v_cvt_pk_bf16_f32 v65, v66, v67\n"
      "v_fmac_f32_e64 v56, s44, v52\n"
      "v_mfma_f32_16x16x32_bf16 v[20:23], a[16:19], a[140:143], v[20:23]\n"
      "ds_write_addtid_b32 v65 offset:38976\n"
      "v_fmac_f32_e64 v57, s44, v53\n"
      "v_mfma_f32_16x16x32_bf16 v[20:23], a[20:23], a[152:155], v[20:23]\n"
      "v_cvt_pk_bf16_f32 v56, v56, v57\n"
      "v_fmac_f32_e64 v58, s44, v54\n"
      "v_mfma_f32_16x16x32_bf16 v[76:79], a[84:87], a[68:71], 0\n"
      "ds_write_addtid_b32 v56 offset:1280\n"
      "v_fmac_f32_e64 v59, s44, v55\n"
      "v_mfma_f32_16x16x32_bf16 v[32:35], a[24:27], a[168:171], 0\n"
      "ds_write_addtid_b32 v60 offset:512\n"
      "v_cvt_pk_bf16_f32 v57, v58, v59\n"
      "v_mfma_f32_16x16x32_bf16 v[32:35], a[28:31], a[180:183], v[32:35]\n"
      "ds_write_addtid_b32 v57 offset:38208\n"
      "v_cvt_pk_bf16_f32 v20, v20, v21\n"
      "v_mfma_f32_16x16x32_bf16 v[32:35], a[32:35], a[192:195], v[32:35]\n"
      "ds_write_addtid_b32 v61 offset:37440\n"
      "v_cvt_pk_bf16_f32 v21, v22, v23\n"
      "v_mfma_f32_16x16x32_bf16 v[32:35], a[0:3], a[96:99], v[32:35]\n"
      "s_mov_b32 m0, s45\n"
      "v_cvt_pk_bf16_f32 v84, v76, v77\n"
      "v_cvt_pk_bf16_f32 v90, v78, v79\n"
      "v_mfma_f32_16x16x32_bf16 v[32:35], a[4:7], a[108:111], v[32:35]\n"
      "s_mov_b32 m0, s59\n"
      "v_mfma_f32_16x16x32_bf16 v[32:35], a[8:11], a[120:123], v[32:35]\n"
      "buffer_load_dwordx4 v19, s[32:35], s96 offen lds\n"
      "v_mfma_f32_16x16x32_bf16 v[32:35], a[12:15], a[132:135], v[32:35]\n"
      "s_mov_b32 m0, s45\n"
      "v_mfma_f32_16x16x32_bf16 v[32:35], a[16:19], a[144:147], v[32:35]\n"
      "ds_write_addtid_b32 v20 offset:48136\n"
      "v_mfma_f32_16x16x32_bf16 v[32:35], a[20:23], a[156:159], v[32:35]\n"
      "ds_write_addtid_b32 v21 offset:48392\n"
      "s_nop 0\n"
      "v_mfma_f32_16x16x32_bf16 v[20:23], a[12:15], a[88:91], 0\n"
      "s_add_u32 s51, s51, s12\n"
      "s_nop 1\n"
      "v_mfma_f32_16x16x32_bf16 v[20:23], a[16:19], a[100:103], v[20:23]\n"
      "s_sub_u32 s47, s51, s48\n"
      "v_cvt_pk_bf16_f32 v32, v32, v33\n"
      "v_cvt_pk_bf16_f32 v33, v34, v35\n"
      "v_mfma_f32_16x16x32_bf16 v[20:23], a[20:23], a[112:115], v[20:23]\n"
      "s_cmp_ge_u32 vcc_lo, 0x40000000\n"
      "ds_write_addtid_b32 v32 offset:35840\n"
      "v_mfma_f32_16x16x32_bf16 v[20:23], a[24:27], a[124:127], v[20:23]\n"
      "s_cselect_b32 s53, s47, s51\n"
      "ds_write_addtid_b32 v33 offset:36096\n"
      "v_mfma_f32_16x16x32_bf16 v[20:23], a[28:31], a[136:139], v[20:23]\n"
      "s_mul_i32 vcc_lo, vcc_lo, 2\n"
      "v_mfma_f32_16x16x32_bf16 v[20:23], a[32:35], a[148:151], v[20:23]\n"
      "v_mfma_f32_16x16x32_bf16 v[24:27], a[12:15], a[92:95], 0\n"
      "s_waitcnt vmcnt(19) lgkmcnt(0)\n"
      "s_barrier\n"
      "v_mfma_f32_16x16x32_bf16 v[24:27], a[16:19], a[104:107], v[24:27]\n"
      "s_add_u32 m0, s58, 13280\n"
      "ds_read_b128 a[0:3], v7 offset:0\n"
      "v_mfma_f32_16x16x32_bf16 v[24:27], a[20:23], a[116:119], v[24:27]\n"
      "ds_read_b128 a[4:7], v7 offset:256\n"
      "v_mfma_f32_16x16x32_bf16 v[24:27], a[24:27], a[128:131], v[24:27]\n"
      "ds_read_b128 a[8:11], v7 offset:512\n"
      "v_mfma_f32_16x16x32_bf16 v[24:27], a[28:31], a[140:143], v[24:27]\n"
      "ds_read_b128 a[36:39], v2 offset:0\n"
      "v_mfma_f32_16x16x32_bf16 v[24:27], a[32:35], a[152:155], v[24:27]\n"
      "ds_read_b128 a[40:43], v3 offset:0\n"
      "v_mfma_f32_16x16x32_bf16 v[68:71], a[84:87], a[60:63], 0\n"
      "ds_read_b128 a[44:47], v4 offset:0\n"
      "s_nop 0\n"
      "v_mfma_f32_16x16x32_bf16 v[28:31], a[12:15], a[96:99], 0\n"
      "ds_read2_b64 a[64:67], v11 offset0:0 offset1:64\n"
      "s_nop 0\n"
      "v_mfma_f32_16x16x32_bf16 v[28:31], a[16:19], a[108:111], v[28:31]\n"
      "buffer_load_dwordx4 v16, s[32:35], s53 offen lds\n"
      "v_mfma_f32_16x16x32_bf16 v[28:31], a[20:23], a[120:123], v[28:31]\n"
      "s_mov_b32 m0, s45\n"
      "ds_read2_b64 a[68:71], v11 offset0:1 offset1:65\n"
      "v_cvt_pk_bf16_f32 v85, v68, v69\n"
      "v_mfma_f32_16x16x32_bf16 v[28:31], a[24:27], a[132:135], v[28:31]\n"
      "ds_read_b128 a[12:15], v6 offset:768\n"
      "v_cvt_pk_bf16_f32 v91, v70, v71\n"
      "v_mfma_f32_16x16x32_bf16 v[28:31], a[28:31], a[144:147], v[28:31]\n"
      "ds_read_b128 a[16:19], v6 offset:1024\n"
      "v_mfma_f32_16x16x32_bf16 v[28:31], a[32:35], a[156:159], v[28:31]\n"
      "s_waitcnt lgkmcnt(6)\n"
      "ds_read_b128 a[20:23], v6 offset:1280\n"
      "v_mfma_f32_16x16x32_bf16 v[20:23], a[0:3], a[160:163], v[20:23]\n"
      "buffer_store_dwordx2 v[84:85], v15, s[36:39], s52 offen sc1\n"
      "v_mfma_f32_16x16x32_bf16 v[20:23], a[4:7], a[172:175], v[20:23]\n"
      "ds_read_b128 a[24:27], v6 offset:5376\n"
      "v_mfma_f32_16x16x32_bf16 v[20:23], a[8:11], a[184:187], v[20:23]\n"
      "ds_read_b128 a[28:31], v6 offset:5632\n"
      "s_nop 0\n"
      "v_mfma_f32_16x16x32_bf16 v[36:39], a[36:39], a[72:75], 0\n"
      "s_waitcnt lgkmcnt(6)\n"
      "ds_read_b128 a[32:35], v6 offset:5888\n"
      "s_nop 0\n"
      "v_mfma_f32_16x16x32_bf16 v[40:43], a[40:43], a[76:79], 0\n"
      "ds_read_b128 a[48:51], v2 offset:64\n"
      "v_cvt_pk_bf16_f32 v20, v20, v21\n"
      "v_mfma_f32_16x16x32_bf16 v[44:47], a[44:47], a[80:83], v[36:39]\n"
      "buffer_store_dwordx2 v[90:91], v15, s[36:39], s52 offen offset:1536 sc1\n"
      "v_cvt_pk_bf16_f32 v21, v22, v23\n"
      "v_mfma_f32_16x16x32_bf16 v[24:27], a[0:3], a[164:167], v[24:27]\n"
      "s_mov_b32 s52, s13\n"
      "ds_write_addtid_b32 v20 offset:57360\n"
      "v_fma_f32 v48, s44, v36, -v40\n"
      "v_mfma_f32_16x16x32_bf16 v[24:27], a[4:7], a[176:179], v[24:27]\n"
      "s_add_u32 s13, s13, s12\n"
      "ds_write_addtid_b32 v21 offset:57616\n"
      "v_fma_f32 v49, s44, v37, -v41\n"
      "v_mfma_f32_16x16x32_bf16 v[24:27], a[8:11], a[188:191], v[24:27]\n"
      "ds_read_b128 a[52:55], v3 offset:64\n"
      "v_cvt_pk_bf16_f32 v48, v48, v49\n"
      "v_mfma_f32_16x16x32_bf16 v[72:75], a[84:87], a[64:67], 0\n"
      "ds_read_b128 a[56:59], v4 offset:64\n"
      "v_fma_f32 v50, s44, v38, -v42\n"
      "v_mfma_f32_16x16x32_bf16 v[28:31], a[0:3], a[168:171], v[28:31]\n"
      "ds_read_b128 a[0:3], v7 offset:768\n"
      "v_fma_f32 v51, s44, v39, -v43\n"
      "v_mfma_f32_16x16x32_bf16 v[28:31], a[4:7], a[180:183], v[28:31]\n"
      "ds_read_b128 a[4:7], v7 offset:1024\n"
      "v_cvt_pk_bf16_f32 v24, v24, v25\n"
      "v_mfma_f32_16x16x32_bf16 v[28:31], a[8:11], a[192:195], v[28:31]\n"
      "s_waitcnt lgkmcnt(6)\n"
      "ds_read_b128 a[8:11], v7 offset:1280\n"
      "v_cvt_pk_bf16_f32 v25, v26, v27\n"
      "v_mfma_f32_16x16x32_bf16 v[76:79], a[84:87], a[68:71], 0\n"
      "ds_write_addtid_b32 v24 offset:45064\n"
      "v_fmac_f32_e32 v40, s44, v36\n"
      "v_mfma_f32_16x16x32_bf16 v[32:35], a[12:15], v[92:95], 0\n"
      "ds_write_addtid_b32 v25 offset:45320\n"
      "v_fmac_f32_e32 v41, s44, v37\n"
      "v_mfma_f32_16x16x32_bf16 v[32:35], a[16:19], v[148:151], v[32:35]\n"
      "s_mov_b32 m0, s18\n"
      "ds_read2_b64 a[60:63], v11 offset0:2 offset1:66\n"
      "v_fmac_f32_e64 v42, s44, v38\n"
      "v_mfma_f32_16x16x32_bf16 v[32:35], a[20:23], v[204:207], v[32:35]\n"
      "ds_write_addtid_b32 v48 offset:6144\n"
      "v_fmac_f32_e32 v43, s44, v39\n"
      "v_mfma_f32_16x16x32_bf16 v[32:35], a[24:27], v[112:115], v[32:35]\n"
      "v_cvt_pk_bf16_f32 v49, v50, v51\n"
      "v_cvt_pk_bf16_f32 v40, v40, v41\n"
      "v_mfma_f32_16x16x32_bf16 v[32:35], a[28:31], a[208:211], v[32:35]\n"
      "v_cvt_pk_bf16_f32 v41, v42, v43\n"
      "v_cvt_pk_bf16_f32 v44, v44, v45\n"
      "v_mfma_f32_16x16x32_bf16 v[32:35], a[32:35], v[220:223], v[32:35]\n"
      "s_waitcnt lgkmcnt(5)\n"
      "ds_write_addtid_b32 v49 offset:43072\n"
      "v_cvt_pk_bf16_f32 v45, v46, v47\n"
      "v_mfma_f32_16x16x32_bf16 v[32:35], a[0:3], a[212:215], v[32:35]\n"
      "ds_write_addtid_b32 v40 offset:5376\n"
      "v_cvt_pk_bf16_f32 v28, v28, v29\n"
      "v_mfma_f32_16x16x32_bf16 v[32:35], a[4:7], v[184:187], v[32:35]\n"
      "s_waitcnt lgkmcnt(6)\n"
      "ds_write_addtid_b32 v41 offset:42304\n"
      "v_cvt_pk_bf16_f32 v29, v30, v31\n"
      "v_mfma_f32_16x16x32_bf16 v[32:35], a[8:11], v[240:243], v[32:35]\n"
      "ds_write_addtid_b32 v44 offset:4608\n"
      "v_cvt_pk_bf16_f32 v80, v72, v73\n"
      "v_mfma_f32_16x16x32_bf16 v[20:23], a[12:15], v[100:103], 0\n"
      "ds_write_addtid_b32 v45 offset:41536\n"
      "v_cvt_pk_bf16_f32 v81, v76, v77\n"
      "v_mfma_f32_16x16x32_bf16 v[20:23], a[16:19], a[200:203], v[20:23]\n"
      "s_mov_b32 m0, s45\n"
      "ds_read_b128 a[36:39], v2 offset:128\n"
      "v_cvt_pk_bf16_f32 v32, v32, v33\n"
      "v_mfma_f32_16x16x32_bf16 v[20:23], a[20:23], v[208:211], v[20:23]\n"
      "ds_write_addtid_b32 v28 offset:32768\n"
      "v_cvt_pk_bf16_f32 v33, v34, v35\n"
      "v_mfma_f32_16x16x32_bf16 v[20:23], a[24:27], a[204:207], v[20:23]\n"
      "ds_write_addtid_b32 v29 offset:33024\n"
      "v_cvt_pk_bf16_f32 v86, v74, v75\n"
      "v_mfma_f32_16x16x32_bf16 v[20:23], a[28:31], v[172:175], v[20:23]\n"
      "s_add_u32 m0, s58, 13280\n"
      "ds_read_b128 a[44:47], v4 offset:128\n"
      "v_cvt_pk_bf16_f32 v87, v78, v79\n"
      "v_mfma_f32_16x16x32_bf16 v[20:23], a[32:35], v[228:231], v[20:23]\n"
      "buffer_load_dwordx4 v17, s[32:35], s53 offen offset:1024 lds\n"
      "v_mfma_f32_16x16x32_bf16 v[20:23], a[0:3], v[136:139], v[20:23]\n"
      "s_mov_b32 m0, s45\n"
      "ds_read2_b64 a[64:67], v11 offset0:3 offset1:67\n"
      "v_mfma_f32_16x16x32_bf16 v[20:23], a[4:7], v[192:195], v[20:23]\n"
      "ds_write_addtid_b32 v32 offset:58384\n"
      "v_mfma_f32_16x16x32_bf16 v[20:23], a[8:11], v[244:247], v[20:23]\n"
      "ds_write_addtid_b32 v33 offset:58640\n"
      "s_nop 0\n"
      "v_mfma_f32_16x16x32_bf16 v[52:55], a[48:51], a[72:75], 0\n"
      "ds_read_b128 a[40:43], v3 offset:128\n"
      "s_nop 0\n"
      "v_mfma_f32_16x16x32_bf16 v[56:59], a[52:55], a[76:79], 0\n"
      "global_load_dword v8, v9, s[54:55]\n"
      "v_mfma_f32_16x16x32_bf16 v[60:63], a[56:59], a[80:83], v[52:55]\n"
      "s_waitcnt lgkmcnt(7)\n"
      "ds_read2_b64 a[68:71], v13 offset0:0 offset1:64\n"
      "v_cvt_pk_bf16_f32 v20, v20, v21\n"
      "v_mfma_f32_16x16x32_bf16 v[68:71], a[84:87], a[60:63], 0\n"
      "ds_write_addtid_b32 v20 offset:46088\n"
      "v_cvt_pk_bf16_f32 v21, v22, v23\n"
      "v_mfma_f32_16x16x32_bf16 v[24:27], a[12:15], a[196:199], 0\n"
      "ds_read_b128 a[12:15], v6 offset:1536\n"
      "v_fma_f32 v64, s44, v52, -v56\n"
      "v_mfma_f32_16x16x32_bf16 v[24:27], a[16:19], v[160:163], v[24:27]\n"
      "ds_read_b128 a[16:19], v6 offset:1792\n"
      "v_fma_f32 v65, s44, v53, -v57\n"
      "v_mfma_f32_16x16x32_bf16 v[24:27], a[20:23], v[216:219], v[24:27]\n"
      "ds_read_b128 a[20:23], v6 offset:2048\n"
      "v_cvt_pk_bf16_f32 v64, v64, v65\n"
      "v_mfma_f32_16x16x32_bf16 v[24:27], a[24:27], v[124:127], v[24:27]\n"
      "ds_read_b128 a[24:27], v6 offset:6144\n"
      "v_fma_f32 v66, s44, v54, -v58\n"
      "v_mfma_f32_16x16x32_bf16 v[24:27], a[28:31], v[180:183], v[24:27]\n"
      "ds_read_b128 a[28:31], v6 offset:6400\n"
      "v_fma_f32 v67, s44, v55, -v59\n"
      "v_mfma_f32_16x16x32_bf16 v[24:27], a[32:35], v[232:235], v[24:27]\n"
      "ds_read_b128 a[32:35], v6 offset:6656\n"
      "v_cvt_pk_bf16_f32 v65, v66, v67\n"
      "v_mfma_f32_16x16x32_bf16 v[24:27], a[0:3], a[216:219], v[24:27]\n"
      "ds_read_b128 a[0:3], v7 offset:1536\n"
      "v_fmac_f32_e64 v56, s44, v52\n"
      "v_mfma_f32_16x16x32_bf16 v[24:27], a[4:7], v[196:199], v[24:27]\n"
      "ds_read_b128 a[4:7], v7 offset:1792\n"
      "v_fmac_f32_e64 v57, s44, v53\n"
      "v_mfma_f32_16x16x32_bf16 v[24:27], a[8:11], v[252:255], v[24:27]\n"
      "ds_read_b128 a[8:11], v7 offset:2048\n"
      "v_cvt_pk_bf16_f32 v56, v56, v57\n"
      "v_mfma_f32_16x16x32_bf16 v[36:39], a[36:39], a[72:75], 0\n"
      "s_waitcnt lgkmcnt(7)\n"
      "ds_write_addtid_b32 v21 offset:46344\n"
      "v_fmac_f32_e32 v58, s44, v54\n"
      "v_mfma_f32_16x16x32_bf16 v[44:47], a[44:47], a[80:83], v[36:39]\n"
      "s_add_u32 m0, s58, 13280\n"
      "v_cvt_pk_bf16_f32 v24, v24, v25\n"
      "v_cvt_pk_bf16_f32 v25, v26, v27\n"
      "v_mfma_f32_16x16x32_bf16 v[72:75], a[84:87], a[64:67], 0\n"
      "buffer_load_dwordx4 v18, s[32:35], s53 offen offset:2048 lds\n"
      "v_fmac_f32_e32 v59, s44, v55\n"
      "v_mfma_f32_16x16x32_bf16 v[28:31], a[12:15], v[96:99], 0\n"
      "s_mov_b32 m0, s45\n"
      "v_cvt_pk_bf16_f32 v57, v58, v59\n"
      "v_cvt_pk_bf16_f32 v60, v60, v61\n"
      "v_mfma_f32_16x16x32_bf16 v[28:31], a[16:19], v[152:155], v[28:31]\n"
      "s_waitcnt lgkmcnt(4)\n"
      "ds_write_addtid_b32 v24 offset:33792\n"
      "v_cvt_pk_bf16_f32 v82, v68, v69\n"
      "v_mfma_f32_16x16x32_bf16 v[28:31], a[20:23], a[220:223], v[28:31]\n"
      "ds_write_addtid_b32 v25 offset:34048\n"
      "v_cvt_pk_bf16_f32 v83, v72, v73\n"
      "v_mfma_f32_16x16x32_bf16 v[28:31], a[24:27], v[116:119], v[28:31]\n"
      "s_mov_b32 m0, s18\n"
      "v_cvt_pk_bf16_f32 v61, v62, v63\n"
      "v_cvt_pk_bf16_f32 v88, v70, v71\n"
      "v_mfma_f32_16x16x32_bf16 v[28:31], a[28:31], v[168:171], v[28:31]\n"
      "ds_write_addtid_b32 v64 offset:6400\n"
      "v_cvt_pk_bf16_f32 v89, v74, v75\n"
      "v_mfma_f32_16x16x32_bf16 v[28:31], a[32:35], v[224:227], v[28:31]\n"
      "s_waitcnt lgkmcnt(4)\n"
      "ds_write_addtid_b32 v65 offset:43328\n"
      "v_cvt_pk_bf16_f32 v44, v44, v45\n"
      "v_mfma_f32_16x16x32_bf16 v[28:31], a[0:3], v[132:135], v[28:31]\n"
      "ds_write_addtid_b32 v56 offset:5632\n"
      "v_cvt_pk_bf16_f32 v45, v46, v47\n"
      "v_mfma_f32_16x16x32_bf16 v[28:31], a[4:7], v[188:191], v[28:31]\n"
      "ds_write_addtid_b32 v57 offset:42560\n"
      "v_mfma_f32_16x16x32_bf16 v[28:31], a[8:11], a[240:243], v[28:31]\n"
      "ds_write_addtid_b32 v60 offset:4864\n"
      "s_nop 0\n"
      "v_mfma_f32_16x16x32_bf16 v[40:43], a[40:43], a[76:79], 0\n"
      "buffer_store_dwordx4 v[80:83], v14, s[36:39], s52 offen sc1\n"
      "s_nop 0\n"
      "v_mfma_f32_16x16x32_bf16 v[20:23], a[12:15], v[104:107], 0\n"
      "ds_write_addtid_b32 v61 offset:41792\n"
      "v_mfma_f32_16x16x32_bf16 v[20:23], a[16:19], v[156:159], v[20:23]\n"
      "s_mov_b32 m0, s45\n"
      "ds_read2_b64 a[60:63], v13 offset0:1 offset1:65\n"
      "v_cvt_pk_bf16_f32 v28, v28, v29\n"
      "v_mfma_f32_16x16x32_bf16 v[20:23], a[20:23], v[212:215], v[20:23]\n"
      "ds_write_addtid_b32 v28 offset:59408\n"
      "v_cvt_pk_bf16_f32 v29, v30, v31\n"
      "v_mfma_f32_16x16x32_bf16 v[20:23], a[24:27], v[120:123], v[20:23]\n"
      "ds_write_addtid_b32 v29 offset:59664\n"
      "v_fma_f32 v48, s44, v36, -v40\n"
      "v_mfma_f32_16x16x32_bf16 v[20:23], a[28:31], v[176:179], v[20:23]\n"
      "buffer_store_dwordx4 v[86:89], v14, s[36:39], s52 offen offset:1536 sc1\n"
      "v_fma_f32 v49, s44, v37, -v41\n"
      "v_mfma_f32_16x16x32_bf16 v[20:23], a[32:35], a[232:235], v[20:23]\n"
      "s_mov_b32 m0, s18\n"
      "v_cvt_pk_bf16_f32 v48, v48, v49\n"
      "v_fma_f32 v50, s44, v38, -v42\n"
      "v_mfma_f32_16x16x32_bf16 v[20:23], a[0:3], v[140:143], v[20:23]\n"
      "s_sub_u32 s18, s19, s18\n"
      "ds_write_addtid_b32 v48 offset:6656\n"
      "v_fma_f32 v51, s44, v39, -v43\n"
      "v_mfma_f32_16x16x32_bf16 v[20:23], a[4:7], a[236:239], v[20:23]\n"
      "v_cvt_pk_bf16_f32 v49, v50, v51\n"
      "v_fmac_f32_e64 v40, s44, v36\n"
      "v_mfma_f32_16x16x32_bf16 v[20:23], a[8:11], v[248:251], v[20:23]\n"
      "ds_write_addtid_b32 v49 offset:43584\n"
      "v_fmac_f32_e64 v41, s44, v37\n"
      "v_mfma_f32_16x16x32_bf16 v[76:79], a[84:87], a[68:71], 0\n"
      "v_cvt_pk_bf16_f32 v40, v40, v41\n"
      "v_fmac_f32_e64 v42, s44, v38\n"
      "v_mfma_f32_16x16x32_bf16 v[32:35], a[12:15], v[108:111], 0\n"
      "ds_write_addtid_b32 v40 offset:5888\n"
      "v_fmac_f32_e64 v43, s44, v39\n"
      "v_mfma_f32_16x16x32_bf16 v[32:35], a[16:19], v[164:167], v[32:35]\n"
      "ds_write_addtid_b32 v44 offset:5120\n"
      "v_cvt_pk_bf16_f32 v41, v42, v43\n"
      "v_mfma_f32_16x16x32_bf16 v[32:35], a[20:23], a[224:227], v[32:35]\n"
      "ds_write_addtid_b32 v41 offset:42816\n"
      "v_cvt_pk_bf16_f32 v20, v20, v21\n"
      "v_mfma_f32_16x16x32_bf16 v[32:35], a[24:27], v[128:131], v[32:35]\n"
      "ds_write_addtid_b32 v45 offset:42048\n"
      "v_cvt_pk_bf16_f32 v21, v22, v23\n"
      "v_mfma_f32_16x16x32_bf16 v[32:35], a[28:31], a[228:231], v[32:35]\n"
      "s_mov_b32 m0, s45\n"
      "v_cvt_pk_bf16_f32 v84, v76, v77\n"
      "v_cvt_pk_bf16_f32 v90, v78, v79\n"
      "v_mfma_f32_16x16x32_bf16 v[32:35], a[32:35], v[236:239], v[32:35]\n"
      "ds_write_addtid_b32 v20 offset:47112\n"
      "v_mfma_f32_16x16x32_bf16 v[32:35], a[0:3], v[144:147], v[32:35]\n"
      "ds_write_addtid_b32 v21 offset:47368\n"
      "v_mfma_f32_16x16x32_bf16 v[32:35], a[4:7], v[200:203], v[32:35]\n"
      "v_mfma_f32_16x16x32_bf16 v[32:35], a[8:11], a[244:247], v[32:35]\n"
      "s_nop 1\n"
      "s_cbranch_vccnz Lconv_loop0\n"
      "Lconv_t0:\n"
      "s_nop 1\n"
      "v_mfma_f32_16x16x32_bf16 v[20:23], a[24:27], v[96:99], 0\n"
      "s_nop 1\n"
      "v_mfma_f32_16x16x32_bf16 v[20:23], a[28:31], v[152:155], v[20:23]\n"
      "v_cvt_pk_bf16_f32 v32, v32, v33\n"
      "v_cvt_pk_bf16_f32 v33, v34, v35\n"
      "v_mfma_f32_16x16x32_bf16 v[20:23], a[32:35], a[220:223], v[20:23]\n"
      "ds_write_addtid_b32 v32 offset:34816\n"
      "v_mfma_f32_16x16x32_bf16 v[20:23], a[0:3], v[116:119], v[20:23]\n"
      "ds_write_addtid_b32 v33 offset:35072\n"
      "v_mfma_f32_16x16x32_bf16 v[20:23], a[4:7], v[168:171], v[20:23]\n"
      "v_mfma_f32_16x16x32_bf16 v[20:23], a[8:11], v[224:227], v[20:23]\n"
      "v_mfma_f32_16x16x32_bf16 v[24:27], a[24:27], v[104:107], 0\n"
      "s_waitcnt lgkmcnt(0)\n"
      "s_barrier\n"
      "v_mfma_f32_16x16x32_bf16 v[24:27], a[28:31], v[156:159], v[24:27]\n"
      "ds_read2_b64 a[64:67], v10 offset0:0 offset1:64\n"
      "v_mfma_f32_16x16x32_bf16 v[24:27], a[32:35], v[212:215], v[24:27]\n"
      "ds_read_b128 a[12:15], v7 offset:6144\n"
      "v_mfma_f32_16x16x32_bf16 v[24:27], a[0:3], v[120:123], v[24:27]\n"
      "ds_read2_b64 a[68:71], v10 offset0:1 offset1:65\n"
      "v_mfma_f32_16x16x32_bf16 v[24:27], a[4:7], v[176:179], v[24:27]\n"
      "ds_read_b128 a[16:19], v7 offset:6400\n"
      "v_mfma_f32_16x16x32_bf16 v[24:27], a[8:11], a[232:235], v[24:27]\n"
      "ds_read_b128 a[20:23], v7 offset:6656\n"
      "v_mfma_f32_16x16x32_bf16 v[68:71], a[84:87], a[60:63], 0\n"
      "ds_read2_b64 a[60:63], v10 offset0:2 offset1:66\n"
      "s_nop 0\n"
      "v_mfma_f32_16x16x32_bf16 v[28:31], a[24:27], v[108:111], 0\n"
      "s_waitcnt lgkmcnt(5)\n"
      "ds_read_b128 a[24:27], v6 offset:5376\n"
      "s_nop 0\n"
      "v_mfma_f32_16x16x32_bf16 v[72:75], a[84:87], a[64:67], 0\n"
      "ds_read2_b64 a[64:67], v10 offset0:3 offset1:67\n"
      "v_cvt_pk_bf16_f32 v85, v68, v69\n"
      "v_mfma_f32_16x16x32_bf16 v[28:31], a[28:31], v[164:167], v[28:31]\n"
      "buffer_store_dwordx2 v[84:85], v15, s[36:39], s52 offen sc1\n"
      "v_cvt_pk_bf16_f32 v91, v70, v71\n"
      "v_mfma_f32_16x16x32_bf16 v[28:31], a[32:35], a[224:227], v[28:31]\n"
      "ds_read_b128 a[28:31], v6 offset:5632\n"
      "v_mfma_f32_16x16x32_bf16 v[28:31], a[0:3], v[128:131], v[28:31]\n"
      "ds_read_b128 a[32:35], v6 offset:5888\n"
      "v_cvt_pk_bf16_f32 v80, v72, v73\n"
      "v_mfma_f32_16x16x32_bf16 v[28:31], a[4:7], a[228:231], v[28:31]\n"
      "ds_read_b128 a[0:3], v7 offset:768\n"
      "v_cvt_pk_bf16_f32 v86, v74, v75\n"
      "v_mfma_f32_16x16x32_bf16 v[28:31], a[8:11], v[236:239], v[28:31]\n"
      "s_waitcnt lgkmcnt(6)\n"
      "ds_read_b128 a[4:7], v7 offset:1024\n"
      "v_mfma_f32_16x16x32_bf16 v[20:23], a[12:15], v[132:135], v[20:23]\n"
      "ds_read_b128 a[8:11], v7 offset:1280\n"
      "v_mfma_f32_16x16x32_bf16 v[76:79], a[84:87], a[68:71], 0\n"
      "s_waitcnt lgkmcnt(6)\n"
      "ds_read2_b64 a[68:71], v12 offset0:0 offset1:64\n"
      "s_nop 0\n"
      "v_mfma_f32_16x16x32_bf16 v[68:71], a[84:87], a[60:63], 0\n"
      "ds_read2_b64 a[60:63], v12 offset0:1 offset1:65\n"
      "s_nop 0\n"
      "v_mfma_f32_16x16x32_bf16 v[20:23], a[16:19], v[188:191], v[20:23]\n"
      "buffer_store_dwordx2 v[90:91], v15, s[36:39], s52 offen offset:1536 sc1\n"
      "v_cvt_pk_bf16_f32 v81, v76, v77\n"
      "v_mfma_f32_16x16x32_bf16 v[20:23], a[20:23], a[240:243], v[20:23]\n"
      "s_mov_b32 s52, s13\n"
      "v_cvt_pk_bf16_f32 v87, v78, v79\n"
      "v_cvt_pk_bf16_f32 v82, v68, v69\n"
      "v_mfma_f32_16x16x32_bf16 v[24:27], a[12:15], v[140:143], v[24:27]\n"
      "s_waitcnt lgkmcnt(5)\n"
      "v_cvt_pk_bf16_f32 v88, v70, v71\n"
      "s_nop 0\n"
      "v_mfma_f32_16x16x32_bf16 v[72:75], a[84:87], a[64:67], 0\n"
      "s_add_u32 s13, s13, s12\n"
      "v_cvt_pk_bf16_f32 v20, v20, v21\n"
      "v_cvt_pk_bf16_f32 v21, v22, v23\n"
      "v_mfma_f32_16x16x32_bf16 v[24:27], a[16:19], a[236:239], v[24:27]\n"
      "ds_write_addtid_b32 v20 offset:62480\n"
      "s_nop 0\n"
      "v_mfma_f32_16x16x32_bf16 v[24:27], a[20:23], v[248:251], v[24:27]\n"
      "ds_write_addtid_b32 v21 offset:62736\n"
      "v_cvt_pk_bf16_f32 v83, v72, v73\n"
      "v_mfma_f32_16x16x32_bf16 v[28:31], a[12:15], v[144:147], v[28:31]\n"
      "s_waitcnt lgkmcnt(3)\n"
      "ds_read_b128 a[12:15], v7 offset:5376\n"
      "v_cvt_pk_bf16_f32 v89, v74, v75\n"
      "v_mfma_f32_16x16x32_bf16 v[76:79], a[84:87], a[68:71], 0\n"
      "buffer_store_dwordx4 v[80:83], v14, s[36:39], s52 offen sc1\n"
      "v_cvt_pk_bf16_f32 v24, v24, v25\n"
      "v_mfma_f32_16x16x32_bf16 v[28:31], a[16:19], v[200:203], v[28:31]\n"
      "ds_read_b128 a[16:19], v7 offset:5632\n"
      "v_cvt_pk_bf16_f32 v25, v26, v27\n"
      "v_mfma_f32_16x16x32_bf16 v[28:31], a[20:23], a[244:247], v[28:31]\n"
      "ds_read_b128 a[20:23], v7 offset:5888\n"
      "v_mfma_f32_16x16x32_bf16 v[32:35], a[24:27], v[92:95], 0\n"
      "s_waitcnt lgkmcnt(5)\n"
      "ds_write_addtid_b32 v24 offset:50184\n"
      "v_cvt_pk_bf16_f32 v84, v76, v77\n"
      "v_mfma_f32_16x16x32_bf16 v[68:71], a[84:87], a[60:63], 0\n"
      "ds_write_addtid_b32 v25 offset:50440\n"
      "v_cvt_pk_bf16_f32 v90, v78, v79\n"
      "v_mfma_f32_16x16x32_bf16 v[32:35], a[28:31], v[148:151], v[32:35]\n"
      "buffer_store_dwordx4 v[86:89], v14, s[36:39], s52 offen offset:1536 sc1\n"
      "v_cvt_pk_bf16_f32 v28, v28, v29\n"
      "v_mfma_f32_16x16x32_bf16 v[32:35], a[32:35], v[204:207], v[32:35]\n"
      "ds_write_addtid_b32 v28 offset:37888\n"
      "v_cvt_pk_bf16_f32 v29, v30, v31\n"
      "v_mfma_f32_16x16x32_bf16 v[32:35], a[0:3], v[112:115], v[32:35]\n"
      "v_cvt_pk_bf16_f32 v85, v68, v69\n"
      "v_cvt_pk_bf16_f32 v91, v70, v71\n"
      "v_mfma_f32_16x16x32_bf16 v[32:35], a[4:7], a[208:211], v[32:35]\n"
      "ds_write_addtid_b32 v29 offset:38144\n"
      "v_mfma_f32_16x16x32_bf16 v[32:35], a[8:11], v[220:223], v[32:35]\n"
      "s_waitcnt lgkmcnt(5)\n"
      "v_mfma_f32_16x16x32_bf16 v[32:35], a[12:15], a[212:215], v[32:35]\n"
      "buffer_store_dwordx2 v[84:85], v15, s[36:39], s52 offen sc1\n"
      "v_mfma_f32_16x16x32_bf16 v[32:35], a[16:19], v[184:187], v[32:35]\n"
      "s_waitcnt lgkmcnt(4)\n"
      "v_mfma_f32_16x16x32_bf16 v[32:35], a[20:23], v[240:243], v[32:35]\n"
      "s_nop 1\n"
      "v_mfma_f32_16x16x32_bf16 v[20:23], a[24:27], v[100:103], 0\n"
      "s_nop 1\n"
      "v_mfma_f32_16x16x32_bf16 v[20:23], a[28:31], a[200:203], v[20:23]\n"
      "s_nop 0\n"
      "v_mfma_f32_16x16x32_bf16 v[20:23], a[32:35], v[208:211], v[20:23]\n"
      "buffer_store_dwordx2 v[90:91], v15, s[36:39], s52 offen offset:1536 sc1\n"
      "v_cvt_pk_bf16_f32 v32, v32, v33\n"
      "v_mfma_f32_16x16x32_bf16 v[20:23], a[0:3], a[204:207], v[20:23]\n"
      "ds_write_addtid_b32 v32 offset:61456\n"
      "v_cvt_pk_bf16_f32 v33, v34, v35\n"
      "v_mfma_f32_16x16x32_bf16 v[20:23], a[4:7], v[172:175], v[20:23]\n"
      "ds_write_addtid_b32 v33 offset:61712\n"
      "v_mfma_f32_16x16x32_bf16 v[20:23], a[8:11], v[228:231], v[20:23]\n"
      "v_mfma_f32_16x16x32_bf16 v[20:23], a[12:15], v[136:139], v[20:23]\n"
      "v_mfma_f32_16x16x32_bf16 v[20:23], a[16:19], v[192:195], v[20:23]\n"
      "v_mfma_f32_16x16x32_bf16 v[20:23], a[20:23], v[244:247], v[20:23]\n"
      "s_nop 1\n"
      "v_mfma_f32_16x16x32_bf16 v[24:27], a[24:27], a[196:199], 0\n"
      "ds_read_b128 a[24:27], v7 offset:4608\n"
      "s_nop 0\n"
      "v_mfma_f32_16x16x32_bf16 v[24:27], a[28:31], v[160:163], v[24:27]\n"
      "ds_read_b128 a[28:31], v7 offset:4864\n"
      "v_mfma_f32_16x16x32_bf16 v[24:27], a[32:35], v[216:219], v[24:27]\n"
      "ds_read_b128 a[32:35], v7 offset:5120\n"
      "v_cvt_pk_bf16_f32 v20, v20, v21\n"
      "v_mfma_f32_16x16x32_bf16 v[24:27], a[0:3], v[124:127], v[24:27]\n"
      "ds_read_b128 a[0:3], v6 offset:4608\n"
      "v_cvt_pk_bf16_f32 v21, v22, v23\n"
      "v_mfma_f32_16x16x32_bf16 v[24:27], a[4:7], v[180:183], v[24:27]\n"
      "ds_read_b128 a[4:7], v6 offset:4864\n"
      "v_mfma_f32_16x16x32_bf16 v[24:27], a[8:11], v[232:235], v[24:27]\n"
      "ds_read_b128 a[8:11], v6 offset:5120\n"
      "v_mfma_f32_16x16x32_bf16 v[24:27], a[12:15], a[216:219], v[24:27]\n"
      "ds_read_b128 a[12:15], v7 offset:0\n"
      "v_mfma_f32_16x16x32_bf16 v[24:27], a[16:19], v[196:199], v[24:27]\n"
      "ds_read_b128 a[16:19], v7 offset:256\n"
      "v_mfma_f32_16x16x32_bf16 v[24:27], a[20:23], v[252:255], v[24:27]\n"
      "s_waitcnt lgkmcnt(7)\n"
      "ds_read_b128 a[20:23], v7 offset:512\n"
      "s_nop 0\n"
      "v_mfma_f32_16x16x32_bf16 v[28:31], a[24:27], a[160:163], 0\n"
      "s_waitcnt lgkmcnt(7)\n"
      "ds_write_addtid_b32 v20 offset:49160\n"
      "s_nop 0\n"
      "v_mfma_f32_16x16x32_bf16 v[28:31], a[28:31], a[172:175], v[28:31]\n"
      "s_waitcnt lgkmcnt(7)\n"
      "ds_write_addtid_b32 v21 offset:49416\n"
      "v_cvt_pk_bf16_f32 v24, v24, v25\n"
      "v_mfma_f32_16x16x32_bf16 v[28:31], a[32:35], a[184:187], v[28:31]\n"
      "s_waitcnt lgkmcnt(7)\n"
      "ds_write_addtid_b32 v24 offset:36864\n"
      "v_cvt_pk_bf16_f32 v25, v26, v27\n"
      "v_mfma_f32_16x16x32_bf16 v[28:31], a[0:3], a[88:91], v[28:31]\n"
      "s_waitcnt lgkmcnt(7)\n"
      "ds_write_addtid_b32 v25 offset:37120\n"
      "v_mfma_f32_16x16x32_bf16 v[28:31], a[4:7], a[100:103], v[28:31]\n"
      "s_waitcnt lgkmcnt(7)\n"
      "v_mfma_f32_16x16x32_bf16 v[28:31], a[8:11], a[112:115], v[28:31]\n"
      "s_waitcnt lgkmcnt(6)\n"
      "v_mfma_f32_16x16x32_bf16 v[28:31], a[12:15], a[124:127], v[28:31]\n"
      "s_waitcnt lgkmcnt(5)\n"
      "v_mfma_f32_16x16x32_bf16 v[28:31], a[16:19], a[136:139], v[28:31]\n"
      "s_waitcnt lgkmcnt(4)\n"
      "v_mfma_f32_16x16x32_bf16 v[28:31], a[20:23], a[148:151], v[28:31]\n"
      "s_nop 1\n"
      "v_mfma_f32_16x16x32_bf16 v[20:23], a[24:27], a[164:167], 0\n"
      "s_nop 1\n"
      "v_mfma_f32_16x16x32_bf16 v[20:23], a[28:31], a[176:179], v[20:23]\n"
      "s_nop 0\n"
      "v_mfma_f32_16x16x32_bf16 v[20:23], a[32:35], a[188:191], v[20:23]\n"
      "v_cvt_pk_bf16_f32 v28, v28, v29\n"
      "v_cvt_pk_bf16_f32 v29, v30, v31\n"
      "v_mfma_f32_16x16x32_bf16 v[20:23], a[0:3], a[92:95], v[20:23]\n"
      "ds_write_addtid_b32 v28 offset:60432\n"
      "v_mfma_f32_16x16x32_bf16 v[20:23], a[4:7], a[104:107], v[20:23]\n"
      "ds_write_addtid_b32 v29 offset:60688\n"
      "v_mfma_f32_16x16x32_bf16 v[20:23], a[8:11], a[116:119], v[20:23]\n"
      "v_mfma_f32_16x16x32_bf16 v[20:23], a[12:15], a[128:131], v[20:23]\n"
      "v_mfma_f32_16x16x32_bf16 v[20:23], a[16:19], a[140:143], v[20:23]\n"
      "v_mfma_f32_16x16x32_bf16 v[20:23], a[20:23], a[152:155], v[20:23]\n"
      "s_nop 1\n"
      "v_mfma_f32_16x16x32_bf16 v[32:35], a[24:27], a[168:171], 0\n"
      "s_nop 1\n"
      "v_mfma_f32_16x16x32_bf16 v[32:35], a[28:31], a[180:183], v[32:35]\n"
      "s_nop 0\n"
      "v_mfma_f32_16x16x32_bf16 v[32:35], a[32:35], a[192:195], v[32:35]\n"
      "v_cvt_pk_bf16_f32 v20, v20, v21\n"
      "v_cvt_pk_bf16_f32 v21, v22, v23\n"
      "v_mfma_f32_16x16x32_bf16 v[32:35], a[0:3], a[96:99], v[32:35]\n"
      "ds_write_addtid_b32 v20 offset:48136\n"
      "v_mfma_f32_16x16x32_bf16 v[32:35], a[4:7], a[108:111], v[32:35]\n"
      "ds_write_addtid_b32 v21 offset:48392\n"
      "v_mfma_f32_16x16x32_bf16 v[32:35], a[8:11], a[120:123], v[32:35]\n"
      "v_mfma_f32_16x16x32_bf16 v[32:35], a[12:15], a[132:135], v[32:35]\n"
      "v_mfma_f32_16x16x32_bf16 v[32:35], a[16:19], a[144:147], v[32:35]\n"
      "v_mfma_f32_16x16x32_bf16 v[32:35], a[20:23], a[156:159], v[32:35]\n"
      "s_nop 1\n"
      "s_nop 5\n"
      "v_cvt_pk_bf16_f32 v32, v32, v33\n"
      "v_cvt_pk_bf16_f32 v33, v34, v35\n"
      "ds_write_addtid_b32 v32 offset:35840\n"
      "ds_write_addtid_b32 v33 offset:36096\n"
      "s_waitcnt lgkmcnt(0)\n"
      "s_barrier\n"
      "ds_read2_b64 a[0:3], v11 offset0:0 offset1:64\n"
      "ds_read2_b64 a[4:7], v11 offset0:1 offset1:65\n"
      "ds_read2_b64 a[8:11], v11 offset0:2 offset1:66\n"
      "ds_read2_b64 a[12:15], v11 offset0:3 offset1:67\n"
      "ds_read2_b64 a[16:19], v13 offset0:0 offset1:64\n"
      "ds_read2_b64 a[20:23], v13 offset0:1 offset1:65\n"
      "s_waitcnt lgkmcnt(5)\n"
      "v_mfma_f32_16x16x32_bf16 v[72:75], a[84:87], a[0:3], 0\n"
      "s_waitcnt lgkmcnt(4)\n"
      "v_mfma_f32_16x16x32_bf16 v[76:79], a[84:87], a[4:7], 0\n"
      "s_waitcnt lgkmcnt(3)\n"
      "v_mfma_f32_16x16x32_bf16 v[68:71], a[84:87], a[8:11], 0\n"
      "s_waitcnt lgkmcnt(2)\n"
      "s_nop 2\n"
      "v_cvt_pk_bf16_f32 v80, v72, v73\n"
      "v_cvt_pk_bf16_f32 v86, v74, v75\n"
      "v_mfma_f32_16x16x32_bf16 v[72:75], a[84:87], a[12:15], 0\n"
      "s_waitcnt lgkmcnt(1)\n"
      "v_cvt_pk_bf16_f32 v81, v76, v77\n"
      "v_cvt_pk_bf16_f32 v87, v78, v79\n"
      "v_cvt_pk_bf16_f32 v82, v68, v69\n"
      "v_cvt_pk_bf16_f32 v88, v70, v71\n"
      "v_mfma_f32_16x16x32_bf16 v[76:79], a[84:87], a[16:19], 0\n"
      "s_waitcnt lgkmcnt(0)\n"
      "v_mfma_f32_16x16x32_bf16 v[68:71], a[84:87], a[20:23], 0\n"
      "s_mov_b32 s52, s13\n"
      "v_cvt_pk_bf16_f32 v83, v72, v73\n"
      "v_cvt_pk_bf16_f32 v89, v74, v75\n"
      "s_add_u32 s13, s13, s12\n"
      "buffer_store_dwordx4 v[80:83], v14, s[36:39], s52 offen sc1\n"
      "buffer_store_dwordx4 v[86:89], v14, s[36:39], s52 offen offset:1536 sc1\n"
      "v_cvt_pk_bf16_f32 v84, v76, v77\n"
      "v_cvt_pk_bf16_f32 v90, v78, v79\n"
      "v_cvt_pk_bf16_f32 v85, v68, v69\n"
      "v_cvt_pk_bf16_f32 v91, v70, v71\n"
      "buffer_store_dwordx2 v[84:85], v15, s[36:39], s52 offen sc1\n"
      "buffer_store_dwordx2 v[90:91], v15, s[36:39], s52 offen offset:1536 sc1\n"
      "Lconv_u0:\n"
      "s_waitcnt vmcnt(0)\n"
      "s_endpgm\n"
      ".p2align 12\n"
      "Lconv_pa1:\n"
      "s_cbranch_execz Lconv_r0\n"
      "s_nop 0\n"
      "Lconv_loop1:\n"
      "v_mfma_f32_16x16x32_bf16 v[20:23], a[24:27], v[96:99], 0\n"
      "s_add_u32 s51, s51, s12\n"
      "v_cvt_pk_bf16_f32 v32, v32, v33\n"
      "v_cvt_pk_bf16_f32 v33, v34, v35\n"
      "v_mfma_f32_16x16x32_bf16 v[20:23], a[28:31], v[152:155], v[20:23]\n"
      "s_sub_u32 s58, s93, s58\n"
      "v_cvt_pk_bf16_f32 v84, v72, v73\n"
      "v_cvt_pk_bf16_f32 v90, v74, v75\n"
      "v_mfma_f32_16x16x32_bf16 v[20:23], a[32:35], a[220:223], v[20:23]\n"
      "s_sub_u32 s59, s94, s59\n"
      "v_cvt_pk_bf16_f32 v85, v76, v77\n"
      "v_cvt_pk_bf16_f32 v91, v78, v79\n"
      "v_mfma_f32_16x16x32_bf16 v[20:23], a[0:3], v[116:119], v[20:23]\n"
      "buffer_store_dwordx2 v[84:85], v15, s[36:39], s52 offen sc1\n"
      "v_mfma_f32_16x16x32_bf16 v[20:23], a[4:7], v[168:171], v[20:23]\n"
      "s_sub_u32 s47, s51, s48\n"
      "ds_write_addtid_b32 v32 offset:34816\n"
      "v_mfma_f32_16x16x32_bf16 v[20:23], a[8:11], v[224:227], v[20:23]\n"
      "s_cmp_ge_u32 vcc_lo, 0x80000000\n"
      "ds_write_addtid_b32 v33 offset:35072\n"
      "v_mfma_f32_16x16x32_bf16 v[24:27], a[24:27], v[104:107], 0\n"
      "buffer_store_dwordx2 v[90:91], v15, s[36:39], s52 offen offset:1536 sc1\n"
      "v_mfma_f32_16x16x32_bf16 v[24:27], a[28:31], v[156:159], v[24:27]\n"
      "s_cselect_b32 s53, s47, s51\n"
      "v_mfma_f32_16x16x32_bf16 v[24:27], a[32:35], v[212:215], v[24:27]\n"
      "s_mov_b32 s52, s13\n"
      "v_mfma_f32_16x16x32_bf16 v[24:27], a[0:3], v[120:123], v[24:27]\n"
      "s_add_u32 s13, s13, s12\n"
      "v_mfma_f32_16x16x32_bf16 v[24:27], a[4:7], v[176:179], v[24:27]\n"
      "s_waitcnt vmcnt(17) lgkmcnt(0)\n"
      "s_barrier\n"
      "v_mfma_f32_16x16x32_bf16 v[24:27], a[8:11], a[232:235], v[24:27]\n"
      "s_mov_b32 m0, s58\n"
      "ds_read2_b64 a[60:63], v10 offset0:0 offset1:64\n"
      "v_mfma_f32_16x16x32_bf16 v[28:31], a[24:27], v[108:111], 0\n"
      "ds_read_b128 a[12:15], v7 offset:6144\n"
      "v_mfma_f32_16x16x32_bf16 v[28:31], a[28:31], v[164:167], v[28:31]\n"
      "ds_read_b128 a[16:19], v7 offset:6400\n"
      "v_mfma_f32_16x16x32_bf16 v[28:31], a[32:35], a[224:227], v[28:31]\n"
      "ds_read_b128 a[20:23], v7 offset:6656\n"
      "v_mfma_f32_16x16x32_bf16 v[28:31], a[0:3], v[128:131], v[28:31]\n"
      "ds_read_b128 a[48:51], v2 offset:13280\n"
      "v_mfma_f32_16x16x32_bf16 v[28:31], a[4:7], a[228:231], v[28:31]\n"
      "ds_read_b128 a[56:59], v4 offset:13280\n"
      "v_mfma_f32_16x16x32_bf16 v[28:31], a[8:11], v[236:239], v[28:31]\n"
      "s_waitcnt lgkmcnt(5)\n"
      "ds_read_b128 a[52:55], v3 offset:13280\n"
      "v_mfma_f32_16x16x32_bf16 v[68:71], a[84:87], a[60:63], 0\n"
      "s_waitcnt lgkmcnt(5)\n"
      "ds_read_b128 a[24:27], v6 offset:5376\n"
      "v_mfma_f32_16x16x32_bf16 v[20:23], a[12:15], v[132:135], v[20:23]\n"
      "s_waitcnt lgkmcnt(5)\n"
      "ds_read_b128 a[28:31], v6 offset:5632\n"
      "v_mfma_f32_16x16x32_bf16 v[20:23], a[16:19], v[188:191], v[20:23]\n"
      "s_waitcnt lgkmcnt(5)\n"
      "ds_read_b128 a[32:35], v6 offset:5888\n"
      "v_cvt_pk_bf16_f32 v80, v68, v69\n"
      "v_mfma_f32_16x16x32_bf16 v[20:23], a[20:23], a[240:243], v[20:23]\n"
      "ds_read_b128 a[0:3], v7 offset:768\n"
      "v_cvt_pk_bf16_f32 v86, v70, v71\n"
      "v_mfma_f32_16x16x32_bf16 v[24:27], a[12:15], v[140:143], v[24:27]\n"
      "ds_read_b128 a[4:7], v7 offset:1024\n"
      "s_nop 0\n"
      "v_mfma_f32_16x16x32_bf16 v[24:27], a[16:19], a[236:239], v[24:27]\n"
      "ds_read_b128 a[8:11], v7 offset:1280\n"
      "v_mfma_f32_16x16x32_bf16 v[24:27], a[20:23], v[248:251], v[24:27]\n"
      "s_waitcnt lgkmcnt(7)\n"
      "ds_read2_b64 a[64:67], v10 offset0:1 offset1:65\n"
      "v_cvt_pk_bf16_f32 v20, v20, v21\n"
      "v_mfma_f32_16x16x32_bf16 v[52:55], a[48:51], a[72:75], 0\n"
      "buffer_load_dwordx4 v16, s[32:35], s53 offen lds\n"
      "v_cvt_pk_bf16_f32 v21, v22, v23\n"
      "v_mfma_f32_16x16x32_bf16 v[60:63], a[56:59], a[80:83], v[52:55]\n"
      "s_mov_b32 m0, s45\n"
      "ds_read_b128 a[36:39], v2 offset:13344\n"
      "v_cvt_pk_bf16_f32 v24, v24, v25\n"
      "v_mfma_f32_16x16x32_bf16 v[28:31], a[12:15], v[144:147], v[28:31]\n"
      "ds_read_b128 a[12:15], v7 offset:5376\n"
      "v_cvt_pk_bf16_f32 v25, v26, v27\n"
      "v_mfma_f32_16x16x32_bf16 v[28:31], a[16:19], v[200:203], v[28:31]\n"
      "ds_read_b128 a[16:19], v7 offset:5632\n"
      "v_cvt_pk_bf16_f32 v60, v60, v61\n"
      "v_mfma_f32_16x16x32_bf16 v[28:31], a[20:23], a[244:247], v[28:31]\n"
      "s_waitcnt lgkmcnt(6)\n"
      "ds_read_b128 a[20:23], v7 offset:5888\n"
      "v_cvt_pk_bf16_f32 v61, v62, v63\n"
      "v_mfma_f32_16x16x32_bf16 v[56:59], a[52:55], a[76:79], 0\n"
      "ds_read_b128 a[40:43], v3 offset:13344\n"
      "s_nop 0\n"
      "v_mfma_f32_16x16x32_bf16 v[32:35], a[24:27], v[92:95], 0\n"
      "ds_write_addtid_b32 v20 offset:62480\n"
      "v_cvt_pk_bf16_f32 v28, v28, v29\n"
      "v_mfma_f32_16x16x32_bf16 v[32:35], a[28:31], v[148:151], v[32:35]\n"
      "ds_read_b128 a[44:47], v4 offset:13344\n"
      "v_cvt_pk_bf16_f32 v29, v30, v31\n"
      "v_mfma_f32_16x16x32_bf16 v[32:35], a[32:35], v[204:207], v[32:35]\n"
      "ds_write_addtid_b32 v21 offset:62736\n"
      "v_fma_f32 v64, s44, v52, -v56\n"
      "v_mfma_f32_16x16x32_bf16 v[32:35], a[0:3], v[112:115], v[32:35]\n"
      "s_waitcnt lgkmcnt(7)\n"
      "ds_read_b128 a[48:51], v2 offset:13408\n"
      "v_fma_f32 v65, s44, v53, -v57\n"
      "v_mfma_f32_16x16x32_bf16 v[32:35], a[4:7], a[208:211], v[32:35]\n"
      "ds_read_b128 a[56:59], v4 offset:13408\n"
      "v_fma_f32 v66, s44, v54, -v58\n"
      "v_mfma_f32_16x16x32_bf16 v[32:35], a[8:11], v[220:223], v[32:35]\n"
      "s_waitcnt lgkmcnt(7)\n"
      "ds_write_addtid_b32 v24 offset:50184\n"
      "v_fma_f32 v67, s44, v55, -v59\n"
      "v_mfma_f32_16x16x32_bf16 v[32:35], a[12:15], a[212:215], v[32:35]\n"
      "ds_read2_b64 a[68:71], v10 offset0:2 offset1:66\n"
      "v_fmac_f32_e32 v56, s44, v52\n"
      "v_mfma_f32_16x16x32_bf16 v[32:35], a[16:19], v[184:187], v[32:35]\n"
      "s_waitcnt lgkmcnt(7)\n"
      "ds_write_addtid_b32 v25 offset:50440\n"
      "v_fmac_f32_e32 v57, s44, v53\n"
      "v_mfma_f32_16x16x32_bf16 v[32:35], a[20:23], v[240:243], v[32:35]\n"
      "ds_write_addtid_b32 v28 offset:37888\n"
      "v_fmac_f32_e64 v58, s44, v54\n"
      "v_mfma_f32_16x16x32_bf16 v[72:75], a[84:87], a[64:67], 0\n"
      "ds_write_addtid_b32 v29 offset:38144\n"
      "v_fmac_f32_e32 v59, s44, v55\n"
      "v_mfma_f32_16x16x32_bf16 v[20:23], a[24:27], v[100:103], 0\n"
      "s_mov_b32 m0, s18\n"
      "v_cvt_pk_bf16_f32 v64, v64, v65\n"
      "v_cvt_pk_bf16_f32 v65, v66, v67\n"
      "v_mfma_f32_16x16x32_bf16 v[20:23], a[28:31], a[200:203], v[20:23]\n"
      "ds_write_addtid_b32 v64 offset:1536\n"
      "v_cvt_pk_bf16_f32 v56, v56, v57\n"
      "v_mfma_f32_16x16x32_bf16 v[20:23], a[32:35], v[208:211], v[20:23]\n"
      "ds_write_addtid_b32 v65 offset:38464\n"
      "v_cvt_pk_bf16_f32 v57, v58, v59\n"
      "v_mfma_f32_16x16x32_bf16 v[20:23], a[0:3], a[204:207], v[20:23]\n"
      "ds_write_addtid_b32 v56 offset:768\n"
      "v_cvt_pk_bf16_f32 v32, v32, v33\n"
      "v_mfma_f32_16x16x32_bf16 v[20:23], a[4:7], v[172:175], v[20:23]\n"
      "ds_write_addtid_b32 v57 offset:37696\n"
      "v_cvt_pk_bf16_f32 v33, v34, v35\n"
      "v_mfma_f32_16x16x32_bf16 v[20:23], a[8:11], v[228:231], v[20:23]\n"
      "ds_write_addtid_b32 v60 offset:0\n"
      "v_cvt_pk_bf16_f32 v81, v72, v73\n"
      "v_mfma_f32_16x16x32_bf16 v[20:23], a[12:15], v[136:139], v[20:23]\n"
      "ds_write_addtid_b32 v61 offset:36928\n"
      "v_cvt_pk_bf16_f32 v87, v74, v75\n"
      "v_mfma_f32_16x16x32_bf16 v[20:23], a[16:19], v[192:195], v[20:23]\n"
      "s_mov_b32 m0, s45\n"
      "ds_read_b128 a[52:55], v3 offset:13408\n"
      "v_add_u32_e32 v2, s91, v2\n"
      "v_mfma_f32_16x16x32_bf16 v[20:23], a[20:23], v[244:247], v[20:23]\n"
      "s_mov_b32 m0, s58\n"
      "ds_read2_b64 a[60:63], v10 offset0:3 offset1:67\n"
      "v_add_u32_e32 v4, s91, v4\n"
      "v_mfma_f32_16x16x32_bf16 v[36:39], a[36:39], a[72:75], 0\n"
      "buffer_load_dwordx4 v17, s[32:35], s53 offen offset:1024 lds\n"
      "v_add_u32_e64 v3, s91, v3\n"
      "v_mfma_f32_16x16x32_bf16 v[40:43], a[40:43], a[76:79], 0\n"
      "s_waitcnt lgkmcnt(11)\n"
      "ds_read2_b64 a[64:67], v12 offset0:0 offset1:64\n"
      "v_cvt_pk_bf16_f32 v20, v20, v21\n"
      "v_mfma_f32_16x16x32_bf16 v[44:47], a[44:47], a[80:83], v[36:39]\n"
      "s_mov_b32 m0, s45\n"
      "v_cvt_pk_bf16_f32 v21, v22, v23\n"
      "s_nop 0\n"
      "v_mfma_f32_16x16x32_bf16 v[52:55], a[48:51], a[72:75], 0\n"
      "s_sub_u32 s91, 0, s91\n"
      "ds_write_addtid_b32 v32 offset:61456\n"
      "v_fma_f32 v48, s44, v36, -v40\n"
      "v_mfma_f32_16x16x32_bf16 v[60:63], a[56:59], a[80:83], v[52:55]\n"
      "ds_write_addtid_b32 v33 offset:61712\n"
      "v_fma_f32 v49, s44, v37, -v41\n"
      "v_mfma_f32_16x16x32_bf16 v[76:79], a[84:87], a[68:71], 0\n"
      "ds_write_addtid_b32 v20 offset:49160\n"
      "v_fma_f32 v50, s44, v38, -v42\n"
      "v_mfma_f32_16x16x32_bf16 v[24:27], a[24:27], a[196:199], 0\n"
      "ds_read_b128 a[24:27], v7 offset:4608\n"
      "v_fma_f32 v51, s44, v39, -v43\n"
      "v_mfma_f32_16x16x32_bf16 v[24:27], a[28:31], v[160:163], v[24:27]\n"
      "ds_read_b128 a[28:31], v7 offset:4864\n"
      "v_fmac_f32_e64 v40, s44, v36\n"
      "v_mfma_f32_16x16x32_bf16 v[24:27], a[32:35], v[216:219], v[24:27]\n"
      "ds_read_b128 a[32:35], v7 offset:5120\n"
      "v_fmac_f32_e64 v41, s44, v37\n"
      "v_mfma_f32_16x16x32_bf16 v[24:27], a[0:3], v[124:127], v[24:27]\n"
      "ds_read_b128 a[0:3], v6 offset:4608\n"
      "v_fmac_f32_e64 v42, s44, v38\n"
      "v_mfma_f32_16x16x32_bf16 v[24:27], a[4:7], v[180:183], v[24:27]\n"
      "ds_read_b128 a[4:7], v6 offset:4864\n"
      "v_fmac_f32_e64 v43, s44, v39\n"
      "v_mfma_f32_16x16x32_bf16 v[24:27], a[8:11], v[232:235], v[24:27]\n"
      "ds_read_b128 a[8:11], v6 offset:5120\n"
      "v_cvt_pk_bf16_f32 v48, v48, v49\n"
      "v_mfma_f32_16x16x32_bf16 v[24:27], a[12:15], a[216:219], v[24:27]\n"
      "ds_read_b128 a[12:15], v7 offset:0\n"
      "v_cvt_pk_bf16_f32 v49, v50, v51\n"
      "v_mfma_f32_16x16x32_bf16 v[24:27], a[16:19], v[196:199], v[24:27]\n"
      "ds_read_b128 a[16:19], v7 offset:256\n"
      "v_cvt_pk_bf16_f32 v40, v40, v41\n"
      "v_mfma_f32_16x16x32_bf16 v[24:27], a[20:23], v[252:255], v[24:27]\n"
      "s_waitcnt lgkmcnt(7)\n"
      "ds_read_b128 a[20:23], v7 offset:512\n"
      "v_cvt_pk_bf16_f32 v41, v42, v43\n"
      "v_mfma_f32_16x16x32_bf16 v[56:59], a[52:55], a[76:79], 0\n"
      "ds_write_addtid_b32 v21 offset:49416\n"
      "v_cvt_pk_bf16_f32 v44, v44, v45\n"
      "v_mfma_f32_16x16x32_bf16 v[28:31], a[24:27], a[160:163], 0\n"
      "s_waitcnt lgkmcnt(7)\n"
      "v_cvt_pk_bf16_f32 v45, v46, v47\n"
      "v_cvt_pk_bf16_f32 v24, v24, v25\n"
      "v_mfma_f32_16x16x32_bf16 v[28:31], a[28:31], a[172:175], v[28:31]\n"
      "s_mov_b32 m0, s18\n"
      "v_cvt_pk_bf16_f32 v25, v26, v27\n"
      "v_fma_f32 v64, s44, v52, -v56\n"
      "v_mfma_f32_16x16x32_bf16 v[28:31], a[32:35], a[184:187], v[28:31]\n"
      "s_waitcnt lgkmcnt(5)\n"
      "ds_write_addtid_b32 v48 offset:1792\n"
      "v_fma_f32 v65, s44, v53, -v57\n"
      "v_mfma_f32_16x16x32_bf16 v[28:31], a[0:3], a[88:91], v[28:31]\n"
      "ds_write_addtid_b32 v49 offset:38720\n"
      "v_cvt_pk_bf16_f32 v64, v64, v65\n"
      "v_mfma_f32_16x16x32_bf16 v[28:31], a[4:7], a[100:103], v[28:31]\n"
      "s_waitcnt lgkmcnt(5)\n"
      "ds_write_addtid_b32 v40 offset:1024\n"
      "v_fma_f32 v66, s44, v54, -v58\n"
      "v_mfma_f32_16x16x32_bf16 v[28:31], a[8:11], a[112:115], v[28:31]\n"
      "ds_write_addtid_b32 v41 offset:37952\n"
      "v_fma_f32 v67, s44, v55, -v59\n"
      "v_mfma_f32_16x16x32_bf16 v[28:31], a[12:15], a[124:127], v[28:31]\n"
      "s_waitcnt lgkmcnt(5)\n"
      "ds_write_addtid_b32 v44 offset:256\n"
      "v_cvt_pk_bf16_f32 v65, v66, v67\n"
      "v_mfma_f32_16x16x32_bf16 v[28:31], a[16:19], a[136:139], v[28:31]\n"
      "ds_write_addtid_b32 v45 offset:37184\n"
      "v_fmac_f32_e64 v56, s44, v52\n"
      "v_mfma_f32_16x16x32_bf16 v[28:31], a[20:23], a[148:151], v[28:31]\n"
      "s_mov_b32 m0, s45\n"
      "v_fmac_f32_e32 v57, s44, v53\n"
      "v_cvt_pk_bf16_f32 v56, v56, v57\n"
      "v_mfma_f32_16x16x32_bf16 v[20:23], a[24:27], a[164:167], 0\n"
      "s_mov_b32 m0, s58\n"
      "v_fmac_f32_e32 v58, s44, v54\n"
      "v_fmac_f32_e64 v59, s44, v55\n"
      "v_mfma_f32_16x16x32_bf16 v[20:23], a[28:31], a[176:179], v[20:23]\n"
      "buffer_load_dwordx4 v18, s[32:35], s53 offen offset:2048 lds\n"
      "v_cvt_pk_bf16_f32 v57, v58, v59\n"
      "v_mfma_f32_16x16x32_bf16 v[20:23], a[32:35], a[188:191], v[20:23]\n"
      "s_mov_b32 m0, s45\n"
      "ds_read2_b64 a[68:71], v12 offset0:1 offset1:65\n"
      "v_cvt_pk_bf16_f32 v60, v60, v61\n"
      "v_mfma_f32_16x16x32_bf16 v[20:23], a[0:3], a[92:95], v[20:23]\n"
      "ds_write_addtid_b32 v24 offset:36864\n"
      "v_cvt_pk_bf16_f32 v61, v62, v63\n"
      "v_mfma_f32_16x16x32_bf16 v[20:23], a[4:7], a[104:107], v[20:23]\n"
      "ds_write_addtid_b32 v25 offset:37120\n"
      "v_cvt_pk_bf16_f32 v28, v28, v29\n"
      "v_mfma_f32_16x16x32_bf16 v[20:23], a[8:11], a[116:119], v[20:23]\n"
      "s_mov_b32 m0, s18\n"
      "v_cvt_pk_bf16_f32 v29, v30, v31\n"
      "v_cvt_pk_bf16_f32 v82, v76, v77\n"
      "v_mfma_f32_16x16x32_bf16 v[20:23], a[12:15], a[128:131], v[20:23]\n"
      "ds_write_addtid_b32 v64 offset:2048\n"
      "v_cvt_pk_bf16_f32 v88, v78, v79\n"
      "v_mfma_f32_16x16x32_bf16 v[20:23], a[16:19], a[140:143], v[20:23]\n"
      "ds_write_addtid_b32 v65 offset:38976\n"
      "v_swap_b32 v6, v7\n"
      "v_mfma_f32_16x16x32_bf16 v[20:23], a[20:23], a[152:155], v[20:23]\n"
      "ds_write_addtid_b32 v56 offset:1280\n"
      "s_nop 0\n"
      "v_mfma_f32_16x16x32_bf16 v[68:71], a[84:87], a[60:63], 0\n"
      "ds_write_addtid_b32 v57 offset:38208\n"
      "s_nop 0\n"
      "v_mfma_f32_16x16x32_bf16 v[32:35], a[24:27], a[168:171], 0\n"
      "ds_write_addtid_b32 v60 offset:512\n"
      "v_mfma_f32_16x16x32_bf16 v[32:35], a[28:31], a[180:183], v[32:35]\n"
      "ds_write_addtid_b32 v61 offset:37440\n"
      "v_cvt_pk_bf16_f32 v20, v20, v21\n"
      "v_mfma_f32_16x16x32_bf16 v[32:35], a[32:35], a[192:195], v[32:35]\n"
      "s_mov_b32 m0, s45\n"
      "v_cvt_pk_bf16_f32 v83, v68, v69\n"
      "v_cvt_pk_bf16_f32 v89, v70, v71\n"
      "v_mfma_f32_16x16x32_bf16 v[32:35], a[0:3], a[96:99], v[32:35]\n"
      "ds_write_addtid_b32 v28 offset:60432\n"
      "v_cvt_pk_bf16_f32 v21, v22, v23\n"
      "v_mfma_f32_16x16x32_bf16 v[32:35], a[4:7], a[108:111], v[32:35]\n"
      "ds_write_addtid_b32 v29 offset:60688\n"
      "v_mfma_f32_16x16x32_bf16 v[32:35], a[8:11], a[120:123], v[32:35]\n"
      "buffer_store_dwordx4 v[80:83], v14, s[36:39], s52 offen sc1\n"
      "v_mfma_f32_16x16x32_bf16 v[32:35], a[12:15], a[132:135], v[32:35]\n"
      "ds_write_addtid_b32 v20 offset:48136\n"
      "v_mfma_f32_16x16x32_bf16 v[32:35], a[16:19], a[144:147], v[32:35]\n"
      "ds_write_addtid_b32 v21 offset:48392\n"
      "v_mfma_f32_16x16x32_bf16 v[32:35], a[20:23], a[156:159], v[32:35]\n"
      "buffer_store_dwordx4 v[86:89], v14, s[36:39], s52 offen offset:1536 sc1\n"
      "s_nop 0\n"
      "v_mfma_f32_16x16x32_bf16 v[72:75], a[84:87], a[64:67], 0\n"
      "s_waitcnt lgkmcnt(12)\n"
      "s_nop 1\n"
      "v_mfma_f32_16x16x32_bf16 v[76:79], a[84:87], a[68:71], 0\n"
      "v_mfma_f32_16x16x32_bf16 v[20:23], a[12:15], a[88:91], 0\n"
      "s_add_u32 s51, s51, s12\n"
      "v_cvt_pk_bf16_f32 v32, v32, v33\n"
      "v_cvt_pk_bf16_f32 v33, v34, v35\n"
      "v_mfma_f32_16x16x32_bf16 v[20:23], a[16:19], a[100:103], v[20:23]\n"
      "s_sub_u32 s47, s51, s48\n"
      "v_cvt_pk_bf16_f32 v84, v72, v73\n"
      "v_cvt_pk_bf16_f32 v90, v74, v75\n"
      "v_mfma_f32_16x16x32_bf16 v[20:23], a[20:23], a[112:115], v[20:23]\n"
      "s_cmp_ge_u32 vcc_lo, 0x40000000\n"
      "v_cvt_pk_bf16_f32 v85, v76, v77\n"
      "v_cvt_pk_bf16_f32 v91, v78, v79\n"
      "v_mfma_f32_16x16x32_bf16 v[20:23], a[24:27], a[124:127], v[20:23]\n"
      "buffer_store_dwordx2 v[84:85], v15, s[36:39], s52 offen sc1\n"
      "v_mfma_f32_16x16x32_bf16 v[20:23], a[28:31], a[136:139], v[20:23]\n"
      "s_cselect_b32 s53, s47, s51\n"
      "ds_write_addtid_b32 v32 offset:35840\n"
      "v_mfma_f32_16x16x32_bf16 v[20:23], a[32:35], a[148:151], v[20:23]\n"
      "s_mul_i32 vcc_lo, vcc_lo, 2\n"
      "ds_write_addtid_b32 v33 offset:36096\n"
      "v_mfma_f32_16x16x32_bf16 v[24:27], a[12:15], a[92:95], 0\n"
      "buffer_store_dwordx2 v[90:91], v15, s[36:39], s52 offen offset:1536 sc1\n"
      "v_mfma_f32_16x16x32_bf16 v[24:27], a[16:19], a[104:107], v[24:27]\n"
      "s_mov_b32 s52, s13\n"
      "v_mfma_f32_16x16x32_bf16 v[24:27], a[20:23], a[116:119], v[24:27]\n"
      "v_mfma_f32_16x16x32_bf16 v[24:27], a[24:27], a[128:131], v[24:27]\n"
      "v_mfma_f32_16x16x32_bf16 v[24:27], a[28:31], a[140:143], v[24:27]\n"
      "s_waitcnt vmcnt(19) lgkmcnt(0)\n"
      "s_barrier\n"
      "v_mfma_f32_16x16x32_bf16 v[24:27], a[32:35], a[152:155], v[24:27]\n"
      "s_add_u32 m0, s58, 13280\n"
      "ds_read2_b64 a[60:63], v11 offset0:0 offset1:64\n"
      "v_mfma_f32_16x16x32_bf16 v[28:31], a[12:15], a[96:99], 0\n"
      "s_add_u32 s13, s13, s12\n"
      "ds_read_b128 a[0:3], v7 offset:0\n"
      "v_mfma_f32_16x16x32_bf16 v[28:31], a[16:19], a[108:111], v[28:31]\n"
      "ds_read_b128 a[4:7], v7 offset:256\n"
      "v_mfma_f32_16x16x32_bf16 v[28:31], a[20:23], a[120:123], v[28:31]\n"
      "ds_read_b128 a[8:11], v7 offset:512\n"
      "v_mfma_f32_16x16x32_bf16 v[28:31], a[24:27], a[132:135], v[28:31]\n"
      "ds_read_b128 a[36:39], v2 offset:0\n"
      "v_mfma_f32_16x16x32_bf16 v[28:31], a[28:31], a[144:147], v[28:31]\n"
      "ds_read_b128 a[44:47], v4 offset:0\n"
      "v_mfma_f32_16x16x32_bf16 v[28:31], a[32:35], a[156:159], v[28:31]\n"
      "s_waitcnt lgkmcnt(5)\n"
      "ds_read_b128 a[40:43], v3 offset:0\n"
      "v_mfma_f32_16x16x32_bf16 v[68:71], a[84:87], a[60:63], 0\n"
      "s_waitcnt lgkmcnt(5)\n"
      "ds_read_b128 a[12:15], v6 offset:768\n"
      "v_mfma_f32_16x16x32_bf16 v[20:23], a[0:3], a[160:163], v[20:23]\n"
      "s_waitcnt lgkmcnt(5)\n"
      "ds_read_b128 a[16:19], v6 offset:1024\n"
      "v_mfma_f32_16x16x32_bf16 v[20:23], a[4:7], a[172:175], v[20:23]\n"
      "s_waitcnt lgkmcnt(5)\n"
      "ds_read_b128 a[20:23], v6 offset:1280\n"
      "v_cvt_pk_bf16_f32 v80, v68, v69\n"
      "v_mfma_f32_16x16x32_bf16 v[20:23], a[8:11], a[184:187], v[20:23]\n"
      "ds_read_b128 a[24:27], v6 offset:5376\n"
      "v_cvt_pk_bf16_f32 v86, v70, v71\n"
      "v_mfma_f32_16x16x32_bf16 v[24:27], a[0:3], a[164:167], v[24:27]\n"
      "ds_read_b128 a[28:31], v6 offset:5632\n"
      "s_nop 0\n"
      "v_mfma_f32_16x16x32_bf16 v[24:27], a[4:7], a[176:179], v[24:27]\n"
      "ds_read_b128 a[32:35], v6 offset:5888\n"
      "v_mfma_f32_16x16x32_bf16 v[24:27], a[8:11], a[188:191], v[24:27]\n"
      "s_waitcnt lgkmcnt(7)\n"
      "ds_read2_b64 a[64:67], v11 offset0:1 offset1:65\n"
      "v_cvt_pk_bf16_f32 v20, v20, v21\n"
      "v_mfma_f32_16x16x32_bf16 v[36:39], a[36:39], a[72:75], 0\n"
      "buffer_load_dwordx4 v16, s[32:35], s53 offen lds\n"
      "v_cvt_pk_bf16_f32 v21, v22, v23\n"
      "v_mfma_f32_16x16x32_bf16 v[44:47], a[44:47], a[80:83], v[36:39]\n"
      "s_mov_b32 m0, s45\n"
      "ds_read_b128 a[48:51], v2 offset:64\n"
      "v_cvt_pk_bf16_f32 v24, v24, v25\n"
      "v_mfma_f32_16x16x32_bf16 v[28:31], a[0:3], a[168:171], v[28:31]\n"
      "ds_read_b128 a[0:3], v7 offset:768\n"
      "v_cvt_pk_bf16_f32 v25, v26, v27\n"
      "v_mfma_f32_16x16x32_bf16 v[28:31], a[4:7], a[180:183], v[28:31]\n"
      "ds_read_b128 a[4:7], v7 offset:1024\n"
      "v_cvt_pk_bf16_f32 v44, v44, v45\n"
      "v_mfma_f32_16x16x32_bf16 v[28:31], a[8:11], a[192:195], v[28:31]\n"
      "s_waitcnt lgkmcnt(6)\n"
      "ds_read_b128 a[8:11], v7 offset:1280\n"
      "v_cvt_pk_bf16_f32 v45, v46, v47\n"
      "v_mfma_f32_16x16x32_bf16 v[40:43], a[40:43], a[76:79], 0\n"
      "ds_read_b128 a[52:55], v3 offset:64\n"
      "s_nop 0\n"
      "v_mfma_f32_16x16x32_bf16 v[32:35], a[12:15], v[92:95], 0\n"
      "ds_write_addtid_b32 v20 offset:57360\n"
      "v_cvt_pk_bf16_f32 v28, v28, v29\n"
      "v_mfma_f32_16x16x32_bf16 v[32:35], a[16:19], v[148:151], v[32:35]\n"
      "ds_read_b128 a[56:59], v4 offset:64\n"
      "v_cvt_pk_bf16_f32 v29, v30, v31\n"
      "v_mfma_f32_16x16x32_bf16 v[32:35], a[20:23], v[204:207], v[32:35]\n"
      "ds_write_addtid_b32 v21 offset:57616\n"
      "v_fma_f32 v48, s44, v36, -v40\n"
      "v_mfma_f32_16x16x32_bf16 v[32:35], a[24:27], v[112:115], v[32:35]\n"
      "s_waitcnt lgkmcnt(7)\n"
      "ds_read_b128 a[36:39], v2 offset:128\n"
      "v_fma_f32 v49, s44, v37, -v41\n"
      "v_mfma_f32_16x16x32_bf16 v[32:35], a[28:31], a[208:211], v[32:35]\n"
      "ds_read_b128 a[44:47], v4 offset:128\n"
      "v_fma_f32 v50, s44, v38, -v42\n"
      "v_mfma_f32_16x16x32_bf16 v[32:35], a[32:35], v[220:223], v[32:35]\n"
      "s_waitcnt lgkmcnt(7)\n"
      "ds_write_addtid_b32 v24 offset:45064\n"
      "v_fma_f32 v51, s44, v39, -v43\n"
      "v_mfma_f32_16x16x32_bf16 v[32:35], a[0:3], a[212:215], v[32:35]\n"
      "ds_read2_b64 a[68:71], v11 offset0:2 offset1:66\n"
      "v_fmac_f32_e32 v40, s44, v36\n"
      "v_mfma_f32_16x16x32_bf16 v[32:35], a[4:7], v[184:187], v[32:35]\n"
      "s_waitcnt lgkmcnt(7)\n"
      "ds_write_addtid_b32 v25 offset:45320\n"
      "v_fmac_f32_e64 v41, s44, v37\n"
      "v_mfma_f32_16x16x32_bf16 v[32:35], a[8:11], v[240:243], v[32:35]\n"
      "ds_write_addtid_b32 v28 offset:32768\n"
      "v_fmac_f32_e64 v42, s44, v38\n"
      "v_mfma_f32_16x16x32_bf16 v[72:75], a[84:87], a[64:67], 0\n"
      "ds_write_addtid_b32 v29 offset:33024\n"
      "v_fmac_f32_e32 v43, s44, v39\n"
      "v_mfma_f32_16x16x32_bf16 v[20:23], a[12:15], v[100:103], 0\n"
      "s_mov_b32 m0, s18\n"
      "v_cvt_pk_bf16_f32 v48, v48, v49\n"
      "v_cvt_pk_bf16_f32 v49, v50, v51\n"
      "v_mfma_f32_16x16x32_bf16 v[20:23], a[16:19], a[200:203], v[20:23]\n"
      "ds_write_addtid_b32 v48 offset:6144\n"
      "v_cvt_pk_bf16_f32 v40, v40, v41\n"
      "v_mfma_f32_16x16x32_bf16 v[20:23], a[20:23], v[208:211], v[20:23]\n"
      "ds_write_addtid_b32 v49 offset:43072\n"
      "v_cvt_pk_bf16_f32 v41, v42, v43\n"
      "v_mfma_f32_16x16x32_bf16 v[20:23], a[24:27], a[204:207], v[20:23]\n"
      "ds_write_addtid_b32 v40 offset:5376\n"
      "v_cvt_pk_bf16_f32 v32, v32, v33\n"
      "v_mfma_f32_16x16x32_bf16 v[20:23], a[28:31], v[172:175], v[20:23]\n"
      "ds_write_addtid_b32 v41 offset:42304\n"
      "v_cvt_pk_bf16_f32 v33, v34, v35\n"
      "v_mfma_f32_16x16x32_bf16 v[20:23], a[32:35], v[228:231], v[20:23]\n"
      "ds_write_addtid_b32 v44 offset:4608\n"
      "v_cvt_pk_bf16_f32 v81, v72, v73\n"
      "v_mfma_f32_16x16x32_bf16 v[20:23], a[0:3], v[136:139], v[20:23]\n"
      "ds_write_addtid_b32 v45 offset:41536\n"
      "v_cvt_pk_bf16_f32 v87, v74, v75\n"
      "v_mfma_f32_16x16x32_bf16 v[20:23], a[4:7], v[192:195], v[20:23]\n"
      "s_mov_b32 m0, s45\n"
      "ds_read_b128 a[40:43], v3 offset:128\n"
      "v_mfma_f32_16x16x32_bf16 v[20:23], a[8:11], v[244:247], v[20:23]\n"
      "s_add_u32 m0, s58, 13280\n"
      "ds_read2_b64 a[60:63], v11 offset0:3 offset1:67\n"
      "s_nop 0\n"
      "v_mfma_f32_16x16x32_bf16 v[52:55], a[48:51], a[72:75], 0\n"
      "buffer_load_dwordx4 v17, s[32:35], s53 offen offset:1024 lds\n"
      "s_nop 0\n"
      "v_mfma_f32_16x16x32_bf16 v[56:59], a[52:55], a[76:79], 0\n"
      "s_waitcnt lgkmcnt(11)\n"
      "ds_read2_b64 a[64:67], v13 offset0:0 offset1:64\n"
      "v_cvt_pk_bf16_f32 v20, v20, v21\n"
      "v_mfma_f32_16x16x32_bf16 v[60:63], a[56:59], a[80:83], v[52:55]\n"
      "s_mov_b32 m0, s45\n"
      "v_cvt_pk_bf16_f32 v21, v22, v23\n"
      "s_nop 0\n"
      "v_mfma_f32_16x16x32_bf16 v[36:39], a[36:39], a[72:75], 0\n"
      "ds_write_addtid_b32 v32 offset:58384\n"
      "v_fma_f32 v64, s44, v52, -v56\n"
      "v_mfma_f32_16x16x32_bf16 v[44:47], a[44:47], a[80:83], v[36:39]\n"
      "ds_write_addtid_b32 v33 offset:58640\n"
      "v_fma_f32 v65, s44, v53, -v57\n"
      "v_mfma_f32_16x16x32_bf16 v[76:79], a[84:87], a[68:71], 0\n"
      "global_load_dword v8, v9, s[54:55]\n"
      "v_fma_f32 v66, s44, v54, -v58\n"
      "v_mfma_f32_16x16x32_bf16 v[24:27], a[12:15], a[196:199], 0\n"
      "ds_read_b128 a[12:15], v6 offset:1536\n"
      "v_fma_f32 v67, s44, v55, -v59\n"
      "v_mfma_f32_16x16x32_bf16 v[24:27], a[16:19], v[160:163], v[24:27]\n"
      "ds_read_b128 a[16:19], v6 offset:1792\n"
      "v_fmac_f32_e64 v56, s44, v52\n"
      "v_mfma_f32_16x16x32_bf16 v[24:27], a[20:23], v[216:219], v[24:27]\n"
      "ds_read_b128 a[20:23], v6 offset:2048\n"
      "v_fmac_f32_e64 v57, s44, v53\n"
      "v_mfma_f32_16x16x32_bf16 v[24:27], a[24:27], v[124:127], v[24:27]\n"
      "ds_read_b128 a[24:27], v6 offset:6144\n"
      "v_fmac_f32_e64 v58, s44, v54\n"
      "v_mfma_f32_16x16x32_bf16 v[24:27], a[28:31], v[180:183], v[24:27]\n"
      "ds_read_b128 a[28:31], v6 offset:6400\n"
      "v_fmac_f32_e64 v59, s44, v55\n"
      "v_mfma_f32_16x16x32_bf16 v[24:27], a[32:35], v[232:235], v[24:27]\n"
      "ds_read_b128 a[32:35], v6 offset:6656\n"
      "v_cvt_pk_bf16_f32 v64, v64, v65\n"
      "v_mfma_f32_16x16x32_bf16 v[24:27], a[0:3], a[216:219], v[24:27]\n"
      "ds_read_b128 a[0:3], v7 offset:1536\n"
      "v_cvt_pk_bf16_f32 v65, v66, v67\n"
      "v_mfma_f32_16x16x32_bf16 v[24:27], a[4:7], v[196:199], v[24:27]\n"
      "ds_read_b128 a[4:7], v7 offset:1792\n"
      "v_cvt_pk_bf16_f32 v56, v56, v57\n"
      "v_mfma_f32_16x16x32_bf16 v[24:27], a[8:11], v[252:255], v[24:27]\n"
      "s_waitcnt lgkmcnt(7)\n"
      "ds_read_b128 a[8:11], v7 offset:2048\n"
      "v_cvt_pk_bf16_f32 v57, v58, v59\n"
      "v_mfma_f32_16x16x32_bf16 v[40:43], a[40:43], a[76:79], 0\n"
      "ds_write_addtid_b32 v20 offset:46088\n"
      "v_cvt_pk_bf16_f32 v60, v60, v61\n"
      "v_mfma_f32_16x16x32_bf16 v[28:31], a[12:15], v[96:99], 0\n"
      "s_waitcnt lgkmcnt(7)\n"
      "ds_write_addtid_b32 v21 offset:46344\n"
      "v_cvt_pk_bf16_f32 v61, v62, v63\n"
      "v_mfma_f32_16x16x32_bf16 v[28:31], a[16:19], v[152:155], v[28:31]\n"
      "s_mov_b32 m0, s18\n"
      "v_cvt_pk_bf16_f32 v24, v24, v25\n"
      "v_cvt_pk_bf16_f32 v25, v26, v27\n"
      "v_mfma_f32_16x16x32_bf16 v[28:31], a[20:23], a[220:223], v[28:31]\n"
      "s_waitcnt lgkmcnt(6)\n"
      "ds_write_addtid_b32 v64 offset:6400\n"
      "v_fma_f32 v48, s44, v36, -v40\n"
      "v_mfma_f32_16x16x32_bf16 v[28:31], a[24:27], v[116:119], v[28:31]\n"
      "ds_write_addtid_b32 v65 offset:43328\n"
      "v_fma_f32 v49, s44, v37, -v41\n"
      "v_mfma_f32_16x16x32_bf16 v[28:31], a[28:31], v[168:171], v[28:31]\n"
      "s_waitcnt lgkmcnt(6)\n"
      "ds_write_addtid_b32 v56 offset:5632\n"
      "v_cvt_pk_bf16_f32 v48, v48, v49\n"
      "v_mfma_f32_16x16x32_bf16 v[28:31], a[32:35], v[224:227], v[28:31]\n"
      "ds_write_addtid_b32 v57 offset:42560\n"
      "v_fma_f32 v50, s44, v38, -v42\n"
      "v_mfma_f32_16x16x32_bf16 v[28:31], a[0:3], v[132:135], v[28:31]\n"
      "s_waitcnt lgkmcnt(6)\n"
      "ds_write_addtid_b32 v60 offset:4864\n"
      "v_fma_f32 v51, s44, v39, -v43\n"
      "v_mfma_f32_16x16x32_bf16 v[28:31], a[4:7], v[188:191], v[28:31]\n"
      "ds_write_addtid_b32 v61 offset:41792\n"
      "v_cvt_pk_bf16_f32 v49, v50, v51\n"
      "v_mfma_f32_16x16x32_bf16 v[28:31], a[8:11], a[240:243], v[28:31]\n"
      "s_mov_b32 m0, s45\n"
      "v_fmac_f32_e32 v40, s44, v36\n"
      "v_fmac_f32_e64 v41, s44, v37\n"
      "v_mfma_f32_16x16x32_bf16 v[20:23], a[12:15], v[104:107], 0\n"
      "s_add_u32 m0, s58, 13280\n"
      "v_cvt_pk_bf16_f32 v40, v40, v41\n"
      "v_fmac_f32_e64 v42, s44, v38\n"
      "v_mfma_f32_16x16x32_bf16 v[20:23], a[16:19], v[156:159], v[20:23]\n"
      "buffer_load_dwordx4 v18, s[32:35], s53 offen offset:2048 lds\n"
      "v_fmac_f32_e32 v43, s44, v39\n"
      "v_mfma_f32_16x16x32_bf16 v[20:23], a[20:23], v[212:215], v[20:23]\n"
      "s_mov_b32 m0, s45\n"
      "v_cvt_pk_bf16_f32 v41, v42, v43\n"
      "v_cvt_pk_bf16_f32 v44, v44, v45\n"
      "v_mfma_f32_16x16x32_bf16 v[20:23], a[24:27], v[120:123], v[20:23]\n"
      "ds_write_addtid_b32 v24 offset:33792\n"
      "v_cvt_pk_bf16_f32 v45, v46, v47\n"
      "v_mfma_f32_16x16x32_bf16 v[20:23], a[28:31], v[176:179], v[20:23]\n"
      "ds_write_addtid_b32 v25 offset:34048\n"
      "v_cvt_pk_bf16_f32 v28, v28, v29\n"
      "v_mfma_f32_16x16x32_bf16 v[20:23], a[32:35], a[232:235], v[20:23]\n"
      "s_mov_b32 m0, s18\n"
      "ds_read2_b64 a[68:71], v13 offset0:1 offset1:65\n"
      "v_cvt_pk_bf16_f32 v29, v30, v31\n"
      "v_mfma_f32_16x16x32_bf16 v[20:23], a[0:3], v[140:143], v[20:23]\n"
      "s_sub_u32 s18, s19, s18\n"
      "ds_write_addtid_b32 v48 offset:6656\n"
      "v_cvt_pk_bf16_f32 v82, v76, v77\n"
      "v_mfma_f32_16x16x32_bf16 v[20:23], a[4:7], a[236:239], v[20:23]\n"
      "ds_write_addtid_b32 v49 offset:43584\n"
      "v_cvt_pk_bf16_f32 v88, v78, v79\n"
      "v_mfma_f32_16x16x32_bf16 v[20:23], a[8:11], v[248:251], v[20:23]\n"
      "ds_write_addtid_b32 v40 offset:5888\n"
      "s_nop 0\n"
      "v_mfma_f32_16x16x32_bf16 v[68:71], a[84:87], a[60:63], 0\n"
      "ds_write_addtid_b32 v41 offset:42816\n"
      "s_nop 0\n"
      "v_mfma_f32_16x16x32_bf16 v[32:35], a[12:15], v[108:111], 0\n"
      "ds_write_addtid_b32 v44 offset:5120\n"
      "v_mfma_f32_16x16x32_bf16 v[32:35], a[16:19], v[164:167], v[32:35]\n"
      "ds_write_addtid_b32 v45 offset:42048\n"
      "v_cvt_pk_bf16_f32 v20, v20, v21\n"
      "v_mfma_f32_16x16x32_bf16 v[32:35], a[20:23], a[224:227], v[32:35]\n"
      "s_mov_b32 m0, s45\n"
      "v_cvt_pk_bf16_f32 v83, v68, v69\n"
      "v_cvt_pk_bf16_f32 v89, v70, v71\n"
      "v_mfma_f32_16x16x32_bf16 v[32:35], a[24:27], v[128:131], v[32:35]\n"
      "ds_write_addtid_b32 v28 offset:59408\n"
      "v_cvt_pk_bf16_f32 v21, v22, v23\n"
      "v_mfma_f32_16x16x32_bf16 v[32:35], a[28:31], a[228:231], v[32:35]\n"
      "ds_write_addtid_b32 v29 offset:59664\n"
      "v_mfma_f32_16x16x32_bf16 v[32:35], a[32:35], v[236:239], v[32:35]\n"
      "buffer_store_dwordx4 v[80:83], v14, s[36:39], s52 offen sc1\n"
      "v_mfma_f32_16x16x32_bf16 v[32:35], a[0:3], v[144:147], v[32:35]\n"
      "ds_write_addtid_b32 v20 offset:47112\n"
      "v_mfma_f32_16x16x32_bf16 v[32:35], a[4:7], v[200:203], v[32:35]\n"
      "ds_write_addtid_b32 v21 offset:47368\n"
      "v_mfma_f32_16x16x32_bf16 v[32:35], a[8:11], a[244:247], v[32:35]\n"
      "buffer_store_dwordx4 v[86:89], v14, s[36:39], s52 offen offset:1536 sc1\n"
      "s_nop 0\n"
      "v_mfma_f32_16x16x32_bf16 v[72:75], a[84:87], a[64:67], 0\n"
      "s_waitcnt lgkmcnt(10)\n"
      "s_nop 1\n"
      "v_mfma_f32_16x16x32_bf16 v[76:79], a[84:87], a[68:71], 0\n"
      "s_cbranch_vccnz Lconv_loop1\n"
      "Lconv_t1:\n"
      "v_mfma_f32_16x16x32_bf16 v[20:23], a[24:27], v[96:99], 0\n"
      "v_cvt_pk_bf16_f32 v32, v32, v33\n"
      "v_cvt_pk_bf16_f32 v33, v34, v35\n"
      "v_mfma_f32_16x16x32_bf16 v[20:23], a[28:31], v[152:155], v[20:23]\n"
      "ds_write_addtid_b32 v32 offset:34816\n"
      "v_cvt_pk_bf16_f32 v84, v72, v73\n"
      "v_mfma_f32_16x16x32_bf16 v[20:23], a[32:35], a[220:223], v[20:23]\n"
      "ds_write_addtid_b32 v33 offset:35072\n"
      "v_cvt_pk_bf16_f32 v90, v74, v75\n"
      "v_mfma_f32_16x16x32_bf16 v[20:23], a[0:3], v[116:119], v[20:23]\n"
      "v_cvt_pk_bf16_f32 v85, v76, v77\n"
      "v_cvt_pk_bf16_f32 v91, v78, v79\n"
      "v_mfma_f32_16x16x32_bf16 v[20:23], a[4:7], v[168:171], v[20:23]\n"
      "buffer_store_dwordx2 v[84:85], v15, s[36:39], s52 offen sc1\n"
      "v_mfma_f32_16x16x32_bf16 v[20:23], a[8:11], v[224:227], v[20:23]\n"
      "v_mfma_f32_16x16x32_bf16 v[24:27], a[24:27], v[104:107], 0\n"
      "v_mfma_f32_16x16x32_bf16 v[24:27], a[28:31], v[156:159], v[24:27]\n"
      "buffer_store_dwordx2 v[90:91], v15, s[36:39], s52 offen offset:1536 sc1\n"
      "v_mfma_f32_16x16x32_bf16 v[24:27], a[32:35], v[212:215], v[24:27]\n"
      "s_mov_b32 s52, s13\n"
      "v_mfma_f32_16x16x32_bf16 v[24:27], a[0:3], v[120:123], v[24:27]\n"
      "s_add_u32 s13, s13, s12\n"
      "v_mfma_f32_16x16x32_bf16 v[24:27], a[4:7], v[176:179], v[24:27]\n"
      "s_waitcnt lgkmcnt(0)\n"
      "s_barrier\n"
      "ds_read2_b64 a[60:63], v10 offset0:0 offset1:64\n"
      "v_mfma_f32_16x16x32_bf16 v[24:27], a[8:11], a[232:235], v[24:27]\n"
      "s_waitcnt lgkmcnt(0)\n"
      "ds_read2_b64 a[64:67], v10 offset0:1 offset1:65\n"
      "v_mfma_f32_16x16x32_bf16 v[68:71], a[84:87], a[60:63], 0\n"
      "ds_read2_b64 a[68:71], v10 offset0:2 offset1:66\n"
      "v_mfma_f32_16x16x32_bf16 v[28:31], a[24:27], v[108:111], 0\n"
      "ds_read_b128 a[12:15], v7 offset:6144\n"
      "v_mfma_f32_16x16x32_bf16 v[28:31], a[28:31], v[164:167], v[28:31]\n"
      "ds_read_b128 a[16:19], v7 offset:6400\n"
      "v_mfma_f32_16x16x32_bf16 v[28:31], a[32:35], a[224:227], v[28:31]\n"
      "ds_read_b128 a[20:23], v7 offset:6656\n"
      "v_mfma_f32_16x16x32_bf16 v[28:31], a[0:3], v[128:131], v[28:31]\n"
      "ds_read2_b64 a[60:63], v10 offset0:3 offset1:67\n"
      "v_cvt_pk_bf16_f32 v80, v68, v69\n"
      "v_mfma_f32_16x16x32_bf16 v[28:31], a[4:7], a[228:231], v[28:31]\n"
      "ds_read_b128 a[24:27], v6 offset:5376\n"
      "v_cvt_pk_bf16_f32 v86, v70, v71\n"
      "v_mfma_f32_16x16x32_bf16 v[28:31], a[8:11], v[236:239], v[28:31]\n"
      "s_waitcnt lgkmcnt(6)\n"
      "ds_read_b128 a[28:31], v6 offset:5632\n"
      "v_mfma_f32_16x16x32_bf16 v[72:75], a[84:87], a[64:67], 0\n"
      "s_waitcnt lgkmcnt(6)\n"
      "ds_read2_b64 a[64:67], v12 offset0:0 offset1:64\n"
      "s_nop 0\n"
      "v_mfma_f32_16x16x32_bf16 v[76:79], a[84:87], a[68:71], 0\n"
      "s_waitcnt lgkmcnt(6)\n"
      "ds_read2_b64 a[68:71], v12 offset0:1 offset1:65\n"
      "s_nop 0\n"
      "v_mfma_f32_16x16x32_bf16 v[20:23], a[12:15], v[132:135], v[20:23]\n"
      "s_waitcnt lgkmcnt(6)\n"
      "ds_read_b128 a[32:35], v6 offset:5888\n"
      "v_cvt_pk_bf16_f32 v81, v72, v73\n"
      "v_mfma_f32_16x16x32_bf16 v[20:23], a[16:19], v[188:191], v[20:23]\n"
      "s_waitcnt lgkmcnt(6)\n"
      "ds_read_b128 a[0:3], v7 offset:768\n"
      "v_cvt_pk_bf16_f32 v82, v76, v77\n"
      "v_mfma_f32_16x16x32_bf16 v[20:23], a[20:23], a[240:243], v[20:23]\n"
      "s_waitcnt lgkmcnt(6)\n"
      "ds_read_b128 a[4:7], v7 offset:1024\n"
      "v_cvt_pk_bf16_f32 v87, v74, v75\n"
      "v_mfma_f32_16x16x32_bf16 v[68:71], a[84:87], a[60:63], 0\n"
      "ds_read_b128 a[8:11], v7 offset:1280\n"
      "v_cvt_pk_bf16_f32 v88, v78, v79\n"
      "v_mfma_f32_16x16x32_bf16 v[24:27], a[12:15], v[140:143], v[24:27]\n"
      "v_mfma_f32_16x16x32_bf16 v[24:27], a[16:19], a[236:239], v[24:27]\n"
      "v_cvt_pk_bf16_f32 v20, v20, v21\n"
      "v_cvt_pk_bf16_f32 v21, v22, v23\n"
      "v_mfma_f32_16x16x32_bf16 v[24:27], a[20:23], v[248:251], v[24:27]\n"
      "s_waitcnt lgkmcnt(5)\n"
      "ds_write_addtid_b32 v20 offset:62480\n"
      "v_cvt_pk_bf16_f32 v83, v68, v69\n"
      "v_mfma_f32_16x16x32_bf16 v[72:75], a[84:87], a[64:67], 0\n"
      "buffer_store_dwordx4 v[80:83], v14, s[36:39], s52 offen sc1\n"
      "v_cvt_pk_bf16_f32 v89, v70, v71\n"
      "v_mfma_f32_16x16x32_bf16 v[28:31], a[12:15], v[144:147], v[28:31]\n"
      "ds_read_b128 a[12:15], v7 offset:5376\n"
      "v_cvt_pk_bf16_f32 v24, v24, v25\n"
      "v_mfma_f32_16x16x32_bf16 v[28:31], a[16:19], v[200:203], v[28:31]\n"
      "ds_read_b128 a[16:19], v7 offset:5632\n"
      "v_cvt_pk_bf16_f32 v25, v26, v27\n"
      "v_mfma_f32_16x16x32_bf16 v[28:31], a[20:23], a[244:247], v[28:31]\n"
      "s_waitcnt lgkmcnt(4)\n"
      "ds_read_b128 a[20:23], v7 offset:5888\n"
      "v_cvt_pk_bf16_f32 v84, v72, v73\n"
      "v_mfma_f32_16x16x32_bf16 v[76:79], a[84:87], a[68:71], 0\n"
      "buffer_store_dwordx4 v[86:89], v14, s[36:39], s52 offen offset:1536 sc1\n"
      "v_cvt_pk_bf16_f32 v90, v74, v75\n"
      "v_mfma_f32_16x16x32_bf16 v[32:35], a[24:27], v[92:95], 0\n"
      "ds_write_addtid_b32 v21 offset:62736\n"
      "v_cvt_pk_bf16_f32 v28, v28, v29\n"
      "v_mfma_f32_16x16x32_bf16 v[32:35], a[28:31], v[148:151], v[32:35]\n"
      "ds_write_addtid_b32 v24 offset:50184\n"
      "v_cvt_pk_bf16_f32 v29, v30, v31\n"
      "v_mfma_f32_16x16x32_bf16 v[32:35], a[32:35], v[204:207], v[32:35]\n"
      "ds_write_addtid_b32 v25 offset:50440\n"
      "v_cvt_pk_bf16_f32 v85, v76, v77\n"
      "v_mfma_f32_16x16x32_bf16 v[32:35], a[0:3], v[112:115], v[32:35]\n"
      "buffer_store_dwordx2 v[84:85], v15, s[36:39], s52 offen sc1\n"
      "v_cvt_pk_bf16_f32 v91, v78, v79\n"
      "v_mfma_f32_16x16x32_bf16 v[32:35], a[4:7], a[208:211], v[32:35]\n"
      "s_waitcnt lgkmcnt(5)\n"
      "ds_write_addtid_b32 v28 offset:37888\n"
      "v_mfma_f32_16x16x32_bf16 v[32:35], a[8:11], v[220:223], v[32:35]\n"
      "ds_write_addtid_b32 v29 offset:38144\n"
      "v_mfma_f32_16x16x32_bf16 v[32:35], a[12:15], a[212:215], v[32:35]\n"
      "s_waitcnt lgkmcnt(5)\n"
      "v_mfma_f32_16x16x32_bf16 v[32:35], a[16:19], v[184:187], v[32:35]\n"
      "buffer_store_dwordx2 v[90:91], v15, s[36:39], s52 offen offset:1536 sc1\n"
      "v_mfma_f32_16x16x32_bf16 v[32:35], a[20:23], v[240:243], v[32:35]\n"
      "s_nop 1\n"
      "v_mfma_f32_16x16x32_bf16 v[20:23], a[24:27], v[100:103], 0\n"
      "s_nop 1\n"
      "v_mfma_f32_16x16x32_bf16 v[20:23], a[28:31], a[200:203], v[20:23]\n"
      "s_nop 0\n"
      "v_mfma_f32_16x16x32_bf16 v[20:23], a[32:35], v[208:211], v[20:23]\n"
      "v_cvt_pk_bf16_f32 v32, v32, v33\n"
      "v_cvt_pk_bf16_f32 v33, v34, v35\n"
      "v_mfma_f32_16x16x32_bf16 v[20:23], a[0:3], a[204:207], v[20:23]\n"
      "ds_write_addtid_b32 v32 offset:61456\n"
      "v_mfma_f32_16x16x32_bf16 v[20:23], a[4:7], v[172:175], v[20:23]\n"
      "ds_write_addtid_b32 v33 offset:61712\n"
      "v_mfma_f32_16x16x32_bf16 v[20:23], a[8:11], v[228:231], v[20:23]\n"
      "v_mfma_f32_16x16x32_bf16 v[20:23], a[12:15], v[136:139], v[20:23]\n"
      "v_mfma_f32_16x16x32_bf16 v[20:23], a[16:19], v[192:195], v[20:23]\n"
      "v_mfma_f32_16x16x32_bf16 v[20:23], a[20:23], v[244:247], v[20:23]\n"
      "s_nop 1\n"
      "v_mfma_f32_16x16x32_bf16 v[24:27], a[24:27], a[196:199], 0\n"
      "ds_read_b128 a[24:27], v7 offset:4608\n"
      "s_nop 0\n"
      "v_mfma_f32_16x16x32_bf16 v[24:27], a[28:31], v[160:163], v[24:27]\n"
      "ds_read_b128 a[28:31], v7 offset:4864\n"
      "v_mfma_f32_16x16x32_bf16 v[24:27], a[32:35], v[216:219], v[24:27]\n"
      "ds_read_b128 a[32:35], v7 offset:5120\n"
      "v_cvt_pk_bf16_f32 v20, v20, v21\n"
      "v_mfma_f32_16x16x32_bf16 v[24:27], a[0:3], v[124:127], v[24:27]\n"
      "ds_read_b128 a[0:3], v6 offset:4608\n"
      "v_cvt_pk_bf16_f32 v21, v22, v23\n"
      "v_mfma_f32_16x16x32_bf16 v[24:27], a[4:7], v[180:183], v[24:27]\n"
      "ds_read_b128 a[4:7], v6 offset:4864\n"
      "v_mfma_f32_16x16x32_bf16 v[24:27], a[8:11], v[232:235], v[24:27]\n"
      "ds_read_b128 a[8:11], v6 offset:5120\n"
      "v_mfma_f32_16x16x32_bf16 v[24:27], a[12:15], a[216:219], v[24:27]\n"
      "ds_read_b128 a[12:15], v7 offset:0\n"
      "v_mfma_f32_16x16x32_bf16 v[24:27], a[16:19], v[196:199], v[24:27]\n"
      "ds_read_b128 a[16:19], v7 offset:256\n"
      "v_mfma_f32_16x16x32_bf16 v[24:27], a[20:23], v[252:255], v[24:27]\n"
      "s_waitcnt lgkmcnt(7)\n"
      "ds_read_b128 a[20:23], v7 offset:512\n"
      "s_nop 0\n"
      "v_mfma_f32_16x16x32_bf16 v[28:31], a[24:27], a[160:163], 0\n"
      "s_waitcnt lgkmcnt(7)\n"
      "ds_write_addtid_b32 v20 offset:49160\n"
      "s_nop 0\n"
      "v_mfma_f32_16x16x32_bf16 v[28:31], a[28:31], a[172:175], v[28:31]\n"
      "s_waitcnt lgkmcnt(7)\n"
      "ds_write_addtid_b32 v21 offset:49416\n"
      "v_cvt_pk_bf16_f32 v24, v24, v25\n"
      "v_mfma_f32_16x16x32_bf16 v[28:31], a[32:35], a[184:187], v[28:31]\n"
      "s_waitcnt lgkmcnt(7)\n"
      "ds_write_addtid_b32 v24 offset:36864\n"
      "v_cvt_pk_bf16_f32 v25, v26, v27\n"
      "v_mfma_f32_16x16x32_bf16 v[28:31], a[0:3], a[88:91], v[28:31]\n"
      "s_waitcnt lgkmcnt(7)\n"
      "ds_write_addtid_b32 v25 offset:37120\n"
      "v_mfma_f32_16x16x32_bf16 v[28:31], a[4:7], a[100:103], v[28:31]\n"
      "s_waitcnt lgkmcnt(7)\n"
      "v_mfma_f32_16x16x32_bf16 v[28:31], a[8:11], a[112:115], v[28:31]\n"
      "s_waitcnt lgkmcnt(6)\n"
      "v_mfma_f32_16x16x32_bf16 v[28:31], a[12:15], a[124:127], v[28:31]\n"
      "s_waitcnt lgkmcnt(5)\n"
      "v_mfma_f32_16x16x32_bf16 v[28:31], a[16:19], a[136:139], v[28:31]\n"
      "s_waitcnt lgkmcnt(4)\n"
      "v_mfma_f32_16x16x32_bf16 v[28:31], a[20:23], a[148:151], v[28:31]\n"
      "s_nop 1\n"
      "v_mfma_f32_16x16x32_bf16 v[20:23], a[24:27], a[164:167], 0\n"
      "s_nop 1\n"
      "v_mfma_f32_16x16x32_bf16 v[20:23], a[28:31], a[176:179], v[20:23]\n"
      "s_nop 0\n"
      "v_mfma_f32_16x16x32_bf16 v[20:23], a[32:35], a[188:191], v[20:23]\n"
      "v_cvt_pk_bf16_f32 v28, v28, v29\n"
      "v_cvt_pk_bf16_f32 v29, v30, v31\n"
      "v_mfma_f32_16x16x32_bf16 v[20:23], a[0:3], a[92:95], v[20:23]\n"
      "ds_write_addtid_b32 v28 offset:60432\n"
      "v_mfma_f32_16x16x32_bf16 v[20:23], a[4:7], a[104:107], v[20:23]\n"
      "ds_write_addtid_b32 v29 offset:60688\n"
      "v_mfma_f32_16x16x32_bf16 v[20:23], a[8:11], a[116:119], v[20:23]\n"
      "v_mfma_f32_16x16x32_bf16 v[20:23], a[12:15], a[128:131], v[20:23]\n"
      "v_mfma_f32_16x16x32_bf16 v[20:23], a[16:19], a[140:143], v[20:23]\n"
      "v_mfma_f32_16x16x32_bf16 v[20:23], a[20:23], a[152:155], v[20:23]\n"
      "s_nop 1\n"
      "v_mfma_f32_16x16x32_bf16 v[32:35], a[24:27], a[168:171], 0\n"
      "s_nop 1\n"
      "v_mfma_f32_16x16x32_bf16 v[32:35], a[28:31], a[180:183], v[32:35]\n"
      "s_nop 0\n"
      "v_mfma_f32_16x16x32_bf16 v[32:35], a[32:35], a[192:195], v[32:35]\n"
      "v_cvt_pk_bf16_f32 v20, v20, v21\n"
      "v_cvt_pk_bf16_f32 v21, v22, v23\n"
      "v_mfma_f32_16x16x32_bf16 v[32:35], a[0:3], a[96:99], v[32:35]\n"
      "ds_write_addtid_b32 v20 offset:48136\n"
      "v_mfma_f32_16x16x32_bf16 v[32:35], a[4:7], a[108:111], v[32:35]\n"
      "ds_write_addtid_b32 v21 offset:48392\n"
      "v_mfma_f32_16x16x32_bf16 v[32:35], a[8:11], a[120:123], v[32:35]\n"
      "v_mfma_f32_16x16x32_bf16 v[32:35], a[12:15], a[132:135], v[32:35]\n"
      "v_mfma_f32_16x16x32_bf16 v[32:35], a[16:19], a[144:147], v[32:35]\n"
      "v_mfma_f32_16x16x32_bf16 v[32:35], a[20:23], a[156:159], v[32:35]\n"
      "s_nop 1\n"
      "s_nop 5\n"
      "v_cvt_pk_bf16_f32 v32, v32, v33\n"
      "v_cvt_pk_bf16_f32 v33, v34, v35\n"
      "ds_write_addtid_b32 v32 offset:35840\n"
      "ds_write_addtid_b32 v33 offset:36096\n"
      "s_waitcnt lgkmcnt(0)\n"
      "s_barrier\n"
      "ds_read2_b64 a[0:3], v11 offset0:0 offset1:64\n"
      "ds_read2_b64 a[4:7], v11 offset0:1 offset1:65\n"
      "ds_read2_b64 a[8:11], v11 offset0:2 offset1:66\n"
      "ds_read2_b64 a[12:15], v11 offset0:3 offset1:67\n"
      "ds_read2_b64 a[16:19], v13 offset0:0 offset1:64\n"
      "ds_read2_b64 a[20:23], v13 offset0:1 offset1:65\n"
      "s_waitcnt lgkmcnt(5)\n"
      "v_mfma_f32_16x16x32_bf16 v[68:71], a[84:87], a[0:3], 0\n"
      "s_waitcnt lgkmcnt(4)\n"
      "v_mfma_f32_16x16x32_bf16 v[72:75], a[84:87], a[4:7], 0\n"
      "s_waitcnt lgkmcnt(3)\n"
      "v_mfma_f32_16x16x32_bf16 v[76:79], a[84:87], a[8:11], 0\n"
      "s_waitcnt lgkmcnt(2)\n"
      "s_nop 2\n"
      "v_cvt_pk_bf16_f32 v80, v68, v69\n"
      "v_cvt_pk_bf16_f32 v86, v70, v71\n"
      "v_mfma_f32_16x16x32_bf16 v[68:71], a[84:87], a[12:15], 0\n"
      "s_waitcnt lgkmcnt(1)\n"
      "v_cvt_pk_bf16_f32 v81, v72, v73\n"
      "v_cvt_pk_bf16_f32 v87, v74, v75\n"
      "v_cvt_pk_bf16_f32 v82, v76, v77\n"
      "v_cvt_pk_bf16_f32 v88, v78, v79\n"
      "v_mfma_f32_16x16x32_bf16 v[72:75], a[84:87], a[16:19], 0\n"
      "s_waitcnt lgkmcnt(0)\n"
      "v_mfma_f32_16x16x32_bf16 v[76:79], a[84:87], a[20:23], 0\n"
      "s_mov_b32 s52, s13\n"
      "v_cvt_pk_bf16_f32 v83, v68, v69\n"
      "v_cvt_pk_bf16_f32 v89, v70, v71\n"
      "s_add_u32 s13, s13, s12\n"
      "buffer_store_dwordx4 v[80:83], v14, s[36:39], s52 offen sc1\n"
      "buffer_store_dwordx4 v[86:89], v14, s[36:39], s52 offen offset:1536 sc1\n"
      "v_cvt_pk_bf16_f32 v84, v72, v73\n"
      "v_cvt_pk_bf16_f32 v90, v74, v75\n"
      "v_cvt_pk_bf16_f32 v85, v76, v77\n"
      "v_cvt_pk_bf16_f32 v91, v78, v79\n"
      "buffer_store_dwordx2 v[84:85], v15, s[36:39], s52 offen sc1\n"
      "buffer_store_dwordx2 v[90:91], v15, s[36:39], s52 offen offset:1536 sc1\n"
      "Lconv_u1:\n"
      "Lconv_tail:\n"
      "s_waitcnt vmcnt(0) lgkmcnt(0)\n"
      "Lconv_end:\n"
      :
      : "v"(threadIdx.x), "s"(blockIdx.x), "s"(__builtin_amdgcn_kernarg_segment_ptr())
      : "v1", "v2", "v3", "v4", "v5", "v6", "v7", "v8", "v9", "v10", "v11", "v12", "v13", "v14", "v15", "v16", "v17", "v18", "v19", "v20", "v21", "v22", "v23", "v24", "v25", "v26", "v27", "v28", "v29", "v30", "v31", "v32", "v33", "v34", "v35", "v36", "v37", "v38", "v39", "v40", "v41", "v42", "v43", "v44", "v45", "v46", "v47", "v48", "v49", "v50", "v51", "v52", "v53", "v54", "v55", "v56", "v57", "v58", "v59", "v60", "v61", "v62", "v63", "v64", "v65", "v66", "v67", "v68", "v69", "v70", "v71", "v72", "v73", "v74", "v75", "v76", "v77", "v78", "v79", "v80", "v81", "v82", "v83", "v84", "v85", "v86", "v87", "v88", "v89", "v90", "v91", "v92", "v93", "v94", "v95", "v96", "v97", "v98", "v99", "v100", "v101", "v102", "v103", "v104", "v105", "v106", "v107", "v108", "v109", "v110", "v111", "v112", "v113", "v114", "v115", "v116", "v117", "v118", "v119", "v120", "v121", "v122", "v123", "v124", "v125", "v126", "v127", "v128", "v129", "v130", "v131", "v132", "v133", "v134", "v135", "v136", "v137", "v138", "v139", "v140", "v141", "v142", "v143", "v144", "v145", "v146", "v147", "v148", "v149", "v150", "v151", "v152", "v153", "v154", "v155", "v156", "v157", "v158", "v159", "v160", "v161", "v162", "v163", "v164", "v165", "v166", "v167", "v168", "v169", "v170", "v171", "v172", "v173", "v174", "v175", "v176", "v177", "v178", "v179", "v180", "v181", "v182", "v183", "v184", "v185", "v186", "v187", "v188", "v189", "v190", "v191", "v192", "v193", "v194", "v195", "v196", "v197", "v198", "v199", "v200", "v201", "v202", "v203", "v204", "v205", "v206", "v207", "v208", "v209", "v210", "v211", "v212", "v213", "v214", "v215", "v216", "v217", "v218", "v219", "v220", "v221", "v222", "v223", "v224", "v225", "v226", "v227", "v228", "v229", "v230", "v231", "v232", "v233", "v234", "v235", "v236", "v237", "v238", "v239", "v240", "v241", "v242", "v243", "v244", "v245", "v246", "v247", "v248", "v249", "v250", "v251", "v252", "v253", "v254", "v255", "a0", "a1", "a2", "a3", "a4", "a5", "a6", "a7", "a8", "a9", "a10", "a11", "a12", "a13", "a14", "a15", "a16", "a17", "a18", "a19", "a20", "a21", "a22", "a23", "a24", "a25", "a26", "a27", "a28", "a29", "a30", "a31", "a32", "a33", "a34", "a35", "a36", "a37", "a38", "a39", "a40", "a41", "a42", "a43", "a44", "a45", "a46", "a47", "a48", "a49", "a50", "a51", "a52", "a53", "a54", "a55", "a56", "a57", "a58", "a59", "a60", "a61", "a62", "a63", "a64", "a65", "a66", "a67", "a68", "a69", "a70", "a71", "a72", "a73", "a74", "a75", "a76", "a77", "a78", "a79", "a80", "a81", "a82", "a83", "a84", "a85", "a86", "a87", "a88", "a89", "a90", "a91", "a92", "a93", "a94", "a95", "a96", "a97", "a98", "a99", "a100", "a101", "a102", "a103", "a104", "a105", "a106", "a107", "a108", "a109", "a110", "a111", "a112", "a113", "a114", "a115", "a116", "a117", "a118", "a119", "a120", "a121", "a122", "a123", "a124", "a125", "a126", "a127", "a128", "a129", "a130", "a131", "a132", "a133", "a134", "a135", "a136", "a137", "a138", "a139", "a140", "a141", "a142", "a143", "a144", "a145", "a146", "a147", "a148", "a149", "a150", "a151", "a152", "a153", "a154", "a155", "a156", "a157", "a158", "a159", "a160", "a161", "a162", "a163", "a164", "a165", "a166", "a167", "a168", "a169", "a170", "a171", "a172", "a173", "a174", "a175", "a176", "a177", "a178", "a179", "a180", "a181", "a182", "a183", "a184", "a185", "a186", "a187", "a188", "a189", "a190", "a191", "a192", "a193", "a194", "a195", "a196", "a197", "a198", "a199", "a200", "a201", "a202", "a203", "a204", "a205", "a206", "a207", "a208", "a209", "a210", "a211", "a212", "a213", "a214", "a215", "a216", "a217", "a218", "a219", "a220", "a221", "a222", "a223", "a224", "a225", "a226", "a227", "a228", "a229", "a230", "a231", "a232", "a233", "a234", "a235", "a236", "a237", "a238", "a239", "a240", "a241", "a242", "a243", "a244", "a245", "a246", "a247", "a248", "a249", "a250", "a251", "a252", "a253", "a254", "a255", "s3", "s4", "s5", "s6", "s7", "s8", "s9", "s10", "s11", "s12", "s13", "s14", "s15", "s16", "s17", "s18", "s19", "s20", "s21", "s22", "s23", "s24", "s25", "s26", "s27", "s28", "s29", "s30", "s31", "s34", "s35", "s36", "s37", "s38", "s39", "s40", "s41", "s42", "s43", "s44", "s45", "s46", "s47", "s48", "s49", "s50", "s51", "s52", "s53", "s54", "s55", "s56", "s57", "s58", "s59", "s60", "s61", "s62", "s63", "s64", "s65", "s66", "s67", "s68", "s69", "s70", "s71", "s72", "s73", "s74", "s75", "s76", "s77", "s78", "s79", "s80", "s81", "s82", "s83", "s84", "s85", "s86", "s87", "s88", "s89", "s90", "s91", "s92", "s93", "s94", "s95", "s96", "s97", "s98", "s99", "scc", "vcc", "memory");
}

extern "C" void kernel_launch(void* const* d_in, const int* in_sizes, int n_in, void* d_out,
                              int out_size, void* d_ws, size_t ws_size, hipStream_t stream) {
  conv_kernel<<<256, 256, 0, stream>>>((const char*)d_in[0], (const char*)d_in[1], (char*)d_out, (char*)d_ws);
}
```

```cpp
#include <hip/hip_runtime.h>
#include <hip/hip_bf16.h>
#include <stdint.h>

__global__ void __launch_bounds__(256, 1) __attribute__((aligned(4096)))
conv_kernel(const char* __restrict__ x, const char* __restrict__ w, char* __restrict__ out, char* __restrict__ dbg) {
  __shared__ __attribute__((aligned(1024))) char smem[163840];
  asm volatile("" :: "v"((unsigned)(uintptr_t)smem) : "memory");
  asm volatile(
      "s_load_dwordx8 s[20:27], %2, 0x0\n"
      "v_readfirstlane_b32 s40, %0\n"
      "v_and_b32 v5, 63, %0\n"
      "v_lshlrev_b32 v8, 4, v5\n"
      "s_lshr_b32 s40, s40, 6\n"
      "s_lshl_b32 s43, s40, 3\n"
      "s_or_b32 s43, s43, 0x80000\n"
      "v_bitop3_b32 v9, v8, v5, 32 bitop3:0x78\n"
      "v_xor_b32 v89, 32, v8\n"
      "s_mul_i32 s92, s40, 3072\n"
      "s_lshr_b32 s14, %1, 7\n"
      "s_sub_u32 s14, s14, 1\n"
      "s_lshr_b32 s41, s40, 1\n"
      "s_and_b32 s42, s40, 1\n"
      "s_mov_b32 s30, 165888\n"
      "s_mov_b32 s31, 0x20000\n"
      "s_lshr_b32 s49, %1, 3\n"
      "s_lshl2_add_u32 s49, s49, s40\n"
      "v_lshlrev_b32 v36, 7, v5\n"
      "s_mul_i32 s50, s49, 384\n"
      "v_min_u32 v37, 256, v36\n"
      "v_add_u32 v37, s50, v37\n"
      "v_min_u32 v66, 1280, v36\n"
      "s_mul_i32 s96, s49, 1408\n"
      "s_getpc_b64 s[54:55]\n"
      "Lconv_start:\n"
      "v_min_u32 v37, Lconv_end-Lconv_start-4, v37\n"
      "s_mov_b32 m0, 162816\n"
      "s_nop 0\n"
      "global_load_lds_dword v37, s[54:55]\n"
      "s_bfe_u32 s99, %1, 0x30003\n"
      "s_mul_i32 s49, s40, 192\n"
      "v_add_u32 v36, s49, v5\n"
      "v_add_u32 v38, 128, v36\n"
      "v_mul_u32_u24 v37, 669, v36\n"
      "v_lshrrev_b32 v37, 16, v37\n"
      "v_mad_i32_i24 v37, v37, -2, v38\n"
      "v_lshlrev_b32 v16, 4, v37\n"
      "v_add_u32 v36, 64, v36\n"
      "v_mul_u32_u24 v37, 669, v36\n"
      "v_lshrrev_b32 v37, 16, v37\n"
      "v_mad_i32_i24 v37, v37, -2, v38\n"
      "v_lshlrev_b32 v17, 4, v37\n"
      "v_add_u32 v36, 64, v36\n"
      "v_mul_u32_u24 v37, 669, v36\n"
      "v_lshrrev_b32 v37, 16, v37\n"
      "v_mad_i32_i24 v37, v37, -2, v38\n"
      "v_lshlrev_b32 v18, 4, v37\n"
      "s_or_b32 s50, s99, s40\n"
      "s_cmp_eq_u32 s50, 0\n"
      "s_cselect_b32 s16, 0xffffff, 0\n"
      "s_mov_b32 s17, 0\n"
      "s_cmp_eq_u32 s99, 7\n"
      "s_cselect_b32 s19, 0xffffff00, 0\n"
      "s_mov_b32 s18, 0\n"
      "v_add_u32 v19, 14080, v8\n"
      "v_cndmask_b32 v16, v16, 2.0, s[16:17]\n"
      "v_cndmask_b32 v19, v19, 2.0, s[18:19]\n"
      "v_and_b32 v36, 16, v8\n"
      "v_mov_b32 v52, 0xffff\n"
      "v_lshlrev_b32 v52, v36, v52\n"
      "s_mov_b32 s16, 0x3000003\n"
      "s_mov_b32 s17, 0x3000003\n"
      "v_cndmask_b32 v48, 0, v52, s[16:17]\n"
      "s_lshl_b64 s[16:17], s[16:17], 2\n"
      "v_cndmask_b32 v49, 0, v52, s[16:17]\n"
      "s_lshl_b64 s[16:17], s[16:17], 2\n"
      "v_cndmask_b32 v50, 0, v52, s[16:17]\n"
      "s_lshl_b64 s[16:17], s[16:17], 2\n"
      "v_cndmask_b32 v51, 0, v52, s[16:17]\n"
      "v_bfe_u32 v53, v5, 5, 1\n"
      "v_xor_b32 v53, s41, v53\n"
      "v_sub_u32 v53, 0, v53\n"
      "v_and_b32 v36, 0x1000100, v53\n"
      "v_xor_b32 v36, 0x3fa83fa8, v36\n"
      "v_and_b32 v68, v36, v48\n"
      "v_and_b32 v69, v36, v49\n"
      "v_and_b32 v70, v36, v50\n"
      "v_and_b32 v71, v36, v51\n"
      "v_xor_b32 v72, 0x80008000, v68\n"
      "v_xor_b32 v73, 0x80008000, v69\n"
      "v_xor_b32 v74, 0x80008000, v70\n"
      "v_xor_b32 v75, 0x80008000, v71\n"
      "v_bfe_i32 v36, v5, 5, 1\n"
      "v_and_b32 v36, 0x3f283f28, v36\n"
      "v_xor_b32 v37, 0x3f283f28, v36\n"
      "v_and_b32 v76, v37, v48\n"
      "v_and_b32 v77, v37, v49\n"
      "v_and_b32 v78, v37, v50\n"
      "v_and_b32 v79, v37, v51\n"
      "v_and_b32 v80, v36, v48\n"
      "v_and_b32 v81, v36, v49\n"
      "v_and_b32 v82, v36, v50\n"
      "v_and_b32 v83, v36, v51\n"
      "s_mul_i32 s50, s42, 96\n"
      "v_bfe_u32 v36, v5, 1, 3\n"
      "v_and_b32 v37, 1, v5\n"
      "v_lshl_add_u32 v36, v37, 3, v36\n"
      "v_mul_u32_u24 v36, 192, v36\n"
      "v_add_u32 v36, 46144, v36\n"
      "v_lshlrev_b32 v37, 5, v37\n"
      "v_and_b32 v38, 16, v5\n"
      "v_add_u32 v38, s50, v38\n"
      "v_and_b32 v39, 32, v5\n"
      "v_mul_u32_u24 v40, 192, v39\n"
      "v_add_u32 v40, v40, v36\n"
      "v_xad_u32 v84, v38, v37, v40\n"
      "v_add_u32 v41, 32, v38\n"
      "v_xad_u32 v85, v41, v37, v40\n"
      "v_add_u32 v41, v39, v38\n"
      "v_xad_u32 v86, v41, v37, v36\n"
      "s_mul_i32 s49, s41, 6144\n"
      "s_add_u32 s49, s49, 46144\n"
      "v_lshrrev_b32 v36, 3, v5\n"
      "v_and_b32 v38, 4, v5\n"
      "v_lshl_add_u32 v36, v38, 1, v36\n"
      "v_mul_u32_u24 v36, 192, v36\n"
      "v_lshlrev_b32 v37, 3, v5\n"
      "v_and_b32 v38, 8, v37\n"
      "v_add3_u32 v36, v36, v38, s49\n"
      "v_and_b32 v38, 32, v37\n"
      "v_and_b32 v37, 16, v37\n"
      "v_add_u32 v37, s50, v37\n"
      "v_xad_u32 v87, v37, v38, v36\n"
      "v_add_u32 v39, 32, v37\n"
      "v_xad_u32 v88, v39, v38, v36\n"
      "s_waitcnt lgkmcnt(0)\n"
      "s_mov_b32 s28, s22\n"
      "s_and_b32 s29, s23, 0xffff\n"
      "buffer_load_dword v66, s[28:31], s96 offen lds\n"
      "s_add_u32 m0, s92, 73824\n"
      "s_and_b32 s97, 0x3c2c24, s14\n"
      "s_xor_b32 s97, 0x120c0600, s97\n"
      "s_bfe_u32 s97, s97, s43\n"
      "s_mul_i32 s97, s97, 3072\n"
      "buffer_load_dwordx4 v8, s[28:31], s97 offen offset:0 lds\n"
      "buffer_load_dwordx4 v9, s[28:31], s97 offen offset:1024 lds\n"
      "buffer_load_dwordx4 v89, s[28:31], s97 offen offset:2048 lds\n"
      "s_add_u32 m0, s92, 86112\n"
      "s_and_b32 s97, 0x2c240000, s14\n"
      "s_xor_b32 s97, 0x2a241e18, s97\n"
      "s_bfe_u32 s97, s97, s43\n"
      "s_mul_i32 s97, s97, 3072\n"
      "buffer_load_dwordx4 v8, s[28:31], s97 offen offset:0 lds\n"
      "buffer_load_dwordx4 v9, s[28:31], s97 offen offset:1024 lds\n"
      "buffer_load_dwordx4 v89, s[28:31], s97 offen offset:2048 lds\n"
      "s_and_b32 s53, %1, 7\n"
      "s_lshl_b32 s53, s53, 6\n"
      "s_bfe_u32 s50, %1, 0x10006\n"
      "s_add_u32 s53, s53, s50\n"
      "s_lshl1_add_u32 s50, s14, 32\n"
      "s_add_u32 s53, s53, s50\n"
      "s_mul_i32 s13, s53, 98304\n"
      "s_or_b32 s12, s14, 1\n"
      "s_mul_i32 s12, s12, 196608\n"
      "s_lshl_b32 s50, s12, 1\n"
      "s_sub_u32 s51, s13, s50\n"
      "s_mul_i32 s99, s99, 12288\n"
      "s_sub_u32 s50, s99, 2432\n"
      "s_ashr_i32 s49, s50, 31\n"
      "s_add_u32 s32, s20, s50\n"
      "s_addc_u32 s33, s21, s49\n"
      "s_and_b32 s33, s33, 0xffff\n"
      "s_sub_u32 s34, 50331648, s50\n"
      "s_mov_b32 s35, 0x20000\n"
      "s_add_u32 s46, s92, 110720\n"
      "v_lshrrev_b32 v36, 1, v5\n"
      "v_and_b32 v37, 4, v36\n"
      "v_and_or_b32 v37, v5, 2, v37\n"
      "v_mul_u32_u24 v37, 1568, v37\n"
      "v_xor_b32 v36, v36, v5\n"
      "v_and_b32 v36, 4, v36\n"
      "v_mad_u32_u24 v37, v36, 48, v37\n"
      "s_mul_i32 s50, s42, 1568\n"
      "s_add_u32 s50, s50, 110720\n"
      "v_and_b32 v36, 16, v5\n"
      "v_lshl_add_u32 v37, v36, 1, v37\n"
      "v_and_b32 v36, 16, v8\n"
      "v_add3_u32 v37, v37, v36, s50\n"
      "v_and_b32 v36, 32, v5\n"
      "v_mad_u32_u24 v38, v36, 24, v37\n"
      "v_mad_u32_u24 v39, v36, 25, v37\n"
      "s_mul_i32 s17, s42, 0xcc00cc00\n"
      "s_mov_b32 s16, 0\n"
      "v_mov_b32 v36, 768\n"
      "v_mov_b32 v37, 736\n"
      "s_mov_b64 exec, 0\n"
      "s_cmp_eq_u32 s40, 0\n"
      "s_cbranch_scc1 Lconv_p1\n"
      "s_cmp_eq_u32 s40, 1\n"
      "s_cbranch_scc1 Lconv_p2\n"
      "s_cmp_eq_u32 s40, 2\n"
      "s_cbranch_scc1 Lconv_pa0\n"
      "s_cmp_eq_u32 s40, 3\n"
      "s_cbranch_scc1 Lconv_pa1\n"
      "Lconv_r0:\n"
      "s_mov_b64 exec, -1\n"
      "s_add_u32 m0, s92, 98400\n"
      "s_and_b32 s97, 0x3c2c243c, s14\n"
      "s_xor_b32 s97, 0xd070130, s97\n"
      "s_bfe_u32 s97, s97, s43\n"
      "s_mul_i32 s97, s97, 3072\n"
      "buffer_load_dwordx4 v8, s[28:31], s97 offen offset:0 lds\n"
      "buffer_load_dwordx4 v9, s[28:31], s97 offen offset:1024 lds\n"
      "buffer_load_dwordx4 v89, s[28:31], s97 offen offset:2048 lds\n"
      "v_cndmask_b32 v36, v36, v37, s[16:17]\n"
      "v_add_u32 v39, v39, v36\n"
      "s_cmp_eq_u32 s41, 0\n"
      "s_cselect_b64 vcc, -1, 0\n"
      "v_add_u32 v40, 384, v38\n"
      "v_add_u32 v41, 384, v39\n"
      "v_cndmask_b32 v2, v40, v39, vcc\n"
      "v_cndmask_b32 v3, v39, v40, vcc\n"
      "v_cndmask_b32 v4, v41, v38, vcc\n"
      "v_and_b32 v36, 3, v5\n"
      "v_mul_u32_u24 v36, 18464, v36\n"
      "v_bfe_u32 v37, v5, 2, 2\n"
      "v_lshrrev_b32 v38, 1, v37\n"
      "v_xor_b32 v37, v37, v38\n"
      "v_lshl_add_u32 v36, v37, 6, v36\n"
      "s_mul_i32 s50, s41, 2304\n"
      "v_and_b32 v37, 48, v5\n"
      "v_add3_u32 v6, v36, v37, s50\n"
      "v_add_u32 v7, 9216, v6\n"
      "s_mov_b32 s16, 0xc0003\n"
      "s_mov_b32 s17, 0xc0003\n"
      "v_cndmask_b32 v48, 0, v52, s[16:17]\n"
      "s_lshl_b64 s[16:17], s[16:17], 4\n"
      "v_cndmask_b32 v49, 0, v52, s[16:17]\n"
      "s_lshl_b64 s[16:17], s[16:17], 4\n"
      "v_cndmask_b32 v50, 0, v52, s[16:17]\n"
      "s_lshl_b64 s[16:17], s[16:17], 4\n"
      "v_cndmask_b32 v51, 0, v52, s[16:17]\n"
      "v_and_b32 v36, 0xff00ff00, v53\n"
      "v_xor_b32 v36, 0xc080c080, v36\n"
      "v_and_b32 v38, v36, v48\n"
      "v_and_b32 v39, v36, v49\n"
      "v_and_b32 v40, v36, v50\n"
      "v_and_b32 v41, v36, v51\n"
      "v_accvgpr_write_b32 a72, v38\n"
      "v_accvgpr_write_b32 a73, v39\n"
      "v_accvgpr_write_b32 a74, v40\n"
      "v_accvgpr_write_b32 a75, v41\n"
      "v_and_b32 v36, 0xff00ff00, v53\n"
      "v_xor_b32 v36, 0xc05dc05d, v36\n"
      "v_and_b32 v38, v36, v48\n"
      "v_and_b32 v39, v36, v49\n"
      "v_and_b32 v40, v36, v50\n"
      "v_and_b32 v41, v36, v51\n"
      "v_accvgpr_write_b32 a76, v38\n"
      "v_accvgpr_write_b32 a77, v39\n"
      "v_accvgpr_write_b32 a78, v40\n"
      "v_accvgpr_write_b32 a79, v41\n"
      "v_and_b32 v36, 0x81008100, v53\n"
      "v_xor_b32 v36, 0x3f803f80, v36\n"
      "v_and_b32 v38, v36, v48\n"
      "v_and_b32 v39, v36, v49\n"
      "v_and_b32 v40, v36, v50\n"
      "v_and_b32 v41, v36, v51\n"
      "v_accvgpr_write_b32 a80, v38\n"
      "v_accvgpr_write_b32 a81, v39\n"
      "v_accvgpr_write_b32 a82, v40\n"
      "v_accvgpr_write_b32 a83, v41\n"
      "s_mov_b32 s44, 0x3fdd0000\n"
      "v_lshlrev_b32 v36, 5, v5\n"
      "v_and_b32 v36, 0x180, v36\n"
      "v_lshlrev_b32 v37, 3, v5\n"
      "v_and_b32 v37, 16, v37\n"
      "v_sub_u32 v38, 0x3e71, v36\n"
      "v_add_u32 v39, 0x3cf1, v36\n"
      "v_mov_b32 v40, 0x3f80\n"
      "s_mov_b64 vcc, 0xffff\n"
      "s_mov_b32 s16, 0xffff0000\n"
      "s_mov_b32 s17, 0xf0f0\n"
      "v_xor_b32 v41, 0x8000, v38\n"
      "v_cndmask_b32 v38, v38, v41, s[16:17]\n"
      "v_cndmask_b32 v38, v38, v40, vcc\n"
      "v_lshlrev_b32 v38, v37, v38\n"
      "v_xor_b32 v41, 0x8000, v39\n"
      "s_xor_b32 s17, s17, 0xffff\n"
      "v_cndmask_b32 v39, v39, v41, s[16:17]\n"
      "v_cndmask_b32 v39, v39, v40, vcc\n"
      "v_lshlrev_b32 v39, v37, v39\n"
      "s_mov_b32 s18, 0x55550005\n"
      "s_mov_b32 s19, 0x5555\n"
      "v_cndmask_b32 v42, 0, v38, s[18:19]\n"
      "s_lshl_b64 s[16:17], s[18:19], 1\n"
      "v_cndmask_b32 v43, 0, v38, s[16:17]\n"
      "v_accvgpr_write_b32 a84, v42\n"
      "v_accvgpr_write_b32 a85, v43\n"
      "s_mov_b32 s18, 0x55555000\n"
      "v_cndmask_b32 v42, 0, v39, s[18:19]\n"
      "s_lshl_b64 s[16:17], s[18:19], 1\n"
      "v_cndmask_b32 v43, 0, v39, s[16:17]\n"
      "v_accvgpr_write_b32 a86, v42\n"
      "v_accvgpr_write_b32 a87, v43\n"
      "v_lshrrev_b32 v36, 4, v5\n"
      "v_min_u32 v37, 2, v36\n"
      "v_lshlrev_b32 v37, 10, v37\n"
      "s_lshl_b32 s50, s41, 7\n"
      "s_lshl_b32 s49, s42, 8\n"
      "s_add_u32 s50, s50, s49\n"
      "s_add_u32 s50, s50, 98416\n"
      "v_and_b32 v38, 8, v5\n"
      "v_lshl_add_u32 v37, v38, 3, v37\n"
      "v_add_u32 v37, s50, v37\n"
      "s_movk_i32 s50, -12296\n"
      "s_mov_b32 s49, 43040\n"
      "v_mov_b32 v42, s49\n"
      "v_bfe_u32 v39, v5, 1, 2\n"
      "v_lshrrev_b32 v40, 2, v38\n"
      "v_add_u32 v39, v39, v40\n"
      "v_lshlrev_b32 v40, 5, v5\n"
      "v_and_b32 v40, 32, v40\n"
      "v_cmp_lt_u32 vcc, 2, v39\n"
      "v_mad_i32_i24 v39, v39, s50, v37\n"
      "v_add_u32 v39, v39, v40\n"
      "v_cndmask_b32 v41, 0, v42, vcc\n"
      "v_add_u32 v10, v39, v41\n"
      "v_add_u32 v11, 3072, v10\n"
      "v_bfe_u32 v39, v5, 2, 1\n"
      "v_lshrrev_b32 v40, 1, v38\n"
      "v_sub_u32 v39, v39, v40\n"
      "v_add_u32 v39, 4, v39\n"
      "v_and_b32 v40, 48, v8\n"
      "v_cmp_lt_u32 vcc, 2, v39\n"
      "v_mad_i32_i24 v39, v39, s50, v37\n"
      "v_add_u32 v39, v39, v40\n"
      "v_cndmask_b32 v41, 0, v42, vcc\n"
      "v_add_u32 v12, v39, v41\n"
      "v_add_u32 v13, 3072, v12\n"
      "v_mul_u32_u24 v36, 384, v36\n"
      "v_and_b32 v37, 15, v5\n"
      "v_lshl_add_u32 v39, v37, 3, v36\n"
      "v_add_u32 v15, 128, v39\n"
      "v_add_lshl_u32 v37, v37, v38, 4\n"
      "v_add_u32 v14, v37, v36\n"
      "s_add_u32 s50, s99, s92\n"
      "s_add_u32 s36, s24, s50\n"
      "s_addc_u32 s37, s25, 0\n"
      "s_and_b32 s37, s37, 0xffff\n"
      "s_sub_u32 s38, 50331648, s50\n"
      "s_mov_b32 s39, 0x20000\n"
      "s_mul_i32 s48, s12, 17\n"
      "s_mul_i32 s45, s42, 6152\n"
      "s_lshl_b32 s50, s41, 9\n"
      "s_add_u32 s45, s45, s50\n"
      "s_add_u32 s45, s45, 41056\n"
      "s_mov_b32 s98, 0\n"
      "s_mov_b32 s91, 26560\n"
      "s_mul_i32 s18, s42, 18464\n"
      "s_mul_i32 s50, s41, 2304\n"
      "s_add_u32 s18, s18, s50\n"
      "s_lshl1_add_u32 s19, s18, 9216\n"
      "s_add_u32 s58, s46, 26560\n"
      "s_lshl1_add_u32 s93, s46, 26560\n"
      "s_mul_i32 s59, s42, -13280\n"
      "s_add_u32 s59, s59, 136256\n"
      "s_lshl1_add_u32 s94, s59, 26560\n"
      "s_mul_i32 s50, s42, 26560\n"
      "s_add_u32 s59, s59, s50\n"
      "s_mul_i32 s15, s42, s12\n"
      "s_sub_u32 s95, s12, s15\n"
      "s_mov_b32 s52, 2.0\n"
      "s_cmp_lt_u32 s40, 3\n"
      "s_cselect_b32 s97, 27680, 0\n"
      "s_add_u32 s97, s97, s92\n"
      "s_add_u32 m0, s97, 36928\n"
      "s_and_b32 s97, 0x24000000, s14\n"
      "s_xor_b32 s97, 0x251f1913, s97\n"
      "s_bfe_u32 s97, s97, s43\n"
      "s_mul_i32 s97, s97, 3072\n"
      "buffer_load_dwordx4 v8, s[28:31], s97 offen offset:0 lds\n"
      "buffer_load_dwordx4 v9, s[28:31], s97 offen offset:1024 lds\n"
      "buffer_load_dwordx4 v89, s[28:31], s97 offen offset:2048 lds\n"
      "s_cmp_lt_u32 s40, 2\n"
      "s_cselect_b32 s97, 27680, 0\n"
      "s_add_u32 s97, s97, s92\n"
      "s_add_u32 m0, s97, 21536\n"
      "s_and_b32 s97, 0x24243c2c, s14\n"
      "s_xor_b32 s97, 0x802312b, s97\n"
      "s_bfe_u32 s97, s97, s43\n"
      "s_mul_i32 s97, s97, 3072\n"
      "buffer_load_dwordx4 v8, s[28:31], s97 offen offset:0 lds\n"
      "buffer_load_dwordx4 v9, s[28:31], s97 offen offset:1024 lds\n"
      "buffer_load_dwordx4 v89, s[28:31], s97 offen offset:2048 lds\n"
      "s_cmp_lt_u32 s40, 1\n"
      "s_cselect_b32 s97, 27680, 0\n"
      "s_add_u32 s97, s97, s92\n"
      "s_add_u32 m0, s97, 6144\n"
      "s_and_b32 s97, 0x3c, s14\n"
      "s_xor_b32 s97, 0x201a140e, s97\n"
      "s_bfe_u32 s97, s97, s43\n"
      "s_mul_i32 s97, s97, 3072\n"
      "buffer_load_dwordx4 v8, s[28:31], s97 offen offset:0 lds\n"
      "buffer_load_dwordx4 v9, s[28:31], s97 offen offset:1024 lds\n"
      "buffer_load_dwordx4 v89, s[28:31], s97 offen offset:2048 lds\n"
      "s_sub_u32 vcc_lo, 0, s41\n"
      "s_mov_b32 vcc_hi, 0\n"
      ".p2align 3\n"
      "s_waitcnt vmcnt(12)\n"
      "s_barrier\n"
      "s_cmp_lt_u32 s40, 3\n"
      "s_cselect_b32 s97, -9248, 0\n"
      "ds_read_b128 a[0:3], v86 offset:30752\n"
      "ds_read_b128 a[4:7], v84 offset:27680\n"
      "ds_read_b128 a[8:11], v85 offset:27680\n"
      "ds_read_b128 a[12:15], v86 offset:30816\n"
      "s_add_u32 s97, s97, s92\n"
      "s_add_u32 m0, s97, 9248\n"
      "s_and_b32 s97, 0x243c2424, s14\n"
      "s_xor_b32 s97, 0x3322c26, s97\n"
      "s_bfe_u32 s97, s97, s43\n"
      "s_mul_i32 s97, s97, 3072\n"
      "s_waitcnt lgkmcnt(3)\n"
      "v_mfma_f32_16x16x32_bf16 v[20:23], a[0:3], v[76:79], 0\n"
      "s_waitcnt lgkmcnt(2)\n"
      "ds_read_b128 a[16:19], v84 offset:27744\n"
      "ds_read_b128 a[20:23], v86 offset:39968\n"
      "buffer_load_dwordx4 v8, s[28:31], s97 offen offset:0 lds\n"
      "v_mfma_f32_16x16x32_bf16 v[24:27], a[4:7], v[68:71], v[20:23]\n"
      "ds_read_b128 a[24:27], v84 offset:36896\n"
      "ds_read_b128 a[28:31], v85 offset:36896\n"
      "v_mfma_f32_16x16x32_bf16 v[20:23], a[4:7], v[72:75], v[20:23]\n"
      "ds_read_b64_tr_b16 a[88:89], v87 offset:27680\n"
      "ds_read_b64_tr_b16 a[92:93], v88 offset:27680\n"
      "buffer_load_dwordx4 v9, s[28:31], s97 offen offset:1024 lds\n"
      "v_mfma_f32_16x16x32_bf16 v[28:31], a[0:3], v[80:83], 0\n"
      "s_waitcnt lgkmcnt(7)\n"
      "ds_read_b64_tr_b16 a[96:97], v87 offset:27744\n"
      "ds_read_b64_tr_b16 a[124:125], v87 offset:36896\n"
      "v_mfma_f32_16x16x32_bf16 v[32:35], a[8:11], v[68:71], v[28:31]\n"
      "ds_read_b64_tr_b16 a[128:129], v88 offset:36896\n"
      "ds_read_b128 a[32:35], v86 offset:40032\n"
      "v_mfma_f32_16x16x32_bf16 v[28:31], a[8:11], v[72:75], v[28:31]\n"
      "s_waitcnt lgkmcnt(10)\n"
      "ds_read_b128 a[0:3], v84 offset:36960\n"
      "ds_read_b64_tr_b16 a[132:133], v87 offset:36960\n"
      "buffer_load_dwordx4 v89, s[28:31], s97 offen offset:2048 lds\n"
      "v_mfma_f32_16x16x32_bf16 v[54:57], a[12:15], v[76:79], 0\n"
      "s_waitcnt lgkmcnt(10)\n"
      "s_cmp_lt_u32 s40, 2\n"
      "s_cselect_b32 s97, -9248, 0\n"
      "s_add_u32 s97, s97, s92\n"
      "s_add_u32 m0, s97, 30784\n"
      "s_and_b32 s97, 0x3c24, s14\n"
      "s_xor_b32 s97, 0x1b150f09, s97\n"
      "s_bfe_u32 s97, s97, s43\n"
      "s_mul_i32 s97, s97, 3072\n"
      "v_cvt_pk_bf16_f32 v96, v20, v21\n"
      "v_cvt_pk_bf16_f32 v97, v22, v23\n"
      "v_mfma_f32_16x16x32_bf16 v[58:61], a[16:19], v[68:71], v[54:57]\n"
      "v_cvt_pk_bf16_f32 v92, v24, v25\n"
      "v_cvt_pk_bf16_f32 v93, v26, v27\n"
      "v_mfma_f32_16x16x32_bf16 v[54:57], a[16:19], v[72:75], v[54:57]\n"
      "buffer_load_dwordx4 v8, s[28:31], s97 offen offset:0 lds\n"
      "v_cvt_pk_bf16_f32 v104, v28, v29\n"
      "v_cvt_pk_bf16_f32 v105, v30, v31\n"
      "v_mfma_f32_16x16x32_bf16 v[62:65], a[20:23], v[76:79], 0\n"
      "s_waitcnt lgkmcnt(4)\n"
      "v_cvt_pk_bf16_f32 v100, v32, v33\n"
      "v_cvt_pk_bf16_f32 v101, v34, v35\n"
      "v_mfma_f32_16x16x32_bf16 v[20:23], a[24:27], v[68:71], v[62:65]\n"
      "v_cvt_pk_bf16_f32 v108, v54, v55\n"
      "v_cvt_pk_bf16_f32 v109, v56, v57\n"
      "v_mfma_f32_16x16x32_bf16 v[62:65], a[24:27], v[72:75], v[62:65]\n"
      "s_waitcnt lgkmcnt(0)\n"
      "s_waitcnt vmcnt(13)\n"
      "s_cbranch_vccnz Lconv_h1\n"
      "s_barrier\n"
      "Lconv_h1:\n"
      "buffer_load_dwordx4 v9, s[28:31], s97 offen offset:1024 lds\n"
      "v_mfma_f32_16x16x32_bf16 v[24:27], a[20:23], v[80:83], 0\n"
      "v_cvt_pk_bf16_f32 v58, v58, v59\n"
      "v_cvt_pk_bf16_f32 v60, v60, v61\n"
      "v_mfma_f32_16x16x32_bf16 v[28:31], a[28:31], v[68:71], v[24:27]\n"
      "s_cbranch_vccz Lconv_h2\n"
      "s_barrier\n"
      "Lconv_h2:\n"
      "ds_read_b128 a[4:7], v86 offset:49184\n"
      "ds_read_b128 a[8:11], v84 offset:46112\n"
      "ds_read_b128 a[12:15], v85 offset:46112\n"
      "ds_read_b128 a[16:19], v86 offset:49248\n"
      "v_mfma_f32_16x16x32_bf16 v[24:27], a[28:31], v[72:75], v[24:27]\n"
      "ds_read_b128 a[20:23], v84 offset:46176\n"
      "ds_read_b128 a[24:27], v86 offset:58400\n"
      "buffer_load_dwordx4 v89, s[28:31], s97 offen offset:2048 lds\n"
      "v_accvgpr_write_b32 a196, v58\n"
      "v_accvgpr_write_b32 a197, v60\n"
      "v_mfma_f32_16x16x32_bf16 v[32:35], a[32:35], v[76:79], 0\n"
      "s_cmp_lt_u32 s40, 1\n"
      "s_cselect_b32 s97, -9248, 0\n"
      "ds_read_b128 a[28:31], v84 offset:55328\n"
      "ds_read_b128 a[32:35], v85 offset:55328\n"
      "s_add_u32 s97, s97, s92\n"
      "s_add_u32 m0, s97, 52320\n"
      "s_and_b32 s97, 0x3c242400, s14\n"
      "s_xor_b32 s97, 0x332d2721, s97\n"
      "s_bfe_u32 s97, s97, s43\n"
      "s_mul_i32 s97, s97, 3072\n"
      "v_cvt_pk_bf16_f32 v116, v62, v63\n"
      "v_cvt_pk_bf16_f32 v117, v64, v65\n"
      "v_mfma_f32_16x16x32_bf16 v[54:57], a[0:3], v[68:71], v[32:35]\n"
      "ds_read_b64_tr_b16 a[160:161], v87 offset:46112\n"
      "ds_read_b64_tr_b16 a[164:165], v88 offset:46112\n"
      "buffer_load_dwordx4 v8, s[28:31], s97 offen offset:0 lds\n"
      "v_cvt_pk_bf16_f32 v112, v20, v21\n"
      "v_cvt_pk_bf16_f32 v113, v22, v23\n"
      "v_mfma_f32_16x16x32_bf16 v[32:35], a[0:3], v[72:75], v[32:35]\n"
      "s_waitcnt lgkmcnt(9)\n"
      "ds_read_b64_tr_b16 a[168:169], v87 offset:46176\n"
      "ds_read_b64_tr_b16 a[90:91], v87 offset:55328\n"
      "v_cvt_pk_bf16_f32 v120, v24, v25\n"
      "v_cvt_pk_bf16_f32 v121, v26, v27\n"
      "v_mfma_f32_16x16x32_bf16 v[58:61], a[4:7], v[76:79], 0\n"
      "s_waitcnt lgkmcnt(10)\n"
      "ds_read_b64_tr_b16 a[94:95], v88 offset:55328\n"
      "ds_read_b128 a[0:3], v86 offset:58464\n"
      "v_mfma_f32_16x16x32_bf16 v[62:65], a[8:11], v[68:71], v[58:61]\n"
      "ds_read_b64_tr_b16 a[98:99], v87 offset:55392\n"
      "buffer_load_dwordx4 v9, s[28:31], s97 offen offset:1024 lds\n"
      "v_cvt_pk_bf16_f32 v28, v28, v29\n"
      "v_cvt_pk_bf16_f32 v30, v30, v31\n"
      "v_accvgpr_write_b32 a204, v28\n"
      "v_mfma_f32_16x16x32_bf16 v[58:61], a[8:11], v[72:75], v[58:61]\n"
      "v_accvgpr_write_b32 a205, v30\n"
      "v_cvt_pk_bf16_f32 v128, v32, v33\n"
      "v_cvt_pk_bf16_f32 v129, v34, v35\n"
      "v_mfma_f32_16x16x32_bf16 v[20:23], a[4:7], v[80:83], 0\n"
      "s_waitcnt lgkmcnt(7)\n"
      "ds_read_b128 a[4:7], v84 offset:55392\n"
      "v_cvt_pk_bf16_f32 v124, v54, v55\n"
      "v_mfma_f32_16x16x32_bf16 v[24:27], a[12:15], v[68:71], v[20:23]\n"
      "buffer_load_dwordx4 v89, s[28:31], s97 offen offset:2048 lds\n"
      "v_cvt_pk_bf16_f32 v125, v56, v57\n"
      "v_mfma_f32_16x16x32_bf16 v[20:23], a[12:15], v[72:75], v[20:23]\n"
      "s_add_u32 m0, s46, 13280\n"
      "s_add_u32 s51, s51, s12\n"
      "v_cvt_pk_bf16_f32 v132, v58, v59\n"
      "v_cvt_pk_bf16_f32 v133, v60, v61\n"
      "v_mfma_f32_16x16x32_bf16 v[28:31], a[16:19], v[76:79], 0\n"
      "v_cvt_pk_bf16_f32 v62, v62, v63\n"
      "v_cvt_pk_bf16_f32 v64, v64, v65\n"
      "v_mfma_f32_16x16x32_bf16 v[32:35], a[20:23], v[68:71], v[28:31]\n"
      "buffer_load_dwordx4 v16, s[32:35], s51 offen lds\n"
      "v_accvgpr_write_b32 a212, v62\n"
      "v_accvgpr_write_b32 a213, v64\n"
      "v_mfma_f32_16x16x32_bf16 v[28:31], a[20:23], v[72:75], v[28:31]\n"
      "s_add_u32 m0, s46, 13280\n"
      "v_cvt_pk_bf16_f32 v140, v20, v21\n"
      "v_cvt_pk_bf16_f32 v141, v22, v23\n"
      "v_mfma_f32_16x16x32_bf16 v[54:57], a[24:27], v[76:79], 0\n"
      "buffer_load_dwordx4 v17, s[32:35], s51 offen offset:1024 lds\n"
      "v_cvt_pk_bf16_f32 v136, v24, v25\n"
      "v_mfma_f32_16x16x32_bf16 v[58:61], a[28:31], v[68:71], v[54:57]\n"
      "s_add_u32 m0, s46, 13280\n"
      "s_add_u32 s96, s51, s15\n"
      "buffer_load_dwordx4 v18, s[32:35], s51 offen offset:2048 lds\n"
      "v_cvt_pk_bf16_f32 v137, v26, v27\n"
      "v_mfma_f32_16x16x32_bf16 v[54:57], a[28:31], v[72:75], v[54:57]\n"
      "s_waitcnt lgkmcnt(0)\n"
      "s_waitcnt vmcnt(12)\n"
      "s_cbranch_vccnz Lconv_h3\n"
      "s_barrier\n"
      "Lconv_h3:\n"
      "s_mov_b32 m0, s59\n"
      "v_cvt_pk_bf16_f32 v144, v28, v29\n"
      "v_cvt_pk_bf16_f32 v145, v30, v31\n"
      "v_mfma_f32_16x16x32_bf16 v[62:65], a[24:27], v[80:83], 0\n"
      "buffer_load_dwordx4 v19, s[32:35], s96 offen lds\n"
      "v_cvt_pk_bf16_f32 v32, v32, v33\n"
      "v_mfma_f32_16x16x32_bf16 v[20:23], a[32:35], v[68:71], v[62:65]\n"
      "s_cbranch_vccz Lconv_h4\n"
      "s_barrier\n"
      "Lconv_h4:\n"
      "s_add_u32 m0, s46, 26560\n"
      "s_add_u32 s51, s51, s12\n"
      "ds_read_b128 a[8:11], v86 offset:21536\n"
      "ds_read_b128 a[12:15], v84 offset:18464\n"
      "ds_read_b128 a[16:19], v85 offset:18464\n"
      "ds_read_b128 a[20:23], v86 offset:21600\n"
      "buffer_load_dwordx4 v16, s[32:35], s51 offen lds\n"
      "v_mfma_f32_16x16x32_bf16 v[62:65], a[32:35], v[72:75], v[62:65]\n"
      "s_add_u32 m0, s46, 26560\n"
      "ds_read_b128 a[24:27], v84 offset:18528\n"
      "ds_read_b128 a[28:31], v86 offset:3072\n"
      "buffer_load_dwordx4 v17, s[32:35], s51 offen offset:1024 lds\n"
      "v_cvt_pk_bf16_f32 v34, v34, v35\n"
      "v_accvgpr_write_b32 a216, v32\n"
      "v_mfma_f32_16x16x32_bf16 v[24:27], a[0:3], v[76:79], 0\n"
      "s_add_u32 m0, s46, 26560\n"
      "ds_read_b128 a[32:35], v84 offset:0\n"
      "ds_read_b128 a[0:3], v85 offset:0\n"
      "buffer_load_dwordx4 v18, s[32:35], s51 offen offset:2048 lds\n"
      "v_accvgpr_write_b32 a217, v34\n"
      "v_cvt_pk_bf16_f32 v98, v54, v55\n"
      "v_mfma_f32_16x16x32_bf16 v[28:31], a[4:7], v[68:71], v[24:27]\n"
      "s_add_u32 m0, s92, 73824\n"
      "s_and_b32 s97, 0x24242c, s14\n"
      "ds_read_b64_tr_b16 a[126:127], v87 offset:18464\n"
      "ds_read_b64_tr_b16 a[130:131], v88 offset:18464\n"
      "s_xor_b32 s97, 0x16100a04, s97\n"
      "s_bfe_u32 s97, s97, s43\n"
      "s_mul_i32 s97, s97, 3072\n"
      "buffer_load_dwordx4 v8, s[28:31], s97 offen offset:0 lds\n"
      "v_cvt_pk_bf16_f32 v99, v56, v57\n"
      "v_cvt_pk_bf16_f32 v94, v58, v59\n"
      "v_mfma_f32_16x16x32_bf16 v[24:27], a[4:7], v[72:75], v[24:27]\n"
      "s_waitcnt lgkmcnt(9)\n"
      "ds_read_b128 a[4:7], v86 offset:3136\n"
      "ds_read_b64_tr_b16 a[134:135], v87 offset:18528\n"
      "v_cvt_pk_bf16_f32 v95, v60, v61\n"
      "v_cvt_pk_bf16_f32 v106, v62, v63\n"
      "v_cvt_pk_bf16_f32 v107, v64, v65\n"
      "v_mfma_f32_16x16x32_bf16 v[32:35], a[8:11], v[76:79], 0\n"
      "s_waitcnt lgkmcnt(10)\n"
      "ds_read_b64_tr_b16 a[162:163], v87 offset:0\n"
      "ds_read_b64_tr_b16 a[166:167], v88 offset:0\n"
      "v_cvt_pk_bf16_f32 v102, v20, v21\n"
      "v_cvt_pk_bf16_f32 v103, v22, v23\n"
      "v_cvt_pk_bf16_f32 v110, v24, v25\n"
      "v_mfma_f32_16x16x32_bf16 v[54:57], a[12:15], v[68:71], v[32:35]\n"
      "ds_read_b64_tr_b16 a[170:171], v87 offset:64\n"
      "v_cvt_pk_bf16_f32 v111, v26, v27\n"
      "v_cvt_pk_bf16_f32 v28, v28, v29\n"
      "v_cvt_pk_bf16_f32 v30, v30, v31\n"
      "v_mfma_f32_16x16x32_bf16 v[32:35], a[12:15], v[72:75], v[32:35]\n"
      "buffer_load_dwordx4 v9, s[28:31], s97 offen offset:1024 lds\n"
      "v_accvgpr_write_b32 a198, v28\n"
      "v_accvgpr_write_b32 a199, v30\n"
      "v_mfma_f32_16x16x32_bf16 v[58:61], a[8:11], v[80:83], 0\n"
      "Lconv_p1:\n"
      "s_cbranch_execz Lconv_r0\n"
      "s_nop 0\n"
      "s_waitcnt lgkmcnt(7)\n"
      "ds_read_b128 a[8:11], v84 offset:64\n"
      "v_add_u32 v86, -46144, v86\n"
      "v_mfma_f32_16x16x32_bf16 v[62:65], a[16:19], v[68:71], v[58:61]\n"
      "ds_read_b128 a[12:15], v86 offset:30752\n"
      "v_cvt_pk_bf16_f32 v118, v32, v33\n"
      "v_cvt_pk_bf16_f32 v119, v34, v35\n"
      "v_cvt_pk_bf16_f32 v114, v54, v55\n"
      "v_cvt_pk_bf16_f32 v115, v56, v57\n"
      "v_mfma_f32_16x16x32_bf16 v[58:61], a[16:19], v[72:75], v[58:61]\n"
      "v_add_u32 v84, -46144, v84\n"
      "v_add_u32 v85, -46144, v85\n"
      "v_mfma_f32_16x16x32_bf16 v[20:23], a[20:23], v[76:79], 0\n"
      "ds_read_b128 a[16:19], v84 offset:27680\n"
      "ds_read_b128 a[20:23], v85 offset:27680\n"
      "buffer_load_dwordx4 v89, s[28:31], s97 offen offset:2048 lds\n"
      "v_mfma_f32_16x16x32_bf16 v[24:27], a[24:27], v[68:71], v[20:23]\n"
      "s_add_u32 m0, s92, 86112\n"
      "s_and_b32 s97, 0x242c0000, s14\n"
      "s_xor_b32 s97, 0x2e28221c, s97\n"
      "s_bfe_u32 s97, s97, s43\n"
      "s_mul_i32 s97, s97, 3072\n"
      "v_cvt_pk_bf16_f32 v122, v58, v59\n"
      "v_cvt_pk_bf16_f32 v123, v60, v61\n"
      "v_mfma_f32_16x16x32_bf16 v[20:23], a[24:27], v[72:75], v[20:23]\n"
      "ds_read_b128 a[24:27], v86 offset:30816\n"
      "v_cvt_pk_bf16_f32 v62, v62, v63\n"
      "v_cvt_pk_bf16_f32 v64, v64, v65\n"
      "v_mfma_f32_16x16x32_bf16 v[28:31], a[28:31], v[76:79], 0\n"
      "v_accvgpr_write_b32 a206, v62\n"
      "v_accvgpr_write_b32 a207, v64\n"
      "v_mfma_f32_16x16x32_bf16 v[32:35], a[32:35], v[68:71], v[28:31]\n"
      "v_add_u32 v87, -46144, v87\n"
      "v_cvt_pk_bf16_f32 v130, v20, v21\n"
      "v_mfma_f32_16x16x32_bf16 v[28:31], a[32:35], v[72:75], v[28:31]\n"
      "ds_read_b64_tr_b16 a[100:101], v87 offset:27680\n"
      "ds_read_b64_tr_b16 a[108:109], v87 offset:27744\n"
      "buffer_load_dwordx4 v8, s[28:31], s97 offen offset:0 lds\n"
      "v_cvt_pk_bf16_f32 v131, v22, v23\n"
      "v_add_u32 v88, -46144, v88\n"
      "v_mfma_f32_16x16x32_bf16 v[54:57], a[28:31], v[80:83], 0\n"
      "ds_read_b64_tr_b16 a[104:105], v88 offset:27680\n"
      "ds_read_b128 a[28:31], v84 offset:27744\n"
      "v_mfma_f32_16x16x32_bf16 v[58:61], a[0:3], v[68:71], v[54:57]\n"
      "v_cvt_pk_bf16_f32 v126, v24, v25\n"
      "v_cvt_pk_bf16_f32 v127, v26, v27\n"
      "v_mfma_f32_16x16x32_bf16 v[54:57], a[0:3], v[72:75], v[54:57]\n"
      "s_waitcnt lgkmcnt(4)\n"
      "v_cvt_pk_bf16_f32 v134, v28, v29\n"
      "v_cvt_pk_bf16_f32 v135, v30, v31\n"
      "v_mfma_f32_16x16x32_bf16 v[62:65], a[4:7], v[76:79], 0\n"
      "buffer_load_dwordx4 v9, s[28:31], s97 offen offset:1024 lds\n"
      "v_cvt_pk_bf16_f32 v32, v32, v33\n"
      "v_mfma_f32_16x16x32_bf16 v[20:23], a[8:11], v[68:71], v[62:65]\n"
      "v_cvt_pk_bf16_f32 v34, v34, v35\n"
      "v_accvgpr_write_b32 a214, v32\n"
      "v_accvgpr_write_b32 a215, v34\n"
      "v_mfma_f32_16x16x32_bf16 v[62:65], a[8:11], v[72:75], v[62:65]\n"
      "v_cvt_pk_bf16_f32 v142, v54, v55\n"
      "v_cvt_pk_bf16_f32 v143, v56, v57\n"
      "v_mfma_f32_16x16x32_bf16 v[24:27], a[12:15], v[76:79], 0\n"
      "v_cvt_pk_bf16_f32 v138, v58, v59\n"
      "v_cvt_pk_bf16_f32 v139, v60, v61\n"
      "v_mfma_f32_16x16x32_bf16 v[28:31], a[16:19], v[68:71], v[24:27]\n"
      "v_cvt_pk_bf16_f32 v20, v20, v21\n"
      "v_cvt_pk_bf16_f32 v22, v22, v23\n"
      "v_mfma_f32_16x16x32_bf16 v[24:27], a[16:19], v[72:75], v[24:27]\n"
      "s_waitcnt lgkmcnt(0)\n"
      "s_waitcnt vmcnt(12)\n"
      "s_cbranch_vccnz Lconv_h5\n"
      "s_barrier\n"
      "Lconv_h5:\n"
      "buffer_load_dwordx4 v89, s[28:31], s97 offen offset:2048 lds\n"
      "v_mfma_f32_16x16x32_bf16 v[32:35], a[12:15], v[80:83], 0\n"
      "s_add_u32 m0, s92, 98400\n"
      "s_and_b32 s97, 0x24242c24, s14\n"
      "s_xor_b32 s97, 0x110b0534, s97\n"
      "s_bfe_u32 s97, s97, s43\n"
      "s_mul_i32 s97, s97, 3072\n"
      "v_cvt_pk_bf16_f32 v146, v62, v63\n"
      "v_cvt_pk_bf16_f32 v147, v64, v65\n"
      "v_mfma_f32_16x16x32_bf16 v[54:57], a[20:23], v[68:71], v[32:35]\n"
      "s_cbranch_vccz Lconv_h6\n"
      "s_barrier\n"
      "Lconv_h6:\n"
      "ds_read_b128 a[32:35], v86 offset:12288\n"
      "ds_read_b128 a[0:3], v84 offset:9216\n"
      "ds_read_b128 a[4:7], v85 offset:9216\n"
      "ds_read_b128 a[8:11], v86 offset:12352\n"
      "v_mfma_f32_16x16x32_bf16 v[32:35], a[20:23], v[72:75], v[32:35]\n"
      "ds_read_b128 a[12:15], v84 offset:9280\n"
      "ds_read_b128 a[16:19], v86 offset:3072\n"
      "v_accvgpr_write_b32 a218, v20\n"
      "v_accvgpr_write_b32 a219, v22\n"
      "v_cvt_pk_bf16_f32 v152, v24, v25\n"
      "v_mfma_f32_16x16x32_bf16 v[58:61], a[24:27], v[76:79], 0\n"
      "ds_read_b128 a[20:23], v84 offset:0\n"
      "ds_read_b128 a[24:27], v85 offset:0\n"
      "buffer_load_dwordx4 v8, s[28:31], s97 offen offset:0 lds\n"
      "v_cvt_pk_bf16_f32 v153, v26, v27\n"
      "v_mfma_f32_16x16x32_bf16 v[62:65], a[28:31], v[68:71], v[58:61]\n"
      "ds_read_b64_tr_b16 a[136:137], v87 offset:9216\n"
      "ds_read_b64_tr_b16 a[140:141], v88 offset:9216\n"
      "v_cvt_pk_bf16_f32 v148, v28, v29\n"
      "v_cvt_pk_bf16_f32 v149, v30, v31\n"
      "v_mfma_f32_16x16x32_bf16 v[58:61], a[28:31], v[72:75], v[58:61]\n"
      "s_waitcnt lgkmcnt(9)\n"
      "ds_read_b128 a[28:31], v86 offset:3136\n"
      "ds_read_b64_tr_b16 a[144:145], v87 offset:9280\n"
      "v_cvt_pk_bf16_f32 v156, v32, v33\n"
      "v_cvt_pk_bf16_f32 v157, v34, v35\n"
      "v_mfma_f32_16x16x32_bf16 v[20:23], a[32:35], v[76:79], 0\n"
      "s_waitcnt lgkmcnt(10)\n"
      "ds_read_b64_tr_b16 a[172:173], v87 offset:0\n"
      "ds_read_b64_tr_b16 a[176:177], v88 offset:0\n"
      "v_mfma_f32_16x16x32_bf16 v[24:27], a[0:3], v[68:71], v[20:23]\n"
      "ds_read_b64_tr_b16 a[180:181], v87 offset:64\n"
      "ds_read_b64_tr_b16 a[102:103], v87 offset:18464\n"
      "v_cvt_pk_bf16_f32 v54, v54, v55\n"
      "v_cvt_pk_bf16_f32 v56, v56, v57\n"
      "v_accvgpr_write_b32 a200, v54\n"
      "v_mfma_f32_16x16x32_bf16 v[20:23], a[0:3], v[72:75], v[20:23]\n"
      "ds_read_b128 a[0:3], v86 offset:21536\n"
      "ds_read_b64_tr_b16 a[106:107], v88 offset:18464\n"
      "buffer_load_dwordx4 v9, s[28:31], s97 offen offset:1024 lds\n"
      "v_accvgpr_write_b32 a201, v56\n"
      "v_cvt_pk_bf16_f32 v164, v58, v59\n"
      "v_mfma_f32_16x16x32_bf16 v[28:31], a[32:35], v[80:83], 0\n"
      "s_waitcnt lgkmcnt(10)\n"
      "ds_read_b128 a[32:35], v84 offset:64\n"
      "ds_read_b64_tr_b16 a[110:111], v87 offset:18528\n"
      "v_cvt_pk_bf16_f32 v165, v60, v61\n"
      "v_mfma_f32_16x16x32_bf16 v[32:35], a[4:7], v[68:71], v[28:31]\n"
      "ds_read_b64_tr_b16 a[138:139], v87 offset:36928\n"
      "ds_read_b64_tr_b16 a[142:143], v88 offset:36928\n"
      "v_cvt_pk_bf16_f32 v160, v62, v63\n"
      "v_cvt_pk_bf16_f32 v161, v64, v65\n"
      "v_mfma_f32_16x16x32_bf16 v[28:31], a[4:7], v[72:75], v[28:31]\n"
      "ds_read_b128 a[4:7], v84 offset:18464\n"
      "ds_read_b64_tr_b16 a[146:147], v87 offset:36992\n"
      "v_cvt_pk_bf16_f32 v168, v20, v21\n"
      "v_cvt_pk_bf16_f32 v169, v22, v23\n"
      "v_mfma_f32_16x16x32_bf16 v[54:57], a[8:11], v[76:79], 0\n"
      "ds_read_b128 a[8:11], v85 offset:18464\n"
      "ds_read_b64_tr_b16 a[174:175], v87 offset:55392\n"
      "buffer_load_dwordx4 v89, s[28:31], s97 offen offset:2048 lds\n"
      "v_mfma_f32_16x16x32_bf16 v[58:61], a[12:15], v[68:71], v[54:57]\n"
      "s_cmp_lt_u32 s40, 3\n"
      "s_cselect_b32 s97, 27680, 0\n"
      "ds_read_b64_tr_b16 a[178:179], v88 offset:55392\n"
      "ds_read_b64_tr_b16 a[182:183], v87 offset:55456\n"
      "s_add_u32 s97, s97, s92\n"
      "s_add_u32 m0, s97, 36928\n"
      "s_and_b32 s97, 0x2c000000, s14\n"
      "s_xor_b32 s97, 0x29231d17, s97\n"
      "s_bfe_u32 s97, s97, s43\n"
      "s_mul_i32 s97, s97, 3072\n"
      "v_cvt_pk_bf16_f32 v24, v24, v25\n"
      "v_cvt_pk_bf16_f32 v26, v26, v27\n"
      "v_accvgpr_write_b32 a208, v24\n"
      "v_mfma_f32_16x16x32_bf16 v[54:57], a[12:15], v[72:75], v[54:57]\n"
      "ds_read_b128 a[12:15], v86 offset:21600\n"
      "v_accvgpr_write_b32 a209, v26\n"
      "v_cvt_pk_bf16_f32 v176, v28, v29\n"
      "v_cvt_pk_bf16_f32 v177, v30, v31\n"
      "v_cvt_pk_bf16_f32 v172, v32, v33\n"
      "v_mfma_f32_16x16x32_bf16 v[62:65], a[16:19], v[76:79], 0\n"
      "v_cvt_pk_bf16_f32 v173, v34, v35\n"
      "v_cvt_pk_bf16_f32 v180, v58, v59\n"
      "v_mfma_f32_16x16x32_bf16 v[20:23], a[20:23], v[68:71], v[62:65]\n"
      "v_cvt_pk_bf16_f32 v54, v54, v55\n"
      "v_cvt_pk_bf16_f32 v56, v56, v57\n"
      "v_mfma_f32_16x16x32_bf16 v[62:65], a[20:23], v[72:75], v[62:65]\n"
      "ds_read_b128 a[20:23], v86 offset:40000\n"
      "buffer_load_dwordx4 v8, s[28:31], s97 offen offset:0 lds\n"
      "v_accvgpr_write_b32 a228, v54\n"
      "v_mfma_f32_16x16x32_bf16 v[24:27], a[16:19], v[80:83], 0\n"
      "ds_read_b128 a[16:19], v84 offset:18528\n"
      "v_accvgpr_write_b32 a229, v56\n"
      "v_mfma_f32_16x16x32_bf16 v[28:31], a[24:27], v[68:71], v[24:27]\n"
      "v_cvt_pk_bf16_f32 v181, v60, v61\n"
      "v_cvt_pk_bf16_f32 v188, v62, v63\n"
      "v_mfma_f32_16x16x32_bf16 v[24:27], a[24:27], v[72:75], v[24:27]\n"
      "s_waitcnt lgkmcnt(2)\n"
      "ds_read_b128 a[24:27], v84 offset:36928\n"
      "v_cvt_pk_bf16_f32 v189, v64, v65\n"
      "v_cvt_pk_bf16_f32 v184, v20, v21\n"
      "v_cvt_pk_bf16_f32 v185, v22, v23\n"
      "v_mfma_f32_16x16x32_bf16 v[32:35], a[28:31], v[76:79], 0\n"
      "ds_read_b128 a[28:31], v85 offset:36928\n"
      "buffer_load_dwordx4 v9, s[28:31], s97 offen offset:1024 lds\n"
      "v_mfma_f32_16x16x32_bf16 v[54:57], a[32:35], v[68:71], v[32:35]\n"
      "v_cvt_pk_bf16_f32 v24, v24, v25\n"
      "v_cvt_pk_bf16_f32 v26, v26, v27\n"
      "v_mfma_f32_16x16x32_bf16 v[32:35], a[32:35], v[72:75], v[32:35]\n"
      "ds_read_b128 a[32:35], v86 offset:40064\n"
      "v_accvgpr_write_b32 a236, v24\n"
      "v_accvgpr_write_b32 a237, v26\n"
      "v_mfma_f32_16x16x32_bf16 v[58:61], a[0:3], v[76:79], 0\n"
      "v_cvt_pk_bf16_f32 v192, v28, v29\n"
      "v_cvt_pk_bf16_f32 v193, v30, v31\n"
      "v_mfma_f32_16x16x32_bf16 v[62:65], a[4:7], v[68:71], v[58:61]\n"
      "v_cvt_pk_bf16_f32 v196, v54, v55\n"
      "v_cvt_pk_bf16_f32 v200, v32, v33\n"
      "v_mfma_f32_16x16x32_bf16 v[58:61], a[4:7], v[72:75], v[58:61]\n"
      "ds_read_b128 a[4:7], v86 offset:58464\n"
      "buffer_load_dwordx4 v89, s[28:31], s97 offen offset:2048 lds\n"
      "v_mfma_f32_16x16x32_bf16 v[20:23], a[0:3], v[80:83], 0\n"
      "s_cmp_lt_u32 s40, 2\n"
      "s_cselect_b32 s97, 6144, 0\n"
      "ds_read_b128 a[0:3], v84 offset:36992\n"
      "s_add_u32 s97, s97, s92\n"
      "s_add_u32 m0, s97, 43072\n"
      "s_and_b32 s97, 0x24242424, s14\n"
      "s_xor_b32 s97, 0x352f352f, s97\n"
      "s_bfe_u32 s97, s97, s43\n"
      "s_mul_i32 s97, s97, 3072\n"
      "v_cvt_pk_bf16_f32 v201, v34, v35\n"
      "v_mfma_f32_16x16x32_bf16 v[24:27], a[8:11], v[68:71], v[20:23]\n"
      "v_cvt_pk_bf16_f32 v197, v56, v57\n"
      "v_cvt_pk_bf16_f32 v154, v58, v59\n"
      "v_cvt_pk_bf16_f32 v155, v60, v61\n"
      "v_mfma_f32_16x16x32_bf16 v[20:23], a[8:11], v[72:75], v[20:23]\n"
      "ds_read_b128 a[8:11], v84 offset:55392\n"
      "v_cvt_pk_bf16_f32 v150, v62, v63\n"
      "v_cvt_pk_bf16_f32 v151, v64, v65\n"
      "v_mfma_f32_16x16x32_bf16 v[28:31], a[12:15], v[76:79], 0\n"
      "s_waitcnt lgkmcnt(3)\n"
      "ds_read_b128 a[12:15], v85 offset:55392\n"
      "buffer_load_dwordx4 v8, s[28:31], s97 offen offset:0 lds\n"
      "v_cvt_pk_bf16_f32 v24, v24, v25\n"
      "v_mfma_f32_16x16x32_bf16 v[32:35], a[16:19], v[68:71], v[28:31]\n"
      "v_cvt_pk_bf16_f32 v158, v20, v21\n"
      "v_cvt_pk_bf16_f32 v159, v22, v23\n"
      "v_mfma_f32_16x16x32_bf16 v[28:31], a[16:19], v[72:75], v[28:31]\n"
      "ds_read_b128 a[16:19], v86 offset:58528\n"
      "v_cvt_pk_bf16_f32 v26, v26, v27\n"
      "v_accvgpr_write_b32 a202, v24\n"
      "v_accvgpr_write_b32 a203, v26\n"
      "v_mfma_f32_16x16x32_bf16 v[54:57], a[20:23], v[76:79], 0\n"
      "v_cvt_pk_bf16_f32 v162, v32, v33\n"
      "v_cvt_pk_bf16_f32 v163, v34, v35\n"
      "v_mfma_f32_16x16x32_bf16 v[58:61], a[24:27], v[68:71], v[54:57]\n"
      "v_cvt_pk_bf16_f32 v166, v28, v29\n"
      "v_cvt_pk_bf16_f32 v167, v30, v31\n"
      "v_mfma_f32_16x16x32_bf16 v[54:57], a[24:27], v[72:75], v[54:57]\n"
      "buffer_load_dwordx4 v9, s[28:31], s97 offen offset:1024 lds\n"
      "v_add_u32 v86, 46144, v86\n"
      "v_add_u32 v85, 46144, v85\n"
      "v_mfma_f32_16x16x32_bf16 v[62:65], a[20:23], v[80:83], 0\n"
      "ds_read_b128 a[20:23], v84 offset:55456\n"
      "v_cvt_pk_bf16_f32 v58, v58, v59\n"
      "v_mfma_f32_16x16x32_bf16 v[20:23], a[28:31], v[68:71], v[62:65]\n"
      "v_cvt_pk_bf16_f32 v60, v60, v61\n"
      "v_cvt_pk_bf16_f32 v170, v54, v55\n"
      "v_mfma_f32_16x16x32_bf16 v[62:65], a[28:31], v[72:75], v[62:65]\n"
      "v_cvt_pk_bf16_f32 v171, v56, v57\n"
      "v_accvgpr_write_b32 a210, v58\n"
      "v_mfma_f32_16x16x32_bf16 v[24:27], a[32:35], v[76:79], 0\n"
      "s_waitcnt lgkmcnt(1)\n"
      "buffer_load_dwordx4 v89, s[28:31], s97 offen offset:2048 lds\n"
      "v_accvgpr_write_b32 a211, v60\n"
      "v_mfma_f32_16x16x32_bf16 v[28:31], a[0:3], v[68:71], v[24:27]\n"
      "s_add_u32 m0, s46, 39840\n"
      "s_add_u32 s51, s51, s12\n"
      "v_add_u32 v84, 46144, v84\n"
      "v_cvt_pk_bf16_f32 v178, v62, v63\n"
      "v_mfma_f32_16x16x32_bf16 v[24:27], a[0:3], v[72:75], v[24:27]\n"
      "v_cvt_pk_bf16_f32 v179, v64, v65\n"
      "v_cvt_pk_bf16_f32 v174, v20, v21\n"
      "v_cvt_pk_bf16_f32 v175, v22, v23\n"
      "v_mfma_f32_16x16x32_bf16 v[32:35], a[4:7], v[76:79], 0\n"
      "v_cvt_pk_bf16_f32 v182, v28, v29\n"
      "v_cvt_pk_bf16_f32 v183, v30, v31\n"
      "v_mfma_f32_16x16x32_bf16 v[54:57], a[8:11], v[68:71], v[32:35]\n"
      "v_add_u32 v87, 46144, v87\n"
      "v_cvt_pk_bf16_f32 v24, v24, v25\n"
      "v_mfma_f32_16x16x32_bf16 v[32:35], a[8:11], v[72:75], v[32:35]\n"
      "s_waitcnt lgkmcnt(0)\n"
      "s_waitcnt vmcnt(6)\n"
      "s_cbranch_vccnz Lconv_h7\n"
      "s_barrier\n"
      "Lconv_h7:\n"
      "buffer_load_dwordx4 v16, s[32:35], s51 offen lds\n"
      "v_mfma_f32_16x16x32_bf16 v[58:61], a[4:7], v[80:83], 0\n"
      "s_add_u32 m0, s46, 39840\n"
      "v_cvt_pk_bf16_f32 v26, v26, v27\n"
      "v_accvgpr_write_b32 a230, v24\n"
      "v_mfma_f32_16x16x32_bf16 v[62:65], a[12:15], v[68:71], v[58:61]\n"
      "s_cbranch_vccz Lconv_h8\n"
      "s_barrier\n"
      "Lconv_h8:\n"
      "ds_read_b128 a[24:27], v86 offset:30752\n"
      "ds_read_b128 a[28:31], v84 offset:27680\n"
      "ds_read_b128 a[32:35], v85 offset:27680\n"
      "ds_read_b128 a[0:3], v86 offset:30816\n"
      "buffer_load_dwordx4 v17, s[32:35], s51 offen offset:1024 lds\n"
      "v_mfma_f32_16x16x32_bf16 v[58:61], a[12:15], v[72:75], v[58:61]\n"
      "s_add_u32 m0, s46, 39840\n"
      "ds_read_b128 a[4:7], v84 offset:27744\n"
      "ds_read_b128 a[8:11], v86 offset:39968\n"
      "buffer_load_dwordx4 v18, s[32:35], s51 offen offset:2048 lds\n"
      "v_accvgpr_write_b32 a231, v26\n"
      "v_cvt_pk_bf16_f32 v190, v32, v33\n"
      "v_mfma_f32_16x16x32_bf16 v[20:23], a[16:19], v[76:79], 0\n"
      "s_sub_u32 m0, s94, s59\n"
      "s_add_u32 s96, s51, s15\n"
      "ds_read_b128 a[12:15], v84 offset:36896\n"
      "ds_read_b128 a[16:19], v85 offset:36896\n"
      "buffer_load_dwordx4 v19, s[32:35], s96 offen lds\n"
      "v_cvt_pk_bf16_f32 v191, v34, v35\n"
      "v_cvt_pk_bf16_f32 v186, v54, v55\n"
      "v_mfma_f32_16x16x32_bf16 v[24:27], a[20:23], v[68:71], v[20:23]\n"
      "s_add_u32 m0, s46, 0\n"
      "s_add_u32 s51, s51, s12\n"
      "ds_read_b128 a[36:39], v2 offset:13280\n"
      "ds_read_b128 a[40:43], v3 offset:13280\n"
      "buffer_load_dwordx4 v16, s[32:35], s51 offen lds\n"
      "v_cvt_pk_bf16_f32 v187, v56, v57\n"
      "v_cvt_pk_bf16_f32 v58, v58, v59\n"
      "v_mfma_f32_16x16x32_bf16 v[20:23], a[20:23], v[72:75], v[20:23]\n"
      "s_waitcnt lgkmcnt(9)\n"
      "s_add_u32 m0, s46, 0\n"
      "ds_read_b128 a[20:23], v86 offset:40032\n"
      "ds_read_b128 a[44:47], v4 offset:13280\n"
      "buffer_load_dwordx4 v17, s[32:35], s51 offen offset:1024 lds\n"
      "v_cvt_pk_bf16_f32 v60, v60, v61\n"
      "v_accvgpr_write_b32 a238, v58\n"
      "v_mfma_f32_16x16x32_bf16 v[28:31], a[24:27], v[76:79], 0\n"
      "s_waitcnt lgkmcnt(10)\n"
      "s_add_u32 m0, s46, 0\n"
      "ds_read_b128 a[48:51], v2 offset:13344\n"
      "ds_read_b128 a[52:55], v3 offset:13344\n"
      "buffer_load_dwordx4 v18, s[32:35], s51 offen offset:2048 lds\n"
      "v_mfma_f32_16x16x32_bf16 v[32:35], a[28:31], v[68:71], v[28:31]\n"
      "s_mov_b32 m0, s18\n"
      "ds_read_b128 a[56:59], v4 offset:13344\n"
      "ds_read_b64_tr_b16 a[112:113], v87 offset:27680\n"
      "v_accvgpr_write_b32 a239, v60\n"
      "v_cvt_pk_bf16_f32 v194, v62, v63\n"
      "v_mfma_f32_16x16x32_bf16 v[28:31], a[28:31], v[72:75], v[28:31]\n"
      "ds_read_b128 a[28:31], v86 offset:49184\n"
      "ds_read_b64_tr_b16 a[120:121], v87 offset:27744\n"
      "v_cvt_pk_bf16_f32 v195, v64, v65\n"
      "v_cvt_pk_bf16_f32 v202, v20, v21\n"
      "v_cvt_pk_bf16_f32 v203, v22, v23\n"
      "v_mfma_f32_16x16x32_bf16 v[54:57], a[24:27], v[80:83], 0\n"
      "s_waitcnt lgkmcnt(10)\n"
      "ds_read_b128 a[24:27], v84 offset:36960\n"
      "ds_read_b64_tr_b16 a[148:149], v87 offset:36896\n"
      "v_cvt_pk_bf16_f32 v198, v24, v25\n"
      "v_mfma_f32_16x16x32_bf16 v[58:61], a[32:35], v[68:71], v[54:57]\n"
      "ds_read_b64_tr_b16 a[156:157], v87 offset:36960\n"
      "ds_read_b64_tr_b16 a[184:185], v87 offset:46112\n"
      "v_cvt_pk_bf16_f32 v199, v26, v27\n"
      "v_cvt_pk_bf16_f32 v28, v28, v29\n"
      "v_mfma_f32_16x16x32_bf16 v[54:57], a[32:35], v[72:75], v[54:57]\n"
      "ds_read_b128 a[32:35], v84 offset:46112\n"
      "ds_read_b64_tr_b16 a[192:193], v87 offset:46176\n"
      "v_cvt_pk_bf16_f32 v30, v30, v31\n"
      "v_accvgpr_write_b32 a220, v28\n"
      "v_mfma_f32_16x16x32_bf16 v[62:65], a[0:3], v[76:79], 0\n"
      "ds_read_b128 a[0:3], v85 offset:46112\n"
      "ds_read_b64_tr_b16 a[114:115], v87 offset:55328\n"
      "v_mfma_f32_16x16x32_bf16 v[20:23], a[4:7], v[68:71], v[62:65]\n"
      "ds_read_b64_tr_b16 a[122:123], v87 offset:55392\n"
      "v_accvgpr_write_b32 a221, v30\n"
      "v_cvt_pk_bf16_f32 v204, v32, v33\n"
      "v_mfma_f32_16x16x32_bf16 v[62:65], a[4:7], v[72:75], v[62:65]\n"
      "ds_read_b128 a[4:7], v86 offset:49248\n"
      "v_cvt_pk_bf16_f32 v205, v34, v35\n"
      "v_cvt_pk_bf16_f32 v212, v54, v55\n"
      "v_cvt_pk_bf16_f32 v213, v56, v57\n"
      "v_cvt_pk_bf16_f32 v208, v58, v59\n"
      "v_mfma_f32_16x16x32_bf16 v[24:27], a[8:11], v[76:79], 0\n"
      "v_cvt_pk_bf16_f32 v209, v60, v61\n"
      "v_cvt_pk_bf16_f32 v216, v20, v21\n"
      "v_mfma_f32_16x16x32_bf16 v[28:31], a[12:15], v[68:71], v[24:27]\n"
      "v_cvt_pk_bf16_f32 v62, v62, v63\n"
      "v_cvt_pk_bf16_f32 v64, v64, v65\n"
      "v_mfma_f32_16x16x32_bf16 v[24:27], a[12:15], v[72:75], v[24:27]\n"
      "ds_read_b128 a[12:15], v86 offset:58400\n"
      "v_accvgpr_write_b32 a224, v62\n"
      "v_mfma_f32_16x16x32_bf16 v[32:35], a[8:11], v[80:83], 0\n"
      "ds_read_b128 a[8:11], v84 offset:46176\n"
      "v_accvgpr_write_b32 a225, v64\n"
      "v_mfma_f32_16x16x32_bf16 v[54:57], a[16:19], v[68:71], v[32:35]\n"
      "v_cvt_pk_bf16_f32 v217, v22, v23\n"
      "v_cvt_pk_bf16_f32 v220, v28, v29\n"
      "v_mfma_f32_16x16x32_bf16 v[32:35], a[16:19], v[72:75], v[32:35]\n"
      "s_waitcnt lgkmcnt(2)\n"
      "ds_read_b128 a[16:19], v84 offset:55328\n"
      "v_cvt_pk_bf16_f32 v224, v24, v25\n"
      "v_cvt_pk_bf16_f32 v225, v26, v27\n"
      "v_mfma_f32_16x16x32_bf16 v[58:61], a[20:23], v[76:79], 0\n"
      "ds_read_b128 a[20:23], v85 offset:55328\n"
      "v_cvt_pk_bf16_f32 v228, v54, v55\n"
      "v_mfma_f32_16x16x32_bf16 v[62:65], a[24:27], v[68:71], v[58:61]\n"
      "v_cvt_pk_bf16_f32 v229, v56, v57\n"
      "v_cvt_pk_bf16_f32 v221, v30, v31\n"
      "v_mfma_f32_16x16x32_bf16 v[58:61], a[24:27], v[72:75], v[58:61]\n"
      "ds_read_b128 a[24:27], v86 offset:58464\n"
      "v_cvt_pk_bf16_f32 v32, v32, v33\n"
      "v_mfma_f32_16x16x32_bf16 v[36:39], a[36:39], a[72:75], 0\n"
      "ds_read_b128 a[36:39], v2 offset:13408\n"
      "v_cvt_pk_bf16_f32 v34, v34, v35\n"
      "v_mfma_f32_16x16x32_bf16 v[40:43], a[40:43], a[76:79], 0\n"
      "ds_read_b128 a[40:43], v3 offset:13408\n"
      "v_cvt_pk_bf16_f32 v232, v62, v63\n"
      "v_cvt_pk_bf16_f32 v236, v58, v59\n"
      "v_cvt_pk_bf16_f32 v237, v60, v61\n"
      "v_accvgpr_write_b32 a232, v32\n"
      "v_mfma_f32_16x16x32_bf16 v[44:47], a[44:47], a[80:83], v[36:39]\n"
      "ds_read_b128 a[44:47], v4 offset:13408\n"
      "v_accvgpr_write_b32 a233, v34\n"
      "v_fma_f32 v48, s44, v36, -v40\n"
      "v_fma_f32 v49, s44, v37, -v41\n"
      "v_fma_f32 v50, s44, v38, -v42\n"
      "v_mfma_f32_16x16x32_bf16 v[52:55], a[48:51], a[72:75], 0\n"
      "ds_read_b128 a[48:51], v2 offset:26560\n"
      "v_fma_f32 v51, s44, v39, -v43\n"
      "v_fmac_f32_e64 v40, s44, v36\n"
      "v_fmac_f32_e64 v41, s44, v37\n"
      "v_fmac_f32_e64 v42, s44, v38\n"
      "v_mfma_f32_16x16x32_bf16 v[56:59], a[52:55], a[76:79], 0\n"
      "ds_read_b128 a[52:55], v3 offset:26560\n"
      "v_fmac_f32_e64 v43, s44, v39\n"
      "v_cvt_pk_bf16_f32 v40, v40, v41\n"
      "v_cvt_pk_bf16_f32 v41, v42, v43\n"
      "v_cvt_pk_bf16_f32 v44, v44, v45\n"
      "v_mfma_f32_16x16x32_bf16 v[60:63], a[56:59], a[80:83], v[52:55]\n"
      "s_waitcnt lgkmcnt(4)\n"
      "ds_write_addtid_b32 v40 offset:768\n"
      "ds_write_addtid_b32 v41 offset:37696\n"
      "ds_write_addtid_b32 v44 offset:0\n"
      "v_cvt_pk_bf16_f32 v45, v46, v47\n"
      "v_cvt_pk_bf16_f32 v48, v48, v49\n"
      "v_mfma_f32_16x16x32_bf16 v[36:39], a[36:39], a[72:75], 0\n"
      "s_waitcnt lgkmcnt(6)\n"
      "ds_write_addtid_b32 v45 offset:36928\n"
      "ds_write_addtid_b32 v48 offset:1536\n"
      "ds_read_b128 a[56:59], v4 offset:26560\n"
      "v_cvt_pk_bf16_f32 v49, v50, v51\n"
      "v_cvt_pk_bf16_f32 v233, v64, v65\n"
      "v_mfma_f32_16x16x32_bf16 v[40:43], a[40:43], a[76:79], 0\n"
      "s_waitcnt lgkmcnt(8)\n"
      "ds_write_addtid_b32 v49 offset:38464\n"
      "ds_read_b128 a[36:39], v2 offset:26624\n"
      "s_mov_b32 m0, s18\n"
      "v_fma_f32 v64, s44, v52, -v56\n"
      "v_fma_f32 v65, s44, v53, -v57\n"
      "v_fma_f32 v66, s44, v54, -v58\n"
      "v_mfma_f32_16x16x32_bf16 v[44:47], a[44:47], a[80:83], v[36:39]\n"
      "ds_read_b128 a[40:43], v3 offset:26624\n"
      "ds_read_b128 a[44:47], v4 offset:26624\n"
      "v_fma_f32 v67, s44, v55, -v59\n"
      "v_fmac_f32_e64 v56, s44, v52\n"
      "v_fmac_f32_e64 v57, s44, v53\n"
      "v_mfma_f32_16x16x32_bf16 v[20:23], a[28:31], v[76:79], 0\n"
      "v_fmac_f32_e64 v58, s44, v54\n"
      "v_fmac_f32_e64 v59, s44, v55\n"
      "v_cvt_pk_bf16_f32 v56, v56, v57\n"
      "v_cvt_pk_bf16_f32 v57, v58, v59\n"
      "v_cvt_pk_bf16_f32 v60, v60, v61\n"
      "v_mfma_f32_16x16x32_bf16 v[24:27], a[32:35], v[68:71], v[20:23]\n"
      "ds_write_addtid_b32 v56 offset:1024\n"
      "ds_write_addtid_b32 v57 offset:37952\n"
      "ds_write_addtid_b32 v60 offset:256\n"
      "v_cvt_pk_bf16_f32 v61, v62, v63\n"
      "v_cvt_pk_bf16_f32 v64, v64, v65\n"
      "v_mfma_f32_16x16x32_bf16 v[20:23], a[32:35], v[72:75], v[20:23]\n"
      "ds_write_addtid_b32 v61 offset:37184\n"
      "ds_write_addtid_b32 v64 offset:1792\n"
      "v_cvt_pk_bf16_f32 v65, v66, v67\n"
      "v_fma_f32 v48, s44, v36, -v40\n"
      "v_fma_f32 v49, s44, v37, -v41\n"
      "v_mfma_f32_16x16x32_bf16 v[28:31], a[28:31], v[80:83], 0\n"
      "ds_write_addtid_b32 v65 offset:38720\n"
      "ds_read_b128 a[28:31], v84 offset:55392\n"
      "s_mov_b32 m0, s18\n"
      "v_fma_f32 v50, s44, v38, -v42\n"
      "v_fma_f32 v51, s44, v39, -v43\n"
      "v_fmac_f32_e64 v40, s44, v36\n"
      "v_mfma_f32_16x16x32_bf16 v[32:35], a[0:3], v[68:71], v[28:31]\n"
      "v_fmac_f32_e64 v41, s44, v37\n"
      "v_fmac_f32_e64 v42, s44, v38\n"
      "v_fmac_f32_e64 v43, s44, v39\n"
      "v_cvt_pk_bf16_f32 v40, v40, v41\n"
      "v_cvt_pk_bf16_f32 v41, v42, v43\n"
      "v_mfma_f32_16x16x32_bf16 v[28:31], a[0:3], v[72:75], v[28:31]\n"
      "ds_write_addtid_b32 v40 offset:1280\n"
      "ds_write_addtid_b32 v41 offset:38208\n"
      "v_cvt_pk_bf16_f32 v44, v44, v45\n"
      "v_cvt_pk_bf16_f32 v45, v46, v47\n"
      "v_cvt_pk_bf16_f32 v48, v48, v49\n"
      "v_mfma_f32_16x16x32_bf16 v[54:57], a[4:7], v[76:79], 0\n"
      "ds_write_addtid_b32 v44 offset:512\n"
      "ds_write_addtid_b32 v45 offset:37440\n"
      "ds_write_addtid_b32 v48 offset:2048\n"
      "v_cvt_pk_bf16_f32 v49, v50, v51\n"
      "v_add_u32 v88, 46144, v88\n"
      "v_mfma_f32_16x16x32_bf16 v[58:61], a[8:11], v[68:71], v[54:57]\n"
      "ds_write_addtid_b32 v49 offset:38976\n"
      "ds_read_b64_tr_b16 a[116:117], v88 offset:27680\n"
      "s_mov_b32 m0, s18\n"
      "v_cvt_pk_bf16_f32 v20, v20, v21\n"
      "v_cvt_pk_bf16_f32 v22, v22, v23\n"
      "v_accvgpr_write_b32 a240, v20\n"
      "v_mfma_f32_16x16x32_bf16 v[54:57], a[8:11], v[72:75], v[54:57]\n"
      "s_waitcnt lgkmcnt(14)\n"
      "ds_read_b64_tr_b16 a[152:153], v88 offset:36896\n"
      "ds_read_b64_tr_b16 a[188:189], v88 offset:46112\n"
      "v_cvt_pk_bf16_f32 v252, v58, v59\n"
      "v_cvt_pk_bf16_f32 v253, v60, v61\n"
      "v_accvgpr_write_b32 a241, v22\n"
      "s_nop 1\n"
      "v_cvt_pk_bf16_f32 v54, v54, v55\n"
      "v_accvgpr_write_b32 a244, v54\n"
      "v_mfma_f32_16x16x32_bf16 v[52:55], a[48:51], a[72:75], 0\n"
      "ds_read_b128 a[48:51], v2 offset:26688\n"
      "ds_read_b64_tr_b16 a[118:119], v88 offset:55328\n"
      "v_cvt_pk_bf16_f32 v56, v56, v57\n"
      "v_accvgpr_write_b32 a245, v56\n"
      "v_mfma_f32_16x16x32_bf16 v[56:59], a[52:55], a[76:79], 0\n"
      "ds_read_b128 a[52:55], v3 offset:26688\n"
      "v_cvt_pk_bf16_f32 v240, v24, v25\n"
      "v_cvt_pk_bf16_f32 v241, v26, v27\n"
      "v_cvt_pk_bf16_f32 v248, v28, v29\n"
      "v_cvt_pk_bf16_f32 v249, v30, v31\n"
      "v_mfma_f32_16x16x32_bf16 v[60:63], a[56:59], a[80:83], v[52:55]\n"
      "ds_read_b128 a[56:59], v4 offset:26688\n"
      "v_cvt_pk_bf16_f32 v244, v32, v33\n"
      "v_fma_f32 v64, s44, v52, -v56\n"
      "v_fma_f32 v65, s44, v53, -v57\n"
      "v_fma_f32 v66, s44, v54, -v58\n"
      "v_mfma_f32_16x16x32_bf16 v[36:39], a[36:39], a[72:75], 0\n"
      "v_fma_f32 v67, s44, v55, -v59\n"
      "v_fmac_f32_e64 v56, s44, v52\n"
      "v_fmac_f32_e64 v57, s44, v53\n"
      "v_fmac_f32_e64 v58, s44, v54\n"
      "v_mfma_f32_16x16x32_bf16 v[40:43], a[40:43], a[76:79], 0\n"
      "Lconv_p2:\n"
      "s_cbranch_execz Lconv_r0\n"
      "s_nop 0\n"
      "v_fmac_f32_e64 v59, s44, v55\n"
      "v_cvt_pk_bf16_f32 v64, v64, v65\n"
      "v_cvt_pk_bf16_f32 v65, v66, v67\n"
      "v_cvt_pk_bf16_f32 v56, v56, v57\n"
      "v_cvt_pk_bf16_f32 v57, v58, v59\n"
      "v_mfma_f32_16x16x32_bf16 v[44:47], a[44:47], a[80:83], v[36:39]\n"
      "s_waitcnt lgkmcnt(3)\n"
      "ds_write_addtid_b32 v64 offset:6144\n"
      "ds_write_addtid_b32 v65 offset:43072\n"
      "ds_write_addtid_b32 v56 offset:5376\n"
      "ds_write_addtid_b32 v57 offset:42304\n"
      "v_cvt_pk_bf16_f32 v60, v60, v61\n"
      "v_mfma_f32_16x16x32_bf16 v[52:55], a[48:51], a[72:75], 0\n"
      "s_waitcnt lgkmcnt(5)\n"
      "ds_write_addtid_b32 v60 offset:4608\n"
      "v_cvt_pk_bf16_f32 v61, v62, v63\n"
      "v_fma_f32 v48, s44, v36, -v40\n"
      "v_fma_f32 v49, s44, v37, -v41\n"
      "v_fma_f32 v50, s44, v38, -v42\n"
      "v_mfma_f32_16x16x32_bf16 v[56:59], a[52:55], a[76:79], 0\n"
      "s_waitcnt lgkmcnt(5)\n"
      "ds_write_addtid_b32 v61 offset:41536\n"
      "s_mov_b32 m0, s18\n"
      "v_fma_f32 v51, s44, v39, -v43\n"
      "v_fmac_f32_e64 v40, s44, v36\n"
      "v_fmac_f32_e64 v41, s44, v37\n"
      "v_fmac_f32_e64 v42, s44, v38\n"
      "v_mfma_f32_16x16x32_bf16 v[60:63], a[56:59], a[80:83], v[52:55]\n"
      "v_fmac_f32_e64 v43, s44, v39\n"
      "v_cvt_pk_bf16_f32 v48, v48, v49\n"
      "v_cvt_pk_bf16_f32 v49, v50, v51\n"
      "v_cvt_pk_bf16_f32 v40, v40, v41\n"
      "v_cvt_pk_bf16_f32 v41, v42, v43\n"
      "v_cvt_pk_bf16_f32 v44, v44, v45\n"
      "v_cvt_pk_bf16_f32 v45, v46, v47\n"
      "ds_write_addtid_b32 v48 offset:6400\n"
      "ds_write_addtid_b32 v49 offset:43328\n"
      "ds_write_addtid_b32 v40 offset:5632\n"
      "ds_write_addtid_b32 v41 offset:42560\n"
      "ds_write_addtid_b32 v44 offset:4864\n"
      "ds_write_addtid_b32 v45 offset:41792\n"
      "v_fma_f32 v64, s44, v52, -v56\n"
      "v_fma_f32 v65, s44, v53, -v57\n"
      "v_fma_f32 v66, s44, v54, -v58\n"
      "v_fma_f32 v67, s44, v55, -v59\n"
      "v_cvt_pk_bf16_f32 v64, v64, v65\n"
      "v_cvt_pk_bf16_f32 v65, v66, v67\n"
      "v_cvt_pk_bf16_f32 v60, v60, v61\n"
      "v_cvt_pk_bf16_f32 v61, v62, v63\n"
      "s_mov_b32 m0, s18\n"
      "s_nop 0\n"
      "ds_write_addtid_b32 v64 offset:6656\n"
      "ds_write_addtid_b32 v65 offset:43584\n"
      "v_mfma_f32_16x16x32_bf16 v[62:65], a[12:15], v[76:79], 0\n"
      "ds_write_addtid_b32 v60 offset:5120\n"
      "ds_write_addtid_b32 v61 offset:42048\n"
      "v_fmac_f32_e64 v56, s44, v52\n"
      "v_fmac_f32_e64 v57, s44, v53\n"
      "v_fmac_f32_e64 v58, s44, v54\n"
      "v_mfma_f32_16x16x32_bf16 v[20:23], a[16:19], v[68:71], v[62:65]\n"
      "v_fmac_f32_e64 v59, s44, v55\n"
      "v_cvt_pk_bf16_f32 v56, v56, v57\n"
      "v_cvt_pk_bf16_f32 v57, v58, v59\n"
      "ds_write_addtid_b32 v56 offset:5888\n"
      "ds_write_addtid_b32 v57 offset:42816\n"
      "v_mfma_f32_16x16x32_bf16 v[62:65], a[16:19], v[72:75], v[62:65]\n"
      "s_waitcnt lgkmcnt(0)\n"
      "s_waitcnt vmcnt(7)\n"
      "s_cbranch_vccnz Lconv_h9\n"
      "s_barrier\n"
      "Lconv_h9:\n"
      "s_add_u32 m0, s46, 13280\n"
      "s_add_u32 s51, s51, s12\n"
      "v_cvt_pk_bf16_f32 v245, v34, v35\n"
      "v_mfma_f32_16x16x32_bf16 v[24:27], a[12:15], v[80:83], 0\n"
      "v_cvt_pk_bf16_f32 v206, v20, v21\n"
      "v_cvt_pk_bf16_f32 v62, v62, v63\n"
      "v_mfma_f32_16x16x32_bf16 v[28:31], a[20:23], v[68:71], v[24:27]\n"
      "s_cbranch_vccz Lconv_h10\n"
      "s_barrier\n"
      "Lconv_h10:\n"
      "ds_read_b128 a[32:35], v86 offset:21536\n"
      "ds_read_b128 a[0:3], v84 offset:18464\n"
      "ds_read_b128 a[4:7], v85 offset:18464\n"
      "ds_read_b128 a[8:11], v86 offset:21600\n"
      "v_mfma_f32_16x16x32_bf16 v[24:27], a[20:23], v[72:75], v[24:27]\n"
      "ds_read_b128 a[12:15], v84 offset:18528\n"
      "ds_read_b128 a[16:19], v86 offset:3072\n"
      "v_mfma_f32_16x16x32_bf16 v[32:35], a[24:27], v[76:79], 0\n"
      "ds_read_b128 a[20:23], v84 offset:0\n"
      "ds_read_b128 a[24:27], v85 offset:0\n"
      "v_mfma_f32_16x16x32_bf16 v[54:57], a[28:31], v[68:71], v[32:35]\n"
      "ds_read_b64_tr_b16 a[150:151], v87 offset:18464\n"
      "ds_read_b64_tr_b16 a[154:155], v88 offset:18464\n"
      "v_cvt_pk_bf16_f32 v64, v64, v65\n"
      "v_accvgpr_write_b32 a222, v62\n"
      "v_mfma_f32_16x16x32_bf16 v[32:35], a[28:31], v[72:75], v[32:35]\n"
      "s_waitcnt lgkmcnt(9)\n"
      "ds_read_b64_tr_b16 a[158:159], v87 offset:18528\n"
      "ds_read_b64_tr_b16 a[186:187], v87 offset:0\n"
      "buffer_load_dwordx4 v16, s[32:35], s51 offen lds\n"
      "v_accvgpr_write_b32 a223, v64\n"
      "v_cvt_pk_bf16_f32 v207, v22, v23\n"
      "v_mfma_f32_16x16x32_bf16 v[58:61], a[32:35], v[76:79], 0\n"
      "s_waitcnt lgkmcnt(10)\n"
      "s_add_u32 m0, s46, 13280\n"
      "ds_read_b64_tr_b16 a[190:191], v88 offset:0\n"
      "ds_read_b128 a[28:31], v86 offset:3136\n"
      "buffer_load_dwordx4 v17, s[32:35], s51 offen offset:1024 lds\n"
      "v_cvt_pk_bf16_f32 v214, v24, v25\n"
      "v_cvt_pk_bf16_f32 v215, v26, v27\n"
      "v_mfma_f32_16x16x32_bf16 v[62:65], a[0:3], v[68:71], v[58:61]\n"
      "s_add_u32 m0, s46, 13280\n"
      "s_add_u32 s96, s51, s15\n"
      "ds_read_b64_tr_b16 a[194:195], v87 offset:64\n"
      "buffer_load_dwordx4 v18, s[32:35], s51 offen offset:2048 lds\n"
      "v_cvt_pk_bf16_f32 v210, v28, v29\n"
      "v_cvt_pk_bf16_f32 v211, v30, v31\n"
      "v_mfma_f32_16x16x32_bf16 v[58:61], a[0:3], v[72:75], v[58:61]\n"
      "s_mov_b32 m0, s59\n"
      "v_cvt_pk_bf16_f32 v32, v32, v33\n"
      "v_cvt_pk_bf16_f32 v34, v34, v35\n"
      "v_mfma_f32_16x16x32_bf16 v[20:23], a[32:35], v[80:83], 0\n"
      "s_waitcnt lgkmcnt(7)\n"
      "ds_read_b128 a[32:35], v84 offset:64\n"
      "buffer_load_dwordx4 v19, s[32:35], s96 offen lds\n"
      "v_mfma_f32_16x16x32_bf16 v[24:27], a[4:7], v[68:71], v[20:23]\n"
      "s_add_u32 m0, s46, 26560\n"
      "s_add_u32 s51, s51, s12\n"
      "buffer_load_dwordx4 v16, s[32:35], s51 offen lds\n"
      "v_accvgpr_write_b32 a226, v32\n"
      "v_accvgpr_write_b32 a227, v34\n"
      "v_mfma_f32_16x16x32_bf16 v[20:23], a[4:7], v[72:75], v[20:23]\n"
      "s_add_u32 m0, s46, 26560\n"
      "v_cvt_pk_bf16_f32 v218, v54, v55\n"
      "v_cvt_pk_bf16_f32 v219, v56, v57\n"
      "v_cvt_pk_bf16_f32 v226, v58, v59\n"
      "v_cvt_pk_bf16_f32 v227, v60, v61\n"
      "v_mfma_f32_16x16x32_bf16 v[28:31], a[8:11], v[76:79], 0\n"
      "buffer_load_dwordx4 v17, s[32:35], s51 offen offset:1024 lds\n"
      "v_cvt_pk_bf16_f32 v222, v62, v63\n"
      "v_mfma_f32_16x16x32_bf16 v[32:35], a[12:15], v[68:71], v[28:31]\n"
      "s_add_u32 m0, s46, 26560\n"
      "v_cvt_pk_bf16_f32 v223, v64, v65\n"
      "v_cvt_pk_bf16_f32 v20, v20, v21\n"
      "v_mfma_f32_16x16x32_bf16 v[28:31], a[12:15], v[72:75], v[28:31]\n"
      "ds_read_b128 a[12:15], v6 offset:0\n"
      "buffer_load_dwordx4 v18, s[32:35], s51 offen offset:2048 lds\n"
      "v_cvt_pk_bf16_f32 v22, v22, v23\n"
      "v_mfma_f32_16x16x32_bf16 v[54:57], a[16:19], v[76:79], 0\n"
      "v_accvgpr_write_b32 a234, v20\n"
      "v_accvgpr_write_b32 a235, v22\n"
      "v_mfma_f32_16x16x32_bf16 v[58:61], a[20:23], v[68:71], v[54:57]\n"
      "v_cvt_pk_bf16_f32 v230, v24, v25\n"
      "v_cvt_pk_bf16_f32 v231, v26, v27\n"
      "v_mfma_f32_16x16x32_bf16 v[54:57], a[20:23], v[72:75], v[54:57]\n"
      "s_waitcnt lgkmcnt(0)\n"
      "s_waitcnt vmcnt(7)\n"
      "s_cbranch_vccnz Lconv_h11\n"
      "s_barrier\n"
      "Lconv_h11:\n"
      "ds_read_b128 a[20:23], v6 offset:512\n"
      "v_cvt_pk_bf16_f32 v238, v28, v29\n"
      "v_cvt_pk_bf16_f32 v239, v30, v31\n"
      "v_mfma_f32_16x16x32_bf16 v[62:65], a[16:19], v[80:83], 0\n"
      "ds_read_b128 a[16:19], v6 offset:256\n"
      "v_cvt_pk_bf16_f32 v234, v32, v33\n"
      "v_mfma_f32_16x16x32_bf16 v[20:23], a[24:27], v[68:71], v[62:65]\n"
      "s_cbranch_vccz Lconv_h12\n"
      "s_barrier\n"
      "Lconv_h12:\n"
      "v_cvt_pk_bf16_f32 v235, v34, v35\n"
      "v_mfma_f32_16x16x32_bf16 v[62:65], a[24:27], v[72:75], v[62:65]\n"
      "ds_read_b128 a[24:27], v6 offset:4608\n"
      "v_cvt_pk_bf16_f32 v242, v58, v59\n"
      "v_cvt_pk_bf16_f32 v243, v60, v61\n"
      "v_mfma_f32_16x16x32_bf16 v[24:27], a[28:31], v[76:79], 0\n"
      "ds_read_b128 a[28:31], v6 offset:4864\n"
      "v_cvt_pk_bf16_f32 v54, v54, v55\n"
      "v_mfma_f32_16x16x32_bf16 v[28:31], a[32:35], v[68:71], v[24:27]\n"
      "v_cvt_pk_bf16_f32 v56, v56, v57\n"
      "v_accvgpr_write_b32 a242, v54\n"
      "v_mfma_f32_16x16x32_bf16 v[24:27], a[32:35], v[72:75], v[24:27]\n"
      "ds_read_b128 a[32:35], v6 offset:5120\n"
      "v_accvgpr_write_b32 a243, v56\n"
      "v_cvt_pk_bf16_f32 v246, v20, v21\n"
      "v_cvt_pk_bf16_f32 v247, v22, v23\n"
      "v_cvt_pk_bf16_f32 v250, v62, v63\n"
      "v_cvt_pk_bf16_f32 v251, v64, v65\n"
      "v_cvt_pk_bf16_f32 v254, v28, v29\n"
      "v_cvt_pk_bf16_f32 v255, v30, v31\n"
      "v_cvt_pk_bf16_f32 v24, v24, v25\n"
      "v_cvt_pk_bf16_f32 v26, v26, v27\n"
      "v_accvgpr_write_b32 a246, v24\n"
      "v_accvgpr_write_b32 a247, v26\n"
      "s_mov_b32 m0, s18\n"
      "ds_read_b128 a[48:51], v2 offset:39840\n"
      "v_lshlrev_b32 v9, 7, v5\n"
      "v_mfma_f32_16x16x32_bf16 v[68:71], a[12:15], a[88:91], 0\n"
      "s_waitcnt lgkmcnt(4)\n"
      "ds_read_b128 a[52:55], v3 offset:39840\n"
      "v_min_u32 v9, Lconv_u0-Lconv_t0, v9\n"
      "v_mfma_f32_16x16x32_bf16 v[68:71], a[16:19], a[100:103], v[68:71]\n"
      "s_mul_i32 s97, s41, Lconv_t1-Lconv_t0\n"
      "ds_read_b128 a[56:59], v4 offset:39840\n"
      "v_add_u32 v9, Lconv_t0-Lconv_start, v9\n"
      "v_mfma_f32_16x16x32_bf16 v[68:71], a[20:23], a[112:115], v[68:71]\n"
      "s_waitcnt lgkmcnt(5)\n"
      "ds_read_b128 a[36:39], v2 offset:39904\n"
      "v_add_u32 v9, s97, v9\n"
      "v_mfma_f32_16x16x32_bf16 v[68:71], a[24:27], a[124:127], v[68:71]\n"
      "s_waitcnt lgkmcnt(5)\n"
      "ds_read_b128 a[40:43], v3 offset:39904\n"
      "v_mfma_f32_16x16x32_bf16 v[68:71], a[28:31], a[136:139], v[68:71]\n"
      "s_waitcnt lgkmcnt(4)\n"
      "ds_read_b128 a[44:47], v4 offset:39904\n"
      "v_mfma_f32_16x16x32_bf16 v[52:55], a[48:51], a[72:75], 0\n"
      "ds_read_b128 a[0:3], v6 offset:768\n"
      "v_mfma_f32_16x16x32_bf16 v[68:71], a[32:35], a[148:151], v[68:71]\n"
      "ds_read_b128 a[48:51], v2 offset:39968\n"
      "v_mfma_f32_16x16x32_bf16 v[72:75], a[12:15], a[92:95], 0\n"
      "s_waitcnt lgkmcnt(6)\n"
      "ds_read_b128 a[4:7], v6 offset:1024\n"
      "v_mfma_f32_16x16x32_bf16 v[56:59], a[52:55], a[76:79], 0\n"
      "ds_read_b128 a[8:11], v6 offset:1280\n"
      "s_nop 0\n"
      "v_mfma_f32_16x16x32_bf16 v[72:75], a[16:19], a[104:107], v[72:75]\n"
      "ds_read_b128 a[52:55], v3 offset:39968\n"
      "s_nop 0\n"
      "v_mfma_f32_16x16x32_bf16 v[72:75], a[20:23], a[116:119], v[72:75]\n"
      "s_waitcnt lgkmcnt(7)\n"
      "ds_read_b128 a[60:63], v6 offset:1536\n"
      "v_fma_f32 v64, s44, v52, -v56\n"
      "v_mfma_f32_16x16x32_bf16 v[60:63], a[56:59], a[80:83], v[52:55]\n"
      "ds_read_b128 a[56:59], v4 offset:39968\n"
      "v_fma_f32 v65, s44, v53, -v57\n"
      "v_mfma_f32_16x16x32_bf16 v[72:75], a[24:27], a[128:131], v[72:75]\n"
      "v_cvt_pk_bf16_f32 v64, v64, v65\n"
      "v_fma_f32 v66, s44, v54, -v58\n"
      "v_mfma_f32_16x16x32_bf16 v[72:75], a[28:31], a[140:143], v[72:75]\n"
      "ds_write_addtid_b32 v64 offset:10752\n"
      "v_fma_f32 v67, s44, v55, -v59\n"
      "v_mfma_f32_16x16x32_bf16 v[72:75], a[32:35], a[152:155], v[72:75]\n"
      "v_cvt_pk_bf16_f32 v65, v66, v67\n"
      "v_fmac_f32_e64 v56, s44, v52\n"
      "v_mfma_f32_16x16x32_bf16 v[36:39], a[36:39], a[72:75], 0\n"
      "ds_write_addtid_b32 v65 offset:47680\n"
      "v_fmac_f32_e64 v57, s44, v53\n"
      "v_mfma_f32_16x16x32_bf16 v[76:79], a[12:15], a[96:99], 0\n"
      "ds_read_b128 a[12:15], v6 offset:5376\n"
      "v_cvt_pk_bf16_f32 v56, v56, v57\n"
      "v_mfma_f32_16x16x32_bf16 v[76:79], a[16:19], a[108:111], v[76:79]\n"
      "s_waitcnt lgkmcnt(5)\n"
      "ds_read_b128 a[16:19], v6 offset:5632\n"
      "v_fmac_f32_e64 v58, s44, v54\n"
      "v_mfma_f32_16x16x32_bf16 v[40:43], a[40:43], a[76:79], 0\n"
      "ds_write_addtid_b32 v56 offset:9984\n"
      "v_fmac_f32_e64 v59, s44, v55\n"
      "v_mfma_f32_16x16x32_bf16 v[76:79], a[20:23], a[120:123], v[76:79]\n"
      "ds_read_b128 a[20:23], v6 offset:5888\n"
      "v_cvt_pk_bf16_f32 v57, v58, v59\n"
      "v_mfma_f32_16x16x32_bf16 v[76:79], a[24:27], a[132:135], v[76:79]\n"
      "ds_write_addtid_b32 v57 offset:46912\n"
      "v_cvt_pk_bf16_f32 v60, v60, v61\n"
      "v_mfma_f32_16x16x32_bf16 v[44:47], a[44:47], a[80:83], v[36:39]\n"
      "ds_write_addtid_b32 v60 offset:9216\n"
      "v_cvt_pk_bf16_f32 v61, v62, v63\n"
      "v_mfma_f32_16x16x32_bf16 v[76:79], a[28:31], a[144:147], v[76:79]\n"
      "ds_write_addtid_b32 v61 offset:46144\n"
      "v_fma_f32 v48, s44, v36, -v40\n"
      "v_mfma_f32_16x16x32_bf16 v[76:79], a[32:35], a[156:159], v[76:79]\n"
      "ds_read_b128 a[36:39], v2 offset:0\n"
      "v_fma_f32 v49, s44, v37, -v41\n"
      "v_mfma_f32_16x16x32_bf16 v[80:83], a[0:3], v[92:95], 0\n"
      "v_cvt_pk_bf16_f32 v48, v48, v49\n"
      "v_fma_f32 v50, s44, v38, -v42\n"
      "v_mfma_f32_16x16x32_bf16 v[52:55], a[48:51], a[72:75], 0\n"
      "ds_write_addtid_b32 v48 offset:11008\n"
      "v_fma_f32 v51, s44, v39, -v43\n"
      "v_mfma_f32_16x16x32_bf16 v[80:83], a[4:7], v[148:151], v[80:83]\n"
      "ds_read_b128 a[40:43], v3 offset:0\n"
      "v_cvt_pk_bf16_f32 v49, v50, v51\n"
      "v_mfma_f32_16x16x32_bf16 v[80:83], a[8:11], v[204:207], v[80:83]\n"
      "ds_write_addtid_b32 v49 offset:47936\n"
      "v_fmac_f32_e64 v40, s44, v36\n"
      "v_mfma_f32_16x16x32_bf16 v[56:59], a[52:55], a[76:79], 0\n"
      "s_waitcnt lgkmcnt(7)\n"
      "v_fmac_f32_e64 v41, s44, v37\n"
      "v_cvt_pk_bf16_f32 v40, v40, v41\n"
      "v_mfma_f32_16x16x32_bf16 v[80:83], a[12:15], v[112:115], v[80:83]\n"
      "ds_write_addtid_b32 v40 offset:10240\n"
      "v_fmac_f32_e64 v42, s44, v38\n"
      "v_mfma_f32_16x16x32_bf16 v[80:83], a[16:19], a[208:211], v[80:83]\n"
      "v_fmac_f32_e64 v43, s44, v39\n"
      "v_cvt_pk_bf16_f32 v41, v42, v43\n"
      "v_mfma_f32_16x16x32_bf16 v[60:63], a[56:59], a[80:83], v[52:55]\n"
      "ds_write_addtid_b32 v41 offset:47168\n"
      "v_cvt_pk_bf16_f32 v44, v44, v45\n"
      "v_mfma_f32_16x16x32_bf16 v[80:83], a[20:23], v[220:223], v[80:83]\n"
      "ds_write_addtid_b32 v44 offset:9472\n"
      "v_cvt_pk_bf16_f32 v45, v46, v47\n"
      "v_mfma_f32_16x16x32_bf16 v[84:87], a[0:3], v[100:103], 0\n"
      "ds_read_b128 a[44:47], v4 offset:0\n"
      "v_fma_f32 v64, s44, v52, -v56\n"
      "v_mfma_f32_16x16x32_bf16 v[84:87], a[4:7], a[200:203], v[84:87]\n"
      "ds_write_addtid_b32 v45 offset:46400\n"
      "v_fma_f32 v65, s44, v53, -v57\n"
      "v_mfma_f32_16x16x32_bf16 v[84:87], a[8:11], v[208:211], v[84:87]\n"
      "ds_read_b128 a[64:67], v6 offset:1792\n"
      "v_cvt_pk_bf16_f32 v64, v64, v65\n"
      "v_mfma_f32_16x16x32_bf16 v[84:87], a[12:15], a[204:207], v[84:87]\n"
      "ds_read_b128 a[68:71], v6 offset:2048\n"
      "v_fma_f32 v66, s44, v54, -v58\n"
      "v_mfma_f32_16x16x32_bf16 v[84:87], a[16:19], v[172:175], v[84:87]\n"
      "ds_read_b128 a[24:27], v6 offset:6144\n"
      "v_fma_f32 v67, s44, v55, -v59\n"
      "v_mfma_f32_16x16x32_bf16 v[84:87], a[20:23], v[228:231], v[84:87]\n"
      "ds_read_b128 a[48:51], v2 offset:64\n"
      "v_cvt_pk_bf16_f32 v65, v66, v67\n"
      "v_mfma_f32_16x16x32_bf16 v[24:27], a[0:3], a[196:199], 0\n"
      "ds_write_addtid_b32 v64 offset:11264\n"
      "v_fmac_f32_e64 v56, s44, v52\n"
      "v_mfma_f32_16x16x32_bf16 v[24:27], a[4:7], v[160:163], v[24:27]\n"
      "s_waitcnt lgkmcnt(11)\n"
      "ds_read_b128 a[28:31], v6 offset:6400\n"
      "v_fmac_f32_e64 v57, s44, v53\n"
      "v_mfma_f32_16x16x32_bf16 v[36:39], a[36:39], a[72:75], 0\n"
      "ds_write_addtid_b32 v65 offset:48192\n"
      "v_cvt_pk_bf16_f32 v56, v56, v57\n"
      "v_mfma_f32_16x16x32_bf16 v[24:27], a[8:11], v[216:219], v[24:27]\n"
      "ds_read_b128 a[32:35], v6 offset:6656\n"
      "v_fmac_f32_e64 v58, s44, v54\n"
      "v_mfma_f32_16x16x32_bf16 v[24:27], a[12:15], v[124:127], v[24:27]\n"
      "ds_write_addtid_b32 v56 offset:10496\n"
      "v_fmac_f32_e64 v59, s44, v55\n"
      "v_mfma_f32_16x16x32_bf16 v[40:43], a[40:43], a[76:79], 0\n"
      "ds_read_b128 a[52:55], v3 offset:64\n"
      "v_cvt_pk_bf16_f32 v57, v58, v59\n"
      "v_mfma_f32_16x16x32_bf16 v[24:27], a[16:19], v[180:183], v[24:27]\n"
      "ds_write_addtid_b32 v57 offset:47424\n"
      "v_cvt_pk_bf16_f32 v60, v60, v61\n"
      "v_mfma_f32_16x16x32_bf16 v[24:27], a[20:23], v[232:235], v[24:27]\n"
      "s_waitcnt lgkmcnt(7)\n"
      "ds_write_addtid_b32 v60 offset:9728\n"
      "v_cvt_pk_bf16_f32 v61, v62, v63\n"
      "v_mfma_f32_16x16x32_bf16 v[44:47], a[44:47], a[80:83], v[36:39]\n"
      "ds_write_addtid_b32 v61 offset:46656\n"
      "v_fma_f32 v48, s44, v36, -v40\n"
      "v_mfma_f32_16x16x32_bf16 v[28:31], a[60:63], v[96:99], 0\n"
      "ds_read_b128 a[56:59], v4 offset:64\n"
      "v_fma_f32 v49, s44, v37, -v41\n"
      "v_mfma_f32_16x16x32_bf16 v[28:31], a[64:67], v[152:155], v[28:31]\n"
      "ds_read_b128 a[36:39], v2 offset:128\n"
      "v_cvt_pk_bf16_f32 v48, v48, v49\n"
      "v_mfma_f32_16x16x32_bf16 v[28:31], a[68:71], a[220:223], v[28:31]\n"
      "ds_read_b128 a[40:43], v3 offset:128\n"
      "v_fma_f32 v50, s44, v38, -v42\n"
      "v_mfma_f32_16x16x32_bf16 v[28:31], a[24:27], v[116:119], v[28:31]\n"
      "s_waitcnt lgkmcnt(0)\n"
      "s_cbranch_vccnz Lconv_h13\n"
      "s_barrier\n"
      "Lconv_h13:\n"
      "v_fma_f32 v51, s44, v39, -v43\n"
      "v_mfma_f32_16x16x32_bf16 v[52:55], a[48:51], a[72:75], 0\n"
      "ds_read_b128 a[44:47], v4 offset:128\n"
      "v_cvt_pk_bf16_f32 v49, v50, v51\n"
      "v_mfma_f32_16x16x32_bf16 v[28:31], a[28:31], v[168:171], v[28:31]\n"
      "ds_write_addtid_b32 v48 offset:15360\n"
      "v_fmac_f32_e64 v40, s44, v36\n"
      "v_mfma_f32_16x16x32_bf16 v[28:31], a[32:35], v[224:227], v[28:31]\n"
      "ds_write_addtid_b32 v49 offset:52288\n"
      "v_fmac_f32_e64 v41, s44, v37\n"
      "v_mfma_f32_16x16x32_bf16 v[56:59], a[52:55], a[76:79], 0\n"
      "s_cbranch_vccz Lconv_h14\n"
      "s_barrier\n"
      "Lconv_h14:\n"
      "v_cvt_pk_bf16_f32 v40, v40, v41\n"
      "v_mfma_f32_16x16x32_bf16 v[20:23], a[60:63], v[104:107], 0\n"
      "s_mov_b64 vcc, 0x2000000\n"
      "ds_write_addtid_b32 v40 offset:14592\n"
      "v_fmac_f32_e64 v42, s44, v38\n"
      "v_mfma_f32_16x16x32_bf16 v[20:23], a[64:67], v[156:159], v[20:23]\n"
      "ds_read_b128 a[0:3], v7 offset:0\n"
      "v_fmac_f32_e64 v43, s44, v39\n"
      "v_mfma_f32_16x16x32_bf16 v[60:63], a[56:59], a[80:83], v[52:55]\n"
      "v_cvt_pk_bf16_f32 v41, v42, v43\n"
      "v_cvt_pk_bf16_f32 v44, v44, v45\n"
      "v_mfma_f32_16x16x32_bf16 v[20:23], a[68:71], v[212:215], v[20:23]\n"
      "ds_write_addtid_b32 v41 offset:51520\n"
      "v_cvt_pk_bf16_f32 v45, v46, v47\n"
      "v_mfma_f32_16x16x32_bf16 v[20:23], a[24:27], v[120:123], v[20:23]\n"
      "ds_write_addtid_b32 v44 offset:13824\n"
      "ds_write_addtid_b32 v45 offset:50752\n"
      "v_mfma_f32_16x16x32_bf16 v[20:23], a[28:31], v[176:179], v[20:23]\n"
      "ds_read_b128 a[4:7], v7 offset:256\n"
      "v_fma_f32 v64, s44, v52, -v56\n"
      "v_mfma_f32_16x16x32_bf16 v[36:39], a[36:39], a[72:75], 0\n"
      "ds_read_b128 a[8:11], v7 offset:512\n"
      "v_fma_f32 v65, s44, v53, -v57\n"
      "v_mfma_f32_16x16x32_bf16 v[20:23], a[32:35], a[232:235], v[20:23]\n"
      "v_fma_f32 v66, s44, v54, -v58\n"
      "v_fma_f32 v67, s44, v55, -v59\n"
      "v_mfma_f32_16x16x32_bf16 v[32:35], a[60:63], v[108:111], 0\n"
      "v_fmac_f32_e64 v56, s44, v52\n"
      "v_fmac_f32_e64 v57, s44, v53\n"
      "v_mfma_f32_16x16x32_bf16 v[40:43], a[40:43], a[76:79], 0\n"
      "v_fmac_f32_e64 v58, s44, v54\n"
      "v_fmac_f32_e64 v59, s44, v55\n"
      "v_mfma_f32_16x16x32_bf16 v[32:35], a[64:67], v[164:167], v[32:35]\n"
      "v_cvt_pk_bf16_f32 v64, v64, v65\n"
      "v_cvt_pk_bf16_f32 v65, v66, v67\n"
      "v_mfma_f32_16x16x32_bf16 v[32:35], a[68:71], a[224:227], v[32:35]\n"
      "s_waitcnt lgkmcnt(5)\n"
      "v_cvt_pk_bf16_f32 v56, v56, v57\n"
      "v_cvt_pk_bf16_f32 v57, v58, v59\n"
      "v_mfma_f32_16x16x32_bf16 v[44:47], a[44:47], a[80:83], v[36:39]\n"
      "ds_write_addtid_b32 v64 offset:15616\n"
      "v_cvt_pk_bf16_f32 v60, v60, v61\n"
      "v_mfma_f32_16x16x32_bf16 v[32:35], a[24:27], v[128:131], v[32:35]\n"
      "ds_write_addtid_b32 v65 offset:52544\n"
      "v_cvt_pk_bf16_f32 v61, v62, v63\n"
      "v_mfma_f32_16x16x32_bf16 v[32:35], a[28:31], a[228:231], v[32:35]\n"
      "ds_write_addtid_b32 v56 offset:14848\n"
      "ds_write_addtid_b32 v57 offset:51776\n"
      "v_mfma_f32_16x16x32_bf16 v[32:35], a[32:35], v[236:239], v[32:35]\n"
      "ds_write_addtid_b32 v60 offset:14080\n"
      "ds_write_addtid_b32 v61 offset:51008\n"
      "v_mfma_f32_16x16x32_bf16 v[68:71], a[0:3], a[160:163], v[68:71]\n"
      "s_waitcnt lgkmcnt(6)\n"
      "ds_read_b128 a[12:15], v7 offset:768\n"
      "v_fma_f32 v48, s44, v36, -v40\n"
      "v_mfma_f32_16x16x32_bf16 v[68:71], a[4:7], a[172:175], v[68:71]\n"
      "s_add_u32 m0, s46, 39840\n"
      "ds_read_b128 a[16:19], v7 offset:1024\n"
      "v_fma_f32 v49, s44, v37, -v41\n"
      "v_mfma_f32_16x16x32_bf16 v[68:71], a[8:11], a[184:187], v[68:71]\n"
      "s_add_u32 s51, s51, s12\n"
      "ds_read_b128 a[20:23], v7 offset:1280\n"
      "v_cvt_pk_bf16_f32 v48, v48, v49\n"
      "v_mfma_f32_16x16x32_bf16 v[72:75], a[0:3], a[164:167], v[72:75]\n"
      "buffer_load_dwordx4 v16, s[32:35], s51 offen lds\n"
      "v_fma_f32 v50, s44, v38, -v42\n"
      "v_mfma_f32_16x16x32_bf16 v[72:75], a[4:7], a[176:179], v[72:75]\n"
      "v_fma_f32 v51, s44, v39, -v43\n"
      "v_cvt_pk_bf16_f32 v49, v50, v51\n"
      "v_mfma_f32_16x16x32_bf16 v[72:75], a[8:11], a[188:191], v[72:75]\n"
      "v_fmac_f32_e64 v40, s44, v36\n"
      "v_fmac_f32_e64 v41, s44, v37\n"
      "v_mfma_f32_16x16x32_bf16 v[76:79], a[0:3], a[168:171], v[76:79]\n"
      "buffer_load_dwordx4 v17, s[32:35], s51 offen offset:1024 lds\n"
      "v_cvt_pk_bf16_f32 v40, v40, v41\n"
      "v_mfma_f32_16x16x32_bf16 v[76:79], a[4:7], a[180:183], v[76:79]\n"
      "ds_read_b128 a[0:3], v7 offset:1536\n"
      "v_fmac_f32_e64 v42, s44, v38\n"
      "v_mfma_f32_16x16x32_bf16 v[76:79], a[8:11], a[192:195], v[76:79]\n"
      "s_waitcnt lgkmcnt(3)\n"
      "ds_read_b128 a[4:7], v7 offset:1792\n"
      "v_fmac_f32_e64 v43, s44, v39\n"
      "v_mfma_f32_16x16x32_bf16 v[80:83], a[12:15], a[212:215], v[80:83]\n"
      "s_waitcnt lgkmcnt(3)\n"
      "ds_read_b128 a[8:11], v7 offset:2048\n"
      "v_cvt_pk_bf16_f32 v41, v42, v43\n"
      "v_mfma_f32_16x16x32_bf16 v[80:83], a[16:19], v[184:187], v[80:83]\n"
      "s_waitcnt lgkmcnt(3)\n"
      "v_cvt_pk_bf16_f32 v44, v44, v45\n"
      "v_cvt_pk_bf16_f32 v45, v46, v47\n"
      "v_mfma_f32_16x16x32_bf16 v[80:83], a[20:23], v[240:243], v[80:83]\n"
      "buffer_load_dwordx4 v18, s[32:35], s51 offen offset:2048 lds\n"
      "v_cvt_pk_bf16_f32 v68, v68, v69\n"
      "v_mfma_f32_16x16x32_bf16 v[84:87], a[12:15], v[136:139], v[84:87]\n"
      "s_mov_b32 m0, s18\n"
      "v_cvt_pk_bf16_f32 v69, v70, v71\n"
      "v_cvt_pk_bf16_f32 v72, v72, v73\n"
      "v_mfma_f32_16x16x32_bf16 v[84:87], a[16:19], v[192:195], v[84:87]\n"
      "ds_write_addtid_b32 v48 offset:15872\n"
      "ds_write_addtid_b32 v49 offset:52800\n"
      "v_mfma_f32_16x16x32_bf16 v[84:87], a[20:23], v[244:247], v[84:87]\n"
      "ds_write_addtid_b32 v40 offset:15104\n"
      "ds_write_addtid_b32 v41 offset:52032\n"
      "v_mfma_f32_16x16x32_bf16 v[24:27], a[12:15], a[216:219], v[24:27]\n"
      "ds_write_addtid_b32 v44 offset:14336\n"
      "ds_write_addtid_b32 v45 offset:51264\n"
      "v_mfma_f32_16x16x32_bf16 v[24:27], a[16:19], v[196:199], v[24:27]\n"
      "s_mov_b32 m0, s45\n"
      "v_cvt_pk_bf16_f32 v73, v74, v75\n"
      "v_cvt_pk_bf16_f32 v76, v76, v77\n"
      "v_mfma_f32_16x16x32_bf16 v[24:27], a[20:23], v[252:255], v[24:27]\n"
      "s_waitcnt lgkmcnt(6)\n"
      "ds_write_addtid_b32 v68 offset:57360\n"
      "ds_write_addtid_b32 v69 offset:57616\n"
      "v_mfma_f32_16x16x32_bf16 v[28:31], a[0:3], v[132:135], v[28:31]\n"
      "ds_write_addtid_b32 v72 offset:45064\n"
      "ds_write_addtid_b32 v73 offset:45320\n"
      "v_mfma_f32_16x16x32_bf16 v[28:31], a[4:7], v[188:191], v[28:31]\n"
      "ds_write_addtid_b32 v76 offset:32768\n"
      "v_cvt_pk_bf16_f32 v77, v78, v79\n"
      "v_mfma_f32_16x16x32_bf16 v[28:31], a[8:11], a[240:243], v[28:31]\n"
      "ds_write_addtid_b32 v77 offset:33024\n"
      "v_cvt_pk_bf16_f32 v80, v80, v81\n"
      "v_mfma_f32_16x16x32_bf16 v[20:23], a[0:3], v[140:143], v[20:23]\n"
      "ds_write_addtid_b32 v80 offset:58384\n"
      "v_cvt_pk_bf16_f32 v81, v82, v83\n"
      "v_mfma_f32_16x16x32_bf16 v[20:23], a[4:7], a[236:239], v[20:23]\n"
      "ds_write_addtid_b32 v81 offset:58640\n"
      "v_cvt_pk_bf16_f32 v84, v84, v85\n"
      "v_mfma_f32_16x16x32_bf16 v[20:23], a[8:11], v[248:251], v[20:23]\n"
      "ds_write_addtid_b32 v84 offset:46088\n"
      "v_cvt_pk_bf16_f32 v85, v86, v87\n"
      "v_mfma_f32_16x16x32_bf16 v[32:35], a[0:3], v[144:147], v[32:35]\n"
      "ds_write_addtid_b32 v85 offset:46344\n"
      "v_cvt_pk_bf16_f32 v24, v24, v25\n"
      "v_mfma_f32_16x16x32_bf16 v[32:35], a[4:7], v[200:203], v[32:35]\n"
      "ds_write_addtid_b32 v24 offset:33792\n"
      "v_cvt_pk_bf16_f32 v25, v26, v27\n"
      "v_mfma_f32_16x16x32_bf16 v[32:35], a[8:11], a[244:247], v[32:35]\n"
      "ds_write_addtid_b32 v25 offset:34048\n"
      "v_cvt_pk_bf16_f32 v28, v28, v29\n"
      "v_cvt_pk_bf16_f32 v29, v30, v31\n"
      "ds_write_addtid_b32 v28 offset:59408\n"
      "ds_write_addtid_b32 v29 offset:59664\n"
      "v_cvt_pk_bf16_f32 v20, v20, v21\n"
      "v_cvt_pk_bf16_f32 v21, v22, v23\n"
      "ds_write_addtid_b32 v20 offset:47112\n"
      "ds_write_addtid_b32 v21 offset:47368\n"
      "s_waitcnt vmcnt(3)\n"
      "s_waitcnt lgkmcnt(0)\n"
      "s_cmp_eq_u32 s41, 1\n"
      "s_cbranch_scc1 Lconv_s1\n"
      "Lconv_s0:\n"
      "s_branch Lconv_loop0\n"
      "Lconv_s1:\n"
      "v_cvt_pk_bf16_f32 v32, v32, v33\n"
      "s_branch Lconv_loop1\n"
      ".p2align 12\n"
      "Lconv_pa0:\n"
      "s_cbranch_execz Lconv_r0\n"
      "s_nop 0\n"
      "Lconv_loop0:\n"
      "v_mfma_f32_16x16x32_bf16 v[20:23], a[24:27], v[96:99], 0\n"
      "s_add_u32 s51, s51, s12\n"
      "s_nop 1\n"
      "v_mfma_f32_16x16x32_bf16 v[20:23], a[28:31], v[152:155], v[20:23]\n"
      "s_sub_u32 s58, s93, s58\n"
      "v_cvt_pk_bf16_f32 v32, v32, v33\n"
      "v_cvt_pk_bf16_f32 v33, v34, v35\n"
      "v_mfma_f32_16x16x32_bf16 v[20:23], a[32:35], a[220:223], v[20:23]\n"
      "s_sub_u32 s59, s94, s59\n"
      "ds_write_addtid_b32 v32 offset:34816\n"
      "v_mfma_f32_16x16x32_bf16 v[20:23], a[0:3], v[116:119], v[20:23]\n"
      "s_sub_u32 s47, s51, s48\n"
      "ds_write_addtid_b32 v33 offset:35072\n"
      "v_mfma_f32_16x16x32_bf16 v[20:23], a[4:7], v[168:171], v[20:23]\n"
      "s_cmp_ge_u32 vcc_lo, 0x80000000\n"
      "v_mfma_f32_16x16x32_bf16 v[20:23], a[8:11], v[224:227], v[20:23]\n"
      "s_cselect_b32 s53, s47, s51\n"
      "v_mfma_f32_16x16x32_bf16 v[24:27], a[24:27], v[104:107], 0\n"
      "s_waitcnt vmcnt(8) lgkmcnt(0)\n"
      "s_barrier\n"
      "v_mfma_f32_16x16x32_bf16 v[24:27], a[28:31], v[156:159], v[24:27]\n"
      "s_mov_b32 m0, s58\n"
      "ds_read_b128 a[12:15], v7 offset:6144\n"
      "v_mfma_f32_16x16x32_bf16 v[24:27], a[32:35], v[212:215], v[24:27]\n"
      "ds_read_b128 a[16:19], v7 offset:6400\n"
      "v_mfma_f32_16x16x32_bf16 v[24:27], a[0:3], v[120:123], v[24:27]\n"
      "ds_read_b128 a[20:23], v7 offset:6656\n"
      "v_mfma_f32_16x16x32_bf16 v[24:27], a[4:7], v[176:179], v[24:27]\n"
      "ds_read_b128 a[48:51], v2 offset:13280\n"
      "v_mfma_f32_16x16x32_bf16 v[24:27], a[8:11], a[232:235], v[24:27]\n"
      "ds_read_b128 a[52:55], v3 offset:13280\n"
      "v_mfma_f32_16x16x32_bf16 v[68:71], a[84:87], a[60:63], 0\n"
      "ds_read_b128 a[56:59], v4 offset:13280\n"
      "s_nop 0\n"
      "v_mfma_f32_16x16x32_bf16 v[28:31], a[24:27], v[108:111], 0\n"
      "ds_read2_b64 a[64:67], v10 offset0:0 offset1:64\n"
      "s_nop 0\n"
      "v_mfma_f32_16x16x32_bf16 v[28:31], a[28:31], v[164:167], v[28:31]\n"
      "buffer_load_dwordx4 v16, s[32:35], s53 offen lds\n"
      "v_mfma_f32_16x16x32_bf16 v[28:31], a[32:35], a[224:227], v[28:31]\n"
      "s_mov_b32 m0, s45\n"
      "ds_read2_b64 a[68:71], v10 offset0:1 offset1:65\n"
      "v_cvt_pk_bf16_f32 v85, v68, v69\n"
      "v_mfma_f32_16x16x32_bf16 v[28:31], a[0:3], v[128:131], v[28:31]\n"
      "ds_read_b128 a[24:27], v6 offset:5376\n"
      "v_cvt_pk_bf16_f32 v91, v70, v71\n"
      "v_mfma_f32_16x16x32_bf16 v[28:31], a[4:7], a[228:231], v[28:31]\n"
      "ds_read_b128 a[28:31], v6 offset:5632\n"
      "v_mfma_f32_16x16x32_bf16 v[28:31], a[8:11], v[236:239], v[28:31]\n"
      "s_waitcnt lgkmcnt(6)\n"
      "ds_read_b128 a[32:35], v6 offset:5888\n"
      "v_mfma_f32_16x16x32_bf16 v[20:23], a[12:15], v[132:135], v[20:23]\n"
      "buffer_store_dwordx2 v[84:85], v15, s[36:39], s52 offen sc1\n"
      "v_mfma_f32_16x16x32_bf16 v[20:23], a[16:19], v[188:191], v[20:23]\n"
      "ds_read_b128 a[0:3], v7 offset:768\n"
      "v_mfma_f32_16x16x32_bf16 v[20:23], a[20:23], a[240:243], v[20:23]\n"
      "ds_read_b128 a[4:7], v7 offset:1024\n"
      "s_nop 0\n"
      "v_mfma_f32_16x16x32_bf16 v[52:55], a[48:51], a[72:75], 0\n"
      "s_waitcnt lgkmcnt(6)\n"
      "ds_read_b128 a[8:11], v7 offset:1280\n"
      "s_nop 0\n"
      "v_mfma_f32_16x16x32_bf16 v[56:59], a[52:55], a[76:79], 0\n"
      "ds_read_b128 a[36:39], v2 offset:13344\n"
      "v_cvt_pk_bf16_f32 v20, v20, v21\n"
      "v_mfma_f32_16x16x32_bf16 v[60:63], a[56:59], a[80:83], v[52:55]\n"
      "buffer_store_dwordx2 v[90:91], v15, s[36:39], s52 offen offset:1536 sc1\n"
      "v_cvt_pk_bf16_f32 v21, v22, v23\n"
      "v_mfma_f32_16x16x32_bf16 v[24:27], a[12:15], v[140:143], v[24:27]\n"
      "s_mov_b32 s52, s13\n"
      "ds_write_addtid_b32 v20 offset:62480\n"
      "v_fma_f32 v64, s44, v52, -v56\n"
      "v_mfma_f32_16x16x32_bf16 v[24:27], a[16:19], a[236:239], v[24:27]\n"
      "s_add_u32 s13, s13, s12\n"
      "ds_read_b128 a[40:43], v3 offset:13344\n"
      "v_fma_f32 v65, s44, v53, -v57\n"
      "v_mfma_f32_16x16x32_bf16 v[24:27], a[20:23], v[248:251], v[24:27]\n"
      "s_sub_u32 s96, s53, s95\n"
      "ds_write_addtid_b32 v21 offset:62736\n"
      "v_cvt_pk_bf16_f32 v64, v64, v65\n"
      "v_mfma_f32_16x16x32_bf16 v[72:75], a[84:87], a[64:67], 0\n"
      "ds_read_b128 a[44:47], v4 offset:13344\n"
      "v_fma_f32 v66, s44, v54, -v58\n"
      "v_mfma_f32_16x16x32_bf16 v[28:31], a[12:15], v[144:147], v[28:31]\n"
      "ds_read_b128 a[12:15], v7 offset:5376\n"
      "v_cvt_pk_bf16_f32 v24, v24, v25\n"
      "v_mfma_f32_16x16x32_bf16 v[28:31], a[16:19], v[200:203], v[28:31]\n"
      "ds_read_b128 a[16:19], v7 offset:5632\n"
      "v_cvt_pk_bf16_f32 v25, v26, v27\n"
      "v_mfma_f32_16x16x32_bf16 v[28:31], a[20:23], a[244:247], v[28:31]\n"
      "s_waitcnt lgkmcnt(6)\n"
      "ds_read_b128 a[20:23], v7 offset:5888\n"
      "v_fma_f32 v67, s44, v55, -v59\n"
      "v_mfma_f32_16x16x32_bf16 v[76:79], a[84:87], a[68:71], 0\n"
      "ds_read2_b64 a[60:63], v10 offset0:2 offset1:66\n"
      "v_cvt_pk_bf16_f32 v65, v66, v67\n"
      "v_mfma_f32_16x16x32_bf16 v[32:35], a[24:27], v[92:95], 0\n"
      "ds_write_addtid_b32 v24 offset:50184\n"
      "ds_write_addtid_b32 v25 offset:50440\n"
      "v_mfma_f32_16x16x32_bf16 v[32:35], a[28:31], v[148:151], v[32:35]\n"
      "s_mov_b32 m0, s18\n"
      "v_fmac_f32_e32 v56, s44, v52\n"
      "v_fmac_f32_e64 v57, s44, v53\n"
      "v_mfma_f32_16x16x32_bf16 v[32:35], a[32:35], v[204:207], v[32:35]\n"
      "ds_write_addtid_b32 v64 offset:1536\n"
      "v_fmac_f32_e64 v58, s44, v54\n"
      "v_mfma_f32_16x16x32_bf16 v[32:35], a[0:3], v[112:115], v[32:35]\n"
      "ds_write_addtid_b32 v65 offset:38464\n"
      "v_fmac_f32_e32 v59, s44, v55\n"
      "v_mfma_f32_16x16x32_bf16 v[32:35], a[4:7], a[208:211], v[32:35]\n"
      "v_cvt_pk_bf16_f32 v56, v56, v57\n"
      "v_cvt_pk_bf16_f32 v57, v58, v59\n"
      "v_mfma_f32_16x16x32_bf16 v[32:35], a[8:11], v[220:223], v[32:35]\n"
      "s_waitcnt lgkmcnt(6)\n"
      "ds_write_addtid_b32 v56 offset:768\n"
      "v_cvt_pk_bf16_f32 v60, v60, v61\n"
      "v_mfma_f32_16x16x32_bf16 v[32:35], a[12:15], a[212:215], v[32:35]\n"
      "ds_write_addtid_b32 v57 offset:37696\n"
      "v_cvt_pk_bf16_f32 v61, v62, v63\n"
      "v_mfma_f32_16x16x32_bf16 v[32:35], a[16:19], v[184:187], v[32:35]\n"
      "s_waitcnt lgkmcnt(6)\n"
      "ds_write_addtid_b32 v60 offset:0\n"
      "ds_write_addtid_b32 v61 offset:36928\n"
      "v_mfma_f32_16x16x32_bf16 v[32:35], a[20:23], v[240:243], v[32:35]\n"
      "s_mov_b32 m0, s45\n"
      "ds_read_b128 a[48:51], v2 offset:13408\n"
      "v_cvt_pk_bf16_f32 v28, v28, v29\n"
      "v_mfma_f32_16x16x32_bf16 v[20:23], a[24:27], v[100:103], 0\n"
      "ds_write_addtid_b32 v28 offset:37888\n"
      "v_cvt_pk_bf16_f32 v29, v30, v31\n"
      "v_mfma_f32_16x16x32_bf16 v[20:23], a[28:31], a[200:203], v[20:23]\n"
      "ds_write_addtid_b32 v29 offset:38144\n"
      "v_cvt_pk_bf16_f32 v32, v32, v33\n"
      "v_mfma_f32_16x16x32_bf16 v[20:23], a[32:35], v[208:211], v[20:23]\n"
      "s_mov_b32 m0, s58\n"
      "ds_read_b128 a[56:59], v4 offset:13408\n"
      "v_cvt_pk_bf16_f32 v33, v34, v35\n"
      "v_mfma_f32_16x16x32_bf16 v[20:23], a[0:3], a[204:207], v[20:23]\n"
      "buffer_load_dwordx4 v17, s[32:35], s53 offen offset:1024 lds\n"
      "v_cvt_pk_bf16_f32 v80, v72, v73\n"
      "v_mfma_f32_16x16x32_bf16 v[20:23], a[4:7], v[172:175], v[20:23]\n"
      "s_mov_b32 m0, s45\n"
      "ds_read2_b64 a[64:67], v10 offset0:3 offset1:67\n"
      "v_cvt_pk_bf16_f32 v81, v76, v77\n"
      "v_mfma_f32_16x16x32_bf16 v[20:23], a[8:11], v[228:231], v[20:23]\n"
      "ds_write_addtid_b32 v32 offset:61456\n"
      "v_cvt_pk_bf16_f32 v86, v74, v75\n"
      "v_mfma_f32_16x16x32_bf16 v[20:23], a[12:15], v[136:139], v[20:23]\n"
      "ds_write_addtid_b32 v33 offset:61712\n"
      "v_cvt_pk_bf16_f32 v87, v78, v79\n"
      "v_mfma_f32_16x16x32_bf16 v[20:23], a[16:19], v[192:195], v[20:23]\n"
      "ds_read_b128 a[52:55], v3 offset:13408\n"
      "v_add_u32_e64 v2, s91, v2\n"
      "v_mfma_f32_16x16x32_bf16 v[20:23], a[20:23], v[244:247], v[20:23]\n"
      "ds_read2_b64 a[68:71], v12 offset0:0 offset1:64\n"
      "v_add_u32_e64 v3, s91, v3\n"
      "v_mfma_f32_16x16x32_bf16 v[36:39], a[36:39], a[72:75], 0\n"
      "v_add_u32_e32 v4, s91, v4\n"
      "s_nop 0\n"
      "v_mfma_f32_16x16x32_bf16 v[40:43], a[40:43], a[76:79], 0\n"
      "s_sub_u32 s91, 0, s91\n"
      "v_mfma_f32_16x16x32_bf16 v[44:47], a[44:47], a[80:83], v[36:39]\n"
      "v_cvt_pk_bf16_f32 v20, v20, v21\n"
      "v_cvt_pk_bf16_f32 v21, v22, v23\n"
      "v_mfma_f32_16x16x32_bf16 v[68:71], a[84:87], a[60:63], 0\n"
      "ds_write_addtid_b32 v20 offset:49160\n"
      "s_nop 0\n"
      "v_mfma_f32_16x16x32_bf16 v[24:27], a[24:27], a[196:199], 0\n"
      "ds_read_b128 a[24:27], v7 offset:4608\n"
      "v_fma_f32 v48, s44, v36, -v40\n"
      "v_mfma_f32_16x16x32_bf16 v[24:27], a[28:31], v[160:163], v[24:27]\n"
      "ds_read_b128 a[28:31], v7 offset:4864\n"
      "v_fma_f32 v49, s44, v37, -v41\n"
      "v_mfma_f32_16x16x32_bf16 v[24:27], a[32:35], v[216:219], v[24:27]\n"
      "ds_read_b128 a[32:35], v7 offset:5120\n"
      "v_cvt_pk_bf16_f32 v48, v48, v49\n"
      "v_mfma_f32_16x16x32_bf16 v[24:27], a[0:3], v[124:127], v[24:27]\n"
      "ds_read_b128 a[0:3], v6 offset:4608\n"
      "v_fma_f32 v50, s44, v38, -v42\n"
      "v_mfma_f32_16x16x32_bf16 v[24:27], a[4:7], v[180:183], v[24:27]\n"
      "ds_read_b128 a[4:7], v6 offset:4864\n"
      "v_fma_f32 v51, s44, v39, -v43\n"
      "v_mfma_f32_16x16x32_bf16 v[24:27], a[8:11], v[232:235], v[24:27]\n"
      "ds_read_b128 a[8:11], v6 offset:5120\n"
      "v_cvt_pk_bf16_f32 v49, v50, v51\n"
      "v_mfma_f32_16x16x32_bf16 v[24:27], a[12:15], a[216:219], v[24:27]\n"
      "ds_read_b128 a[12:15], v7 offset:0\n"
      "v_fmac_f32_e64 v40, s44, v36\n"
      "v_mfma_f32_16x16x32_bf16 v[24:27], a[16:19], v[196:199], v[24:27]\n"
      "ds_read_b128 a[16:19], v7 offset:256\n"
      "v_fmac_f32_e32 v41, s44, v37\n"
      "v_mfma_f32_16x16x32_bf16 v[24:27], a[20:23], v[252:255], v[24:27]\n"
      "s_waitcnt lgkmcnt(7)\n"
      "ds_read_b128 a[20:23], v7 offset:512\n"
      "v_cvt_pk_bf16_f32 v40, v40, v41\n"
      "v_mfma_f32_16x16x32_bf16 v[52:55], a[48:51], a[72:75], 0\n"
      "ds_write_addtid_b32 v21 offset:49416\n"
      "v_fmac_f32_e32 v42, s44, v38\n"
      "v_mfma_f32_16x16x32_bf16 v[60:63], a[56:59], a[80:83], v[52:55]\n"
      "s_mov_b32 m0, s58\n"
      "v_cvt_pk_bf16_f32 v24, v24, v25\n"
      "v_cvt_pk_bf16_f32 v25, v26, v27\n"
      "v_mfma_f32_16x16x32_bf16 v[72:75], a[84:87], a[64:67], 0\n"
      "buffer_load_dwordx4 v18, s[32:35], s53 offen offset:2048 lds\n"
      "v_fmac_f32_e64 v43, s44, v39\n"
      "v_mfma_f32_16x16x32_bf16 v[28:31], a[24:27], a[160:163], 0\n"
      "s_waitcnt lgkmcnt(5)\n"
      "v_cvt_pk_bf16_f32 v41, v42, v43\n"
      "v_cvt_pk_bf16_f32 v44, v44, v45\n"
      "v_mfma_f32_16x16x32_bf16 v[28:31], a[28:31], a[172:175], v[28:31]\n"
      "s_mov_b32 m0, s45\n"
      "v_cvt_pk_bf16_f32 v82, v68, v69\n"
      "v_cvt_pk_bf16_f32 v83, v72, v73\n"
      "v_mfma_f32_16x16x32_bf16 v[28:31], a[32:35], a[184:187], v[28:31]\n"
      "ds_write_addtid_b32 v24 offset:36864\n"
      "ds_write_addtid_b32 v25 offset:37120\n"
      "v_mfma_f32_16x16x32_bf16 v[28:31], a[0:3], a[88:91], v[28:31]\n"
      "s_mov_b32 m0, s18\n"
      "v_cvt_pk_bf16_f32 v45, v46, v47\n"
      "v_cvt_pk_bf16_f32 v88, v70, v71\n"
      "v_mfma_f32_16x16x32_bf16 v[28:31], a[4:7], a[100:103], v[28:31]\n"
      "s_waitcnt lgkmcnt(3)\n"
      "ds_write_addtid_b32 v48 offset:1792\n"
      "ds_write_addtid_b32 v49 offset:38720\n"
      "v_mfma_f32_16x16x32_bf16 v[28:31], a[8:11], a[112:115], v[28:31]\n"
      "ds_write_addtid_b32 v40 offset:1024\n"
      "ds_write_addtid_b32 v41 offset:37952\n"
      "v_mfma_f32_16x16x32_bf16 v[28:31], a[12:15], a[124:127], v[28:31]\n"
      "ds_write_addtid_b32 v44 offset:256\n"
      "ds_write_addtid_b32 v45 offset:37184\n"
      "v_mfma_f32_16x16x32_bf16 v[28:31], a[16:19], a[136:139], v[28:31]\n"
      "buffer_store_dwordx4 v[80:83], v14, s[36:39], s52 offen sc1\n"
      "v_cvt_pk_bf16_f32 v89, v74, v75\n"
      "v_mfma_f32_16x16x32_bf16 v[28:31], a[20:23], a[148:151], v[28:31]\n"
      "s_mov_b32 m0, s45\n"
      "ds_read2_b64 a[60:63], v12 offset0:1 offset1:65\n"
      "v_cvt_pk_bf16_f32 v60, v60, v61\n"
      "v_mfma_f32_16x16x32_bf16 v[56:59], a[52:55], a[76:79], 0\n"
      "v_cvt_pk_bf16_f32 v61, v62, v63\n"
      "v_swap_b32 v6, v7\n"
      "v_mfma_f32_16x16x32_bf16 v[20:23], a[24:27], a[164:167], 0\n"
      "v_mfma_f32_16x16x32_bf16 v[20:23], a[28:31], a[176:179], v[20:23]\n"
      "v_cvt_pk_bf16_f32 v28, v28, v29\n"
      "v_cvt_pk_bf16_f32 v29, v30, v31\n"
      "v_mfma_f32_16x16x32_bf16 v[20:23], a[32:35], a[188:191], v[20:23]\n"
      "buffer_store_dwordx4 v[86:89], v14, s[36:39], s52 offen offset:1536 sc1\n"
      "v_fma_f32 v64, s44, v52, -v56\n"
      "v_mfma_f32_16x16x32_bf16 v[20:23], a[0:3], a[92:95], v[20:23]\n"
      "ds_write_addtid_b32 v28 offset:60432\n"
      "ds_write_addtid_b32 v29 offset:60688\n"
      "v_mfma_f32_16x16x32_bf16 v[20:23], a[4:7], a[104:107], v[20:23]\n"
      "s_mov_b32 m0, s18\n"
      "v_fma_f32 v65, s44, v53, -v57\n"
      "v_cvt_pk_bf16_f32 v64, v64, v65\n"
      "v_mfma_f32_16x16x32_bf16 v[20:23], a[8:11], a[116:119], v[20:23]\n"
      "ds_write_addtid_b32 v64 offset:2048\n"
      "v_fma_f32 v66, s44, v54, -v58\n"
      "v_mfma_f32_16x16x32_bf16 v[20:23], a[12:15], a[128:131], v[20:23]\n"
      "v_fma_f32 v67, s44, v55, -v59\n"
      "v_cvt_pk_bf16_f32 v65, v66, v67\n"
      "v_mfma_f32_16x16x32_bf16 v[20:23], a[16:19], a[140:143], v[20:23]\n"
      "ds_write_addtid_b32 v65 offset:38976\n"
      "v_fmac_f32_e32 v56, s44, v52\n"
      "v_mfma_f32_16x16x32_bf16 v[20:23], a[20:23], a[152:155], v[20:23]\n"
      "v_fmac_f32_e64 v57, s44, v53\n"
      "v_cvt_pk_bf16_f32 v56, v56, v57\n"
      "v_mfma_f32_16x16x32_bf16 v[76:79], a[84:87], a[68:71], 0\n"
      "ds_write_addtid_b32 v56 offset:1280\n"
      "v_fmac_f32_e64 v58, s44, v54\n"
      "v_mfma_f32_16x16x32_bf16 v[32:35], a[24:27], a[168:171], 0\n"
      "ds_write_addtid_b32 v60 offset:512\n"
      "v_fmac_f32_e32 v59, s44, v55\n"
      "v_mfma_f32_16x16x32_bf16 v[32:35], a[28:31], a[180:183], v[32:35]\n"
      "ds_write_addtid_b32 v61 offset:37440\n"
      "v_cvt_pk_bf16_f32 v57, v58, v59\n"
      "v_mfma_f32_16x16x32_bf16 v[32:35], a[32:35], a[192:195], v[32:35]\n"
      "ds_write_addtid_b32 v57 offset:38208\n"
      "v_cvt_pk_bf16_f32 v20, v20, v21\n"
      "v_mfma_f32_16x16x32_bf16 v[32:35], a[0:3], a[96:99], v[32:35]\n"
      "s_mov_b32 m0, s45\n"
      "v_cvt_pk_bf16_f32 v21, v22, v23\n"
      "v_cvt_pk_bf16_f32 v84, v76, v77\n"
      "v_mfma_f32_16x16x32_bf16 v[32:35], a[4:7], a[108:111], v[32:35]\n"
      "s_mov_b32 m0, s59\n"
      "v_cvt_pk_bf16_f32 v90, v78, v79\n"
      "v_mfma_f32_16x16x32_bf16 v[32:35], a[8:11], a[120:123], v[32:35]\n"
      "buffer_load_dwordx4 v19, s[32:35], s96 offen lds\n"
      "v_mfma_f32_16x16x32_bf16 v[32:35], a[12:15], a[132:135], v[32:35]\n"
      "s_mov_b32 m0, s45\n"
      "v_mfma_f32_16x16x32_bf16 v[32:35], a[16:19], a[144:147], v[32:35]\n"
      "ds_write_addtid_b32 v20 offset:48136\n"
      "ds_write_addtid_b32 v21 offset:48392\n"
      "v_mfma_f32_16x16x32_bf16 v[32:35], a[20:23], a[156:159], v[32:35]\n"
      "s_nop 1\n"
      "v_mfma_f32_16x16x32_bf16 v[20:23], a[12:15], a[88:91], 0\n"
      "s_add_u32 s51, s51, s12\n"
      "s_nop 1\n"
      "v_mfma_f32_16x16x32_bf16 v[20:23], a[16:19], a[100:103], v[20:23]\n"
      "s_sub_u32 s47, s51, s48\n"
      "v_cvt_pk_bf16_f32 v32, v32, v33\n"
      "v_cvt_pk_bf16_f32 v33, v34, v35\n"
      "v_mfma_f32_16x16x32_bf16 v[20:23], a[20:23], a[112:115], v[20:23]\n"
      "s_cmp_ge_u32 vcc_lo, 0x40000000\n"
      "ds_write_addtid_b32 v32 offset:35840\n"
      "v_mfma_f32_16x16x32_bf16 v[20:23], a[24:27], a[124:127], v[20:23]\n"
      "s_cselect_b32 s53, s47, s51\n"
      "ds_write_addtid_b32 v33 offset:36096\n"
      "v_mfma_f32_16x16x32_bf16 v[20:23], a[28:31], a[136:139], v[20:23]\n"
      "s_mul_i32 vcc_lo, vcc_lo, 2\n"
      "v_mfma_f32_16x16x32_bf16 v[20:23], a[32:35], a[148:151], v[20:23]\n"
      "v_mfma_f32_16x16x32_bf16 v[24:27], a[12:15], a[92:95], 0\n"
      "s_waitcnt vmcnt(19) lgkmcnt(0)\n"
      "s_barrier\n"
      "v_mfma_f32_16x16x32_bf16 v[24:27], a[16:19], a[104:107], v[24:27]\n"
      "s_add_u32 m0, s58, 13280\n"
      "ds_read_b128 a[0:3], v7 offset:0\n"
      "v_mfma_f32_16x16x32_bf16 v[24:27], a[20:23], a[116:119], v[24:27]\n"
      "ds_read_b128 a[4:7], v7 offset:256\n"
      "v_mfma_f32_16x16x32_bf16 v[24:27], a[24:27], a[128:131], v[24:27]\n"
      "ds_read_b128 a[8:11], v7 offset:512\n"
      "v_mfma_f32_16x16x32_bf16 v[24:27], a[28:31], a[140:143], v[24:27]\n"
      "ds_read_b128 a[36:39], v2 offset:0\n"
      "v_mfma_f32_16x16x32_bf16 v[24:27], a[32:35], a[152:155], v[24:27]\n"
      "ds_read_b128 a[40:43], v3 offset:0\n"
      "v_mfma_f32_16x16x32_bf16 v[68:71], a[84:87], a[60:63], 0\n"
      "ds_read_b128 a[44:47], v4 offset:0\n"
      "s_nop 0\n"
      "v_mfma_f32_16x16x32_bf16 v[28:31], a[12:15], a[96:99], 0\n"
      "ds_read2_b64 a[64:67], v11 offset0:0 offset1:64\n"
      "s_nop 0\n"
      "v_mfma_f32_16x16x32_bf16 v[28:31], a[16:19], a[108:111], v[28:31]\n"
      "buffer_load_dwordx4 v16, s[32:35], s53 offen lds\n"
      "v_mfma_f32_16x16x32_bf16 v[28:31], a[20:23], a[120:123], v[28:31]\n"
      "s_mov_b32 m0, s45\n"
      "ds_read2_b64 a[68:71], v11 offset0:1 offset1:65\n"
      "v_cvt_pk_bf16_f32 v85, v68, v69\n"
      "v_mfma_f32_16x16x32_bf16 v[28:31], a[24:27], a[132:135], v[28:31]\n"
      "ds_read_b128 a[12:15], v6 offset:768\n"
      "v_cvt_pk_bf16_f32 v91, v70, v71\n"
      "v_mfma_f32_16x16x32_bf16 v[28:31], a[28:31], a[144:147], v[28:31]\n"
      "ds_read_b128 a[16:19], v6 offset:1024\n"
      "v_mfma_f32_16x16x32_bf16 v[28:31], a[32:35], a[156:159], v[28:31]\n"
      "s_waitcnt lgkmcnt(6)\n"
      "ds_read_b128 a[20:23], v6 offset:1280\n"
      "v_mfma_f32_16x16x32_bf16 v[20:23], a[0:3], a[160:163], v[20:23]\n"
      "buffer_store_dwordx2 v[84:85], v15, s[36:39], s52 offen sc1\n"
      "v_mfma_f32_16x16x32_bf16 v[20:23], a[4:7], a[172:175], v[20:23]\n"
      "ds_read_b128 a[24:27], v6 offset:5376\n"
      "v_mfma_f32_16x16x32_bf16 v[20:23], a[8:11], a[184:187], v[20:23]\n"
      "ds_read_b128 a[28:31], v6 offset:5632\n"
      "s_nop 0\n"
      "v_mfma_f32_16x16x32_bf16 v[36:39], a[36:39], a[72:75], 0\n"
      "s_waitcnt lgkmcnt(6)\n"
      "ds_read_b128 a[32:35], v6 offset:5888\n"
      "s_nop 0\n"
      "v_mfma_f32_16x16x32_bf16 v[40:43], a[40:43], a[76:79], 0\n"
      "ds_read_b128 a[48:51], v2 offset:64\n"
      "v_cvt_pk_bf16_f32 v20, v20, v21\n"
      "v_mfma_f32_16x16x32_bf16 v[44:47], a[44:47], a[80:83], v[36:39]\n"
      "buffer_store_dwordx2 v[90:91], v15, s[36:39], s52 offen offset:1536 sc1\n"
      "v_cvt_pk_bf16_f32 v21, v22, v23\n"
      "v_mfma_f32_16x16x32_bf16 v[24:27], a[0:3], a[164:167], v[24:27]\n"
      "s_mov_b32 s52, s13\n"
      "ds_write_addtid_b32 v20 offset:57360\n"
      "v_fma_f32 v48, s44, v36, -v40\n"
      "v_mfma_f32_16x16x32_bf16 v[24:27], a[4:7], a[176:179], v[24:27]\n"
      "s_add_u32 s13, s13, s12\n"
      "ds_read_b128 a[52:55], v3 offset:64\n"
      "v_fma_f32 v49, s44, v37, -v41\n"
      "v_mfma_f32_16x16x32_bf16 v[24:27], a[8:11], a[188:191], v[24:27]\n"
      "ds_write_addtid_b32 v21 offset:57616\n"
      "v_cvt_pk_bf16_f32 v48, v48, v49\n"
      "v_mfma_f32_16x16x32_bf16 v[72:75], a[84:87], a[64:67], 0\n"
      "ds_read_b128 a[56:59], v4 offset:64\n"
      "v_fma_f32 v50, s44, v38, -v42\n"
      "v_mfma_f32_16x16x32_bf16 v[28:31], a[0:3], a[168:171], v[28:31]\n"
      "ds_read_b128 a[0:3], v7 offset:768\n"
      "v_fma_f32 v51, s44, v39, -v43\n"
      "v_mfma_f32_16x16x32_bf16 v[28:31], a[4:7], a[180:183], v[28:31]\n"
      "ds_read_b128 a[4:7], v7 offset:1024\n"
      "v_cvt_pk_bf16_f32 v24, v24, v25\n"
      "v_mfma_f32_16x16x32_bf16 v[28:31], a[8:11], a[192:195], v[28:31]\n"
      "s_waitcnt lgkmcnt(6)\n"
      "ds_read_b128 a[8:11], v7 offset:1280\n"
      "v_cvt_pk_bf16_f32 v25, v26, v27\n"
      "v_mfma_f32_16x16x32_bf16 v[76:79], a[84:87], a[68:71], 0\n"
      "ds_read2_b64 a[60:63], v11 offset0:2 offset1:66\n"
      "v_cvt_pk_bf16_f32 v49, v50, v51\n"
      "v_mfma_f32_16x16x32_bf16 v[32:35], a[12:15], v[92:95], 0\n"
      "ds_write_addtid_b32 v24 offset:45064\n"
      "ds_write_addtid_b32 v25 offset:45320\n"
      "v_mfma_f32_16x16x32_bf16 v[32:35], a[16:19], v[148:151], v[32:35]\n"
      "s_mov_b32 m0, s18\n"
      "v_fmac_f32_e32 v40, s44, v36\n"
      "v_fmac_f32_e64 v41, s44, v37\n"
      "v_mfma_f32_16x16x32_bf16 v[32:35], a[20:23], v[204:207], v[32:35]\n"
      "ds_write_addtid_b32 v48 offset:6144\n"
      "v_fmac_f32_e64 v42, s44, v38\n"
      "v_mfma_f32_16x16x32_bf16 v[32:35], a[24:27], v[112:115], v[32:35]\n"
      "ds_write_addtid_b32 v49 offset:43072\n"
      "v_fmac_f32_e64 v43, s44, v39\n"
      "v_mfma_f32_16x16x32_bf16 v[32:35], a[28:31], a[208:211], v[32:35]\n"
      "v_cvt_pk_bf16_f32 v40, v40, v41\n"
      "v_cvt_pk_bf16_f32 v41, v42, v43\n"
      "v_mfma_f32_16x16x32_bf16 v[32:35], a[32:35], v[220:223], v[32:35]\n"
      "s_waitcnt lgkmcnt(6)\n"
      "ds_write_addtid_b32 v40 offset:5376\n"
      "v_cvt_pk_bf16_f32 v44, v44, v45\n"
      "v_mfma_f32_16x16x32_bf16 v[32:35], a[0:3], a[212:215], v[32:35]\n"
      "ds_write_addtid_b32 v41 offset:42304\n"
      "v_cvt_pk_bf16_f32 v45, v46, v47\n"
      "v_mfma_f32_16x16x32_bf16 v[32:35], a[4:7], v[184:187], v[32:35]\n"
      "s_waitcnt lgkmcnt(6)\n"
      "ds_write_addtid_b32 v44 offset:4608\n"
      "ds_write_addtid_b32 v45 offset:41536\n"
      "v_mfma_f32_16x16x32_bf16 v[32:35], a[8:11], v[240:243], v[32:35]\n"
      "s_mov_b32 m0, s45\n"
      "ds_read_b128 a[36:39], v2 offset:128\n"
      "v_cvt_pk_bf16_f32 v28, v28, v29\n"
      "v_mfma_f32_16x16x32_bf16 v[20:23], a[12:15], v[100:103], 0\n"
      "ds_write_addtid_b32 v28 offset:32768\n"
      "v_cvt_pk_bf16_f32 v29, v30, v31\n"
      "v_mfma_f32_16x16x32_bf16 v[20:23], a[16:19], a[200:203], v[20:23]\n"
      "ds_write_addtid_b32 v29 offset:33024\n"
      "v_cvt_pk_bf16_f32 v32, v32, v33\n"
      "v_mfma_f32_16x16x32_bf16 v[20:23], a[20:23], v[208:211], v[20:23]\n"
      "s_add_u32 m0, s58, 13280\n"
      "ds_read_b128 a[44:47], v4 offset:128\n"
      "v_cvt_pk_bf16_f32 v33, v34, v35\n"
      "v_mfma_f32_16x16x32_bf16 v[20:23], a[24:27], a[204:207], v[20:23]\n"
      "buffer_load_dwordx4 v17, s[32:35], s53 offen offset:1024 lds\n"
      "v_cvt_pk_bf16_f32 v80, v72, v73\n"
      "v_mfma_f32_16x16x32_bf16 v[20:23], a[28:31], v[172:175], v[20:23]\n"
      "s_mov_b32 m0, s45\n"
      "ds_read2_b64 a[64:67], v11 offset0:3 offset1:67\n"
      "v_cvt_pk_bf16_f32 v81, v76, v77\n"
      "v_mfma_f32_16x16x32_bf16 v[20:23], a[32:35], v[228:231], v[20:23]\n"
      "ds_write_addtid_b32 v32 offset:58384\n"
      "v_cvt_pk_bf16_f32 v86, v74, v75\n"
      "v_mfma_f32_16x16x32_bf16 v[20:23], a[0:3], v[136:139], v[20:23]\n"
      "ds_write_addtid_b32 v33 offset:58640\n"
      "v_cvt_pk_bf16_f32 v87, v78, v79\n"
      "v_mfma_f32_16x16x32_bf16 v[20:23], a[4:7], v[192:195], v[20:23]\n"
      "ds_read_b128 a[40:43], v3 offset:128\n"
      "v_mfma_f32_16x16x32_bf16 v[20:23], a[8:11], v[244:247], v[20:23]\n"
      "global_load_dword v8, v9, s[54:55]\n"
      "s_nop 0\n"
      "v_mfma_f32_16x16x32_bf16 v[52:55], a[48:51], a[72:75], 0\n"
      "ds_read2_b64 a[68:71], v13 offset0:0 offset1:64\n"
      "s_nop 0\n"
      "v_mfma_f32_16x16x32_bf16 v[56:59], a[52:55], a[76:79], 0\n"
      "s_nop 0\n"
      "v_mfma_f32_16x16x32_bf16 v[60:63], a[56:59], a[80:83], v[52:55]\n"
      "v_cvt_pk_bf16_f32 v20, v20, v21\n"
      "v_cvt_pk_bf16_f32 v21, v22, v23\n"
      "v_mfma_f32_16x16x32_bf16 v[68:71], a[84:87], a[60:63], 0\n"
      "ds_write_addtid_b32 v20 offset:46088\n"
      "s_nop 0\n"
      "v_mfma_f32_16x16x32_bf16 v[24:27], a[12:15], a[196:199], 0\n"
      "ds_read_b128 a[12:15], v6 offset:1536\n"
      "v_fma_f32 v64, s44, v52, -v56\n"
      "v_mfma_f32_16x16x32_bf16 v[24:27], a[16:19], v[160:163], v[24:27]\n"
      "ds_read_b128 a[16:19], v6 offset:1792\n"
      "v_fma_f32 v65, s44, v53, -v57\n"
      "v_mfma_f32_16x16x32_bf16 v[24:27], a[20:23], v[216:219], v[24:27]\n"
      "ds_read_b128 a[20:23], v6 offset:2048\n"
      "v_cvt_pk_bf16_f32 v64, v64, v65\n"
      "v_mfma_f32_16x16x32_bf16 v[24:27], a[24:27], v[124:127], v[24:27]\n"
      "ds_read_b128 a[24:27], v6 offset:6144\n"
      "v_fma_f32 v66, s44, v54, -v58\n"
      "v_mfma_f32_16x16x32_bf16 v[24:27], a[28:31], v[180:183], v[24:27]\n"
      "ds_read_b128 a[28:31], v6 offset:6400\n"
      "v_fma_f32 v67, s44, v55, -v59\n"
      "v_mfma_f32_16x16x32_bf16 v[24:27], a[32:35], v[232:235], v[24:27]\n"
      "ds_read_b128 a[32:35], v6 offset:6656\n"
      "v_cvt_pk_bf16_f32 v65, v66, v67\n"
      "v_mfma_f32_16x16x32_bf16 v[24:27], a[0:3], a[216:219], v[24:27]\n"
      "ds_read_b128 a[0:3], v7 offset:1536\n"
      "v_fmac_f32_e64 v56, s44, v52\n"
      "v_mfma_f32_16x16x32_bf16 v[24:27], a[4:7], v[196:199], v[24:27]\n"
      "ds_read_b128 a[4:7], v7 offset:1792\n"
      "v_fmac_f32_e32 v57, s44, v53\n"
      "v_mfma_f32_16x16x32_bf16 v[24:27], a[8:11], v[252:255], v[24:27]\n"
      "s_waitcnt lgkmcnt(7)\n"
      "ds_read_b128 a[8:11], v7 offset:2048\n"
      "v_cvt_pk_bf16_f32 v56, v56, v57\n"
      "v_mfma_f32_16x16x32_bf16 v[36:39], a[36:39], a[72:75], 0\n"
      "ds_write_addtid_b32 v21 offset:46344\n"
      "v_fmac_f32_e64 v58, s44, v54\n"
      "v_mfma_f32_16x16x32_bf16 v[44:47], a[44:47], a[80:83], v[36:39]\n"
      "s_add_u32 m0, s58, 13280\n"
      "v_cvt_pk_bf16_f32 v24, v24, v25\n"
      "v_cvt_pk_bf16_f32 v25, v26, v27\n"
      "v_mfma_f32_16x16x32_bf16 v[72:75], a[84:87], a[64:67], 0\n"
      "buffer_load_dwordx4 v18, s[32:35], s53 offen offset:2048 lds\n"
      "v_fmac_f32_e64 v59, s44, v55\n"
      "v_mfma_f32_16x16x32_bf16 v[28:31], a[12:15], v[96:99], 0\n"
      "s_waitcnt lgkmcnt(5)\n"
      "v_cvt_pk_bf16_f32 v57, v58, v59\n"
      "v_cvt_pk_bf16_f32 v60, v60, v61\n"
      "v_mfma_f32_16x16x32_bf16 v[28:31], a[16:19], v[152:155], v[28:31]\n"
      "s_mov_b32 m0, s45\n"
      "v_cvt_pk_bf16_f32 v82, v68, v69\n"
      "v_cvt_pk_bf16_f32 v83, v72, v73\n"
      "v_mfma_f32_16x16x32_bf16 v[28:31], a[20:23], a[220:223], v[28:31]\n"
      "ds_write_addtid_b32 v24 offset:33792\n"
      "ds_write_addtid_b32 v25 offset:34048\n"
      "v_mfma_f32_16x16x32_bf16 v[28:31], a[24:27], v[116:119], v[28:31]\n"
      "s_mov_b32 m0, s18\n"
      "v_cvt_pk_bf16_f32 v61, v62, v63\n"
      "v_cvt_pk_bf16_f32 v88, v70, v71\n"
      "v_mfma_f32_16x16x32_bf16 v[28:31], a[28:31], v[168:171], v[28:31]\n"
      "s_waitcnt lgkmcnt(3)\n"
      "ds_write_addtid_b32 v64 offset:6400\n"
      "ds_write_addtid_b32 v65 offset:43328\n"
      "v_mfma_f32_16x16x32_bf16 v[28:31], a[32:35], v[224:227], v[28:31]\n"
      "ds_write_addtid_b32 v56 offset:5632\n"
      "ds_write_addtid_b32 v57 offset:42560\n"
      "v_mfma_f32_16x16x32_bf16 v[28:31], a[0:3], v[132:135], v[28:31]\n"
      "ds_write_addtid_b32 v60 offset:4864\n"
      "ds_write_addtid_b32 v61 offset:41792\n"
      "v_mfma_f32_16x16x32_bf16 v[28:31], a[4:7], v[188:191], v[28:31]\n"
      "buffer_store_dwordx4 v[80:83], v14, s[36:39], s52 offen sc1\n"
      "v_cvt_pk_bf16_f32 v89, v74, v75\n"
      "v_mfma_f32_16x16x32_bf16 v[28:31], a[8:11], a[240:243], v[28:31]\n"
      "s_mov_b32 m0, s45\n"
      "ds_read2_b64 a[60:63], v13 offset0:1 offset1:65\n"
      "v_cvt_pk_bf16_f32 v44, v44, v45\n"
      "v_mfma_f32_16x16x32_bf16 v[40:43], a[40:43], a[76:79], 0\n"
      "v_cvt_pk_bf16_f32 v45, v46, v47\n"
      "s_nop 0\n"
      "v_mfma_f32_16x16x32_bf16 v[20:23], a[12:15], v[104:107], 0\n"
      "v_mfma_f32_16x16x32_bf16 v[20:23], a[16:19], v[156:159], v[20:23]\n"
      "v_cvt_pk_bf16_f32 v28, v28, v29\n"
      "v_cvt_pk_bf16_f32 v29, v30, v31\n"
      "v_mfma_f32_16x16x32_bf16 v[20:23], a[20:23], v[212:215], v[20:23]\n"
      "buffer_store_dwordx4 v[86:89], v14, s[36:39], s52 offen offset:1536 sc1\n"
      "v_fma_f32 v48, s44, v36, -v40\n"
      "v_mfma_f32_16x16x32_bf16 v[20:23], a[24:27], v[120:123], v[20:23]\n"
      "ds_write_addtid_b32 v28 offset:59408\n"
      "ds_write_addtid_b32 v29 offset:59664\n"
      "v_mfma_f32_16x16x32_bf16 v[20:23], a[28:31], v[176:179], v[20:23]\n"
      "s_mov_b32 m0, s18\n"
      "v_fma_f32 v49, s44, v37, -v41\n"
      "v_cvt_pk_bf16_f32 v48, v48, v49\n"
      "v_mfma_f32_16x16x32_bf16 v[20:23], a[32:35], a[232:235], v[20:23]\n"
      "s_sub_u32 s18, s19, s18\n"
      "ds_write_addtid_b32 v48 offset:6656\n"
      "v_fma_f32 v50, s44, v38, -v42\n"
      "v_mfma_f32_16x16x32_bf16 v[20:23], a[0:3], v[140:143], v[20:23]\n"
      "v_fma_f32 v51, s44, v39, -v43\n"
      "v_cvt_pk_bf16_f32 v49, v50, v51\n"
      "v_mfma_f32_16x16x32_bf16 v[20:23], a[4:7], a[236:239], v[20:23]\n"
      "ds_write_addtid_b32 v49 offset:43584\n"
      "v_fmac_f32_e64 v40, s44, v36\n"
      "v_mfma_f32_16x16x32_bf16 v[20:23], a[8:11], v[248:251], v[20:23]\n"
      "v_fmac_f32_e64 v41, s44, v37\n"
      "v_cvt_pk_bf16_f32 v40, v40, v41\n"
      "v_mfma_f32_16x16x32_bf16 v[76:79], a[84:87], a[68:71], 0\n"
      "ds_write_addtid_b32 v40 offset:5888\n"
      "v_fmac_f32_e64 v42, s44, v38\n"
      "v_mfma_f32_16x16x32_bf16 v[32:35], a[12:15], v[108:111], 0\n"
      "ds_write_addtid_b32 v44 offset:5120\n"
      "v_fmac_f32_e32 v43, s44, v39\n"
      "v_mfma_f32_16x16x32_bf16 v[32:35], a[16:19], v[164:167], v[32:35]\n"
      "ds_write_addtid_b32 v45 offset:42048\n"
      "v_cvt_pk_bf16_f32 v41, v42, v43\n"
      "v_mfma_f32_16x16x32_bf16 v[32:35], a[20:23], a[224:227], v[32:35]\n"
      "ds_write_addtid_b32 v41 offset:42816\n"
      "v_cvt_pk_bf16_f32 v20, v20, v21\n"
      "v_mfma_f32_16x16x32_bf16 v[32:35], a[24:27], v[128:131], v[32:35]\n"
      "s_mov_b32 m0, s45\n"
      "v_cvt_pk_bf16_f32 v21, v22, v23\n"
      "v_cvt_pk_bf16_f32 v84, v76, v77\n"
      "v_mfma_f32_16x16x32_bf16 v[32:35], a[28:31], a[228:231], v[32:35]\n"
      "ds_write_addtid_b32 v20 offset:47112\n"
      "v_cvt_pk_bf16_f32 v90, v78, v79\n"
      "v_mfma_f32_16x16x32_bf16 v[32:35], a[32:35], v[236:239], v[32:35]\n"
      "ds_write_addtid_b32 v21 offset:47368\n"
      "v_mfma_f32_16x16x32_bf16 v[32:35], a[0:3], v[144:147], v[32:35]\n"
      "v_mfma_f32_16x16x32_bf16 v[32:35], a[4:7], v[200:203], v[32:35]\n"
      "v_mfma_f32_16x16x32_bf16 v[32:35], a[8:11], a[244:247], v[32:35]\n"
      "s_nop 1\n"
      "s_cbranch_vccnz Lconv_loop0\n"
      "Lconv_t0:\n"
      "s_nop 1\n"
      "v_mfma_f32_16x16x32_bf16 v[20:23], a[24:27], v[96:99], 0\n"
      "s_nop 1\n"
      "v_mfma_f32_16x16x32_bf16 v[20:23], a[28:31], v[152:155], v[20:23]\n"
      "v_cvt_pk_bf16_f32 v32, v32, v33\n"
      "v_cvt_pk_bf16_f32 v33, v34, v35\n"
      "v_mfma_f32_16x16x32_bf16 v[20:23], a[32:35], a[220:223], v[20:23]\n"
      "ds_write_addtid_b32 v32 offset:34816\n"
      "v_mfma_f32_16x16x32_bf16 v[20:23], a[0:3], v[116:119], v[20:23]\n"
      "ds_write_addtid_b32 v33 offset:35072\n"
      "v_mfma_f32_16x16x32_bf16 v[20:23], a[4:7], v[168:171], v[20:23]\n"
      "v_mfma_f32_16x16x32_bf16 v[20:23], a[8:11], v[224:227], v[20:23]\n"
      "v_mfma_f32_16x16x32_bf16 v[24:27], a[24:27], v[104:107], 0\n"
      "s_waitcnt lgkmcnt(0)\n"
      "s_barrier\n"
      "v_mfma_f32_16x16x32_bf16 v[24:27], a[28:31], v[156:159], v[24:27]\n"
      "ds_read2_b64 a[64:67], v10 offset0:0 offset1:64\n"
      "v_mfma_f32_16x16x32_bf16 v[24:27], a[32:35], v[212:215], v[24:27]\n"
      "ds_read_b128 a[12:15], v7 offset:6144\n"
      "v_mfma_f32_16x16x32_bf16 v[24:27], a[0:3], v[120:123], v[24:27]\n"
      "ds_read2_b64 a[68:71], v10 offset0:1 offset1:65\n"
      "v_mfma_f32_16x16x32_bf16 v[24:27], a[4:7], v[176:179], v[24:27]\n"
      "ds_read_b128 a[16:19], v7 offset:6400\n"
      "v_mfma_f32_16x16x32_bf16 v[24:27], a[8:11], a[232:235], v[24:27]\n"
      "ds_read_b128 a[20:23], v7 offset:6656\n"
      "v_mfma_f32_16x16x32_bf16 v[68:71], a[84:87], a[60:63], 0\n"
      "ds_read2_b64 a[60:63], v10 offset0:2 offset1:66\n"
      "s_nop 0\n"
      "v_mfma_f32_16x16x32_bf16 v[28:31], a[24:27], v[108:111], 0\n"
      "s_waitcnt lgkmcnt(5)\n"
      "ds_read_b128 a[24:27], v6 offset:5376\n"
      "s_nop 0\n"
      "v_mfma_f32_16x16x32_bf16 v[72:75], a[84:87], a[64:67], 0\n"
      "ds_read2_b64 a[64:67], v10 offset0:3 offset1:67\n"
      "v_cvt_pk_bf16_f32 v85, v68, v69\n"
      "v_mfma_f32_16x16x32_bf16 v[28:31], a[28:31], v[164:167], v[28:31]\n"
      "ds_read_b128 a[28:31], v6 offset:5632\n"
      "v_cvt_pk_bf16_f32 v91, v70, v71\n"
      "v_mfma_f32_16x16x32_bf16 v[28:31], a[32:35], a[224:227], v[28:31]\n"
      "buffer_store_dwordx2 v[84:85], v15, s[36:39], s52 offen sc1\n"
      "v_mfma_f32_16x16x32_bf16 v[28:31], a[0:3], v[128:131], v[28:31]\n"
      "ds_read_b128 a[32:35], v6 offset:5888\n"
      "v_cvt_pk_bf16_f32 v80, v72, v73\n"
      "v_mfma_f32_16x16x32_bf16 v[28:31], a[4:7], a[228:231], v[28:31]\n"
      "ds_read_b128 a[0:3], v7 offset:768\n"
      "v_cvt_pk_bf16_f32 v86, v74, v75\n"
      "v_mfma_f32_16x16x32_bf16 v[28:31], a[8:11], v[236:239], v[28:31]\n"
      "s_waitcnt lgkmcnt(6)\n"
      "ds_read_b128 a[4:7], v7 offset:1024\n"
      "v_mfma_f32_16x16x32_bf16 v[20:23], a[12:15], v[132:135], v[20:23]\n"
      "ds_read_b128 a[8:11], v7 offset:1280\n"
      "v_mfma_f32_16x16x32_bf16 v[76:79], a[84:87], a[68:71], 0\n"
      "s_waitcnt lgkmcnt(6)\n"
      "ds_read2_b64 a[68:71], v12 offset0:0 offset1:64\n"
      "s_nop 0\n"
      "v_mfma_f32_16x16x32_bf16 v[68:71], a[84:87], a[60:63], 0\n"
      "ds_read2_b64 a[60:63], v12 offset0:1 offset1:65\n"
      "s_nop 0\n"
      "v_mfma_f32_16x16x32_bf16 v[20:23], a[16:19], v[188:191], v[20:23]\n"
      "buffer_store_dwordx2 v[90:91], v15, s[36:39], s52 offen offset:1536 sc1\n"
      "v_cvt_pk_bf16_f32 v81, v76, v77\n"
      "v_mfma_f32_16x16x32_bf16 v[20:23], a[20:23], a[240:243], v[20:23]\n"
      "s_mov_b32 s52, s13\n"
      "v_cvt_pk_bf16_f32 v87, v78, v79\n"
      "v_cvt_pk_bf16_f32 v82, v68, v69\n"
      "v_mfma_f32_16x16x32_bf16 v[24:27], a[12:15], v[140:143], v[24:27]\n"
      "s_waitcnt lgkmcnt(5)\n"
      "v_cvt_pk_bf16_f32 v88, v70, v71\n"
      "s_nop 0\n"
      "v_mfma_f32_16x16x32_bf16 v[72:75], a[84:87], a[64:67], 0\n"
      "s_add_u32 s13, s13, s12\n"
      "v_cvt_pk_bf16_f32 v20, v20, v21\n"
      "v_cvt_pk_bf16_f32 v21, v22, v23\n"
      "v_mfma_f32_16x16x32_bf16 v[24:27], a[16:19], a[236:239], v[24:27]\n"
      "ds_write_addtid_b32 v20 offset:62480\n"
      "s_nop 0\n"
      "v_mfma_f32_16x16x32_bf16 v[24:27], a[20:23], v[248:251], v[24:27]\n"
      "ds_write_addtid_b32 v21 offset:62736\n"
      "v_cvt_pk_bf16_f32 v83, v72, v73\n"
      "v_mfma_f32_16x16x32_bf16 v[28:31], a[12:15], v[144:147], v[28:31]\n"
      "s_waitcnt lgkmcnt(3)\n"
      "ds_read_b128 a[12:15], v7 offset:5376\n"
      "v_cvt_pk_bf16_f32 v89, v74, v75\n"
      "v_mfma_f32_16x16x32_bf16 v[76:79], a[84:87], a[68:71], 0\n"
      "buffer_store_dwordx4 v[80:83], v14, s[36:39], s52 offen sc1\n"
      "v_cvt_pk_bf16_f32 v24, v24, v25\n"
      "v_mfma_f32_16x16x32_bf16 v[28:31], a[16:19], v[200:203], v[28:31]\n"
      "ds_read_b128 a[16:19], v7 offset:5632\n"
      "v_cvt_pk_bf16_f32 v25, v26, v27\n"
      "v_mfma_f32_16x16x32_bf16 v[28:31], a[20:23], a[244:247], v[28:31]\n"
      "ds_read_b128 a[20:23], v7 offset:5888\n"
      "v_mfma_f32_16x16x32_bf16 v[32:35], a[24:27], v[92:95], 0\n"
      "s_waitcnt lgkmcnt(5)\n"
      "ds_write_addtid_b32 v24 offset:50184\n"
      "v_cvt_pk_bf16_f32 v84, v76, v77\n"
      "v_mfma_f32_16x16x32_bf16 v[68:71], a[84:87], a[60:63], 0\n"
      "ds_write_addtid_b32 v25 offset:50440\n"
      "v_cvt_pk_bf16_f32 v90, v78, v79\n"
      "v_mfma_f32_16x16x32_bf16 v[32:35], a[28:31], v[148:151], v[32:35]\n"
      "buffer_store_dwordx4 v[86:89], v14, s[36:39], s52 offen offset:1536 sc1\n"
      "v_cvt_pk_bf16_f32 v28, v28, v29\n"
      "v_mfma_f32_16x16x32_bf16 v[32:35], a[32:35], v[204:207], v[32:35]\n"
      "ds_write_addtid_b32 v28 offset:37888\n"
      "v_cvt_pk_bf16_f32 v29, v30, v31\n"
      "v_mfma_f32_16x16x32_bf16 v[32:35], a[0:3], v[112:115], v[32:35]\n"
      "v_cvt_pk_bf16_f32 v85, v68, v69\n"
      "v_cvt_pk_bf16_f32 v91, v70, v71\n"
      "v_mfma_f32_16x16x32_bf16 v[32:35], a[4:7], a[208:211], v[32:35]\n"
      "ds_write_addtid_b32 v29 offset:38144\n"
      "v_mfma_f32_16x16x32_bf16 v[32:35], a[8:11], v[220:223], v[32:35]\n"
      "s_waitcnt lgkmcnt(5)\n"
      "v_mfma_f32_16x16x32_bf16 v[32:35], a[12:15], a[212:215], v[32:35]\n"
      "buffer_store_dwordx2 v[84:85], v15, s[36:39], s52 offen sc1\n"
      "v_mfma_f32_16x16x32_bf16 v[32:35], a[16:19], v[184:187], v[32:35]\n"
      "s_waitcnt lgkmcnt(4)\n"
      "v_mfma_f32_16x16x32_bf16 v[32:35], a[20:23], v[240:243], v[32:35]\n"
      "s_nop 1\n"
      "v_mfma_f32_16x16x32_bf16 v[20:23], a[24:27], v[100:103], 0\n"
      "s_nop 1\n"
      "v_mfma_f32_16x16x32_bf16 v[20:23], a[28:31], a[200:203], v[20:23]\n"
      "s_nop 0\n"
      "v_mfma_f32_16x16x32_bf16 v[20:23], a[32:35], v[208:211], v[20:23]\n"
      "buffer_store_dwordx2 v[90:91], v15, s[36:39], s52 offen offset:1536 sc1\n"
      "v_cvt_pk_bf16_f32 v32, v32, v33\n"
      "v_mfma_f32_16x16x32_bf16 v[20:23], a[0:3], a[204:207], v[20:23]\n"
      "ds_write_addtid_b32 v32 offset:61456\n"
      "v_cvt_pk_bf16_f32 v33, v34, v35\n"
      "v_mfma_f32_16x16x32_bf16 v[20:23], a[4:7], v[172:175], v[20:23]\n"
      "ds_write_addtid_b32 v33 offset:61712\n"
      "v_mfma_f32_16x16x32_bf16 v[20:23], a[8:11], v[228:231], v[20:23]\n"
      "v_mfma_f32_16x16x32_bf16 v[20:23], a[12:15], v[136:139], v[20:23]\n"
      "v_mfma_f32_16x16x32_bf16 v[20:23], a[16:19], v[192:195], v[20:23]\n"
      "v_mfma_f32_16x16x32_bf16 v[20:23], a[20:23], v[244:247], v[20:23]\n"
      "s_nop 1\n"
      "v_mfma_f32_16x16x32_bf16 v[24:27], a[24:27], a[196:199], 0\n"
      "ds_read_b128 a[24:27], v7 offset:4608\n"
      "s_nop 0\n"
      "v_mfma_f32_16x16x32_bf16 v[24:27], a[28:31], v[160:163], v[24:27]\n"
      "ds_read_b128 a[28:31], v7 offset:4864\n"
      "v_mfma_f32_16x16x32_bf16 v[24:27], a[32:35], v[216:219], v[24:27]\n"
      "ds_read_b128 a[32:35], v7 offset:5120\n"
      "v_cvt_pk_bf16_f32 v20, v20, v21\n"
      "v_mfma_f32_16x16x32_bf16 v[24:27], a[0:3], v[124:127], v[24:27]\n"
      "ds_read_b128 a[0:3], v6 offset:4608\n"
      "v_cvt_pk_bf16_f32 v21, v22, v23\n"
      "v_mfma_f32_16x16x32_bf16 v[24:27], a[4:7], v[180:183], v[24:27]\n"
      "ds_read_b128 a[4:7], v6 offset:4864\n"
      "v_mfma_f32_16x16x32_bf16 v[24:27], a[8:11], v[232:235], v[24:27]\n"
      "ds_read_b128 a[8:11], v6 offset:5120\n"
      "v_mfma_f32_16x16x32_bf16 v[24:27], a[12:15], a[216:219], v[24:27]\n"
      "ds_read_b128 a[12:15], v7 offset:0\n"
      "v_mfma_f32_16x16x32_bf16 v[24:27], a[16:19], v[196:199], v[24:27]\n"
      "ds_read_b128 a[16:19], v7 offset:256\n"
      "v_mfma_f32_16x16x32_bf16 v[24:27], a[20:23], v[252:255], v[24:27]\n"
      "s_waitcnt lgkmcnt(7)\n"
      "ds_read_b128 a[20:23], v7 offset:512\n"
      "s_nop 0\n"
      "v_mfma_f32_16x16x32_bf16 v[28:31], a[24:27], a[160:163], 0\n"
      "s_waitcnt lgkmcnt(7)\n"
      "ds_write_addtid_b32 v20 offset:49160\n"
      "ds_write_addtid_b32 v21 offset:49416\n"
      "v_mfma_f32_16x16x32_bf16 v[28:31], a[28:31], a[172:175], v[28:31]\n"
      "s_waitcnt lgkmcnt(8)\n"
      "v_cvt_pk_bf16_f32 v24, v24, v25\n"
      "v_cvt_pk_bf16_f32 v25, v26, v27\n"
      "v_mfma_f32_16x16x32_bf16 v[28:31], a[32:35], a[184:187], v[28:31]\n"
      "s_waitcnt lgkmcnt(7)\n"
      "ds_write_addtid_b32 v24 offset:36864\n"
      "v_mfma_f32_16x16x32_bf16 v[28:31], a[0:3], a[88:91], v[28:31]\n"
      "s_waitcnt lgkmcnt(7)\n"
      "ds_write_addtid_b32 v25 offset:37120\n"
      "v_mfma_f32_16x16x32_bf16 v[28:31], a[4:7], a[100:103], v[28:31]\n"
      "s_waitcnt lgkmcnt(7)\n"
      "v_mfma_f32_16x16x32_bf16 v[28:31], a[8:11], a[112:115], v[28:31]\n"
      "s_waitcnt lgkmcnt(6)\n"
      "v_mfma_f32_16x16x32_bf16 v[28:31], a[12:15], a[124:127], v[28:31]\n"
      "s_waitcnt lgkmcnt(5)\n"
      "v_mfma_f32_16x16x32_bf16 v[28:31], a[16:19], a[136:139], v[28:31]\n"
      "s_waitcnt lgkmcnt(4)\n"
      "v_mfma_f32_16x16x32_bf16 v[28:31], a[20:23], a[148:151], v[28:31]\n"
      "s_nop 1\n"
      "v_mfma_f32_16x16x32_bf16 v[20:23], a[24:27], a[164:167], 0\n"
      "s_nop 1\n"
      "v_mfma_f32_16x16x32_bf16 v[20:23], a[28:31], a[176:179], v[20:23]\n"
      "s_nop 0\n"
      "v_mfma_f32_16x16x32_bf16 v[20:23], a[32:35], a[188:191], v[20:23]\n"
      "v_cvt_pk_bf16_f32 v28, v28, v29\n"
      "v_cvt_pk_bf16_f32 v29, v30, v31\n"
      "v_mfma_f32_16x16x32_bf16 v[20:23], a[0:3], a[92:95], v[20:23]\n"
      "ds_write_addtid_b32 v28 offset:60432\n"
      "v_mfma_f32_16x16x32_bf16 v[20:23], a[4:7], a[104:107], v[20:23]\n"
      "ds_write_addtid_b32 v29 offset:60688\n"
      "v_mfma_f32_16x16x32_bf16 v[20:23], a[8:11], a[116:119], v[20:23]\n"
      "v_mfma_f32_16x16x32_bf16 v[20:23], a[12:15], a[128:131], v[20:23]\n"
      "v_mfma_f32_16x16x32_bf16 v[20:23], a[16:19], a[140:143], v[20:23]\n"
      "v_mfma_f32_16x16x32_bf16 v[20:23], a[20:23], a[152:155], v[20:23]\n"
      "s_nop 1\n"
      "v_mfma_f32_16x16x32_bf16 v[32:35], a[24:27], a[168:171], 0\n"
      "s_nop 1\n"
      "v_mfma_f32_16x16x32_bf16 v[32:35], a[28:31], a[180:183], v[32:35]\n"
      "s_nop 0\n"
      "v_mfma_f32_16x16x32_bf16 v[32:35], a[32:35], a[192:195], v[32:35]\n"
      "v_cvt_pk_bf16_f32 v20, v20, v21\n"
      "v_cvt_pk_bf16_f32 v21, v22, v23\n"
      "v_mfma_f32_16x16x32_bf16 v[32:35], a[0:3], a[96:99], v[32:35]\n"
      "ds_write_addtid_b32 v20 offset:48136\n"
      "v_mfma_f32_16x16x32_bf16 v[32:35], a[4:7], a[108:111], v[32:35]\n"
      "ds_write_addtid_b32 v21 offset:48392\n"
      "v_mfma_f32_16x16x32_bf16 v[32:35], a[8:11], a[120:123], v[32:35]\n"
      "v_mfma_f32_16x16x32_bf16 v[32:35], a[12:15], a[132:135], v[32:35]\n"
      "v_mfma_f32_16x16x32_bf16 v[32:35], a[16:19], a[144:147], v[32:35]\n"
      "v_mfma_f32_16x16x32_bf16 v[32:35], a[20:23], a[156:159], v[32:35]\n"
      "s_nop 1\n"
      "s_nop 5\n"
      "v_cvt_pk_bf16_f32 v32, v32, v33\n"
      "v_cvt_pk_bf16_f32 v33, v34, v35\n"
      "ds_write_addtid_b32 v32 offset:35840\n"
      "ds_write_addtid_b32 v33 offset:36096\n"
      "s_waitcnt lgkmcnt(0)\n"
      "s_barrier\n"
      "ds_read2_b64 a[0:3], v11 offset0:0 offset1:64\n"
      "ds_read2_b64 a[4:7], v11 offset0:1 offset1:65\n"
      "ds_read2_b64 a[8:11], v11 offset0:2 offset1:66\n"
      "ds_read2_b64 a[12:15], v11 offset0:3 offset1:67\n"
      "ds_read2_b64 a[16:19], v13 offset0:0 offset1:64\n"
      "ds_read2_b64 a[20:23], v13 offset0:1 offset1:65\n"
      "s_waitcnt lgkmcnt(5)\n"
      "v_mfma_f32_16x16x32_bf16 v[72:75], a[84:87], a[0:3], 0\n"
      "s_waitcnt lgkmcnt(4)\n"
      "v_mfma_f32_16x16x32_bf16 v[76:79], a[84:87], a[4:7], 0\n"
      "s_waitcnt lgkmcnt(3)\n"
      "v_mfma_f32_16x16x32_bf16 v[68:71], a[84:87], a[8:11], 0\n"
      "s_waitcnt lgkmcnt(2)\n"
      "s_nop 2\n"
      "v_cvt_pk_bf16_f32 v80, v72, v73\n"
      "v_cvt_pk_bf16_f32 v86, v74, v75\n"
      "v_mfma_f32_16x16x32_bf16 v[72:75], a[84:87], a[12:15], 0\n"
      "s_waitcnt lgkmcnt(1)\n"
      "v_cvt_pk_bf16_f32 v81, v76, v77\n"
      "v_cvt_pk_bf16_f32 v87, v78, v79\n"
      "v_cvt_pk_bf16_f32 v82, v68, v69\n"
      "v_cvt_pk_bf16_f32 v88, v70, v71\n"
      "v_mfma_f32_16x16x32_bf16 v[76:79], a[84:87], a[16:19], 0\n"
      "s_waitcnt lgkmcnt(0)\n"
      "v_mfma_f32_16x16x32_bf16 v[68:71], a[84:87], a[20:23], 0\n"
      "s_mov_b32 s52, s13\n"
      "v_cvt_pk_bf16_f32 v83, v72, v73\n"
      "v_cvt_pk_bf16_f32 v89, v74, v75\n"
      "s_add_u32 s13, s13, s12\n"
      "buffer_store_dwordx4 v[80:83], v14, s[36:39], s52 offen sc1\n"
      "buffer_store_dwordx4 v[86:89], v14, s[36:39], s52 offen offset:1536 sc1\n"
      "v_cvt_pk_bf16_f32 v84, v76, v77\n"
      "v_cvt_pk_bf16_f32 v90, v78, v79\n"
      "v_cvt_pk_bf16_f32 v85, v68, v69\n"
      "v_cvt_pk_bf16_f32 v91, v70, v71\n"
      "buffer_store_dwordx2 v[84:85], v15, s[36:39], s52 offen sc1\n"
      "buffer_store_dwordx2 v[90:91], v15, s[36:39], s52 offen offset:1536 sc1\n"
      "Lconv_u0:\n"
      "s_waitcnt vmcnt(0)\n"
      "s_endpgm\n"
      ".p2align 12\n"
      "Lconv_pa1:\n"
      "s_cbranch_execz Lconv_r0\n"
      "s_nop 0\n"
      "Lconv_loop1:\n"
      "v_mfma_f32_16x16x32_bf16 v[20:23], a[24:27], v[96:99], 0\n"
      "s_add_u32 s51, s51, s12\n"
      "ds_write_addtid_b32 v32 offset:34816\n"
      "v_cvt_pk_bf16_f32 v33, v34, v35\n"
      "v_mfma_f32_16x16x32_bf16 v[20:23], a[28:31], v[152:155], v[20:23]\n"
      "s_sub_u32 s58, s93, s58\n"
      "ds_write_addtid_b32 v33 offset:35072\n"
      "v_cvt_pk_bf16_f32 v84, v72, v73\n"
      "v_mfma_f32_16x16x32_bf16 v[20:23], a[32:35], a[220:223], v[20:23]\n"
      "s_sub_u32 s59, s94, s59\n"
      "v_cvt_pk_bf16_f32 v85, v76, v77\n"
      "v_cvt_pk_bf16_f32 v90, v74, v75\n"
      "v_mfma_f32_16x16x32_bf16 v[20:23], a[0:3], v[116:119], v[20:23]\n"
      "buffer_store_dwordx2 v[84:85], v15, s[36:39], s52 offen sc1\n"
      "v_cvt_pk_bf16_f32 v91, v78, v79\n"
      "v_mfma_f32_16x16x32_bf16 v[20:23], a[4:7], v[168:171], v[20:23]\n"
      "s_sub_u32 s47, s51, s48\n"
      "v_mfma_f32_16x16x32_bf16 v[20:23], a[8:11], v[224:227], v[20:23]\n"
      "s_cmp_ge_u32 vcc_lo, 0x80000000\n"
      "v_mfma_f32_16x16x32_bf16 v[24:27], a[24:27], v[104:107], 0\n"
      "buffer_store_dwordx2 v[90:91], v15, s[36:39], s52 offen offset:1536 sc1\n"
      "v_mfma_f32_16x16x32_bf16 v[24:27], a[28:31], v[156:159], v[24:27]\n"
      "s_cselect_b32 s53, s47, s51\n"
      "v_mfma_f32_16x16x32_bf16 v[24:27], a[32:35], v[212:215], v[24:27]\n"
      "s_mov_b32 s52, s13\n"
      "v_mfma_f32_16x16x32_bf16 v[24:27], a[0:3], v[120:123], v[24:27]\n"
      "s_add_u32 s13, s13, s12\n"
      "v_mfma_f32_16x16x32_bf16 v[24:27], a[4:7], v[176:179], v[24:27]\n"
      "s_waitcnt vmcnt(17) lgkmcnt(0)\n"
      "s_barrier\n"
      "v_mfma_f32_16x16x32_bf16 v[24:27], a[8:11], a[232:235], v[24:27]\n"
      "s_mov_b32 m0, s58\n"
      "ds_read2_b64 a[60:63], v10 offset0:0 offset1:64\n"
      "v_mfma_f32_16x16x32_bf16 v[28:31], a[24:27], v[108:111], 0\n"
      "ds_read_b128 a[12:15], v7 offset:6144\n"
      "v_mfma_f32_16x16x32_bf16 v[28:31], a[28:31], v[164:167], v[28:31]\n"
      "ds_read_b128 a[16:19], v7 offset:6400\n"
      "v_mfma_f32_16x16x32_bf16 v[28:31], a[32:35], a[224:227], v[28:31]\n"
      "ds_read_b128 a[20:23], v7 offset:6656\n"
      "v_mfma_f32_16x16x32_bf16 v[28:31], a[0:3], v[128:131], v[28:31]\n"
      "ds_read_b128 a[48:51], v2 offset:13280\n"
      "v_mfma_f32_16x16x32_bf16 v[28:31], a[4:7], a[228:231], v[28:31]\n"
      "ds_read_b128 a[56:59], v4 offset:13280\n"
      "v_mfma_f32_16x16x32_bf16 v[28:31], a[8:11], v[236:239], v[28:31]\n"
      "s_waitcnt lgkmcnt(5)\n"
      "ds_read_b128 a[52:55], v3 offset:13280\n"
      "v_mfma_f32_16x16x32_bf16 v[68:71], a[84:87], a[60:63], 0\n"
      "s_waitcnt lgkmcnt(5)\n"
      "ds_read_b128 a[24:27], v6 offset:5376\n"
      "v_mfma_f32_16x16x32_bf16 v[20:23], a[12:15], v[132:135], v[20:23]\n"
      "s_waitcnt lgkmcnt(5)\n"
      "ds_read_b128 a[28:31], v6 offset:5632\n"
      "v_mfma_f32_16x16x32_bf16 v[20:23], a[16:19], v[188:191], v[20:23]\n"
      "s_waitcnt lgkmcnt(5)\n"
      "ds_read_b128 a[32:35], v6 offset:5888\n"
      "v_cvt_pk_bf16_f32 v80, v68, v69\n"
      "v_mfma_f32_16x16x32_bf16 v[20:23], a[20:23], a[240:243], v[20:23]\n"
      "ds_read_b128 a[0:3], v7 offset:768\n"
      "v_cvt_pk_bf16_f32 v86, v70, v71\n"
      "v_mfma_f32_16x16x32_bf16 v[24:27], a[12:15], v[140:143], v[24:27]\n"
      "buffer_load_dwordx4 v16, s[32:35], s53 offen lds\n"
      "s_nop 0\n"
      "v_mfma_f32_16x16x32_bf16 v[24:27], a[16:19], a[236:239], v[24:27]\n"
      "s_mov_b32 m0, s45\n"
      "ds_read_b128 a[4:7], v7 offset:1024\n"
      "v_cvt_pk_bf16_f32 v20, v20, v21\n"
      "v_mfma_f32_16x16x32_bf16 v[24:27], a[20:23], v[248:251], v[24:27]\n"
      "s_waitcnt lgkmcnt(6)\n"
      "ds_read_b128 a[8:11], v7 offset:1280\n"
      "v_cvt_pk_bf16_f32 v21, v22, v23\n"
      "v_mfma_f32_16x16x32_bf16 v[52:55], a[48:51], a[72:75], 0\n"
      "ds_read2_b64 a[64:67], v10 offset0:1 offset1:65\n"
      "s_nop 0\n"
      "v_mfma_f32_16x16x32_bf16 v[60:63], a[56:59], a[80:83], v[52:55]\n"
      "ds_write_addtid_b32 v20 offset:62480\n"
      "v_cvt_pk_bf16_f32 v24, v24, v25\n"
      "v_mfma_f32_16x16x32_bf16 v[28:31], a[12:15], v[144:147], v[28:31]\n"
      "ds_read_b128 a[12:15], v7 offset:5376\n"
      "v_cvt_pk_bf16_f32 v25, v26, v27\n"
      "v_mfma_f32_16x16x32_bf16 v[28:31], a[16:19], v[200:203], v[28:31]\n"
      "ds_read_b128 a[16:19], v7 offset:5632\n"
      "v_mfma_f32_16x16x32_bf16 v[28:31], a[20:23], a[244:247], v[28:31]\n"
      "s_waitcnt lgkmcnt(6)\n"
      "ds_read_b128 a[20:23], v7 offset:5888\n"
      "v_cvt_pk_bf16_f32 v60, v60, v61\n"
      "v_mfma_f32_16x16x32_bf16 v[56:59], a[52:55], a[76:79], 0\n"
      "ds_write_addtid_b32 v21 offset:62736\n"
      "v_cvt_pk_bf16_f32 v61, v62, v63\n"
      "v_mfma_f32_16x16x32_bf16 v[32:35], a[24:27], v[92:95], 0\n"
      "ds_read_b128 a[36:39], v2 offset:13344\n"
      "v_cvt_pk_bf16_f32 v28, v28, v29\n"
      "v_mfma_f32_16x16x32_bf16 v[32:35], a[28:31], v[148:151], v[32:35]\n"
      "ds_write_addtid_b32 v24 offset:50184\n"
      "v_cvt_pk_bf16_f32 v29, v30, v31\n"
      "v_mfma_f32_16x16x32_bf16 v[32:35], a[32:35], v[204:207], v[32:35]\n"
      "ds_read_b128 a[40:43], v3 offset:13344\n"
      "v_fma_f32 v64, s44, v52, -v56\n"
      "v_mfma_f32_16x16x32_bf16 v[32:35], a[0:3], v[112:115], v[32:35]\n"
      "s_waitcnt lgkmcnt(8)\n"
      "ds_write_addtid_b32 v25 offset:50440\n"
      "v_fma_f32 v65, s44, v53, -v57\n"
      "v_mfma_f32_16x16x32_bf16 v[32:35], a[4:7], a[208:211], v[32:35]\n"
      "ds_read_b128 a[44:47], v4 offset:13344\n"
      "v_fma_f32 v66, s44, v54, -v58\n"
      "v_mfma_f32_16x16x32_bf16 v[32:35], a[8:11], v[220:223], v[32:35]\n"
      "s_waitcnt lgkmcnt(7)\n"
      "ds_read_b128 a[48:51], v2 offset:13408\n"
      "v_fma_f32 v67, s44, v55, -v59\n"
      "v_mfma_f32_16x16x32_bf16 v[32:35], a[12:15], a[212:215], v[32:35]\n"
      "ds_write_addtid_b32 v28 offset:37888\n"
      "v_fmac_f32_e32 v56, s44, v52\n"
      "v_mfma_f32_16x16x32_bf16 v[32:35], a[16:19], v[184:187], v[32:35]\n"
      "s_waitcnt lgkmcnt(8)\n"
      "ds_write_addtid_b32 v29 offset:38144\n"
      "v_fmac_f32_e32 v57, s44, v53\n"
      "v_mfma_f32_16x16x32_bf16 v[32:35], a[20:23], v[240:243], v[32:35]\n"
      "s_mov_b32 m0, s18\n"
      "ds_read_b128 a[56:59], v4 offset:13408\n"
      "v_fmac_f32_e32 v58, s44, v54\n"
      "v_mfma_f32_16x16x32_bf16 v[72:75], a[84:87], a[64:67], 0\n"
      "ds_read2_b64 a[68:71], v10 offset0:2 offset1:66\n"
      "v_fmac_f32_e64 v59, s44, v55\n"
      "v_mfma_f32_16x16x32_bf16 v[20:23], a[24:27], v[100:103], 0\n"
      "v_cvt_pk_bf16_f32 v64, v64, v65\n"
      "v_cvt_pk_bf16_f32 v65, v66, v67\n"
      "v_mfma_f32_16x16x32_bf16 v[20:23], a[28:31], a[200:203], v[20:23]\n"
      "ds_write_addtid_b32 v64 offset:1536\n"
      "v_cvt_pk_bf16_f32 v56, v56, v57\n"
      "v_mfma_f32_16x16x32_bf16 v[20:23], a[32:35], v[208:211], v[20:23]\n"
      "ds_write_addtid_b32 v65 offset:38464\n"
      "v_cvt_pk_bf16_f32 v57, v58, v59\n"
      "v_mfma_f32_16x16x32_bf16 v[20:23], a[0:3], a[204:207], v[20:23]\n"
      "ds_write_addtid_b32 v56 offset:768\n"
      "v_cvt_pk_bf16_f32 v32, v32, v33\n"
      "v_mfma_f32_16x16x32_bf16 v[20:23], a[4:7], v[172:175], v[20:23]\n"
      "ds_write_addtid_b32 v57 offset:37696\n"
      "v_cvt_pk_bf16_f32 v33, v34, v35\n"
      "v_mfma_f32_16x16x32_bf16 v[20:23], a[8:11], v[228:231], v[20:23]\n"
      "ds_write_addtid_b32 v60 offset:0\n"
      "v_cvt_pk_bf16_f32 v81, v72, v73\n"
      "v_mfma_f32_16x16x32_bf16 v[20:23], a[12:15], v[136:139], v[20:23]\n"
      "ds_write_addtid_b32 v61 offset:36928\n"
      "v_cvt_pk_bf16_f32 v87, v74, v75\n"
      "v_mfma_f32_16x16x32_bf16 v[20:23], a[16:19], v[192:195], v[20:23]\n"
      "s_mov_b32 m0, s45\n"
      "ds_read_b128 a[52:55], v3 offset:13408\n"
      "v_add_u32_e32 v2, s91, v2\n"
      "v_mfma_f32_16x16x32_bf16 v[20:23], a[20:23], v[244:247], v[20:23]\n"
      "s_waitcnt lgkmcnt(7)\n"
      "ds_read2_b64 a[60:63], v10 offset0:3 offset1:67\n"
      "v_add_u32_e64 v4, s91, v4\n"
      "v_mfma_f32_16x16x32_bf16 v[36:39], a[36:39], a[72:75], 0\n"
      "s_mov_b32 m0, s58\n"
      "ds_read2_b64 a[64:67], v12 offset0:0 offset1:64\n"
      "v_add_u32_e32 v3, s91, v3\n"
      "v_mfma_f32_16x16x32_bf16 v[40:43], a[40:43], a[76:79], 0\n"
      "buffer_load_dwordx4 v17, s[32:35], s53 offen offset:1024 lds\n"
      "v_cvt_pk_bf16_f32 v20, v20, v21\n"
      "v_mfma_f32_16x16x32_bf16 v[44:47], a[44:47], a[80:83], v[36:39]\n"
      "s_mov_b32 m0, s45\n"
      "v_cvt_pk_bf16_f32 v21, v22, v23\n"
      "s_nop 0\n"
      "v_mfma_f32_16x16x32_bf16 v[52:55], a[48:51], a[72:75], 0\n"
      "s_sub_u32 s91, 0, s91\n"
      "ds_write_addtid_b32 v32 offset:61456\n"
      "v_fma_f32 v48, s44, v36, -v40\n"
      "v_mfma_f32_16x16x32_bf16 v[60:63], a[56:59], a[80:83], v[52:55]\n"
      "ds_write_addtid_b32 v33 offset:61712\n"
      "v_fma_f32 v49, s44, v37, -v41\n"
      "v_mfma_f32_16x16x32_bf16 v[76:79], a[84:87], a[68:71], 0\n"
      "ds_write_addtid_b32 v20 offset:49160\n"
      "v_fma_f32 v50, s44, v38, -v42\n"
      "v_mfma_f32_16x16x32_bf16 v[24:27], a[24:27], a[196:199], 0\n"
      "ds_read_b128 a[24:27], v7 offset:4608\n"
      "v_fma_f32 v51, s44, v39, -v43\n"
      "v_mfma_f32_16x16x32_bf16 v[24:27], a[28:31], v[160:163], v[24:27]\n"
      "ds_read_b128 a[28:31], v7 offset:4864\n"
      "v_fmac_f32_e64 v40, s44, v36\n"
      "v_mfma_f32_16x16x32_bf16 v[24:27], a[32:35], v[216:219], v[24:27]\n"
      "ds_read_b128 a[32:35], v7 offset:5120\n"
      "v_fmac_f32_e64 v41, s44, v37\n"
      "v_mfma_f32_16x16x32_bf16 v[24:27], a[0:3], v[124:127], v[24:27]\n"
      "ds_read_b128 a[0:3], v6 offset:4608\n"
      "v_fmac_f32_e64 v42, s44, v38\n"
      "v_mfma_f32_16x16x32_bf16 v[24:27], a[4:7], v[180:183], v[24:27]\n"
      "ds_read_b128 a[4:7], v6 offset:4864\n"
      "v_fmac_f32_e64 v43, s44, v39\n"
      "v_mfma_f32_16x16x32_bf16 v[24:27], a[8:11], v[232:235], v[24:27]\n"
      "ds_read_b128 a[8:11], v6 offset:5120\n"
      "v_cvt_pk_bf16_f32 v48, v48, v49\n"
      "v_mfma_f32_16x16x32_bf16 v[24:27], a[12:15], a[216:219], v[24:27]\n"
      "ds_read_b128 a[12:15], v7 offset:0\n"
      "v_cvt_pk_bf16_f32 v49, v50, v51\n"
      "v_mfma_f32_16x16x32_bf16 v[24:27], a[16:19], v[196:199], v[24:27]\n"
      "ds_read_b128 a[16:19], v7 offset:256\n"
      "v_cvt_pk_bf16_f32 v40, v40, v41\n"
      "v_mfma_f32_16x16x32_bf16 v[24:27], a[20:23], v[252:255], v[24:27]\n"
      "s_waitcnt lgkmcnt(7)\n"
      "ds_read_b128 a[20:23], v7 offset:512\n"
      "v_cvt_pk_bf16_f32 v41, v42, v43\n"
      "v_mfma_f32_16x16x32_bf16 v[56:59], a[52:55], a[76:79], 0\n"
      "ds_write_addtid_b32 v21 offset:49416\n"
      "v_cvt_pk_bf16_f32 v44, v44, v45\n"
      "v_mfma_f32_16x16x32_bf16 v[28:31], a[24:27], a[160:163], 0\n"
      "s_waitcnt lgkmcnt(7)\n"
      "v_cvt_pk_bf16_f32 v45, v46, v47\n"
      "v_cvt_pk_bf16_f32 v24, v24, v25\n"
      "v_mfma_f32_16x16x32_bf16 v[28:31], a[28:31], a[172:175], v[28:31]\n"
      "s_mov_b32 m0, s18\n"
      "v_cvt_pk_bf16_f32 v25, v26, v27\n"
      "v_fma_f32 v64, s44, v52, -v56\n"
      "v_mfma_f32_16x16x32_bf16 v[28:31], a[32:35], a[184:187], v[28:31]\n"
      "s_waitcnt lgkmcnt(5)\n"
      "ds_write_addtid_b32 v48 offset:1792\n"
      "v_fma_f32 v65, s44, v53, -v57\n"
      "v_mfma_f32_16x16x32_bf16 v[28:31], a[0:3], a[88:91], v[28:31]\n"
      "ds_write_addtid_b32 v49 offset:38720\n"
      "v_cvt_pk_bf16_f32 v64, v64, v65\n"
      "v_mfma_f32_16x16x32_bf16 v[28:31], a[4:7], a[100:103], v[28:31]\n"
      "s_waitcnt lgkmcnt(5)\n"
      "ds_write_addtid_b32 v40 offset:1024\n"
      "v_fma_f32 v66, s44, v54, -v58\n"
      "v_mfma_f32_16x16x32_bf16 v[28:31], a[8:11], a[112:115], v[28:31]\n"
      "ds_write_addtid_b32 v41 offset:37952\n"
      "v_fma_f32 v67, s44, v55, -v59\n"
      "v_mfma_f32_16x16x32_bf16 v[28:31], a[12:15], a[124:127], v[28:31]\n"
      "s_waitcnt lgkmcnt(5)\n"
      "ds_write_addtid_b32 v44 offset:256\n"
      "v_cvt_pk_bf16_f32 v65, v66, v67\n"
      "v_mfma_f32_16x16x32_bf16 v[28:31], a[16:19], a[136:139], v[28:31]\n"
      "ds_write_addtid_b32 v45 offset:37184\n"
      "v_fmac_f32_e64 v56, s44, v52\n"
      "v_mfma_f32_16x16x32_bf16 v[28:31], a[20:23], a[148:151], v[28:31]\n"
      "s_mov_b32 m0, s45\n"
      "v_fmac_f32_e32 v57, s44, v53\n"
      "v_cvt_pk_bf16_f32 v56, v56, v57\n"
      "v_mfma_f32_16x16x32_bf16 v[20:23], a[24:27], a[164:167], 0\n"
      "s_mov_b32 m0, s58\n"
      "v_fmac_f32_e32 v58, s44, v54\n"
      "v_fmac_f32_e32 v59, s44, v55\n"
      "v_mfma_f32_16x16x32_bf16 v[20:23], a[28:31], a[176:179], v[20:23]\n"
      "buffer_load_dwordx4 v18, s[32:35], s53 offen offset:2048 lds\n"
      "v_cvt_pk_bf16_f32 v57, v58, v59\n"
      "v_mfma_f32_16x16x32_bf16 v[20:23], a[32:35], a[188:191], v[20:23]\n"
      "s_mov_b32 m0, s45\n"
      "ds_read2_b64 a[68:71], v12 offset0:1 offset1:65\n"
      "v_cvt_pk_bf16_f32 v60, v60, v61\n"
      "v_mfma_f32_16x16x32_bf16 v[20:23], a[0:3], a[92:95], v[20:23]\n"
      "ds_write_addtid_b32 v24 offset:36864\n"
      "v_cvt_pk_bf16_f32 v61, v62, v63\n"
      "v_mfma_f32_16x16x32_bf16 v[20:23], a[4:7], a[104:107], v[20:23]\n"
      "ds_write_addtid_b32 v25 offset:37120\n"
      "v_cvt_pk_bf16_f32 v28, v28, v29\n"
      "v_mfma_f32_16x16x32_bf16 v[20:23], a[8:11], a[116:119], v[20:23]\n"
      "s_mov_b32 m0, s18\n"
      "v_cvt_pk_bf16_f32 v29, v30, v31\n"
      "v_cvt_pk_bf16_f32 v82, v76, v77\n"
      "v_mfma_f32_16x16x32_bf16 v[20:23], a[12:15], a[128:131], v[20:23]\n"
      "ds_write_addtid_b32 v64 offset:2048\n"
      "v_cvt_pk_bf16_f32 v88, v78, v79\n"
      "v_mfma_f32_16x16x32_bf16 v[20:23], a[16:19], a[140:143], v[20:23]\n"
      "ds_write_addtid_b32 v65 offset:38976\n"
      "v_swap_b32 v6, v7\n"
      "v_mfma_f32_16x16x32_bf16 v[20:23], a[20:23], a[152:155], v[20:23]\n"
      "ds_write_addtid_b32 v56 offset:1280\n"
      "s_nop 0\n"
      "v_mfma_f32_16x16x32_bf16 v[68:71], a[84:87], a[60:63], 0\n"
      "ds_write_addtid_b32 v57 offset:38208\n"
      "s_nop 0\n"
      "v_mfma_f32_16x16x32_bf16 v[32:35], a[24:27], a[168:171], 0\n"
      "ds_write_addtid_b32 v60 offset:512\n"
      "v_mfma_f32_16x16x32_bf16 v[32:35], a[28:31], a[180:183], v[32:35]\n"
      "ds_write_addtid_b32 v61 offset:37440\n"
      "v_cvt_pk_bf16_f32 v20, v20, v21\n"
      "v_mfma_f32_16x16x32_bf16 v[32:35], a[32:35], a[192:195], v[32:35]\n"
      "s_mov_b32 m0, s45\n"
      "v_cvt_pk_bf16_f32 v83, v68, v69\n"
      "v_cvt_pk_bf16_f32 v21, v22, v23\n"
      "v_mfma_f32_16x16x32_bf16 v[32:35], a[0:3], a[96:99], v[32:35]\n"
      "ds_write_addtid_b32 v28 offset:60432\n"
      "v_cvt_pk_bf16_f32 v89, v70, v71\n"
      "v_mfma_f32_16x16x32_bf16 v[32:35], a[4:7], a[108:111], v[32:35]\n"
      "ds_write_addtid_b32 v29 offset:60688\n"
      "v_mfma_f32_16x16x32_bf16 v[32:35], a[8:11], a[120:123], v[32:35]\n"
      "ds_write_addtid_b32 v20 offset:48136\n"
      "v_mfma_f32_16x16x32_bf16 v[32:35], a[12:15], a[132:135], v[32:35]\n"
      "buffer_store_dwordx4 v[80:83], v14, s[36:39], s52 offen sc1\n"
      "v_mfma_f32_16x16x32_bf16 v[32:35], a[16:19], a[144:147], v[32:35]\n"
      "ds_write_addtid_b32 v21 offset:48392\n"
      "v_mfma_f32_16x16x32_bf16 v[32:35], a[20:23], a[156:159], v[32:35]\n"
      "s_nop 1\n"
      "v_mfma_f32_16x16x32_bf16 v[72:75], a[84:87], a[64:67], 0\n"
      "s_waitcnt lgkmcnt(12)\n"
      "s_nop 1\n"
      "v_mfma_f32_16x16x32_bf16 v[76:79], a[84:87], a[68:71], 0\n"
      "buffer_store_dwordx4 v[86:89], v14, s[36:39], s52 offen offset:1536 sc1\n"
      "v_cvt_pk_bf16_f32 v32, v32, v33\n"
      "v_mfma_f32_16x16x32_bf16 v[20:23], a[12:15], a[88:91], 0\n"
      "s_add_u32 s51, s51, s12\n"
      "ds_write_addtid_b32 v32 offset:35840\n"
      "v_cvt_pk_bf16_f32 v33, v34, v35\n"
      "v_mfma_f32_16x16x32_bf16 v[20:23], a[16:19], a[100:103], v[20:23]\n"
      "s_sub_u32 s47, s51, s48\n"
      "ds_write_addtid_b32 v33 offset:36096\n"
      "v_cvt_pk_bf16_f32 v84, v72, v73\n"
      "v_mfma_f32_16x16x32_bf16 v[20:23], a[20:23], a[112:115], v[20:23]\n"
      "s_cmp_ge_u32 vcc_lo, 0x40000000\n"
      "v_cvt_pk_bf16_f32 v85, v76, v77\n"
      "v_cvt_pk_bf16_f32 v90, v74, v75\n"
      "v_mfma_f32_16x16x32_bf16 v[20:23], a[24:27], a[124:127], v[20:23]\n"
      "buffer_store_dwordx2 v[84:85], v15, s[36:39], s52 offen sc1\n"
      "v_cvt_pk_bf16_f32 v91, v78, v79\n"
      "v_mfma_f32_16x16x32_bf16 v[20:23], a[28:31], a[136:139], v[20:23]\n"
      "s_cselect_b32 s53, s47, s51\n"
      "v_mfma_f32_16x16x32_bf16 v[20:23], a[32:35], a[148:151], v[20:23]\n"
      "s_mul_i32 vcc_lo, vcc_lo, 2\n"
      "v_mfma_f32_16x16x32_bf16 v[24:27], a[12:15], a[92:95], 0\n"
      "buffer_store_dwordx2 v[90:91], v15, s[36:39], s52 offen offset:1536 sc1\n"
      "v_mfma_f32_16x16x32_bf16 v[24:27], a[16:19], a[104:107], v[24:27]\n"
      "s_mov_b32 s52, s13\n"
      "v_mfma_f32_16x16x32_bf16 v[24:27], a[20:23], a[116:119], v[24:27]\n"
      "v_mfma_f32_16x16x32_bf16 v[24:27], a[24:27], a[128:131], v[24:27]\n"
      "v_mfma_f32_16x16x32_bf16 v[24:27], a[28:31], a[140:143], v[24:27]\n"
      "s_waitcnt vmcnt(19) lgkmcnt(0)\n"
      "s_barrier\n"
      "v_mfma_f32_16x16x32_bf16 v[24:27], a[32:35], a[152:155], v[24:27]\n"
      "s_add_u32 m0, s58, 13280\n"
      "ds_read2_b64 a[60:63], v11 offset0:0 offset1:64\n"
      "v_mfma_f32_16x16x32_bf16 v[28:31], a[12:15], a[96:99], 0\n"
      "s_add_u32 s13, s13, s12\n"
      "ds_read_b128 a[0:3], v7 offset:0\n"
      "v_mfma_f32_16x16x32_bf16 v[28:31], a[16:19], a[108:111], v[28:31]\n"
      "ds_read_b128 a[4:7], v7 offset:256\n"
      "v_mfma_f32_16x16x32_bf16 v[28:31], a[20:23], a[120:123], v[28:31]\n"
      "ds_read_b128 a[8:11], v7 offset:512\n"
      "v_mfma_f32_16x16x32_bf16 v[28:31], a[24:27], a[132:135], v[28:31]\n"
      "ds_read_b128 a[36:39], v2 offset:0\n"
      "v_mfma_f32_16x16x32_bf16 v[28:31], a[28:31], a[144:147], v[28:31]\n"
      "ds_read_b128 a[44:47], v4 offset:0\n"
      "v_mfma_f32_16x16x32_bf16 v[28:31], a[32:35], a[156:159], v[28:31]\n"
      "s_waitcnt lgkmcnt(5)\n"
      "ds_read_b128 a[40:43], v3 offset:0\n"
      "v_mfma_f32_16x16x32_bf16 v[68:71], a[84:87], a[60:63], 0\n"
      "s_waitcnt lgkmcnt(5)\n"
      "ds_read_b128 a[12:15], v6 offset:768\n"
      "v_mfma_f32_16x16x32_bf16 v[20:23], a[0:3], a[160:163], v[20:23]\n"
      "s_waitcnt lgkmcnt(5)\n"
      "ds_read_b128 a[16:19], v6 offset:1024\n"
      "v_mfma_f32_16x16x32_bf16 v[20:23], a[4:7], a[172:175], v[20:23]\n"
      "s_waitcnt lgkmcnt(5)\n"
      "ds_read_b128 a[20:23], v6 offset:1280\n"
      "v_cvt_pk_bf16_f32 v80, v68, v69\n"
      "v_mfma_f32_16x16x32_bf16 v[20:23], a[8:11], a[184:187], v[20:23]\n"
      "ds_read_b128 a[24:27], v6 offset:5376\n"
      "v_cvt_pk_bf16_f32 v86, v70, v71\n"
      "v_mfma_f32_16x16x32_bf16 v[24:27], a[0:3], a[164:167], v[24:27]\n"
      "buffer_load_dwordx4 v16, s[32:35], s53 offen lds\n"
      "s_nop 0\n"
      "v_mfma_f32_16x16x32_bf16 v[24:27], a[4:7], a[176:179], v[24:27]\n"
      "s_mov_b32 m0, s45\n"
      "ds_read_b128 a[28:31], v6 offset:5632\n"
      "v_cvt_pk_bf16_f32 v20, v20, v21\n"
      "v_mfma_f32_16x16x32_bf16 v[24:27], a[8:11], a[188:191], v[24:27]\n"
      "s_waitcnt lgkmcnt(6)\n"
      "ds_read_b128 a[32:35], v6 offset:5888\n"
      "v_cvt_pk_bf16_f32 v21, v22, v23\n"
      "v_mfma_f32_16x16x32_bf16 v[36:39], a[36:39], a[72:75], 0\n"
      "ds_read2_b64 a[64:67], v11 offset0:1 offset1:65\n"
      "s_nop 0\n"
      "v_mfma_f32_16x16x32_bf16 v[44:47], a[44:47], a[80:83], v[36:39]\n"
      "ds_write_addtid_b32 v20 offset:57360\n"
      "v_cvt_pk_bf16_f32 v24, v24, v25\n"
      "v_mfma_f32_16x16x32_bf16 v[28:31], a[0:3], a[168:171], v[28:31]\n"
      "ds_read_b128 a[0:3], v7 offset:768\n"
      "v_cvt_pk_bf16_f32 v25, v26, v27\n"
      "v_mfma_f32_16x16x32_bf16 v[28:31], a[4:7], a[180:183], v[28:31]\n"
      "ds_read_b128 a[4:7], v7 offset:1024\n"
      "v_mfma_f32_16x16x32_bf16 v[28:31], a[8:11], a[192:195], v[28:31]\n"
      "s_waitcnt lgkmcnt(6)\n"
      "ds_read_b128 a[8:11], v7 offset:1280\n"
      "v_cvt_pk_bf16_f32 v44, v44, v45\n"
      "v_mfma_f32_16x16x32_bf16 v[40:43], a[40:43], a[76:79], 0\n"
      "ds_write_addtid_b32 v21 offset:57616\n"
      "v_cvt_pk_bf16_f32 v45, v46, v47\n"
      "v_mfma_f32_16x16x32_bf16 v[32:35], a[12:15], v[92:95], 0\n"
      "ds_read_b128 a[48:51], v2 offset:64\n"
      "v_cvt_pk_bf16_f32 v28, v28, v29\n"
      "v_mfma_f32_16x16x32_bf16 v[32:35], a[16:19], v[148:151], v[32:35]\n"
      "ds_write_addtid_b32 v24 offset:45064\n"
      "v_cvt_pk_bf16_f32 v29, v30, v31\n"
      "v_mfma_f32_16x16x32_bf16 v[32:35], a[20:23], v[204:207], v[32:35]\n"
      "ds_read_b128 a[52:55], v3 offset:64\n"
      "v_fma_f32 v48, s44, v36, -v40\n"
      "v_mfma_f32_16x16x32_bf16 v[32:35], a[24:27], v[112:115], v[32:35]\n"
      "s_waitcnt lgkmcnt(8)\n"
      "ds_write_addtid_b32 v25 offset:45320\n"
      "v_fma_f32 v49, s44, v37, -v41\n"
      "v_mfma_f32_16x16x32_bf16 v[32:35], a[28:31], a[208:211], v[32:35]\n"
      "ds_read_b128 a[56:59], v4 offset:64\n"
      "v_fma_f32 v50, s44, v38, -v42\n"
      "v_mfma_f32_16x16x32_bf16 v[32:35], a[32:35], v[220:223], v[32:35]\n"
      "s_waitcnt lgkmcnt(7)\n"
      "ds_read_b128 a[36:39], v2 offset:128\n"
      "v_fma_f32 v51, s44, v39, -v43\n"
      "v_mfma_f32_16x16x32_bf16 v[32:35], a[0:3], a[212:215], v[32:35]\n"
      "ds_write_addtid_b32 v28 offset:32768\n"
      "v_fmac_f32_e32 v40, s44, v36\n"
      "v_mfma_f32_16x16x32_bf16 v[32:35], a[4:7], v[184:187], v[32:35]\n"
      "s_waitcnt lgkmcnt(8)\n"
      "ds_write_addtid_b32 v29 offset:33024\n"
      "v_fmac_f32_e32 v41, s44, v37\n"
      "v_mfma_f32_16x16x32_bf16 v[32:35], a[8:11], v[240:243], v[32:35]\n"
      "s_mov_b32 m0, s18\n"
      "ds_read_b128 a[44:47], v4 offset:128\n"
      "v_fmac_f32_e32 v42, s44, v38\n"
      "v_mfma_f32_16x16x32_bf16 v[72:75], a[84:87], a[64:67], 0\n"
      "ds_read2_b64 a[68:71], v11 offset0:2 offset1:66\n"
      "v_fmac_f32_e64 v43, s44, v39\n"
      "v_mfma_f32_16x16x32_bf16 v[20:23], a[12:15], v[100:103], 0\n"
      "v_cvt_pk_bf16_f32 v48, v48, v49\n"
      "v_cvt_pk_bf16_f32 v49, v50, v51\n"
      "v_mfma_f32_16x16x32_bf16 v[20:23], a[16:19], a[200:203], v[20:23]\n"
      "ds_write_addtid_b32 v48 offset:6144\n"
      "v_cvt_pk_bf16_f32 v40, v40, v41\n"
      "v_mfma_f32_16x16x32_bf16 v[20:23], a[20:23], v[208:211], v[20:23]\n"
      "ds_write_addtid_b32 v49 offset:43072\n"
      "v_cvt_pk_bf16_f32 v41, v42, v43\n"
      "v_mfma_f32_16x16x32_bf16 v[20:23], a[24:27], a[204:207], v[20:23]\n"
      "ds_write_addtid_b32 v40 offset:5376\n"
      "v_cvt_pk_bf16_f32 v32, v32, v33\n"
      "v_mfma_f32_16x16x32_bf16 v[20:23], a[28:31], v[172:175], v[20:23]\n"
      "ds_write_addtid_b32 v41 offset:42304\n"
      "v_cvt_pk_bf16_f32 v33, v34, v35\n"
      "v_mfma_f32_16x16x32_bf16 v[20:23], a[32:35], v[228:231], v[20:23]\n"
      "ds_write_addtid_b32 v44 offset:4608\n"
      "v_cvt_pk_bf16_f32 v81, v72, v73\n"
      "v_mfma_f32_16x16x32_bf16 v[20:23], a[0:3], v[136:139], v[20:23]\n"
      "ds_write_addtid_b32 v45 offset:41536\n"
      "v_cvt_pk_bf16_f32 v87, v74, v75\n"
      "v_mfma_f32_16x16x32_bf16 v[20:23], a[4:7], v[192:195], v[20:23]\n"
      "s_mov_b32 m0, s45\n"
      "ds_read_b128 a[40:43], v3 offset:128\n"
      "v_mfma_f32_16x16x32_bf16 v[20:23], a[8:11], v[244:247], v[20:23]\n"
      "s_waitcnt lgkmcnt(7)\n"
      "ds_read2_b64 a[60:63], v11 offset0:3 offset1:67\n"
      "s_nop 0\n"
      "v_mfma_f32_16x16x32_bf16 v[52:55], a[48:51], a[72:75], 0\n"
      "s_add_u32 m0, s58, 13280\n"
      "ds_read2_b64 a[64:67], v13 offset0:0 offset1:64\n"
      "s_nop 0\n"
      "v_mfma_f32_16x16x32_bf16 v[56:59], a[52:55], a[76:79], 0\n"
      "buffer_load_dwordx4 v17, s[32:35], s53 offen offset:1024 lds\n"
      "v_cvt_pk_bf16_f32 v20, v20, v21\n"
      "v_mfma_f32_16x16x32_bf16 v[60:63], a[56:59], a[80:83], v[52:55]\n"
      "s_mov_b32 m0, s45\n"
      "v_cvt_pk_bf16_f32 v21, v22, v23\n"
      "s_nop 0\n"
      "v_mfma_f32_16x16x32_bf16 v[36:39], a[36:39], a[72:75], 0\n"
      "ds_write_addtid_b32 v32 offset:58384\n"
      "v_fma_f32 v64, s44, v52, -v56\n"
      "v_mfma_f32_16x16x32_bf16 v[44:47], a[44:47], a[80:83], v[36:39]\n"
      "ds_write_addtid_b32 v33 offset:58640\n"
      "v_fma_f32 v65, s44, v53, -v57\n"
      "v_mfma_f32_16x16x32_bf16 v[76:79], a[84:87], a[68:71], 0\n"
      "global_load_dword v8, v9, s[54:55]\n"
      "v_fma_f32 v66, s44, v54, -v58\n"
      "v_mfma_f32_16x16x32_bf16 v[24:27], a[12:15], a[196:199], 0\n"
      "ds_read_b128 a[12:15], v6 offset:1536\n"
      "v_fma_f32 v67, s44, v55, -v59\n"
      "v_mfma_f32_16x16x32_bf16 v[24:27], a[16:19], v[160:163], v[24:27]\n"
      "ds_read_b128 a[16:19], v6 offset:1792\n"
      "v_fmac_f32_e64 v56, s44, v52\n"
      "v_mfma_f32_16x16x32_bf16 v[24:27], a[20:23], v[216:219], v[24:27]\n"
      "ds_read_b128 a[20:23], v6 offset:2048\n"
      "v_fmac_f32_e64 v57, s44, v53\n"
      "v_mfma_f32_16x16x32_bf16 v[24:27], a[24:27], v[124:127], v[24:27]\n"
      "ds_read_b128 a[24:27], v6 offset:6144\n"
      "v_fmac_f32_e64 v58, s44, v54\n"
      "v_mfma_f32_16x16x32_bf16 v[24:27], a[28:31], v[180:183], v[24:27]\n"
      "ds_read_b128 a[28:31], v6 offset:6400\n"
      "v_fmac_f32_e64 v59, s44, v55\n"
      "v_mfma_f32_16x16x32_bf16 v[24:27], a[32:35], v[232:235], v[24:27]\n"
      "ds_read_b128 a[32:35], v6 offset:6656\n"
      "v_cvt_pk_bf16_f32 v64, v64, v65\n"
      "v_mfma_f32_16x16x32_bf16 v[24:27], a[0:3], a[216:219], v[24:27]\n"
      "ds_read_b128 a[0:3], v7 offset:1536\n"
      "v_cvt_pk_bf16_f32 v65, v66, v67\n"
      "v_mfma_f32_16x16x32_bf16 v[24:27], a[4:7], v[196:199], v[24:27]\n"
      "ds_read_b128 a[4:7], v7 offset:1792\n"
      "v_cvt_pk_bf16_f32 v56, v56, v57\n"
      "v_mfma_f32_16x16x32_bf16 v[24:27], a[8:11], v[252:255], v[24:27]\n"
      "s_waitcnt lgkmcnt(7)\n"
      "ds_read_b128 a[8:11], v7 offset:2048\n"
      "v_cvt_pk_bf16_f32 v57, v58, v59\n"
      "v_mfma_f32_16x16x32_bf16 v[40:43], a[40:43], a[76:79], 0\n"
      "ds_write_addtid_b32 v20 offset:46088\n"
      "v_cvt_pk_bf16_f32 v60, v60, v61\n"
      "v_mfma_f32_16x16x32_bf16 v[28:31], a[12:15], v[96:99], 0\n"
      "s_waitcnt lgkmcnt(7)\n"
      "ds_write_addtid_b32 v21 offset:46344\n"
      "v_cvt_pk_bf16_f32 v61, v62, v63\n"
      "v_mfma_f32_16x16x32_bf16 v[28:31], a[16:19], v[152:155], v[28:31]\n"
      "s_mov_b32 m0, s18\n"
      "v_cvt_pk_bf16_f32 v24, v24, v25\n"
      "v_cvt_pk_bf16_f32 v25, v26, v27\n"
      "v_mfma_f32_16x16x32_bf16 v[28:31], a[20:23], a[220:223], v[28:31]\n"
      "s_waitcnt lgkmcnt(6)\n"
      "ds_write_addtid_b32 v64 offset:6400\n"
      "v_fma_f32 v48, s44, v36, -v40\n"
      "v_mfma_f32_16x16x32_bf16 v[28:31], a[24:27], v[116:119], v[28:31]\n"
      "ds_write_addtid_b32 v65 offset:43328\n"
      "v_fma_f32 v49, s44, v37, -v41\n"
      "v_mfma_f32_16x16x32_bf16 v[28:31], a[28:31], v[168:171], v[28:31]\n"
      "s_waitcnt lgkmcnt(6)\n"
      "ds_write_addtid_b32 v56 offset:5632\n"
      "v_cvt_pk_bf16_f32 v48, v48, v49\n"
      "v_mfma_f32_16x16x32_bf16 v[28:31], a[32:35], v[224:227], v[28:31]\n"
      "ds_write_addtid_b32 v57 offset:42560\n"
      "v_fma_f32 v50, s44, v38, -v42\n"
      "v_mfma_f32_16x16x32_bf16 v[28:31], a[0:3], v[132:135], v[28:31]\n"
      "s_waitcnt lgkmcnt(6)\n"
      "ds_write_addtid_b32 v60 offset:4864\n"
      "v_fma_f32 v51, s44, v39, -v43\n"
      "v_mfma_f32_16x16x32_bf16 v[28:31], a[4:7], v[188:191], v[28:31]\n"
      "ds_write_addtid_b32 v61 offset:41792\n"
      "v_cvt_pk_bf16_f32 v49, v50, v51\n"
      "v_mfma_f32_16x16x32_bf16 v[28:31], a[8:11], a[240:243], v[28:31]\n"
      "s_mov_b32 m0, s45\n"
      "v_fmac_f32_e32 v40, s44, v36\n"
      "v_fmac_f32_e64 v41, s44, v37\n"
      "v_mfma_f32_16x16x32_bf16 v[20:23], a[12:15], v[104:107], 0\n"
      "s_add_u32 m0, s58, 13280\n"
      "v_cvt_pk_bf16_f32 v40, v40, v41\n"
      "v_fmac_f32_e64 v42, s44, v38\n"
      "v_mfma_f32_16x16x32_bf16 v[20:23], a[16:19], v[156:159], v[20:23]\n"
      "buffer_load_dwordx4 v18, s[32:35], s53 offen offset:2048 lds\n"
      "v_fmac_f32_e32 v43, s44, v39\n"
      "v_mfma_f32_16x16x32_bf16 v[20:23], a[20:23], v[212:215], v[20:23]\n"
      "s_mov_b32 m0, s45\n"
      "v_cvt_pk_bf16_f32 v41, v42, v43\n"
      "v_cvt_pk_bf16_f32 v44, v44, v45\n"
      "v_mfma_f32_16x16x32_bf16 v[20:23], a[24:27], v[120:123], v[20:23]\n"
      "ds_write_addtid_b32 v24 offset:33792\n"
      "v_cvt_pk_bf16_f32 v45, v46, v47\n"
      "v_mfma_f32_16x16x32_bf16 v[20:23], a[28:31], v[176:179], v[20:23]\n"
      "ds_write_addtid_b32 v25 offset:34048\n"
      "v_cvt_pk_bf16_f32 v28, v28, v29\n"
      "v_mfma_f32_16x16x32_bf16 v[20:23], a[32:35], a[232:235], v[20:23]\n"
      "s_mov_b32 m0, s18\n"
      "ds_read2_b64 a[68:71], v13 offset0:1 offset1:65\n"
      "v_cvt_pk_bf16_f32 v29, v30, v31\n"
      "v_mfma_f32_16x16x32_bf16 v[20:23], a[0:3], v[140:143], v[20:23]\n"
      "s_sub_u32 s18, s19, s18\n"
      "ds_write_addtid_b32 v48 offset:6656\n"
      "v_cvt_pk_bf16_f32 v82, v76, v77\n"
      "v_mfma_f32_16x16x32_bf16 v[20:23], a[4:7], a[236:239], v[20:23]\n"
      "ds_write_addtid_b32 v49 offset:43584\n"
      "v_cvt_pk_bf16_f32 v88, v78, v79\n"
      "v_mfma_f32_16x16x32_bf16 v[20:23], a[8:11], v[248:251], v[20:23]\n"
      "ds_write_addtid_b32 v40 offset:5888\n"
      "s_nop 0\n"
      "v_mfma_f32_16x16x32_bf16 v[68:71], a[84:87], a[60:63], 0\n"
      "ds_write_addtid_b32 v41 offset:42816\n"
      "s_nop 0\n"
      "v_mfma_f32_16x16x32_bf16 v[32:35], a[12:15], v[108:111], 0\n"
      "ds_write_addtid_b32 v44 offset:5120\n"
      "v_mfma_f32_16x16x32_bf16 v[32:35], a[16:19], v[164:167], v[32:35]\n"
      "ds_write_addtid_b32 v45 offset:42048\n"
      "v_cvt_pk_bf16_f32 v20, v20, v21\n"
      "v_mfma_f32_16x16x32_bf16 v[32:35], a[20:23], a[224:227], v[32:35]\n"
      "s_mov_b32 m0, s45\n"
      "v_cvt_pk_bf16_f32 v83, v68, v69\n"
      "v_cvt_pk_bf16_f32 v21, v22, v23\n"
      "v_mfma_f32_16x16x32_bf16 v[32:35], a[24:27], v[128:131], v[32:35]\n"
      "ds_write_addtid_b32 v28 offset:59408\n"
      "v_cvt_pk_bf16_f32 v89, v70, v71\n"
      "v_mfma_f32_16x16x32_bf16 v[32:35], a[28:31], a[228:231], v[32:35]\n"
      "ds_write_addtid_b32 v29 offset:59664\n"
      "v_mfma_f32_16x16x32_bf16 v[32:35], a[32:35], v[236:239], v[32:35]\n"
      "ds_write_addtid_b32 v20 offset:47112\n"
      "v_mfma_f32_16x16x32_bf16 v[32:35], a[0:3], v[144:147], v[32:35]\n"
      "buffer_store_dwordx4 v[80:83], v14, s[36:39], s52 offen sc1\n"
      "v_mfma_f32_16x16x32_bf16 v[32:35], a[4:7], v[200:203], v[32:35]\n"
      "ds_write_addtid_b32 v21 offset:47368\n"
      "v_mfma_f32_16x16x32_bf16 v[32:35], a[8:11], a[244:247], v[32:35]\n"
      "s_nop 1\n"
      "v_mfma_f32_16x16x32_bf16 v[72:75], a[84:87], a[64:67], 0\n"
      "s_waitcnt lgkmcnt(10)\n"
      "s_nop 1\n"
      "v_mfma_f32_16x16x32_bf16 v[76:79], a[84:87], a[68:71], 0\n"
      "buffer_store_dwordx4 v[86:89], v14, s[36:39], s52 offen offset:1536 sc1\n"
      "v_cvt_pk_bf16_f32 v32, v32, v33\n"
      "s_cbranch_vccnz Lconv_loop1\n"
      "Lconv_t1:\n"
      "ds_write_addtid_b32 v32 offset:34816\n"
      "v_cvt_pk_bf16_f32 v33, v34, v35\n"
      "v_mfma_f32_16x16x32_bf16 v[20:23], a[24:27], v[96:99], 0\n"
      "ds_write_addtid_b32 v33 offset:35072\n"
      "v_cvt_pk_bf16_f32 v84, v72, v73\n"
      "v_mfma_f32_16x16x32_bf16 v[20:23], a[28:31], v[152:155], v[20:23]\n"
      "v_cvt_pk_bf16_f32 v90, v74, v75\n"
      "v_cvt_pk_bf16_f32 v85, v76, v77\n"
      "v_mfma_f32_16x16x32_bf16 v[20:23], a[32:35], a[220:223], v[20:23]\n"
      "buffer_store_dwordx2 v[84:85], v15, s[36:39], s52 offen sc1\n"
      "v_cvt_pk_bf16_f32 v91, v78, v79\n"
      "v_mfma_f32_16x16x32_bf16 v[20:23], a[0:3], v[116:119], v[20:23]\n"
      "v_mfma_f32_16x16x32_bf16 v[20:23], a[4:7], v[168:171], v[20:23]\n"
      "v_mfma_f32_16x16x32_bf16 v[20:23], a[8:11], v[224:227], v[20:23]\n"
      "buffer_store_dwordx2 v[90:91], v15, s[36:39], s52 offen offset:1536 sc1\n"
      "v_mfma_f32_16x16x32_bf16 v[24:27], a[24:27], v[104:107], 0\n"
      "s_mov_b32 s52, s13\n"
      "v_mfma_f32_16x16x32_bf16 v[24:27], a[28:31], v[156:159], v[24:27]\n"
      "s_add_u32 s13, s13, s12\n"
      "v_mfma_f32_16x16x32_bf16 v[24:27], a[32:35], v[212:215], v[24:27]\n"
      "v_mfma_f32_16x16x32_bf16 v[24:27], a[0:3], v[120:123], v[24:27]\n"
      "v_mfma_f32_16x16x32_bf16 v[24:27], a[4:7], v[176:179], v[24:27]\n"
      "s_waitcnt lgkmcnt(0)\n"
      "s_barrier\n"
      "ds_read2_b64 a[60:63], v10 offset0:0 offset1:64\n"
      "v_mfma_f32_16x16x32_bf16 v[24:27], a[8:11], a[232:235], v[24:27]\n"
      "s_waitcnt lgkmcnt(0)\n"
      "ds_read2_b64 a[64:67], v10 offset0:1 offset1:65\n"
      "v_mfma_f32_16x16x32_bf16 v[68:71], a[84:87], a[60:63], 0\n"
      "ds_read2_b64 a[68:71], v10 offset0:2 offset1:66\n"
      "v_mfma_f32_16x16x32_bf16 v[28:31], a[24:27], v[108:111], 0\n"
      "ds_read_b128 a[12:15], v7 offset:6144\n"
      "v_mfma_f32_16x16x32_bf16 v[28:31], a[28:31], v[164:167], v[28:31]\n"
      "ds_read_b128 a[16:19], v7 offset:6400\n"
      "v_mfma_f32_16x16x32_bf16 v[28:31], a[32:35], a[224:227], v[28:31]\n"
      "ds_read_b128 a[20:23], v7 offset:6656\n"
      "v_mfma_f32_16x16x32_bf16 v[28:31], a[0:3], v[128:131], v[28:31]\n"
      "ds_read2_b64 a[60:63], v10 offset0:3 offset1:67\n"
      "v_cvt_pk_bf16_f32 v80, v68, v69\n"
      "v_mfma_f32_16x16x32_bf16 v[28:31], a[4:7], a[228:231], v[28:31]\n"
      "ds_read_b128 a[24:27], v6 offset:5376\n"
      "v_cvt_pk_bf16_f32 v86, v70, v71\n"
      "v_mfma_f32_16x16x32_bf16 v[28:31], a[8:11], v[236:239], v[28:31]\n"
      "s_waitcnt lgkmcnt(6)\n"
      "ds_read_b128 a[28:31], v6 offset:5632\n"
      "v_mfma_f32_16x16x32_bf16 v[72:75], a[84:87], a[64:67], 0\n"
      "s_waitcnt lgkmcnt(6)\n"
      "ds_read2_b64 a[64:67], v12 offset0:0 offset1:64\n"
      "v_mfma_f32_16x16x32_bf16 v[76:79], a[84:87], a[68:71], 0\n"
      "s_waitcnt lgkmcnt(6)\n"
      "ds_read2_b64 a[68:71], v12 offset0:1 offset1:65\n"
      "s_nop 0\n"
      "v_mfma_f32_16x16x32_bf16 v[20:23], a[12:15], v[132:135], v[20:23]\n"
      "s_waitcnt lgkmcnt(6)\n"
      "ds_read_b128 a[32:35], v6 offset:5888\n"
      "v_cvt_pk_bf16_f32 v81, v72, v73\n"
      "v_mfma_f32_16x16x32_bf16 v[20:23], a[16:19], v[188:191], v[20:23]\n"
      "s_waitcnt lgkmcnt(6)\n"
      "ds_read_b128 a[0:3], v7 offset:768\n"
      "v_cvt_pk_bf16_f32 v82, v76, v77\n"
      "v_mfma_f32_16x16x32_bf16 v[20:23], a[20:23], a[240:243], v[20:23]\n"
      "s_waitcnt lgkmcnt(6)\n"
      "ds_read_b128 a[4:7], v7 offset:1024\n"
      "v_cvt_pk_bf16_f32 v87, v74, v75\n"
      "v_mfma_f32_16x16x32_bf16 v[68:71], a[84:87], a[60:63], 0\n"
      "ds_read_b128 a[8:11], v7 offset:1280\n"
      "v_cvt_pk_bf16_f32 v88, v78, v79\n"
      "v_mfma_f32_16x16x32_bf16 v[24:27], a[12:15], v[140:143], v[24:27]\n"
      "v_mfma_f32_16x16x32_bf16 v[24:27], a[16:19], a[236:239], v[24:27]\n"
      "v_cvt_pk_bf16_f32 v20, v20, v21\n"
      "v_cvt_pk_bf16_f32 v21, v22, v23\n"
      "v_mfma_f32_16x16x32_bf16 v[24:27], a[20:23], v[248:251], v[24:27]\n"
      "s_waitcnt lgkmcnt(5)\n"
      "ds_write_addtid_b32 v20 offset:62480\n"
      "v_cvt_pk_bf16_f32 v83, v68, v69\n"
      "v_mfma_f32_16x16x32_bf16 v[72:75], a[84:87], a[64:67], 0\n"
      "buffer_store_dwordx4 v[80:83], v14, s[36:39], s52 offen sc1\n"
      "v_cvt_pk_bf16_f32 v89, v70, v71\n"
      "v_mfma_f32_16x16x32_bf16 v[28:31], a[12:15], v[144:147], v[28:31]\n"
      "ds_read_b128 a[12:15], v7 offset:5376\n"
      "v_cvt_pk_bf16_f32 v24, v24, v25\n"
      "v_mfma_f32_16x16x32_bf16 v[28:31], a[16:19], v[200:203], v[28:31]\n"
      "ds_read_b128 a[16:19], v7 offset:5632\n"
      "v_cvt_pk_bf16_f32 v25, v26, v27\n"
      "v_mfma_f32_16x16x32_bf16 v[28:31], a[20:23], a[244:247], v[28:31]\n"
      "s_waitcnt lgkmcnt(4)\n"
      "ds_read_b128 a[20:23], v7 offset:5888\n"
      "v_cvt_pk_bf16_f32 v84, v72, v73\n"
      "v_mfma_f32_16x16x32_bf16 v[76:79], a[84:87], a[68:71], 0\n"
      "ds_write_addtid_b32 v21 offset:62736\n"
      "v_cvt_pk_bf16_f32 v90, v74, v75\n"
      "v_mfma_f32_16x16x32_bf16 v[32:35], a[24:27], v[92:95], 0\n"
      "buffer_store_dwordx4 v[86:89], v14, s[36:39], s52 offen offset:1536 sc1\n"
      "v_cvt_pk_bf16_f32 v28, v28, v29\n"
      "v_mfma_f32_16x16x32_bf16 v[32:35], a[28:31], v[148:151], v[32:35]\n"
      "ds_write_addtid_b32 v24 offset:50184\n"
      "v_cvt_pk_bf16_f32 v29, v30, v31\n"
      "v_mfma_f32_16x16x32_bf16 v[32:35], a[32:35], v[204:207], v[32:35]\n"
      "ds_write_addtid_b32 v25 offset:50440\n"
      "v_cvt_pk_bf16_f32 v85, v76, v77\n"
      "v_mfma_f32_16x16x32_bf16 v[32:35], a[0:3], v[112:115], v[32:35]\n"
      "ds_write_addtid_b32 v28 offset:37888\n"
      "v_cvt_pk_bf16_f32 v91, v78, v79\n"
      "v_mfma_f32_16x16x32_bf16 v[32:35], a[4:7], a[208:211], v[32:35]\n"
      "s_waitcnt lgkmcnt(6)\n"
      "ds_write_addtid_b32 v29 offset:38144\n"
      "v_mfma_f32_16x16x32_bf16 v[32:35], a[8:11], v[220:223], v[32:35]\n"
      "buffer_store_dwordx2 v[84:85], v15, s[36:39], s52 offen sc1\n"
      "v_mfma_f32_16x16x32_bf16 v[32:35], a[12:15], a[212:215], v[32:35]\n"
      "s_waitcnt lgkmcnt(5)\n"
      "v_mfma_f32_16x16x32_bf16 v[32:35], a[16:19], v[184:187], v[32:35]\n"
      "v_mfma_f32_16x16x32_bf16 v[32:35], a[20:23], v[240:243], v[32:35]\n"
      "buffer_store_dwordx2 v[90:91], v15, s[36:39], s52 offen offset:1536 sc1\n"
      "s_nop 0\n"
      "v_mfma_f32_16x16x32_bf16 v[20:23], a[24:27], v[100:103], 0\n"
      "s_nop 1\n"
      "v_mfma_f32_16x16x32_bf16 v[20:23], a[28:31], a[200:203], v[20:23]\n"
      "s_nop 0\n"
      "v_mfma_f32_16x16x32_bf16 v[20:23], a[32:35], v[208:211], v[20:23]\n"
      "v_cvt_pk_bf16_f32 v32, v32, v33\n"
      "v_cvt_pk_bf16_f32 v33, v34, v35\n"
      "v_mfma_f32_16x16x32_bf16 v[20:23], a[0:3], a[204:207], v[20:23]\n"
      "ds_write_addtid_b32 v32 offset:61456\n"
      "v_mfma_f32_16x16x32_bf16 v[20:23], a[4:7], v[172:175], v[20:23]\n"
      "ds_write_addtid_b32 v33 offset:61712\n"
      "v_mfma_f32_16x16x32_bf16 v[20:23], a[8:11], v[228:231], v[20:23]\n"
      "v_mfma_f32_16x16x32_bf16 v[20:23], a[12:15], v[136:139], v[20:23]\n"
      "v_mfma_f32_16x16x32_bf16 v[20:23], a[16:19], v[192:195], v[20:23]\n"
      "v_mfma_f32_16x16x32_bf16 v[20:23], a[20:23], v[244:247], v[20:23]\n"
      "s_nop 1\n"
      "v_mfma_f32_16x16x32_bf16 v[24:27], a[24:27], a[196:199], 0\n"
      "ds_read_b128 a[24:27], v7 offset:4608\n"
      "s_nop 0\n"
      "v_mfma_f32_16x16x32_bf16 v[24:27], a[28:31], v[160:163], v[24:27]\n"
      "ds_read_b128 a[28:31], v7 offset:4864\n"
      "v_mfma_f32_16x16x32_bf16 v[24:27], a[32:35], v[216:219], v[24:27]\n"
      "ds_read_b128 a[32:35], v7 offset:5120\n"
      "v_cvt_pk_bf16_f32 v20, v20, v21\n"
      "v_mfma_f32_16x16x32_bf16 v[24:27], a[0:3], v[124:127], v[24:27]\n"
      "ds_read_b128 a[0:3], v6 offset:4608\n"
      "v_cvt_pk_bf16_f32 v21, v22, v23\n"
      "v_mfma_f32_16x16x32_bf16 v[24:27], a[4:7], v[180:183], v[24:27]\n"
      "ds_read_b128 a[4:7], v6 offset:4864\n"
      "v_mfma_f32_16x16x32_bf16 v[24:27], a[8:11], v[232:235], v[24:27]\n"
      "ds_read_b128 a[8:11], v6 offset:5120\n"
      "v_mfma_f32_16x16x32_bf16 v[24:27], a[12:15], a[216:219], v[24:27]\n"
      "ds_read_b128 a[12:15], v7 offset:0\n"
      "v_mfma_f32_16x16x32_bf16 v[24:27], a[16:19], v[196:199], v[24:27]\n"
      "ds_read_b128 a[16:19], v7 offset:256\n"
      "v_mfma_f32_16x16x32_bf16 v[24:27], a[20:23], v[252:255], v[24:27]\n"
      "s_waitcnt lgkmcnt(7)\n"
      "ds_read_b128 a[20:23], v7 offset:512\n"
      "s_nop 0\n"
      "v_mfma_f32_16x16x32_bf16 v[28:31], a[24:27], a[160:163], 0\n"
      "s_waitcnt lgkmcnt(7)\n"
      "ds_write_addtid_b32 v20 offset:49160\n"
      "s_nop 0\n"
      "v_mfma_f32_16x16x32_bf16 v[28:31], a[28:31], a[172:175], v[28:31]\n"
      "s_waitcnt lgkmcnt(7)\n"
      "ds_write_addtid_b32 v21 offset:49416\n"
      "v_cvt_pk_bf16_f32 v24, v24, v25\n"
      "v_mfma_f32_16x16x32_bf16 v[28:31], a[32:35], a[184:187], v[28:31]\n"
      "s_waitcnt lgkmcnt(7)\n"
      "ds_write_addtid_b32 v24 offset:36864\n"
      "v_cvt_pk_bf16_f32 v25, v26, v27\n"
      "v_mfma_f32_16x16x32_bf16 v[28:31], a[0:3], a[88:91], v[28:31]\n"
      "s_waitcnt lgkmcnt(7)\n"
      "ds_write_addtid_b32 v25 offset:37120\n"
      "v_mfma_f32_16x16x32_bf16 v[28:31], a[4:7], a[100:103], v[28:31]\n"
      "s_waitcnt lgkmcnt(7)\n"
      "v_mfma_f32_16x16x32_bf16 v[28:31], a[8:11], a[112:115], v[28:31]\n"
      "s_waitcnt lgkmcnt(6)\n"
      "v_mfma_f32_16x16x32_bf16 v[28:31], a[12:15], a[124:127], v[28:31]\n"
      "s_waitcnt lgkmcnt(5)\n"
      "v_mfma_f32_16x16x32_bf16 v[28:31], a[16:19], a[136:139], v[28:31]\n"
      "s_waitcnt lgkmcnt(4)\n"
      "v_mfma_f32_16x16x32_bf16 v[28:31], a[20:23], a[148:151], v[28:31]\n"
      "s_nop 1\n"
      "v_mfma_f32_16x16x32_bf16 v[20:23], a[24:27], a[164:167], 0\n"
      "s_nop 1\n"
      "v_mfma_f32_16x16x32_bf16 v[20:23], a[28:31], a[176:179], v[20:23]\n"
      "s_nop 0\n"
      "v_mfma_f32_16x16x32_bf16 v[20:23], a[32:35], a[188:191], v[20:23]\n"
      "v_cvt_pk_bf16_f32 v28, v28, v29\n"
      "v_cvt_pk_bf16_f32 v29, v30, v31\n"
      "v_mfma_f32_16x16x32_bf16 v[20:23], a[0:3], a[92:95], v[20:23]\n"
      "ds_write_addtid_b32 v28 offset:60432\n"
      "v_mfma_f32_16x16x32_bf16 v[20:23], a[4:7], a[104:107], v[20:23]\n"
      "ds_write_addtid_b32 v29 offset:60688\n"
      "v_mfma_f32_16x16x32_bf16 v[20:23], a[8:11], a[116:119], v[20:23]\n"
      "v_mfma_f32_16x16x32_bf16 v[20:23], a[12:15], a[128:131], v[20:23]\n"
      "v_mfma_f32_16x16x32_bf16 v[20:23], a[16:19], a[140:143], v[20:23]\n"
      "v_mfma_f32_16x16x32_bf16 v[20:23], a[20:23], a[152:155], v[20:23]\n"
      "s_nop 1\n"
      "v_mfma_f32_16x16x32_bf16 v[32:35], a[24:27], a[168:171], 0\n"
      "s_nop 1\n"
      "v_mfma_f32_16x16x32_bf16 v[32:35], a[28:31], a[180:183], v[32:35]\n"
      "s_nop 0\n"
      "v_mfma_f32_16x16x32_bf16 v[32:35], a[32:35], a[192:195], v[32:35]\n"
      "v_cvt_pk_bf16_f32 v20, v20, v21\n"
      "v_cvt_pk_bf16_f32 v21, v22, v23\n"
      "v_mfma_f32_16x16x32_bf16 v[32:35], a[0:3], a[96:99], v[32:35]\n"
      "ds_write_addtid_b32 v20 offset:48136\n"
      "v_mfma_f32_16x16x32_bf16 v[32:35], a[4:7], a[108:111], v[32:35]\n"
      "ds_write_addtid_b32 v21 offset:48392\n"
      "v_mfma_f32_16x16x32_bf16 v[32:35], a[8:11], a[120:123], v[32:35]\n"
      "v_mfma_f32_16x16x32_bf16 v[32:35], a[12:15], a[132:135], v[32:35]\n"
      "v_mfma_f32_16x16x32_bf16 v[32:35], a[16:19], a[144:147], v[32:35]\n"
      "v_mfma_f32_16x16x32_bf16 v[32:35], a[20:23], a[156:159], v[32:35]\n"
      "s_nop 1\n"
      "s_nop 5\n"
      "v_cvt_pk_bf16_f32 v32, v32, v33\n"
      "v_cvt_pk_bf16_f32 v33, v34, v35\n"
      "ds_write_addtid_b32 v32 offset:35840\n"
      "ds_write_addtid_b32 v33 offset:36096\n"
      "s_waitcnt lgkmcnt(0)\n"
      "s_barrier\n"
      "ds_read2_b64 a[0:3], v11 offset0:0 offset1:64\n"
      "ds_read2_b64 a[4:7], v11 offset0:1 offset1:65\n"
      "ds_read2_b64 a[8:11], v11 offset0:2 offset1:66\n"
      "ds_read2_b64 a[12:15], v11 offset0:3 offset1:67\n"
      "ds_read2_b64 a[16:19], v13 offset0:0 offset1:64\n"
      "ds_read2_b64 a[20:23], v13 offset0:1 offset1:65\n"
      "s_waitcnt lgkmcnt(5)\n"
      "v_mfma_f32_16x16x32_bf16 v[68:71], a[84:87], a[0:3], 0\n"
      "s_waitcnt lgkmcnt(4)\n"
      "v_mfma_f32_16x16x32_bf16 v[72:75], a[84:87], a[4:7], 0\n"
      "s_waitcnt lgkmcnt(3)\n"
      "v_mfma_f32_16x16x32_bf16 v[76:79], a[84:87], a[8:11], 0\n"
      "s_waitcnt lgkmcnt(2)\n"
      "s_nop 2\n"
      "v_cvt_pk_bf16_f32 v80, v68, v69\n"
      "v_cvt_pk_bf16_f32 v86, v70, v71\n"
      "v_mfma_f32_16x16x32_bf16 v[68:71], a[84:87], a[12:15], 0\n"
      "s_waitcnt lgkmcnt(1)\n"
      "v_cvt_pk_bf16_f32 v81, v72, v73\n"
      "v_cvt_pk_bf16_f32 v87, v74, v75\n"
      "v_cvt_pk_bf16_f32 v82, v76, v77\n"
      "v_cvt_pk_bf16_f32 v88, v78, v79\n"
      "v_mfma_f32_16x16x32_bf16 v[72:75], a[84:87], a[16:19], 0\n"
      "s_waitcnt lgkmcnt(0)\n"
      "v_mfma_f32_16x16x32_bf16 v[76:79], a[84:87], a[20:23], 0\n"
      "s_mov_b32 s52, s13\n"
      "v_cvt_pk_bf16_f32 v83, v68, v69\n"
      "v_cvt_pk_bf16_f32 v89, v70, v71\n"
      "s_add_u32 s13, s13, s12\n"
      "buffer_store_dwordx4 v[80:83], v14, s[36:39], s52 offen sc1\n"
      "buffer_store_dwordx4 v[86:89], v14, s[36:39], s52 offen offset:1536 sc1\n"
      "v_cvt_pk_bf16_f32 v84, v72, v73\n"
      "v_cvt_pk_bf16_f32 v90, v74, v75\n"
      "v_cvt_pk_bf16_f32 v85, v76, v77\n"
      "v_cvt_pk_bf16_f32 v91, v78, v79\n"
      "buffer_store_dwordx2 v[84:85], v15, s[36:39], s52 offen sc1\n"
      "buffer_store_dwordx2 v[90:91], v15, s[36:39], s52 offen offset:1536 sc1\n"
      "Lconv_u1:\n"
      "Lconv_tail:\n"
      "s_waitcnt vmcnt(0) lgkmcnt(0)\n"
      "Lconv_end:\n"
      :
      : "v"(threadIdx.x), "s"(blockIdx.x), "s"(__builtin_amdgcn_kernarg_segment_ptr())
      : "v1", "v2", "v3", "v4", "v5", "v6", "v7", "v8", "v9", "v10", "v11", "v12", "v13", "v14", "v15", "v16", "v17", "v18", "v19", "v20", "v21", "v22", "v23", "v24", "v25", "v26", "v27", "v28", "v29", "v30", "v31", "v32", "v33", "v34", "v35", "v36", "v37", "v38", "v39", "v40", "v41", "v42", "v43", "v44", "v45", "v46", "v47", "v48", "v49", "v50", "v51", "v52", "v53", "v54", "v55", "v56", "v57", "v58", "v59", "v60", "v61", "v62", "v63", "v64", "v65", "v66", "v67", "v68", "v69", "v70", "v71", "v72", "v73", "v74", "v75", "v76", "v77", "v78", "v79", "v80", "v81", "v82", "v83", "v84", "v85", "v86", "v87", "v88", "v89", "v90", "v91", "v92", "v93", "v94", "v95", "v96", "v97", "v98", "v99", "v100", "v101", "v102", "v103", "v104", "v105", "v106", "v107", "v108", "v109", "v110", "v111", "v112", "v113", "v114", "v115", "v116", "v117", "v118", "v119", "v120", "v121", "v122", "v123", "v124", "v125", "v126", "v127", "v128", "v129", "v130", "v131", "v132", "v133", "v134", "v135", "v136", "v137", "v138", "v139", "v140", "v141", "v142", "v143", "v144", "v145", "v146", "v147", "v148", "v149", "v150", "v151", "v152", "v153", "v154", "v155", "v156", "v157", "v158", "v159", "v160", "v161", "v162", "v163", "v164", "v165", "v166", "v167", "v168", "v169", "v170", "v171", "v172", "v173", "v174", "v175", "v176", "v177", "v178", "v179", "v180", "v181", "v182", "v183", "v184", "v185", "v186", "v187", "v188", "v189", "v190", "v191", "v192", "v193", "v194", "v195", "v196", "v197", "v198", "v199", "v200", "v201", "v202", "v203", "v204", "v205", "v206", "v207", "v208", "v209", "v210", "v211", "v212", "v213", "v214", "v215", "v216", "v217", "v218", "v219", "v220", "v221", "v222", "v223", "v224", "v225", "v226", "v227", "v228", "v229", "v230", "v231", "v232", "v233", "v234", "v235", "v236", "v237", "v238", "v239", "v240", "v241", "v242", "v243", "v244", "v245", "v246", "v247", "v248", "v249", "v250", "v251", "v252", "v253", "v254", "v255", "a0", "a1", "a2", "a3", "a4", "a5", "a6", "a7", "a8", "a9", "a10", "a11", "a12", "a13", "a14", "a15", "a16", "a17", "a18", "a19", "a20", "a21", "a22", "a23", "a24", "a25", "a26", "a27", "a28", "a29", "a30", "a31", "a32", "a33", "a34", "a35", "a36", "a37", "a38", "a39", "a40", "a41", "a42", "a43", "a44", "a45", "a46", "a47", "a48", "a49", "a50", "a51", "a52", "a53", "a54", "a55", "a56", "a57", "a58", "a59", "a60", "a61", "a62", "a63", "a64", "a65", "a66", "a67", "a68", "a69", "a70", "a71", "a72", "a73", "a74", "a75", "a76", "a77", "a78", "a79", "a80", "a81", "a82", "a83", "a84", "a85", "a86", "a87", "a88", "a89", "a90", "a91", "a92", "a93", "a94", "a95", "a96", "a97", "a98", "a99", "a100", "a101", "a102", "a103", "a104", "a105", "a106", "a107", "a108", "a109", "a110", "a111", "a112", "a113", "a114", "a115", "a116", "a117", "a118", "a119", "a120", "a121", "a122", "a123", "a124", "a125", "a126", "a127", "a128", "a129", "a130", "a131", "a132", "a133", "a134", "a135", "a136", "a137", "a138", "a139", "a140", "a141", "a142", "a143", "a144", "a145", "a146", "a147", "a148", "a149", "a150", "a151", "a152", "a153", "a154", "a155", "a156", "a157", "a158", "a159", "a160", "a161", "a162", "a163", "a164", "a165", "a166", "a167", "a168", "a169", "a170", "a171", "a172", "a173", "a174", "a175", "a176", "a177", "a178", "a179", "a180", "a181", "a182", "a183", "a184", "a185", "a186", "a187", "a188", "a189", "a190", "a191", "a192", "a193", "a194", "a195", "a196", "a197", "a198", "a199", "a200", "a201", "a202", "a203", "a204", "a205", "a206", "a207", "a208", "a209", "a210", "a211", "a212", "a213", "a214", "a215", "a216", "a217", "a218", "a219", "a220", "a221", "a222", "a223", "a224", "a225", "a226", "a227", "a228", "a229", "a230", "a231", "a232", "a233", "a234", "a235", "a236", "a237", "a238", "a239", "a240", "a241", "a242", "a243", "a244", "a245", "a246", "a247", "a248", "a249", "a250", "a251", "a252", "a253", "a254", "a255", "s3", "s4", "s5", "s6", "s7", "s8", "s9", "s10", "s11", "s12", "s13", "s14", "s15", "s16", "s17", "s18", "s19", "s20", "s21", "s22", "s23", "s24", "s25", "s26", "s27", "s28", "s29", "s30", "s31", "s34", "s35", "s36", "s37", "s38", "s39", "s40", "s41", "s42", "s43", "s44", "s45", "s46", "s47", "s48", "s49", "s50", "s51", "s52", "s53", "s54", "s55", "s56", "s57", "s58", "s59", "s60", "s61", "s62", "s63", "s64", "s65", "s66", "s67", "s68", "s69", "s70", "s71", "s72", "s73", "s74", "s75", "s76", "s77", "s78", "s79", "s80", "s81", "s82", "s83", "s84", "s85", "s86", "s87", "s88", "s89", "s90", "s91", "s92", "s93", "s94", "s95", "s96", "s97", "s98", "s99", "scc", "vcc", "memory");
}

extern "C" void kernel_launch(void* const* d_in, const int* in_sizes, int n_in, void* d_out,
                              int out_size, void* d_ws, size_t ws_size, hipStream_t stream) {
  conv_kernel<<<256, 256, 0, stream>>>((const char*)d_in[0], (const char*)d_in[1], (char*)d_out, (char*)d_ws);
}
```

```cpp
#include <hip/hip_runtime.h>
#include <hip/hip_bf16.h>
#include <stdint.h>

__global__ void __launch_bounds__(256, 1) __attribute__((aligned(4096)))
conv_kernel(const char* __restrict__ x, const char* __restrict__ w, char* __restrict__ out, char* __restrict__ dbg) {
  __shared__ __attribute__((aligned(1024))) char smem[163840];
  asm volatile("" :: "v"((unsigned)(uintptr_t)smem) : "memory");
  asm volatile(
      "s_load_dwordx8 s[20:27], %2, 0x0\n"
      "v_readfirstlane_b32 s40, %0\n"
      "v_and_b32 v5, 63, %0\n"
      "v_lshlrev_b32 v8, 4, v5\n"
      "s_lshr_b32 s40, s40, 6\n"
      "s_lshl_b32 s43, s40, 3\n"
      "s_or_b32 s43, s43, 0x80000\n"
      "v_bitop3_b32 v9, v8, v5, 32 bitop3:0x78\n"
      "v_xor_b32 v89, 32, v8\n"
      "s_mul_i32 s92, s40, 3072\n"
      "s_lshr_b32 s14, %1, 7\n"
      "s_sub_u32 s14, s14, 1\n"
      "s_lshr_b32 s41, s40, 1\n"
      "s_and_b32 s42, s40, 1\n"
      "s_mov_b32 s30, 165888\n"
      "s_mov_b32 s31, 0x20000\n"
      "s_lshr_b32 s49, %1, 3\n"
      "s_lshl2_add_u32 s49, s49, s40\n"
      "v_lshlrev_b32 v36, 7, v5\n"
      "s_mul_i32 s50, s49, 384\n"
      "v_min_u32 v37, 256, v36\n"
      "v_add_u32 v37, s50, v37\n"
      "v_min_u32 v66, 1280, v36\n"
      "s_mul_i32 s96, s49, 1408\n"
      "s_getpc_b64 s[54:55]\n"
      "Lconv_start:\n"
      "v_min_u32 v37, Lconv_end-Lconv_start-4, v37\n"
      "s_mov_b32 m0, 162816\n"
      "s_nop 0\n"
      "global_load_lds_dword v37, s[54:55]\n"
      "s_bfe_u32 s99, %1, 0x30003\n"
      "s_mul_i32 s49, s40, 192\n"
      "v_add_u32 v36, s49, v5\n"
      "v_add_u32 v38, 128, v36\n"
      "v_mul_u32_u24 v37, 669, v36\n"
      "v_lshrrev_b32 v37, 16, v37\n"
      "v_mad_i32_i24 v37, v37, -2, v38\n"
      "v_lshlrev_b32 v16, 4, v37\n"
      "v_add_u32 v36, 64, v36\n"
      "v_mul_u32_u24 v37, 669, v36\n"
      "v_lshrrev_b32 v37, 16, v37\n"
      "v_mad_i32_i24 v37, v37, -2, v38\n"
      "v_lshlrev_b32 v17, 4, v37\n"
      "v_add_u32 v36, 64, v36\n"
      "v_mul_u32_u24 v37, 669, v36\n"
      "v_lshrrev_b32 v37, 16, v37\n"
      "v_mad_i32_i24 v37, v37, -2, v38\n"
      "v_lshlrev_b32 v18, 4, v37\n"
      "s_or_b32 s50, s99, s40\n"
      "s_cmp_eq_u32 s50, 0\n"
      "s_cselect_b32 s16, 0xffffff, 0\n"
      "s_mov_b32 s17, 0\n"
      "s_cmp_eq_u32 s99, 7\n"
      "s_cselect_b32 s19, 0xffffff00, 0\n"
      "s_mov_b32 s18, 0\n"
      "v_add_u32 v19, 14080, v8\n"
      "v_cndmask_b32 v16, v16, 2.0, s[16:17]\n"
      "v_cndmask_b32 v19, v19, 2.0, s[18:19]\n"
      "v_and_b32 v36, 16, v8\n"
      "v_mov_b32 v52, 0xffff\n"
      "v_lshlrev_b32 v52, v36, v52\n"
      "s_mov_b32 s16, 0x3000003\n"
      "s_mov_b32 s17, 0x3000003\n"
      "v_cndmask_b32 v48, 0, v52, s[16:17]\n"
      "s_lshl_b64 s[16:17], s[16:17], 2\n"
      "v_cndmask_b32 v49, 0, v52, s[16:17]\n"
      "s_lshl_b64 s[16:17], s[16:17], 2\n"
      "v_cndmask_b32 v50, 0, v52, s[16:17]\n"
      "s_lshl_b64 s[16:17], s[16:17], 2\n"
      "v_cndmask_b32 v51, 0, v52, s[16:17]\n"
      "v_bfe_u32 v53, v5, 5, 1\n"
      "v_xor_b32 v53, s41, v53\n"
      "v_sub_u32 v53, 0, v53\n"
      "v_and_b32 v36, 0x1000100, v53\n"
      "v_xor_b32 v36, 0x3fbf3fbf, v36\n"
      "v_and_b32 v68, v36, v48\n"
      "v_and_b32 v69, v36, v49\n"
      "v_and_b32 v70, v36, v50\n"
      "v_and_b32 v71, v36, v51\n"
      "v_xor_b32 v72, 0x80008000, v68\n"
      "v_xor_b32 v73, 0x80008000, v69\n"
      "v_xor_b32 v74, 0x80008000, v70\n"
      "v_xor_b32 v75, 0x80008000, v71\n"
      "v_bfe_i32 v36, v5, 5, 1\n"
      "v_and_b32 v36, 0x3f3f3f3f, v36\n"
      "v_xor_b32 v37, 0x3f3f3f3f, v36\n"
      "v_and_b32 v76, v37, v48\n"
      "v_and_b32 v77, v37, v49\n"
      "v_and_b32 v78, v37, v50\n"
      "v_and_b32 v79, v37, v51\n"
      "v_and_b32 v80, v36, v48\n"
      "v_and_b32 v81, v36, v49\n"
      "v_and_b32 v82, v36, v50\n"
      "v_and_b32 v83, v36, v51\n"
      "s_mul_i32 s50, s42, 96\n"
      "v_bfe_u32 v36, v5, 1, 3\n"
      "v_and_b32 v37, 1, v5\n"
      "v_lshl_add_u32 v36, v37, 3, v36\n"
      "v_mul_u32_u24 v36, 192, v36\n"
      "v_add_u32 v36, 46144, v36\n"
      "v_lshlrev_b32 v37, 5, v37\n"
      "v_and_b32 v38, 16, v5\n"
      "v_add_u32 v38, s50, v38\n"
      "v_and_b32 v39, 32, v5\n"
      "v_mul_u32_u24 v40, 192, v39\n"
      "v_add_u32 v40, v40, v36\n"
      "v_xad_u32 v84, v38, v37, v40\n"
      "v_add_u32 v41, 32, v38\n"
      "v_xad_u32 v85, v41, v37, v40\n"
      "v_add_u32 v41, v39, v38\n"
      "v_xad_u32 v86, v41, v37, v36\n"
      "s_mul_i32 s49, s41, 6144\n"
      "s_add_u32 s49, s49, 46144\n"
      "v_lshrrev_b32 v36, 3, v5\n"
      "v_and_b32 v38, 4, v5\n"
      "v_lshl_add_u32 v36, v38, 1, v36\n"
      "v_mul_u32_u24 v36, 192, v36\n"
      "v_lshlrev_b32 v37, 3, v5\n"
      "v_and_b32 v38, 8, v37\n"
      "v_add3_u32 v36, v36, v38, s49\n"
      "v_and_b32 v38, 32, v37\n"
      "v_and_b32 v37, 16, v37\n"
      "v_add_u32 v37, s50, v37\n"
      "v_xad_u32 v87, v37, v38, v36\n"
      "v_add_u32 v39, 32, v37\n"
      "v_xad_u32 v88, v39, v38, v36\n"
      "s_waitcnt lgkmcnt(0)\n"
      "s_mov_b32 s28, s22\n"
      "s_and_b32 s29, s23, 0xffff\n"
      "buffer_load_dword v66, s[28:31], s96 offen lds\n"
      "s_add_u32 m0, s92, 73824\n"
      "s_and_b32 s97, 0x3c2c24, s14\n"
      "s_xor_b32 s97, 0x120c0600, s97\n"
      "s_bfe_u32 s97, s97, s43\n"
      "s_mul_i32 s97, s97, 3072\n"
      "buffer_load_dwordx4 v8, s[28:31], s97 offen offset:0 lds\n"
      "buffer_load_dwordx4 v9, s[28:31], s97 offen offset:1024 lds\n"
      "buffer_load_dwordx4 v89, s[28:31], s97 offen offset:2048 lds\n"
      "s_add_u32 m0, s92, 86112\n"
      "s_and_b32 s97, 0x2c240000, s14\n"
      "s_xor_b32 s97, 0x2a241e18, s97\n"
      "s_bfe_u32 s97, s97, s43\n"
      "s_mul_i32 s97, s97, 3072\n"
      "buffer_load_dwordx4 v8, s[28:31], s97 offen offset:0 lds\n"
      "buffer_load_dwordx4 v9, s[28:31], s97 offen offset:1024 lds\n"
      "buffer_load_dwordx4 v89, s[28:31], s97 offen offset:2048 lds\n"
      "s_and_b32 s53, %1, 7\n"
      "s_lshl_b32 s53, s53, 6\n"
      "s_bfe_u32 s50, %1, 0x10006\n"
      "s_add_u32 s53, s53, s50\n"
      "s_lshl1_add_u32 s50, s14, 32\n"
      "s_add_u32 s53, s53, s50\n"
      "s_mul_i32 s13, s53, 98304\n"
      "s_or_b32 s12, s14, 1\n"
      "s_mul_i32 s12, s12, 196608\n"
      "s_lshl_b32 s50, s12, 1\n"
      "s_sub_u32 s51, s13, s50\n"
      "s_mul_i32 s99, s99, 12288\n"
      "s_sub_u32 s50, s99, 2432\n"
      "s_ashr_i32 s49, s50, 31\n"
      "s_add_u32 s32, s20, s50\n"
      "s_addc_u32 s33, s21, s49\n"
      "s_and_b32 s33, s33, 0xffff\n"
      "s_sub_u32 s34, 50331648, s50\n"
      "s_mov_b32 s35, 0x20000\n"
      "s_add_u32 s46, s92, 110720\n"
      "v_lshrrev_b32 v36, 1, v5\n"
      "v_and_b32 v37, 4, v36\n"
      "v_and_or_b32 v37, v5, 2, v37\n"
      "v_mul_u32_u24 v37, 1568, v37\n"
      "v_xor_b32 v36, v36, v5\n"
      "v_and_b32 v36, 4, v36\n"
      "v_mad_u32_u24 v37, v36, 48, v37\n"
      "s_mul_i32 s50, s42, 1568\n"
      "s_add_u32 s50, s50, 110720\n"
      "v_and_b32 v36, 16, v5\n"
      "v_lshl_add_u32 v37, v36, 1, v37\n"
      "v_and_b32 v36, 16, v8\n"
      "v_add3_u32 v37, v37, v36, s50\n"
      "v_and_b32 v36, 32, v5\n"
      "v_mad_u32_u24 v38, v36, 24, v37\n"
      "v_mad_u32_u24 v39, v36, 25, v37\n"
      "s_mul_i32 s17, s42, 0xcc00cc00\n"
      "s_mov_b32 s16, 0\n"
      "v_mov_b32 v36, 768\n"
      "v_mov_b32 v37, 736\n"
      "s_mov_b64 exec, 0\n"
      "s_cmp_eq_u32 s40, 0\n"
      "s_cbranch_scc1 Lconv_p1\n"
      "s_cmp_eq_u32 s40, 1\n"
      "s_cbranch_scc1 Lconv_p2\n"
      "s_cmp_eq_u32 s40, 2\n"
      "s_cbranch_scc1 Lconv_pa0\n"
      "s_cmp_eq_u32 s40, 3\n"
      "s_cbranch_scc1 Lconv_pa1\n"
      "Lconv_r0:\n"
      "s_mov_b64 exec, -1\n"
      "s_add_u32 m0, s92, 98400\n"
      "s_and_b32 s97, 0x3c2c243c, s14\n"
      "s_xor_b32 s97, 0xd070130, s97\n"
      "s_bfe_u32 s97, s97, s43\n"
      "s_mul_i32 s97, s97, 3072\n"
      "buffer_load_dwordx4 v8, s[28:31], s97 offen offset:0 lds\n"
      "buffer_load_dwordx4 v9, s[28:31], s97 offen offset:1024 lds\n"
      "buffer_load_dwordx4 v89, s[28:31], s97 offen offset:2048 lds\n"
      "v_cndmask_b32 v36, v36, v37, s[16:17]\n"
      "v_add_u32 v39, v39, v36\n"
      "s_cmp_eq_u32 s41, 0\n"
      "s_cselect_b64 vcc, -1, 0\n"
      "v_add_u32 v40, 384, v38\n"
      "v_add_u32 v41, 384, v39\n"
      "v_cndmask_b32 v2, v40, v39, vcc\n"
      "v_cndmask_b32 v3, v39, v40, vcc\n"
      "v_cndmask_b32 v4, v41, v38, vcc\n"
      "v_and_b32 v36, 3, v5\n"
      "v_mul_u32_u24 v36, 18464, v36\n"
      "v_bfe_u32 v37, v5, 2, 2\n"
      "v_lshrrev_b32 v38, 1, v37\n"
      "v_xor_b32 v37, v37, v38\n"
      "v_lshl_add_u32 v36, v37, 6, v36\n"
      "s_mul_i32 s50, s41, 2304\n"
      "v_and_b32 v37, 48, v5\n"
      "v_add3_u32 v6, v36, v37, s50\n"
      "v_add_u32 v7, 9216, v6\n"
      "s_mov_b32 s16, 0xc0003\n"
      "s_mov_b32 s17, 0xc0003\n"
      "v_cndmask_b32 v48, 0, v52, s[16:17]\n"
      "s_lshl_b64 s[16:17], s[16:17], 4\n"
      "v_cndmask_b32 v49, 0, v52, s[16:17]\n"
      "s_lshl_b64 s[16:17], s[16:17], 4\n"
      "v_cndmask_b32 v50, 0, v52, s[16:17]\n"
      "s_lshl_b64 s[16:17], s[16:17], 4\n"
      "v_cndmask_b32 v51, 0, v52, s[16:17]\n"
      "v_and_b32 v36, 0xff00ff00, v53\n"
      "v_xor_b32 v36, 0xc080c080, v36\n"
      "v_and_b32 v38, v36, v48\n"
      "v_and_b32 v39, v36, v49\n"
      "v_and_b32 v40, v36, v50\n"
      "v_and_b32 v41, v36, v51\n"
      "v_accvgpr_write_b32 a72, v38\n"
      "v_accvgpr_write_b32 a73, v39\n"
      "v_accvgpr_write_b32 a74, v40\n"
      "v_accvgpr_write_b32 a75, v41\n"
      "v_and_b32 v36, 0xff00ff00, v53\n"
      "v_xor_b32 v36, 0xc000c000, v36\n"
      "v_and_b32 v38, v36, v48\n"
      "v_and_b32 v39, v36, v49\n"
      "v_and_b32 v40, v36, v50\n"
      "v_and_b32 v41, v36, v51\n"
      "v_accvgpr_write_b32 a76, v38\n"
      "v_accvgpr_write_b32 a77, v39\n"
      "v_accvgpr_write_b32 a78, v40\n"
      "v_accvgpr_write_b32 a79, v41\n"
      "v_and_b32 v36, 0x81008100, v53\n"
      "v_xor_b32 v36, 0x3f803f80, v36\n"
      "v_and_b32 v38, v36, v48\n"
      "v_and_b32 v39, v36, v49\n"
      "v_and_b32 v40, v36, v50\n"
      "v_and_b32 v41, v36, v51\n"
      "v_accvgpr_write_b32 a80, v38\n"
      "v_accvgpr_write_b32 a81, v39\n"
      "v_accvgpr_write_b32 a82, v40\n"
      "v_accvgpr_write_b32 a83, v41\n"
      "v_lshlrev_b32 v36, 5, v5\n"
      "v_and_b32 v36, 0x180, v36\n"
      "v_lshlrev_b32 v37, 3, v5\n"
      "v_and_b32 v37, 16, v37\n"
      "v_sub_u32 v38, 0x3eb7, v36\n"
      "v_add_u32 v39, 0x3d37, v36\n"
      "v_mov_b32 v40, 0x3f80\n"
      "s_mov_b64 vcc, 0xffff\n"
      "s_mov_b32 s16, 0xffff0000\n"
      "s_mov_b32 s17, 0xf0f0\n"
      "v_xor_b32 v41, 0x8000, v38\n"
      "v_cndmask_b32 v38, v38, v41, s[16:17]\n"
      "v_cndmask_b32 v38, v38, v40, vcc\n"
      "v_lshlrev_b32 v38, v37, v38\n"
      "v_xor_b32 v41, 0x8000, v39\n"
      "s_xor_b32 s17, s17, 0xffff\n"
      "v_cndmask_b32 v39, v39, v41, s[16:17]\n"
      "v_cndmask_b32 v39, v39, v40, vcc\n"
      "v_lshlrev_b32 v39, v37, v39\n"
      "s_mov_b32 s18, 0x55550005\n"
      "s_mov_b32 s19, 0x5555\n"
      "v_cndmask_b32 v42, 0, v38, s[18:19]\n"
      "s_lshl_b64 s[16:17], s[18:19], 1\n"
      "v_cndmask_b32 v43, 0, v38, s[16:17]\n"
      "v_accvgpr_write_b32 a84, v42\n"
      "v_accvgpr_write_b32 a85, v43\n"
      "s_mov_b32 s18, 0x55555000\n"
      "v_cndmask_b32 v42, 0, v39, s[18:19]\n"
      "s_lshl_b64 s[16:17], s[18:19], 1\n"
      "v_cndmask_b32 v43, 0, v39, s[16:17]\n"
      "v_accvgpr_write_b32 a86, v42\n"
      "v_accvgpr_write_b32 a87, v43\n"
      "v_lshrrev_b32 v36, 4, v5\n"
      "v_min_u32 v37, 2, v36\n"
      "v_lshlrev_b32 v37, 10, v37\n"
      "s_lshl_b32 s50, s41, 7\n"
      "s_lshl_b32 s49, s42, 8\n"
      "s_add_u32 s50, s50, s49\n"
      "s_add_u32 s50, s50, 98416\n"
      "v_and_b32 v38, 8, v5\n"
      "v_lshl_add_u32 v37, v38, 3, v37\n"
      "v_add_u32 v37, s50, v37\n"
      "s_movk_i32 s50, -12296\n"
      "s_mov_b32 s49, 43040\n"
      "v_mov_b32 v42, s49\n"
      "v_bfe_u32 v39, v5, 1, 2\n"
      "v_lshrrev_b32 v40, 2, v38\n"
      "v_add_u32 v39, v39, v40\n"
      "v_lshlrev_b32 v40, 5, v5\n"
      "v_and_b32 v40, 32, v40\n"
      "v_cmp_lt_u32 vcc, 2, v39\n"
      "v_mad_i32_i24 v39, v39, s50, v37\n"
      "v_add_u32 v39, v39, v40\n"
      "v_cndmask_b32 v41, 0, v42, vcc\n"
      "v_add_u32 v10, v39, v41\n"
      "v_add_u32 v11, 3072, v10\n"
      "v_bfe_u32 v39, v5, 2, 1\n"
      "v_lshrrev_b32 v40, 1, v38\n"
      "v_sub_u32 v39, v39, v40\n"
      "v_add_u32 v39, 4, v39\n"
      "v_and_b32 v40, 48, v8\n"
      "v_cmp_lt_u32 vcc, 2, v39\n"
      "v_mad_i32_i24 v39, v39, s50, v37\n"
      "v_add_u32 v39, v39, v40\n"
      "v_cndmask_b32 v41, 0, v42, vcc\n"
      "v_add_u32 v12, v39, v41\n"
      "v_add_u32 v13, 3072, v12\n"
      "v_mul_u32_u24 v36, 384, v36\n"
      "v_and_b32 v37, 15, v5\n"
      "v_lshl_add_u32 v39, v37, 3, v36\n"
      "v_add_u32 v15, 128, v39\n"
      "v_add_lshl_u32 v37, v37, v38, 4\n"
      "v_add_u32 v14, v37, v36\n"
      "s_add_u32 s50, s99, s92\n"
      "s_add_u32 s36, s24, s50\n"
      "s_addc_u32 s37, s25, 0\n"
      "s_and_b32 s37, s37, 0xffff\n"
      "s_sub_u32 s38, 50331648, s50\n"
      "s_mov_b32 s39, 0x20000\n"
      "s_mul_i32 s48, s12, 17\n"
      "s_mul_i32 s45, s42, 6152\n"
      "s_lshl_b32 s50, s41, 9\n"
      "s_add_u32 s45, s45, s50\n"
      "s_add_u32 s45, s45, 41056\n"
      "s_mov_b32 s98, 0\n"
      "s_mov_b32 s91, 26560\n"
      "s_mul_i32 s18, s42, 18464\n"
      "s_mul_i32 s50, s41, 2304\n"
      "s_add_u32 s18, s18, s50\n"
      "s_lshl1_add_u32 s19, s18, 9216\n"
      "s_add_u32 s58, s46, 26560\n"
      "s_lshl1_add_u32 s93, s46, 26560\n"
      "s_mul_i32 s59, s42, -13280\n"
      "s_add_u32 s59, s59, 136256\n"
      "s_lshl1_add_u32 s94, s59, 26560\n"
      "s_mul_i32 s50, s42, 26560\n"
      "s_add_u32 s59, s59, s50\n"
      "s_mul_i32 s15, s42, s12\n"
      "s_sub_u32 s95, s12, s15\n"
      "s_mov_b32 s52, 2.0\n"
      "s_cmp_lt_u32 s40, 3\n"
      "s_cselect_b32 s97, 27680, 0\n"
      "s_add_u32 s97, s97, s92\n"
      "s_add_u32 m0, s97, 36928\n"
      "s_and_b32 s97, 0x24000000, s14\n"
      "s_xor_b32 s97, 0x251f1913, s97\n"
      "s_bfe_u32 s97, s97, s43\n"
      "s_mul_i32 s97, s97, 3072\n"
      "buffer_load_dwordx4 v8, s[28:31], s97 offen offset:0 lds\n"
      "buffer_load_dwordx4 v9, s[28:31], s97 offen offset:1024 lds\n"
      "buffer_load_dwordx4 v89, s[28:31], s97 offen offset:2048 lds\n"
      "s_cmp_lt_u32 s40, 2\n"
      "s_cselect_b32 s97, 27680, 0\n"
      "s_add_u32 s97, s97, s92\n"
      "s_add_u32 m0, s97, 21536\n"
      "s_and_b32 s97, 0x24243c2c, s14\n"
      "s_xor_b32 s97, 0x802312b, s97\n"
      "s_bfe_u32 s97, s97, s43\n"
      "s_mul_i32 s97, s97, 3072\n"
      "buffer_load_dwordx4 v8, s[28:31], s97 offen offset:0 lds\n"
      "buffer_load_dwordx4 v9, s[28:31], s97 offen offset:1024 lds\n"
      "buffer_load_dwordx4 v89, s[28:31], s97 offen offset:2048 lds\n"
      "s_cmp_lt_u32 s40, 1\n"
      "s_cselect_b32 s97, 27680, 0\n"
      "s_add_u32 s97, s97, s92\n"
      "s_add_u32 m0, s97, 6144\n"
      "s_and_b32 s97, 0x3c, s14\n"
      "s_xor_b32 s97, 0x201a140e, s97\n"
      "s_bfe_u32 s97, s97, s43\n"
      "s_mul_i32 s97, s97, 3072\n"
      "buffer_load_dwordx4 v8, s[28:31], s97 offen offset:0 lds\n"
      "buffer_load_dwordx4 v9, s[28:31], s97 offen offset:1024 lds\n"
      "buffer_load_dwordx4 v89, s[28:31], s97 offen offset:2048 lds\n"
      "s_sub_u32 vcc_lo, 0, s41\n"
      "s_mov_b32 vcc_hi, 0\n"
      ".p2align 3\n"
      "s_waitcnt vmcnt(12)\n"
      "s_barrier\n"
      "s_cmp_lt_u32 s40, 3\n"
      "s_cselect_b32 s97, -9248, 0\n"
      "ds_read_b128 a[0:3], v86 offset:30752\n"
      "ds_read_b128 a[4:7], v84 offset:27680\n"
      "ds_read_b128 a[8:11], v85 offset:27680\n"
      "ds_read_b128 a[12:15], v86 offset:30816\n"
      "s_add_u32 s97, s97, s92\n"
      "s_add_u32 m0, s97, 9248\n"
      "s_and_b32 s97, 0x243c2424, s14\n"
      "s_xor_b32 s97, 0x3322c26, s97\n"
      "s_bfe_u32 s97, s97, s43\n"
      "s_mul_i32 s97, s97, 3072\n"
      "s_waitcnt lgkmcnt(3)\n"
      "v_mfma_f32_16x16x32_bf16 v[20:23], a[0:3], v[76:79], 0\n"
      "s_waitcnt lgkmcnt(2)\n"
      "ds_read_b128 a[16:19], v84 offset:27744\n"
      "ds_read_b128 a[20:23], v86 offset:39968\n"
      "buffer_load_dwordx4 v8, s[28:31], s97 offen offset:0 lds\n"
      "v_mfma_f32_16x16x32_bf16 v[24:27], a[4:7], v[68:71], v[20:23]\n"
      "ds_read_b128 a[24:27], v84 offset:36896\n"
      "ds_read_b128 a[28:31], v85 offset:36896\n"
      "v_mfma_f32_16x16x32_bf16 v[20:23], a[4:7], v[72:75], v[20:23]\n"
      "ds_read_b64_tr_b16 a[88:89], v87 offset:27680\n"
      "ds_read_b64_tr_b16 a[92:93], v88 offset:27680\n"
      "buffer_load_dwordx4 v9, s[28:31], s97 offen offset:1024 lds\n"
      "v_mfma_f32_16x16x32_bf16 v[28:31], a[0:3], v[80:83], 0\n"
      "s_waitcnt lgkmcnt(7)\n"
      "ds_read_b64_tr_b16 a[96:97], v87 offset:27744\n"
      "ds_read_b64_tr_b16 a[124:125], v87 offset:36896\n"
      "v_mfma_f32_16x16x32_bf16 v[32:35], a[8:11], v[68:71], v[28:31]\n"
      "ds_read_b64_tr_b16 a[128:129], v88 offset:36896\n"
      "ds_read_b128 a[32:35], v86 offset:40032\n"
      "v_mfma_f32_16x16x32_bf16 v[28:31], a[8:11], v[72:75], v[28:31]\n"
      "s_waitcnt lgkmcnt(10)\n"
      "ds_read_b128 a[0:3], v84 offset:36960\n"
      "ds_read_b64_tr_b16 a[132:133], v87 offset:36960\n"
      "buffer_load_dwordx4 v89, s[28:31], s97 offen offset:2048 lds\n"
      "v_mfma_f32_16x16x32_bf16 v[54:57], a[12:15], v[76:79], 0\n"
      "s_waitcnt lgkmcnt(10)\n"
      "s_cmp_lt_u32 s40, 2\n"
      "s_cselect_b32 s97, -9248, 0\n"
      "s_add_u32 s97, s97, s92\n"
      "s_add_u32 m0, s97, 30784\n"
      "s_and_b32 s97, 0x3c24, s14\n"
      "s_xor_b32 s97, 0x1b150f09, s97\n"
      "s_bfe_u32 s97, s97, s43\n"
      "s_mul_i32 s97, s97, 3072\n"
      "v_cvt_pk_bf16_f32 v96, v20, v21\n"
      "v_cvt_pk_bf16_f32 v97, v22, v23\n"
      "v_mfma_f32_16x16x32_bf16 v[58:61], a[16:19], v[68:71], v[54:57]\n"
      "v_cvt_pk_bf16_f32 v92, v24, v25\n"
      "v_cvt_pk_bf16_f32 v93, v26, v27\n"
      "v_mfma_f32_16x16x32_bf16 v[54:57], a[16:19], v[72:75], v[54:57]\n"
      "buffer_load_dwordx4 v8, s[28:31], s97 offen offset:0 lds\n"
      "v_cvt_pk_bf16_f32 v104, v28, v29\n"
      "v_cvt_pk_bf16_f32 v105, v30, v31\n"
      "v_mfma_f32_16x16x32_bf16 v[62:65], a[20:23], v[76:79], 0\n"
      "s_waitcnt lgkmcnt(4)\n"
      "v_cvt_pk_bf16_f32 v100, v32, v33\n"
      "v_cvt_pk_bf16_f32 v101, v34, v35\n"
      "v_mfma_f32_16x16x32_bf16 v[20:23], a[24:27], v[68:71], v[62:65]\n"
      "v_cvt_pk_bf16_f32 v108, v54, v55\n"
      "v_cvt_pk_bf16_f32 v109, v56, v57\n"
      "v_mfma_f32_16x16x32_bf16 v[62:65], a[24:27], v[72:75], v[62:65]\n"
      "s_waitcnt lgkmcnt(0)\n"
      "s_waitcnt vmcnt(13)\n"
      "s_cbranch_vccnz Lconv_h1\n"
      "s_barrier\n"
      "Lconv_h1:\n"
      "buffer_load_dwordx4 v9, s[28:31], s97 offen offset:1024 lds\n"
      "v_mfma_f32_16x16x32_bf16 v[24:27], a[20:23], v[80:83], 0\n"
      "v_cvt_pk_bf16_f32 v58, v58, v59\n"
      "v_cvt_pk_bf16_f32 v60, v60, v61\n"
      "v_mfma_f32_16x16x32_bf16 v[28:31], a[28:31], v[68:71], v[24:27]\n"
      "s_cbranch_vccz Lconv_h2\n"
      "s_barrier\n"
      "Lconv_h2:\n"
      "ds_read_b128 a[4:7], v86 offset:49184\n"
      "ds_read_b128 a[8:11], v84 offset:46112\n"
      "ds_read_b128 a[12:15], v85 offset:46112\n"
      "ds_read_b128 a[16:19], v86 offset:49248\n"
      "v_mfma_f32_16x16x32_bf16 v[24:27], a[28:31], v[72:75], v[24:27]\n"
      "ds_read_b128 a[20:23], v84 offset:46176\n"
      "ds_read_b128 a[24:27], v86 offset:58400\n"
      "buffer_load_dwordx4 v89, s[28:31], s97 offen offset:2048 lds\n"
      "v_accvgpr_write_b32 a196, v58\n"
      "v_accvgpr_write_b32 a197, v60\n"
      "v_mfma_f32_16x16x32_bf16 v[32:35], a[32:35], v[76:79], 0\n"
      "s_cmp_lt_u32 s40, 1\n"
      "s_cselect_b32 s97, -9248, 0\n"
      "ds_read_b128 a[28:31], v84 offset:55328\n"
      "ds_read_b128 a[32:35], v85 offset:55328\n"
      "s_add_u32 s97, s97, s92\n"
      "s_add_u32 m0, s97, 52320\n"
      "s_and_b32 s97, 0x3c242400, s14\n"
      "s_xor_b32 s97, 0x332d2721, s97\n"
      "s_bfe_u32 s97, s97, s43\n"
      "s_mul_i32 s97, s97, 3072\n"
      "v_cvt_pk_bf16_f32 v116, v62, v63\n"
      "v_cvt_pk_bf16_f32 v117, v64, v65\n"
      "v_mfma_f32_16x16x32_bf16 v[54:57], a[0:3], v[68:71], v[32:35]\n"
      "ds_read_b64_tr_b16 a[160:161], v87 offset:46112\n"
      "ds_read_b64_tr_b16 a[164:165], v88 offset:46112\n"
      "buffer_load_dwordx4 v8, s[28:31], s97 offen offset:0 lds\n"
      "v_cvt_pk_bf16_f32 v112, v20, v21\n"
      "v_cvt_pk_bf16_f32 v113, v22, v23\n"
      "v_mfma_f32_16x16x32_bf16 v[32:35], a[0:3], v[72:75], v[32:35]\n"
      "s_waitcnt lgkmcnt(9)\n"
      "ds_read_b64_tr_b16 a[168:169], v87 offset:46176\n"
      "ds_read_b64_tr_b16 a[90:91], v87 offset:55328\n"
      "v_cvt_pk_bf16_f32 v120, v24, v25\n"
      "v_cvt_pk_bf16_f32 v121, v26, v27\n"
      "v_mfma_f32_16x16x32_bf16 v[58:61], a[4:7], v[76:79], 0\n"
      "s_waitcnt lgkmcnt(10)\n"
      "ds_read_b64_tr_b16 a[94:95], v88 offset:55328\n"
      "ds_read_b128 a[0:3], v86 offset:58464\n"
      "v_mfma_f32_16x16x32_bf16 v[62:65], a[8:11], v[68:71], v[58:61]\n"
      "ds_read_b64_tr_b16 a[98:99], v87 offset:55392\n"
      "buffer_load_dwordx4 v9, s[28:31], s97 offen offset:1024 lds\n"
      "v_cvt_pk_bf16_f32 v28, v28, v29\n"
      "v_cvt_pk_bf16_f32 v30, v30, v31\n"
      "v_accvgpr_write_b32 a204, v28\n"
      "v_mfma_f32_16x16x32_bf16 v[58:61], a[8:11], v[72:75], v[58:61]\n"
      "v_accvgpr_write_b32 a205, v30\n"
      "v_cvt_pk_bf16_f32 v128, v32, v33\n"
      "v_cvt_pk_bf16_f32 v129, v34, v35\n"
      "v_mfma_f32_16x16x32_bf16 v[20:23], a[4:7], v[80:83], 0\n"
      "s_waitcnt lgkmcnt(7)\n"
      "ds_read_b128 a[4:7], v84 offset:55392\n"
      "v_cvt_pk_bf16_f32 v124, v54, v55\n"
      "v_mfma_f32_16x16x32_bf16 v[24:27], a[12:15], v[68:71], v[20:23]\n"
      "buffer_load_dwordx4 v89, s[28:31], s97 offen offset:2048 lds\n"
      "v_cvt_pk_bf16_f32 v125, v56, v57\n"
      "v_mfma_f32_16x16x32_bf16 v[20:23], a[12:15], v[72:75], v[20:23]\n"
      "s_add_u32 m0, s46, 13280\n"
      "s_add_u32 s51, s51, s12\n"
      "v_cvt_pk_bf16_f32 v132, v58, v59\n"
      "v_cvt_pk_bf16_f32 v133, v60, v61\n"
      "v_mfma_f32_16x16x32_bf16 v[28:31], a[16:19], v[76:79], 0\n"
      "v_cvt_pk_bf16_f32 v62, v62, v63\n"
      "v_cvt_pk_bf16_f32 v64, v64, v65\n"
      "v_mfma_f32_16x16x32_bf16 v[32:35], a[20:23], v[68:71], v[28:31]\n"
      "buffer_load_dwordx4 v16, s[32:35], s51 offen lds\n"
      "v_accvgpr_write_b32 a212, v62\n"
      "v_accvgpr_write_b32 a213, v64\n"
      "v_mfma_f32_16x16x32_bf16 v[28:31], a[20:23], v[72:75], v[28:31]\n"
      "s_add_u32 m0, s46, 13280\n"
      "v_cvt_pk_bf16_f32 v140, v20, v21\n"
      "v_cvt_pk_bf16_f32 v141, v22, v23\n"
      "v_mfma_f32_16x16x32_bf16 v[54:57], a[24:27], v[76:79], 0\n"
      "buffer_load_dwordx4 v17, s[32:35], s51 offen offset:1024 lds\n"
      "v_cvt_pk_bf16_f32 v136, v24, v25\n"
      "v_mfma_f32_16x16x32_bf16 v[58:61], a[28:31], v[68:71], v[54:57]\n"
      "s_add_u32 m0, s46, 13280\n"
      "s_add_u32 s96, s51, s15\n"
      "buffer_load_dwordx4 v18, s[32:35], s51 offen offset:2048 lds\n"
      "v_cvt_pk_bf16_f32 v137, v26, v27\n"
      "v_mfma_f32_16x16x32_bf16 v[54:57], a[28:31], v[72:75], v[54:57]\n"
      "s_waitcnt lgkmcnt(0)\n"
      "s_waitcnt vmcnt(12)\n"
      "s_cbranch_vccnz Lconv_h3\n"
      "s_barrier\n"
      "Lconv_h3:\n"
      "s_mov_b32 m0, s59\n"
      "v_cvt_pk_bf16_f32 v144, v28, v29\n"
      "v_cvt_pk_bf16_f32 v145, v30, v31\n"
      "v_mfma_f32_16x16x32_bf16 v[62:65], a[24:27], v[80:83], 0\n"
      "buffer_load_dwordx4 v19, s[32:35], s96 offen lds\n"
      "v_cvt_pk_bf16_f32 v32, v32, v33\n"
      "v_mfma_f32_16x16x32_bf16 v[20:23], a[32:35], v[68:71], v[62:65]\n"
      "s_cbranch_vccz Lconv_h4\n"
      "s_barrier\n"
      "Lconv_h4:\n"
      "s_add_u32 m0, s46, 26560\n"
      "s_add_u32 s51, s51, s12\n"
      "ds_read_b128 a[8:11], v86 offset:21536\n"
      "ds_read_b128 a[12:15], v84 offset:18464\n"
      "ds_read_b128 a[16:19], v85 offset:18464\n"
      "ds_read_b128 a[20:23], v86 offset:21600\n"
      "buffer_load_dwordx4 v16, s[32:35], s51 offen lds\n"
      "v_mfma_f32_16x16x32_bf16 v[62:65], a[32:35], v[72:75], v[62:65]\n"
      "s_add_u32 m0, s46, 26560\n"
      "ds_read_b128 a[24:27], v84 offset:18528\n"
      "ds_read_b128 a[28:31], v86 offset:3072\n"
      "buffer_load_dwordx4 v17, s[32:35], s51 offen offset:1024 lds\n"
      "v_cvt_pk_bf16_f32 v34, v34, v35\n"
      "v_accvgpr_write_b32 a216, v32\n"
      "v_mfma_f32_16x16x32_bf16 v[24:27], a[0:3], v[76:79], 0\n"
      "s_add_u32 m0, s46, 26560\n"
      "ds_read_b128 a[32:35], v84 offset:0\n"
      "ds_read_b128 a[0:3], v85 offset:0\n"
      "buffer_load_dwordx4 v18, s[32:35], s51 offen offset:2048 lds\n"
      "v_accvgpr_write_b32 a217, v34\n"
      "v_cvt_pk_bf16_f32 v98, v54, v55\n"
      "v_mfma_f32_16x16x32_bf16 v[28:31], a[4:7], v[68:71], v[24:27]\n"
      "s_add_u32 m0, s92, 73824\n"
      "s_and_b32 s97, 0x24242c, s14\n"
      "ds_read_b64_tr_b16 a[126:127], v87 offset:18464\n"
      "ds_read_b64_tr_b16 a[130:131], v88 offset:18464\n"
      "s_xor_b32 s97, 0x16100a04, s97\n"
      "s_bfe_u32 s97, s97, s43\n"
      "s_mul_i32 s97, s97, 3072\n"
      "buffer_load_dwordx4 v8, s[28:31], s97 offen offset:0 lds\n"
      "v_cvt_pk_bf16_f32 v99, v56, v57\n"
      "v_cvt_pk_bf16_f32 v94, v58, v59\n"
      "v_mfma_f32_16x16x32_bf16 v[24:27], a[4:7], v[72:75], v[24:27]\n"
      "s_waitcnt lgkmcnt(9)\n"
      "ds_read_b128 a[4:7], v86 offset:3136\n"
      "ds_read_b64_tr_b16 a[134:135], v87 offset:18528\n"
      "v_cvt_pk_bf16_f32 v95, v60, v61\n"
      "v_cvt_pk_bf16_f32 v106, v62, v63\n"
      "v_cvt_pk_bf16_f32 v107, v64, v65\n"
      "v_mfma_f32_16x16x32_bf16 v[32:35], a[8:11], v[76:79], 0\n"
      "s_waitcnt lgkmcnt(10)\n"
      "ds_read_b64_tr_b16 a[162:163], v87 offset:0\n"
      "ds_read_b64_tr_b16 a[166:167], v88 offset:0\n"
      "v_cvt_pk_bf16_f32 v102, v20, v21\n"
      "v_cvt_pk_bf16_f32 v103, v22, v23\n"
      "v_cvt_pk_bf16_f32 v110, v24, v25\n"
      "v_mfma_f32_16x16x32_bf16 v[54:57], a[12:15], v[68:71], v[32:35]\n"
      "ds_read_b64_tr_b16 a[170:171], v87 offset:64\n"
      "v_cvt_pk_bf16_f32 v111, v26, v27\n"
      "v_cvt_pk_bf16_f32 v28, v28, v29\n"
      "v_cvt_pk_bf16_f32 v30, v30, v31\n"
      "v_mfma_f32_16x16x32_bf16 v[32:35], a[12:15], v[72:75], v[32:35]\n"
      "buffer_load_dwordx4 v9, s[28:31], s97 offen offset:1024 lds\n"
      "v_accvgpr_write_b32 a198, v28\n"
      "v_accvgpr_write_b32 a199, v30\n"
      "v_mfma_f32_16x16x32_bf16 v[58:61], a[8:11], v[80:83], 0\n"
      "s_waitcnt lgkmcnt(7)\n"
      "ds_read_b128 a[8:11], v84 offset:64\n"
      "v_add_u32 v86, -46144, v86\n"
      "v_mfma_f32_16x16x32_bf16 v[62:65], a[16:19], v[68:71], v[58:61]\n"
      "Lconv_p1:\n"
      "s_cbranch_execz Lconv_r0\n"
      "s_nop 0\n"
      "ds_read_b128 a[12:15], v86 offset:30752\n"
      "v_cvt_pk_bf16_f32 v118, v32, v33\n"
      "v_cvt_pk_bf16_f32 v119, v34, v35\n"
      "v_cvt_pk_bf16_f32 v114, v54, v55\n"
      "v_cvt_pk_bf16_f32 v115, v56, v57\n"
      "v_mfma_f32_16x16x32_bf16 v[58:61], a[16:19], v[72:75], v[58:61]\n"
      "v_add_u32 v84, -46144, v84\n"
      "v_add_u32 v85, -46144, v85\n"
      "v_mfma_f32_16x16x32_bf16 v[20:23], a[20:23], v[76:79], 0\n"
      "ds_read_b128 a[16:19], v84 offset:27680\n"
      "ds_read_b128 a[20:23], v85 offset:27680\n"
      "buffer_load_dwordx4 v89, s[28:31], s97 offen offset:2048 lds\n"
      "v_mfma_f32_16x16x32_bf16 v[24:27], a[24:27], v[68:71], v[20:23]\n"
      "s_add_u32 m0, s92, 86112\n"
      "s_and_b32 s97, 0x242c0000, s14\n"
      "s_xor_b32 s97, 0x2e28221c, s97\n"
      "s_bfe_u32 s97, s97, s43\n"
      "s_mul_i32 s97, s97, 3072\n"
      "v_cvt_pk_bf16_f32 v122, v58, v59\n"
      "v_cvt_pk_bf16_f32 v123, v60, v61\n"
      "v_mfma_f32_16x16x32_bf16 v[20:23], a[24:27], v[72:75], v[20:23]\n"
      "ds_read_b128 a[24:27], v86 offset:30816\n"
      "v_cvt_pk_bf16_f32 v62, v62, v63\n"
      "v_cvt_pk_bf16_f32 v64, v64, v65\n"
      "v_mfma_f32_16x16x32_bf16 v[28:31], a[28:31], v[76:79], 0\n"
      "v_accvgpr_write_b32 a206, v62\n"
      "v_accvgpr_write_b32 a207, v64\n"
      "v_mfma_f32_16x16x32_bf16 v[32:35], a[32:35], v[68:71], v[28:31]\n"
      "v_add_u32 v87, -46144, v87\n"
      "v_cvt_pk_bf16_f32 v130, v20, v21\n"
      "v_mfma_f32_16x16x32_bf16 v[28:31], a[32:35], v[72:75], v[28:31]\n"
      "ds_read_b64_tr_b16 a[100:101], v87 offset:27680\n"
      "ds_read_b64_tr_b16 a[108:109], v87 offset:27744\n"
      "buffer_load_dwordx4 v8, s[28:31], s97 offen offset:0 lds\n"
      "v_cvt_pk_bf16_f32 v131, v22, v23\n"
      "v_add_u32 v88, -46144, v88\n"
      "v_mfma_f32_16x16x32_bf16 v[54:57], a[28:31], v[80:83], 0\n"
      "ds_read_b64_tr_b16 a[104:105], v88 offset:27680\n"
      "ds_read_b128 a[28:31], v84 offset:27744\n"
      "v_mfma_f32_16x16x32_bf16 v[58:61], a[0:3], v[68:71], v[54:57]\n"
      "v_cvt_pk_bf16_f32 v126, v24, v25\n"
      "v_cvt_pk_bf16_f32 v127, v26, v27\n"
      "v_mfma_f32_16x16x32_bf16 v[54:57], a[0:3], v[72:75], v[54:57]\n"
      "s_waitcnt lgkmcnt(4)\n"
      "v_cvt_pk_bf16_f32 v134, v28, v29\n"
      "v_cvt_pk_bf16_f32 v135, v30, v31\n"
      "v_mfma_f32_16x16x32_bf16 v[62:65], a[4:7], v[76:79], 0\n"
      "buffer_load_dwordx4 v9, s[28:31], s97 offen offset:1024 lds\n"
      "v_cvt_pk_bf16_f32 v32, v32, v33\n"
      "v_mfma_f32_16x16x32_bf16 v[20:23], a[8:11], v[68:71], v[62:65]\n"
      "v_cvt_pk_bf16_f32 v34, v34, v35\n"
      "v_accvgpr_write_b32 a214, v32\n"
      "v_accvgpr_write_b32 a215, v34\n"
      "v_mfma_f32_16x16x32_bf16 v[62:65], a[8:11], v[72:75], v[62:65]\n"
      "v_cvt_pk_bf16_f32 v142, v54, v55\n"
      "v_cvt_pk_bf16_f32 v143, v56, v57\n"
      "v_mfma_f32_16x16x32_bf16 v[24:27], a[12:15], v[76:79], 0\n"
      "v_cvt_pk_bf16_f32 v138, v58, v59\n"
      "v_cvt_pk_bf16_f32 v139, v60, v61\n"
      "v_mfma_f32_16x16x32_bf16 v[28:31], a[16:19], v[68:71], v[24:27]\n"
      "v_cvt_pk_bf16_f32 v20, v20, v21\n"
      "v_cvt_pk_bf16_f32 v22, v22, v23\n"
      "v_mfma_f32_16x16x32_bf16 v[24:27], a[16:19], v[72:75], v[24:27]\n"
      "s_waitcnt lgkmcnt(0)\n"
      "s_waitcnt vmcnt(12)\n"
      "s_cbranch_vccnz Lconv_h5\n"
      "s_barrier\n"
      "Lconv_h5:\n"
      "buffer_load_dwordx4 v89, s[28:31], s97 offen offset:2048 lds\n"
      "v_mfma_f32_16x16x32_bf16 v[32:35], a[12:15], v[80:83], 0\n"
      "s_add_u32 m0, s92, 98400\n"
      "s_and_b32 s97, 0x24242c24, s14\n"
      "s_xor_b32 s97, 0x110b0534, s97\n"
      "s_bfe_u32 s97, s97, s43\n"
      "s_mul_i32 s97, s97, 3072\n"
      "v_cvt_pk_bf16_f32 v146, v62, v63\n"
      "v_cvt_pk_bf16_f32 v147, v64, v65\n"
      "v_mfma_f32_16x16x32_bf16 v[54:57], a[20:23], v[68:71], v[32:35]\n"
      "s_cbranch_vccz Lconv_h6\n"
      "s_barrier\n"
      "Lconv_h6:\n"
      "ds_read_b128 a[32:35], v86 offset:12288\n"
      "ds_read_b128 a[0:3], v84 offset:9216\n"
      "ds_read_b128 a[4:7], v85 offset:9216\n"
      "ds_read_b128 a[8:11], v86 offset:12352\n"
      "v_mfma_f32_16x16x32_bf16 v[32:35], a[20:23], v[72:75], v[32:35]\n"
      "ds_read_b128 a[12:15], v84 offset:9280\n"
      "ds_read_b128 a[16:19], v86 offset:3072\n"
      "v_accvgpr_write_b32 a218, v20\n"
      "v_accvgpr_write_b32 a219, v22\n"
      "v_cvt_pk_bf16_f32 v152, v24, v25\n"
      "v_mfma_f32_16x16x32_bf16 v[58:61], a[24:27], v[76:79], 0\n"
      "ds_read_b128 a[20:23], v84 offset:0\n"
      "ds_read_b128 a[24:27], v85 offset:0\n"
      "buffer_load_dwordx4 v8, s[28:31], s97 offen offset:0 lds\n"
      "v_cvt_pk_bf16_f32 v153, v26, v27\n"
      "v_mfma_f32_16x16x32_bf16 v[62:65], a[28:31], v[68:71], v[58:61]\n"
      "ds_read_b64_tr_b16 a[136:137], v87 offset:9216\n"
      "ds_read_b64_tr_b16 a[140:141], v88 offset:9216\n"
      "v_cvt_pk_bf16_f32 v148, v28, v29\n"
      "v_cvt_pk_bf16_f32 v149, v30, v31\n"
      "v_mfma_f32_16x16x32_bf16 v[58:61], a[28:31], v[72:75], v[58:61]\n"
      "s_waitcnt lgkmcnt(9)\n"
      "ds_read_b128 a[28:31], v86 offset:3136\n"
      "ds_read_b64_tr_b16 a[144:145], v87 offset:9280\n"
      "v_cvt_pk_bf16_f32 v156, v32, v33\n"
      "v_cvt_pk_bf16_f32 v157, v34, v35\n"
      "v_mfma_f32_16x16x32_bf16 v[20:23], a[32:35], v[76:79], 0\n"
      "s_waitcnt lgkmcnt(10)\n"
      "ds_read_b64_tr_b16 a[172:173], v87 offset:0\n"
      "ds_read_b64_tr_b16 a[176:177], v88 offset:0\n"
      "v_mfma_f32_16x16x32_bf16 v[24:27], a[0:3], v[68:71], v[20:23]\n"
      "ds_read_b64_tr_b16 a[180:181], v87 offset:64\n"
      "ds_read_b64_tr_b16 a[102:103], v87 offset:18464\n"
      "v_cvt_pk_bf16_f32 v54, v54, v55\n"
      "v_cvt_pk_bf16_f32 v56, v56, v57\n"
      "v_accvgpr_write_b32 a200, v54\n"
      "v_mfma_f32_16x16x32_bf16 v[20:23], a[0:3], v[72:75], v[20:23]\n"
      "ds_read_b128 a[0:3], v86 offset:21536\n"
      "ds_read_b64_tr_b16 a[106:107], v88 offset:18464\n"
      "buffer_load_dwordx4 v9, s[28:31], s97 offen offset:1024 lds\n"
      "v_accvgpr_write_b32 a201, v56\n"
      "v_cvt_pk_bf16_f32 v164, v58, v59\n"
      "v_mfma_f32_16x16x32_bf16 v[28:31], a[32:35], v[80:83], 0\n"
      "s_waitcnt lgkmcnt(10)\n"
      "ds_read_b128 a[32:35], v84 offset:64\n"
      "ds_read_b64_tr_b16 a[110:111], v87 offset:18528\n"
      "v_cvt_pk_bf16_f32 v165, v60, v61\n"
      "v_mfma_f32_16x16x32_bf16 v[32:35], a[4:7], v[68:71], v[28:31]\n"
      "ds_read_b64_tr_b16 a[138:139], v87 offset:36928\n"
      "ds_read_b64_tr_b16 a[142:143], v88 offset:36928\n"
      "v_cvt_pk_bf16_f32 v160, v62, v63\n"
      "v_cvt_pk_bf16_f32 v161, v64, v65\n"
      "v_mfma_f32_16x16x32_bf16 v[28:31], a[4:7], v[72:75], v[28:31]\n"
      "ds_read_b128 a[4:7], v84 offset:18464\n"
      "ds_read_b64_tr_b16 a[146:147], v87 offset:36992\n"
      "v_cvt_pk_bf16_f32 v168, v20, v21\n"
      "v_cvt_pk_bf16_f32 v169, v22, v23\n"
      "v_mfma_f32_16x16x32_bf16 v[54:57], a[8:11], v[76:79], 0\n"
      "ds_read_b128 a[8:11], v85 offset:18464\n"
      "ds_read_b64_tr_b16 a[174:175], v87 offset:55392\n"
      "buffer_load_dwordx4 v89, s[28:31], s97 offen offset:2048 lds\n"
      "v_mfma_f32_16x16x32_bf16 v[58:61], a[12:15], v[68:71], v[54:57]\n"
      "s_cmp_lt_u32 s40, 3\n"
      "s_cselect_b32 s97, 27680, 0\n"
      "ds_read_b64_tr_b16 a[178:179], v88 offset:55392\n"
      "ds_read_b64_tr_b16 a[182:183], v87 offset:55456\n"
      "s_add_u32 s97, s97, s92\n"
      "s_add_u32 m0, s97, 36928\n"
      "s_and_b32 s97, 0x2c000000, s14\n"
      "s_xor_b32 s97, 0x29231d17, s97\n"
      "s_bfe_u32 s97, s97, s43\n"
      "s_mul_i32 s97, s97, 3072\n"
      "v_cvt_pk_bf16_f32 v24, v24, v25\n"
      "v_cvt_pk_bf16_f32 v26, v26, v27\n"
      "v_accvgpr_write_b32 a208, v24\n"
      "v_mfma_f32_16x16x32_bf16 v[54:57], a[12:15], v[72:75], v[54:57]\n"
      "ds_read_b128 a[12:15], v86 offset:21600\n"
      "v_accvgpr_write_b32 a209, v26\n"
      "v_cvt_pk_bf16_f32 v176, v28, v29\n"
      "v_cvt_pk_bf16_f32 v177, v30, v31\n"
      "v_cvt_pk_bf16_f32 v172, v32, v33\n"
      "v_mfma_f32_16x16x32_bf16 v[62:65], a[16:19], v[76:79], 0\n"
      "v_cvt_pk_bf16_f32 v173, v34, v35\n"
      "v_cvt_pk_bf16_f32 v180, v58, v59\n"
      "v_mfma_f32_16x16x32_bf16 v[20:23], a[20:23], v[68:71], v[62:65]\n"
      "v_cvt_pk_bf16_f32 v54, v54, v55\n"
      "v_cvt_pk_bf16_f32 v56, v56, v57\n"
      "v_mfma_f32_16x16x32_bf16 v[62:65], a[20:23], v[72:75], v[62:65]\n"
      "ds_read_b128 a[20:23], v86 offset:40000\n"
      "buffer_load_dwordx4 v8, s[28:31], s97 offen offset:0 lds\n"
      "v_accvgpr_write_b32 a228, v54\n"
      "v_mfma_f32_16x16x32_bf16 v[24:27], a[16:19], v[80:83], 0\n"
      "ds_read_b128 a[16:19], v84 offset:18528\n"
      "v_accvgpr_write_b32 a229, v56\n"
      "v_mfma_f32_16x16x32_bf16 v[28:31], a[24:27], v[68:71], v[24:27]\n"
      "v_cvt_pk_bf16_f32 v181, v60, v61\n"
      "v_cvt_pk_bf16_f32 v188, v62, v63\n"
      "v_mfma_f32_16x16x32_bf16 v[24:27], a[24:27], v[72:75], v[24:27]\n"
      "s_waitcnt lgkmcnt(2)\n"
      "ds_read_b128 a[24:27], v84 offset:36928\n"
      "v_cvt_pk_bf16_f32 v189, v64, v65\n"
      "v_cvt_pk_bf16_f32 v184, v20, v21\n"
      "v_cvt_pk_bf16_f32 v185, v22, v23\n"
      "v_mfma_f32_16x16x32_bf16 v[32:35], a[28:31], v[76:79], 0\n"
      "ds_read_b128 a[28:31], v85 offset:36928\n"
      "buffer_load_dwordx4 v9, s[28:31], s97 offen offset:1024 lds\n"
      "v_mfma_f32_16x16x32_bf16 v[54:57], a[32:35], v[68:71], v[32:35]\n"
      "v_cvt_pk_bf16_f32 v24, v24, v25\n"
      "v_cvt_pk_bf16_f32 v26, v26, v27\n"
      "v_mfma_f32_16x16x32_bf16 v[32:35], a[32:35], v[72:75], v[32:35]\n"
      "ds_read_b128 a[32:35], v86 offset:40064\n"
      "v_accvgpr_write_b32 a236, v24\n"
      "v_accvgpr_write_b32 a237, v26\n"
      "v_mfma_f32_16x16x32_bf16 v[58:61], a[0:3], v[76:79], 0\n"
      "v_cvt_pk_bf16_f32 v192, v28, v29\n"
      "v_cvt_pk_bf16_f32 v193, v30, v31\n"
      "v_mfma_f32_16x16x32_bf16 v[62:65], a[4:7], v[68:71], v[58:61]\n"
      "v_cvt_pk_bf16_f32 v196, v54, v55\n"
      "v_cvt_pk_bf16_f32 v200, v32, v33\n"
      "v_mfma_f32_16x16x32_bf16 v[58:61], a[4:7], v[72:75], v[58:61]\n"
      "ds_read_b128 a[4:7], v86 offset:58464\n"
      "buffer_load_dwordx4 v89, s[28:31], s97 offen offset:2048 lds\n"
      "v_mfma_f32_16x16x32_bf16 v[20:23], a[0:3], v[80:83], 0\n"
      "s_cmp_lt_u32 s40, 2\n"
      "s_cselect_b32 s97, 6144, 0\n"
      "ds_read_b128 a[0:3], v84 offset:36992\n"
      "s_add_u32 s97, s97, s92\n"
      "s_add_u32 m0, s97, 43072\n"
      "s_and_b32 s97, 0x24242424, s14\n"
      "s_xor_b32 s97, 0x352f352f, s97\n"
      "s_bfe_u32 s97, s97, s43\n"
      "s_mul_i32 s97, s97, 3072\n"
      "v_cvt_pk_bf16_f32 v201, v34, v35\n"
      "v_mfma_f32_16x16x32_bf16 v[24:27], a[8:11], v[68:71], v[20:23]\n"
      "v_cvt_pk_bf16_f32 v197, v56, v57\n"
      "v_cvt_pk_bf16_f32 v154, v58, v59\n"
      "v_cvt_pk_bf16_f32 v155, v60, v61\n"
      "v_mfma_f32_16x16x32_bf16 v[20:23], a[8:11], v[72:75], v[20:23]\n"
      "ds_read_b128 a[8:11], v84 offset:55392\n"
      "v_cvt_pk_bf16_f32 v150, v62, v63\n"
      "v_cvt_pk_bf16_f32 v151, v64, v65\n"
      "v_mfma_f32_16x16x32_bf16 v[28:31], a[12:15], v[76:79], 0\n"
      "s_waitcnt lgkmcnt(3)\n"
      "ds_read_b128 a[12:15], v85 offset:55392\n"
      "buffer_load_dwordx4 v8, s[28:31], s97 offen offset:0 lds\n"
      "v_cvt_pk_bf16_f32 v24, v24, v25\n"
      "v_mfma_f32_16x16x32_bf16 v[32:35], a[16:19], v[68:71], v[28:31]\n"
      "v_cvt_pk_bf16_f32 v158, v20, v21\n"
      "v_cvt_pk_bf16_f32 v159, v22, v23\n"
      "v_mfma_f32_16x16x32_bf16 v[28:31], a[16:19], v[72:75], v[28:31]\n"
      "ds_read_b128 a[16:19], v86 offset:58528\n"
      "v_cvt_pk_bf16_f32 v26, v26, v27\n"
      "v_accvgpr_write_b32 a202, v24\n"
      "v_accvgpr_write_b32 a203, v26\n"
      "v_mfma_f32_16x16x32_bf16 v[54:57], a[20:23], v[76:79], 0\n"
      "v_cvt_pk_bf16_f32 v162, v32, v33\n"
      "v_cvt_pk_bf16_f32 v163, v34, v35\n"
      "v_mfma_f32_16x16x32_bf16 v[58:61], a[24:27], v[68:71], v[54:57]\n"
      "v_cvt_pk_bf16_f32 v166, v28, v29\n"
      "v_cvt_pk_bf16_f32 v167, v30, v31\n"
      "v_mfma_f32_16x16x32_bf16 v[54:57], a[24:27], v[72:75], v[54:57]\n"
      "buffer_load_dwordx4 v9, s[28:31], s97 offen offset:1024 lds\n"
      "v_add_u32 v86, 46144, v86\n"
      "v_add_u32 v85, 46144, v85\n"
      "v_mfma_f32_16x16x32_bf16 v[62:65], a[20:23], v[80:83], 0\n"
      "ds_read_b128 a[20:23], v84 offset:55456\n"
      "v_cvt_pk_bf16_f32 v58, v58, v59\n"
      "v_mfma_f32_16x16x32_bf16 v[20:23], a[28:31], v[68:71], v[62:65]\n"
      "v_cvt_pk_bf16_f32 v60, v60, v61\n"
      "v_cvt_pk_bf16_f32 v170, v54, v55\n"
      "v_mfma_f32_16x16x32_bf16 v[62:65], a[28:31], v[72:75], v[62:65]\n"
      "v_cvt_pk_bf16_f32 v171, v56, v57\n"
      "v_accvgpr_write_b32 a210, v58\n"
      "v_mfma_f32_16x16x32_bf16 v[24:27], a[32:35], v[76:79], 0\n"
      "s_waitcnt lgkmcnt(1)\n"
      "buffer_load_dwordx4 v89, s[28:31], s97 offen offset:2048 lds\n"
      "v_accvgpr_write_b32 a211, v60\n"
      "v_mfma_f32_16x16x32_bf16 v[28:31], a[0:3], v[68:71], v[24:27]\n"
      "s_add_u32 m0, s46, 39840\n"
      "s_add_u32 s51, s51, s12\n"
      "v_add_u32 v84, 46144, v84\n"
      "v_cvt_pk_bf16_f32 v178, v62, v63\n"
      "v_mfma_f32_16x16x32_bf16 v[24:27], a[0:3], v[72:75], v[24:27]\n"
      "v_cvt_pk_bf16_f32 v179, v64, v65\n"
      "v_cvt_pk_bf16_f32 v174, v20, v21\n"
      "v_cvt_pk_bf16_f32 v175, v22, v23\n"
      "v_mfma_f32_16x16x32_bf16 v[32:35], a[4:7], v[76:79], 0\n"
      "v_cvt_pk_bf16_f32 v182, v28, v29\n"
      "v_cvt_pk_bf16_f32 v183, v30, v31\n"
      "v_mfma_f32_16x16x32_bf16 v[54:57], a[8:11], v[68:71], v[32:35]\n"
      "v_add_u32 v87, 46144, v87\n"
      "v_cvt_pk_bf16_f32 v24, v24, v25\n"
      "v_mfma_f32_16x16x32_bf16 v[32:35], a[8:11], v[72:75], v[32:35]\n"
      "s_waitcnt lgkmcnt(0)\n"
      "s_waitcnt vmcnt(6)\n"
      "s_cbranch_vccnz Lconv_h7\n"
      "s_barrier\n"
      "Lconv_h7:\n"
      "buffer_load_dwordx4 v16, s[32:35], s51 offen lds\n"
      "v_mfma_f32_16x16x32_bf16 v[58:61], a[4:7], v[80:83], 0\n"
      "s_add_u32 m0, s46, 39840\n"
      "v_cvt_pk_bf16_f32 v26, v26, v27\n"
      "v_accvgpr_write_b32 a230, v24\n"
      "v_mfma_f32_16x16x32_bf16 v[62:65], a[12:15], v[68:71], v[58:61]\n"
      "s_cbranch_vccz Lconv_h8\n"
      "s_barrier\n"
      "Lconv_h8:\n"
      "ds_read_b128 a[24:27], v86 offset:30752\n"
      "ds_read_b128 a[28:31], v84 offset:27680\n"
      "ds_read_b128 a[32:35], v85 offset:27680\n"
      "ds_read_b128 a[0:3], v86 offset:30816\n"
      "buffer_load_dwordx4 v17, s[32:35], s51 offen offset:1024 lds\n"
      "v_mfma_f32_16x16x32_bf16 v[58:61], a[12:15], v[72:75], v[58:61]\n"
      "s_add_u32 m0, s46, 39840\n"
      "ds_read_b128 a[4:7], v84 offset:27744\n"
      "ds_read_b128 a[8:11], v86 offset:39968\n"
      "buffer_load_dwordx4 v18, s[32:35], s51 offen offset:2048 lds\n"
      "v_accvgpr_write_b32 a231, v26\n"
      "v_cvt_pk_bf16_f32 v190, v32, v33\n"
      "v_mfma_f32_16x16x32_bf16 v[20:23], a[16:19], v[76:79], 0\n"
      "s_sub_u32 m0, s94, s59\n"
      "s_add_u32 s96, s51, s15\n"
      "ds_read_b128 a[12:15], v84 offset:36896\n"
      "ds_read_b128 a[16:19], v85 offset:36896\n"
      "buffer_load_dwordx4 v19, s[32:35], s96 offen lds\n"
      "v_cvt_pk_bf16_f32 v191, v34, v35\n"
      "v_cvt_pk_bf16_f32 v186, v54, v55\n"
      "v_mfma_f32_16x16x32_bf16 v[24:27], a[20:23], v[68:71], v[20:23]\n"
      "s_add_u32 m0, s46, 0\n"
      "s_add_u32 s51, s51, s12\n"
      "ds_read_b128 a[36:39], v2 offset:13280\n"
      "ds_read_b128 a[40:43], v3 offset:13280\n"
      "buffer_load_dwordx4 v16, s[32:35], s51 offen lds\n"
      "v_cvt_pk_bf16_f32 v187, v56, v57\n"
      "v_cvt_pk_bf16_f32 v58, v58, v59\n"
      "v_mfma_f32_16x16x32_bf16 v[20:23], a[20:23], v[72:75], v[20:23]\n"
      "s_waitcnt lgkmcnt(9)\n"
      "s_add_u32 m0, s46, 0\n"
      "ds_read_b128 a[20:23], v86 offset:40032\n"
      "ds_read_b128 a[44:47], v4 offset:13280\n"
      "buffer_load_dwordx4 v17, s[32:35], s51 offen offset:1024 lds\n"
      "v_cvt_pk_bf16_f32 v60, v60, v61\n"
      "v_accvgpr_write_b32 a238, v58\n"
      "v_mfma_f32_16x16x32_bf16 v[28:31], a[24:27], v[76:79], 0\n"
      "s_waitcnt lgkmcnt(10)\n"
      "s_add_u32 m0, s46, 0\n"
      "ds_read_b128 a[48:51], v2 offset:13344\n"
      "ds_read_b128 a[52:55], v3 offset:13344\n"
      "buffer_load_dwordx4 v18, s[32:35], s51 offen offset:2048 lds\n"
      "v_mfma_f32_16x16x32_bf16 v[32:35], a[28:31], v[68:71], v[28:31]\n"
      "s_mov_b32 m0, s18\n"
      "ds_read_b128 a[56:59], v4 offset:13344\n"
      "ds_read_b64_tr_b16 a[112:113], v87 offset:27680\n"
      "v_accvgpr_write_b32 a239, v60\n"
      "v_cvt_pk_bf16_f32 v194, v62, v63\n"
      "v_mfma_f32_16x16x32_bf16 v[28:31], a[28:31], v[72:75], v[28:31]\n"
      "ds_read_b128 a[28:31], v86 offset:49184\n"
      "ds_read_b64_tr_b16 a[120:121], v87 offset:27744\n"
      "v_cvt_pk_bf16_f32 v195, v64, v65\n"
      "v_cvt_pk_bf16_f32 v202, v20, v21\n"
      "v_cvt_pk_bf16_f32 v203, v22, v23\n"
      "v_mfma_f32_16x16x32_bf16 v[54:57], a[24:27], v[80:83], 0\n"
      "s_waitcnt lgkmcnt(10)\n"
      "ds_read_b128 a[24:27], v84 offset:36960\n"
      "ds_read_b64_tr_b16 a[148:149], v87 offset:36896\n"
      "v_cvt_pk_bf16_f32 v198, v24, v25\n"
      "v_mfma_f32_16x16x32_bf16 v[58:61], a[32:35], v[68:71], v[54:57]\n"
      "ds_read_b64_tr_b16 a[156:157], v87 offset:36960\n"
      "ds_read_b64_tr_b16 a[184:185], v87 offset:46112\n"
      "v_cvt_pk_bf16_f32 v199, v26, v27\n"
      "v_cvt_pk_bf16_f32 v28, v28, v29\n"
      "v_mfma_f32_16x16x32_bf16 v[54:57], a[32:35], v[72:75], v[54:57]\n"
      "ds_read_b128 a[32:35], v84 offset:46112\n"
      "ds_read_b64_tr_b16 a[192:193], v87 offset:46176\n"
      "v_cvt_pk_bf16_f32 v30, v30, v31\n"
      "v_accvgpr_write_b32 a220, v28\n"
      "v_mfma_f32_16x16x32_bf16 v[62:65], a[0:3], v[76:79], 0\n"
      "ds_read_b128 a[0:3], v85 offset:46112\n"
      "ds_read_b64_tr_b16 a[114:115], v87 offset:55328\n"
      "v_mfma_f32_16x16x32_bf16 v[20:23], a[4:7], v[68:71], v[62:65]\n"
      "ds_read_b64_tr_b16 a[122:123], v87 offset:55392\n"
      "v_accvgpr_write_b32 a221, v30\n"
      "v_cvt_pk_bf16_f32 v204, v32, v33\n"
      "v_mfma_f32_16x16x32_bf16 v[62:65], a[4:7], v[72:75], v[62:65]\n"
      "ds_read_b128 a[4:7], v86 offset:49248\n"
      "v_cvt_pk_bf16_f32 v205, v34, v35\n"
      "v_cvt_pk_bf16_f32 v212, v54, v55\n"
      "v_cvt_pk_bf16_f32 v213, v56, v57\n"
      "v_cvt_pk_bf16_f32 v208, v58, v59\n"
      "v_mfma_f32_16x16x32_bf16 v[24:27], a[8:11], v[76:79], 0\n"
      "v_cvt_pk_bf16_f32 v209, v60, v61\n"
      "v_cvt_pk_bf16_f32 v216, v20, v21\n"
      "v_mfma_f32_16x16x32_bf16 v[28:31], a[12:15], v[68:71], v[24:27]\n"
      "v_cvt_pk_bf16_f32 v62, v62, v63\n"
      "v_cvt_pk_bf16_f32 v64, v64, v65\n"
      "v_mfma_f32_16x16x32_bf16 v[24:27], a[12:15], v[72:75], v[24:27]\n"
      "ds_read_b128 a[12:15], v86 offset:58400\n"
      "v_accvgpr_write_b32 a224, v62\n"
      "v_mfma_f32_16x16x32_bf16 v[32:35], a[8:11], v[80:83], 0\n"
      "ds_read_b128 a[8:11], v84 offset:46176\n"
      "v_accvgpr_write_b32 a225, v64\n"
      "v_mfma_f32_16x16x32_bf16 v[54:57], a[16:19], v[68:71], v[32:35]\n"
      "v_cvt_pk_bf16_f32 v217, v22, v23\n"
      "v_cvt_pk_bf16_f32 v220, v28, v29\n"
      "v_mfma_f32_16x16x32_bf16 v[32:35], a[16:19], v[72:75], v[32:35]\n"
      "s_waitcnt lgkmcnt(2)\n"
      "ds_read_b128 a[16:19], v84 offset:55328\n"
      "v_cvt_pk_bf16_f32 v224, v24, v25\n"
      "v_cvt_pk_bf16_f32 v225, v26, v27\n"
      "v_mfma_f32_16x16x32_bf16 v[58:61], a[20:23], v[76:79], 0\n"
      "ds_read_b128 a[20:23], v85 offset:55328\n"
      "v_cvt_pk_bf16_f32 v228, v54, v55\n"
      "v_mfma_f32_16x16x32_bf16 v[62:65], a[24:27], v[68:71], v[58:61]\n"
      "v_cvt_pk_bf16_f32 v229, v56, v57\n"
      "v_cvt_pk_bf16_f32 v221, v30, v31\n"
      "v_mfma_f32_16x16x32_bf16 v[58:61], a[24:27], v[72:75], v[58:61]\n"
      "ds_read_b128 a[24:27], v86 offset:58464\n"
      "v_cvt_pk_bf16_f32 v32, v32, v33\n"
      "v_mfma_f32_16x16x32_bf16 v[36:39], a[36:39], a[72:75], 0\n"
      "ds_read_b128 a[36:39], v2 offset:13408\n"
      "v_cvt_pk_bf16_f32 v34, v34, v35\n"
      "v_mfma_f32_16x16x32_bf16 v[40:43], a[40:43], a[76:79], 0\n"
      "ds_read_b128 a[40:43], v3 offset:13408\n"
      "v_cvt_pk_bf16_f32 v232, v62, v63\n"
      "v_cvt_pk_bf16_f32 v236, v58, v59\n"
      "v_cvt_pk_bf16_f32 v237, v60, v61\n"
      "v_accvgpr_write_b32 a232, v32\n"
      "v_mfma_f32_16x16x32_bf16 v[44:47], a[44:47], a[80:83], v[36:39]\n"
      "ds_read_b128 a[44:47], v4 offset:13408\n"
      "v_accvgpr_write_b32 a233, v34\n"
      "v_sub_f32_e32 v48, v36, v40\n"
      "v_sub_f32_e32 v49, v37, v41\n"
      "v_sub_f32_e32 v50, v38, v42\n"
      "v_mfma_f32_16x16x32_bf16 v[52:55], a[48:51], a[72:75], 0\n"
      "ds_read_b128 a[48:51], v2 offset:26560\n"
      "v_sub_f32_e32 v51, v39, v43\n"
      "v_add_f32_e32 v40, v36, v40\n"
      "v_add_f32_e32 v41, v37, v41\n"
      "v_add_f32_e32 v42, v38, v42\n"
      "v_mfma_f32_16x16x32_bf16 v[56:59], a[52:55], a[76:79], 0\n"
      "ds_read_b128 a[52:55], v3 offset:26560\n"
      "v_add_f32_e32 v43, v39, v43\n"
      "v_cvt_pk_bf16_f32 v40, v40, v41\n"
      "v_cvt_pk_bf16_f32 v41, v42, v43\n"
      "v_cvt_pk_bf16_f32 v44, v44, v45\n"
      "v_mfma_f32_16x16x32_bf16 v[60:63], a[56:59], a[80:83], v[52:55]\n"
      "s_waitcnt lgkmcnt(4)\n"
      "ds_write_addtid_b32 v40 offset:768\n"
      "ds_write_addtid_b32 v41 offset:37696\n"
      "ds_write_addtid_b32 v44 offset:0\n"
      "v_cvt_pk_bf16_f32 v45, v46, v47\n"
      "v_cvt_pk_bf16_f32 v48, v48, v49\n"
      "v_mfma_f32_16x16x32_bf16 v[36:39], a[36:39], a[72:75], 0\n"
      "s_waitcnt lgkmcnt(6)\n"
      "ds_write_addtid_b32 v45 offset:36928\n"
      "ds_write_addtid_b32 v48 offset:1536\n"
      "ds_read_b128 a[56:59], v4 offset:26560\n"
      "v_cvt_pk_bf16_f32 v49, v50, v51\n"
      "v_cvt_pk_bf16_f32 v233, v64, v65\n"
      "v_mfma_f32_16x16x32_bf16 v[40:43], a[40:43], a[76:79], 0\n"
      "s_waitcnt lgkmcnt(8)\n"
      "ds_write_addtid_b32 v49 offset:38464\n"
      "ds_read_b128 a[36:39], v2 offset:26624\n"
      "s_mov_b32 m0, s18\n"
      "v_sub_f32_e32 v64, v52, v56\n"
      "v_sub_f32_e32 v65, v53, v57\n"
      "v_sub_f32_e32 v66, v54, v58\n"
      "v_mfma_f32_16x16x32_bf16 v[44:47], a[44:47], a[80:83], v[36:39]\n"
      "ds_read_b128 a[40:43], v3 offset:26624\n"
      "ds_read_b128 a[44:47], v4 offset:26624\n"
      "v_sub_f32_e32 v67, v55, v59\n"
      "v_add_f32_e32 v56, v52, v56\n"
      "v_add_f32_e32 v57, v53, v57\n"
      "v_mfma_f32_16x16x32_bf16 v[20:23], a[28:31], v[76:79], 0\n"
      "v_add_f32_e32 v58, v54, v58\n"
      "v_add_f32_e32 v59, v55, v59\n"
      "v_cvt_pk_bf16_f32 v56, v56, v57\n"
      "v_cvt_pk_bf16_f32 v57, v58, v59\n"
      "v_cvt_pk_bf16_f32 v60, v60, v61\n"
      "v_mfma_f32_16x16x32_bf16 v[24:27], a[32:35], v[68:71], v[20:23]\n"
      "ds_write_addtid_b32 v56 offset:1024\n"
      "ds_write_addtid_b32 v57 offset:37952\n"
      "ds_write_addtid_b32 v60 offset:256\n"
      "v_cvt_pk_bf16_f32 v61, v62, v63\n"
      "v_cvt_pk_bf16_f32 v64, v64, v65\n"
      "v_mfma_f32_16x16x32_bf16 v[20:23], a[32:35], v[72:75], v[20:23]\n"
      "ds_write_addtid_b32 v61 offset:37184\n"
      "ds_write_addtid_b32 v64 offset:1792\n"
      "v_cvt_pk_bf16_f32 v65, v66, v67\n"
      "v_sub_f32_e32 v48, v36, v40\n"
      "v_sub_f32_e32 v49, v37, v41\n"
      "v_mfma_f32_16x16x32_bf16 v[28:31], a[28:31], v[80:83], 0\n"
      "ds_write_addtid_b32 v65 offset:38720\n"
      "ds_read_b128 a[28:31], v84 offset:55392\n"
      "s_mov_b32 m0, s18\n"
      "v_sub_f32_e32 v50, v38, v42\n"
      "v_sub_f32_e32 v51, v39, v43\n"
      "v_add_f32_e32 v40, v36, v40\n"
      "v_mfma_f32_16x16x32_bf16 v[32:35], a[0:3], v[68:71], v[28:31]\n"
      "v_add_f32_e32 v41, v37, v41\n"
      "v_add_f32_e32 v42, v38, v42\n"
      "v_add_f32_e32 v43, v39, v43\n"
      "v_cvt_pk_bf16_f32 v40, v40, v41\n"
      "v_cvt_pk_bf16_f32 v41, v42, v43\n"
      "v_mfma_f32_16x16x32_bf16 v[28:31], a[0:3], v[72:75], v[28:31]\n"
      "ds_write_addtid_b32 v40 offset:1280\n"
      "ds_write_addtid_b32 v41 offset:38208\n"
      "v_cvt_pk_bf16_f32 v44, v44, v45\n"
      "v_cvt_pk_bf16_f32 v45, v46, v47\n"
      "v_cvt_pk_bf16_f32 v48, v48, v49\n"
      "v_mfma_f32_16x16x32_bf16 v[54:57], a[4:7], v[76:79], 0\n"
      "ds_write_addtid_b32 v44 offset:512\n"
      "ds_write_addtid_b32 v45 offset:37440\n"
      "ds_write_addtid_b32 v48 offset:2048\n"
      "v_cvt_pk_bf16_f32 v49, v50, v51\n"
      "v_add_u32 v88, 46144, v88\n"
      "v_mfma_f32_16x16x32_bf16 v[58:61], a[8:11], v[68:71], v[54:57]\n"
      "ds_write_addtid_b32 v49 offset:38976\n"
      "ds_read_b64_tr_b16 a[116:117], v88 offset:27680\n"
      "s_mov_b32 m0, s18\n"
      "v_cvt_pk_bf16_f32 v20, v20, v21\n"
      "v_cvt_pk_bf16_f32 v22, v22, v23\n"
      "v_accvgpr_write_b32 a240, v20\n"
      "v_mfma_f32_16x16x32_bf16 v[54:57], a[8:11], v[72:75], v[54:57]\n"
      "s_waitcnt lgkmcnt(14)\n"
      "ds_read_b64_tr_b16 a[152:153], v88 offset:36896\n"
      "ds_read_b64_tr_b16 a[188:189], v88 offset:46112\n"
      "v_cvt_pk_bf16_f32 v252, v58, v59\n"
      "v_cvt_pk_bf16_f32 v253, v60, v61\n"
      "v_accvgpr_write_b32 a241, v22\n"
      "s_nop 1\n"
      "v_cvt_pk_bf16_f32 v54, v54, v55\n"
      "v_accvgpr_write_b32 a244, v54\n"
      "v_mfma_f32_16x16x32_bf16 v[52:55], a[48:51], a[72:75], 0\n"
      "ds_read_b128 a[48:51], v2 offset:26688\n"
      "ds_read_b64_tr_b16 a[118:119], v88 offset:55328\n"
      "v_cvt_pk_bf16_f32 v56, v56, v57\n"
      "v_accvgpr_write_b32 a245, v56\n"
      "v_mfma_f32_16x16x32_bf16 v[56:59], a[52:55], a[76:79], 0\n"
      "ds_read_b128 a[52:55], v3 offset:26688\n"
      "v_cvt_pk_bf16_f32 v240, v24, v25\n"
      "v_cvt_pk_bf16_f32 v241, v26, v27\n"
      "v_cvt_pk_bf16_f32 v248, v28, v29\n"
      "v_cvt_pk_bf16_f32 v249, v30, v31\n"
      "v_mfma_f32_16x16x32_bf16 v[60:63], a[56:59], a[80:83], v[52:55]\n"
      "ds_read_b128 a[56:59], v4 offset:26688\n"
      "v_cvt_pk_bf16_f32 v244, v32, v33\n"
      "v_sub_f32_e32 v64, v52, v56\n"
      "v_sub_f32_e32 v65, v53, v57\n"
      "v_sub_f32_e32 v66, v54, v58\n"
      "v_mfma_f32_16x16x32_bf16 v[36:39], a[36:39], a[72:75], 0\n"
      "v_sub_f32_e32 v67, v55, v59\n"
      "v_add_f32_e32 v56, v52, v56\n"
      "v_add_f32_e32 v57, v53, v57\n"
      "v_add_f32_e32 v58, v54, v58\n"
      "v_mfma_f32_16x16x32_bf16 v[40:43], a[40:43], a[76:79], 0\n"
      "v_add_f32_e32 v59, v55, v59\n"
      "v_cvt_pk_bf16_f32 v64, v64, v65\n"
      "v_cvt_pk_bf16_f32 v65, v66, v67\n"
      "v_cvt_pk_bf16_f32 v56, v56, v57\n"
      "v_cvt_pk_bf16_f32 v57, v58, v59\n"
      "v_mfma_f32_16x16x32_bf16 v[44:47], a[44:47], a[80:83], v[36:39]\n"
      "s_waitcnt lgkmcnt(3)\n"
      "ds_write_addtid_b32 v64 offset:6144\n"
      "ds_write_addtid_b32 v65 offset:43072\n"
      "ds_write_addtid_b32 v56 offset:5376\n"
      "ds_write_addtid_b32 v57 offset:42304\n"
      "v_cvt_pk_bf16_f32 v60, v60, v61\n"
      "v_mfma_f32_16x16x32_bf16 v[52:55], a[48:51], a[72:75], 0\n"
      "s_waitcnt lgkmcnt(5)\n"
      "ds_write_addtid_b32 v60 offset:4608\n"
      "v_cvt_pk_bf16_f32 v61, v62, v63\n"
      "v_sub_f32_e32 v48, v36, v40\n"
      "v_sub_f32_e32 v49, v37, v41\n"
      "v_sub_f32_e32 v50, v38, v42\n"
      "v_mfma_f32_16x16x32_bf16 v[56:59], a[52:55], a[76:79], 0\n"
      "Lconv_p2:\n"
      "s_cbranch_execz Lconv_r0\n"
      "s_nop 0\n"
      "s_waitcnt lgkmcnt(5)\n"
      "ds_write_addtid_b32 v61 offset:41536\n"
      "s_mov_b32 m0, s18\n"
      "v_sub_f32_e32 v51, v39, v43\n"
      "v_add_f32_e32 v40, v36, v40\n"
      "v_add_f32_e32 v41, v37, v41\n"
      "v_add_f32_e32 v42, v38, v42\n"
      "v_mfma_f32_16x16x32_bf16 v[60:63], a[56:59], a[80:83], v[52:55]\n"
      "v_add_f32_e32 v43, v39, v43\n"
      "v_cvt_pk_bf16_f32 v48, v48, v49\n"
      "v_cvt_pk_bf16_f32 v49, v50, v51\n"
      "v_cvt_pk_bf16_f32 v40, v40, v41\n"
      "v_cvt_pk_bf16_f32 v41, v42, v43\n"
      "v_cvt_pk_bf16_f32 v44, v44, v45\n"
      "v_cvt_pk_bf16_f32 v45, v46, v47\n"
      "ds_write_addtid_b32 v48 offset:6400\n"
      "ds_write_addtid_b32 v49 offset:43328\n"
      "ds_write_addtid_b32 v40 offset:5632\n"
      "ds_write_addtid_b32 v41 offset:42560\n"
      "ds_write_addtid_b32 v44 offset:4864\n"
      "ds_write_addtid_b32 v45 offset:41792\n"
      "v_sub_f32_e32 v64, v52, v56\n"
      "v_sub_f32_e32 v65, v53, v57\n"
      "v_sub_f32_e32 v66, v54, v58\n"
      "v_sub_f32_e32 v67, v55, v59\n"
      "v_cvt_pk_bf16_f32 v64, v64, v65\n"
      "v_cvt_pk_bf16_f32 v65, v66, v67\n"
      "v_cvt_pk_bf16_f32 v60, v60, v61\n"
      "v_cvt_pk_bf16_f32 v61, v62, v63\n"
      "s_mov_b32 m0, s18\n"
      "s_nop 0\n"
      "ds_write_addtid_b32 v64 offset:6656\n"
      "ds_write_addtid_b32 v65 offset:43584\n"
      "v_mfma_f32_16x16x32_bf16 v[62:65], a[12:15], v[76:79], 0\n"
      "ds_write_addtid_b32 v60 offset:5120\n"
      "ds_write_addtid_b32 v61 offset:42048\n"
      "v_add_f32_e32 v56, v52, v56\n"
      "v_add_f32_e32 v57, v53, v57\n"
      "v_add_f32_e32 v58, v54, v58\n"
      "v_mfma_f32_16x16x32_bf16 v[20:23], a[16:19], v[68:71], v[62:65]\n"
      "v_add_f32_e32 v59, v55, v59\n"
      "v_cvt_pk_bf16_f32 v56, v56, v57\n"
      "v_cvt_pk_bf16_f32 v57, v58, v59\n"
      "ds_write_addtid_b32 v56 offset:5888\n"
      "ds_write_addtid_b32 v57 offset:42816\n"
      "v_mfma_f32_16x16x32_bf16 v[62:65], a[16:19], v[72:75], v[62:65]\n"
      "s_waitcnt lgkmcnt(0)\n"
      "s_waitcnt vmcnt(7)\n"
      "s_cbranch_vccnz Lconv_h9\n"
      "s_barrier\n"
      "Lconv_h9:\n"
      "s_add_u32 m0, s46, 13280\n"
      "s_add_u32 s51, s51, s12\n"
      "v_cvt_pk_bf16_f32 v245, v34, v35\n"
      "v_mfma_f32_16x16x32_bf16 v[24:27], a[12:15], v[80:83], 0\n"
      "v_cvt_pk_bf16_f32 v206, v20, v21\n"
      "v_cvt_pk_bf16_f32 v62, v62, v63\n"
      "v_mfma_f32_16x16x32_bf16 v[28:31], a[20:23], v[68:71], v[24:27]\n"
      "s_cbranch_vccz Lconv_h10\n"
      "s_barrier\n"
      "Lconv_h10:\n"
      "ds_read_b128 a[32:35], v86 offset:21536\n"
      "ds_read_b128 a[0:3], v84 offset:18464\n"
      "ds_read_b128 a[4:7], v85 offset:18464\n"
      "ds_read_b128 a[8:11], v86 offset:21600\n"
      "v_mfma_f32_16x16x32_bf16 v[24:27], a[20:23], v[72:75], v[24:27]\n"
      "ds_read_b128 a[12:15], v84 offset:18528\n"
      "ds_read_b128 a[16:19], v86 offset:3072\n"
      "v_mfma_f32_16x16x32_bf16 v[32:35], a[24:27], v[76:79], 0\n"
      "ds_read_b128 a[20:23], v84 offset:0\n"
      "ds_read_b128 a[24:27], v85 offset:0\n"
      "v_mfma_f32_16x16x32_bf16 v[54:57], a[28:31], v[68:71], v[32:35]\n"
      "ds_read_b64_tr_b16 a[150:151], v87 offset:18464\n"
      "ds_read_b64_tr_b16 a[154:155], v88 offset:18464\n"
      "v_cvt_pk_bf16_f32 v64, v64, v65\n"
      "v_accvgpr_write_b32 a222, v62\n"
      "v_mfma_f32_16x16x32_bf16 v[32:35], a[28:31], v[72:75], v[32:35]\n"
      "s_waitcnt lgkmcnt(9)\n"
      "ds_read_b64_tr_b16 a[158:159], v87 offset:18528\n"
      "ds_read_b64_tr_b16 a[186:187], v87 offset:0\n"
      "buffer_load_dwordx4 v16, s[32:35], s51 offen lds\n"
      "v_accvgpr_write_b32 a223, v64\n"
      "v_cvt_pk_bf16_f32 v207, v22, v23\n"
      "v_mfma_f32_16x16x32_bf16 v[58:61], a[32:35], v[76:79], 0\n"
      "s_waitcnt lgkmcnt(10)\n"
      "s_add_u32 m0, s46, 13280\n"
      "ds_read_b64_tr_b16 a[190:191], v88 offset:0\n"
      "ds_read_b128 a[28:31], v86 offset:3136\n"
      "buffer_load_dwordx4 v17, s[32:35], s51 offen offset:1024 lds\n"
      "v_cvt_pk_bf16_f32 v214, v24, v25\n"
      "v_cvt_pk_bf16_f32 v215, v26, v27\n"
      "v_mfma_f32_16x16x32_bf16 v[62:65], a[0:3], v[68:71], v[58:61]\n"
      "s_add_u32 m0, s46, 13280\n"
      "s_add_u32 s96, s51, s15\n"
      "ds_read_b64_tr_b16 a[194:195], v87 offset:64\n"
      "buffer_load_dwordx4 v18, s[32:35], s51 offen offset:2048 lds\n"
      "v_cvt_pk_bf16_f32 v210, v28, v29\n"
      "v_cvt_pk_bf16_f32 v211, v30, v31\n"
      "v_mfma_f32_16x16x32_bf16 v[58:61], a[0:3], v[72:75], v[58:61]\n"
      "s_mov_b32 m0, s59\n"
      "v_cvt_pk_bf16_f32 v32, v32, v33\n"
      "v_cvt_pk_bf16_f32 v34, v34, v35\n"
      "v_mfma_f32_16x16x32_bf16 v[20:23], a[32:35], v[80:83], 0\n"
      "s_waitcnt lgkmcnt(7)\n"
      "ds_read_b128 a[32:35], v84 offset:64\n"
      "buffer_load_dwordx4 v19, s[32:35], s96 offen lds\n"
      "v_mfma_f32_16x16x32_bf16 v[24:27], a[4:7], v[68:71], v[20:23]\n"
      "s_add_u32 m0, s46, 26560\n"
      "s_add_u32 s51, s51, s12\n"
      "buffer_load_dwordx4 v16, s[32:35], s51 offen lds\n"
      "v_accvgpr_write_b32 a226, v32\n"
      "v_accvgpr_write_b32 a227, v34\n"
      "v_mfma_f32_16x16x32_bf16 v[20:23], a[4:7], v[72:75], v[20:23]\n"
      "s_add_u32 m0, s46, 26560\n"
      "v_cvt_pk_bf16_f32 v218, v54, v55\n"
      "v_cvt_pk_bf16_f32 v219, v56, v57\n"
      "v_cvt_pk_bf16_f32 v226, v58, v59\n"
      "v_cvt_pk_bf16_f32 v227, v60, v61\n"
      "v_mfma_f32_16x16x32_bf16 v[28:31], a[8:11], v[76:79], 0\n"
      "buffer_load_dwordx4 v17, s[32:35], s51 offen offset:1024 lds\n"
      "v_cvt_pk_bf16_f32 v222, v62, v63\n"
      "v_mfma_f32_16x16x32_bf16 v[32:35], a[12:15], v[68:71], v[28:31]\n"
      "s_add_u32 m0, s46, 26560\n"
      "v_cvt_pk_bf16_f32 v223, v64, v65\n"
      "v_cvt_pk_bf16_f32 v20, v20, v21\n"
      "v_mfma_f32_16x16x32_bf16 v[28:31], a[12:15], v[72:75], v[28:31]\n"
      "ds_read_b128 a[12:15], v6 offset:0\n"
      "buffer_load_dwordx4 v18, s[32:35], s51 offen offset:2048 lds\n"
      "v_cvt_pk_bf16_f32 v22, v22, v23\n"
      "v_mfma_f32_16x16x32_bf16 v[54:57], a[16:19], v[76:79], 0\n"
      "v_accvgpr_write_b32 a234, v20\n"
      "v_accvgpr_write_b32 a235, v22\n"
      "v_mfma_f32_16x16x32_bf16 v[58:61], a[20:23], v[68:71], v[54:57]\n"
      "v_cvt_pk_bf16_f32 v230, v24, v25\n"
      "v_cvt_pk_bf16_f32 v231, v26, v27\n"
      "v_mfma_f32_16x16x32_bf16 v[54:57], a[20:23], v[72:75], v[54:57]\n"
      "s_waitcnt lgkmcnt(0)\n"
      "s_waitcnt vmcnt(7)\n"
      "s_cbranch_vccnz Lconv_h11\n"
      "s_barrier\n"
      "Lconv_h11:\n"
      "ds_read_b128 a[20:23], v6 offset:512\n"
      "v_cvt_pk_bf16_f32 v238, v28, v29\n"
      "v_cvt_pk_bf16_f32 v239, v30, v31\n"
      "v_mfma_f32_16x16x32_bf16 v[62:65], a[16:19], v[80:83], 0\n"
      "ds_read_b128 a[16:19], v6 offset:256\n"
      "v_cvt_pk_bf16_f32 v234, v32, v33\n"
      "v_mfma_f32_16x16x32_bf16 v[20:23], a[24:27], v[68:71], v[62:65]\n"
      "s_cbranch_vccz Lconv_h12\n"
      "s_barrier\n"
      "Lconv_h12:\n"
      "v_cvt_pk_bf16_f32 v235, v34, v35\n"
      "v_mfma_f32_16x16x32_bf16 v[62:65], a[24:27], v[72:75], v[62:65]\n"
      "ds_read_b128 a[24:27], v6 offset:4608\n"
      "v_cvt_pk_bf16_f32 v242, v58, v59\n"
      "v_cvt_pk_bf16_f32 v243, v60, v61\n"
      "v_mfma_f32_16x16x32_bf16 v[24:27], a[28:31], v[76:79], 0\n"
      "ds_read_b128 a[28:31], v6 offset:4864\n"
      "v_cvt_pk_bf16_f32 v54, v54, v55\n"
      "v_mfma_f32_16x16x32_bf16 v[28:31], a[32:35], v[68:71], v[24:27]\n"
      "v_cvt_pk_bf16_f32 v56, v56, v57\n"
      "v_accvgpr_write_b32 a242, v54\n"
      "v_mfma_f32_16x16x32_bf16 v[24:27], a[32:35], v[72:75], v[24:27]\n"
      "ds_read_b128 a[32:35], v6 offset:5120\n"
      "v_accvgpr_write_b32 a243, v56\n"
      "v_cvt_pk_bf16_f32 v246, v20, v21\n"
      "v_cvt_pk_bf16_f32 v247, v22, v23\n"
      "v_cvt_pk_bf16_f32 v250, v62, v63\n"
      "v_cvt_pk_bf16_f32 v251, v64, v65\n"
      "v_cvt_pk_bf16_f32 v254, v28, v29\n"
      "v_cvt_pk_bf16_f32 v255, v30, v31\n"
      "v_cvt_pk_bf16_f32 v24, v24, v25\n"
      "v_cvt_pk_bf16_f32 v26, v26, v27\n"
      "v_accvgpr_write_b32 a246, v24\n"
      "v_accvgpr_write_b32 a247, v26\n"
      "s_mov_b32 m0, s18\n"
      "ds_read_b128 a[48:51], v2 offset:39840\n"
      "v_lshlrev_b32 v9, 7, v5\n"
      "v_mfma_f32_16x16x32_bf16 v[68:71], a[12:15], a[88:91], 0\n"
      "s_waitcnt lgkmcnt(4)\n"
      "ds_read_b128 a[52:55], v3 offset:39840\n"
      "v_min_u32 v9, Lconv_u0-Lconv_t0, v9\n"
      "v_mfma_f32_16x16x32_bf16 v[68:71], a[16:19], a[100:103], v[68:71]\n"
      "s_mul_i32 s97, s41, Lconv_t1-Lconv_t0\n"
      "ds_read_b128 a[56:59], v4 offset:39840\n"
      "v_add_u32 v9, Lconv_t0-Lconv_start, v9\n"
      "v_mfma_f32_16x16x32_bf16 v[68:71], a[20:23], a[112:115], v[68:71]\n"
      "s_waitcnt lgkmcnt(5)\n"
      "ds_read_b128 a[36:39], v2 offset:39904\n"
      "v_add_u32 v9, s97, v9\n"
      "v_mfma_f32_16x16x32_bf16 v[68:71], a[24:27], a[124:127], v[68:71]\n"
      "s_waitcnt lgkmcnt(5)\n"
      "ds_read_b128 a[40:43], v3 offset:39904\n"
      "v_mfma_f32_16x16x32_bf16 v[68:71], a[28:31], a[136:139], v[68:71]\n"
      "s_waitcnt lgkmcnt(4)\n"
      "ds_read_b128 a[44:47], v4 offset:39904\n"
      "v_mfma_f32_16x16x32_bf16 v[52:55], a[48:51], a[72:75], 0\n"
      "ds_read_b128 a[0:3], v6 offset:768\n"
      "v_mfma_f32_16x16x32_bf16 v[68:71], a[32:35], a[148:151], v[68:71]\n"
      "ds_read_b128 a[48:51], v2 offset:39968\n"
      "v_mfma_f32_16x16x32_bf16 v[72:75], a[12:15], a[92:95], 0\n"
      "s_waitcnt lgkmcnt(6)\n"
      "ds_read_b128 a[4:7], v6 offset:1024\n"
      "v_mfma_f32_16x16x32_bf16 v[56:59], a[52:55], a[76:79], 0\n"
      "ds_read_b128 a[8:11], v6 offset:1280\n"
      "s_nop 0\n"
      "v_mfma_f32_16x16x32_bf16 v[72:75], a[16:19], a[104:107], v[72:75]\n"
      "ds_read_b128 a[52:55], v3 offset:39968\n"
      "s_nop 0\n"
      "v_mfma_f32_16x16x32_bf16 v[72:75], a[20:23], a[116:119], v[72:75]\n"
      "s_waitcnt lgkmcnt(7)\n"
      "ds_read_b128 a[60:63], v6 offset:1536\n"
      "v_sub_f32_e32 v64, v52, v56\n"
      "v_mfma_f32_16x16x32_bf16 v[60:63], a[56:59], a[80:83], v[52:55]\n"
      "ds_read_b128 a[56:59], v4 offset:39968\n"
      "v_sub_f32_e32 v65, v53, v57\n"
      "v_mfma_f32_16x16x32_bf16 v[72:75], a[24:27], a[128:131], v[72:75]\n"
      "v_cvt_pk_bf16_f32 v64, v64, v65\n"
      "v_sub_f32_e32 v66, v54, v58\n"
      "v_mfma_f32_16x16x32_bf16 v[72:75], a[28:31], a[140:143], v[72:75]\n"
      "ds_write_addtid_b32 v64 offset:10752\n"
      "v_sub_f32_e32 v67, v55, v59\n"
      "v_mfma_f32_16x16x32_bf16 v[72:75], a[32:35], a[152:155], v[72:75]\n"
      "v_cvt_pk_bf16_f32 v65, v66, v67\n"
      "v_add_f32_e32 v56, v52, v56\n"
      "v_mfma_f32_16x16x32_bf16 v[36:39], a[36:39], a[72:75], 0\n"
      "ds_write_addtid_b32 v65 offset:47680\n"
      "v_add_f32_e32 v57, v53, v57\n"
      "v_mfma_f32_16x16x32_bf16 v[76:79], a[12:15], a[96:99], 0\n"
      "ds_read_b128 a[12:15], v6 offset:5376\n"
      "v_cvt_pk_bf16_f32 v56, v56, v57\n"
      "v_mfma_f32_16x16x32_bf16 v[76:79], a[16:19], a[108:111], v[76:79]\n"
      "s_waitcnt lgkmcnt(5)\n"
      "ds_read_b128 a[16:19], v6 offset:5632\n"
      "v_add_f32_e32 v58, v54, v58\n"
      "v_mfma_f32_16x16x32_bf16 v[40:43], a[40:43], a[76:79], 0\n"
      "ds_write_addtid_b32 v56 offset:9984\n"
      "v_add_f32_e32 v59, v55, v59\n"
      "v_mfma_f32_16x16x32_bf16 v[76:79], a[20:23], a[120:123], v[76:79]\n"
      "ds_read_b128 a[20:23], v6 offset:5888\n"
      "v_cvt_pk_bf16_f32 v57, v58, v59\n"
      "v_mfma_f32_16x16x32_bf16 v[76:79], a[24:27], a[132:135], v[76:79]\n"
      "ds_write_addtid_b32 v57 offset:46912\n"
      "v_cvt_pk_bf16_f32 v60, v60, v61\n"
      "v_mfma_f32_16x16x32_bf16 v[44:47], a[44:47], a[80:83], v[36:39]\n"
      "ds_write_addtid_b32 v60 offset:9216\n"
      "v_cvt_pk_bf16_f32 v61, v62, v63\n"
      "v_mfma_f32_16x16x32_bf16 v[76:79], a[28:31], a[144:147], v[76:79]\n"
      "ds_write_addtid_b32 v61 offset:46144\n"
      "v_sub_f32_e32 v48, v36, v40\n"
      "v_mfma_f32_16x16x32_bf16 v[76:79], a[32:35], a[156:159], v[76:79]\n"
      "ds_read_b128 a[36:39], v2 offset:0\n"
      "v_sub_f32_e32 v49, v37, v41\n"
      "v_mfma_f32_16x16x32_bf16 v[80:83], a[0:3], v[92:95], 0\n"
      "v_cvt_pk_bf16_f32 v48, v48, v49\n"
      "v_sub_f32_e32 v50, v38, v42\n"
      "v_mfma_f32_16x16x32_bf16 v[52:55], a[48:51], a[72:75], 0\n"
      "ds_write_addtid_b32 v48 offset:11008\n"
      "v_sub_f32_e32 v51, v39, v43\n"
      "v_mfma_f32_16x16x32_bf16 v[80:83], a[4:7], v[148:151], v[80:83]\n"
      "ds_read_b128 a[40:43], v3 offset:0\n"
      "v_cvt_pk_bf16_f32 v49, v50, v51\n"
      "v_mfma_f32_16x16x32_bf16 v[80:83], a[8:11], v[204:207], v[80:83]\n"
      "ds_write_addtid_b32 v49 offset:47936\n"
      "v_add_f32_e32 v40, v36, v40\n"
      "v_mfma_f32_16x16x32_bf16 v[56:59], a[52:55], a[76:79], 0\n"
      "s_waitcnt lgkmcnt(7)\n"
      "v_add_f32_e32 v41, v37, v41\n"
      "v_cvt_pk_bf16_f32 v40, v40, v41\n"
      "v_mfma_f32_16x16x32_bf16 v[80:83], a[12:15], v[112:115], v[80:83]\n"
      "ds_write_addtid_b32 v40 offset:10240\n"
      "v_add_f32_e32 v42, v38, v42\n"
      "v_mfma_f32_16x16x32_bf16 v[80:83], a[16:19], a[208:211], v[80:83]\n"
      "v_add_f32_e32 v43, v39, v43\n"
      "v_cvt_pk_bf16_f32 v41, v42, v43\n"
      "v_mfma_f32_16x16x32_bf16 v[60:63], a[56:59], a[80:83], v[52:55]\n"
      "ds_write_addtid_b32 v41 offset:47168\n"
      "v_cvt_pk_bf16_f32 v44, v44, v45\n"
      "v_mfma_f32_16x16x32_bf16 v[80:83], a[20:23], v[220:223], v[80:83]\n"
      "ds_write_addtid_b32 v44 offset:9472\n"
      "v_cvt_pk_bf16_f32 v45, v46, v47\n"
      "v_mfma_f32_16x16x32_bf16 v[84:87], a[0:3], v[100:103], 0\n"
      "ds_read_b128 a[44:47], v4 offset:0\n"
      "v_sub_f32_e32 v64, v52, v56\n"
      "v_mfma_f32_16x16x32_bf16 v[84:87], a[4:7], a[200:203], v[84:87]\n"
      "ds_write_addtid_b32 v45 offset:46400\n"
      "v_sub_f32_e32 v65, v53, v57\n"
      "v_mfma_f32_16x16x32_bf16 v[84:87], a[8:11], v[208:211], v[84:87]\n"
      "ds_read_b128 a[64:67], v6 offset:1792\n"
      "v_cvt_pk_bf16_f32 v64, v64, v65\n"
      "v_mfma_f32_16x16x32_bf16 v[84:87], a[12:15], a[204:207], v[84:87]\n"
      "ds_read_b128 a[68:71], v6 offset:2048\n"
      "v_sub_f32_e32 v66, v54, v58\n"
      "v_mfma_f32_16x16x32_bf16 v[84:87], a[16:19], v[172:175], v[84:87]\n"
      "ds_read_b128 a[24:27], v6 offset:6144\n"
      "v_sub_f32_e32 v67, v55, v59\n"
      "v_mfma_f32_16x16x32_bf16 v[84:87], a[20:23], v[228:231], v[84:87]\n"
      "ds_read_b128 a[48:51], v2 offset:64\n"
      "v_cvt_pk_bf16_f32 v65, v66, v67\n"
      "v_mfma_f32_16x16x32_bf16 v[24:27], a[0:3], a[196:199], 0\n"
      "ds_write_addtid_b32 v64 offset:11264\n"
      "v_add_f32_e32 v56, v52, v56\n"
      "v_mfma_f32_16x16x32_bf16 v[24:27], a[4:7], v[160:163], v[24:27]\n"
      "s_waitcnt lgkmcnt(11)\n"
      "ds_read_b128 a[28:31], v6 offset:6400\n"
      "v_add_f32_e32 v57, v53, v57\n"
      "v_mfma_f32_16x16x32_bf16 v[36:39], a[36:39], a[72:75], 0\n"
      "ds_write_addtid_b32 v65 offset:48192\n"
      "v_cvt_pk_bf16_f32 v56, v56, v57\n"
      "v_mfma_f32_16x16x32_bf16 v[24:27], a[8:11], v[216:219], v[24:27]\n"
      "ds_read_b128 a[32:35], v6 offset:6656\n"
      "v_add_f32_e32 v58, v54, v58\n"
      "v_mfma_f32_16x16x32_bf16 v[24:27], a[12:15], v[124:127], v[24:27]\n"
      "ds_write_addtid_b32 v56 offset:10496\n"
      "v_add_f32_e32 v59, v55, v59\n"
      "v_mfma_f32_16x16x32_bf16 v[40:43], a[40:43], a[76:79], 0\n"
      "ds_read_b128 a[52:55], v3 offset:64\n"
      "v_cvt_pk_bf16_f32 v57, v58, v59\n"
      "v_mfma_f32_16x16x32_bf16 v[24:27], a[16:19], v[180:183], v[24:27]\n"
      "ds_write_addtid_b32 v57 offset:47424\n"
      "v_cvt_pk_bf16_f32 v60, v60, v61\n"
      "v_mfma_f32_16x16x32_bf16 v[24:27], a[20:23], v[232:235], v[24:27]\n"
      "s_waitcnt lgkmcnt(7)\n"
      "ds_write_addtid_b32 v60 offset:9728\n"
      "v_cvt_pk_bf16_f32 v61, v62, v63\n"
      "v_mfma_f32_16x16x32_bf16 v[44:47], a[44:47], a[80:83], v[36:39]\n"
      "ds_write_addtid_b32 v61 offset:46656\n"
      "v_sub_f32_e32 v48, v36, v40\n"
      "v_mfma_f32_16x16x32_bf16 v[28:31], a[60:63], v[96:99], 0\n"
      "ds_read_b128 a[56:59], v4 offset:64\n"
      "v_sub_f32_e32 v49, v37, v41\n"
      "v_mfma_f32_16x16x32_bf16 v[28:31], a[64:67], v[152:155], v[28:31]\n"
      "ds_read_b128 a[36:39], v2 offset:128\n"
      "v_cvt_pk_bf16_f32 v48, v48, v49\n"
      "v_mfma_f32_16x16x32_bf16 v[28:31], a[68:71], a[220:223], v[28:31]\n"
      "ds_read_b128 a[40:43], v3 offset:128\n"
      "v_sub_f32_e32 v50, v38, v42\n"
      "v_mfma_f32_16x16x32_bf16 v[28:31], a[24:27], v[116:119], v[28:31]\n"
      "s_waitcnt lgkmcnt(0)\n"
      "s_cbranch_vccnz Lconv_h13\n"
      "s_barrier\n"
      "Lconv_h13:\n"
      "v_sub_f32_e32 v51, v39, v43\n"
      "v_mfma_f32_16x16x32_bf16 v[52:55], a[48:51], a[72:75], 0\n"
      "ds_read_b128 a[44:47], v4 offset:128\n"
      "v_cvt_pk_bf16_f32 v49, v50, v51\n"
      "v_mfma_f32_16x16x32_bf16 v[28:31], a[28:31], v[168:171], v[28:31]\n"
      "ds_write_addtid_b32 v48 offset:15360\n"
      "v_add_f32_e32 v40, v36, v40\n"
      "v_mfma_f32_16x16x32_bf16 v[28:31], a[32:35], v[224:227], v[28:31]\n"
      "ds_write_addtid_b32 v49 offset:52288\n"
      "v_add_f32_e32 v41, v37, v41\n"
      "v_mfma_f32_16x16x32_bf16 v[56:59], a[52:55], a[76:79], 0\n"
      "s_cbranch_vccz Lconv_h14\n"
      "s_barrier\n"
      "Lconv_h14:\n"
      "v_cvt_pk_bf16_f32 v40, v40, v41\n"
      "v_mfma_f32_16x16x32_bf16 v[20:23], a[60:63], v[104:107], 0\n"
      "s_mov_b64 vcc, 0x2000000\n"
      "ds_write_addtid_b32 v40 offset:14592\n"
      "v_add_f32_e32 v42, v38, v42\n"
      "v_mfma_f32_16x16x32_bf16 v[20:23], a[64:67], v[156:159], v[20:23]\n"
      "ds_read_b128 a[0:3], v7 offset:0\n"
      "v_add_f32_e32 v43, v39, v43\n"
      "v_mfma_f32_16x16x32_bf16 v[60:63], a[56:59], a[80:83], v[52:55]\n"
      "v_cvt_pk_bf16_f32 v41, v42, v43\n"
      "v_cvt_pk_bf16_f32 v44, v44, v45\n"
      "v_mfma_f32_16x16x32_bf16 v[20:23], a[68:71], v[212:215], v[20:23]\n"
      "ds_write_addtid_b32 v41 offset:51520\n"
      "v_cvt_pk_bf16_f32 v45, v46, v47\n"
      "v_mfma_f32_16x16x32_bf16 v[20:23], a[24:27], v[120:123], v[20:23]\n"
      "ds_write_addtid_b32 v44 offset:13824\n"
      "ds_write_addtid_b32 v45 offset:50752\n"
      "v_mfma_f32_16x16x32_bf16 v[20:23], a[28:31], v[176:179], v[20:23]\n"
      "ds_read_b128 a[4:7], v7 offset:256\n"
      "v_sub_f32_e32 v64, v52, v56\n"
      "v_mfma_f32_16x16x32_bf16 v[36:39], a[36:39], a[72:75], 0\n"
      "ds_read_b128 a[8:11], v7 offset:512\n"
      "v_sub_f32_e32 v65, v53, v57\n"
      "v_mfma_f32_16x16x32_bf16 v[20:23], a[32:35], a[232:235], v[20:23]\n"
      "v_sub_f32_e32 v66, v54, v58\n"
      "v_sub_f32_e32 v67, v55, v59\n"
      "v_mfma_f32_16x16x32_bf16 v[32:35], a[60:63], v[108:111], 0\n"
      "v_add_f32_e32 v56, v52, v56\n"
      "v_add_f32_e32 v57, v53, v57\n"
      "v_mfma_f32_16x16x32_bf16 v[40:43], a[40:43], a[76:79], 0\n"
      "v_add_f32_e32 v58, v54, v58\n"
      "v_add_f32_e32 v59, v55, v59\n"
      "v_mfma_f32_16x16x32_bf16 v[32:35], a[64:67], v[164:167], v[32:35]\n"
      "v_cvt_pk_bf16_f32 v64, v64, v65\n"
      "v_cvt_pk_bf16_f32 v65, v66, v67\n"
      "v_mfma_f32_16x16x32_bf16 v[32:35], a[68:71], a[224:227], v[32:35]\n"
      "s_waitcnt lgkmcnt(5)\n"
      "v_cvt_pk_bf16_f32 v56, v56, v57\n"
      "v_cvt_pk_bf16_f32 v57, v58, v59\n"
      "v_mfma_f32_16x16x32_bf16 v[44:47], a[44:47], a[80:83], v[36:39]\n"
      "ds_write_addtid_b32 v64 offset:15616\n"
      "v_cvt_pk_bf16_f32 v60, v60, v61\n"
      "v_mfma_f32_16x16x32_bf16 v[32:35], a[24:27], v[128:131], v[32:35]\n"
      "ds_write_addtid_b32 v65 offset:52544\n"
      "v_cvt_pk_bf16_f32 v61, v62, v63\n"
      "v_mfma_f32_16x16x32_bf16 v[32:35], a[28:31], a[228:231], v[32:35]\n"
      "ds_write_addtid_b32 v56 offset:14848\n"
      "ds_write_addtid_b32 v57 offset:51776\n"
      "v_mfma_f32_16x16x32_bf16 v[32:35], a[32:35], v[236:239], v[32:35]\n"
      "ds_write_addtid_b32 v60 offset:14080\n"
      "ds_write_addtid_b32 v61 offset:51008\n"
      "v_mfma_f32_16x16x32_bf16 v[68:71], a[0:3], a[160:163], v[68:71]\n"
      "s_waitcnt lgkmcnt(6)\n"
      "ds_read_b128 a[12:15], v7 offset:768\n"
      "v_sub_f32_e32 v48, v36, v40\n"
      "v_mfma_f32_16x16x32_bf16 v[68:71], a[4:7], a[172:175], v[68:71]\n"
      "s_add_u32 m0, s46, 39840\n"
      "ds_read_b128 a[16:19], v7 offset:1024\n"
      "v_sub_f32_e32 v49, v37, v41\n"
      "v_mfma_f32_16x16x32_bf16 v[68:71], a[8:11], a[184:187], v[68:71]\n"
      "s_add_u32 s51, s51, s12\n"
      "ds_read_b128 a[20:23], v7 offset:1280\n"
      "v_cvt_pk_bf16_f32 v48, v48, v49\n"
      "v_mfma_f32_16x16x32_bf16 v[72:75], a[0:3], a[164:167], v[72:75]\n"
      "buffer_load_dwordx4 v16, s[32:35], s51 offen lds\n"
      "v_sub_f32_e32 v50, v38, v42\n"
      "v_mfma_f32_16x16x32_bf16 v[72:75], a[4:7], a[176:179], v[72:75]\n"
      "v_sub_f32_e32 v51, v39, v43\n"
      "v_cvt_pk_bf16_f32 v49, v50, v51\n"
      "v_mfma_f32_16x16x32_bf16 v[72:75], a[8:11], a[188:191], v[72:75]\n"
      "v_add_f32_e32 v40, v36, v40\n"
      "v_add_f32_e32 v41, v37, v41\n"
      "v_mfma_f32_16x16x32_bf16 v[76:79], a[0:3], a[168:171], v[76:79]\n"
      "buffer_load_dwordx4 v17, s[32:35], s51 offen offset:1024 lds\n"
      "v_cvt_pk_bf16_f32 v40, v40, v41\n"
      "v_mfma_f32_16x16x32_bf16 v[76:79], a[4:7], a[180:183], v[76:79]\n"
      "ds_read_b128 a[0:3], v7 offset:1536\n"
      "v_add_f32_e32 v42, v38, v42\n"
      "v_mfma_f32_16x16x32_bf16 v[76:79], a[8:11], a[192:195], v[76:79]\n"
      "s_waitcnt lgkmcnt(3)\n"
      "ds_read_b128 a[4:7], v7 offset:1792\n"
      "v_add_f32_e32 v43, v39, v43\n"
      "v_mfma_f32_16x16x32_bf16 v[80:83], a[12:15], a[212:215], v[80:83]\n"
      "s_waitcnt lgkmcnt(3)\n"
      "ds_read_b128 a[8:11], v7 offset:2048\n"
      "v_cvt_pk_bf16_f32 v41, v42, v43\n"
      "v_mfma_f32_16x16x32_bf16 v[80:83], a[16:19], v[184:187], v[80:83]\n"
      "s_waitcnt lgkmcnt(3)\n"
      "v_cvt_pk_bf16_f32 v44, v44, v45\n"
      "v_cvt_pk_bf16_f32 v45, v46, v47\n"
      "v_mfma_f32_16x16x32_bf16 v[80:83], a[20:23], v[240:243], v[80:83]\n"
      "buffer_load_dwordx4 v18, s[32:35], s51 offen offset:2048 lds\n"
      "v_cvt_pk_bf16_f32 v68, v68, v69\n"
      "v_mfma_f32_16x16x32_bf16 v[84:87], a[12:15], v[136:139], v[84:87]\n"
      "s_mov_b32 m0, s18\n"
      "v_cvt_pk_bf16_f32 v69, v70, v71\n"
      "v_cvt_pk_bf16_f32 v72, v72, v73\n"
      "v_mfma_f32_16x16x32_bf16 v[84:87], a[16:19], v[192:195], v[84:87]\n"
      "ds_write_addtid_b32 v48 offset:15872\n"
      "ds_write_addtid_b32 v49 offset:52800\n"
      "v_mfma_f32_16x16x32_bf16 v[84:87], a[20:23], v[244:247], v[84:87]\n"
      "ds_write_addtid_b32 v40 offset:15104\n"
      "ds_write_addtid_b32 v41 offset:52032\n"
      "v_mfma_f32_16x16x32_bf16 v[24:27], a[12:15], a[216:219], v[24:27]\n"
      "ds_write_addtid_b32 v44 offset:14336\n"
      "ds_write_addtid_b32 v45 offset:51264\n"
      "v_mfma_f32_16x16x32_bf16 v[24:27], a[16:19], v[196:199], v[24:27]\n"
      "s_mov_b32 m0, s45\n"
      "v_cvt_pk_bf16_f32 v73, v74, v75\n"
      "v_cvt_pk_bf16_f32 v76, v76, v77\n"
      "v_mfma_f32_16x16x32_bf16 v[24:27], a[20:23], v[252:255], v[24:27]\n"
      "s_waitcnt lgkmcnt(6)\n"
      "ds_write_addtid_b32 v68 offset:57360\n"
      "ds_write_addtid_b32 v69 offset:57616\n"
      "v_mfma_f32_16x16x32_bf16 v[28:31], a[0:3], v[132:135], v[28:31]\n"
      "ds_write_addtid_b32 v72 offset:45064\n"
      "ds_write_addtid_b32 v73 offset:45320\n"
      "v_mfma_f32_16x16x32_bf16 v[28:31], a[4:7], v[188:191], v[28:31]\n"
      "ds_write_addtid_b32 v76 offset:32768\n"
      "v_cvt_pk_bf16_f32 v77, v78, v79\n"
      "v_mfma_f32_16x16x32_bf16 v[28:31], a[8:11], a[240:243], v[28:31]\n"
      "ds_write_addtid_b32 v77 offset:33024\n"
      "v_cvt_pk_bf16_f32 v80, v80, v81\n"
      "v_mfma_f32_16x16x32_bf16 v[20:23], a[0:3], v[140:143], v[20:23]\n"
      "ds_write_addtid_b32 v80 offset:58384\n"
      "v_cvt_pk_bf16_f32 v81, v82, v83\n"
      "v_mfma_f32_16x16x32_bf16 v[20:23], a[4:7], a[236:239], v[20:23]\n"
      "ds_write_addtid_b32 v81 offset:58640\n"
      "v_cvt_pk_bf16_f32 v84, v84, v85\n"
      "v_mfma_f32_16x16x32_bf16 v[20:23], a[8:11], v[248:251], v[20:23]\n"
      "ds_write_addtid_b32 v84 offset:46088\n"
      "v_cvt_pk_bf16_f32 v85, v86, v87\n"
      "v_mfma_f32_16x16x32_bf16 v[32:35], a[0:3], v[144:147], v[32:35]\n"
      "ds_write_addtid_b32 v85 offset:46344\n"
      "v_cvt_pk_bf16_f32 v24, v24, v25\n"
      "v_mfma_f32_16x16x32_bf16 v[32:35], a[4:7], v[200:203], v[32:35]\n"
      "ds_write_addtid_b32 v24 offset:33792\n"
      "v_cvt_pk_bf16_f32 v25, v26, v27\n"
      "v_mfma_f32_16x16x32_bf16 v[32:35], a[8:11], a[244:247], v[32:35]\n"
      "ds_write_addtid_b32 v25 offset:34048\n"
      "v_cvt_pk_bf16_f32 v28, v28, v29\n"
      "v_cvt_pk_bf16_f32 v29, v30, v31\n"
      "ds_write_addtid_b32 v28 offset:59408\n"
      "ds_write_addtid_b32 v29 offset:59664\n"
      "v_cvt_pk_bf16_f32 v20, v20, v21\n"
      "v_cvt_pk_bf16_f32 v21, v22, v23\n"
      "ds_write_addtid_b32 v20 offset:47112\n"
      "ds_write_addtid_b32 v21 offset:47368\n"
      "s_waitcnt vmcnt(3)\n"
      "s_waitcnt lgkmcnt(0)\n"
      "s_cmp_eq_u32 s41, 1\n"
      "s_cbranch_scc1 Lconv_s1\n"
      "Lconv_s0:\n"
      "s_branch Lconv_loop0\n"
      "Lconv_s1:\n"
      "v_cvt_pk_bf16_f32 v32, v32, v33\n"
      "s_branch Lconv_loop1\n"
      ".p2align 12\n"
      "Lconv_pa0:\n"
      "s_cbranch_execz Lconv_r0\n"
      "s_nop 0\n"
      "Lconv_loop0:\n"
      "v_mfma_f32_16x16x32_bf16 v[20:23], a[24:27], v[96:99], 0\n"
      "s_add_u32 s51, s51, s12\n"
      "s_nop 1\n"
      "v_mfma_f32_16x16x32_bf16 v[20:23], a[28:31], v[152:155], v[20:23]\n"
      "s_sub_u32 s58, s93, s58\n"
      "v_cvt_pk_bf16_f32 v32, v32, v33\n"
      "v_cvt_pk_bf16_f32 v33, v34, v35\n"
      "v_mfma_f32_16x16x32_bf16 v[20:23], a[32:35], a[220:223], v[20:23]\n"
      "s_sub_u32 s59, s94, s59\n"
      "ds_write_addtid_b32 v32 offset:34816\n"
      "v_mfma_f32_16x16x32_bf16 v[20:23], a[0:3], v[116:119], v[20:23]\n"
      "s_sub_u32 s47, s51, s48\n"
      "ds_write_addtid_b32 v33 offset:35072\n"
      "v_mfma_f32_16x16x32_bf16 v[20:23], a[4:7], v[168:171], v[20:23]\n"
      "s_cmp_ge_u32 vcc_lo, 0x80000000\n"
      "v_mfma_f32_16x16x32_bf16 v[20:23], a[8:11], v[224:227], v[20:23]\n"
      "s_cselect_b32 s53, s47, s51\n"
      "v_mfma_f32_16x16x32_bf16 v[24:27], a[24:27], v[104:107], 0\n"
      "s_waitcnt vmcnt(8) lgkmcnt(0)\n"
      "s_barrier\n"
      "v_mfma_f32_16x16x32_bf16 v[24:27], a[28:31], v[156:159], v[24:27]\n"
      "s_mov_b32 m0, s58\n"
      "ds_read_b128 a[12:15], v7 offset:6144\n"
      "v_mfma_f32_16x16x32_bf16 v[24:27], a[32:35], v[212:215], v[24:27]\n"
      "ds_read_b128 a[16:19], v7 offset:6400\n"
      "v_mfma_f32_16x16x32_bf16 v[24:27], a[0:3], v[120:123], v[24:27]\n"
      "ds_read_b128 a[20:23], v7 offset:6656\n"
      "v_mfma_f32_16x16x32_bf16 v[24:27], a[4:7], v[176:179], v[24:27]\n"
      "ds_read_b128 a[48:51], v2 offset:13280\n"
      "v_mfma_f32_16x16x32_bf16 v[24:27], a[8:11], a[232:235], v[24:27]\n"
      "ds_read_b128 a[52:55], v3 offset:13280\n"
      "v_mfma_f32_16x16x32_bf16 v[68:71], a[84:87], a[60:63], 0\n"
      "ds_read_b128 a[56:59], v4 offset:13280\n"
      "s_nop 0\n"
      "v_mfma_f32_16x16x32_bf16 v[28:31], a[24:27], v[108:111], 0\n"
      "ds_read2_b64 a[64:67], v10 offset0:0 offset1:64\n"
      "s_nop 0\n"
      "v_mfma_f32_16x16x32_bf16 v[28:31], a[28:31], v[164:167], v[28:31]\n"
      "buffer_load_dwordx4 v16, s[32:35], s53 offen lds\n"
      "v_mfma_f32_16x16x32_bf16 v[28:31], a[32:35], a[224:227], v[28:31]\n"
      "s_mov_b32 m0, s45\n"
      "ds_read2_b64 a[68:71], v10 offset0:1 offset1:65\n"
      "v_cvt_pk_bf16_f32 v85, v68, v69\n"
      "v_mfma_f32_16x16x32_bf16 v[28:31], a[0:3], v[128:131], v[28:31]\n"
      "ds_read_b128 a[24:27], v6 offset:5376\n"
      "v_cvt_pk_bf16_f32 v91, v70, v71\n"
      "v_mfma_f32_16x16x32_bf16 v[28:31], a[4:7], a[228:231], v[28:31]\n"
      "ds_read_b128 a[28:31], v6 offset:5632\n"
      "v_mfma_f32_16x16x32_bf16 v[28:31], a[8:11], v[236:239], v[28:31]\n"
      "s_waitcnt lgkmcnt(6)\n"
      "ds_read_b128 a[32:35], v6 offset:5888\n"
      "v_mfma_f32_16x16x32_bf16 v[20:23], a[12:15], v[132:135], v[20:23]\n"
      "buffer_store_dwordx2 v[84:85], v15, s[36:39], s52 offen sc1\n"
      "v_mfma_f32_16x16x32_bf16 v[20:23], a[16:19], v[188:191], v[20:23]\n"
      "ds_read_b128 a[0:3], v7 offset:768\n"
      "v_mfma_f32_16x16x32_bf16 v[20:23], a[20:23], a[240:243], v[20:23]\n"
      "ds_read_b128 a[4:7], v7 offset:1024\n"
      "s_nop 0\n"
      "v_mfma_f32_16x16x32_bf16 v[52:55], a[48:51], a[72:75], 0\n"
      "s_waitcnt lgkmcnt(6)\n"
      "ds_read_b128 a[8:11], v7 offset:1280\n"
      "s_nop 0\n"
      "v_mfma_f32_16x16x32_bf16 v[56:59], a[52:55], a[76:79], 0\n"
      "ds_read_b128 a[36:39], v2 offset:13344\n"
      "v_cvt_pk_bf16_f32 v20, v20, v21\n"
      "v_mfma_f32_16x16x32_bf16 v[60:63], a[56:59], a[80:83], v[52:55]\n"
      "buffer_store_dwordx2 v[90:91], v15, s[36:39], s52 offen offset:1536 sc1\n"
      "v_cvt_pk_bf16_f32 v21, v22, v23\n"
      "v_mfma_f32_16x16x32_bf16 v[24:27], a[12:15], v[140:143], v[24:27]\n"
      "s_mov_b32 s52, s13\n"
      "ds_write_addtid_b32 v20 offset:62480\n"
      "v_sub_f32_e32 v64, v52, v56\n"
      "v_mfma_f32_16x16x32_bf16 v[24:27], a[16:19], a[236:239], v[24:27]\n"
      "s_add_u32 s13, s13, s12\n"
      "ds_read_b128 a[40:43], v3 offset:13344\n"
      "v_sub_f32_e32 v65, v53, v57\n"
      "v_mfma_f32_16x16x32_bf16 v[24:27], a[20:23], v[248:251], v[24:27]\n"
      "s_sub_u32 s96, s53, s95\n"
      "ds_write_addtid_b32 v21 offset:62736\n"
      "v_cvt_pk_bf16_f32 v64, v64, v65\n"
      "v_mfma_f32_16x16x32_bf16 v[72:75], a[84:87], a[64:67], 0\n"
      "ds_read_b128 a[44:47], v4 offset:13344\n"
      "v_sub_f32_e32 v66, v54, v58\n"
      "v_mfma_f32_16x16x32_bf16 v[28:31], a[12:15], v[144:147], v[28:31]\n"
      "ds_read_b128 a[12:15], v7 offset:5376\n"
      "v_cvt_pk_bf16_f32 v24, v24, v25\n"
      "v_mfma_f32_16x16x32_bf16 v[28:31], a[16:19], v[200:203], v[28:31]\n"
      "ds_read_b128 a[16:19], v7 offset:5632\n"
      "v_cvt_pk_bf16_f32 v25, v26, v27\n"
      "v_mfma_f32_16x16x32_bf16 v[28:31], a[20:23], a[244:247], v[28:31]\n"
      "s_waitcnt lgkmcnt(6)\n"
      "ds_read_b128 a[20:23], v7 offset:5888\n"
      "v_sub_f32_e32 v67, v55, v59\n"
      "v_mfma_f32_16x16x32_bf16 v[76:79], a[84:87], a[68:71], 0\n"
      "ds_read2_b64 a[60:63], v10 offset0:2 offset1:66\n"
      "v_cvt_pk_bf16_f32 v65, v66, v67\n"
      "v_mfma_f32_16x16x32_bf16 v[32:35], a[24:27], v[92:95], 0\n"
      "ds_write_addtid_b32 v24 offset:50184\n"
      "ds_write_addtid_b32 v25 offset:50440\n"
      "v_mfma_f32_16x16x32_bf16 v[32:35], a[28:31], v[148:151], v[32:35]\n"
      "s_mov_b32 m0, s18\n"
      "v_add_f32_e32 v56, v52, v56\n"
      "v_add_f32_e32 v57, v53, v57\n"
      "v_mfma_f32_16x16x32_bf16 v[32:35], a[32:35], v[204:207], v[32:35]\n"
      "ds_write_addtid_b32 v64 offset:1536\n"
      "v_add_f32_e32 v58, v54, v58\n"
      "v_mfma_f32_16x16x32_bf16 v[32:35], a[0:3], v[112:115], v[32:35]\n"
      "ds_write_addtid_b32 v65 offset:38464\n"
      "v_add_f32_e32 v59, v55, v59\n"
      "v_mfma_f32_16x16x32_bf16 v[32:35], a[4:7], a[208:211], v[32:35]\n"
      "v_cvt_pk_bf16_f32 v56, v56, v57\n"
      "v_cvt_pk_bf16_f32 v57, v58, v59\n"
      "v_mfma_f32_16x16x32_bf16 v[32:35], a[8:11], v[220:223], v[32:35]\n"
      "s_waitcnt lgkmcnt(6)\n"
      "ds_write_addtid_b32 v56 offset:768\n"
      "v_cvt_pk_bf16_f32 v60, v60, v61\n"
      "v_mfma_f32_16x16x32_bf16 v[32:35], a[12:15], a[212:215], v[32:35]\n"
      "ds_write_addtid_b32 v57 offset:37696\n"
      "v_cvt_pk_bf16_f32 v61, v62, v63\n"
      "v_mfma_f32_16x16x32_bf16 v[32:35], a[16:19], v[184:187], v[32:35]\n"
      "s_waitcnt lgkmcnt(6)\n"
      "ds_write_addtid_b32 v60 offset:0\n"
      "ds_write_addtid_b32 v61 offset:36928\n"
      "v_mfma_f32_16x16x32_bf16 v[32:35], a[20:23], v[240:243], v[32:35]\n"
      "s_mov_b32 m0, s45\n"
      "ds_read_b128 a[48:51], v2 offset:13408\n"
      "v_cvt_pk_bf16_f32 v28, v28, v29\n"
      "v_mfma_f32_16x16x32_bf16 v[20:23], a[24:27], v[100:103], 0\n"
      "ds_write_addtid_b32 v28 offset:37888\n"
      "v_cvt_pk_bf16_f32 v29, v30, v31\n"
      "v_mfma_f32_16x16x32_bf16 v[20:23], a[28:31], a[200:203], v[20:23]\n"
      "ds_write_addtid_b32 v29 offset:38144\n"
      "v_cvt_pk_bf16_f32 v32, v32, v33\n"
      "v_mfma_f32_16x16x32_bf16 v[20:23], a[32:35], v[208:211], v[20:23]\n"
      "s_mov_b32 m0, s58\n"
      "ds_read_b128 a[56:59], v4 offset:13408\n"
      "v_cvt_pk_bf16_f32 v33, v34, v35\n"
      "v_mfma_f32_16x16x32_bf16 v[20:23], a[0:3], a[204:207], v[20:23]\n"
      "buffer_load_dwordx4 v17, s[32:35], s53 offen offset:1024 lds\n"
      "v_cvt_pk_bf16_f32 v80, v72, v73\n"
      "v_mfma_f32_16x16x32_bf16 v[20:23], a[4:7], v[172:175], v[20:23]\n"
      "s_mov_b32 m0, s45\n"
      "ds_read2_b64 a[64:67], v10 offset0:3 offset1:67\n"
      "v_cvt_pk_bf16_f32 v81, v76, v77\n"
      "v_mfma_f32_16x16x32_bf16 v[20:23], a[8:11], v[228:231], v[20:23]\n"
      "ds_write_addtid_b32 v32 offset:61456\n"
      "v_cvt_pk_bf16_f32 v86, v74, v75\n"
      "v_mfma_f32_16x16x32_bf16 v[20:23], a[12:15], v[136:139], v[20:23]\n"
      "ds_write_addtid_b32 v33 offset:61712\n"
      "v_cvt_pk_bf16_f32 v87, v78, v79\n"
      "v_mfma_f32_16x16x32_bf16 v[20:23], a[16:19], v[192:195], v[20:23]\n"
      "ds_read_b128 a[52:55], v3 offset:13408\n"
      "v_add_u32_e64 v2, s91, v2\n"
      "v_mfma_f32_16x16x32_bf16 v[20:23], a[20:23], v[244:247], v[20:23]\n"
      "ds_read2_b64 a[68:71], v12 offset0:0 offset1:64\n"
      "v_add_u32_e64 v3, s91, v3\n"
      "v_mfma_f32_16x16x32_bf16 v[36:39], a[36:39], a[72:75], 0\n"
      "v_add_u32_e32 v4, s91, v4\n"
      "s_nop 0\n"
      "v_mfma_f32_16x16x32_bf16 v[40:43], a[40:43], a[76:79], 0\n"
      "s_sub_u32 s91, 0, s91\n"
      "v_mfma_f32_16x16x32_bf16 v[44:47], a[44:47], a[80:83], v[36:39]\n"
      "v_cvt_pk_bf16_f32 v20, v20, v21\n"
      "v_cvt_pk_bf16_f32 v21, v22, v23\n"
      "v_mfma_f32_16x16x32_bf16 v[68:71], a[84:87], a[60:63], 0\n"
      "ds_write_addtid_b32 v20 offset:49160\n"
      "s_nop 0\n"
      "v_mfma_f32_16x16x32_bf16 v[24:27], a[24:27], a[196:199], 0\n"
      "ds_read_b128 a[24:27], v7 offset:4608\n"
      "v_sub_f32_e32 v48, v36, v40\n"
      "v_mfma_f32_16x16x32_bf16 v[24:27], a[28:31], v[160:163], v[24:27]\n"
      "ds_read_b128 a[28:31], v7 offset:4864\n"
      "v_sub_f32_e32 v49, v37, v41\n"
      "v_mfma_f32_16x16x32_bf16 v[24:27], a[32:35], v[216:219], v[24:27]\n"
      "ds_read_b128 a[32:35], v7 offset:5120\n"
      "v_cvt_pk_bf16_f32 v48, v48, v49\n"
      "v_mfma_f32_16x16x32_bf16 v[24:27], a[0:3], v[124:127], v[24:27]\n"
      "ds_read_b128 a[0:3], v6 offset:4608\n"
      "v_sub_f32_e32 v50, v38, v42\n"
      "v_mfma_f32_16x16x32_bf16 v[24:27], a[4:7], v[180:183], v[24:27]\n"
      "ds_read_b128 a[4:7], v6 offset:4864\n"
      "v_sub_f32_e32 v51, v39, v43\n"
      "v_mfma_f32_16x16x32_bf16 v[24:27], a[8:11], v[232:235], v[24:27]\n"
      "ds_read_b128 a[8:11], v6 offset:5120\n"
      "v_cvt_pk_bf16_f32 v49, v50, v51\n"
      "v_mfma_f32_16x16x32_bf16 v[24:27], a[12:15], a[216:219], v[24:27]\n"
      "ds_read_b128 a[12:15], v7 offset:0\n"
      "v_add_f32_e32 v40, v36, v40\n"
      "v_mfma_f32_16x16x32_bf16 v[24:27], a[16:19], v[196:199], v[24:27]\n"
      "ds_read_b128 a[16:19], v7 offset:256\n"
      "v_add_f32_e32 v41, v37, v41\n"
      "v_mfma_f32_16x16x32_bf16 v[24:27], a[20:23], v[252:255], v[24:27]\n"
      "s_waitcnt lgkmcnt(7)\n"
      "ds_read_b128 a[20:23], v7 offset:512\n"
      "v_cvt_pk_bf16_f32 v40, v40, v41\n"
      "v_mfma_f32_16x16x32_bf16 v[52:55], a[48:51], a[72:75], 0\n"
      "ds_write_addtid_b32 v21 offset:49416\n"
      "v_add_f32_e32 v42, v38, v42\n"
      "v_mfma_f32_16x16x32_bf16 v[60:63], a[56:59], a[80:83], v[52:55]\n"
      "s_mov_b32 m0, s58\n"
      "v_cvt_pk_bf16_f32 v24, v24, v25\n"
      "v_cvt_pk_bf16_f32 v25, v26, v27\n"
      "v_mfma_f32_16x16x32_bf16 v[72:75], a[84:87], a[64:67], 0\n"
      "buffer_load_dwordx4 v18, s[32:35], s53 offen offset:2048 lds\n"
      "v_add_f32_e32 v43, v39, v43\n"
      "v_mfma_f32_16x16x32_bf16 v[28:31], a[24:27], a[160:163], 0\n"
      "s_waitcnt lgkmcnt(5)\n"
      "v_cvt_pk_bf16_f32 v41, v42, v43\n"
      "v_cvt_pk_bf16_f32 v44, v44, v45\n"
      "v_mfma_f32_16x16x32_bf16 v[28:31], a[28:31], a[172:175], v[28:31]\n"
      "s_mov_b32 m0, s45\n"
      "v_cvt_pk_bf16_f32 v82, v68, v69\n"
      "v_cvt_pk_bf16_f32 v83, v72, v73\n"
      "v_mfma_f32_16x16x32_bf16 v[28:31], a[32:35], a[184:187], v[28:31]\n"
      "ds_write_addtid_b32 v24 offset:36864\n"
      "ds_write_addtid_b32 v25 offset:37120\n"
      "v_mfma_f32_16x16x32_bf16 v[28:31], a[0:3], a[88:91], v[28:31]\n"
      "s_mov_b32 m0, s18\n"
      "v_cvt_pk_bf16_f32 v45, v46, v47\n"
      "v_cvt_pk_bf16_f32 v88, v70, v71\n"
      "v_mfma_f32_16x16x32_bf16 v[28:31], a[4:7], a[100:103], v[28:31]\n"
      "s_waitcnt lgkmcnt(3)\n"
      "ds_write_addtid_b32 v48 offset:1792\n"
      "ds_write_addtid_b32 v49 offset:38720\n"
      "v_mfma_f32_16x16x32_bf16 v[28:31], a[8:11], a[112:115], v[28:31]\n"
      "ds_write_addtid_b32 v40 offset:1024\n"
      "ds_write_addtid_b32 v41 offset:37952\n"
      "v_mfma_f32_16x16x32_bf16 v[28:31], a[12:15], a[124:127], v[28:31]\n"
      "ds_write_addtid_b32 v44 offset:256\n"
      "ds_write_addtid_b32 v45 offset:37184\n"
      "v_mfma_f32_16x16x32_bf16 v[28:31], a[16:19], a[136:139], v[28:31]\n"
      "buffer_store_dwordx4 v[80:83], v14, s[36:39], s52 offen sc1\n"
      "v_cvt_pk_bf16_f32 v89, v74, v75\n"
      "v_mfma_f32_16x16x32_bf16 v[28:31], a[20:23], a[148:151], v[28:31]\n"
      "s_mov_b32 m0, s45\n"
      "ds_read2_b64 a[60:63], v12 offset0:1 offset1:65\n"
      "v_cvt_pk_bf16_f32 v60, v60, v61\n"
      "v_mfma_f32_16x16x32_bf16 v[56:59], a[52:55], a[76:79], 0\n"
      "v_cvt_pk_bf16_f32 v61, v62, v63\n"
      "v_swap_b32 v6, v7\n"
      "v_mfma_f32_16x16x32_bf16 v[20:23], a[24:27], a[164:167], 0\n"
      "v_mfma_f32_16x16x32_bf16 v[20:23], a[28:31], a[176:179], v[20:23]\n"
      "v_cvt_pk_bf16_f32 v28, v28, v29\n"
      "v_cvt_pk_bf16_f32 v29, v30, v31\n"
      "v_mfma_f32_16x16x32_bf16 v[20:23], a[32:35], a[188:191], v[20:23]\n"
      "buffer_store_dwordx4 v[86:89], v14, s[36:39], s52 offen offset:1536 sc1\n"
      "v_sub_f32_e32 v64, v52, v56\n"
      "v_mfma_f32_16x16x32_bf16 v[20:23], a[0:3], a[92:95], v[20:23]\n"
      "ds_write_addtid_b32 v28 offset:60432\n"
      "ds_write_addtid_b32 v29 offset:60688\n"
      "v_mfma_f32_16x16x32_bf16 v[20:23], a[4:7], a[104:107], v[20:23]\n"
      "s_mov_b32 m0, s18\n"
      "v_sub_f32_e32 v65, v53, v57\n"
      "v_cvt_pk_bf16_f32 v64, v64, v65\n"
      "v_mfma_f32_16x16x32_bf16 v[20:23], a[8:11], a[116:119], v[20:23]\n"
      "ds_write_addtid_b32 v64 offset:2048\n"
      "v_sub_f32_e32 v66, v54, v58\n"
      "v_mfma_f32_16x16x32_bf16 v[20:23], a[12:15], a[128:131], v[20:23]\n"
      "v_sub_f32_e32 v67, v55, v59\n"
      "v_cvt_pk_bf16_f32 v65, v66, v67\n"
      "v_mfma_f32_16x16x32_bf16 v[20:23], a[16:19], a[140:143], v[20:23]\n"
      "ds_write_addtid_b32 v65 offset:38976\n"
      "v_add_f32_e32 v56, v52, v56\n"
      "v_mfma_f32_16x16x32_bf16 v[20:23], a[20:23], a[152:155], v[20:23]\n"
      "v_add_f32_e32 v57, v53, v57\n"
      "v_cvt_pk_bf16_f32 v56, v56, v57\n"
      "v_mfma_f32_16x16x32_bf16 v[76:79], a[84:87], a[68:71], 0\n"
      "ds_write_addtid_b32 v56 offset:1280\n"
      "v_add_f32_e32 v58, v54, v58\n"
      "v_mfma_f32_16x16x32_bf16 v[32:35], a[24:27], a[168:171], 0\n"
      "ds_write_addtid_b32 v60 offset:512\n"
      "v_add_f32_e32 v59, v55, v59\n"
      "v_mfma_f32_16x16x32_bf16 v[32:35], a[28:31], a[180:183], v[32:35]\n"
      "ds_write_addtid_b32 v61 offset:37440\n"
      "v_cvt_pk_bf16_f32 v57, v58, v59\n"
      "v_mfma_f32_16x16x32_bf16 v[32:35], a[32:35], a[192:195], v[32:35]\n"
      "ds_write_addtid_b32 v57 offset:38208\n"
      "v_cvt_pk_bf16_f32 v20, v20, v21\n"
      "v_mfma_f32_16x16x32_bf16 v[32:35], a[0:3], a[96:99], v[32:35]\n"
      "s_mov_b32 m0, s45\n"
      "v_cvt_pk_bf16_f32 v21, v22, v23\n"
      "v_cvt_pk_bf16_f32 v84, v76, v77\n"
      "v_mfma_f32_16x16x32_bf16 v[32:35], a[4:7], a[108:111], v[32:35]\n"
      "s_mov_b32 m0, s59\n"
      "v_cvt_pk_bf16_f32 v90, v78, v79\n"
      "v_mfma_f32_16x16x32_bf16 v[32:35], a[8:11], a[120:123], v[32:35]\n"
      "buffer_load_dwordx4 v19, s[32:35], s96 offen lds\n"
      "v_mfma_f32_16x16x32_bf16 v[32:35], a[12:15], a[132:135], v[32:35]\n"
      "s_mov_b32 m0, s45\n"
      "v_mfma_f32_16x16x32_bf16 v[32:35], a[16:19], a[144:147], v[32:35]\n"
      "ds_write_addtid_b32 v20 offset:48136\n"
      "ds_write_addtid_b32 v21 offset:48392\n"
      "v_mfma_f32_16x16x32_bf16 v[32:35], a[20:23], a[156:159], v[32:35]\n"
      "s_nop 1\n"
      "v_mfma_f32_16x16x32_bf16 v[20:23], a[12:15], a[88:91], 0\n"
      "s_add_u32 s51, s51, s12\n"
      "s_nop 1\n"
      "v_mfma_f32_16x16x32_bf16 v[20:23], a[16:19], a[100:103], v[20:23]\n"
      "s_sub_u32 s47, s51, s48\n"
      "v_cvt_pk_bf16_f32 v32, v32, v33\n"
      "v_cvt_pk_bf16_f32 v33, v34, v35\n"
      "v_mfma_f32_16x16x32_bf16 v[20:23], a[20:23], a[112:115], v[20:23]\n"
      "s_cmp_ge_u32 vcc_lo, 0x40000000\n"
      "ds_write_addtid_b32 v32 offset:35840\n"
      "v_mfma_f32_16x16x32_bf16 v[20:23], a[24:27], a[124:127], v[20:23]\n"
      "s_cselect_b32 s53, s47, s51\n"
      "ds_write_addtid_b32 v33 offset:36096\n"
      "v_mfma_f32_16x16x32_bf16 v[20:23], a[28:31], a[136:139], v[20:23]\n"
      "s_mul_i32 vcc_lo, vcc_lo, 2\n"
      "v_mfma_f32_16x16x32_bf16 v[20:23], a[32:35], a[148:151], v[20:23]\n"
      "v_mfma_f32_16x16x32_bf16 v[24:27], a[12:15], a[92:95], 0\n"
      "s_waitcnt vmcnt(19) lgkmcnt(0)\n"
      "s_barrier\n"
      "v_mfma_f32_16x16x32_bf16 v[24:27], a[16:19], a[104:107], v[24:27]\n"
      "s_add_u32 m0, s58, 13280\n"
      "ds_read_b128 a[0:3], v7 offset:0\n"
      "v_mfma_f32_16x16x32_bf16 v[24:27], a[20:23], a[116:119], v[24:27]\n"
      "ds_read_b128 a[4:7], v7 offset:256\n"
      "v_mfma_f32_16x16x32_bf16 v[24:27], a[24:27], a[128:131], v[24:27]\n"
      "ds_read_b128 a[8:11], v7 offset:512\n"
      "v_mfma_f32_16x16x32_bf16 v[24:27], a[28:31], a[140:143], v[24:27]\n"
      "ds_read_b128 a[36:39], v2 offset:0\n"
      "v_mfma_f32_16x16x32_bf16 v[24:27], a[32:35], a[152:155], v[24:27]\n"
      "ds_read_b128 a[40:43], v3 offset:0\n"
      "v_mfma_f32_16x16x32_bf16 v[68:71], a[84:87], a[60:63], 0\n"
      "ds_read_b128 a[44:47], v4 offset:0\n"
      "s_nop 0\n"
      "v_mfma_f32_16x16x32_bf16 v[28:31], a[12:15], a[96:99], 0\n"
      "ds_read2_b64 a[64:67], v11 offset0:0 offset1:64\n"
      "s_nop 0\n"
      "v_mfma_f32_16x16x32_bf16 v[28:31], a[16:19], a[108:111], v[28:31]\n"
      "buffer_load_dwordx4 v16, s[32:35], s53 offen lds\n"
      "v_mfma_f32_16x16x32_bf16 v[28:31], a[20:23], a[120:123], v[28:31]\n"
      "s_mov_b32 m0, s45\n"
      "ds_read2_b64 a[68:71], v11 offset0:1 offset1:65\n"
      "v_cvt_pk_bf16_f32 v85, v68, v69\n"
      "v_mfma_f32_16x16x32_bf16 v[28:31], a[24:27], a[132:135], v[28:31]\n"
      "ds_read_b128 a[12:15], v6 offset:768\n"
      "v_cvt_pk_bf16_f32 v91, v70, v71\n"
      "v_mfma_f32_16x16x32_bf16 v[28:31], a[28:31], a[144:147], v[28:31]\n"
      "ds_read_b128 a[16:19], v6 offset:1024\n"
      "v_mfma_f32_16x16x32_bf16 v[28:31], a[32:35], a[156:159], v[28:31]\n"
      "s_waitcnt lgkmcnt(6)\n"
      "ds_read_b128 a[20:23], v6 offset:1280\n"
      "v_mfma_f32_16x16x32_bf16 v[20:23], a[0:3], a[160:163], v[20:23]\n"
      "buffer_store_dwordx2 v[84:85], v15, s[36:39], s52 offen sc1\n"
      "v_mfma_f32_16x16x32_bf16 v[20:23], a[4:7], a[172:175], v[20:23]\n"
      "ds_read_b128 a[24:27], v6 offset:5376\n"
      "v_mfma_f32_16x16x32_bf16 v[20:23], a[8:11], a[184:187], v[20:23]\n"
      "ds_read_b128 a[28:31], v6 offset:5632\n"
      "s_nop 0\n"
      "v_mfma_f32_16x16x32_bf16 v[36:39], a[36:39], a[72:75], 0\n"
      "s_waitcnt lgkmcnt(6)\n"
      "ds_read_b128 a[32:35], v6 offset:5888\n"
      "s_nop 0\n"
      "v_mfma_f32_16x16x32_bf16 v[40:43], a[40:43], a[76:79], 0\n"
      "ds_read_b128 a[48:51], v2 offset:64\n"
      "v_cvt_pk_bf16_f32 v20, v20, v21\n"
      "v_mfma_f32_16x16x32_bf16 v[44:47], a[44:47], a[80:83], v[36:39]\n"
      "buffer_store_dwordx2 v[90:91], v15, s[36:39], s52 offen offset:1536 sc1\n"
      "v_cvt_pk_bf16_f32 v21, v22, v23\n"
      "v_mfma_f32_16x16x32_bf16 v[24:27], a[0:3], a[164:167], v[24:27]\n"
      "s_mov_b32 s52, s13\n"
      "ds_write_addtid_b32 v20 offset:57360\n"
      "v_sub_f32_e32 v48, v36, v40\n"
      "v_mfma_f32_16x16x32_bf16 v[24:27], a[4:7], a[176:179], v[24:27]\n"
      "s_add_u32 s13, s13, s12\n"
      "ds_read_b128 a[52:55], v3 offset:64\n"
      "v_sub_f32_e32 v49, v37, v41\n"
      "v_mfma_f32_16x16x32_bf16 v[24:27], a[8:11], a[188:191], v[24:27]\n"
      "ds_write_addtid_b32 v21 offset:57616\n"
      "v_cvt_pk_bf16_f32 v48, v48, v49\n"
      "v_mfma_f32_16x16x32_bf16 v[72:75], a[84:87], a[64:67], 0\n"
      "ds_read_b128 a[56:59], v4 offset:64\n"
      "v_sub_f32_e32 v50, v38, v42\n"
      "v_mfma_f32_16x16x32_bf16 v[28:31], a[0:3], a[168:171], v[28:31]\n"
      "ds_read_b128 a[0:3], v7 offset:768\n"
      "v_sub_f32_e32 v51, v39, v43\n"
      "v_mfma_f32_16x16x32_bf16 v[28:31], a[4:7], a[180:183], v[28:31]\n"
      "ds_read_b128 a[4:7], v7 offset:1024\n"
      "v_cvt_pk_bf16_f32 v24, v24, v25\n"
      "v_mfma_f32_16x16x32_bf16 v[28:31], a[8:11], a[192:195], v[28:31]\n"
      "s_waitcnt lgkmcnt(6)\n"
      "ds_read_b128 a[8:11], v7 offset:1280\n"
      "v_cvt_pk_bf16_f32 v25, v26, v27\n"
      "v_mfma_f32_16x16x32_bf16 v[76:79], a[84:87], a[68:71], 0\n"
      "ds_read2_b64 a[60:63], v11 offset0:2 offset1:66\n"
      "v_cvt_pk_bf16_f32 v49, v50, v51\n"
      "v_mfma_f32_16x16x32_bf16 v[32:35], a[12:15], v[92:95], 0\n"
      "ds_write_addtid_b32 v24 offset:45064\n"
      "ds_write_addtid_b32 v25 offset:45320\n"
      "v_mfma_f32_16x16x32_bf16 v[32:35], a[16:19], v[148:151], v[32:35]\n"
      "s_mov_b32 m0, s18\n"
      "v_add_f32_e32 v40, v36, v40\n"
      "v_add_f32_e32 v41, v37, v41\n"
      "v_mfma_f32_16x16x32_bf16 v[32:35], a[20:23], v[204:207], v[32:35]\n"
      "ds_write_addtid_b32 v48 offset:6144\n"
      "v_add_f32_e32 v42, v38, v42\n"
      "v_mfma_f32_16x16x32_bf16 v[32:35], a[24:27], v[112:115], v[32:35]\n"
      "ds_write_addtid_b32 v49 offset:43072\n"
      "v_add_f32_e32 v43, v39, v43\n"
      "v_mfma_f32_16x16x32_bf16 v[32:35], a[28:31], a[208:211], v[32:35]\n"
      "v_cvt_pk_bf16_f32 v40, v40, v41\n"
      "v_cvt_pk_bf16_f32 v41, v42, v43\n"
      "v_mfma_f32_16x16x32_bf16 v[32:35], a[32:35], v[220:223], v[32:35]\n"
      "s_waitcnt lgkmcnt(6)\n"
      "ds_write_addtid_b32 v40 offset:5376\n"
      "v_cvt_pk_bf16_f32 v44, v44, v45\n"
      "v_mfma_f32_16x16x32_bf16 v[32:35], a[0:3], a[212:215], v[32:35]\n"
      "ds_write_addtid_b32 v41 offset:42304\n"
      "v_cvt_pk_bf16_f32 v45, v46, v47\n"
      "v_mfma_f32_16x16x32_bf16 v[32:35], a[4:7], v[184:187], v[32:35]\n"
      "s_waitcnt lgkmcnt(6)\n"
      "ds_write_addtid_b32 v44 offset:4608\n"
      "ds_write_addtid_b32 v45 offset:41536\n"
      "v_mfma_f32_16x16x32_bf16 v[32:35], a[8:11], v[240:243], v[32:35]\n"
      "s_mov_b32 m0, s45\n"
      "ds_read_b128 a[36:39], v2 offset:128\n"
      "v_cvt_pk_bf16_f32 v28, v28, v29\n"
      "v_mfma_f32_16x16x32_bf16 v[20:23], a[12:15], v[100:103], 0\n"
      "ds_write_addtid_b32 v28 offset:32768\n"
      "v_cvt_pk_bf16_f32 v29, v30, v31\n"
      "v_mfma_f32_16x16x32_bf16 v[20:23], a[16:19], a[200:203], v[20:23]\n"
      "ds_write_addtid_b32 v29 offset:33024\n"
      "v_cvt_pk_bf16_f32 v32, v32, v33\n"
      "v_mfma_f32_16x16x32_bf16 v[20:23], a[20:23], v[208:211], v[20:23]\n"
      "s_add_u32 m0, s58, 13280\n"
      "ds_read_b128 a[44:47], v4 offset:128\n"
      "v_cvt_pk_bf16_f32 v33, v34, v35\n"
      "v_mfma_f32_16x16x32_bf16 v[20:23], a[24:27], a[204:207], v[20:23]\n"
      "buffer_load_dwordx4 v17, s[32:35], s53 offen offset:1024 lds\n"
      "v_cvt_pk_bf16_f32 v80, v72, v73\n"
      "v_mfma_f32_16x16x32_bf16 v[20:23], a[28:31], v[172:175], v[20:23]\n"
      "s_mov_b32 m0, s45\n"
      "ds_read2_b64 a[64:67], v11 offset0:3 offset1:67\n"
      "v_cvt_pk_bf16_f32 v81, v76, v77\n"
      "v_mfma_f32_16x16x32_bf16 v[20:23], a[32:35], v[228:231], v[20:23]\n"
      "ds_write_addtid_b32 v32 offset:58384\n"
      "v_cvt_pk_bf16_f32 v86, v74, v75\n"
      "v_mfma_f32_16x16x32_bf16 v[20:23], a[0:3], v[136:139], v[20:23]\n"
      "ds_write_addtid_b32 v33 offset:58640\n"
      "v_cvt_pk_bf16_f32 v87, v78, v79\n"
      "v_mfma_f32_16x16x32_bf16 v[20:23], a[4:7], v[192:195], v[20:23]\n"
      "ds_read_b128 a[40:43], v3 offset:128\n"
      "v_mfma_f32_16x16x32_bf16 v[20:23], a[8:11], v[244:247], v[20:23]\n"
      "global_load_dword v8, v9, s[54:55]\n"
      "s_nop 0\n"
      "v_mfma_f32_16x16x32_bf16 v[52:55], a[48:51], a[72:75], 0\n"
      "ds_read2_b64 a[68:71], v13 offset0:0 offset1:64\n"
      "s_nop 0\n"
      "v_mfma_f32_16x16x32_bf16 v[56:59], a[52:55], a[76:79], 0\n"
      "s_nop 0\n"
      "v_mfma_f32_16x16x32_bf16 v[60:63], a[56:59], a[80:83], v[52:55]\n"
      "v_cvt_pk_bf16_f32 v20, v20, v21\n"
      "v_cvt_pk_bf16_f32 v21, v22, v23\n"
      "v_mfma_f32_16x16x32_bf16 v[68:71], a[84:87], a[60:63], 0\n"
      "ds_write_addtid_b32 v20 offset:46088\n"
      "s_nop 0\n"
      "v_mfma_f32_16x16x32_bf16 v[24:27], a[12:15], a[196:199], 0\n"
      "ds_read_b128 a[12:15], v6 offset:1536\n"
      "v_sub_f32_e32 v64, v52, v56\n"
      "v_mfma_f32_16x16x32_bf16 v[24:27], a[16:19], v[160:163], v[24:27]\n"
      "ds_read_b128 a[16:19], v6 offset:1792\n"
      "v_sub_f32_e32 v65, v53, v57\n"
      "v_mfma_f32_16x16x32_bf16 v[24:27], a[20:23], v[216:219], v[24:27]\n"
      "ds_read_b128 a[20:23], v6 offset:2048\n"
      "v_cvt_pk_bf16_f32 v64, v64, v65\n"
      "v_mfma_f32_16x16x32_bf16 v[24:27], a[24:27], v[124:127], v[24:27]\n"
      "ds_read_b128 a[24:27], v6 offset:6144\n"
      "v_sub_f32_e32 v66, v54, v58\n"
      "v_mfma_f32_16x16x32_bf16 v[24:27], a[28:31], v[180:183], v[24:27]\n"
      "ds_read_b128 a[28:31], v6 offset:6400\n"
      "v_sub_f32_e32 v67, v55, v59\n"
      "v_mfma_f32_16x16x32_bf16 v[24:27], a[32:35], v[232:235], v[24:27]\n"
      "ds_read_b128 a[32:35], v6 offset:6656\n"
      "v_cvt_pk_bf16_f32 v65, v66, v67\n"
      "v_mfma_f32_16x16x32_bf16 v[24:27], a[0:3], a[216:219], v[24:27]\n"
      "ds_read_b128 a[0:3], v7 offset:1536\n"
      "v_add_f32_e32 v56, v52, v56\n"
      "v_mfma_f32_16x16x32_bf16 v[24:27], a[4:7], v[196:199], v[24:27]\n"
      "ds_read_b128 a[4:7], v7 offset:1792\n"
      "v_add_f32_e32 v57, v53, v57\n"
      "v_mfma_f32_16x16x32_bf16 v[24:27], a[8:11], v[252:255], v[24:27]\n"
      "s_waitcnt lgkmcnt(7)\n"
      "ds_read_b128 a[8:11], v7 offset:2048\n"
      "v_cvt_pk_bf16_f32 v56, v56, v57\n"
      "v_mfma_f32_16x16x32_bf16 v[36:39], a[36:39], a[72:75], 0\n"
      "ds_write_addtid_b32 v21 offset:46344\n"
      "v_add_f32_e32 v58, v54, v58\n"
      "v_mfma_f32_16x16x32_bf16 v[44:47], a[44:47], a[80:83], v[36:39]\n"
      "s_add_u32 m0, s58, 13280\n"
      "v_cvt_pk_bf16_f32 v24, v24, v25\n"
      "v_cvt_pk_bf16_f32 v25, v26, v27\n"
      "v_mfma_f32_16x16x32_bf16 v[72:75], a[84:87], a[64:67], 0\n"
      "buffer_load_dwordx4 v18, s[32:35], s53 offen offset:2048 lds\n"
      "v_add_f32_e32 v59, v55, v59\n"
      "v_mfma_f32_16x16x32_bf16 v[28:31], a[12:15], v[96:99], 0\n"
      "s_waitcnt lgkmcnt(5)\n"
      "v_cvt_pk_bf16_f32 v57, v58, v59\n"
      "v_cvt_pk_bf16_f32 v60, v60, v61\n"
      "v_mfma_f32_16x16x32_bf16 v[28:31], a[16:19], v[152:155], v[28:31]\n"
      "s_mov_b32 m0, s45\n"
      "v_cvt_pk_bf16_f32 v82, v68, v69\n"
      "v_cvt_pk_bf16_f32 v83, v72, v73\n"
      "v_mfma_f32_16x16x32_bf16 v[28:31], a[20:23], a[220:223], v[28:31]\n"
      "ds_write_addtid_b32 v24 offset:33792\n"
      "ds_write_addtid_b32 v25 offset:34048\n"
      "v_mfma_f32_16x16x32_bf16 v[28:31], a[24:27], v[116:119], v[28:31]\n"
      "s_mov_b32 m0, s18\n"
      "v_cvt_pk_bf16_f32 v61, v62, v63\n"
      "v_cvt_pk_bf16_f32 v88, v70, v71\n"
      "v_mfma_f32_16x16x32_bf16 v[28:31], a[28:31], v[168:171], v[28:31]\n"
      "s_waitcnt lgkmcnt(3)\n"
      "ds_write_addtid_b32 v64 offset:6400\n"
      "ds_write_addtid_b32 v65 offset:43328\n"
      "v_mfma_f32_16x16x32_bf16 v[28:31], a[32:35], v[224:227], v[28:31]\n"
      "ds_write_addtid_b32 v56 offset:5632\n"
      "ds_write_addtid_b32 v57 offset:42560\n"
      "v_mfma_f32_16x16x32_bf16 v[28:31], a[0:3], v[132:135], v[28:31]\n"
      "ds_write_addtid_b32 v60 offset:4864\n"
      "ds_write_addtid_b32 v61 offset:41792\n"
      "v_mfma_f32_16x16x32_bf16 v[28:31], a[4:7], v[188:191], v[28:31]\n"
      "buffer_store_dwordx4 v[80:83], v14, s[36:39], s52 offen sc1\n"
      "v_cvt_pk_bf16_f32 v89, v74, v75\n"
      "v_mfma_f32_16x16x32_bf16 v[28:31], a[8:11], a[240:243], v[28:31]\n"
      "s_mov_b32 m0, s45\n"
      "ds_read2_b64 a[60:63], v13 offset0:1 offset1:65\n"
      "v_cvt_pk_bf16_f32 v44, v44, v45\n"
      "v_mfma_f32_16x16x32_bf16 v[40:43], a[40:43], a[76:79], 0\n"
      "v_cvt_pk_bf16_f32 v45, v46, v47\n"
      "s_nop 0\n"
      "v_mfma_f32_16x16x32_bf16 v[20:23], a[12:15], v[104:107], 0\n"
      "v_mfma_f32_16x16x32_bf16 v[20:23], a[16:19], v[156:159], v[20:23]\n"
      "v_cvt_pk_bf16_f32 v28, v28, v29\n"
      "v_cvt_pk_bf16_f32 v29, v30, v31\n"
      "v_mfma_f32_16x16x32_bf16 v[20:23], a[20:23], v[212:215], v[20:23]\n"
      "buffer_store_dwordx4 v[86:89], v14, s[36:39], s52 offen offset:1536 sc1\n"
      "v_sub_f32_e32 v48, v36, v40\n"
      "v_mfma_f32_16x16x32_bf16 v[20:23], a[24:27], v[120:123], v[20:23]\n"
      "ds_write_addtid_b32 v28 offset:59408\n"
      "ds_write_addtid_b32 v29 offset:59664\n"
      "v_mfma_f32_16x16x32_bf16 v[20:23], a[28:31], v[176:179], v[20:23]\n"
      "s_mov_b32 m0, s18\n"
      "v_sub_f32_e32 v49, v37, v41\n"
      "v_cvt_pk_bf16_f32 v48, v48, v49\n"
      "v_mfma_f32_16x16x32_bf16 v[20:23], a[32:35], a[232:235], v[20:23]\n"
      "s_sub_u32 s18, s19, s18\n"
      "ds_write_addtid_b32 v48 offset:6656\n"
      "v_sub_f32_e32 v50, v38, v42\n"
      "v_mfma_f32_16x16x32_bf16 v[20:23], a[0:3], v[140:143], v[20:23]\n"
      "v_sub_f32_e32 v51, v39, v43\n"
      "v_cvt_pk_bf16_f32 v49, v50, v51\n"
      "v_mfma_f32_16x16x32_bf16 v[20:23], a[4:7], a[236:239], v[20:23]\n"
      "ds_write_addtid_b32 v49 offset:43584\n"
      "v_add_f32_e32 v40, v36, v40\n"
      "v_mfma_f32_16x16x32_bf16 v[20:23], a[8:11], v[248:251], v[20:23]\n"
      "v_add_f32_e32 v41, v37, v41\n"
      "v_cvt_pk_bf16_f32 v40, v40, v41\n"
      "v_mfma_f32_16x16x32_bf16 v[76:79], a[84:87], a[68:71], 0\n"
      "ds_write_addtid_b32 v40 offset:5888\n"
      "v_add_f32_e32 v42, v38, v42\n"
      "v_mfma_f32_16x16x32_bf16 v[32:35], a[12:15], v[108:111], 0\n"
      "ds_write_addtid_b32 v44 offset:5120\n"
      "v_add_f32_e32 v43, v39, v43\n"
      "v_mfma_f32_16x16x32_bf16 v[32:35], a[16:19], v[164:167], v[32:35]\n"
      "ds_write_addtid_b32 v45 offset:42048\n"
      "v_cvt_pk_bf16_f32 v41, v42, v43\n"
      "v_mfma_f32_16x16x32_bf16 v[32:35], a[20:23], a[224:227], v[32:35]\n"
      "ds_write_addtid_b32 v41 offset:42816\n"
      "v_cvt_pk_bf16_f32 v20, v20, v21\n"
      "v_mfma_f32_16x16x32_bf16 v[32:35], a[24:27], v[128:131], v[32:35]\n"
      "s_mov_b32 m0, s45\n"
      "v_cvt_pk_bf16_f32 v21, v22, v23\n"
      "v_cvt_pk_bf16_f32 v84, v76, v77\n"
      "v_mfma_f32_16x16x32_bf16 v[32:35], a[28:31], a[228:231], v[32:35]\n"
      "ds_write_addtid_b32 v20 offset:47112\n"
      "v_cvt_pk_bf16_f32 v90, v78, v79\n"
      "v_mfma_f32_16x16x32_bf16 v[32:35], a[32:35], v[236:239], v[32:35]\n"
      "ds_write_addtid_b32 v21 offset:47368\n"
      "v_mfma_f32_16x16x32_bf16 v[32:35], a[0:3], v[144:147], v[32:35]\n"
      "v_mfma_f32_16x16x32_bf16 v[32:35], a[4:7], v[200:203], v[32:35]\n"
      "v_mfma_f32_16x16x32_bf16 v[32:35], a[8:11], a[244:247], v[32:35]\n"
      "s_nop 1\n"
      "s_cbranch_vccnz Lconv_loop0\n"
      "Lconv_t0:\n"
      "s_nop 1\n"
      "v_mfma_f32_16x16x32_bf16 v[20:23], a[24:27], v[96:99], 0\n"
      "s_nop 1\n"
      "v_mfma_f32_16x16x32_bf16 v[20:23], a[28:31], v[152:155], v[20:23]\n"
      "v_cvt_pk_bf16_f32 v32, v32, v33\n"
      "v_cvt_pk_bf16_f32 v33, v34, v35\n"
      "v_mfma_f32_16x16x32_bf16 v[20:23], a[32:35], a[220:223], v[20:23]\n"
      "ds_write_addtid_b32 v32 offset:34816\n"
      "v_mfma_f32_16x16x32_bf16 v[20:23], a[0:3], v[116:119], v[20:23]\n"
      "ds_write_addtid_b32 v33 offset:35072\n"
      "v_mfma_f32_16x16x32_bf16 v[20:23], a[4:7], v[168:171], v[20:23]\n"
      "v_mfma_f32_16x16x32_bf16 v[20:23], a[8:11], v[224:227], v[20:23]\n"
      "v_mfma_f32_16x16x32_bf16 v[24:27], a[24:27], v[104:107], 0\n"
      "s_waitcnt lgkmcnt(0)\n"
      "s_barrier\n"
      "v_mfma_f32_16x16x32_bf16 v[24:27], a[28:31], v[156:159], v[24:27]\n"
      "ds_read2_b64 a[64:67], v10 offset0:0 offset1:64\n"
      "v_mfma_f32_16x16x32_bf16 v[24:27], a[32:35], v[212:215], v[24:27]\n"
      "ds_read_b128 a[12:15], v7 offset:6144\n"
      "v_mfma_f32_16x16x32_bf16 v[24:27], a[0:3], v[120:123], v[24:27]\n"
      "ds_read2_b64 a[68:71], v10 offset0:1 offset1:65\n"
      "v_mfma_f32_16x16x32_bf16 v[24:27], a[4:7], v[176:179], v[24:27]\n"
      "ds_read_b128 a[16:19], v7 offset:6400\n"
      "v_mfma_f32_16x16x32_bf16 v[24:27], a[8:11], a[232:235], v[24:27]\n"
      "ds_read_b128 a[20:23], v7 offset:6656\n"
      "v_mfma_f32_16x16x32_bf16 v[68:71], a[84:87], a[60:63], 0\n"
      "ds_read2_b64 a[60:63], v10 offset0:2 offset1:66\n"
      "s_nop 0\n"
      "v_mfma_f32_16x16x32_bf16 v[28:31], a[24:27], v[108:111], 0\n"
      "s_waitcnt lgkmcnt(5)\n"
      "ds_read_b128 a[24:27], v6 offset:5376\n"
      "s_nop 0\n"
      "v_mfma_f32_16x16x32_bf16 v[72:75], a[84:87], a[64:67], 0\n"
      "ds_read2_b64 a[64:67], v10 offset0:3 offset1:67\n"
      "v_cvt_pk_bf16_f32 v85, v68, v69\n"
      "v_mfma_f32_16x16x32_bf16 v[28:31], a[28:31], v[164:167], v[28:31]\n"
      "ds_read_b128 a[28:31], v6 offset:5632\n"
      "v_cvt_pk_bf16_f32 v91, v70, v71\n"
      "v_mfma_f32_16x16x32_bf16 v[28:31], a[32:35], a[224:227], v[28:31]\n"
      "buffer_store_dwordx2 v[84:85], v15, s[36:39], s52 offen sc1\n"
      "v_mfma_f32_16x16x32_bf16 v[28:31], a[0:3], v[128:131], v[28:31]\n"
      "ds_read_b128 a[32:35], v6 offset:5888\n"
      "v_cvt_pk_bf16_f32 v80, v72, v73\n"
      "v_mfma_f32_16x16x32_bf16 v[28:31], a[4:7], a[228:231], v[28:31]\n"
      "ds_read_b128 a[0:3], v7 offset:768\n"
      "v_cvt_pk_bf16_f32 v86, v74, v75\n"
      "v_mfma_f32_16x16x32_bf16 v[28:31], a[8:11], v[236:239], v[28:31]\n"
      "s_waitcnt lgkmcnt(6)\n"
      "ds_read_b128 a[4:7], v7 offset:1024\n"
      "v_mfma_f32_16x16x32_bf16 v[20:23], a[12:15], v[132:135], v[20:23]\n"
      "ds_read_b128 a[8:11], v7 offset:1280\n"
      "v_mfma_f32_16x16x32_bf16 v[76:79], a[84:87], a[68:71], 0\n"
      "s_waitcnt lgkmcnt(6)\n"
      "ds_read2_b64 a[68:71], v12 offset0:0 offset1:64\n"
      "s_nop 0\n"
      "v_mfma_f32_16x16x32_bf16 v[68:71], a[84:87], a[60:63], 0\n"
      "ds_read2_b64 a[60:63], v12 offset0:1 offset1:65\n"
      "s_nop 0\n"
      "v_mfma_f32_16x16x32_bf16 v[20:23], a[16:19], v[188:191], v[20:23]\n"
      "buffer_store_dwordx2 v[90:91], v15, s[36:39], s52 offen offset:1536 sc1\n"
      "v_cvt_pk_bf16_f32 v81, v76, v77\n"
      "v_mfma_f32_16x16x32_bf16 v[20:23], a[20:23], a[240:243], v[20:23]\n"
      "s_mov_b32 s52, s13\n"
      "v_cvt_pk_bf16_f32 v87, v78, v79\n"
      "v_cvt_pk_bf16_f32 v82, v68, v69\n"
      "v_mfma_f32_16x16x32_bf16 v[24:27], a[12:15], v[140:143], v[24:27]\n"
      "s_waitcnt lgkmcnt(5)\n"
      "v_cvt_pk_bf16_f32 v88, v70, v71\n"
      "s_nop 0\n"
      "v_mfma_f32_16x16x32_bf16 v[72:75], a[84:87], a[64:67], 0\n"
      "s_add_u32 s13, s13, s12\n"
      "v_cvt_pk_bf16_f32 v20, v20, v21\n"
      "v_cvt_pk_bf16_f32 v21, v22, v23\n"
      "v_mfma_f32_16x16x32_bf16 v[24:27], a[16:19], a[236:239], v[24:27]\n"
      "ds_write_addtid_b32 v20 offset:62480\n"
      "s_nop 0\n"
      "v_mfma_f32_16x16x32_bf16 v[24:27], a[20:23], v[248:251], v[24:27]\n"
      "ds_write_addtid_b32 v21 offset:62736\n"
      "v_cvt_pk_bf16_f32 v83, v72, v73\n"
      "v_mfma_f32_16x16x32_bf16 v[28:31], a[12:15], v[144:147], v[28:31]\n"
      "s_waitcnt lgkmcnt(3)\n"
      "ds_read_b128 a[12:15], v7 offset:5376\n"
      "v_cvt_pk_bf16_f32 v89, v74, v75\n"
      "v_mfma_f32_16x16x32_bf16 v[76:79], a[84:87], a[68:71], 0\n"
      "buffer_store_dwordx4 v[80:83], v14, s[36:39], s52 offen sc1\n"
      "v_cvt_pk_bf16_f32 v24, v24, v25\n"
      "v_mfma_f32_16x16x32_bf16 v[28:31], a[16:19], v[200:203], v[28:31]\n"
      "ds_read_b128 a[16:19], v7 offset:5632\n"
      "v_cvt_pk_bf16_f32 v25, v26, v27\n"
      "v_mfma_f32_16x16x32_bf16 v[28:31], a[20:23], a[244:247], v[28:31]\n"
      "ds_read_b128 a[20:23], v7 offset:5888\n"
      "v_mfma_f32_16x16x32_bf16 v[32:35], a[24:27], v[92:95], 0\n"
      "s_waitcnt lgkmcnt(5)\n"
      "ds_write_addtid_b32 v24 offset:50184\n"
      "v_cvt_pk_bf16_f32 v84, v76, v77\n"
      "v_mfma_f32_16x16x32_bf16 v[68:71], a[84:87], a[60:63], 0\n"
      "ds_write_addtid_b32 v25 offset:50440\n"
      "v_cvt_pk_bf16_f32 v90, v78, v79\n"
      "v_mfma_f32_16x16x32_bf16 v[32:35], a[28:31], v[148:151], v[32:35]\n"
      "buffer_store_dwordx4 v[86:89], v14, s[36:39], s52 offen offset:1536 sc1\n"
      "v_cvt_pk_bf16_f32 v28, v28, v29\n"
      "v_mfma_f32_16x16x32_bf16 v[32:35], a[32:35], v[204:207], v[32:35]\n"
      "ds_write_addtid_b32 v28 offset:37888\n"
      "v_cvt_pk_bf16_f32 v29, v30, v31\n"
      "v_mfma_f32_16x16x32_bf16 v[32:35], a[0:3], v[112:115], v[32:35]\n"
      "v_cvt_pk_bf16_f32 v85, v68, v69\n"
      "v_cvt_pk_bf16_f32 v91, v70, v71\n"
      "v_mfma_f32_16x16x32_bf16 v[32:35], a[4:7], a[208:211], v[32:35]\n"
      "ds_write_addtid_b32 v29 offset:38144\n"
      "v_mfma_f32_16x16x32_bf16 v[32:35], a[8:11], v[220:223], v[32:35]\n"
      "s_waitcnt lgkmcnt(5)\n"
      "v_mfma_f32_16x16x32_bf16 v[32:35], a[12:15], a[212:215], v[32:35]\n"
      "buffer_store_dwordx2 v[84:85], v15, s[36:39], s52 offen sc1\n"
      "v_mfma_f32_16x16x32_bf16 v[32:35], a[16:19], v[184:187], v[32:35]\n"
      "s_waitcnt lgkmcnt(4)\n"
      "v_mfma_f32_16x16x32_bf16 v[32:35], a[20:23], v[240:243], v[32:35]\n"
      "s_nop 1\n"
      "v_mfma_f32_16x16x32_bf16 v[20:23], a[24:27], v[100:103], 0\n"
      "s_nop 1\n"
      "v_mfma_f32_16x16x32_bf16 v[20:23], a[28:31], a[200:203], v[20:23]\n"
      "s_nop 0\n"
      "v_mfma_f32_16x16x32_bf16 v[20:23], a[32:35], v[208:211], v[20:23]\n"
      "buffer_store_dwordx2 v[90:91], v15, s[36:39], s52 offen offset:1536 sc1\n"
      "v_cvt_pk_bf16_f32 v32, v32, v33\n"
      "v_mfma_f32_16x16x32_bf16 v[20:23], a[0:3], a[204:207], v[20:23]\n"
      "ds_write_addtid_b32 v32 offset:61456\n"
      "v_cvt_pk_bf16_f32 v33, v34, v35\n"
      "v_mfma_f32_16x16x32_bf16 v[20:23], a[4:7], v[172:175], v[20:23]\n"
      "ds_write_addtid_b32 v33 offset:61712\n"
      "v_mfma_f32_16x16x32_bf16 v[20:23], a[8:11], v[228:231], v[20:23]\n"
      "v_mfma_f32_16x16x32_bf16 v[20:23], a[12:15], v[136:139], v[20:23]\n"
      "v_mfma_f32_16x16x32_bf16 v[20:23], a[16:19], v[192:195], v[20:23]\n"
      "v_mfma_f32_16x16x32_bf16 v[20:23], a[20:23], v[244:247], v[20:23]\n"
      "s_nop 1\n"
      "v_mfma_f32_16x16x32_bf16 v[24:27], a[24:27], a[196:199], 0\n"
      "ds_read_b128 a[24:27], v7 offset:4608\n"
      "s_nop 0\n"
      "v_mfma_f32_16x16x32_bf16 v[24:27], a[28:31], v[160:163], v[24:27]\n"
      "ds_read_b128 a[28:31], v7 offset:4864\n"
      "v_mfma_f32_16x16x32_bf16 v[24:27], a[32:35], v[216:219], v[24:27]\n"
      "ds_read_b128 a[32:35], v7 offset:5120\n"
      "v_cvt_pk_bf16_f32 v20, v20, v21\n"
      "v_mfma_f32_16x16x32_bf16 v[24:27], a[0:3], v[124:127], v[24:27]\n"
      "ds_read_b128 a[0:3], v6 offset:4608\n"
      "v_cvt_pk_bf16_f32 v21, v22, v23\n"
      "v_mfma_f32_16x16x32_bf16 v[24:27], a[4:7], v[180:183], v[24:27]\n"
      "ds_read_b128 a[4:7], v6 offset:4864\n"
      "v_mfma_f32_16x16x32_bf16 v[24:27], a[8:11], v[232:235], v[24:27]\n"
      "ds_read_b128 a[8:11], v6 offset:5120\n"
      "v_mfma_f32_16x16x32_bf16 v[24:27], a[12:15], a[216:219], v[24:27]\n"
      "ds_read_b128 a[12:15], v7 offset:0\n"
      "v_mfma_f32_16x16x32_bf16 v[24:27], a[16:19], v[196:199], v[24:27]\n"
      "ds_read_b128 a[16:19], v7 offset:256\n"
      "v_mfma_f32_16x16x32_bf16 v[24:27], a[20:23], v[252:255], v[24:27]\n"
      "s_waitcnt lgkmcnt(7)\n"
      "ds_read_b128 a[20:23], v7 offset:512\n"
      "s_nop 0\n"
      "v_mfma_f32_16x16x32_bf16 v[28:31], a[24:27], a[160:163], 0\n"
      "s_waitcnt lgkmcnt(7)\n"
      "ds_write_addtid_b32 v20 offset:49160\n"
      "ds_write_addtid_b32 v21 offset:49416\n"
      "v_mfma_f32_16x16x32_bf16 v[28:31], a[28:31], a[172:175], v[28:31]\n"
      "s_waitcnt lgkmcnt(8)\n"
      "v_cvt_pk_bf16_f32 v24, v24, v25\n"
      "v_cvt_pk_bf16_f32 v25, v26, v27\n"
      "v_mfma_f32_16x16x32_bf16 v[28:31], a[32:35], a[184:187], v[28:31]\n"
      "s_waitcnt lgkmcnt(7)\n"
      "ds_write_addtid_b32 v24 offset:36864\n"
      "v_mfma_f32_16x16x32_bf16 v[28:31], a[0:3], a[88:91], v[28:31]\n"
      "s_waitcnt lgkmcnt(7)\n"
      "ds_write_addtid_b32 v25 offset:37120\n"
      "v_mfma_f32_16x16x32_bf16 v[28:31], a[4:7], a[100:103], v[28:31]\n"
      "s_waitcnt lgkmcnt(7)\n"
      "v_mfma_f32_16x16x32_bf16 v[28:31], a[8:11], a[112:115], v[28:31]\n"
      "s_waitcnt lgkmcnt(6)\n"
      "v_mfma_f32_16x16x32_bf16 v[28:31], a[12:15], a[124:127], v[28:31]\n"
      "s_waitcnt lgkmcnt(5)\n"
      "v_mfma_f32_16x16x32_bf16 v[28:31], a[16:19], a[136:139], v[28:31]\n"
      "s_waitcnt lgkmcnt(4)\n"
      "v_mfma_f32_16x16x32_bf16 v[28:31], a[20:23], a[148:151], v[28:31]\n"
      "s_nop 1\n"
      "v_mfma_f32_16x16x32_bf16 v[20:23], a[24:27], a[164:167], 0\n"
      "s_nop 1\n"
      "v_mfma_f32_16x16x32_bf16 v[20:23], a[28:31], a[176:179], v[20:23]\n"
      "s_nop 0\n"
      "v_mfma_f32_16x16x32_bf16 v[20:23], a[32:35], a[188:191], v[20:23]\n"
      "v_cvt_pk_bf16_f32 v28, v28, v29\n"
      "v_cvt_pk_bf16_f32 v29, v30, v31\n"
      "v_mfma_f32_16x16x32_bf16 v[20:23], a[0:3], a[92:95], v[20:23]\n"
      "ds_write_addtid_b32 v28 offset:60432\n"
      "v_mfma_f32_16x16x32_bf16 v[20:23], a[4:7], a[104:107], v[20:23]\n"
      "ds_write_addtid_b32 v29 offset:60688\n"
      "v_mfma_f32_16x16x32_bf16 v[20:23], a[8:11], a[116:119], v[20:23]\n"
      "v_mfma_f32_16x16x32_bf16 v[20:23], a[12:15], a[128:131], v[20:23]\n"
      "v_mfma_f32_16x16x32_bf16 v[20:23], a[16:19], a[140:143], v[20:23]\n"
      "v_mfma_f32_16x16x32_bf16 v[20:23], a[20:23], a[152:155], v[20:23]\n"
      "s_nop 1\n"
      "v_mfma_f32_16x16x32_bf16 v[32:35], a[24:27], a[168:171], 0\n"
      "s_nop 1\n"
      "v_mfma_f32_16x16x32_bf16 v[32:35], a[28:31], a[180:183], v[32:35]\n"
      "s_nop 0\n"
      "v_mfma_f32_16x16x32_bf16 v[32:35], a[32:35], a[192:195], v[32:35]\n"
      "v_cvt_pk_bf16_f32 v20, v20, v21\n"
      "v_cvt_pk_bf16_f32 v21, v22, v23\n"
      "v_mfma_f32_16x16x32_bf16 v[32:35], a[0:3], a[96:99], v[32:35]\n"
      "ds_write_addtid_b32 v20 offset:48136\n"
      "v_mfma_f32_16x16x32_bf16 v[32:35], a[4:7], a[108:111], v[32:35]\n"
      "ds_write_addtid_b32 v21 offset:48392\n"
      "v_mfma_f32_16x16x32_bf16 v[32:35], a[8:11], a[120:123], v[32:35]\n"
      "v_mfma_f32_16x16x32_bf16 v[32:35], a[12:15], a[132:135], v[32:35]\n"
      "v_mfma_f32_16x16x32_bf16 v[32:35], a[16:19], a[144:147], v[32:35]\n"
      "v_mfma_f32_16x16x32_bf16 v[32:35], a[20:23], a[156:159], v[32:35]\n"
      "s_nop 1\n"
      "s_nop 5\n"
      "v_cvt_pk_bf16_f32 v32, v32, v33\n"
      "v_cvt_pk_bf16_f32 v33, v34, v35\n"
      "ds_write_addtid_b32 v32 offset:35840\n"
      "ds_write_addtid_b32 v33 offset:36096\n"
      "s_waitcnt lgkmcnt(0)\n"
      "s_barrier\n"
      "ds_read2_b64 a[0:3], v11 offset0:0 offset1:64\n"
      "ds_read2_b64 a[4:7], v11 offset0:1 offset1:65\n"
      "ds_read2_b64 a[8:11], v11 offset0:2 offset1:66\n"
      "ds_read2_b64 a[12:15], v11 offset0:3 offset1:67\n"
      "ds_read2_b64 a[16:19], v13 offset0:0 offset1:64\n"
      "ds_read2_b64 a[20:23], v13 offset0:1 offset1:65\n"
      "s_waitcnt lgkmcnt(5)\n"
      "v_mfma_f32_16x16x32_bf16 v[72:75], a[84:87], a[0:3], 0\n"
      "s_waitcnt lgkmcnt(4)\n"
      "v_mfma_f32_16x16x32_bf16 v[76:79], a[84:87], a[4:7], 0\n"
      "s_waitcnt lgkmcnt(3)\n"
      "v_mfma_f32_16x16x32_bf16 v[68:71], a[84:87], a[8:11], 0\n"
      "s_waitcnt lgkmcnt(2)\n"
      "s_nop 2\n"
      "v_cvt_pk_bf16_f32 v80, v72, v73\n"
      "v_cvt_pk_bf16_f32 v86, v74, v75\n"
      "v_mfma_f32_16x16x32_bf16 v[72:75], a[84:87], a[12:15], 0\n"
      "s_waitcnt lgkmcnt(1)\n"
      "v_cvt_pk_bf16_f32 v81, v76, v77\n"
      "v_cvt_pk_bf16_f32 v87, v78, v79\n"
      "v_cvt_pk_bf16_f32 v82, v68, v69\n"
      "v_cvt_pk_bf16_f32 v88, v70, v71\n"
      "v_mfma_f32_16x16x32_bf16 v[76:79], a[84:87], a[16:19], 0\n"
      "s_waitcnt lgkmcnt(0)\n"
      "v_mfma_f32_16x16x32_bf16 v[68:71], a[84:87], a[20:23], 0\n"
      "s_mov_b32 s52, s13\n"
      "v_cvt_pk_bf16_f32 v83, v72, v73\n"
      "v_cvt_pk_bf16_f32 v89, v74, v75\n"
      "s_add_u32 s13, s13, s12\n"
      "buffer_store_dwordx4 v[80:83], v14, s[36:39], s52 offen sc1\n"
      "buffer_store_dwordx4 v[86:89], v14, s[36:39], s52 offen offset:1536 sc1\n"
      "v_cvt_pk_bf16_f32 v84, v76, v77\n"
      "v_cvt_pk_bf16_f32 v90, v78, v79\n"
      "v_cvt_pk_bf16_f32 v85, v68, v69\n"
      "v_cvt_pk_bf16_f32 v91, v70, v71\n"
      "buffer_store_dwordx2 v[84:85], v15, s[36:39], s52 offen sc1\n"
      "buffer_store_dwordx2 v[90:91], v15, s[36:39], s52 offen offset:1536 sc1\n"
      "Lconv_u0:\n"
      "s_waitcnt vmcnt(0)\n"
      "s_endpgm\n"
      ".p2align 12\n"
      "Lconv_pa1:\n"
      "s_cbranch_execz Lconv_r0\n"
      "s_nop 0\n"
      "Lconv_loop1:\n"
      "v_mfma_f32_16x16x32_bf16 v[20:23], a[24:27], v[96:99], 0\n"
      "s_add_u32 s51, s51, s12\n"
      "ds_write_addtid_b32 v32 offset:34816\n"
      "v_cvt_pk_bf16_f32 v33, v34, v35\n"
      "v_mfma_f32_16x16x32_bf16 v[20:23], a[28:31], v[152:155], v[20:23]\n"
      "s_sub_u32 s58, s93, s58\n"
      "ds_write_addtid_b32 v33 offset:35072\n"
      "v_cvt_pk_bf16_f32 v84, v72, v73\n"
      "v_mfma_f32_16x16x32_bf16 v[20:23], a[32:35], a[220:223], v[20:23]\n"
      "s_sub_u32 s59, s94, s59\n"
      "v_cvt_pk_bf16_f32 v85, v76, v77\n"
      "v_cvt_pk_bf16_f32 v90, v74, v75\n"
      "v_mfma_f32_16x16x32_bf16 v[20:23], a[0:3], v[116:119], v[20:23]\n"
      "buffer_store_dwordx2 v[84:85], v15, s[36:39], s52 offen sc1\n"
      "v_cvt_pk_bf16_f32 v91, v78, v79\n"
      "v_mfma_f32_16x16x32_bf16 v[20:23], a[4:7], v[168:171], v[20:23]\n"
      "s_sub_u32 s47, s51, s48\n"
      "v_mfma_f32_16x16x32_bf16 v[20:23], a[8:11], v[224:227], v[20:23]\n"
      "s_cmp_ge_u32 vcc_lo, 0x80000000\n"
      "v_mfma_f32_16x16x32_bf16 v[24:27], a[24:27], v[104:107], 0\n"
      "buffer_store_dwordx2 v[90:91], v15, s[36:39], s52 offen offset:1536 sc1\n"
      "v_mfma_f32_16x16x32_bf16 v[24:27], a[28:31], v[156:159], v[24:27]\n"
      "s_cselect_b32 s53, s47, s51\n"
      "v_mfma_f32_16x16x32_bf16 v[24:27], a[32:35], v[212:215], v[24:27]\n"
      "s_mov_b32 s52, s13\n"
      "v_mfma_f32_16x16x32_bf16 v[24:27], a[0:3], v[120:123], v[24:27]\n"
      "s_add_u32 s13, s13, s12\n"
      "v_mfma_f32_16x16x32_bf16 v[24:27], a[4:7], v[176:179], v[24:27]\n"
      "s_waitcnt vmcnt(17) lgkmcnt(0)\n"
      "s_barrier\n"
      "v_mfma_f32_16x16x32_bf16 v[24:27], a[8:11], a[232:235], v[24:27]\n"
      "s_mov_b32 m0, s58\n"
      "ds_read2_b64 a[60:63], v10 offset0:0 offset1:64\n"
      "v_mfma_f32_16x16x32_bf16 v[28:31], a[24:27], v[108:111], 0\n"
      "ds_read_b128 a[12:15], v7 offset:6144\n"
      "v_mfma_f32_16x16x32_bf16 v[28:31], a[28:31], v[164:167], v[28:31]\n"
      "ds_read_b128 a[16:19], v7 offset:6400\n"
      "v_mfma_f32_16x16x32_bf16 v[28:31], a[32:35], a[224:227], v[28:31]\n"
      "ds_read_b128 a[20:23], v7 offset:6656\n"
      "v_mfma_f32_16x16x32_bf16 v[28:31], a[0:3], v[128:131], v[28:31]\n"
      "ds_read_b128 a[48:51], v2 offset:13280\n"
      "v_mfma_f32_16x16x32_bf16 v[28:31], a[4:7], a[228:231], v[28:31]\n"
      "ds_read_b128 a[56:59], v4 offset:13280\n"
      "v_mfma_f32_16x16x32_bf16 v[28:31], a[8:11], v[236:239], v[28:31]\n"
      "s_waitcnt lgkmcnt(5)\n"
      "ds_read_b128 a[52:55], v3 offset:13280\n"
      "v_mfma_f32_16x16x32_bf16 v[68:71], a[84:87], a[60:63], 0\n"
      "s_waitcnt lgkmcnt(5)\n"
      "ds_read_b128 a[24:27], v6 offset:5376\n"
      "v_mfma_f32_16x16x32_bf16 v[20:23], a[12:15], v[132:135], v[20:23]\n"
      "s_waitcnt lgkmcnt(5)\n"
      "ds_read_b128 a[28:31], v6 offset:5632\n"
      "v_mfma_f32_16x16x32_bf16 v[20:23], a[16:19], v[188:191], v[20:23]\n"
      "s_waitcnt lgkmcnt(5)\n"
      "ds_read_b128 a[32:35], v6 offset:5888\n"
      "v_cvt_pk_bf16_f32 v80, v68, v69\n"
      "v_mfma_f32_16x16x32_bf16 v[20:23], a[20:23], a[240:243], v[20:23]\n"
      "ds_read_b128 a[0:3], v7 offset:768\n"
      "v_cvt_pk_bf16_f32 v86, v70, v71\n"
      "v_mfma_f32_16x16x32_bf16 v[24:27], a[12:15], v[140:143], v[24:27]\n"
      "buffer_load_dwordx4 v16, s[32:35], s53 offen lds\n"
      "s_nop 0\n"
      "v_mfma_f32_16x16x32_bf16 v[24:27], a[16:19], a[236:239], v[24:27]\n"
      "s_mov_b32 m0, s45\n"
      "ds_read_b128 a[4:7], v7 offset:1024\n"
      "v_cvt_pk_bf16_f32 v20, v20, v21\n"
      "v_mfma_f32_16x16x32_bf16 v[24:27], a[20:23], v[248:251], v[24:27]\n"
      "s_waitcnt lgkmcnt(6)\n"
      "ds_read_b128 a[8:11], v7 offset:1280\n"
      "v_cvt_pk_bf16_f32 v21, v22, v23\n"
      "v_mfma_f32_16x16x32_bf16 v[52:55], a[48:51], a[72:75], 0\n"
      "ds_read2_b64 a[64:67], v10 offset0:1 offset1:65\n"
      "s_nop 0\n"
      "v_mfma_f32_16x16x32_bf16 v[60:63], a[56:59], a[80:83], v[52:55]\n"
      "ds_write_addtid_b32 v20 offset:62480\n"
      "v_cvt_pk_bf16_f32 v24, v24, v25\n"
      "v_mfma_f32_16x16x32_bf16 v[28:31], a[12:15], v[144:147], v[28:31]\n"
      "ds_read_b128 a[12:15], v7 offset:5376\n"
      "v_cvt_pk_bf16_f32 v25, v26, v27\n"
      "v_mfma_f32_16x16x32_bf16 v[28:31], a[16:19], v[200:203], v[28:31]\n"
      "ds_read_b128 a[16:19], v7 offset:5632\n"
      "v_mfma_f32_16x16x32_bf16 v[28:31], a[20:23], a[244:247], v[28:31]\n"
      "s_waitcnt lgkmcnt(6)\n"
      "ds_read_b128 a[20:23], v7 offset:5888\n"
      "v_cvt_pk_bf16_f32 v60, v60, v61\n"
      "v_mfma_f32_16x16x32_bf16 v[56:59], a[52:55], a[76:79], 0\n"
      "ds_write_addtid_b32 v21 offset:62736\n"
      "v_cvt_pk_bf16_f32 v61, v62, v63\n"
      "v_mfma_f32_16x16x32_bf16 v[32:35], a[24:27], v[92:95], 0\n"
      "ds_read_b128 a[36:39], v2 offset:13344\n"
      "v_cvt_pk_bf16_f32 v28, v28, v29\n"
      "v_mfma_f32_16x16x32_bf16 v[32:35], a[28:31], v[148:151], v[32:35]\n"
      "ds_write_addtid_b32 v24 offset:50184\n"
      "v_cvt_pk_bf16_f32 v29, v30, v31\n"
      "v_mfma_f32_16x16x32_bf16 v[32:35], a[32:35], v[204:207], v[32:35]\n"
      "ds_read_b128 a[40:43], v3 offset:13344\n"
      "v_sub_f32_e32 v64, v52, v56\n"
      "v_mfma_f32_16x16x32_bf16 v[32:35], a[0:3], v[112:115], v[32:35]\n"
      "s_waitcnt lgkmcnt(8)\n"
      "ds_write_addtid_b32 v25 offset:50440\n"
      "v_sub_f32_e32 v65, v53, v57\n"
      "v_mfma_f32_16x16x32_bf16 v[32:35], a[4:7], a[208:211], v[32:35]\n"
      "ds_read_b128 a[44:47], v4 offset:13344\n"
      "v_sub_f32_e32 v66, v54, v58\n"
      "v_mfma_f32_16x16x32_bf16 v[32:35], a[8:11], v[220:223], v[32:35]\n"
      "s_waitcnt lgkmcnt(7)\n"
      "ds_read_b128 a[48:51], v2 offset:13408\n"
      "v_sub_f32_e32 v67, v55, v59\n"
      "v_mfma_f32_16x16x32_bf16 v[32:35], a[12:15], a[212:215], v[32:35]\n"
      "ds_write_addtid_b32 v28 offset:37888\n"
      "v_add_f32_e32 v56, v52, v56\n"
      "v_mfma_f32_16x16x32_bf16 v[32:35], a[16:19], v[184:187], v[32:35]\n"
      "s_waitcnt lgkmcnt(8)\n"
      "ds_write_addtid_b32 v29 offset:38144\n"
      "v_add_f32_e32 v57, v53, v57\n"
      "v_mfma_f32_16x16x32_bf16 v[32:35], a[20:23], v[240:243], v[32:35]\n"
      "s_mov_b32 m0, s18\n"
      "ds_read_b128 a[56:59], v4 offset:13408\n"
      "v_add_f32_e32 v58, v54, v58\n"
      "v_mfma_f32_16x16x32_bf16 v[72:75], a[84:87], a[64:67], 0\n"
      "ds_read2_b64 a[68:71], v10 offset0:2 offset1:66\n"
      "v_add_f32_e32 v59, v55, v59\n"
      "v_mfma_f32_16x16x32_bf16 v[20:23], a[24:27], v[100:103], 0\n"
      "v_cvt_pk_bf16_f32 v64, v64, v65\n"
      "v_cvt_pk_bf16_f32 v65, v66, v67\n"
      "v_mfma_f32_16x16x32_bf16 v[20:23], a[28:31], a[200:203], v[20:23]\n"
      "ds_write_addtid_b32 v64 offset:1536\n"
      "v_cvt_pk_bf16_f32 v56, v56, v57\n"
      "v_mfma_f32_16x16x32_bf16 v[20:23], a[32:35], v[208:211], v[20:23]\n"
      "ds_write_addtid_b32 v65 offset:38464\n"
      "v_cvt_pk_bf16_f32 v57, v58, v59\n"
      "v_mfma_f32_16x16x32_bf16 v[20:23], a[0:3], a[204:207], v[20:23]\n"
      "ds_write_addtid_b32 v56 offset:768\n"
      "v_cvt_pk_bf16_f32 v32, v32, v33\n"
      "v_mfma_f32_16x16x32_bf16 v[20:23], a[4:7], v[172:175], v[20:23]\n"
      "ds_write_addtid_b32 v57 offset:37696\n"
      "v_cvt_pk_bf16_f32 v33, v34, v35\n"
      "v_mfma_f32_16x16x32_bf16 v[20:23], a[8:11], v[228:231], v[20:23]\n"
      "ds_write_addtid_b32 v60 offset:0\n"
      "v_cvt_pk_bf16_f32 v81, v72, v73\n"
      "v_mfma_f32_16x16x32_bf16 v[20:23], a[12:15], v[136:139], v[20:23]\n"
      "ds_write_addtid_b32 v61 offset:36928\n"
      "v_cvt_pk_bf16_f32 v87, v74, v75\n"
      "v_mfma_f32_16x16x32_bf16 v[20:23], a[16:19], v[192:195], v[20:23]\n"
      "s_mov_b32 m0, s45\n"
      "ds_read_b128 a[52:55], v3 offset:13408\n"
      "v_add_u32_e32 v2, s91, v2\n"
      "v_mfma_f32_16x16x32_bf16 v[20:23], a[20:23], v[244:247], v[20:23]\n"
      "s_waitcnt lgkmcnt(7)\n"
      "ds_read2_b64 a[60:63], v10 offset0:3 offset1:67\n"
      "v_add_u32_e32 v4, s91, v4\n"
      "v_mfma_f32_16x16x32_bf16 v[36:39], a[36:39], a[72:75], 0\n"
      "s_mov_b32 m0, s58\n"
      "ds_read2_b64 a[64:67], v12 offset0:0 offset1:64\n"
      "v_add_u32_e32 v3, s91, v3\n"
      "v_mfma_f32_16x16x32_bf16 v[40:43], a[40:43], a[76:79], 0\n"
      "buffer_load_dwordx4 v17, s[32:35], s53 offen offset:1024 lds\n"
      "v_cvt_pk_bf16_f32 v20, v20, v21\n"
      "v_mfma_f32_16x16x32_bf16 v[44:47], a[44:47], a[80:83], v[36:39]\n"
      "s_mov_b32 m0, s45\n"
      "v_cvt_pk_bf16_f32 v21, v22, v23\n"
      "s_nop 0\n"
      "v_mfma_f32_16x16x32_bf16 v[52:55], a[48:51], a[72:75], 0\n"
      "s_sub_u32 s91, 0, s91\n"
      "ds_write_addtid_b32 v32 offset:61456\n"
      "v_sub_f32_e32 v48, v36, v40\n"
      "v_mfma_f32_16x16x32_bf16 v[60:63], a[56:59], a[80:83], v[52:55]\n"
      "ds_write_addtid_b32 v33 offset:61712\n"
      "v_sub_f32_e32 v49, v37, v41\n"
      "v_mfma_f32_16x16x32_bf16 v[76:79], a[84:87], a[68:71], 0\n"
      "ds_write_addtid_b32 v20 offset:49160\n"
      "v_sub_f32_e32 v50, v38, v42\n"
      "v_mfma_f32_16x16x32_bf16 v[24:27], a[24:27], a[196:199], 0\n"
      "ds_read_b128 a[24:27], v7 offset:4608\n"
      "v_sub_f32_e32 v51, v39, v43\n"
      "v_mfma_f32_16x16x32_bf16 v[24:27], a[28:31], v[160:163], v[24:27]\n"
      "ds_read_b128 a[28:31], v7 offset:4864\n"
      "v_add_f32_e32 v40, v36, v40\n"
      "v_mfma_f32_16x16x32_bf16 v[24:27], a[32:35], v[216:219], v[24:27]\n"
      "ds_read_b128 a[32:35], v7 offset:5120\n"
      "v_add_f32_e32 v41, v37, v41\n"
      "v_mfma_f32_16x16x32_bf16 v[24:27], a[0:3], v[124:127], v[24:27]\n"
      "ds_read_b128 a[0:3], v6 offset:4608\n"
      "v_add_f32_e32 v42, v38, v42\n"
      "v_mfma_f32_16x16x32_bf16 v[24:27], a[4:7], v[180:183], v[24:27]\n"
      "ds_read_b128 a[4:7], v6 offset:4864\n"
      "v_add_f32_e32 v43, v39, v43\n"
      "v_mfma_f32_16x16x32_bf16 v[24:27], a[8:11], v[232:235], v[24:27]\n"
      "ds_read_b128 a[8:11], v6 offset:5120\n"
      "v_cvt_pk_bf16_f32 v48, v48, v49\n"
      "v_mfma_f32_16x16x32_bf16 v[24:27], a[12:15], a[216:219], v[24:27]\n"
      "ds_read_b128 a[12:15], v7 offset:0\n"
      "v_cvt_pk_bf16_f32 v49, v50, v51\n"
      "v_mfma_f32_16x16x32_bf16 v[24:27], a[16:19], v[196:199], v[24:27]\n"
      "ds_read_b128 a[16:19], v7 offset:256\n"
      "v_cvt_pk_bf16_f32 v40, v40, v41\n"
      "v_mfma_f32_16x16x32_bf16 v[24:27], a[20:23], v[252:255], v[24:27]\n"
      "s_waitcnt lgkmcnt(7)\n"
      "ds_read_b128 a[20:23], v7 offset:512\n"
      "v_cvt_pk_bf16_f32 v41, v42, v43\n"
      "v_mfma_f32_16x16x32_bf16 v[56:59], a[52:55], a[76:79], 0\n"
      "ds_write_addtid_b32 v21 offset:49416\n"
      "v_cvt_pk_bf16_f32 v44, v44, v45\n"
      "v_mfma_f32_16x16x32_bf16 v[28:31], a[24:27], a[160:163], 0\n"
      "s_waitcnt lgkmcnt(7)\n"
      "v_cvt_pk_bf16_f32 v45, v46, v47\n"
      "v_cvt_pk_bf16_f32 v24, v24, v25\n"
      "v_mfma_f32_16x16x32_bf16 v[28:31], a[28:31], a[172:175], v[28:31]\n"
      "s_mov_b32 m0, s18\n"
      "v_cvt_pk_bf16_f32 v25, v26, v27\n"
      "v_sub_f32_e32 v64, v52, v56\n"
      "v_mfma_f32_16x16x32_bf16 v[28:31], a[32:35], a[184:187], v[28:31]\n"
      "s_waitcnt lgkmcnt(5)\n"
      "ds_write_addtid_b32 v48 offset:1792\n"
      "v_sub_f32_e32 v65, v53, v57\n"
      "v_mfma_f32_16x16x32_bf16 v[28:31], a[0:3], a[88:91], v[28:31]\n"
      "ds_write_addtid_b32 v49 offset:38720\n"
      "v_cvt_pk_bf16_f32 v64, v64, v65\n"
      "v_mfma_f32_16x16x32_bf16 v[28:31], a[4:7], a[100:103], v[28:31]\n"
      "s_waitcnt lgkmcnt(5)\n"
      "ds_write_addtid_b32 v40 offset:1024\n"
      "v_sub_f32_e32 v66, v54, v58\n"
      "v_mfma_f32_16x16x32_bf16 v[28:31], a[8:11], a[112:115], v[28:31]\n"
      "ds_write_addtid_b32 v41 offset:37952\n"
      "v_sub_f32_e32 v67, v55, v59\n"
      "v_mfma_f32_16x16x32_bf16 v[28:31], a[12:15], a[124:127], v[28:31]\n"
      "s_waitcnt lgkmcnt(5)\n"
      "ds_write_addtid_b32 v44 offset:256\n"
      "v_cvt_pk_bf16_f32 v65, v66, v67\n"
      "v_mfma_f32_16x16x32_bf16 v[28:31], a[16:19], a[136:139], v[28:31]\n"
      "ds_write_addtid_b32 v45 offset:37184\n"
      "v_add_f32_e32 v56, v52, v56\n"
      "v_mfma_f32_16x16x32_bf16 v[28:31], a[20:23], a[148:151], v[28:31]\n"
      "s_mov_b32 m0, s45\n"
      "v_add_f32_e32 v57, v53, v57\n"
      "v_cvt_pk_bf16_f32 v56, v56, v57\n"
      "v_mfma_f32_16x16x32_bf16 v[20:23], a[24:27], a[164:167], 0\n"
      "s_mov_b32 m0, s58\n"
      "v_add_f32_e32 v58, v54, v58\n"
      "v_add_f32_e32 v59, v55, v59\n"
      "v_mfma_f32_16x16x32_bf16 v[20:23], a[28:31], a[176:179], v[20:23]\n"
      "buffer_load_dwordx4 v18, s[32:35], s53 offen offset:2048 lds\n"
      "v_cvt_pk_bf16_f32 v57, v58, v59\n"
      "v_mfma_f32_16x16x32_bf16 v[20:23], a[32:35], a[188:191], v[20:23]\n"
      "s_mov_b32 m0, s45\n"
      "ds_read2_b64 a[68:71], v12 offset0:1 offset1:65\n"
      "v_cvt_pk_bf16_f32 v60, v60, v61\n"
      "v_mfma_f32_16x16x32_bf16 v[20:23], a[0:3], a[92:95], v[20:23]\n"
      "ds_write_addtid_b32 v24 offset:36864\n"
      "v_cvt_pk_bf16_f32 v61, v62, v63\n"
      "v_mfma_f32_16x16x32_bf16 v[20:23], a[4:7], a[104:107], v[20:23]\n"
      "ds_write_addtid_b32 v25 offset:37120\n"
      "v_cvt_pk_bf16_f32 v28, v28, v29\n"
      "v_mfma_f32_16x16x32_bf16 v[20:23], a[8:11], a[116:119], v[20:23]\n"
      "s_mov_b32 m0, s18\n"
      "v_cvt_pk_bf16_f32 v29, v30, v31\n"
      "v_cvt_pk_bf16_f32 v82, v76, v77\n"
      "v_mfma_f32_16x16x32_bf16 v[20:23], a[12:15], a[128:131], v[20:23]\n"
      "ds_write_addtid_b32 v64 offset:2048\n"
      "v_cvt_pk_bf16_f32 v88, v78, v79\n"
      "v_mfma_f32_16x16x32_bf16 v[20:23], a[16:19], a[140:143], v[20:23]\n"
      "ds_write_addtid_b32 v65 offset:38976\n"
      "v_swap_b32 v6, v7\n"
      "v_mfma_f32_16x16x32_bf16 v[20:23], a[20:23], a[152:155], v[20:23]\n"
      "ds_write_addtid_b32 v56 offset:1280\n"
      "s_nop 0\n"
      "v_mfma_f32_16x16x32_bf16 v[68:71], a[84:87], a[60:63], 0\n"
      "ds_write_addtid_b32 v57 offset:38208\n"
      "s_nop 0\n"
      "v_mfma_f32_16x16x32_bf16 v[32:35], a[24:27], a[168:171], 0\n"
      "ds_write_addtid_b32 v60 offset:512\n"
      "v_mfma_f32_16x16x32_bf16 v[32:35], a[28:31], a[180:183], v[32:35]\n"
      "ds_write_addtid_b32 v61 offset:37440\n"
      "v_cvt_pk_bf16_f32 v20, v20, v21\n"
      "v_mfma_f32_16x16x32_bf16 v[32:35], a[32:35], a[192:195], v[32:35]\n"
      "s_mov_b32 m0, s45\n"
      "v_cvt_pk_bf16_f32 v83, v68, v69\n"
      "v_cvt_pk_bf16_f32 v21, v22, v23\n"
      "v_mfma_f32_16x16x32_bf16 v[32:35], a[0:3], a[96:99], v[32:35]\n"
      "ds_write_addtid_b32 v28 offset:60432\n"
      "v_cvt_pk_bf16_f32 v89, v70, v71\n"
      "v_mfma_f32_16x16x32_bf16 v[32:35], a[4:7], a[108:111], v[32:35]\n"
      "ds_write_addtid_b32 v29 offset:60688\n"
      "v_mfma_f32_16x16x32_bf16 v[32:35], a[8:11], a[120:123], v[32:35]\n"
      "ds_write_addtid_b32 v20 offset:48136\n"
      "v_mfma_f32_16x16x32_bf16 v[32:35], a[12:15], a[132:135], v[32:35]\n"
      "buffer_store_dwordx4 v[80:83], v14, s[36:39], s52 offen sc1\n"
      "v_mfma_f32_16x16x32_bf16 v[32:35], a[16:19], a[144:147], v[32:35]\n"
      "ds_write_addtid_b32 v21 offset:48392\n"
      "v_mfma_f32_16x16x32_bf16 v[32:35], a[20:23], a[156:159], v[32:35]\n"
      "s_nop 1\n"
      "v_mfma_f32_16x16x32_bf16 v[72:75], a[84:87], a[64:67], 0\n"
      "s_waitcnt lgkmcnt(12)\n"
      "s_nop 1\n"
      "v_mfma_f32_16x16x32_bf16 v[76:79], a[84:87], a[68:71], 0\n"
      "buffer_store_dwordx4 v[86:89], v14, s[36:39], s52 offen offset:1536 sc1\n"
      "v_cvt_pk_bf16_f32 v32, v32, v33\n"
      "v_mfma_f32_16x16x32_bf16 v[20:23], a[12:15], a[88:91], 0\n"
      "s_add_u32 s51, s51, s12\n"
      "ds_write_addtid_b32 v32 offset:35840\n"
      "v_cvt_pk_bf16_f32 v33, v34, v35\n"
      "v_mfma_f32_16x16x32_bf16 v[20:23], a[16:19], a[100:103], v[20:23]\n"
      "s_sub_u32 s47, s51, s48\n"
      "ds_write_addtid_b32 v33 offset:36096\n"
      "v_cvt_pk_bf16_f32 v84, v72, v73\n"
      "v_mfma_f32_16x16x32_bf16 v[20:23], a[20:23], a[112:115], v[20:23]\n"
      "s_cmp_ge_u32 vcc_lo, 0x40000000\n"
      "v_cvt_pk_bf16_f32 v85, v76, v77\n"
      "v_cvt_pk_bf16_f32 v90, v74, v75\n"
      "v_mfma_f32_16x16x32_bf16 v[20:23], a[24:27], a[124:127], v[20:23]\n"
      "buffer_store_dwordx2 v[84:85], v15, s[36:39], s52 offen sc1\n"
      "v_cvt_pk_bf16_f32 v91, v78, v79\n"
      "v_mfma_f32_16x16x32_bf16 v[20:23], a[28:31], a[136:139], v[20:23]\n"
      "s_cselect_b32 s53, s47, s51\n"
      "v_mfma_f32_16x16x32_bf16 v[20:23], a[32:35], a[148:151], v[20:23]\n"
      "s_mul_i32 vcc_lo, vcc_lo, 2\n"
      "v_mfma_f32_16x16x32_bf16 v[24:27], a[12:15], a[92:95], 0\n"
      "buffer_store_dwordx2 v[90:91], v15, s[36:39], s52 offen offset:1536 sc1\n"
      "v_mfma_f32_16x16x32_bf16 v[24:27], a[16:19], a[104:107], v[24:27]\n"
      "s_mov_b32 s52, s13\n"
      "v_mfma_f32_16x16x32_bf16 v[24:27], a[20:23], a[116:119], v[24:27]\n"
      "v_mfma_f32_16x16x32_bf16 v[24:27], a[24:27], a[128:131], v[24:27]\n"
      "v_mfma_f32_16x16x32_bf16 v[24:27], a[28:31], a[140:143], v[24:27]\n"
      "s_waitcnt vmcnt(19) lgkmcnt(0)\n"
      "s_barrier\n"
      "v_mfma_f32_16x16x32_bf16 v[24:27], a[32:35], a[152:155], v[24:27]\n"
      "s_add_u32 m0, s58, 13280\n"
      "ds_read2_b64 a[60:63], v11 offset0:0 offset1:64\n"
      "v_mfma_f32_16x16x32_bf16 v[28:31], a[12:15], a[96:99], 0\n"
      "s_add_u32 s13, s13, s12\n"
      "ds_read_b128 a[0:3], v7 offset:0\n"
      "v_mfma_f32_16x16x32_bf16 v[28:31], a[16:19], a[108:111], v[28:31]\n"
      "ds_read_b128 a[4:7], v7 offset:256\n"
      "v_mfma_f32_16x16x32_bf16 v[28:31], a[20:23], a[120:123], v[28:31]\n"
      "ds_read_b128 a[8:11], v7 offset:512\n"
      "v_mfma_f32_16x16x32_bf16 v[28:31], a[24:27], a[132:135], v[28:31]\n"
      "ds_read_b128 a[36:39], v2 offset:0\n"
      "v_mfma_f32_16x16x32_bf16 v[28:31], a[28:31], a[144:147], v[28:31]\n"
      "ds_read_b128 a[44:47], v4 offset:0\n"
      "v_mfma_f32_16x16x32_bf16 v[28:31], a[32:35], a[156:159], v[28:31]\n"
      "s_waitcnt lgkmcnt(5)\n"
      "ds_read_b128 a[40:43], v3 offset:0\n"
      "v_mfma_f32_16x16x32_bf16 v[68:71], a[84:87], a[60:63], 0\n"
      "s_waitcnt lgkmcnt(5)\n"
      "ds_read_b128 a[12:15], v6 offset:768\n"
      "v_mfma_f32_16x16x32_bf16 v[20:23], a[0:3], a[160:163], v[20:23]\n"
      "s_waitcnt lgkmcnt(5)\n"
      "ds_read_b128 a[16:19], v6 offset:1024\n"
      "v_mfma_f32_16x16x32_bf16 v[20:23], a[4:7], a[172:175], v[20:23]\n"
      "s_waitcnt lgkmcnt(5)\n"
      "ds_read_b128 a[20:23], v6 offset:1280\n"
      "v_cvt_pk_bf16_f32 v80, v68, v69\n"
      "v_mfma_f32_16x16x32_bf16 v[20:23], a[8:11], a[184:187], v[20:23]\n"
      "ds_read_b128 a[24:27], v6 offset:5376\n"
      "v_cvt_pk_bf16_f32 v86, v70, v71\n"
      "v_mfma_f32_16x16x32_bf16 v[24:27], a[0:3], a[164:167], v[24:27]\n"
      "buffer_load_dwordx4 v16, s[32:35], s53 offen lds\n"
      "s_nop 0\n"
      "v_mfma_f32_16x16x32_bf16 v[24:27], a[4:7], a[176:179], v[24:27]\n"
      "s_mov_b32 m0, s45\n"
      "ds_read_b128 a[28:31], v6 offset:5632\n"
      "v_cvt_pk_bf16_f32 v20, v20, v21\n"
      "v_mfma_f32_16x16x32_bf16 v[24:27], a[8:11], a[188:191], v[24:27]\n"
      "s_waitcnt lgkmcnt(6)\n"
      "ds_read_b128 a[32:35], v6 offset:5888\n"
      "v_cvt_pk_bf16_f32 v21, v22, v23\n"
      "v_mfma_f32_16x16x32_bf16 v[36:39], a[36:39], a[72:75], 0\n"
      "ds_read2_b64 a[64:67], v11 offset0:1 offset1:65\n"
      "s_nop 0\n"
      "v_mfma_f32_16x16x32_bf16 v[44:47], a[44:47], a[80:83], v[36:39]\n"
      "ds_write_addtid_b32 v20 offset:57360\n"
      "v_cvt_pk_bf16_f32 v24, v24, v25\n"
      "v_mfma_f32_16x16x32_bf16 v[28:31], a[0:3], a[168:171], v[28:31]\n"
      "ds_read_b128 a[0:3], v7 offset:768\n"
      "v_cvt_pk_bf16_f32 v25, v26, v27\n"
      "v_mfma_f32_16x16x32_bf16 v[28:31], a[4:7], a[180:183], v[28:31]\n"
      "ds_read_b128 a[4:7], v7 offset:1024\n"
      "v_mfma_f32_16x16x32_bf16 v[28:31], a[8:11], a[192:195], v[28:31]\n"
      "s_waitcnt lgkmcnt(6)\n"
      "ds_read_b128 a[8:11], v7 offset:1280\n"
      "v_cvt_pk_bf16_f32 v44, v44, v45\n"
      "v_mfma_f32_16x16x32_bf16 v[40:43], a[40:43], a[76:79], 0\n"
      "ds_write_addtid_b32 v21 offset:57616\n"
      "v_cvt_pk_bf16_f32 v45, v46, v47\n"
      "v_mfma_f32_16x16x32_bf16 v[32:35], a[12:15], v[92:95], 0\n"
      "ds_read_b128 a[48:51], v2 offset:64\n"
      "v_cvt_pk_bf16_f32 v28, v28, v29\n"
      "v_mfma_f32_16x16x32_bf16 v[32:35], a[16:19], v[148:151], v[32:35]\n"
      "ds_write_addtid_b32 v24 offset:45064\n"
      "v_cvt_pk_bf16_f32 v29, v30, v31\n"
      "v_mfma_f32_16x16x32_bf16 v[32:35], a[20:23], v[204:207], v[32:35]\n"
      "ds_read_b128 a[52:55], v3 offset:64\n"
      "v_sub_f32_e32 v48, v36, v40\n"
      "v_mfma_f32_16x16x32_bf16 v[32:35], a[24:27], v[112:115], v[32:35]\n"
      "s_waitcnt lgkmcnt(8)\n"
      "ds_write_addtid_b32 v25 offset:45320\n"
      "v_sub_f32_e32 v49, v37, v41\n"
      "v_mfma_f32_16x16x32_bf16 v[32:35], a[28:31], a[208:211], v[32:35]\n"
      "ds_read_b128 a[56:59], v4 offset:64\n"
      "v_sub_f32_e32 v50, v38, v42\n"
      "v_mfma_f32_16x16x32_bf16 v[32:35], a[32:35], v[220:223], v[32:35]\n"
      "s_waitcnt lgkmcnt(7)\n"
      "ds_read_b128 a[36:39], v2 offset:128\n"
      "v_sub_f32_e32 v51, v39, v43\n"
      "v_mfma_f32_16x16x32_bf16 v[32:35], a[0:3], a[212:215], v[32:35]\n"
      "ds_write_addtid_b32 v28 offset:32768\n"
      "v_add_f32_e32 v40, v36, v40\n"
      "v_mfma_f32_16x16x32_bf16 v[32:35], a[4:7], v[184:187], v[32:35]\n"
      "s_waitcnt lgkmcnt(8)\n"
      "ds_write_addtid_b32 v29 offset:33024\n"
      "v_add_f32_e32 v41, v37, v41\n"
      "v_mfma_f32_16x16x32_bf16 v[32:35], a[8:11], v[240:243], v[32:35]\n"
      "s_mov_b32 m0, s18\n"
      "ds_read_b128 a[44:47], v4 offset:128\n"
      "v_add_f32_e32 v42, v38, v42\n"
      "v_mfma_f32_16x16x32_bf16 v[72:75], a[84:87], a[64:67], 0\n"
      "ds_read2_b64 a[68:71], v11 offset0:2 offset1:66\n"
      "v_add_f32_e32 v43, v39, v43\n"
      "v_mfma_f32_16x16x32_bf16 v[20:23], a[12:15], v[100:103], 0\n"
      "v_cvt_pk_bf16_f32 v48, v48, v49\n"
      "v_cvt_pk_bf16_f32 v49, v50, v51\n"
      "v_mfma_f32_16x16x32_bf16 v[20:23], a[16:19], a[200:203], v[20:23]\n"
      "ds_write_addtid_b32 v48 offset:6144\n"
      "v_cvt_pk_bf16_f32 v40, v40, v41\n"
      "v_mfma_f32_16x16x32_bf16 v[20:23], a[20:23], v[208:211], v[20:23]\n"
      "ds_write_addtid_b32 v49 offset:43072\n"
      "v_cvt_pk_bf16_f32 v41, v42, v43\n"
      "v_mfma_f32_16x16x32_bf16 v[20:23], a[24:27], a[204:207], v[20:23]\n"
      "ds_write_addtid_b32 v40 offset:5376\n"
      "v_cvt_pk_bf16_f32 v32, v32, v33\n"
      "v_mfma_f32_16x16x32_bf16 v[20:23], a[28:31], v[172:175], v[20:23]\n"
      "ds_write_addtid_b32 v41 offset:42304\n"
      "v_cvt_pk_bf16_f32 v33, v34, v35\n"
      "v_mfma_f32_16x16x32_bf16 v[20:23], a[32:35], v[228:231], v[20:23]\n"
      "ds_write_addtid_b32 v44 offset:4608\n"
      "v_cvt_pk_bf16_f32 v81, v72, v73\n"
      "v_mfma_f32_16x16x32_bf16 v[20:23], a[0:3], v[136:139], v[20:23]\n"
      "ds_write_addtid_b32 v45 offset:41536\n"
      "v_cvt_pk_bf16_f32 v87, v74, v75\n"
      "v_mfma_f32_16x16x32_bf16 v[20:23], a[4:7], v[192:195], v[20:23]\n"
      "s_mov_b32 m0, s45\n"
      "ds_read_b128 a[40:43], v3 offset:128\n"
      "v_mfma_f32_16x16x32_bf16 v[20:23], a[8:11], v[244:247], v[20:23]\n"
      "s_waitcnt lgkmcnt(7)\n"
      "ds_read2_b64 a[60:63], v11 offset0:3 offset1:67\n"
      "s_nop 0\n"
      "v_mfma_f32_16x16x32_bf16 v[52:55], a[48:51], a[72:75], 0\n"
      "s_add_u32 m0, s58, 13280\n"
      "ds_read2_b64 a[64:67], v13 offset0:0 offset1:64\n"
      "s_nop 0\n"
      "v_mfma_f32_16x16x32_bf16 v[56:59], a[52:55], a[76:79], 0\n"
      "buffer_load_dwordx4 v17, s[32:35], s53 offen offset:1024 lds\n"
      "v_cvt_pk_bf16_f32 v20, v20, v21\n"
      "v_mfma_f32_16x16x32_bf16 v[60:63], a[56:59], a[80:83], v[52:55]\n"
      "s_mov_b32 m0, s45\n"
      "v_cvt_pk_bf16_f32 v21, v22, v23\n"
      "s_nop 0\n"
      "v_mfma_f32_16x16x32_bf16 v[36:39], a[36:39], a[72:75], 0\n"
      "ds_write_addtid_b32 v32 offset:58384\n"
      "v_sub_f32_e32 v64, v52, v56\n"
      "v_mfma_f32_16x16x32_bf16 v[44:47], a[44:47], a[80:83], v[36:39]\n"
      "ds_write_addtid_b32 v33 offset:58640\n"
      "v_sub_f32_e32 v65, v53, v57\n"
      "v_mfma_f32_16x16x32_bf16 v[76:79], a[84:87], a[68:71], 0\n"
      "global_load_dword v8, v9, s[54:55]\n"
      "v_sub_f32_e32 v66, v54, v58\n"
      "v_mfma_f32_16x16x32_bf16 v[24:27], a[12:15], a[196:199], 0\n"
      "ds_read_b128 a[12:15], v6 offset:1536\n"
      "v_sub_f32_e32 v67, v55, v59\n"
      "v_mfma_f32_16x16x32_bf16 v[24:27], a[16:19], v[160:163], v[24:27]\n"
      "ds_read_b128 a[16:19], v6 offset:1792\n"
      "v_add_f32_e32 v56, v52, v56\n"
      "v_mfma_f32_16x16x32_bf16 v[24:27], a[20:23], v[216:219], v[24:27]\n"
      "ds_read_b128 a[20:23], v6 offset:2048\n"
      "v_add_f32_e32 v57, v53, v57\n"
      "v_mfma_f32_16x16x32_bf16 v[24:27], a[24:27], v[124:127], v[24:27]\n"
      "ds_read_b128 a[24:27], v6 offset:6144\n"
      "v_add_f32_e32 v58, v54, v58\n"
      "v_mfma_f32_16x16x32_bf16 v[24:27], a[28:31], v[180:183], v[24:27]\n"
      "ds_read_b128 a[28:31], v6 offset:6400\n"
      "v_add_f32_e32 v59, v55, v59\n"
      "v_mfma_f32_16x16x32_bf16 v[24:27], a[32:35], v[232:235], v[24:27]\n"
      "ds_read_b128 a[32:35], v6 offset:6656\n"
      "v_cvt_pk_bf16_f32 v64, v64, v65\n"
      "v_mfma_f32_16x16x32_bf16 v[24:27], a[0:3], a[216:219], v[24:27]\n"
      "ds_read_b128 a[0:3], v7 offset:1536\n"
      "v_cvt_pk_bf16_f32 v65, v66, v67\n"
      "v_mfma_f32_16x16x32_bf16 v[24:27], a[4:7], v[196:199], v[24:27]\n"
      "ds_read_b128 a[4:7], v7 offset:1792\n"
      "v_cvt_pk_bf16_f32 v56, v56, v57\n"
      "v_mfma_f32_16x16x32_bf16 v[24:27], a[8:11], v[252:255], v[24:27]\n"
      "s_waitcnt lgkmcnt(7)\n"
      "ds_read_b128 a[8:11], v7 offset:2048\n"
      "v_cvt_pk_bf16_f32 v57, v58, v59\n"
      "v_mfma_f32_16x16x32_bf16 v[40:43], a[40:43], a[76:79], 0\n"
      "ds_write_addtid_b32 v20 offset:46088\n"
      "v_cvt_pk_bf16_f32 v60, v60, v61\n"
      "v_mfma_f32_16x16x32_bf16 v[28:31], a[12:15], v[96:99], 0\n"
      "s_waitcnt lgkmcnt(7)\n"
      "ds_write_addtid_b32 v21 offset:46344\n"
      "v_cvt_pk_bf16_f32 v61, v62, v63\n"
      "v_mfma_f32_16x16x32_bf16 v[28:31], a[16:19], v[152:155], v[28:31]\n"
      "s_mov_b32 m0, s18\n"
      "v_cvt_pk_bf16_f32 v24, v24, v25\n"
      "v_cvt_pk_bf16_f32 v25, v26, v27\n"
      "v_mfma_f32_16x16x32_bf16 v[28:31], a[20:23], a[220:223], v[28:31]\n"
      "s_waitcnt lgkmcnt(6)\n"
      "ds_write_addtid_b32 v64 offset:6400\n"
      "v_sub_f32_e32 v48, v36, v40\n"
      "v_mfma_f32_16x16x32_bf16 v[28:31], a[24:27], v[116:119], v[28:31]\n"
      "ds_write_addtid_b32 v65 offset:43328\n"
      "v_sub_f32_e32 v49, v37, v41\n"
      "v_mfma_f32_16x16x32_bf16 v[28:31], a[28:31], v[168:171], v[28:31]\n"
      "s_waitcnt lgkmcnt(6)\n"
      "ds_write_addtid_b32 v56 offset:5632\n"
      "v_cvt_pk_bf16_f32 v48, v48, v49\n"
      "v_mfma_f32_16x16x32_bf16 v[28:31], a[32:35], v[224:227], v[28:31]\n"
      "ds_write_addtid_b32 v57 offset:42560\n"
      "v_sub_f32_e32 v50, v38, v42\n"
      "v_mfma_f32_16x16x32_bf16 v[28:31], a[0:3], v[132:135], v[28:31]\n"
      "s_waitcnt lgkmcnt(6)\n"
      "ds_write_addtid_b32 v60 offset:4864\n"
      "v_sub_f32_e32 v51, v39, v43\n"
      "v_mfma_f32_16x16x32_bf16 v[28:31], a[4:7], v[188:191], v[28:31]\n"
      "ds_write_addtid_b32 v61 offset:41792\n"
      "v_cvt_pk_bf16_f32 v49, v50, v51\n"
      "v_mfma_f32_16x16x32_bf16 v[28:31], a[8:11], a[240:243], v[28:31]\n"
      "s_mov_b32 m0, s45\n"
      "v_add_f32_e32 v40, v36, v40\n"
      "v_add_f32_e32 v41, v37, v41\n"
      "v_mfma_f32_16x16x32_bf16 v[20:23], a[12:15], v[104:107], 0\n"
      "s_add_u32 m0, s58, 13280\n"
      "v_cvt_pk_bf16_f32 v40, v40, v41\n"
      "v_add_f32_e32 v42, v38, v42\n"
      "v_mfma_f32_16x16x32_bf16 v[20:23], a[16:19], v[156:159], v[20:23]\n"
      "buffer_load_dwordx4 v18, s[32:35], s53 offen offset:2048 lds\n"
      "v_add_f32_e32 v43, v39, v43\n"
      "v_mfma_f32_16x16x32_bf16 v[20:23], a[20:23], v[212:215], v[20:23]\n"
      "s_mov_b32 m0, s45\n"
      "v_cvt_pk_bf16_f32 v41, v42, v43\n"
      "v_cvt_pk_bf16_f32 v44, v44, v45\n"
      "v_mfma_f32_16x16x32_bf16 v[20:23], a[24:27], v[120:123], v[20:23]\n"
      "ds_write_addtid_b32 v24 offset:33792\n"
      "v_cvt_pk_bf16_f32 v45, v46, v47\n"
      "v_mfma_f32_16x16x32_bf16 v[20:23], a[28:31], v[176:179], v[20:23]\n"
      "ds_write_addtid_b32 v25 offset:34048\n"
      "v_cvt_pk_bf16_f32 v28, v28, v29\n"
      "v_mfma_f32_16x16x32_bf16 v[20:23], a[32:35], a[232:235], v[20:23]\n"
      "s_mov_b32 m0, s18\n"
      "ds_read2_b64 a[68:71], v13 offset0:1 offset1:65\n"
      "v_cvt_pk_bf16_f32 v29, v30, v31\n"
      "v_mfma_f32_16x16x32_bf16 v[20:23], a[0:3], v[140:143], v[20:23]\n"
      "s_sub_u32 s18, s19, s18\n"
      "ds_write_addtid_b32 v48 offset:6656\n"
      "v_cvt_pk_bf16_f32 v82, v76, v77\n"
      "v_mfma_f32_16x16x32_bf16 v[20:23], a[4:7], a[236:239], v[20:23]\n"
      "ds_write_addtid_b32 v49 offset:43584\n"
      "v_cvt_pk_bf16_f32 v88, v78, v79\n"
      "v_mfma_f32_16x16x32_bf16 v[20:23], a[8:11], v[248:251], v[20:23]\n"
      "ds_write_addtid_b32 v40 offset:5888\n"
      "s_nop 0\n"
      "v_mfma_f32_16x16x32_bf16 v[68:71], a[84:87], a[60:63], 0\n"
      "ds_write_addtid_b32 v41 offset:42816\n"
      "s_nop 0\n"
      "v_mfma_f32_16x16x32_bf16 v[32:35], a[12:15], v[108:111], 0\n"
      "ds_write_addtid_b32 v44 offset:5120\n"
      "v_mfma_f32_16x16x32_bf16 v[32:35], a[16:19], v[164:167], v[32:35]\n"
      "ds_write_addtid_b32 v45 offset:42048\n"
      "v_cvt_pk_bf16_f32 v20, v20, v21\n"
      "v_mfma_f32_16x16x32_bf16 v[32:35], a[20:23], a[224:227], v[32:35]\n"
      "s_mov_b32 m0, s45\n"
      "v_cvt_pk_bf16_f32 v83, v68, v69\n"
      "v_cvt_pk_bf16_f32 v21, v22, v23\n"
      "v_mfma_f32_16x16x32_bf16 v[32:35], a[24:27], v[128:131], v[32:35]\n"
      "ds_write_addtid_b32 v28 offset:59408\n"
      "v_cvt_pk_bf16_f32 v89, v70, v71\n"
      "v_mfma_f32_16x16x32_bf16 v[32:35], a[28:31], a[228:231], v[32:35]\n"
      "ds_write_addtid_b32 v29 offset:59664\n"
      "v_mfma_f32_16x16x32_bf16 v[32:35], a[32:35], v[236:239], v[32:35]\n"
      "ds_write_addtid_b32 v20 offset:47112\n"
      "v_mfma_f32_16x16x32_bf16 v[32:35], a[0:3], v[144:147], v[32:35]\n"
      "buffer_store_dwordx4 v[80:83], v14, s[36:39], s52 offen sc1\n"
      "v_mfma_f32_16x16x32_bf16 v[32:35], a[4:7], v[200:203], v[32:35]\n"
      "ds_write_addtid_b32 v21 offset:47368\n"
      "v_mfma_f32_16x16x32_bf16 v[32:35], a[8:11], a[244:247], v[32:35]\n"
      "s_nop 1\n"
      "v_mfma_f32_16x16x32_bf16 v[72:75], a[84:87], a[64:67], 0\n"
      "s_waitcnt lgkmcnt(10)\n"
      "s_nop 1\n"
      "v_mfma_f32_16x16x32_bf16 v[76:79], a[84:87], a[68:71], 0\n"
      "buffer_store_dwordx4 v[86:89], v14, s[36:39], s52 offen offset:1536 sc1\n"
      "v_cvt_pk_bf16_f32 v32, v32, v33\n"
      "s_cbranch_vccnz Lconv_loop1\n"
      "Lconv_t1:\n"
      "ds_write_addtid_b32 v32 offset:34816\n"
      "v_cvt_pk_bf16_f32 v33, v34, v35\n"
      "v_mfma_f32_16x16x32_bf16 v[20:23], a[24:27], v[96:99], 0\n"
      "ds_write_addtid_b32 v33 offset:35072\n"
      "v_cvt_pk_bf16_f32 v84, v72, v73\n"
      "v_mfma_f32_16x16x32_bf16 v[20:23], a[28:31], v[152:155], v[20:23]\n"
      "v_cvt_pk_bf16_f32 v90, v74, v75\n"
      "v_cvt_pk_bf16_f32 v85, v76, v77\n"
      "v_mfma_f32_16x16x32_bf16 v[20:23], a[32:35], a[220:223], v[20:23]\n"
      "buffer_store_dwordx2 v[84:85], v15, s[36:39], s52 offen sc1\n"
      "v_cvt_pk_bf16_f32 v91, v78, v79\n"
      "v_mfma_f32_16x16x32_bf16 v[20:23], a[0:3], v[116:119], v[20:23]\n"
      "v_mfma_f32_16x16x32_bf16 v[20:23], a[4:7], v[168:171], v[20:23]\n"
      "v_mfma_f32_16x16x32_bf16 v[20:23], a[8:11], v[224:227], v[20:23]\n"
      "buffer_store_dwordx2 v[90:91], v15, s[36:39], s52 offen offset:1536 sc1\n"
      "v_mfma_f32_16x16x32_bf16 v[24:27], a[24:27], v[104:107], 0\n"
      "s_mov_b32 s52, s13\n"
      "v_mfma_f32_16x16x32_bf16 v[24:27], a[28:31], v[156:159], v[24:27]\n"
      "s_add_u32 s13, s13, s12\n"
      "v_mfma_f32_16x16x32_bf16 v[24:27], a[32:35], v[212:215], v[24:27]\n"
      "v_mfma_f32_16x16x32_bf16 v[24:27], a[0:3], v[120:123], v[24:27]\n"
      "v_mfma_f32_16x16x32_bf16 v[24:27], a[4:7], v[176:179], v[24:27]\n"
      "s_waitcnt lgkmcnt(0)\n"
      "s_barrier\n"
      "ds_read2_b64 a[60:63], v10 offset0:0 offset1:64\n"
      "v_mfma_f32_16x16x32_bf16 v[24:27], a[8:11], a[232:235], v[24:27]\n"
      "s_waitcnt lgkmcnt(0)\n"
      "ds_read2_b64 a[64:67], v10 offset0:1 offset1:65\n"
      "v_mfma_f32_16x16x32_bf16 v[68:71], a[84:87], a[60:63], 0\n"
      "ds_read2_b64 a[68:71], v10 offset0:2 offset1:66\n"
      "v_mfma_f32_16x16x32_bf16 v[28:31], a[24:27], v[108:111], 0\n"
      "ds_read_b128 a[12:15], v7 offset:6144\n"
      "v_mfma_f32_16x16x32_bf16 v[28:31], a[28:31], v[164:167], v[28:31]\n"
      "ds_read_b128 a[16:19], v7 offset:6400\n"
      "v_mfma_f32_16x16x32_bf16 v[28:31], a[32:35], a[224:227], v[28:31]\n"
      "ds_read_b128 a[20:23], v7 offset:6656\n"
      "v_mfma_f32_16x16x32_bf16 v[28:31], a[0:3], v[128:131], v[28:31]\n"
      "ds_read2_b64 a[60:63], v10 offset0:3 offset1:67\n"
      "v_cvt_pk_bf16_f32 v80, v68, v69\n"
      "v_mfma_f32_16x16x32_bf16 v[28:31], a[4:7], a[228:231], v[28:31]\n"
      "ds_read_b128 a[24:27], v6 offset:5376\n"
      "v_cvt_pk_bf16_f32 v86, v70, v71\n"
      "v_mfma_f32_16x16x32_bf16 v[28:31], a[8:11], v[236:239], v[28:31]\n"
      "s_waitcnt lgkmcnt(6)\n"
      "ds_read_b128 a[28:31], v6 offset:5632\n"
      "v_mfma_f32_16x16x32_bf16 v[72:75], a[84:87], a[64:67], 0\n"
      "s_waitcnt lgkmcnt(6)\n"
      "ds_read2_b64 a[64:67], v12 offset0:0 offset1:64\n"
      "v_mfma_f32_16x16x32_bf16 v[76:79], a[84:87], a[68:71], 0\n"
      "s_waitcnt lgkmcnt(6)\n"
      "ds_read2_b64 a[68:71], v12 offset0:1 offset1:65\n"
      "s_nop 0\n"
      "v_mfma_f32_16x16x32_bf16 v[20:23], a[12:15], v[132:135], v[20:23]\n"
      "s_waitcnt lgkmcnt(6)\n"
      "ds_read_b128 a[32:35], v6 offset:5888\n"
      "v_cvt_pk_bf16_f32 v81, v72, v73\n"
      "v_mfma_f32_16x16x32_bf16 v[20:23], a[16:19], v[188:191], v[20:23]\n"
      "s_waitcnt lgkmcnt(6)\n"
      "ds_read_b128 a[0:3], v7 offset:768\n"
      "v_cvt_pk_bf16_f32 v82, v76, v77\n"
      "v_mfma_f32_16x16x32_bf16 v[20:23], a[20:23], a[240:243], v[20:23]\n"
      "s_waitcnt lgkmcnt(6)\n"
      "ds_read_b128 a[4:7], v7 offset:1024\n"
      "v_cvt_pk_bf16_f32 v87, v74, v75\n"
      "v_mfma_f32_16x16x32_bf16 v[68:71], a[84:87], a[60:63], 0\n"
      "ds_read_b128 a[8:11], v7 offset:1280\n"
      "v_cvt_pk_bf16_f32 v88, v78, v79\n"
      "v_mfma_f32_16x16x32_bf16 v[24:27], a[12:15], v[140:143], v[24:27]\n"
      "v_mfma_f32_16x16x32_bf16 v[24:27], a[16:19], a[236:239], v[24:27]\n"
      "v_cvt_pk_bf16_f32 v20, v20, v21\n"
      "v_cvt_pk_bf16_f32 v21, v22, v23\n"
      "v_mfma_f32_16x16x32_bf16 v[24:27], a[20:23], v[248:251], v[24:27]\n"
      "s_waitcnt lgkmcnt(5)\n"
      "ds_write_addtid_b32 v20 offset:62480\n"
      "v_cvt_pk_bf16_f32 v83, v68, v69\n"
      "v_mfma_f32_16x16x32_bf16 v[72:75], a[84:87], a[64:67], 0\n"
      "buffer_store_dwordx4 v[80:83], v14, s[36:39], s52 offen sc1\n"
      "v_cvt_pk_bf16_f32 v89, v70, v71\n"
      "v_mfma_f32_16x16x32_bf16 v[28:31], a[12:15], v[144:147], v[28:31]\n"
      "ds_read_b128 a[12:15], v7 offset:5376\n"
      "v_cvt_pk_bf16_f32 v24, v24, v25\n"
      "v_mfma_f32_16x16x32_bf16 v[28:31], a[16:19], v[200:203], v[28:31]\n"
      "ds_read_b128 a[16:19], v7 offset:5632\n"
      "v_cvt_pk_bf16_f32 v25, v26, v27\n"
      "v_mfma_f32_16x16x32_bf16 v[28:31], a[20:23], a[244:247], v[28:31]\n"
      "s_waitcnt lgkmcnt(4)\n"
      "ds_read_b128 a[20:23], v7 offset:5888\n"
      "v_cvt_pk_bf16_f32 v84, v72, v73\n"
      "v_mfma_f32_16x16x32_bf16 v[76:79], a[84:87], a[68:71], 0\n"
      "ds_write_addtid_b32 v21 offset:62736\n"
      "v_cvt_pk_bf16_f32 v90, v74, v75\n"
      "v_mfma_f32_16x16x32_bf16 v[32:35], a[24:27], v[92:95], 0\n"
      "buffer_store_dwordx4 v[86:89], v14, s[36:39], s52 offen offset:1536 sc1\n"
      "v_cvt_pk_bf16_f32 v28, v28, v29\n"
      "v_mfma_f32_16x16x32_bf16 v[32:35], a[28:31], v[148:151], v[32:35]\n"
      "ds_write_addtid_b32 v24 offset:50184\n"
      "v_cvt_pk_bf16_f32 v29, v30, v31\n"
      "v_mfma_f32_16x16x32_bf16 v[32:35], a[32:35], v[204:207], v[32:35]\n"
      "ds_write_addtid_b32 v25 offset:50440\n"
      "v_cvt_pk_bf16_f32 v85, v76, v77\n"
      "v_mfma_f32_16x16x32_bf16 v[32:35], a[0:3], v[112:115], v[32:35]\n"
      "ds_write_addtid_b32 v28 offset:37888\n"
      "v_cvt_pk_bf16_f32 v91, v78, v79\n"
      "v_mfma_f32_16x16x32_bf16 v[32:35], a[4:7], a[208:211], v[32:35]\n"
      "s_waitcnt lgkmcnt(6)\n"
      "ds_write_addtid_b32 v29 offset:38144\n"
      "v_mfma_f32_16x16x32_bf16 v[32:35], a[8:11], v[220:223], v[32:35]\n"
      "buffer_store_dwordx2 v[84:85], v15, s[36:39], s52 offen sc1\n"
      "v_mfma_f32_16x16x32_bf16 v[32:35], a[12:15], a[212:215], v[32:35]\n"
      "s_waitcnt lgkmcnt(5)\n"
      "v_mfma_f32_16x16x32_bf16 v[32:35], a[16:19], v[184:187], v[32:35]\n"
      "v_mfma_f32_16x16x32_bf16 v[32:35], a[20:23], v[240:243], v[32:35]\n"
      "buffer_store_dwordx2 v[90:91], v15, s[36:39], s52 offen offset:1536 sc1\n"
      "s_nop 0\n"
      "v_mfma_f32_16x16x32_bf16 v[20:23], a[24:27], v[100:103], 0\n"
      "s_nop 1\n"
      "v_mfma_f32_16x16x32_bf16 v[20:23], a[28:31], a[200:203], v[20:23]\n"
      "s_nop 0\n"
      "v_mfma_f32_16x16x32_bf16 v[20:23], a[32:35], v[208:211], v[20:23]\n"
      "v_cvt_pk_bf16_f32 v32, v32, v33\n"
      "v_cvt_pk_bf16_f32 v33, v34, v35\n"
      "v_mfma_f32_16x16x32_bf16 v[20:23], a[0:3], a[204:207], v[20:23]\n"
      "ds_write_addtid_b32 v32 offset:61456\n"
      "v_mfma_f32_16x16x32_bf16 v[20:23], a[4:7], v[172:175], v[20:23]\n"
      "ds_write_addtid_b32 v33 offset:61712\n"
      "v_mfma_f32_16x16x32_bf16 v[20:23], a[8:11], v[228:231], v[20:23]\n"
      "v_mfma_f32_16x16x32_bf16 v[20:23], a[12:15], v[136:139], v[20:23]\n"
      "v_mfma_f32_16x16x32_bf16 v[20:23], a[16:19], v[192:195], v[20:23]\n"
      "v_mfma_f32_16x16x32_bf16 v[20:23], a[20:23], v[244:247], v[20:23]\n"
      "s_nop 1\n"
      "v_mfma_f32_16x16x32_bf16 v[24:27], a[24:27], a[196:199], 0\n"
      "ds_read_b128 a[24:27], v7 offset:4608\n"
      "s_nop 0\n"
      "v_mfma_f32_16x16x32_bf16 v[24:27], a[28:31], v[160:163], v[24:27]\n"
      "ds_read_b128 a[28:31], v7 offset:4864\n"
      "v_mfma_f32_16x16x32_bf16 v[24:27], a[32:35], v[216:219], v[24:27]\n"
      "ds_read_b128 a[32:35], v7 offset:5120\n"
      "v_cvt_pk_bf16_f32 v20, v20, v21\n"
      "v_mfma_f32_16x16x32_bf16 v[24:27], a[0:3], v[124:127], v[24:27]\n"
      "ds_read_b128 a[0:3], v6 offset:4608\n"
      "v_cvt_pk_bf16_f32 v21, v22, v23\n"
      "v_mfma_f32_16x16x32_bf16 v[24:27], a[4:7], v[180:183], v[24:27]\n"
      "ds_read_b128 a[4:7], v6 offset:4864\n"
      "v_mfma_f32_16x16x32_bf16 v[24:27], a[8:11], v[232:235], v[24:27]\n"
      "ds_read_b128 a[8:11], v6 offset:5120\n"
      "v_mfma_f32_16x16x32_bf16 v[24:27], a[12:15], a[216:219], v[24:27]\n"
      "ds_read_b128 a[12:15], v7 offset:0\n"
      "v_mfma_f32_16x16x32_bf16 v[24:27], a[16:19], v[196:199], v[24:27]\n"
      "ds_read_b128 a[16:19], v7 offset:256\n"
      "v_mfma_f32_16x16x32_bf16 v[24:27], a[20:23], v[252:255], v[24:27]\n"
      "s_waitcnt lgkmcnt(7)\n"
      "ds_read_b128 a[20:23], v7 offset:512\n"
      "s_nop 0\n"
      "v_mfma_f32_16x16x32_bf16 v[28:31], a[24:27], a[160:163], 0\n"
      "s_waitcnt lgkmcnt(7)\n"
      "ds_write_addtid_b32 v20 offset:49160\n"
      "s_nop 0\n"
      "v_mfma_f32_16x16x32_bf16 v[28:31], a[28:31], a[172:175], v[28:31]\n"
      "s_waitcnt lgkmcnt(7)\n"
      "ds_write_addtid_b32 v21 offset:49416\n"
      "v_cvt_pk_bf16_f32 v24, v24, v25\n"
      "v_mfma_f32_16x16x32_bf16 v[28:31], a[32:35], a[184:187], v[28:31]\n"
      "s_waitcnt lgkmcnt(7)\n"
      "ds_write_addtid_b32 v24 offset:36864\n"
      "v_cvt_pk_bf16_f32 v25, v26, v27\n"
      "v_mfma_f32_16x16x32_bf16 v[28:31], a[0:3], a[88:91], v[28:31]\n"
      "s_waitcnt lgkmcnt(7)\n"
      "ds_write_addtid_b32 v25 offset:37120\n"
      "v_mfma_f32_16x16x32_bf16 v[28:31], a[4:7], a[100:103], v[28:31]\n"
      "s_waitcnt lgkmcnt(7)\n"
      "v_mfma_f32_16x16x32_bf16 v[28:31], a[8:11], a[112:115], v[28:31]\n"
      "s_waitcnt lgkmcnt(6)\n"
      "v_mfma_f32_16x16x32_bf16 v[28:31], a[12:15], a[124:127], v[28:31]\n"
      "s_waitcnt lgkmcnt(5)\n"
      "v_mfma_f32_16x16x32_bf16 v[28:31], a[16:19], a[136:139], v[28:31]\n"
      "s_waitcnt lgkmcnt(4)\n"
      "v_mfma_f32_16x16x32_bf16 v[28:31], a[20:23], a[148:151], v[28:31]\n"
      "s_nop 1\n"
      "v_mfma_f32_16x16x32_bf16 v[20:23], a[24:27], a[164:167], 0\n"
      "s_nop 1\n"
      "v_mfma_f32_16x16x32_bf16 v[20:23], a[28:31], a[176:179], v[20:23]\n"
      "s_nop 0\n"
      "v_mfma_f32_16x16x32_bf16 v[20:23], a[32:35], a[188:191], v[20:23]\n"
      "v_cvt_pk_bf16_f32 v28, v28, v29\n"
      "v_cvt_pk_bf16_f32 v29, v30, v31\n"
      "v_mfma_f32_16x16x32_bf16 v[20:23], a[0:3], a[92:95], v[20:23]\n"
      "ds_write_addtid_b32 v28 offset:60432\n"
      "v_mfma_f32_16x16x32_bf16 v[20:23], a[4:7], a[104:107], v[20:23]\n"
      "ds_write_addtid_b32 v29 offset:60688\n"
      "v_mfma_f32_16x16x32_bf16 v[20:23], a[8:11], a[116:119], v[20:23]\n"
      "v_mfma_f32_16x16x32_bf16 v[20:23], a[12:15], a[128:131], v[20:23]\n"
      "v_mfma_f32_16x16x32_bf16 v[20:23], a[16:19], a[140:143], v[20:23]\n"
      "v_mfma_f32_16x16x32_bf16 v[20:23], a[20:23], a[152:155], v[20:23]\n"
      "s_nop 1\n"
      "v_mfma_f32_16x16x32_bf16 v[32:35], a[24:27], a[168:171], 0\n"
      "s_nop 1\n"
      "v_mfma_f32_16x16x32_bf16 v[32:35], a[28:31], a[180:183], v[32:35]\n"
      "s_nop 0\n"
      "v_mfma_f32_16x16x32_bf16 v[32:35], a[32:35], a[192:195], v[32:35]\n"
      "v_cvt_pk_bf16_f32 v20, v20, v21\n"
      "v_cvt_pk_bf16_f32 v21, v22, v23\n"
      "v_mfma_f32_16x16x32_bf16 v[32:35], a[0:3], a[96:99], v[32:35]\n"
      "ds_write_addtid_b32 v20 offset:48136\n"
      "v_mfma_f32_16x16x32_bf16 v[32:35], a[4:7], a[108:111], v[32:35]\n"
      "ds_write_addtid_b32 v21 offset:48392\n"
      "v_mfma_f32_16x16x32_bf16 v[32:35], a[8:11], a[120:123], v[32:35]\n"
      "v_mfma_f32_16x16x32_bf16 v[32:35], a[12:15], a[132:135], v[32:35]\n"
      "v_mfma_f32_16x16x32_bf16 v[32:35], a[16:19], a[144:147], v[32:35]\n"
      "v_mfma_f32_16x16x32_bf16 v[32:35], a[20:23], a[156:159], v[32:35]\n"
      "s_nop 1\n"
      "s_nop 5\n"
      "v_cvt_pk_bf16_f32 v32, v32, v33\n"
      "v_cvt_pk_bf16_f32 v33, v34, v35\n"
      "ds_write_addtid_b32 v32 offset:35840\n"
      "ds_write_addtid_b32 v33 offset:36096\n"
      "s_waitcnt lgkmcnt(0)\n"
      "s_barrier\n"
      "ds_read2_b64 a[0:3], v11 offset0:0 offset1:64\n"
      "ds_read2_b64 a[4:7], v11 offset0:1 offset1:65\n"
      "ds_read2_b64 a[8:11], v11 offset0:2 offset1:66\n"
      "ds_read2_b64 a[12:15], v11 offset0:3 offset1:67\n"
      "ds_read2_b64 a[16:19], v13 offset0:0 offset1:64\n"
      "ds_read2_b64 a[20:23], v13 offset0:1 offset1:65\n"
      "s_waitcnt lgkmcnt(5)\n"
      "v_mfma_f32_16x16x32_bf16 v[68:71], a[84:87], a[0:3], 0\n"
      "s_waitcnt lgkmcnt(4)\n"
      "v_mfma_f32_16x16x32_bf16 v[72:75], a[84:87], a[4:7], 0\n"
      "s_waitcnt lgkmcnt(3)\n"
      "v_mfma_f32_16x16x32_bf16 v[76:79], a[84:87], a[8:11], 0\n"
      "s_waitcnt lgkmcnt(2)\n"
      "s_nop 2\n"
      "v_cvt_pk_bf16_f32 v80, v68, v69\n"
      "v_cvt_pk_bf16_f32 v86, v70, v71\n"
      "v_mfma_f32_16x16x32_bf16 v[68:71], a[84:87], a[12:15], 0\n"
      "s_waitcnt lgkmcnt(1)\n"
      "v_cvt_pk_bf16_f32 v81, v72, v73\n"
      "v_cvt_pk_bf16_f32 v87, v74, v75\n"
      "v_cvt_pk_bf16_f32 v82, v76, v77\n"
      "v_cvt_pk_bf16_f32 v88, v78, v79\n"
      "v_mfma_f32_16x16x32_bf16 v[72:75], a[84:87], a[16:19], 0\n"
      "s_waitcnt lgkmcnt(0)\n"
      "v_mfma_f32_16x16x32_bf16 v[76:79], a[84:87], a[20:23], 0\n"
      "s_mov_b32 s52, s13\n"
      "v_cvt_pk_bf16_f32 v83, v68, v69\n"
      "v_cvt_pk_bf16_f32 v89, v70, v71\n"
      "s_add_u32 s13, s13, s12\n"
      "buffer_store_dwordx4 v[80:83], v14, s[36:39], s52 offen sc1\n"
      "buffer_store_dwordx4 v[86:89], v14, s[36:39], s52 offen offset:1536 sc1\n"
      "v_cvt_pk_bf16_f32 v84, v72, v73\n"
      "v_cvt_pk_bf16_f32 v90, v74, v75\n"
      "v_cvt_pk_bf16_f32 v85, v76, v77\n"
      "v_cvt_pk_bf16_f32 v91, v78, v79\n"
      "buffer_store_dwordx2 v[84:85], v15, s[36:39], s52 offen sc1\n"
      "buffer_store_dwordx2 v[90:91], v15, s[36:39], s52 offen offset:1536 sc1\n"
      "Lconv_u1:\n"
      "Lconv_tail:\n"
      "s_waitcnt vmcnt(0) lgkmcnt(0)\n"
      "Lconv_end:\n"
      :
      : "v"(threadIdx.x), "s"(blockIdx.x), "s"(__builtin_amdgcn_kernarg_segment_ptr())
      : "v1", "v2", "v3", "v4", "v5", "v6", "v7", "v8", "v9", "v10", "v11", "v12", "v13", "v14", "v15", "v16", "v17", "v18", "v19", "v20", "v21", "v22", "v23", "v24", "v25", "v26", "v27", "v28", "v29", "v30", "v31", "v32", "v33", "v34", "v35", "v36", "v37", "v38", "v39", "v40", "v41", "v42", "v43", "v44", "v45", "v46", "v47", "v48", "v49", "v50", "v51", "v52", "v53", "v54", "v55", "v56", "v57", "v58", "v59", "v60", "v61", "v62", "v63", "v64", "v65", "v66", "v67", "v68", "v69", "v70", "v71", "v72", "v73", "v74", "v75", "v76", "v77", "v78", "v79", "v80", "v81", "v82", "v83", "v84", "v85", "v86", "v87", "v88", "v89", "v90", "v91", "v92", "v93", "v94", "v95", "v96", "v97", "v98", "v99", "v100", "v101", "v102", "v103", "v104", "v105", "v106", "v107", "v108", "v109", "v110", "v111", "v112", "v113", "v114", "v115", "v116", "v117", "v118", "v119", "v120", "v121", "v122", "v123", "v124", "v125", "v126", "v127", "v128", "v129", "v130", "v131", "v132", "v133", "v134", "v135", "v136", "v137", "v138", "v139", "v140", "v141", "v142", "v143", "v144", "v145", "v146", "v147", "v148", "v149", "v150", "v151", "v152", "v153", "v154", "v155", "v156", "v157", "v158", "v159", "v160", "v161", "v162", "v163", "v164", "v165", "v166", "v167", "v168", "v169", "v170", "v171", "v172", "v173", "v174", "v175", "v176", "v177", "v178", "v179", "v180", "v181", "v182", "v183", "v184", "v185", "v186", "v187", "v188", "v189", "v190", "v191", "v192", "v193", "v194", "v195", "v196", "v197", "v198", "v199", "v200", "v201", "v202", "v203", "v204", "v205", "v206", "v207", "v208", "v209", "v210", "v211", "v212", "v213", "v214", "v215", "v216", "v217", "v218", "v219", "v220", "v221", "v222", "v223", "v224", "v225", "v226", "v227", "v228", "v229", "v230", "v231", "v232", "v233", "v234", "v235", "v236", "v237", "v238", "v239", "v240", "v241", "v242", "v243", "v244", "v245", "v246", "v247", "v248", "v249", "v250", "v251", "v252", "v253", "v254", "v255", "a0", "a1", "a2", "a3", "a4", "a5", "a6", "a7", "a8", "a9", "a10", "a11", "a12", "a13", "a14", "a15", "a16", "a17", "a18", "a19", "a20", "a21", "a22", "a23", "a24", "a25", "a26", "a27", "a28", "a29", "a30", "a31", "a32", "a33", "a34", "a35", "a36", "a37", "a38", "a39", "a40", "a41", "a42", "a43", "a44", "a45", "a46", "a47", "a48", "a49", "a50", "a51", "a52", "a53", "a54", "a55", "a56", "a57", "a58", "a59", "a60", "a61", "a62", "a63", "a64", "a65", "a66", "a67", "a68", "a69", "a70", "a71", "a72", "a73", "a74", "a75", "a76", "a77", "a78", "a79", "a80", "a81", "a82", "a83", "a84", "a85", "a86", "a87", "a88", "a89", "a90", "a91", "a92", "a93", "a94", "a95", "a96", "a97", "a98", "a99", "a100", "a101", "a102", "a103", "a104", "a105", "a106", "a107", "a108", "a109", "a110", "a111", "a112", "a113", "a114", "a115", "a116", "a117", "a118", "a119", "a120", "a121", "a122", "a123", "a124", "a125", "a126", "a127", "a128", "a129", "a130", "a131", "a132", "a133", "a134", "a135", "a136", "a137", "a138", "a139", "a140", "a141", "a142", "a143", "a144", "a145", "a146", "a147", "a148", "a149", "a150", "a151", "a152", "a153", "a154", "a155", "a156", "a157", "a158", "a159", "a160", "a161", "a162", "a163", "a164", "a165", "a166", "a167", "a168", "a169", "a170", "a171", "a172", "a173", "a174", "a175", "a176", "a177", "a178", "a179", "a180", "a181", "a182", "a183", "a184", "a185", "a186", "a187", "a188", "a189", "a190", "a191", "a192", "a193", "a194", "a195", "a196", "a197", "a198", "a199", "a200", "a201", "a202", "a203", "a204", "a205", "a206", "a207", "a208", "a209", "a210", "a211", "a212", "a213", "a214", "a215", "a216", "a217", "a218", "a219", "a220", "a221", "a222", "a223", "a224", "a225", "a226", "a227", "a228", "a229", "a230", "a231", "a232", "a233", "a234", "a235", "a236", "a237", "a238", "a239", "a240", "a241", "a242", "a243", "a244", "a245", "a246", "a247", "a248", "a249", "a250", "a251", "a252", "a253", "a254", "a255", "s3", "s4", "s5", "s6", "s7", "s8", "s9", "s10", "s11", "s12", "s13", "s14", "s15", "s16", "s17", "s18", "s19", "s20", "s21", "s22", "s23", "s24", "s25", "s26", "s27", "s28", "s29", "s30", "s31", "s34", "s35", "s36", "s37", "s38", "s39", "s40", "s41", "s42", "s43", "s44", "s45", "s46", "s47", "s48", "s49", "s50", "s51", "s52", "s53", "s54", "s55", "s56", "s57", "s58", "s59", "s60", "s61", "s62", "s63", "s64", "s65", "s66", "s67", "s68", "s69", "s70", "s71", "s72", "s73", "s74", "s75", "s76", "s77", "s78", "s79", "s80", "s81", "s82", "s83", "s84", "s85", "s86", "s87", "s88", "s89", "s90", "s91", "s92", "s93", "s94", "s95", "s96", "s97", "s98", "s99", "scc", "vcc", "memory");
}

extern "C" void kernel_launch(void* const* d_in, const int* in_sizes, int n_in, void* d_out,
                              int out_size, void* d_ws, size_t ws_size, hipStream_t stream) {
  conv_kernel<<<256, 256, 0, stream>>>((const char*)d_in[0], (const char*)d_in[1], (char*)d_out, (char*)d_ws);
}
```

```cpp
#include <hip/hip_runtime.h>
#include <hip/hip_bf16.h>
#include <stdint.h>

__global__ void __launch_bounds__(256, 1) __attribute__((aligned(4096)))
conv_kernel(const char* __restrict__ x, const char* __restrict__ w, char* __restrict__ out, char* __restrict__ dbg) {
  __shared__ __attribute__((aligned(1024))) char smem[163840];
  asm volatile("" :: "v"((unsigned)(uintptr_t)smem) : "memory");
  asm volatile(
      "s_load_dwordx8 s[20:27], %2, 0x0\n"
      "v_readfirstlane_b32 s40, %0\n"
      "v_and_b32 v5, 63, %0\n"
      "v_lshlrev_b32 v8, 4, v5\n"
      "s_lshr_b32 s40, s40, 6\n"
      "s_lshl_b32 s43, s40, 3\n"
      "s_or_b32 s43, s43, 0x80000\n"
      "v_bitop3_b32 v9, v8, v5, 32 bitop3:0x78\n"
      "v_xor_b32 v89, 32, v8\n"
      "s_mul_i32 s92, s40, 3072\n"
      "s_lshr_b32 s14, %1, 7\n"
      "s_sub_u32 s14, s14, 1\n"
      "s_lshr_b32 s41, s40, 1\n"
      "s_and_b32 s42, s40, 1\n"
      "s_mov_b32 s30, 165888\n"
      "s_mov_b32 s31, 0x20000\n"
      "s_lshr_b32 s49, %1, 3\n"
      "s_lshl2_add_u32 s49, s49, s40\n"
      "v_lshlrev_b32 v36, 7, v5\n"
      "s_mul_i32 s50, s49, 384\n"
      "v_min_u32 v37, 256, v36\n"
      "v_add_u32 v37, s50, v37\n"
      "v_min_u32 v66, 1280, v36\n"
      "s_mul_i32 s96, s49, 1408\n"
      "s_getpc_b64 s[54:55]\n"
      "Lconv_start:\n"
      "v_min_u32 v67, Lconv_end-Lconv_start-4, v37\n"
      "s_mov_b32 m0, 162816\n"
      "s_nop 0\n"
      "global_load_lds_dword v67, s[54:55]\n"
      "s_bfe_u32 s99, %1, 0x30003\n"
      "s_mul_i32 s49, s40, 192\n"
      "v_add_u32 v36, s49, v5\n"
      "v_add_u32 v38, 128, v36\n"
      "v_mul_u32_u24 v37, 669, v36\n"
      "v_lshrrev_b32 v37, 16, v37\n"
      "v_mad_i32_i24 v37, v37, -2, v38\n"
      "v_lshlrev_b32 v16, 4, v37\n"
      "v_add_u32 v36, 64, v36\n"
      "v_mul_u32_u24 v37, 669, v36\n"
      "v_lshrrev_b32 v37, 16, v37\n"
      "v_mad_i32_i24 v37, v37, -2, v38\n"
      "v_lshlrev_b32 v17, 4, v37\n"
      "v_add_u32 v36, 64, v36\n"
      "v_mul_u32_u24 v37, 669, v36\n"
      "v_lshrrev_b32 v37, 16, v37\n"
      "v_mad_i32_i24 v37, v37, -2, v38\n"
      "v_lshlrev_b32 v18, 4, v37\n"
      "s_or_b32 s50, s99, s40\n"
      "s_cmp_eq_u32 s50, 0\n"
      "s_cselect_b32 s16, 0xffffff, 0\n"
      "s_mov_b32 s17, 0\n"
      "s_cmp_eq_u32 s99, 7\n"
      "s_cselect_b32 s19, 0xffffff00, 0\n"
      "s_mov_b32 s18, 0\n"
      "v_add_u32 v19, 14080, v8\n"
      "v_cndmask_b32 v16, v16, 2.0, s[16:17]\n"
      "v_cndmask_b32 v19, v19, 2.0, s[18:19]\n"
      "v_and_b32 v36, 16, v8\n"
      "v_mov_b32 v52, 0xffff\n"
      "v_lshlrev_b32 v52, v36, v52\n"
      "s_mov_b32 s16, 0x3000003\n"
      "s_mov_b32 s17, 0x3000003\n"
      "v_cndmask_b32 v48, 0, v52, s[16:17]\n"
      "s_lshl_b64 s[16:17], s[16:17], 2\n"
      "v_cndmask_b32 v49, 0, v52, s[16:17]\n"
      "s_lshl_b64 s[16:17], s[16:17], 2\n"
      "v_cndmask_b32 v50, 0, v52, s[16:17]\n"
      "s_lshl_b64 s[16:17], s[16:17], 2\n"
      "v_cndmask_b32 v51, 0, v52, s[16:17]\n"
      "v_bfe_u32 v53, v5, 5, 1\n"
      "v_xor_b32 v53, s41, v53\n"
      "v_sub_u32 v53, 0, v53\n"
      "v_and_b32 v36, 0x1000100, v53\n"
      "v_xor_b32 v36, 0x3fbf3fbf, v36\n"
      "v_and_b32 v68, v36, v48\n"
      "v_and_b32 v69, v36, v49\n"
      "v_and_b32 v70, v36, v50\n"
      "v_and_b32 v71, v36, v51\n"
      "v_xor_b32 v72, 0x80008000, v68\n"
      "v_xor_b32 v73, 0x80008000, v69\n"
      "v_xor_b32 v74, 0x80008000, v70\n"
      "v_xor_b32 v75, 0x80008000, v71\n"
      "v_bfe_i32 v36, v5, 5, 1\n"
      "v_and_b32 v36, 0x3f3f3f3f, v36\n"
      "v_xor_b32 v37, 0x3f3f3f3f, v36\n"
      "v_and_b32 v76, v37, v48\n"
      "v_and_b32 v77, v37, v49\n"
      "v_and_b32 v78, v37, v50\n"
      "v_and_b32 v79, v37, v51\n"
      "v_and_b32 v80, v36, v48\n"
      "v_and_b32 v81, v36, v49\n"
      "v_and_b32 v82, v36, v50\n"
      "v_and_b32 v83, v36, v51\n"
      "s_mul_i32 s50, s42, 96\n"
      "v_bfe_u32 v36, v5, 1, 3\n"
      "v_and_b32 v37, 1, v5\n"
      "v_lshl_add_u32 v36, v37, 3, v36\n"
      "v_mul_u32_u24 v36, 192, v36\n"
      "v_add_u32 v36, 46144, v36\n"
      "v_lshlrev_b32 v37, 5, v37\n"
      "v_and_b32 v38, 16, v5\n"
      "v_add_u32 v38, s50, v38\n"
      "v_and_b32 v39, 32, v5\n"
      "v_mul_u32_u24 v40, 192, v39\n"
      "v_add_u32 v40, v40, v36\n"
      "v_xad_u32 v84, v38, v37, v40\n"
      "v_add_u32 v41, 32, v38\n"
      "v_xad_u32 v85, v41, v37, v40\n"
      "v_add_u32 v41, v39, v38\n"
      "v_xad_u32 v86, v41, v37, v36\n"
      "s_mul_i32 s49, s41, 6144\n"
      "s_add_u32 s49, s49, 46144\n"
      "v_lshrrev_b32 v36, 3, v5\n"
      "v_and_b32 v38, 4, v5\n"
      "v_lshl_add_u32 v36, v38, 1, v36\n"
      "v_mul_u32_u24 v36, 192, v36\n"
      "v_lshlrev_b32 v37, 3, v5\n"
      "v_and_b32 v38, 8, v37\n"
      "v_add3_u32 v36, v36, v38, s49\n"
      "v_and_b32 v38, 32, v37\n"
      "v_and_b32 v37, 16, v37\n"
      "v_add_u32 v37, s50, v37\n"
      "v_xad_u32 v87, v37, v38, v36\n"
      "v_add_u32 v39, 32, v37\n"
      "v_xad_u32 v88, v39, v38, v36\n"
      "s_waitcnt lgkmcnt(0)\n"
      "s_mov_b32 s28, s22\n"
      "s_and_b32 s29, s23, 0xffff\n"
      "buffer_load_dword v66, s[28:31], s96 offen lds\n"
      "s_add_u32 m0, s92, 73824\n"
      "s_and_b32 s97, 0x3c2c24, s14\n"
      "s_xor_b32 s97, 0x120c0600, s97\n"
      "s_bfe_u32 s97, s97, s43\n"
      "s_mul_i32 s97, s97, 3072\n"
      "buffer_load_dwordx4 v8, s[28:31], s97 offen offset:0 lds\n"
      "buffer_load_dwordx4 v9, s[28:31], s97 offen offset:1024 lds\n"
      "buffer_load_dwordx4 v89, s[28:31], s97 offen offset:2048 lds\n"
      "s_add_u32 m0, s92, 86112\n"
      "s_and_b32 s97, 0x2c240000, s14\n"
      "s_xor_b32 s97, 0x2a241e18, s97\n"
      "s_bfe_u32 s97, s97, s43\n"
      "s_mul_i32 s97, s97, 3072\n"
      "buffer_load_dwordx4 v8, s[28:31], s97 offen offset:0 lds\n"
      "buffer_load_dwordx4 v9, s[28:31], s97 offen offset:1024 lds\n"
      "buffer_load_dwordx4 v89, s[28:31], s97 offen offset:2048 lds\n"
      "s_and_b32 s53, %1, 7\n"
      "s_lshl_b32 s53, s53, 6\n"
      "s_bfe_u32 s50, %1, 0x10006\n"
      "s_add_u32 s53, s53, s50\n"
      "s_lshl1_add_u32 s50, s14, 32\n"
      "s_add_u32 s53, s53, s50\n"
      "s_mul_i32 s13, s53, 98304\n"
      "s_or_b32 s12, s14, 1\n"
      "s_mul_i32 s12, s12, 196608\n"
      "s_lshl_b32 s50, s12, 1\n"
      "s_sub_u32 s51, s13, s50\n"
      "s_mul_i32 s99, s99, 12288\n"
      "s_sub_u32 s50, s99, 2432\n"
      "s_ashr_i32 s49, s50, 31\n"
      "s_add_u32 s32, s20, s50\n"
      "s_addc_u32 s33, s21, s49\n"
      "s_and_b32 s33, s33, 0xffff\n"
      "s_sub_u32 s34, 50331648, s50\n"
      "s_mov_b32 s35, 0x20000\n"
      "s_add_u32 s46, s92, 110720\n"
      "v_lshrrev_b32 v36, 1, v5\n"
      "v_and_b32 v37, 4, v36\n"
      "v_and_or_b32 v37, v5, 2, v37\n"
      "v_mul_u32_u24 v37, 1568, v37\n"
      "v_xor_b32 v36, v36, v5\n"
      "v_and_b32 v36, 4, v36\n"
      "v_mad_u32_u24 v37, v36, 48, v37\n"
      "s_mul_i32 s50, s42, 1568\n"
      "s_add_u32 s50, s50, 110720\n"
      "v_and_b32 v36, 16, v5\n"
      "v_lshl_add_u32 v37, v36, 1, v37\n"
      "v_and_b32 v36, 16, v8\n"
      "v_add3_u32 v37, v37, v36, s50\n"
      "v_and_b32 v36, 32, v5\n"
      "v_mad_u32_u24 v38, v36, 24, v37\n"
      "v_mad_u32_u24 v39, v36, 25, v37\n"
      "s_mul_i32 s17, s42, 0xcc00cc00\n"
      "s_mov_b32 s16, 0\n"
      "v_mov_b32 v36, 768\n"
      "v_mov_b32 v37, 736\n"
      "s_mov_b64 exec, 0\n"
      "s_cmp_eq_u32 s40, 0\n"
      "s_cbranch_scc1 Lconv_p1\n"
      "s_cmp_eq_u32 s40, 1\n"
      "s_cbranch_scc1 Lconv_p2\n"
      "s_cmp_eq_u32 s40, 2\n"
      "s_cbranch_scc1 Lconv_pa0\n"
      "s_cmp_eq_u32 s40, 3\n"
      "s_cbranch_scc1 Lconv_pa1\n"
      "Lconv_r0:\n"
      "s_mov_b64 exec, -1\n"
      "s_add_u32 m0, s92, 98400\n"
      "s_and_b32 s97, 0x3c2c243c, s14\n"
      "s_xor_b32 s97, 0xd070130, s97\n"
      "s_bfe_u32 s97, s97, s43\n"
      "s_mul_i32 s97, s97, 3072\n"
      "buffer_load_dwordx4 v8, s[28:31], s97 offen offset:0 lds\n"
      "buffer_load_dwordx4 v9, s[28:31], s97 offen offset:1024 lds\n"
      "buffer_load_dwordx4 v89, s[28:31], s97 offen offset:2048 lds\n"
      "v_cndmask_b32 v36, v36, v37, s[16:17]\n"
      "v_add_u32 v39, v39, v36\n"
      "s_cmp_eq_u32 s41, 0\n"
      "s_cselect_b64 vcc, -1, 0\n"
      "v_add_u32 v40, 384, v38\n"
      "v_add_u32 v41, 384, v39\n"
      "v_cndmask_b32 v2, v40, v39, vcc\n"
      "v_cndmask_b32 v3, v39, v40, vcc\n"
      "v_cndmask_b32 v4, v41, v38, vcc\n"
      "v_and_b32 v36, 3, v5\n"
      "v_mul_u32_u24 v36, 18464, v36\n"
      "v_bfe_u32 v37, v5, 2, 2\n"
      "v_lshrrev_b32 v38, 1, v37\n"
      "v_xor_b32 v37, v37, v38\n"
      "v_lshl_add_u32 v36, v37, 6, v36\n"
      "s_mul_i32 s50, s41, 2304\n"
      "v_and_b32 v37, 48, v5\n"
      "v_add3_u32 v6, v36, v37, s50\n"
      "v_add_u32 v7, 9216, v6\n"
      "s_mov_b32 s16, 0xc0003\n"
      "s_mov_b32 s17, 0xc0003\n"
      "v_cndmask_b32 v48, 0, v52, s[16:17]\n"
      "s_lshl_b64 s[16:17], s[16:17], 4\n"
      "v_cndmask_b32 v49, 0, v52, s[16:17]\n"
      "s_lshl_b64 s[16:17], s[16:17], 4\n"
      "v_cndmask_b32 v50, 0, v52, s[16:17]\n"
      "s_lshl_b64 s[16:17], s[16:17], 4\n"
      "v_cndmask_b32 v51, 0, v52, s[16:17]\n"
      "v_and_b32 v36, 0xff00ff00, v53\n"
      "v_xor_b32 v36, 0xc080c080, v36\n"
      "v_and_b32 v38, v36, v48\n"
      "v_and_b32 v39, v36, v49\n"
      "v_and_b32 v40, v36, v50\n"
      "v_and_b32 v41, v36, v51\n"
      "v_accvgpr_write_b32 a72, v38\n"
      "v_accvgpr_write_b32 a73, v39\n"
      "v_accvgpr_write_b32 a74, v40\n"
      "v_accvgpr_write_b32 a75, v41\n"
      "v_and_b32 v36, 0xff00ff00, v53\n"
      "v_xor_b32 v36, 0xc000c000, v36\n"
      "v_and_b32 v38, v36, v48\n"
      "v_and_b32 v39, v36, v49\n"
      "v_and_b32 v40, v36, v50\n"
      "v_and_b32 v41, v36, v51\n"
      "v_accvgpr_write_b32 a76, v38\n"
      "v_accvgpr_write_b32 a77, v39\n"
      "v_accvgpr_write_b32 a78, v40\n"
      "v_accvgpr_write_b32 a79, v41\n"
      "v_xor_b32 v38, 0x80008000, v38\n"
      "v_accvgpr_write_b32 a252, v38\n"
      "v_xor_b32 v39, 0x80008000, v39\n"
      "v_accvgpr_write_b32 a253, v39\n"
      "v_xor_b32 v40, 0x80008000, v40\n"
      "v_accvgpr_write_b32 a254, v40\n"
      "v_xor_b32 v41, 0x80008000, v41\n"
      "v_accvgpr_write_b32 a255, v41\n"
      "v_and_b32 v36, 0x81008100, v53\n"
      "v_xor_b32 v36, 0x3f803f80, v36\n"
      "v_and_b32 v38, v36, v48\n"
      "v_and_b32 v39, v36, v49\n"
      "v_and_b32 v40, v36, v50\n"
      "v_and_b32 v41, v36, v51\n"
      "v_accvgpr_write_b32 a80, v38\n"
      "v_accvgpr_write_b32 a81, v39\n"
      "v_accvgpr_write_b32 a82, v40\n"
      "v_accvgpr_write_b32 a83, v41\n"
      "v_lshlrev_b32 v36, 5, v5\n"
      "v_and_b32 v36, 0x180, v36\n"
      "v_lshlrev_b32 v37, 3, v5\n"
      "v_and_b32 v37, 16, v37\n"
      "v_sub_u32 v38, 0x3eb7, v36\n"
      "v_add_u32 v39, 0x3d37, v36\n"
      "v_mov_b32 v40, 0x3f80\n"
      "s_mov_b64 vcc, 0xffff\n"
      "s_mov_b32 s16, 0xffff0000\n"
      "s_mov_b32 s17, 0xf0f0\n"
      "v_xor_b32 v41, 0x8000, v38\n"
      "v_cndmask_b32 v38, v38, v41, s[16:17]\n"
      "v_cndmask_b32 v38, v38, v40, vcc\n"
      "v_lshlrev_b32 v38, v37, v38\n"
      "v_xor_b32 v41, 0x8000, v39\n"
      "s_xor_b32 s17, s17, 0xffff\n"
      "v_cndmask_b32 v39, v39, v41, s[16:17]\n"
      "v_cndmask_b32 v39, v39, v40, vcc\n"
      "v_lshlrev_b32 v39, v37, v39\n"
      "s_mov_b32 s18, 0x55550005\n"
      "s_mov_b32 s19, 0x5555\n"
      "v_cndmask_b32 v42, 0, v38, s[18:19]\n"
      "s_lshl_b64 s[16:17], s[18:19], 1\n"
      "v_cndmask_b32 v43, 0, v38, s[16:17]\n"
      "v_accvgpr_write_b32 a84, v42\n"
      "v_accvgpr_write_b32 a85, v43\n"
      "s_mov_b32 s18, 0x55555000\n"
      "v_cndmask_b32 v42, 0, v39, s[18:19]\n"
      "s_lshl_b64 s[16:17], s[18:19], 1\n"
      "v_cndmask_b32 v43, 0, v39, s[16:17]\n"
      "v_accvgpr_write_b32 a86, v42\n"
      "v_accvgpr_write_b32 a87, v43\n"
      "v_lshrrev_b32 v36, 4, v5\n"
      "v_min_u32 v37, 2, v36\n"
      "v_lshlrev_b32 v37, 10, v37\n"
      "s_lshl_b32 s50, s41, 7\n"
      "s_lshl_b32 s49, s42, 8\n"
      "s_add_u32 s50, s50, s49\n"
      "s_add_u32 s50, s50, 98416\n"
      "v_and_b32 v38, 8, v5\n"
      "v_lshl_add_u32 v37, v38, 3, v37\n"
      "v_add_u32 v37, s50, v37\n"
      "s_movk_i32 s50, -12296\n"
      "s_mov_b32 s49, 43040\n"
      "v_mov_b32 v42, s49\n"
      "v_bfe_u32 v39, v5, 1, 2\n"
      "v_lshrrev_b32 v40, 2, v38\n"
      "v_add_u32 v39, v39, v40\n"
      "v_lshlrev_b32 v40, 5, v5\n"
      "v_and_b32 v40, 32, v40\n"
      "v_cmp_lt_u32 vcc, 2, v39\n"
      "v_mad_i32_i24 v39, v39, s50, v37\n"
      "v_add_u32 v39, v39, v40\n"
      "v_cndmask_b32 v41, 0, v42, vcc\n"
      "v_add_u32 v10, v39, v41\n"
      "v_add_u32 v11, 3072, v10\n"
      "v_bfe_u32 v39, v5, 2, 1\n"
      "v_lshrrev_b32 v40, 1, v38\n"
      "v_sub_u32 v39, v39, v40\n"
      "v_add_u32 v39, 4, v39\n"
      "v_and_b32 v40, 48, v8\n"
      "v_cmp_lt_u32 vcc, 2, v39\n"
      "v_mad_i32_i24 v39, v39, s50, v37\n"
      "v_add_u32 v39, v39, v40\n"
      "v_cndmask_b32 v41, 0, v42, vcc\n"
      "v_add_u32 v12, v39, v41\n"
      "v_add_u32 v13, 3072, v12\n"
      "v_mul_u32_u24 v36, 384, v36\n"
      "v_and_b32 v37, 15, v5\n"
      "v_lshl_add_u32 v39, v37, 3, v36\n"
      "v_add_u32 v15, 128, v39\n"
      "v_add_lshl_u32 v37, v37, v38, 4\n"
      "v_add_u32 v14, v37, v36\n"
      "s_add_u32 s50, s99, s92\n"
      "s_add_u32 s36, s24, s50\n"
      "s_addc_u32 s37, s25, 0\n"
      "s_and_b32 s37, s37, 0xffff\n"
      "s_sub_u32 s38, 50331648, s50\n"
      "s_mov_b32 s39, 0x20000\n"
      "s_mul_i32 s48, s12, 17\n"
      "s_mul_i32 s45, s42, 6152\n"
      "s_lshl_b32 s50, s41, 9\n"
      "s_add_u32 s45, s45, s50\n"
      "s_add_u32 s45, s45, 41056\n"
      "s_mov_b32 s98, 0\n"
      "s_mov_b32 s91, 26560\n"
      "s_mul_i32 s18, s42, 18464\n"
      "s_mul_i32 s50, s41, 2304\n"
      "s_add_u32 s18, s18, s50\n"
      "s_lshl1_add_u32 s19, s18, 9216\n"
      "s_add_u32 s58, s46, 26560\n"
      "s_lshl1_add_u32 s93, s46, 26560\n"
      "s_mul_i32 s59, s42, -13280\n"
      "s_add_u32 s59, s59, 136256\n"
      "s_lshl1_add_u32 s94, s59, 26560\n"
      "s_mul_i32 s50, s42, 26560\n"
      "s_add_u32 s59, s59, s50\n"
      "s_mul_i32 s15, s42, s12\n"
      "s_sub_u32 s95, s12, s15\n"
      "s_mov_b32 s52, 2.0\n"
      "s_cmp_lt_u32 s40, 3\n"
      "s_cselect_b32 s97, 27680, 0\n"
      "s_add_u32 s97, s97, s92\n"
      "s_add_u32 m0, s97, 36928\n"
      "s_and_b32 s97, 0x24000000, s14\n"
      "s_xor_b32 s97, 0x251f1913, s97\n"
      "s_bfe_u32 s97, s97, s43\n"
      "s_mul_i32 s97, s97, 3072\n"
      "buffer_load_dwordx4 v8, s[28:31], s97 offen offset:0 lds\n"
      "buffer_load_dwordx4 v9, s[28:31], s97 offen offset:1024 lds\n"
      "buffer_load_dwordx4 v89, s[28:31], s97 offen offset:2048 lds\n"
      "s_cmp_lt_u32 s40, 2\n"
      "s_cselect_b32 s97, 27680, 0\n"
      "s_add_u32 s97, s97, s92\n"
      "s_add_u32 m0, s97, 21536\n"
      "s_and_b32 s97, 0x24243c2c, s14\n"
      "s_xor_b32 s97, 0x802312b, s97\n"
      "s_bfe_u32 s97, s97, s43\n"
      "s_mul_i32 s97, s97, 3072\n"
      "buffer_load_dwordx4 v8, s[28:31], s97 offen offset:0 lds\n"
      "buffer_load_dwordx4 v9, s[28:31], s97 offen offset:1024 lds\n"
      "buffer_load_dwordx4 v89, s[28:31], s97 offen offset:2048 lds\n"
      "s_cmp_lt_u32 s40, 1\n"
      "s_cselect_b32 s97, 27680, 0\n"
      "s_add_u32 s97, s97, s92\n"
      "s_add_u32 m0, s97, 6144\n"
      "s_and_b32 s97, 0x3c, s14\n"
      "s_xor_b32 s97, 0x201a140e, s97\n"
      "s_bfe_u32 s97, s97, s43\n"
      "s_mul_i32 s97, s97, 3072\n"
      "buffer_load_dwordx4 v8, s[28:31], s97 offen offset:0 lds\n"
      "buffer_load_dwordx4 v9, s[28:31], s97 offen offset:1024 lds\n"
      "buffer_load_dwordx4 v89, s[28:31], s97 offen offset:2048 lds\n"
      "s_sub_u32 vcc_lo, 0, s41\n"
      "s_mov_b32 vcc_hi, 0\n"
      ".p2align 3\n"
      "s_waitcnt vmcnt(12)\n"
      "s_barrier\n"
      "s_cmp_lt_u32 s40, 3\n"
      "s_cselect_b32 s97, -9248, 0\n"
      "ds_read_b128 a[0:3], v86 offset:30752\n"
      "ds_read_b128 a[4:7], v84 offset:27680\n"
      "ds_read_b128 a[8:11], v85 offset:27680\n"
      "ds_read_b128 a[12:15], v86 offset:30816\n"
      "s_add_u32 s97, s97, s92\n"
      "s_add_u32 m0, s97, 9248\n"
      "s_and_b32 s97, 0x243c2424, s14\n"
      "s_xor_b32 s97, 0x3322c26, s97\n"
      "s_bfe_u32 s97, s97, s43\n"
      "s_mul_i32 s97, s97, 3072\n"
      "s_waitcnt lgkmcnt(3)\n"
      "v_mfma_f32_16x16x32_bf16 v[20:23], a[0:3], v[76:79], 0\n"
      "s_waitcnt lgkmcnt(2)\n"
      "ds_read_b128 a[16:19], v84 offset:27744\n"
      "ds_read_b128 a[20:23], v86 offset:39968\n"
      "buffer_load_dwordx4 v8, s[28:31], s97 offen offset:0 lds\n"
      "v_mfma_f32_16x16x32_bf16 v[24:27], a[4:7], v[68:71], v[20:23]\n"
      "ds_read_b128 a[24:27], v84 offset:36896\n"
      "ds_read_b128 a[28:31], v85 offset:36896\n"
      "v_mfma_f32_16x16x32_bf16 v[20:23], a[4:7], v[72:75], v[20:23]\n"
      "ds_read_b64_tr_b16 a[88:89], v87 offset:27680\n"
      "ds_read_b64_tr_b16 a[92:93], v88 offset:27680\n"
      "buffer_load_dwordx4 v9, s[28:31], s97 offen offset:1024 lds\n"
      "v_mfma_f32_16x16x32_bf16 v[28:31], a[0:3], v[80:83], 0\n"
      "s_waitcnt lgkmcnt(7)\n"
      "ds_read_b64_tr_b16 a[96:97], v87 offset:27744\n"
      "ds_read_b64_tr_b16 a[124:125], v87 offset:36896\n"
      "v_mfma_f32_16x16x32_bf16 v[32:35], a[8:11], v[68:71], v[28:31]\n"
      "ds_read_b64_tr_b16 a[128:129], v88 offset:36896\n"
      "ds_read_b128 a[32:35], v86 offset:40032\n"
      "v_mfma_f32_16x16x32_bf16 v[28:31], a[8:11], v[72:75], v[28:31]\n"
      "s_waitcnt lgkmcnt(10)\n"
      "ds_read_b128 a[0:3], v84 offset:36960\n"
      "ds_read_b64_tr_b16 a[132:133], v87 offset:36960\n"
      "buffer_load_dwordx4 v89, s[28:31], s97 offen offset:2048 lds\n"
      "v_mfma_f32_16x16x32_bf16 v[54:57], a[12:15], v[76:79], 0\n"
      "s_waitcnt lgkmcnt(10)\n"
      "s_cmp_lt_u32 s40, 2\n"
      "s_cselect_b32 s97, -9248, 0\n"
      "s_add_u32 s97, s97, s92\n"
      "s_add_u32 m0, s97, 30784\n"
      "s_and_b32 s97, 0x3c24, s14\n"
      "s_xor_b32 s97, 0x1b150f09, s97\n"
      "s_bfe_u32 s97, s97, s43\n"
      "s_mul_i32 s97, s97, 3072\n"
      "v_cvt_pk_bf16_f32 v96, v20, v21\n"
      "v_cvt_pk_bf16_f32 v97, v22, v23\n"
      "v_mfma_f32_16x16x32_bf16 v[58:61], a[16:19], v[68:71], v[54:57]\n"
      "v_cvt_pk_bf16_f32 v92, v24, v25\n"
      "v_cvt_pk_bf16_f32 v93, v26, v27\n"
      "v_mfma_f32_16x16x32_bf16 v[54:57], a[16:19], v[72:75], v[54:57]\n"
      "buffer_load_dwordx4 v8, s[28:31], s97 offen offset:0 lds\n"
      "v_cvt_pk_bf16_f32 v104, v28, v29\n"
      "v_cvt_pk_bf16_f32 v105, v30, v31\n"
      "v_mfma_f32_16x16x32_bf16 v[62:65], a[20:23], v[76:79], 0\n"
      "s_waitcnt lgkmcnt(4)\n"
      "v_cvt_pk_bf16_f32 v100, v32, v33\n"
      "v_cvt_pk_bf16_f32 v101, v34, v35\n"
      "v_mfma_f32_16x16x32_bf16 v[20:23], a[24:27], v[68:71], v[62:65]\n"
      "v_cvt_pk_bf16_f32 v108, v54, v55\n"
      "v_cvt_pk_bf16_f32 v109, v56, v57\n"
      "v_mfma_f32_16x16x32_bf16 v[62:65], a[24:27], v[72:75], v[62:65]\n"
      "s_waitcnt lgkmcnt(0)\n"
      "s_waitcnt vmcnt(13)\n"
      "s_cbranch_vccnz Lconv_h1\n"
      "s_barrier\n"
      "Lconv_h1:\n"
      "buffer_load_dwordx4 v9, s[28:31], s97 offen offset:1024 lds\n"
      "v_mfma_f32_16x16x32_bf16 v[24:27], a[20:23], v[80:83], 0\n"
      "v_cvt_pk_bf16_f32 v58, v58, v59\n"
      "v_cvt_pk_bf16_f32 v60, v60, v61\n"
      "v_mfma_f32_16x16x32_bf16 v[28:31], a[28:31], v[68:71], v[24:27]\n"
      "s_cbranch_vccz Lconv_h2\n"
      "s_barrier\n"
      "Lconv_h2:\n"
      "ds_read_b128 a[4:7], v86 offset:49184\n"
      "ds_read_b128 a[8:11], v84 offset:46112\n"
      "ds_read_b128 a[12:15], v85 offset:46112\n"
      "ds_read_b128 a[16:19], v86 offset:49248\n"
      "v_mfma_f32_16x16x32_bf16 v[24:27], a[28:31], v[72:75], v[24:27]\n"
      "ds_read_b128 a[20:23], v84 offset:46176\n"
      "ds_read_b128 a[24:27], v86 offset:58400\n"
      "buffer_load_dwordx4 v89, s[28:31], s97 offen offset:2048 lds\n"
      "v_accvgpr_write_b32 a196, v58\n"
      "v_accvgpr_write_b32 a197, v60\n"
      "v_mfma_f32_16x16x32_bf16 v[32:35], a[32:35], v[76:79], 0\n"
      "s_cmp_lt_u32 s40, 1\n"
      "s_cselect_b32 s97, -9248, 0\n"
      "ds_read_b128 a[28:31], v84 offset:55328\n"
      "ds_read_b128 a[32:35], v85 offset:55328\n"
      "s_add_u32 s97, s97, s92\n"
      "s_add_u32 m0, s97, 52320\n"
      "s_and_b32 s97, 0x3c242400, s14\n"
      "s_xor_b32 s97, 0x332d2721, s97\n"
      "s_bfe_u32 s97, s97, s43\n"
      "s_mul_i32 s97, s97, 3072\n"
      "v_cvt_pk_bf16_f32 v116, v62, v63\n"
      "v_cvt_pk_bf16_f32 v117, v64, v65\n"
      "v_mfma_f32_16x16x32_bf16 v[54:57], a[0:3], v[68:71], v[32:35]\n"
      "ds_read_b64_tr_b16 a[160:161], v87 offset:46112\n"
      "ds_read_b64_tr_b16 a[164:165], v88 offset:46112\n"
      "buffer_load_dwordx4 v8, s[28:31], s97 offen offset:0 lds\n"
      "v_cvt_pk_bf16_f32 v112, v20, v21\n"
      "v_cvt_pk_bf16_f32 v113, v22, v23\n"
      "v_mfma_f32_16x16x32_bf16 v[32:35], a[0:3], v[72:75], v[32:35]\n"
      "s_waitcnt lgkmcnt(9)\n"
      "ds_read_b64_tr_b16 a[168:169], v87 offset:46176\n"
      "ds_read_b64_tr_b16 a[90:91], v87 offset:55328\n"
      "v_cvt_pk_bf16_f32 v120, v24, v25\n"
      "v_cvt_pk_bf16_f32 v121, v26, v27\n"
      "v_mfma_f32_16x16x32_bf16 v[58:61], a[4:7], v[76:79], 0\n"
      "s_waitcnt lgkmcnt(10)\n"
      "ds_read_b64_tr_b16 a[94:95], v88 offset:55328\n"
      "ds_read_b128 a[0:3], v86 offset:58464\n"
      "v_mfma_f32_16x16x32_bf16 v[62:65], a[8:11], v[68:71], v[58:61]\n"
      "ds_read_b64_tr_b16 a[98:99], v87 offset:55392\n"
      "buffer_load_dwordx4 v9, s[28:31], s97 offen offset:1024 lds\n"
      "v_cvt_pk_bf16_f32 v28, v28, v29\n"
      "v_cvt_pk_bf16_f32 v30, v30, v31\n"
      "v_accvgpr_write_b32 a204, v28\n"
      "v_mfma_f32_16x16x32_bf16 v[58:61], a[8:11], v[72:75], v[58:61]\n"
      "v_accvgpr_write_b32 a205, v30\n"
      "v_cvt_pk_bf16_f32 v128, v32, v33\n"
      "v_cvt_pk_bf16_f32 v129, v34, v35\n"
      "v_mfma_f32_16x16x32_bf16 v[20:23], a[4:7], v[80:83], 0\n"
      "s_waitcnt lgkmcnt(7)\n"
      "ds_read_b128 a[4:7], v84 offset:55392\n"
      "v_cvt_pk_bf16_f32 v124, v54, v55\n"
      "v_mfma_f32_16x16x32_bf16 v[24:27], a[12:15], v[68:71], v[20:23]\n"
      "buffer_load_dwordx4 v89, s[28:31], s97 offen offset:2048 lds\n"
      "v_cvt_pk_bf16_f32 v125, v56, v57\n"
      "v_mfma_f32_16x16x32_bf16 v[20:23], a[12:15], v[72:75], v[20:23]\n"
      "s_add_u32 m0, s46, 13280\n"
      "s_add_u32 s51, s51, s12\n"
      "v_cvt_pk_bf16_f32 v132, v58, v59\n"
      "v_cvt_pk_bf16_f32 v133, v60, v61\n"
      "v_mfma_f32_16x16x32_bf16 v[28:31], a[16:19], v[76:79], 0\n"
      "v_cvt_pk_bf16_f32 v62, v62, v63\n"
      "v_cvt_pk_bf16_f32 v64, v64, v65\n"
      "v_mfma_f32_16x16x32_bf16 v[32:35], a[20:23], v[68:71], v[28:31]\n"
      "buffer_load_dwordx4 v16, s[32:35], s51 offen lds\n"
      "v_accvgpr_write_b32 a212, v62\n"
      "v_accvgpr_write_b32 a213, v64\n"
      "v_mfma_f32_16x16x32_bf16 v[28:31], a[20:23], v[72:75], v[28:31]\n"
      "s_add_u32 m0, s46, 13280\n"
      "v_cvt_pk_bf16_f32 v140, v20, v21\n"
      "v_cvt_pk_bf16_f32 v141, v22, v23\n"
      "v_mfma_f32_16x16x32_bf16 v[54:57], a[24:27], v[76:79], 0\n"
      "buffer_load_dwordx4 v17, s[32:35], s51 offen offset:1024 lds\n"
      "v_cvt_pk_bf16_f32 v136, v24, v25\n"
      "v_mfma_f32_16x16x32_bf16 v[58:61], a[28:31], v[68:71], v[54:57]\n"
      "s_add_u32 m0, s46, 13280\n"
      "s_add_u32 s96, s51, s15\n"
      "buffer_load_dwordx4 v18, s[32:35], s51 offen offset:2048 lds\n"
      "v_cvt_pk_bf16_f32 v137, v26, v27\n"
      "v_mfma_f32_16x16x32_bf16 v[54:57], a[28:31], v[72:75], v[54:57]\n"
      "s_waitcnt lgkmcnt(0)\n"
      "s_waitcnt vmcnt(12)\n"
      "s_cbranch_vccnz Lconv_h3\n"
      "s_barrier\n"
      "Lconv_h3:\n"
      "s_mov_b32 m0, s59\n"
      "v_cvt_pk_bf16_f32 v144, v28, v29\n"
      "v_cvt_pk_bf16_f32 v145, v30, v31\n"
      "v_mfma_f32_16x16x32_bf16 v[62:65], a[24:27], v[80:83], 0\n"
      "buffer_load_dwordx4 v19, s[32:35], s96 offen lds\n"
      "v_cvt_pk_bf16_f32 v32, v32, v33\n"
      "v_mfma_f32_16x16x32_bf16 v[20:23], a[32:35], v[68:71], v[62:65]\n"
      "s_cbranch_vccz Lconv_h4\n"
      "s_barrier\n"
      "Lconv_h4:\n"
      "s_add_u32 m0, s46, 26560\n"
      "s_add_u32 s51, s51, s12\n"
      "ds_read_b128 a[8:11], v86 offset:21536\n"
      "ds_read_b128 a[12:15], v84 offset:18464\n"
      "ds_read_b128 a[16:19], v85 offset:18464\n"
      "ds_read_b128 a[20:23], v86 offset:21600\n"
      "buffer_load_dwordx4 v16, s[32:35], s51 offen lds\n"
      "v_mfma_f32_16x16x32_bf16 v[62:65], a[32:35], v[72:75], v[62:65]\n"
      "s_add_u32 m0, s46, 26560\n"
      "ds_read_b128 a[24:27], v84 offset:18528\n"
      "ds_read_b128 a[28:31], v86 offset:3072\n"
      "buffer_load_dwordx4 v17, s[32:35], s51 offen offset:1024 lds\n"
      "v_cvt_pk_bf16_f32 v34, v34, v35\n"
      "v_accvgpr_write_b32 a216, v32\n"
      "v_mfma_f32_16x16x32_bf16 v[24:27], a[0:3], v[76:79], 0\n"
      "s_add_u32 m0, s46, 26560\n"
      "ds_read_b128 a[32:35], v84 offset:0\n"
      "ds_read_b128 a[0:3], v85 offset:0\n"
      "buffer_load_dwordx4 v18, s[32:35], s51 offen offset:2048 lds\n"
      "v_accvgpr_write_b32 a217, v34\n"
      "v_cvt_pk_bf16_f32 v98, v54, v55\n"
      "v_mfma_f32_16x16x32_bf16 v[28:31], a[4:7], v[68:71], v[24:27]\n"
      "s_add_u32 m0, s92, 73824\n"
      "s_and_b32 s97, 0x24242c, s14\n"
      "ds_read_b64_tr_b16 a[126:127], v87 offset:18464\n"
      "ds_read_b64_tr_b16 a[130:131], v88 offset:18464\n"
      "s_xor_b32 s97, 0x16100a04, s97\n"
      "s_bfe_u32 s97, s97, s43\n"
      "s_mul_i32 s97, s97, 3072\n"
      "buffer_load_dwordx4 v8, s[28:31], s97 offen offset:0 lds\n"
      "v_cvt_pk_bf16_f32 v99, v56, v57\n"
      "v_cvt_pk_bf16_f32 v94, v58, v59\n"
      "v_mfma_f32_16x16x32_bf16 v[24:27], a[4:7], v[72:75], v[24:27]\n"
      "s_waitcnt lgkmcnt(9)\n"
      "ds_read_b128 a[4:7], v86 offset:3136\n"
      "ds_read_b64_tr_b16 a[134:135], v87 offset:18528\n"
      "v_cvt_pk_bf16_f32 v95, v60, v61\n"
      "v_cvt_pk_bf16_f32 v106, v62, v63\n"
      "v_cvt_pk_bf16_f32 v107, v64, v65\n"
      "v_mfma_f32_16x16x32_bf16 v[32:35], a[8:11], v[76:79], 0\n"
      "s_waitcnt lgkmcnt(10)\n"
      "ds_read_b64_tr_b16 a[162:163], v87 offset:0\n"
      "ds_read_b64_tr_b16 a[166:167], v88 offset:0\n"
      "v_cvt_pk_bf16_f32 v102, v20, v21\n"
      "v_cvt_pk_bf16_f32 v103, v22, v23\n"
      "v_cvt_pk_bf16_f32 v110, v24, v25\n"
      "v_mfma_f32_16x16x32_bf16 v[54:57], a[12:15], v[68:71], v[32:35]\n"
      "ds_read_b64_tr_b16 a[170:171], v87 offset:64\n"
      "v_cvt_pk_bf16_f32 v111, v26, v27\n"
      "v_cvt_pk_bf16_f32 v28, v28, v29\n"
      "v_cvt_pk_bf16_f32 v30, v30, v31\n"
      "v_mfma_f32_16x16x32_bf16 v[32:35], a[12:15], v[72:75], v[32:35]\n"
      "Lconv_p1:\n"
      "s_cbranch_execz Lconv_r0\n"
      "s_nop 0\n"
      "buffer_load_dwordx4 v9, s[28:31], s97 offen offset:1024 lds\n"
      "v_accvgpr_write_b32 a198, v28\n"
      "v_accvgpr_write_b32 a199, v30\n"
      "v_mfma_f32_16x16x32_bf16 v[58:61], a[8:11], v[80:83], 0\n"
      "s_waitcnt lgkmcnt(7)\n"
      "ds_read_b128 a[8:11], v84 offset:64\n"
      "v_add_u32 v86, -46144, v86\n"
      "v_mfma_f32_16x16x32_bf16 v[62:65], a[16:19], v[68:71], v[58:61]\n"
      "ds_read_b128 a[12:15], v86 offset:30752\n"
      "v_cvt_pk_bf16_f32 v118, v32, v33\n"
      "v_cvt_pk_bf16_f32 v119, v34, v35\n"
      "v_cvt_pk_bf16_f32 v114, v54, v55\n"
      "v_cvt_pk_bf16_f32 v115, v56, v57\n"
      "v_mfma_f32_16x16x32_bf16 v[58:61], a[16:19], v[72:75], v[58:61]\n"
      "v_add_u32 v84, -46144, v84\n"
      "v_add_u32 v85, -46144, v85\n"
      "v_mfma_f32_16x16x32_bf16 v[20:23], a[20:23], v[76:79], 0\n"
      "ds_read_b128 a[16:19], v84 offset:27680\n"
      "ds_read_b128 a[20:23], v85 offset:27680\n"
      "buffer_load_dwordx4 v89, s[28:31], s97 offen offset:2048 lds\n"
      "v_mfma_f32_16x16x32_bf16 v[24:27], a[24:27], v[68:71], v[20:23]\n"
      "s_add_u32 m0, s92, 86112\n"
      "s_and_b32 s97, 0x242c0000, s14\n"
      "s_xor_b32 s97, 0x2e28221c, s97\n"
      "s_bfe_u32 s97, s97, s43\n"
      "s_mul_i32 s97, s97, 3072\n"
      "v_cvt_pk_bf16_f32 v122, v58, v59\n"
      "v_cvt_pk_bf16_f32 v123, v60, v61\n"
      "v_mfma_f32_16x16x32_bf16 v[20:23], a[24:27], v[72:75], v[20:23]\n"
      "ds_read_b128 a[24:27], v86 offset:30816\n"
      "v_cvt_pk_bf16_f32 v62, v62, v63\n"
      "v_cvt_pk_bf16_f32 v64, v64, v65\n"
      "v_mfma_f32_16x16x32_bf16 v[28:31], a[28:31], v[76:79], 0\n"
      "v_accvgpr_write_b32 a206, v62\n"
      "v_accvgpr_write_b32 a207, v64\n"
      "v_mfma_f32_16x16x32_bf16 v[32:35], a[32:35], v[68:71], v[28:31]\n"
      "v_add_u32 v87, -46144, v87\n"
      "v_cvt_pk_bf16_f32 v130, v20, v21\n"
      "v_mfma_f32_16x16x32_bf16 v[28:31], a[32:35], v[72:75], v[28:31]\n"
      "ds_read_b64_tr_b16 a[100:101], v87 offset:27680\n"
      "ds_read_b64_tr_b16 a[108:109], v87 offset:27744\n"
      "buffer_load_dwordx4 v8, s[28:31], s97 offen offset:0 lds\n"
      "v_cvt_pk_bf16_f32 v131, v22, v23\n"
      "v_add_u32 v88, -46144, v88\n"
      "v_mfma_f32_16x16x32_bf16 v[54:57], a[28:31], v[80:83], 0\n"
      "ds_read_b64_tr_b16 a[104:105], v88 offset:27680\n"
      "ds_read_b128 a[28:31], v84 offset:27744\n"
      "v_mfma_f32_16x16x32_bf16 v[58:61], a[0:3], v[68:71], v[54:57]\n"
      "v_cvt_pk_bf16_f32 v126, v24, v25\n"
      "v_cvt_pk_bf16_f32 v127, v26, v27\n"
      "v_mfma_f32_16x16x32_bf16 v[54:57], a[0:3], v[72:75], v[54:57]\n"
      "s_waitcnt lgkmcnt(4)\n"
      "v_cvt_pk_bf16_f32 v134, v28, v29\n"
      "v_cvt_pk_bf16_f32 v135, v30, v31\n"
      "v_mfma_f32_16x16x32_bf16 v[62:65], a[4:7], v[76:79], 0\n"
      "buffer_load_dwordx4 v9, s[28:31], s97 offen offset:1024 lds\n"
      "v_cvt_pk_bf16_f32 v32, v32, v33\n"
      "v_mfma_f32_16x16x32_bf16 v[20:23], a[8:11], v[68:71], v[62:65]\n"
      "v_cvt_pk_bf16_f32 v34, v34, v35\n"
      "v_accvgpr_write_b32 a214, v32\n"
      "v_accvgpr_write_b32 a215, v34\n"
      "v_mfma_f32_16x16x32_bf16 v[62:65], a[8:11], v[72:75], v[62:65]\n"
      "v_cvt_pk_bf16_f32 v142, v54, v55\n"
      "v_cvt_pk_bf16_f32 v143, v56, v57\n"
      "v_mfma_f32_16x16x32_bf16 v[24:27], a[12:15], v[76:79], 0\n"
      "v_cvt_pk_bf16_f32 v138, v58, v59\n"
      "v_cvt_pk_bf16_f32 v139, v60, v61\n"
      "v_mfma_f32_16x16x32_bf16 v[28:31], a[16:19], v[68:71], v[24:27]\n"
      "v_cvt_pk_bf16_f32 v20, v20, v21\n"
      "v_cvt_pk_bf16_f32 v22, v22, v23\n"
      "v_mfma_f32_16x16x32_bf16 v[24:27], a[16:19], v[72:75], v[24:27]\n"
      "s_waitcnt lgkmcnt(0)\n"
      "s_waitcnt vmcnt(12)\n"
      "s_cbranch_vccnz Lconv_h5\n"
      "s_barrier\n"
      "Lconv_h5:\n"
      "buffer_load_dwordx4 v89, s[28:31], s97 offen offset:2048 lds\n"
      "v_mfma_f32_16x16x32_bf16 v[32:35], a[12:15], v[80:83], 0\n"
      "s_add_u32 m0, s92, 98400\n"
      "s_and_b32 s97, 0x24242c24, s14\n"
      "s_xor_b32 s97, 0x110b0534, s97\n"
      "s_bfe_u32 s97, s97, s43\n"
      "s_mul_i32 s97, s97, 3072\n"
      "v_cvt_pk_bf16_f32 v146, v62, v63\n"
      "v_cvt_pk_bf16_f32 v147, v64, v65\n"
      "v_mfma_f32_16x16x32_bf16 v[54:57], a[20:23], v[68:71], v[32:35]\n"
      "s_cbranch_vccz Lconv_h6\n"
      "s_barrier\n"
      "Lconv_h6:\n"
      "ds_read_b128 a[32:35], v86 offset:12288\n"
      "ds_read_b128 a[0:3], v84 offset:9216\n"
      "ds_read_b128 a[4:7], v85 offset:9216\n"
      "ds_read_b128 a[8:11], v86 offset:12352\n"
      "v_mfma_f32_16x16x32_bf16 v[32:35], a[20:23], v[72:75], v[32:35]\n"
      "ds_read_b128 a[12:15], v84 offset:9280\n"
      "ds_read_b128 a[16:19], v86 offset:3072\n"
      "v_accvgpr_write_b32 a218, v20\n"
      "v_accvgpr_write_b32 a219, v22\n"
      "v_cvt_pk_bf16_f32 v152, v24, v25\n"
      "v_mfma_f32_16x16x32_bf16 v[58:61], a[24:27], v[76:79], 0\n"
      "ds_read_b128 a[20:23], v84 offset:0\n"
      "ds_read_b128 a[24:27], v85 offset:0\n"
      "buffer_load_dwordx4 v8, s[28:31], s97 offen offset:0 lds\n"
      "v_cvt_pk_bf16_f32 v153, v26, v27\n"
      "v_mfma_f32_16x16x32_bf16 v[62:65], a[28:31], v[68:71], v[58:61]\n"
      "ds_read_b64_tr_b16 a[136:137], v87 offset:9216\n"
      "ds_read_b64_tr_b16 a[140:141], v88 offset:9216\n"
      "v_cvt_pk_bf16_f32 v148, v28, v29\n"
      "v_cvt_pk_bf16_f32 v149, v30, v31\n"
      "v_mfma_f32_16x16x32_bf16 v[58:61], a[28:31], v[72:75], v[58:61]\n"
      "s_waitcnt lgkmcnt(9)\n"
      "ds_read_b128 a[28:31], v86 offset:3136\n"
      "ds_read_b64_tr_b16 a[144:145], v87 offset:9280\n"
      "v_cvt_pk_bf16_f32 v156, v32, v33\n"
      "v_cvt_pk_bf16_f32 v157, v34, v35\n"
      "v_mfma_f32_16x16x32_bf16 v[20:23], a[32:35], v[76:79], 0\n"
      "s_waitcnt lgkmcnt(10)\n"
      "ds_read_b64_tr_b16 a[172:173], v87 offset:0\n"
      "ds_read_b64_tr_b16 a[176:177], v88 offset:0\n"
      "v_mfma_f32_16x16x32_bf16 v[24:27], a[0:3], v[68:71], v[20:23]\n"
      "ds_read_b64_tr_b16 a[180:181], v87 offset:64\n"
      "ds_read_b64_tr_b16 a[102:103], v87 offset:18464\n"
      "v_cvt_pk_bf16_f32 v54, v54, v55\n"
      "v_cvt_pk_bf16_f32 v56, v56, v57\n"
      "v_accvgpr_write_b32 a200, v54\n"
      "v_mfma_f32_16x16x32_bf16 v[20:23], a[0:3], v[72:75], v[20:23]\n"
      "ds_read_b128 a[0:3], v86 offset:21536\n"
      "ds_read_b64_tr_b16 a[106:107], v88 offset:18464\n"
      "buffer_load_dwordx4 v9, s[28:31], s97 offen offset:1024 lds\n"
      "v_accvgpr_write_b32 a201, v56\n"
      "v_cvt_pk_bf16_f32 v164, v58, v59\n"
      "v_mfma_f32_16x16x32_bf16 v[28:31], a[32:35], v[80:83], 0\n"
      "s_waitcnt lgkmcnt(10)\n"
      "ds_read_b128 a[32:35], v84 offset:64\n"
      "ds_read_b64_tr_b16 a[110:111], v87 offset:18528\n"
      "v_cvt_pk_bf16_f32 v165, v60, v61\n"
      "v_mfma_f32_16x16x32_bf16 v[32:35], a[4:7], v[68:71], v[28:31]\n"
      "ds_read_b64_tr_b16 a[138:139], v87 offset:36928\n"
      "ds_read_b64_tr_b16 a[142:143], v88 offset:36928\n"
      "v_cvt_pk_bf16_f32 v160, v62, v63\n"
      "v_cvt_pk_bf16_f32 v161, v64, v65\n"
      "v_mfma_f32_16x16x32_bf16 v[28:31], a[4:7], v[72:75], v[28:31]\n"
      "ds_read_b128 a[4:7], v84 offset:18464\n"
      "ds_read_b64_tr_b16 a[146:147], v87 offset:36992\n"
      "v_cvt_pk_bf16_f32 v168, v20, v21\n"
      "v_cvt_pk_bf16_f32 v169, v22, v23\n"
      "v_mfma_f32_16x16x32_bf16 v[54:57], a[8:11], v[76:79], 0\n"
      "ds_read_b128 a[8:11], v85 offset:18464\n"
      "ds_read_b64_tr_b16 a[174:175], v87 offset:55392\n"
      "buffer_load_dwordx4 v89, s[28:31], s97 offen offset:2048 lds\n"
      "v_mfma_f32_16x16x32_bf16 v[58:61], a[12:15], v[68:71], v[54:57]\n"
      "s_cmp_lt_u32 s40, 3\n"
      "s_cselect_b32 s97, 27680, 0\n"
      "ds_read_b64_tr_b16 a[178:179], v88 offset:55392\n"
      "ds_read_b64_tr_b16 a[182:183], v87 offset:55456\n"
      "s_add_u32 s97, s97, s92\n"
      "s_add_u32 m0, s97, 36928\n"
      "s_and_b32 s97, 0x2c000000, s14\n"
      "s_xor_b32 s97, 0x29231d17, s97\n"
      "s_bfe_u32 s97, s97, s43\n"
      "s_mul_i32 s97, s97, 3072\n"
      "v_cvt_pk_bf16_f32 v24, v24, v25\n"
      "v_cvt_pk_bf16_f32 v26, v26, v27\n"
      "v_accvgpr_write_b32 a208, v24\n"
      "v_mfma_f32_16x16x32_bf16 v[54:57], a[12:15], v[72:75], v[54:57]\n"
      "ds_read_b128 a[12:15], v86 offset:21600\n"
      "v_accvgpr_write_b32 a209, v26\n"
      "v_cvt_pk_bf16_f32 v176, v28, v29\n"
      "v_cvt_pk_bf16_f32 v177, v30, v31\n"
      "v_cvt_pk_bf16_f32 v172, v32, v33\n"
      "v_mfma_f32_16x16x32_bf16 v[62:65], a[16:19], v[76:79], 0\n"
      "v_cvt_pk_bf16_f32 v173, v34, v35\n"
      "v_cvt_pk_bf16_f32 v180, v58, v59\n"
      "v_mfma_f32_16x16x32_bf16 v[20:23], a[20:23], v[68:71], v[62:65]\n"
      "v_cvt_pk_bf16_f32 v54, v54, v55\n"
      "v_cvt_pk_bf16_f32 v56, v56, v57\n"
      "v_mfma_f32_16x16x32_bf16 v[62:65], a[20:23], v[72:75], v[62:65]\n"
      "ds_read_b128 a[20:23], v86 offset:40000\n"
      "buffer_load_dwordx4 v8, s[28:31], s97 offen offset:0 lds\n"
      "v_accvgpr_write_b32 a228, v54\n"
      "v_mfma_f32_16x16x32_bf16 v[24:27], a[16:19], v[80:83], 0\n"
      "ds_read_b128 a[16:19], v84 offset:18528\n"
      "v_accvgpr_write_b32 a229, v56\n"
      "v_mfma_f32_16x16x32_bf16 v[28:31], a[24:27], v[68:71], v[24:27]\n"
      "v_cvt_pk_bf16_f32 v181, v60, v61\n"
      "v_cvt_pk_bf16_f32 v188, v62, v63\n"
      "v_mfma_f32_16x16x32_bf16 v[24:27], a[24:27], v[72:75], v[24:27]\n"
      "s_waitcnt lgkmcnt(2)\n"
      "ds_read_b128 a[24:27], v84 offset:36928\n"
      "v_cvt_pk_bf16_f32 v189, v64, v65\n"
      "v_cvt_pk_bf16_f32 v184, v20, v21\n"
      "v_cvt_pk_bf16_f32 v185, v22, v23\n"
      "v_mfma_f32_16x16x32_bf16 v[32:35], a[28:31], v[76:79], 0\n"
      "ds_read_b128 a[28:31], v85 offset:36928\n"
      "buffer_load_dwordx4 v9, s[28:31], s97 offen offset:1024 lds\n"
      "v_mfma_f32_16x16x32_bf16 v[54:57], a[32:35], v[68:71], v[32:35]\n"
      "v_cvt_pk_bf16_f32 v24, v24, v25\n"
      "v_cvt_pk_bf16_f32 v26, v26, v27\n"
      "v_mfma_f32_16x16x32_bf16 v[32:35], a[32:35], v[72:75], v[32:35]\n"
      "ds_read_b128 a[32:35], v86 offset:40064\n"
      "v_accvgpr_write_b32 a236, v24\n"
      "v_accvgpr_write_b32 a237, v26\n"
      "v_mfma_f32_16x16x32_bf16 v[58:61], a[0:3], v[76:79], 0\n"
      "v_cvt_pk_bf16_f32 v192, v28, v29\n"
      "v_cvt_pk_bf16_f32 v193, v30, v31\n"
      "v_mfma_f32_16x16x32_bf16 v[62:65], a[4:7], v[68:71], v[58:61]\n"
      "v_cvt_pk_bf16_f32 v196, v54, v55\n"
      "v_cvt_pk_bf16_f32 v200, v32, v33\n"
      "v_mfma_f32_16x16x32_bf16 v[58:61], a[4:7], v[72:75], v[58:61]\n"
      "ds_read_b128 a[4:7], v86 offset:58464\n"
      "buffer_load_dwordx4 v89, s[28:31], s97 offen offset:2048 lds\n"
      "v_mfma_f32_16x16x32_bf16 v[20:23], a[0:3], v[80:83], 0\n"
      "s_cmp_lt_u32 s40, 2\n"
      "s_cselect_b32 s97, 6144, 0\n"
      "ds_read_b128 a[0:3], v84 offset:36992\n"
      "s_add_u32 s97, s97, s92\n"
      "s_add_u32 m0, s97, 43072\n"
      "s_and_b32 s97, 0x24242424, s14\n"
      "s_xor_b32 s97, 0x352f352f, s97\n"
      "s_bfe_u32 s97, s97, s43\n"
      "s_mul_i32 s97, s97, 3072\n"
      "v_cvt_pk_bf16_f32 v201, v34, v35\n"
      "v_mfma_f32_16x16x32_bf16 v[24:27], a[8:11], v[68:71], v[20:23]\n"
      "v_cvt_pk_bf16_f32 v197, v56, v57\n"
      "v_cvt_pk_bf16_f32 v154, v58, v59\n"
      "v_cvt_pk_bf16_f32 v155, v60, v61\n"
      "v_mfma_f32_16x16x32_bf16 v[20:23], a[8:11], v[72:75], v[20:23]\n"
      "ds_read_b128 a[8:11], v84 offset:55392\n"
      "v_cvt_pk_bf16_f32 v150, v62, v63\n"
      "v_cvt_pk_bf16_f32 v151, v64, v65\n"
      "v_mfma_f32_16x16x32_bf16 v[28:31], a[12:15], v[76:79], 0\n"
      "s_waitcnt lgkmcnt(3)\n"
      "ds_read_b128 a[12:15], v85 offset:55392\n"
      "buffer_load_dwordx4 v8, s[28:31], s97 offen offset:0 lds\n"
      "v_cvt_pk_bf16_f32 v24, v24, v25\n"
      "v_mfma_f32_16x16x32_bf16 v[32:35], a[16:19], v[68:71], v[28:31]\n"
      "v_cvt_pk_bf16_f32 v158, v20, v21\n"
      "v_cvt_pk_bf16_f32 v159, v22, v23\n"
      "v_mfma_f32_16x16x32_bf16 v[28:31], a[16:19], v[72:75], v[28:31]\n"
      "ds_read_b128 a[16:19], v86 offset:58528\n"
      "v_cvt_pk_bf16_f32 v26, v26, v27\n"
      "v_accvgpr_write_b32 a202, v24\n"
      "v_accvgpr_write_b32 a203, v26\n"
      "v_mfma_f32_16x16x32_bf16 v[54:57], a[20:23], v[76:79], 0\n"
      "v_cvt_pk_bf16_f32 v162, v32, v33\n"
      "v_cvt_pk_bf16_f32 v163, v34, v35\n"
      "v_mfma_f32_16x16x32_bf16 v[58:61], a[24:27], v[68:71], v[54:57]\n"
      "v_cvt_pk_bf16_f32 v166, v28, v29\n"
      "v_cvt_pk_bf16_f32 v167, v30, v31\n"
      "v_mfma_f32_16x16x32_bf16 v[54:57], a[24:27], v[72:75], v[54:57]\n"
      "buffer_load_dwordx4 v9, s[28:31], s97 offen offset:1024 lds\n"
      "v_add_u32 v86, 46144, v86\n"
      "v_add_u32 v85, 46144, v85\n"
      "v_mfma_f32_16x16x32_bf16 v[62:65], a[20:23], v[80:83], 0\n"
      "ds_read_b128 a[20:23], v84 offset:55456\n"
      "v_cvt_pk_bf16_f32 v58, v58, v59\n"
      "v_mfma_f32_16x16x32_bf16 v[20:23], a[28:31], v[68:71], v[62:65]\n"
      "v_cvt_pk_bf16_f32 v60, v60, v61\n"
      "v_cvt_pk_bf16_f32 v170, v54, v55\n"
      "v_mfma_f32_16x16x32_bf16 v[62:65], a[28:31], v[72:75], v[62:65]\n"
      "v_cvt_pk_bf16_f32 v171, v56, v57\n"
      "v_accvgpr_write_b32 a210, v58\n"
      "v_mfma_f32_16x16x32_bf16 v[24:27], a[32:35], v[76:79], 0\n"
      "s_waitcnt lgkmcnt(1)\n"
      "buffer_load_dwordx4 v89, s[28:31], s97 offen offset:2048 lds\n"
      "v_accvgpr_write_b32 a211, v60\n"
      "v_mfma_f32_16x16x32_bf16 v[28:31], a[0:3], v[68:71], v[24:27]\n"
      "s_add_u32 m0, s46, 39840\n"
      "s_add_u32 s51, s51, s12\n"
      "v_add_u32 v84, 46144, v84\n"
      "v_cvt_pk_bf16_f32 v178, v62, v63\n"
      "v_mfma_f32_16x16x32_bf16 v[24:27], a[0:3], v[72:75], v[24:27]\n"
      "v_cvt_pk_bf16_f32 v179, v64, v65\n"
      "v_cvt_pk_bf16_f32 v174, v20, v21\n"
      "v_cvt_pk_bf16_f32 v175, v22, v23\n"
      "v_mfma_f32_16x16x32_bf16 v[32:35], a[4:7], v[76:79], 0\n"
      "v_cvt_pk_bf16_f32 v182, v28, v29\n"
      "v_cvt_pk_bf16_f32 v183, v30, v31\n"
      "v_mfma_f32_16x16x32_bf16 v[54:57], a[8:11], v[68:71], v[32:35]\n"
      "v_add_u32 v87, 46144, v87\n"
      "v_cvt_pk_bf16_f32 v24, v24, v25\n"
      "v_mfma_f32_16x16x32_bf16 v[32:35], a[8:11], v[72:75], v[32:35]\n"
      "s_waitcnt lgkmcnt(0)\n"
      "s_waitcnt vmcnt(6)\n"
      "s_cbranch_vccnz Lconv_h7\n"
      "s_barrier\n"
      "Lconv_h7:\n"
      "buffer_load_dwordx4 v16, s[32:35], s51 offen lds\n"
      "v_mfma_f32_16x16x32_bf16 v[58:61], a[4:7], v[80:83], 0\n"
      "s_add_u32 m0, s46, 39840\n"
      "v_cvt_pk_bf16_f32 v26, v26, v27\n"
      "v_accvgpr_write_b32 a230, v24\n"
      "v_mfma_f32_16x16x32_bf16 v[62:65], a[12:15], v[68:71], v[58:61]\n"
      "s_cbranch_vccz Lconv_h8\n"
      "s_barrier\n"
      "Lconv_h8:\n"
      "ds_read_b128 a[24:27], v86 offset:30752\n"
      "ds_read_b128 a[28:31], v84 offset:27680\n"
      "ds_read_b128 a[32:35], v85 offset:27680\n"
      "ds_read_b128 a[0:3], v86 offset:30816\n"
      "buffer_load_dwordx4 v17, s[32:35], s51 offen offset:1024 lds\n"
      "v_mfma_f32_16x16x32_bf16 v[58:61], a[12:15], v[72:75], v[58:61]\n"
      "s_add_u32 m0, s46, 39840\n"
      "ds_read_b128 a[4:7], v84 offset:27744\n"
      "ds_read_b128 a[8:11], v86 offset:39968\n"
      "buffer_load_dwordx4 v18, s[32:35], s51 offen offset:2048 lds\n"
      "v_accvgpr_write_b32 a231, v26\n"
      "v_cvt_pk_bf16_f32 v190, v32, v33\n"
      "v_mfma_f32_16x16x32_bf16 v[20:23], a[16:19], v[76:79], 0\n"
      "s_sub_u32 m0, s94, s59\n"
      "s_add_u32 s96, s51, s15\n"
      "ds_read_b128 a[12:15], v84 offset:36896\n"
      "ds_read_b128 a[16:19], v85 offset:36896\n"
      "buffer_load_dwordx4 v19, s[32:35], s96 offen lds\n"
      "v_cvt_pk_bf16_f32 v191, v34, v35\n"
      "v_cvt_pk_bf16_f32 v186, v54, v55\n"
      "v_mfma_f32_16x16x32_bf16 v[24:27], a[20:23], v[68:71], v[20:23]\n"
      "s_add_u32 m0, s46, 0\n"
      "s_add_u32 s51, s51, s12\n"
      "ds_read_b128 a[36:39], v2 offset:13280\n"
      "ds_read_b128 a[40:43], v3 offset:13280\n"
      "buffer_load_dwordx4 v16, s[32:35], s51 offen lds\n"
      "v_cvt_pk_bf16_f32 v187, v56, v57\n"
      "v_cvt_pk_bf16_f32 v58, v58, v59\n"
      "v_mfma_f32_16x16x32_bf16 v[20:23], a[20:23], v[72:75], v[20:23]\n"
      "s_waitcnt lgkmcnt(9)\n"
      "s_add_u32 m0, s46, 0\n"
      "ds_read_b128 a[20:23], v86 offset:40032\n"
      "ds_read_b128 a[44:47], v4 offset:13280\n"
      "buffer_load_dwordx4 v17, s[32:35], s51 offen offset:1024 lds\n"
      "v_cvt_pk_bf16_f32 v60, v60, v61\n"
      "v_accvgpr_write_b32 a238, v58\n"
      "v_mfma_f32_16x16x32_bf16 v[28:31], a[24:27], v[76:79], 0\n"
      "s_waitcnt lgkmcnt(10)\n"
      "s_add_u32 m0, s46, 0\n"
      "ds_read_b128 a[48:51], v2 offset:13344\n"
      "ds_read_b128 a[52:55], v3 offset:13344\n"
      "buffer_load_dwordx4 v18, s[32:35], s51 offen offset:2048 lds\n"
      "v_mfma_f32_16x16x32_bf16 v[32:35], a[28:31], v[68:71], v[28:31]\n"
      "s_mov_b32 m0, s18\n"
      "ds_read_b128 a[56:59], v4 offset:13344\n"
      "ds_read_b64_tr_b16 a[112:113], v87 offset:27680\n"
      "v_accvgpr_write_b32 a239, v60\n"
      "v_cvt_pk_bf16_f32 v194, v62, v63\n"
      "v_mfma_f32_16x16x32_bf16 v[28:31], a[28:31], v[72:75], v[28:31]\n"
      "ds_read_b128 a[28:31], v86 offset:49184\n"
      "ds_read_b64_tr_b16 a[120:121], v87 offset:27744\n"
      "v_cvt_pk_bf16_f32 v195, v64, v65\n"
      "v_cvt_pk_bf16_f32 v202, v20, v21\n"
      "v_cvt_pk_bf16_f32 v203, v22, v23\n"
      "v_mfma_f32_16x16x32_bf16 v[54:57], a[24:27], v[80:83], 0\n"
      "s_waitcnt lgkmcnt(10)\n"
      "ds_read_b128 a[24:27], v84 offset:36960\n"
      "ds_read_b64_tr_b16 a[148:149], v87 offset:36896\n"
      "v_cvt_pk_bf16_f32 v198, v24, v25\n"
      "v_mfma_f32_16x16x32_bf16 v[58:61], a[32:35], v[68:71], v[54:57]\n"
      "ds_read_b64_tr_b16 a[156:157], v87 offset:36960\n"
      "ds_read_b64_tr_b16 a[184:185], v87 offset:46112\n"
      "v_cvt_pk_bf16_f32 v199, v26, v27\n"
      "v_cvt_pk_bf16_f32 v28, v28, v29\n"
      "v_mfma_f32_16x16x32_bf16 v[54:57], a[32:35], v[72:75], v[54:57]\n"
      "ds_read_b128 a[32:35], v84 offset:46112\n"
      "ds_read_b64_tr_b16 a[192:193], v87 offset:46176\n"
      "v_cvt_pk_bf16_f32 v30, v30, v31\n"
      "v_accvgpr_write_b32 a220, v28\n"
      "v_mfma_f32_16x16x32_bf16 v[62:65], a[0:3], v[76:79], 0\n"
      "ds_read_b128 a[0:3], v85 offset:46112\n"
      "v_accvgpr_write_b32 a221, v30\n"
      "v_mfma_f32_16x16x32_bf16 v[20:23], a[4:7], v[68:71], v[62:65]\n"
      "v_cvt_pk_bf16_f32 v204, v32, v33\n"
      "v_cvt_pk_bf16_f32 v205, v34, v35\n"
      "v_mfma_f32_16x16x32_bf16 v[62:65], a[4:7], v[72:75], v[62:65]\n"
      "ds_read_b128 a[4:7], v86 offset:49248\n"
      "v_cvt_pk_bf16_f32 v212, v54, v55\n"
      "v_cvt_pk_bf16_f32 v213, v56, v57\n"
      "v_cvt_pk_bf16_f32 v208, v58, v59\n"
      "v_mfma_f32_16x16x32_bf16 v[24:27], a[8:11], v[76:79], 0\n"
      "v_cvt_pk_bf16_f32 v209, v60, v61\n"
      "v_add_u32 v88, 46144, v88\n"
      "v_mfma_f32_16x16x32_bf16 v[28:31], a[12:15], v[68:71], v[24:27]\n"
      "ds_read_b64_tr_b16 a[116:117], v88 offset:27680\n"
      "ds_read_b64_tr_b16 a[152:153], v88 offset:36896\n"
      "v_mfma_f32_16x16x32_bf16 v[24:27], a[12:15], v[72:75], v[24:27]\n"
      "ds_read_b64_tr_b16 a[188:189], v88 offset:46112\n"
      "v_cvt_pk_bf16_f32 v62, v62, v63\n"
      "v_cvt_pk_bf16_f32 v64, v64, v65\n"
      "v_mfma_f32_16x16x32_bf16 v[32:35], a[8:11], v[80:83], 0\n"
      "ds_read_b128 a[8:11], v84 offset:46176\n"
      "v_accvgpr_write_b32 a224, v62\n"
      "v_mfma_f32_16x16x32_bf16 v[54:57], a[16:19], v[68:71], v[32:35]\n"
      "v_accvgpr_write_b32 a225, v64\n"
      "v_cvt_pk_bf16_f32 v216, v20, v21\n"
      "v_mfma_f32_16x16x32_bf16 v[32:35], a[16:19], v[72:75], v[32:35]\n"
      "s_waitcnt lgkmcnt(4)\n"
      "v_cvt_pk_bf16_f32 v217, v22, v23\n"
      "v_cvt_pk_bf16_f32 v224, v24, v25\n"
      "v_cvt_pk_bf16_f32 v225, v26, v27\n"
      "v_mfma_f32_16x16x32_bf16 v[58:61], a[20:23], v[76:79], 0\n"
      "v_cvt_pk_bf16_f32 v228, v54, v55\n"
      "v_cvt_pk_bf16_f32 v229, v56, v57\n"
      "v_mfma_f32_16x16x32_bf16 v[62:65], a[24:27], v[68:71], v[58:61]\n"
      "v_cvt_pk_bf16_f32 v220, v28, v29\n"
      "v_cvt_pk_bf16_f32 v221, v30, v31\n"
      "v_mfma_f32_16x16x32_bf16 v[58:61], a[24:27], v[72:75], v[58:61]\n"
      "v_cvt_pk_bf16_f32 v32, v32, v33\n"
      "v_cvt_pk_bf16_f32 v34, v34, v35\n"
      "v_mfma_f32_16x16x32_bf16 v[36:39], a[36:39], a[72:75], 0\n"
      "ds_read_b128 a[36:39], v2 offset:13408\n"
      "v_accvgpr_write_b32 a232, v32\n"
      "v_mfma_f32_16x16x32_bf16 v[40:43], a[40:43], a[76:79], v[36:39]\n"
      "v_cvt_pk_bf16_f32 v233, v64, v65\n"
      "v_cvt_pk_bf16_f32 v232, v62, v63\n"
      "v_mfma_f32_16x16x32_bf16 v[48:51], a[40:43], a[252:255], v[36:39]\n"
      "ds_read_b128 a[40:43], v3 offset:13408\n"
      "v_cvt_pk_bf16_f32 v236, v58, v59\n"
      "v_mfma_f32_16x16x32_bf16 v[44:47], a[44:47], a[80:83], v[36:39]\n"
      "ds_read_b128 a[44:47], v4 offset:13408\n"
      "v_cvt_pk_bf16_f32 v237, v60, v61\n"
      "v_mfma_f32_16x16x32_bf16 v[52:55], a[48:51], a[72:75], 0\n"
      "ds_read_b128 a[48:51], v2 offset:26560\n"
      "v_cvt_pk_bf16_f32 v40, v40, v41\n"
      "v_mfma_f32_16x16x32_bf16 v[56:59], a[52:55], a[76:79], v[52:55]\n"
      "ds_write_addtid_b32 v40 offset:768\n"
      "v_cvt_pk_bf16_f32 v41, v42, v43\n"
      "v_mfma_f32_16x16x32_bf16 v[64:67], a[52:55], a[252:255], v[52:55]\n"
      "ds_read_b128 a[52:55], v3 offset:26560\n"
      "ds_write_addtid_b32 v41 offset:37696\n"
      "v_cvt_pk_bf16_f32 v48, v48, v49\n"
      "v_mfma_f32_16x16x32_bf16 v[60:63], a[56:59], a[80:83], v[52:55]\n"
      "s_waitcnt lgkmcnt(6)\n"
      "ds_read_b128 a[56:59], v4 offset:26560\n"
      "ds_write_addtid_b32 v48 offset:1536\n"
      "v_cvt_pk_bf16_f32 v49, v50, v51\n"
      "v_cvt_pk_bf16_f32 v44, v44, v45\n"
      "v_mfma_f32_16x16x32_bf16 v[36:39], a[36:39], a[72:75], 0\n"
      "s_waitcnt lgkmcnt(7)\n"
      "ds_write_addtid_b32 v49 offset:38464\n"
      "ds_read_b128 a[36:39], v2 offset:26624\n"
      "ds_write_addtid_b32 v44 offset:0\n"
      "v_cvt_pk_bf16_f32 v45, v46, v47\n"
      "v_mfma_f32_16x16x32_bf16 v[40:43], a[40:43], a[76:79], v[36:39]\n"
      "ds_write_addtid_b32 v45 offset:36928\n"
      "s_mov_b32 m0, s18\n"
      "v_cvt_pk_bf16_f32 v56, v56, v57\n"
      "v_mfma_f32_16x16x32_bf16 v[48:51], a[40:43], a[252:255], v[36:39]\n"
      "s_waitcnt lgkmcnt(9)\n"
      "ds_read_b128 a[40:43], v3 offset:26624\n"
      "ds_write_addtid_b32 v56 offset:1024\n"
      "v_cvt_pk_bf16_f32 v57, v58, v59\n"
      "v_cvt_pk_bf16_f32 v64, v64, v65\n"
      "v_mfma_f32_16x16x32_bf16 v[44:47], a[44:47], a[80:83], v[36:39]\n"
      "ds_write_addtid_b32 v57 offset:37952\n"
      "ds_read_b128 a[44:47], v4 offset:26624\n"
      "ds_write_addtid_b32 v64 offset:1792\n"
      "v_cvt_pk_bf16_f32 v65, v66, v67\n"
      "v_cvt_pk_bf16_f32 v60, v60, v61\n"
      "v_mfma_f32_16x16x32_bf16 v[52:55], a[48:51], a[72:75], 0\n"
      "s_waitcnt lgkmcnt(12)\n"
      "ds_write_addtid_b32 v65 offset:38720\n"
      "ds_read_b128 a[48:51], v2 offset:26688\n"
      "ds_write_addtid_b32 v60 offset:256\n"
      "v_cvt_pk_bf16_f32 v61, v62, v63\n"
      "v_mfma_f32_16x16x32_bf16 v[56:59], a[52:55], a[76:79], v[52:55]\n"
      "ds_write_addtid_b32 v61 offset:37184\n"
      "s_mov_b32 m0, s18\n"
      "v_cvt_pk_bf16_f32 v40, v40, v41\n"
      "v_mfma_f32_16x16x32_bf16 v[64:67], a[52:55], a[252:255], v[52:55]\n"
      "s_waitcnt lgkmcnt(11)\n"
      "ds_read_b128 a[52:55], v3 offset:26688\n"
      "ds_write_addtid_b32 v40 offset:1280\n"
      "v_cvt_pk_bf16_f32 v41, v42, v43\n"
      "v_cvt_pk_bf16_f32 v48, v48, v49\n"
      "v_mfma_f32_16x16x32_bf16 v[60:63], a[56:59], a[80:83], v[52:55]\n"
      "ds_write_addtid_b32 v41 offset:38208\n"
      "ds_read_b128 a[56:59], v4 offset:26688\n"
      "ds_write_addtid_b32 v48 offset:2048\n"
      "v_cvt_pk_bf16_f32 v49, v50, v51\n"
      "v_cvt_pk_bf16_f32 v44, v44, v45\n"
      "v_mfma_f32_16x16x32_bf16 v[36:39], a[36:39], a[72:75], 0\n"
      "s_waitcnt lgkmcnt(13)\n"
      "ds_write_addtid_b32 v49 offset:38976\n"
      "ds_write_addtid_b32 v44 offset:512\n"
      "v_cvt_pk_bf16_f32 v45, v46, v47\n"
      "v_mfma_f32_16x16x32_bf16 v[40:43], a[40:43], a[76:79], v[36:39]\n"
      "ds_write_addtid_b32 v45 offset:37440\n"
      "s_mov_b32 m0, s18\n"
      "v_cvt_pk_bf16_f32 v56, v56, v57\n"
      "v_mfma_f32_16x16x32_bf16 v[48:51], a[40:43], a[252:255], v[36:39]\n"
      "s_waitcnt lgkmcnt(10)\n"
      "ds_write_addtid_b32 v56 offset:5376\n"
      "v_cvt_pk_bf16_f32 v57, v58, v59\n"
      "v_cvt_pk_bf16_f32 v64, v64, v65\n"
      "v_mfma_f32_16x16x32_bf16 v[44:47], a[44:47], a[80:83], v[36:39]\n"
      "ds_write_addtid_b32 v57 offset:42304\n"
      "ds_write_addtid_b32 v64 offset:6144\n"
      "v_cvt_pk_bf16_f32 v65, v66, v67\n"
      "v_cvt_pk_bf16_f32 v60, v60, v61\n"
      "v_cvt_pk_bf16_f32 v61, v62, v63\n"
      "v_mfma_f32_16x16x32_bf16 v[52:55], a[48:51], a[72:75], 0\n"
      "s_waitcnt lgkmcnt(10)\n"
      "ds_write_addtid_b32 v65 offset:43072\n"
      "ds_write_addtid_b32 v60 offset:4608\n"
      "ds_write_addtid_b32 v61 offset:41536\n"
      "s_mov_b32 m0, s18\n"
      "v_cvt_pk_bf16_f32 v48, v48, v49\n"
      "v_cvt_pk_bf16_f32 v49, v50, v51\n"
      "v_mfma_f32_16x16x32_bf16 v[56:59], a[52:55], a[76:79], v[52:55]\n"
      "ds_write_addtid_b32 v48 offset:6400\n"
      "ds_write_addtid_b32 v49 offset:43328\n"
      "v_cvt_pk_bf16_f32 v40, v40, v41\n"
      "v_cvt_pk_bf16_f32 v41, v42, v43\n"
      "v_cvt_pk_bf16_f32 v44, v44, v45\n"
      "v_mfma_f32_16x16x32_bf16 v[64:67], a[52:55], a[252:255], v[52:55]\n"
      "s_waitcnt lgkmcnt(12)\n"
      "ds_write_addtid_b32 v40 offset:5632\n"
      "ds_write_addtid_b32 v41 offset:42560\n"
      "ds_write_addtid_b32 v44 offset:4864\n"
      "v_cvt_pk_bf16_f32 v45, v46, v47\n"
      "v_cvt_pk_bf16_f32 v56, v56, v57\n"
      "v_mfma_f32_16x16x32_bf16 v[60:63], a[56:59], a[80:83], v[52:55]\n"
      "ds_write_addtid_b32 v45 offset:41792\n"
      "s_mov_b32 m0, s18\n"
      "v_cvt_pk_bf16_f32 v64, v64, v65\n"
      "v_cvt_pk_bf16_f32 v65, v66, v67\n"
      "v_cvt_pk_bf16_f32 v57, v58, v59\n"
      "v_mfma_f32_16x16x32_bf16 v[20:23], a[28:31], v[76:79], 0\n"
      "ds_write_addtid_b32 v64 offset:6656\n"
      "ds_write_addtid_b32 v65 offset:43584\n"
      "ds_write_addtid_b32 v56 offset:5888\n"
      "ds_write_addtid_b32 v57 offset:42816\n"
      "v_cvt_pk_bf16_f32 v60, v60, v61\n"
      "v_mfma_f32_16x16x32_bf16 v[24:27], a[32:35], v[68:71], v[20:23]\n"
      "ds_write_addtid_b32 v60 offset:5120\n"
      "v_cvt_pk_bf16_f32 v61, v62, v63\n"
      "ds_write_addtid_b32 v61 offset:42048\n"
      "v_mfma_f32_16x16x32_bf16 v[20:23], a[32:35], v[72:75], v[20:23]\n"
      "s_waitcnt lgkmcnt(0)\n"
      "s_waitcnt vmcnt(7)\n"
      "s_cbranch_vccnz Lconv_h9\n"
      "s_barrier\n"
      "Lconv_h9:\n"
      "s_add_u32 m0, s46, 13280\n"
      "s_add_u32 s51, s51, s12\n"
      "v_accvgpr_write_b32 a233, v34\n"
      "v_mfma_f32_16x16x32_bf16 v[28:31], a[28:31], v[80:83], 0\n"
      "Lconv_p2:\n"
      "s_cbranch_execz Lconv_r0\n"
      "s_nop 0\n"
      "v_cvt_pk_bf16_f32 v240, v24, v25\n"
      "v_cvt_pk_bf16_f32 v20, v20, v21\n"
      "v_mfma_f32_16x16x32_bf16 v[32:35], a[0:3], v[68:71], v[28:31]\n"
      "s_cbranch_vccz Lconv_h10\n"
      "s_barrier\n"
      "Lconv_h10:\n"
      "ds_read_b128 a[12:15], v86 offset:58400\n"
      "ds_read_b128 a[16:19], v84 offset:55328\n"
      "ds_read_b128 a[20:23], v85 offset:55328\n"
      "ds_read_b128 a[24:27], v86 offset:58464\n"
      "v_mfma_f32_16x16x32_bf16 v[28:31], a[0:3], v[72:75], v[28:31]\n"
      "ds_read_b128 a[28:31], v84 offset:55392\n"
      "ds_read_b128 a[32:35], v86 offset:21536\n"
      "v_mfma_f32_16x16x32_bf16 v[54:57], a[4:7], v[76:79], 0\n"
      "ds_read_b128 a[0:3], v84 offset:18464\n"
      "ds_read_b128 a[4:7], v85 offset:18464\n"
      "v_mfma_f32_16x16x32_bf16 v[58:61], a[8:11], v[68:71], v[54:57]\n"
      "ds_read_b64_tr_b16 a[114:115], v87 offset:55328\n"
      "ds_read_b64_tr_b16 a[118:119], v88 offset:55328\n"
      "v_cvt_pk_bf16_f32 v22, v22, v23\n"
      "v_accvgpr_write_b32 a240, v20\n"
      "v_mfma_f32_16x16x32_bf16 v[54:57], a[8:11], v[72:75], v[54:57]\n"
      "s_waitcnt lgkmcnt(9)\n"
      "ds_read_b128 a[8:11], v86 offset:21600\n"
      "ds_read_b64_tr_b16 a[122:123], v87 offset:55392\n"
      "buffer_load_dwordx4 v16, s[32:35], s51 offen lds\n"
      "v_accvgpr_write_b32 a241, v22\n"
      "v_cvt_pk_bf16_f32 v241, v26, v27\n"
      "v_mfma_f32_16x16x32_bf16 v[62:65], a[12:15], v[76:79], 0\n"
      "s_waitcnt lgkmcnt(10)\n"
      "s_add_u32 m0, s46, 13280\n"
      "ds_read_b64_tr_b16 a[150:151], v87 offset:18464\n"
      "ds_read_b64_tr_b16 a[154:155], v88 offset:18464\n"
      "buffer_load_dwordx4 v17, s[32:35], s51 offen offset:1024 lds\n"
      "v_cvt_pk_bf16_f32 v248, v28, v29\n"
      "v_cvt_pk_bf16_f32 v249, v30, v31\n"
      "v_mfma_f32_16x16x32_bf16 v[20:23], a[16:19], v[68:71], v[62:65]\n"
      "s_add_u32 m0, s46, 13280\n"
      "s_add_u32 s96, s51, s15\n"
      "ds_read_b64_tr_b16 a[158:159], v87 offset:18528\n"
      "ds_read_b64_tr_b16 a[186:187], v87 offset:0\n"
      "buffer_load_dwordx4 v18, s[32:35], s51 offen offset:2048 lds\n"
      "v_cvt_pk_bf16_f32 v244, v32, v33\n"
      "v_cvt_pk_bf16_f32 v245, v34, v35\n"
      "v_mfma_f32_16x16x32_bf16 v[62:65], a[16:19], v[72:75], v[62:65]\n"
      "s_mov_b32 m0, s59\n"
      "ds_read_b128 a[16:19], v86 offset:3072\n"
      "ds_read_b64_tr_b16 a[190:191], v88 offset:0\n"
      "buffer_load_dwordx4 v19, s[32:35], s96 offen lds\n"
      "v_cvt_pk_bf16_f32 v54, v54, v55\n"
      "v_cvt_pk_bf16_f32 v56, v56, v57\n"
      "v_mfma_f32_16x16x32_bf16 v[24:27], a[12:15], v[80:83], 0\n"
      "s_waitcnt lgkmcnt(10)\n"
      "ds_read_b128 a[12:15], v84 offset:18528\n"
      "ds_read_b64_tr_b16 a[194:195], v87 offset:64\n"
      "v_accvgpr_write_b32 a244, v54\n"
      "v_accvgpr_write_b32 a245, v56\n"
      "v_mfma_f32_16x16x32_bf16 v[28:31], a[20:23], v[68:71], v[24:27]\n"
      "v_cvt_pk_bf16_f32 v252, v58, v59\n"
      "v_cvt_pk_bf16_f32 v253, v60, v61\n"
      "v_mfma_f32_16x16x32_bf16 v[24:27], a[20:23], v[72:75], v[24:27]\n"
      "ds_read_b128 a[20:23], v84 offset:0\n"
      "v_cvt_pk_bf16_f32 v62, v62, v63\n"
      "v_cvt_pk_bf16_f32 v64, v64, v65\n"
      "v_mfma_f32_16x16x32_bf16 v[32:35], a[24:27], v[76:79], 0\n"
      "ds_read_b128 a[24:27], v85 offset:0\n"
      "v_accvgpr_write_b32 a222, v62\n"
      "v_mfma_f32_16x16x32_bf16 v[54:57], a[28:31], v[68:71], v[32:35]\n"
      "v_accvgpr_write_b32 a223, v64\n"
      "v_cvt_pk_bf16_f32 v206, v20, v21\n"
      "v_mfma_f32_16x16x32_bf16 v[32:35], a[28:31], v[72:75], v[32:35]\n"
      "ds_read_b128 a[28:31], v86 offset:3136\n"
      "v_cvt_pk_bf16_f32 v207, v22, v23\n"
      "v_cvt_pk_bf16_f32 v214, v24, v25\n"
      "v_cvt_pk_bf16_f32 v215, v26, v27\n"
      "v_mfma_f32_16x16x32_bf16 v[58:61], a[32:35], v[76:79], 0\n"
      "v_cvt_pk_bf16_f32 v210, v28, v29\n"
      "v_cvt_pk_bf16_f32 v211, v30, v31\n"
      "v_mfma_f32_16x16x32_bf16 v[62:65], a[0:3], v[68:71], v[58:61]\n"
      "v_cvt_pk_bf16_f32 v32, v32, v33\n"
      "v_cvt_pk_bf16_f32 v34, v34, v35\n"
      "v_mfma_f32_16x16x32_bf16 v[58:61], a[0:3], v[72:75], v[58:61]\n"
      "v_accvgpr_write_b32 a226, v32\n"
      "v_accvgpr_write_b32 a227, v34\n"
      "v_mfma_f32_16x16x32_bf16 v[20:23], a[32:35], v[80:83], 0\n"
      "ds_read_b128 a[32:35], v84 offset:64\n"
      "v_cvt_pk_bf16_f32 v218, v54, v55\n"
      "v_mfma_f32_16x16x32_bf16 v[24:27], a[4:7], v[68:71], v[20:23]\n"
      "v_cvt_pk_bf16_f32 v219, v56, v57\n"
      "v_cvt_pk_bf16_f32 v222, v62, v63\n"
      "v_mfma_f32_16x16x32_bf16 v[20:23], a[4:7], v[72:75], v[20:23]\n"
      "s_waitcnt lgkmcnt(1)\n"
      "v_cvt_pk_bf16_f32 v226, v58, v59\n"
      "v_cvt_pk_bf16_f32 v227, v60, v61\n"
      "v_mfma_f32_16x16x32_bf16 v[28:31], a[8:11], v[76:79], 0\n"
      "v_cvt_pk_bf16_f32 v223, v64, v65\n"
      "v_cvt_pk_bf16_f32 v230, v24, v25\n"
      "v_mfma_f32_16x16x32_bf16 v[32:35], a[12:15], v[68:71], v[28:31]\n"
      "v_cvt_pk_bf16_f32 v231, v26, v27\n"
      "v_cvt_pk_bf16_f32 v20, v20, v21\n"
      "v_mfma_f32_16x16x32_bf16 v[28:31], a[12:15], v[72:75], v[28:31]\n"
      "ds_read_b128 a[12:15], v6 offset:0\n"
      "v_cvt_pk_bf16_f32 v22, v22, v23\n"
      "v_mfma_f32_16x16x32_bf16 v[54:57], a[16:19], v[76:79], 0\n"
      "v_accvgpr_write_b32 a234, v20\n"
      "v_accvgpr_write_b32 a235, v22\n"
      "v_mfma_f32_16x16x32_bf16 v[58:61], a[20:23], v[68:71], v[54:57]\n"
      "v_cvt_pk_bf16_f32 v234, v32, v33\n"
      "v_cvt_pk_bf16_f32 v235, v34, v35\n"
      "v_mfma_f32_16x16x32_bf16 v[54:57], a[20:23], v[72:75], v[54:57]\n"
      "s_waitcnt lgkmcnt(0)\n"
      "s_waitcnt vmcnt(4)\n"
      "s_cbranch_vccnz Lconv_h11\n"
      "s_barrier\n"
      "Lconv_h11:\n"
      "ds_read_b128 a[20:23], v6 offset:512\n"
      "v_mfma_f32_16x16x32_bf16 v[62:65], a[16:19], v[80:83], 0\n"
      "ds_read_b128 a[16:19], v6 offset:256\n"
      "v_cvt_pk_bf16_f32 v238, v28, v29\n"
      "v_mfma_f32_16x16x32_bf16 v[20:23], a[24:27], v[68:71], v[62:65]\n"
      "s_cbranch_vccz Lconv_h12\n"
      "s_barrier\n"
      "Lconv_h12:\n"
      "v_cvt_pk_bf16_f32 v239, v30, v31\n"
      "v_mfma_f32_16x16x32_bf16 v[62:65], a[24:27], v[72:75], v[62:65]\n"
      "ds_read_b128 a[24:27], v6 offset:4608\n"
      "v_cvt_pk_bf16_f32 v242, v58, v59\n"
      "v_cvt_pk_bf16_f32 v243, v60, v61\n"
      "v_mfma_f32_16x16x32_bf16 v[24:27], a[28:31], v[76:79], 0\n"
      "ds_read_b128 a[28:31], v6 offset:4864\n"
      "v_cvt_pk_bf16_f32 v54, v54, v55\n"
      "v_mfma_f32_16x16x32_bf16 v[28:31], a[32:35], v[68:71], v[24:27]\n"
      "v_cvt_pk_bf16_f32 v56, v56, v57\n"
      "v_accvgpr_write_b32 a242, v54\n"
      "v_mfma_f32_16x16x32_bf16 v[24:27], a[32:35], v[72:75], v[24:27]\n"
      "ds_read_b128 a[32:35], v6 offset:5120\n"
      "v_accvgpr_write_b32 a243, v56\n"
      "v_cvt_pk_bf16_f32 v246, v20, v21\n"
      "v_cvt_pk_bf16_f32 v247, v22, v23\n"
      "v_cvt_pk_bf16_f32 v250, v62, v63\n"
      "v_cvt_pk_bf16_f32 v251, v64, v65\n"
      "v_cvt_pk_bf16_f32 v254, v28, v29\n"
      "v_cvt_pk_bf16_f32 v255, v30, v31\n"
      "v_cvt_pk_bf16_f32 v24, v24, v25\n"
      "v_cvt_pk_bf16_f32 v26, v26, v27\n"
      "v_accvgpr_write_b32 a246, v24\n"
      "v_accvgpr_write_b32 a247, v26\n"
      "s_mov_b32 m0, s18\n"
      "ds_read_b128 a[48:51], v2 offset:39840\n"
      "v_lshlrev_b32 v9, 7, v5\n"
      "v_mfma_f32_16x16x32_bf16 v[68:71], a[12:15], a[88:91], 0\n"
      "s_waitcnt lgkmcnt(4)\n"
      "ds_read_b128 a[52:55], v3 offset:39840\n"
      "v_min_u32 v9, Lconv_u0-Lconv_t0, v9\n"
      "v_mfma_f32_16x16x32_bf16 v[68:71], a[16:19], a[100:103], v[68:71]\n"
      "s_mul_i32 s97, s41, Lconv_t1-Lconv_t0\n"
      "ds_read_b128 a[56:59], v4 offset:39840\n"
      "v_add_u32 v9, Lconv_t0-Lconv_start, v9\n"
      "v_mfma_f32_16x16x32_bf16 v[68:71], a[20:23], a[112:115], v[68:71]\n"
      "s_waitcnt lgkmcnt(5)\n"
      "ds_read_b128 a[36:39], v2 offset:39904\n"
      "v_add_u32 v9, s97, v9\n"
      "v_mfma_f32_16x16x32_bf16 v[68:71], a[24:27], a[124:127], v[68:71]\n"
      "s_waitcnt lgkmcnt(5)\n"
      "ds_read_b128 a[40:43], v3 offset:39904\n"
      "v_mfma_f32_16x16x32_bf16 v[68:71], a[28:31], a[136:139], v[68:71]\n"
      "s_waitcnt lgkmcnt(4)\n"
      "ds_read_b128 a[44:47], v4 offset:39904\n"
      "v_mfma_f32_16x16x32_bf16 v[52:55], a[48:51], a[72:75], 0\n"
      "ds_read_b128 a[0:3], v6 offset:768\n"
      "v_mfma_f32_16x16x32_bf16 v[68:71], a[32:35], a[148:151], v[68:71]\n"
      "ds_read_b128 a[48:51], v2 offset:39968\n"
      "v_mfma_f32_16x16x32_bf16 v[72:75], a[12:15], a[92:95], 0\n"
      "s_waitcnt lgkmcnt(6)\n"
      "ds_read_b128 a[4:7], v6 offset:1024\n"
      "v_mfma_f32_16x16x32_bf16 v[56:59], a[52:55], a[76:79], 0\n"
      "ds_read_b128 a[8:11], v6 offset:1280\n"
      "s_nop 0\n"
      "v_mfma_f32_16x16x32_bf16 v[72:75], a[16:19], a[104:107], v[72:75]\n"
      "ds_read_b128 a[52:55], v3 offset:39968\n"
      "s_nop 0\n"
      "v_mfma_f32_16x16x32_bf16 v[72:75], a[20:23], a[116:119], v[72:75]\n"
      "s_waitcnt lgkmcnt(7)\n"
      "ds_read_b128 a[60:63], v6 offset:1536\n"
      "v_sub_f32_e32 v64, v52, v56\n"
      "v_mfma_f32_16x16x32_bf16 v[60:63], a[56:59], a[80:83], v[52:55]\n"
      "ds_read_b128 a[56:59], v4 offset:39968\n"
      "v_sub_f32_e32 v65, v53, v57\n"
      "v_mfma_f32_16x16x32_bf16 v[72:75], a[24:27], a[128:131], v[72:75]\n"
      "v_cvt_pk_bf16_f32 v64, v64, v65\n"
      "v_sub_f32_e32 v66, v54, v58\n"
      "v_mfma_f32_16x16x32_bf16 v[72:75], a[28:31], a[140:143], v[72:75]\n"
      "ds_write_addtid_b32 v64 offset:10752\n"
      "v_sub_f32_e32 v67, v55, v59\n"
      "v_mfma_f32_16x16x32_bf16 v[72:75], a[32:35], a[152:155], v[72:75]\n"
      "v_cvt_pk_bf16_f32 v65, v66, v67\n"
      "v_add_f32_e32 v56, v52, v56\n"
      "v_mfma_f32_16x16x32_bf16 v[36:39], a[36:39], a[72:75], 0\n"
      "ds_write_addtid_b32 v65 offset:47680\n"
      "v_add_f32_e32 v57, v53, v57\n"
      "v_mfma_f32_16x16x32_bf16 v[76:79], a[12:15], a[96:99], 0\n"
      "ds_read_b128 a[12:15], v6 offset:5376\n"
      "v_cvt_pk_bf16_f32 v56, v56, v57\n"
      "v_mfma_f32_16x16x32_bf16 v[76:79], a[16:19], a[108:111], v[76:79]\n"
      "s_waitcnt lgkmcnt(5)\n"
      "ds_read_b128 a[16:19], v6 offset:5632\n"
      "v_add_f32_e32 v58, v54, v58\n"
      "v_mfma_f32_16x16x32_bf16 v[40:43], a[40:43], a[76:79], 0\n"
      "ds_write_addtid_b32 v56 offset:9984\n"
      "v_add_f32_e32 v59, v55, v59\n"
      "v_mfma_f32_16x16x32_bf16 v[76:79], a[20:23], a[120:123], v[76:79]\n"
      "ds_read_b128 a[20:23], v6 offset:5888\n"
      "v_cvt_pk_bf16_f32 v57, v58, v59\n"
      "v_mfma_f32_16x16x32_bf16 v[76:79], a[24:27], a[132:135], v[76:79]\n"
      "ds_write_addtid_b32 v57 offset:46912\n"
      "v_cvt_pk_bf16_f32 v60, v60, v61\n"
      "v_mfma_f32_16x16x32_bf16 v[44:47], a[44:47], a[80:83], v[36:39]\n"
      "ds_write_addtid_b32 v60 offset:9216\n"
      "v_cvt_pk_bf16_f32 v61, v62, v63\n"
      "v_mfma_f32_16x16x32_bf16 v[76:79], a[28:31], a[144:147], v[76:79]\n"
      "ds_write_addtid_b32 v61 offset:46144\n"
      "v_sub_f32_e32 v48, v36, v40\n"
      "v_mfma_f32_16x16x32_bf16 v[76:79], a[32:35], a[156:159], v[76:79]\n"
      "ds_read_b128 a[36:39], v2 offset:0\n"
      "v_sub_f32_e32 v49, v37, v41\n"
      "v_mfma_f32_16x16x32_bf16 v[80:83], a[0:3], v[92:95], 0\n"
      "v_cvt_pk_bf16_f32 v48, v48, v49\n"
      "v_sub_f32_e32 v50, v38, v42\n"
      "v_mfma_f32_16x16x32_bf16 v[52:55], a[48:51], a[72:75], 0\n"
      "ds_write_addtid_b32 v48 offset:11008\n"
      "v_sub_f32_e32 v51, v39, v43\n"
      "v_mfma_f32_16x16x32_bf16 v[80:83], a[4:7], v[148:151], v[80:83]\n"
      "ds_read_b128 a[40:43], v3 offset:0\n"
      "v_cvt_pk_bf16_f32 v49, v50, v51\n"
      "v_mfma_f32_16x16x32_bf16 v[80:83], a[8:11], v[204:207], v[80:83]\n"
      "ds_write_addtid_b32 v49 offset:47936\n"
      "v_add_f32_e32 v40, v36, v40\n"
      "v_mfma_f32_16x16x32_bf16 v[56:59], a[52:55], a[76:79], 0\n"
      "s_waitcnt lgkmcnt(7)\n"
      "v_add_f32_e32 v41, v37, v41\n"
      "v_cvt_pk_bf16_f32 v40, v40, v41\n"
      "v_mfma_f32_16x16x32_bf16 v[80:83], a[12:15], v[112:115], v[80:83]\n"
      "ds_write_addtid_b32 v40 offset:10240\n"
      "v_add_f32_e32 v42, v38, v42\n"
      "v_mfma_f32_16x16x32_bf16 v[80:83], a[16:19], a[208:211], v[80:83]\n"
      "v_add_f32_e32 v43, v39, v43\n"
      "v_cvt_pk_bf16_f32 v41, v42, v43\n"
      "v_mfma_f32_16x16x32_bf16 v[60:63], a[56:59], a[80:83], v[52:55]\n"
      "ds_write_addtid_b32 v41 offset:47168\n"
      "v_cvt_pk_bf16_f32 v44, v44, v45\n"
      "v_mfma_f32_16x16x32_bf16 v[80:83], a[20:23], v[220:223], v[80:83]\n"
      "ds_write_addtid_b32 v44 offset:9472\n"
      "v_cvt_pk_bf16_f32 v45, v46, v47\n"
      "v_mfma_f32_16x16x32_bf16 v[84:87], a[0:3], v[100:103], 0\n"
      "ds_read_b128 a[44:47], v4 offset:0\n"
      "v_sub_f32_e32 v64, v52, v56\n"
      "v_mfma_f32_16x16x32_bf16 v[84:87], a[4:7], a[200:203], v[84:87]\n"
      "ds_write_addtid_b32 v45 offset:46400\n"
      "v_sub_f32_e32 v65, v53, v57\n"
      "v_mfma_f32_16x16x32_bf16 v[84:87], a[8:11], v[208:211], v[84:87]\n"
      "ds_read_b128 a[64:67], v6 offset:1792\n"
      "v_cvt_pk_bf16_f32 v64, v64, v65\n"
      "v_mfma_f32_16x16x32_bf16 v[84:87], a[12:15], a[204:207], v[84:87]\n"
      "ds_read_b128 a[68:71], v6 offset:2048\n"
      "v_sub_f32_e32 v66, v54, v58\n"
      "v_mfma_f32_16x16x32_bf16 v[84:87], a[16:19], v[172:175], v[84:87]\n"
      "ds_read_b128 a[24:27], v6 offset:6144\n"
      "v_sub_f32_e32 v67, v55, v59\n"
      "v_mfma_f32_16x16x32_bf16 v[84:87], a[20:23], v[228:231], v[84:87]\n"
      "ds_read_b128 a[48:51], v2 offset:64\n"
      "v_cvt_pk_bf16_f32 v65, v66, v67\n"
      "v_mfma_f32_16x16x32_bf16 v[24:27], a[0:3], a[196:199], 0\n"
      "ds_write_addtid_b32 v64 offset:11264\n"
      "v_add_f32_e32 v56, v52, v56\n"
      "v_mfma_f32_16x16x32_bf16 v[24:27], a[4:7], v[160:163], v[24:27]\n"
      "s_waitcnt lgkmcnt(11)\n"
      "ds_read_b128 a[28:31], v6 offset:6400\n"
      "v_add_f32_e32 v57, v53, v57\n"
      "v_mfma_f32_16x16x32_bf16 v[36:39], a[36:39], a[72:75], 0\n"
      "ds_write_addtid_b32 v65 offset:48192\n"
      "v_cvt_pk_bf16_f32 v56, v56, v57\n"
      "v_mfma_f32_16x16x32_bf16 v[24:27], a[8:11], v[216:219], v[24:27]\n"
      "ds_read_b128 a[32:35], v6 offset:6656\n"
      "v_add_f32_e32 v58, v54, v58\n"
      "v_mfma_f32_16x16x32_bf16 v[24:27], a[12:15], v[124:127], v[24:27]\n"
      "ds_write_addtid_b32 v56 offset:10496\n"
      "v_add_f32_e32 v59, v55, v59\n"
      "v_mfma_f32_16x16x32_bf16 v[40:43], a[40:43], a[76:79], 0\n"
      "ds_read_b128 a[52:55], v3 offset:64\n"
      "v_cvt_pk_bf16_f32 v57, v58, v59\n"
      "v_mfma_f32_16x16x32_bf16 v[24:27], a[16:19], v[180:183], v[24:27]\n"
      "ds_write_addtid_b32 v57 offset:47424\n"
      "v_cvt_pk_bf16_f32 v60, v60, v61\n"
      "v_mfma_f32_16x16x32_bf16 v[24:27], a[20:23], v[232:235], v[24:27]\n"
      "s_waitcnt lgkmcnt(7)\n"
      "ds_write_addtid_b32 v60 offset:9728\n"
      "v_cvt_pk_bf16_f32 v61, v62, v63\n"
      "v_mfma_f32_16x16x32_bf16 v[44:47], a[44:47], a[80:83], v[36:39]\n"
      "ds_write_addtid_b32 v61 offset:46656\n"
      "v_sub_f32_e32 v48, v36, v40\n"
      "v_mfma_f32_16x16x32_bf16 v[28:31], a[60:63], v[96:99], 0\n"
      "s_add_u32 m0, s46, 26560\n"
      "ds_read_b128 a[56:59], v4 offset:64\n"
      "v_sub_f32_e32 v49, v37, v41\n"
      "v_mfma_f32_16x16x32_bf16 v[28:31], a[64:67], v[152:155], v[28:31]\n"
      "s_add_u32 s51, s51, s12\n"
      "ds_read_b128 a[36:39], v2 offset:128\n"
      "v_cvt_pk_bf16_f32 v48, v48, v49\n"
      "v_mfma_f32_16x16x32_bf16 v[28:31], a[68:71], a[220:223], v[28:31]\n"
      "buffer_load_dwordx4 v16, s[32:35], s51 offen lds\n"
      "v_sub_f32_e32 v50, v38, v42\n"
      "v_mfma_f32_16x16x32_bf16 v[28:31], a[24:27], v[116:119], v[28:31]\n"
      "s_waitcnt lgkmcnt(0)\n"
      "s_cbranch_vccnz Lconv_h13\n"
      "s_barrier\n"
      "Lconv_h13:\n"
      "v_sub_f32_e32 v51, v39, v43\n"
      "v_mfma_f32_16x16x32_bf16 v[52:55], a[48:51], a[72:75], 0\n"
      "ds_read_b128 a[40:43], v3 offset:128\n"
      "v_cvt_pk_bf16_f32 v49, v50, v51\n"
      "v_mfma_f32_16x16x32_bf16 v[28:31], a[28:31], v[168:171], v[28:31]\n"
      "buffer_load_dwordx4 v17, s[32:35], s51 offen offset:1024 lds\n"
      "v_add_f32_e32 v40, v36, v40\n"
      "v_mfma_f32_16x16x32_bf16 v[28:31], a[32:35], v[224:227], v[28:31]\n"
      "ds_read_b128 a[44:47], v4 offset:128\n"
      "v_add_f32_e32 v41, v37, v41\n"
      "v_mfma_f32_16x16x32_bf16 v[56:59], a[52:55], a[76:79], 0\n"
      "s_cbranch_vccz Lconv_h14\n"
      "s_barrier\n"
      "Lconv_h14:\n"
      "v_cvt_pk_bf16_f32 v40, v40, v41\n"
      "v_mfma_f32_16x16x32_bf16 v[20:23], a[60:63], v[104:107], 0\n"
      "buffer_load_dwordx4 v18, s[32:35], s51 offen offset:2048 lds\n"
      "v_add_f32_e32 v42, v38, v42\n"
      "v_mfma_f32_16x16x32_bf16 v[20:23], a[64:67], v[156:159], v[20:23]\n"
      "s_mov_b32 m0, s18\n"
      "ds_read_b128 a[0:3], v7 offset:0\n"
      "v_add_f32_e32 v43, v39, v43\n"
      "v_mfma_f32_16x16x32_bf16 v[60:63], a[56:59], a[80:83], v[52:55]\n"
      "s_mov_b64 vcc, 0x2000000\n"
      "ds_write_addtid_b32 v48 offset:15360\n"
      "ds_write_addtid_b32 v49 offset:52288\n"
      "v_mfma_f32_16x16x32_bf16 v[20:23], a[68:71], v[212:215], v[20:23]\n"
      "ds_write_addtid_b32 v40 offset:14592\n"
      "v_cvt_pk_bf16_f32 v41, v42, v43\n"
      "v_mfma_f32_16x16x32_bf16 v[20:23], a[24:27], v[120:123], v[20:23]\n"
      "ds_write_addtid_b32 v41 offset:51520\n"
      "v_cvt_pk_bf16_f32 v44, v44, v45\n"
      "v_mfma_f32_16x16x32_bf16 v[20:23], a[28:31], v[176:179], v[20:23]\n"
      "ds_write_addtid_b32 v44 offset:13824\n"
      "v_cvt_pk_bf16_f32 v45, v46, v47\n"
      "v_mfma_f32_16x16x32_bf16 v[36:39], a[36:39], a[72:75], 0\n"
      "ds_read_b128 a[4:7], v7 offset:256\n"
      "v_sub_f32_e32 v64, v52, v56\n"
      "v_mfma_f32_16x16x32_bf16 v[20:23], a[32:35], a[232:235], v[20:23]\n"
      "ds_write_addtid_b32 v45 offset:50752\n"
      "v_sub_f32_e32 v65, v53, v57\n"
      "v_mfma_f32_16x16x32_bf16 v[32:35], a[60:63], v[108:111], 0\n"
      "s_waitcnt lgkmcnt(8)\n"
      "ds_read_b128 a[8:11], v7 offset:512\n"
      "v_cvt_pk_bf16_f32 v64, v64, v65\n"
      "v_mfma_f32_16x16x32_bf16 v[40:43], a[40:43], a[76:79], 0\n"
      "ds_write_addtid_b32 v64 offset:15616\n"
      "v_sub_f32_e32 v66, v54, v58\n"
      "v_mfma_f32_16x16x32_bf16 v[32:35], a[64:67], v[164:167], v[32:35]\n"
      "ds_read_b128 a[12:15], v7 offset:768\n"
      "v_sub_f32_e32 v67, v55, v59\n"
      "v_mfma_f32_16x16x32_bf16 v[32:35], a[68:71], a[224:227], v[32:35]\n"
      "v_cvt_pk_bf16_f32 v65, v66, v67\n"
      "v_add_f32_e32 v56, v52, v56\n"
      "v_mfma_f32_16x16x32_bf16 v[44:47], a[44:47], a[80:83], v[36:39]\n"
      "ds_write_addtid_b32 v65 offset:52544\n"
      "v_add_f32_e32 v57, v53, v57\n"
      "v_mfma_f32_16x16x32_bf16 v[32:35], a[24:27], v[128:131], v[32:35]\n"
      "ds_read_b128 a[16:19], v7 offset:1024\n"
      "v_cvt_pk_bf16_f32 v56, v56, v57\n"
      "v_mfma_f32_16x16x32_bf16 v[32:35], a[28:31], a[228:231], v[32:35]\n"
      "ds_write_addtid_b32 v56 offset:14848\n"
      "v_add_f32_e32 v58, v54, v58\n"
      "v_mfma_f32_16x16x32_bf16 v[32:35], a[32:35], v[236:239], v[32:35]\n"
      "s_waitcnt lgkmcnt(7)\n"
      "ds_read_b128 a[20:23], v7 offset:1280\n"
      "v_add_f32_e32 v59, v55, v59\n"
      "v_mfma_f32_16x16x32_bf16 v[68:71], a[0:3], a[160:163], v[68:71]\n"
      "v_cvt_pk_bf16_f32 v57, v58, v59\n"
      "v_cvt_pk_bf16_f32 v60, v60, v61\n"
      "v_mfma_f32_16x16x32_bf16 v[68:71], a[4:7], a[172:175], v[68:71]\n"
      "s_waitcnt lgkmcnt(6)\n"
      "ds_write_addtid_b32 v57 offset:51776\n"
      "v_cvt_pk_bf16_f32 v61, v62, v63\n"
      "v_mfma_f32_16x16x32_bf16 v[68:71], a[8:11], a[184:187], v[68:71]\n"
      "ds_write_addtid_b32 v60 offset:14080\n"
      "ds_write_addtid_b32 v61 offset:51008\n"
      "v_mfma_f32_16x16x32_bf16 v[72:75], a[0:3], a[164:167], v[72:75]\n"
      "v_sub_f32_e32 v48, v36, v40\n"
      "v_sub_f32_e32 v49, v37, v41\n"
      "v_mfma_f32_16x16x32_bf16 v[72:75], a[4:7], a[176:179], v[72:75]\n"
      "v_cvt_pk_bf16_f32 v48, v48, v49\n"
      "v_sub_f32_e32 v50, v38, v42\n"
      "v_mfma_f32_16x16x32_bf16 v[72:75], a[8:11], a[188:191], v[72:75]\n"
      "ds_write_addtid_b32 v48 offset:15872\n"
      "v_sub_f32_e32 v51, v39, v43\n"
      "v_mfma_f32_16x16x32_bf16 v[76:79], a[0:3], a[168:171], v[76:79]\n"
      "ds_read_b128 a[0:3], v7 offset:1536\n"
      "v_cvt_pk_bf16_f32 v49, v50, v51\n"
      "v_mfma_f32_16x16x32_bf16 v[76:79], a[4:7], a[180:183], v[76:79]\n"
      "ds_write_addtid_b32 v49 offset:52800\n"
      "v_add_f32_e32 v40, v36, v40\n"
      "v_mfma_f32_16x16x32_bf16 v[76:79], a[8:11], a[192:195], v[76:79]\n"
      "s_waitcnt lgkmcnt(6)\n"
      "ds_read_b128 a[4:7], v7 offset:1792\n"
      "v_add_f32_e32 v41, v37, v41\n"
      "v_mfma_f32_16x16x32_bf16 v[80:83], a[12:15], a[212:215], v[80:83]\n"
      "ds_read_b128 a[8:11], v7 offset:2048\n"
      "v_cvt_pk_bf16_f32 v40, v40, v41\n"
      "v_mfma_f32_16x16x32_bf16 v[80:83], a[16:19], v[184:187], v[80:83]\n"
      "ds_write_addtid_b32 v40 offset:15104\n"
      "v_add_f32_e32 v42, v38, v42\n"
      "v_mfma_f32_16x16x32_bf16 v[80:83], a[20:23], v[240:243], v[80:83]\n"
      "v_add_f32_e32 v43, v39, v43\n"
      "v_cvt_pk_bf16_f32 v41, v42, v43\n"
      "v_mfma_f32_16x16x32_bf16 v[84:87], a[12:15], v[136:139], v[84:87]\n"
      "ds_write_addtid_b32 v41 offset:52032\n"
      "v_cvt_pk_bf16_f32 v44, v44, v45\n"
      "v_mfma_f32_16x16x32_bf16 v[84:87], a[16:19], v[192:195], v[84:87]\n"
      "ds_write_addtid_b32 v44 offset:14336\n"
      "v_cvt_pk_bf16_f32 v45, v46, v47\n"
      "v_mfma_f32_16x16x32_bf16 v[84:87], a[20:23], v[244:247], v[84:87]\n"
      "ds_write_addtid_b32 v45 offset:51264\n"
      "v_cvt_pk_bf16_f32 v68, v68, v69\n"
      "v_mfma_f32_16x16x32_bf16 v[24:27], a[12:15], a[216:219], v[24:27]\n"
      "s_mov_b32 m0, s45\n"
      "v_cvt_pk_bf16_f32 v69, v70, v71\n"
      "v_cvt_pk_bf16_f32 v72, v72, v73\n"
      "v_mfma_f32_16x16x32_bf16 v[24:27], a[16:19], v[196:199], v[24:27]\n"
      "ds_write_addtid_b32 v68 offset:57360\n"
      "ds_write_addtid_b32 v69 offset:57616\n"
      "v_mfma_f32_16x16x32_bf16 v[24:27], a[20:23], v[252:255], v[24:27]\n"
      "s_waitcnt lgkmcnt(6)\n"
      "ds_write_addtid_b32 v72 offset:45064\n"
      "v_cvt_pk_bf16_f32 v73, v74, v75\n"
      "v_mfma_f32_16x16x32_bf16 v[28:31], a[0:3], v[132:135], v[28:31]\n"
      "ds_write_addtid_b32 v73 offset:45320\n"
      "v_cvt_pk_bf16_f32 v76, v76, v77\n"
      "v_mfma_f32_16x16x32_bf16 v[28:31], a[4:7], v[188:191], v[28:31]\n"
      "ds_write_addtid_b32 v76 offset:32768\n"
      "v_cvt_pk_bf16_f32 v77, v78, v79\n"
      "v_mfma_f32_16x16x32_bf16 v[28:31], a[8:11], a[240:243], v[28:31]\n"
      "ds_write_addtid_b32 v77 offset:33024\n"
      "v_cvt_pk_bf16_f32 v80, v80, v81\n"
      "v_mfma_f32_16x16x32_bf16 v[20:23], a[0:3], v[140:143], v[20:23]\n"
      "ds_write_addtid_b32 v80 offset:58384\n"
      "v_cvt_pk_bf16_f32 v81, v82, v83\n"
      "v_mfma_f32_16x16x32_bf16 v[20:23], a[4:7], a[236:239], v[20:23]\n"
      "ds_write_addtid_b32 v81 offset:58640\n"
      "v_cvt_pk_bf16_f32 v84, v84, v85\n"
      "v_mfma_f32_16x16x32_bf16 v[20:23], a[8:11], v[248:251], v[20:23]\n"
      "ds_write_addtid_b32 v84 offset:46088\n"
      "v_cvt_pk_bf16_f32 v85, v86, v87\n"
      "v_mfma_f32_16x16x32_bf16 v[32:35], a[0:3], v[144:147], v[32:35]\n"
      "ds_write_addtid_b32 v85 offset:46344\n"
      "v_cvt_pk_bf16_f32 v24, v24, v25\n"
      "v_mfma_f32_16x16x32_bf16 v[32:35], a[4:7], v[200:203], v[32:35]\n"
      "ds_write_addtid_b32 v24 offset:33792\n"
      "v_cvt_pk_bf16_f32 v25, v26, v27\n"
      "v_mfma_f32_16x16x32_bf16 v[32:35], a[8:11], a[244:247], v[32:35]\n"
      "ds_write_addtid_b32 v25 offset:34048\n"
      "v_cvt_pk_bf16_f32 v28, v28, v29\n"
      "v_cvt_pk_bf16_f32 v29, v30, v31\n"
      "ds_write_addtid_b32 v28 offset:59408\n"
      "ds_write_addtid_b32 v29 offset:59664\n"
      "v_cvt_pk_bf16_f32 v20, v20, v21\n"
      "v_cvt_pk_bf16_f32 v21, v22, v23\n"
      "ds_write_addtid_b32 v20 offset:47112\n"
      "ds_write_addtid_b32 v21 offset:47368\n"
      "s_add_u32 m0, s46, 39840\n"
      "s_add_u32 s51, s51, s12\n"
      "buffer_load_dwordx4 v16, s[32:35], s51 offen lds\n"
      "buffer_load_dwordx4 v17, s[32:35], s51 offen offset:1024 lds\n"
      "buffer_load_dwordx4 v18, s[32:35], s51 offen offset:2048 lds\n"
      "s_mov_b32 m0, s45\n"
      "s_waitcnt vmcnt(6)\n"
      "s_waitcnt lgkmcnt(0)\n"
      "s_cmp_eq_u32 s41, 1\n"
      "s_cbranch_scc1 Lconv_s1\n"
      "Lconv_s0:\n"
      "s_branch Lconv_loop0\n"
      "Lconv_s1:\n"
      "v_cvt_pk_bf16_f32 v32, v32, v33\n"
      "s_branch Lconv_loop1\n"
      ".p2align 12\n"
      "Lconv_pa0:\n"
      "s_cbranch_execz Lconv_r0\n"
      "s_nop 0\n"
      "Lconv_loop0:\n"
      "v_mfma_f32_16x16x32_bf16 v[20:23], a[24:27], v[96:99], 0\n"
      "s_add_u32 s51, s51, s12\n"
      "s_nop 1\n"
      "v_mfma_f32_16x16x32_bf16 v[20:23], a[28:31], v[152:155], v[20:23]\n"
      "s_sub_u32 s58, s93, s58\n"
      "v_cvt_pk_bf16_f32 v32, v32, v33\n"
      "v_cvt_pk_bf16_f32 v33, v34, v35\n"
      "v_mfma_f32_16x16x32_bf16 v[20:23], a[32:35], a[220:223], v[20:23]\n"
      "s_sub_u32 s59, s94, s59\n"
      "ds_write_addtid_b32 v32 offset:34816\n"
      "v_mfma_f32_16x16x32_bf16 v[20:23], a[0:3], v[116:119], v[20:23]\n"
      "s_sub_u32 s47, s51, s48\n"
      "ds_write_addtid_b32 v33 offset:35072\n"
      "v_mfma_f32_16x16x32_bf16 v[20:23], a[4:7], v[168:171], v[20:23]\n"
      "s_cmp_ge_u32 vcc_lo, 0x80000000\n"
      "v_mfma_f32_16x16x32_bf16 v[20:23], a[8:11], v[224:227], v[20:23]\n"
      "s_cselect_b32 s53, s47, s51\n"
      "v_mfma_f32_16x16x32_bf16 v[24:27], a[24:27], v[104:107], 0\n"
      "s_waitcnt vmcnt(8) lgkmcnt(0)\n"
      "s_barrier\n"
      "v_mfma_f32_16x16x32_bf16 v[24:27], a[28:31], v[156:159], v[24:27]\n"
      "s_mov_b32 m0, s58\n"
      "ds_read_b128 a[12:15], v7 offset:6144\n"
      "v_mfma_f32_16x16x32_bf16 v[24:27], a[32:35], v[212:215], v[24:27]\n"
      "ds_read_b128 a[16:19], v7 offset:6400\n"
      "v_mfma_f32_16x16x32_bf16 v[24:27], a[0:3], v[120:123], v[24:27]\n"
      "ds_read_b128 a[20:23], v7 offset:6656\n"
      "v_mfma_f32_16x16x32_bf16 v[24:27], a[4:7], v[176:179], v[24:27]\n"
      "ds_read_b128 a[48:51], v2 offset:13280\n"
      "v_mfma_f32_16x16x32_bf16 v[24:27], a[8:11], a[232:235], v[24:27]\n"
      "ds_read_b128 a[52:55], v3 offset:13280\n"
      "v_mfma_f32_16x16x32_bf16 v[68:71], a[84:87], a[60:63], 0\n"
      "ds_read_b128 a[56:59], v4 offset:13280\n"
      "s_nop 0\n"
      "v_mfma_f32_16x16x32_bf16 v[28:31], a[24:27], v[108:111], 0\n"
      "ds_read2_b64 a[64:67], v10 offset0:0 offset1:64\n"
      "s_nop 0\n"
      "v_mfma_f32_16x16x32_bf16 v[28:31], a[28:31], v[164:167], v[28:31]\n"
      "buffer_load_dwordx4 v16, s[32:35], s53 offen lds\n"
      "v_mfma_f32_16x16x32_bf16 v[28:31], a[32:35], a[224:227], v[28:31]\n"
      "s_mov_b32 m0, s45\n"
      "ds_read2_b64 a[68:71], v10 offset0:1 offset1:65\n"
      "v_cvt_pk_bf16_f32 v85, v68, v69\n"
      "v_mfma_f32_16x16x32_bf16 v[28:31], a[0:3], v[128:131], v[28:31]\n"
      "ds_read_b128 a[24:27], v6 offset:5376\n"
      "v_cvt_pk_bf16_f32 v91, v70, v71\n"
      "v_mfma_f32_16x16x32_bf16 v[28:31], a[4:7], a[228:231], v[28:31]\n"
      "ds_read_b128 a[28:31], v6 offset:5632\n"
      "v_mfma_f32_16x16x32_bf16 v[28:31], a[8:11], v[236:239], v[28:31]\n"
      "s_waitcnt lgkmcnt(6)\n"
      "ds_read_b128 a[32:35], v6 offset:5888\n"
      "v_mfma_f32_16x16x32_bf16 v[20:23], a[12:15], v[132:135], v[20:23]\n"
      "buffer_store_dwordx2 v[84:85], v15, s[36:39], s52 offen sc1\n"
      "v_mfma_f32_16x16x32_bf16 v[20:23], a[16:19], v[188:191], v[20:23]\n"
      "ds_read_b128 a[0:3], v7 offset:768\n"
      "v_mfma_f32_16x16x32_bf16 v[20:23], a[20:23], a[240:243], v[20:23]\n"
      "ds_read_b128 a[4:7], v7 offset:1024\n"
      "s_nop 0\n"
      "v_mfma_f32_16x16x32_bf16 v[52:55], a[48:51], a[72:75], 0\n"
      "s_waitcnt lgkmcnt(6)\n"
      "ds_read_b128 a[8:11], v7 offset:1280\n"
      "s_nop 0\n"
      "v_mfma_f32_16x16x32_bf16 v[56:59], a[52:55], a[76:79], 0\n"
      "ds_read_b128 a[36:39], v2 offset:13344\n"
      "v_cvt_pk_bf16_f32 v20, v20, v21\n"
      "v_mfma_f32_16x16x32_bf16 v[60:63], a[56:59], a[80:83], v[52:55]\n"
      "buffer_store_dwordx2 v[90:91], v15, s[36:39], s52 offen offset:1536 sc1\n"
      "v_cvt_pk_bf16_f32 v21, v22, v23\n"
      "v_mfma_f32_16x16x32_bf16 v[24:27], a[12:15], v[140:143], v[24:27]\n"
      "s_mov_b32 s52, s13\n"
      "ds_write_addtid_b32 v20 offset:62480\n"
      "v_sub_f32_e32 v64, v52, v56\n"
      "v_mfma_f32_16x16x32_bf16 v[24:27], a[16:19], a[236:239], v[24:27]\n"
      "s_add_u32 s13, s13, s12\n"
      "ds_read_b128 a[40:43], v3 offset:13344\n"
      "v_sub_f32_e32 v65, v53, v57\n"
      "v_mfma_f32_16x16x32_bf16 v[24:27], a[20:23], v[248:251], v[24:27]\n"
      "s_sub_u32 s96, s53, s95\n"
      "ds_write_addtid_b32 v21 offset:62736\n"
      "v_cvt_pk_bf16_f32 v64, v64, v65\n"
      "v_mfma_f32_16x16x32_bf16 v[72:75], a[84:87], a[64:67], 0\n"
      "ds_read_b128 a[44:47], v4 offset:13344\n"
      "v_sub_f32_e32 v66, v54, v58\n"
      "v_mfma_f32_16x16x32_bf16 v[28:31], a[12:15], v[144:147], v[28:31]\n"
      "ds_read_b128 a[12:15], v7 offset:5376\n"
      "v_cvt_pk_bf16_f32 v24, v24, v25\n"
      "v_mfma_f32_16x16x32_bf16 v[28:31], a[16:19], v[200:203], v[28:31]\n"
      "ds_read_b128 a[16:19], v7 offset:5632\n"
      "v_cvt_pk_bf16_f32 v25, v26, v27\n"
      "v_mfma_f32_16x16x32_bf16 v[28:31], a[20:23], a[244:247], v[28:31]\n"
      "s_waitcnt lgkmcnt(6)\n"
      "ds_read_b128 a[20:23], v7 offset:5888\n"
      "v_sub_f32_e32 v67, v55, v59\n"
      "v_mfma_f32_16x16x32_bf16 v[76:79], a[84:87], a[68:71], 0\n"
      "ds_read2_b64 a[60:63], v10 offset0:2 offset1:66\n"
      "v_cvt_pk_bf16_f32 v65, v66, v67\n"
      "v_mfma_f32_16x16x32_bf16 v[32:35], a[24:27], v[92:95], 0\n"
      "ds_write_addtid_b32 v24 offset:50184\n"
      "ds_write_addtid_b32 v25 offset:50440\n"
      "v_mfma_f32_16x16x32_bf16 v[32:35], a[28:31], v[148:151], v[32:35]\n"
      "s_mov_b32 m0, s18\n"
      "v_add_f32_e32 v56, v52, v56\n"
      "v_add_f32_e32 v57, v53, v57\n"
      "v_mfma_f32_16x16x32_bf16 v[32:35], a[32:35], v[204:207], v[32:35]\n"
      "ds_write_addtid_b32 v64 offset:1536\n"
      "v_add_f32_e32 v58, v54, v58\n"
      "v_mfma_f32_16x16x32_bf16 v[32:35], a[0:3], v[112:115], v[32:35]\n"
      "ds_write_addtid_b32 v65 offset:38464\n"
      "v_add_f32_e32 v59, v55, v59\n"
      "v_mfma_f32_16x16x32_bf16 v[32:35], a[4:7], a[208:211], v[32:35]\n"
      "v_cvt_pk_bf16_f32 v56, v56, v57\n"
      "v_cvt_pk_bf16_f32 v57, v58, v59\n"
      "v_mfma_f32_16x16x32_bf16 v[32:35], a[8:11], v[220:223], v[32:35]\n"
      "s_waitcnt lgkmcnt(6)\n"
      "ds_write_addtid_b32 v56 offset:768\n"
      "v_cvt_pk_bf16_f32 v60, v60, v61\n"
      "v_mfma_f32_16x16x32_bf16 v[32:35], a[12:15], a[212:215], v[32:35]\n"
      "ds_write_addtid_b32 v57 offset:37696\n"
      "v_cvt_pk_bf16_f32 v61, v62, v63\n"
      "v_mfma_f32_16x16x32_bf16 v[32:35], a[16:19], v[184:187], v[32:35]\n"
      "s_waitcnt lgkmcnt(6)\n"
      "ds_write_addtid_b32 v60 offset:0\n"
      "ds_write_addtid_b32 v61 offset:36928\n"
      "v_mfma_f32_16x16x32_bf16 v[32:35], a[20:23], v[240:243], v[32:35]\n"
      "s_mov_b32 m0, s45\n"
      "ds_read_b128 a[48:51], v2 offset:13408\n"
      "v_cvt_pk_bf16_f32 v28, v28, v29\n"
      "v_mfma_f32_16x16x32_bf16 v[20:23], a[24:27], v[100:103], 0\n"
      "ds_write_addtid_b32 v28 offset:37888\n"
      "v_cvt_pk_bf16_f32 v29, v30, v31\n"
      "v_mfma_f32_16x16x32_bf16 v[20:23], a[28:31], a[200:203], v[20:23]\n"
      "ds_write_addtid_b32 v29 offset:38144\n"
      "v_cvt_pk_bf16_f32 v32, v32, v33\n"
      "v_mfma_f32_16x16x32_bf16 v[20:23], a[32:35], v[208:211], v[20:23]\n"
      "s_mov_b32 m0, s58\n"
      "ds_read_b128 a[56:59], v4 offset:13408\n"
      "v_cvt_pk_bf16_f32 v33, v34, v35\n"
      "v_mfma_f32_16x16x32_bf16 v[20:23], a[0:3], a[204:207], v[20:23]\n"
      "buffer_load_dwordx4 v17, s[32:35], s53 offen offset:1024 lds\n"
      "v_cvt_pk_bf16_f32 v80, v72, v73\n"
      "v_mfma_f32_16x16x32_bf16 v[20:23], a[4:7], v[172:175], v[20:23]\n"
      "s_mov_b32 m0, s45\n"
      "ds_read2_b64 a[64:67], v10 offset0:3 offset1:67\n"
      "v_cvt_pk_bf16_f32 v81, v76, v77\n"
      "v_mfma_f32_16x16x32_bf16 v[20:23], a[8:11], v[228:231], v[20:23]\n"
      "ds_write_addtid_b32 v32 offset:61456\n"
      "v_cvt_pk_bf16_f32 v86, v74, v75\n"
      "v_mfma_f32_16x16x32_bf16 v[20:23], a[12:15], v[136:139], v[20:23]\n"
      "ds_write_addtid_b32 v33 offset:61712\n"
      "v_cvt_pk_bf16_f32 v87, v78, v79\n"
      "v_mfma_f32_16x16x32_bf16 v[20:23], a[16:19], v[192:195], v[20:23]\n"
      "ds_read_b128 a[52:55], v3 offset:13408\n"
      "v_add_u32_e64 v2, s91, v2\n"
      "v_mfma_f32_16x16x32_bf16 v[20:23], a[20:23], v[244:247], v[20:23]\n"
      "ds_read2_b64 a[68:71], v12 offset0:0 offset1:64\n"
      "v_add_u32_e64 v3, s91, v3\n"
      "v_mfma_f32_16x16x32_bf16 v[36:39], a[36:39], a[72:75], 0\n"
      "v_add_u32_e32 v4, s91, v4\n"
      "s_nop 0\n"
      "v_mfma_f32_16x16x32_bf16 v[40:43], a[40:43], a[76:79], 0\n"
      "s_sub_u32 s91, 0, s91\n"
      "v_mfma_f32_16x16x32_bf16 v[44:47], a[44:47], a[80:83], v[36:39]\n"
      "v_cvt_pk_bf16_f32 v20, v20, v21\n"
      "v_cvt_pk_bf16_f32 v21, v22, v23\n"
      "v_mfma_f32_16x16x32_bf16 v[68:71], a[84:87], a[60:63], 0\n"
      "ds_write_addtid_b32 v20 offset:49160\n"
      "s_nop 0\n"
      "v_mfma_f32_16x16x32_bf16 v[24:27], a[24:27], a[196:199], 0\n"
      "ds_read_b128 a[24:27], v7 offset:4608\n"
      "v_sub_f32_e32 v48, v36, v40\n"
      "v_mfma_f32_16x16x32_bf16 v[24:27], a[28:31], v[160:163], v[24:27]\n"
      "ds_read_b128 a[28:31], v7 offset:4864\n"
      "v_sub_f32_e32 v49, v37, v41\n"
      "v_mfma_f32_16x16x32_bf16 v[24:27], a[32:35], v[216:219], v[24:27]\n"
      "ds_read_b128 a[32:35], v7 offset:5120\n"
      "v_cvt_pk_bf16_f32 v48, v48, v49\n"
      "v_mfma_f32_16x16x32_bf16 v[24:27], a[0:3], v[124:127], v[24:27]\n"
      "ds_read_b128 a[0:3], v6 offset:4608\n"
      "v_sub_f32_e32 v50, v38, v42\n"
      "v_mfma_f32_16x16x32_bf16 v[24:27], a[4:7], v[180:183], v[24:27]\n"
      "ds_read_b128 a[4:7], v6 offset:4864\n"
      "v_sub_f32_e32 v51, v39, v43\n"
      "v_mfma_f32_16x16x32_bf16 v[24:27], a[8:11], v[232:235], v[24:27]\n"
      "ds_read_b128 a[8:11], v6 offset:5120\n"
      "v_cvt_pk_bf16_f32 v49, v50, v51\n"
      "v_mfma_f32_16x16x32_bf16 v[24:27], a[12:15], a[216:219], v[24:27]\n"
      "ds_read_b128 a[12:15], v7 offset:0\n"
      "v_add_f32_e32 v40, v36, v40\n"
      "v_mfma_f32_16x16x32_bf16 v[24:27], a[16:19], v[196:199], v[24:27]\n"
      "ds_read_b128 a[16:19], v7 offset:256\n"
      "v_add_f32_e32 v41, v37, v41\n"
      "v_mfma_f32_16x16x32_bf16 v[24:27], a[20:23], v[252:255], v[24:27]\n"
      "s_waitcnt lgkmcnt(7)\n"
      "ds_read_b128 a[20:23], v7 offset:512\n"
      "v_cvt_pk_bf16_f32 v40, v40, v41\n"
      "v_mfma_f32_16x16x32_bf16 v[52:55], a[48:51], a[72:75], 0\n"
      "ds_write_addtid_b32 v21 offset:49416\n"
      "v_add_f32_e32 v42, v38, v42\n"
      "v_mfma_f32_16x16x32_bf16 v[60:63], a[56:59], a[80:83], v[52:55]\n"
      "s_mov_b32 m0, s58\n"
      "v_cvt_pk_bf16_f32 v24, v24, v25\n"
      "v_cvt_pk_bf16_f32 v25, v26, v27\n"
      "v_mfma_f32_16x16x32_bf16 v[72:75], a[84:87], a[64:67], 0\n"
      "buffer_load_dwordx4 v18, s[32:35], s53 offen offset:2048 lds\n"
      "v_add_f32_e32 v43, v39, v43\n"
      "v_mfma_f32_16x16x32_bf16 v[28:31], a[24:27], a[160:163], 0\n"
      "s_waitcnt lgkmcnt(5)\n"
      "v_cvt_pk_bf16_f32 v41, v42, v43\n"
      "v_cvt_pk_bf16_f32 v44, v44, v45\n"
      "v_mfma_f32_16x16x32_bf16 v[28:31], a[28:31], a[172:175], v[28:31]\n"
      "s_mov_b32 m0, s45\n"
      "v_cvt_pk_bf16_f32 v82, v68, v69\n"
      "v_cvt_pk_bf16_f32 v83, v72, v73\n"
      "v_mfma_f32_16x16x32_bf16 v[28:31], a[32:35], a[184:187], v[28:31]\n"
      "ds_write_addtid_b32 v24 offset:36864\n"
      "ds_write_addtid_b32 v25 offset:37120\n"
      "v_mfma_f32_16x16x32_bf16 v[28:31], a[0:3], a[88:91], v[28:31]\n"
      "s_mov_b32 m0, s18\n"
      "v_cvt_pk_bf16_f32 v45, v46, v47\n"
      "v_cvt_pk_bf16_f32 v88, v70, v71\n"
      "v_mfma_f32_16x16x32_bf16 v[28:31], a[4:7], a[100:103], v[28:31]\n"
      "s_waitcnt lgkmcnt(3)\n"
      "ds_write_addtid_b32 v48 offset:1792\n"
      "ds_write_addtid_b32 v49 offset:38720\n"
      "v_mfma_f32_16x16x32_bf16 v[28:31], a[8:11], a[112:115], v[28:31]\n"
      "ds_write_addtid_b32 v40 offset:1024\n"
      "ds_write_addtid_b32 v41 offset:37952\n"
      "v_mfma_f32_16x16x32_bf16 v[28:31], a[12:15], a[124:127], v[28:31]\n"
      "ds_write_addtid_b32 v44 offset:256\n"
      "ds_write_addtid_b32 v45 offset:37184\n"
      "v_mfma_f32_16x16x32_bf16 v[28:31], a[16:19], a[136:139], v[28:31]\n"
      "buffer_store_dwordx4 v[80:83], v14, s[36:39], s52 offen sc1\n"
      "v_cvt_pk_bf16_f32 v89, v74, v75\n"
      "v_mfma_f32_16x16x32_bf16 v[28:31], a[20:23], a[148:151], v[28:31]\n"
      "s_mov_b32 m0, s45\n"
      "ds_read2_b64 a[60:63], v12 offset0:1 offset1:65\n"
      "v_cvt_pk_bf16_f32 v60, v60, v61\n"
      "v_mfma_f32_16x16x32_bf16 v[56:59], a[52:55], a[76:79], 0\n"
      "v_cvt_pk_bf16_f32 v61, v62, v63\n"
      "v_swap_b32 v6, v7\n"
      "v_mfma_f32_16x16x32_bf16 v[20:23], a[24:27], a[164:167], 0\n"
      "v_mfma_f32_16x16x32_bf16 v[20:23], a[28:31], a[176:179], v[20:23]\n"
      "v_cvt_pk_bf16_f32 v28, v28, v29\n"
      "v_cvt_pk_bf16_f32 v29, v30, v31\n"
      "v_mfma_f32_16x16x32_bf16 v[20:23], a[32:35], a[188:191], v[20:23]\n"
      "buffer_store_dwordx4 v[86:89], v14, s[36:39], s52 offen offset:1536 sc1\n"
      "v_sub_f32_e32 v64, v52, v56\n"
      "v_mfma_f32_16x16x32_bf16 v[20:23], a[0:3], a[92:95], v[20:23]\n"
      "ds_write_addtid_b32 v28 offset:60432\n"
      "ds_write_addtid_b32 v29 offset:60688\n"
      "v_mfma_f32_16x16x32_bf16 v[20:23], a[4:7], a[104:107], v[20:23]\n"
      "s_mov_b32 m0, s18\n"
      "v_sub_f32_e32 v65, v53, v57\n"
      "v_cvt_pk_bf16_f32 v64, v64, v65\n"
      "v_mfma_f32_16x16x32_bf16 v[20:23], a[8:11], a[116:119], v[20:23]\n"
      "ds_write_addtid_b32 v64 offset:2048\n"
      "v_sub_f32_e32 v66, v54, v58\n"
      "v_mfma_f32_16x16x32_bf16 v[20:23], a[12:15], a[128:131], v[20:23]\n"
      "v_sub_f32_e32 v67, v55, v59\n"
      "v_cvt_pk_bf16_f32 v65, v66, v67\n"
      "v_mfma_f32_16x16x32_bf16 v[20:23], a[16:19], a[140:143], v[20:23]\n"
      "ds_write_addtid_b32 v65 offset:38976\n"
      "v_add_f32_e32 v56, v52, v56\n"
      "v_mfma_f32_16x16x32_bf16 v[20:23], a[20:23], a[152:155], v[20:23]\n"
      "v_add_f32_e32 v57, v53, v57\n"
      "v_cvt_pk_bf16_f32 v56, v56, v57\n"
      "v_mfma_f32_16x16x32_bf16 v[76:79], a[84:87], a[68:71], 0\n"
      "ds_write_addtid_b32 v56 offset:1280\n"
      "v_add_f32_e32 v58, v54, v58\n"
      "v_mfma_f32_16x16x32_bf16 v[32:35], a[24:27], a[168:171], 0\n"
      "ds_write_addtid_b32 v60 offset:512\n"
      "v_add_f32_e32 v59, v55, v59\n"
      "v_mfma_f32_16x16x32_bf16 v[32:35], a[28:31], a[180:183], v[32:35]\n"
      "ds_write_addtid_b32 v61 offset:37440\n"
      "v_cvt_pk_bf16_f32 v57, v58, v59\n"
      "v_mfma_f32_16x16x32_bf16 v[32:35], a[32:35], a[192:195], v[32:35]\n"
      "ds_write_addtid_b32 v57 offset:38208\n"
      "v_cvt_pk_bf16_f32 v20, v20, v21\n"
      "v_mfma_f32_16x16x32_bf16 v[32:35], a[0:3], a[96:99], v[32:35]\n"
      "s_mov_b32 m0, s45\n"
      "v_cvt_pk_bf16_f32 v21, v22, v23\n"
      "v_cvt_pk_bf16_f32 v84, v76, v77\n"
      "v_mfma_f32_16x16x32_bf16 v[32:35], a[4:7], a[108:111], v[32:35]\n"
      "s_mov_b32 m0, s59\n"
      "v_cvt_pk_bf16_f32 v90, v78, v79\n"
      "v_mfma_f32_16x16x32_bf16 v[32:35], a[8:11], a[120:123], v[32:35]\n"
      "buffer_load_dwordx4 v19, s[32:35], s96 offen lds\n"
      "v_mfma_f32_16x16x32_bf16 v[32:35], a[12:15], a[132:135], v[32:35]\n"
      "s_mov_b32 m0, s45\n"
      "v_mfma_f32_16x16x32_bf16 v[32:35], a[16:19], a[144:147], v[32:35]\n"
      "ds_write_addtid_b32 v20 offset:48136\n"
      "ds_write_addtid_b32 v21 offset:48392\n"
      "v_mfma_f32_16x16x32_bf16 v[32:35], a[20:23], a[156:159], v[32:35]\n"
      "s_nop 1\n"
      "v_mfma_f32_16x16x32_bf16 v[20:23], a[12:15], a[88:91], 0\n"
      "s_add_u32 s51, s51, s12\n"
      "s_nop 1\n"
      "v_mfma_f32_16x16x32_bf16 v[20:23], a[16:19], a[100:103], v[20:23]\n"
      "s_sub_u32 s47, s51, s48\n"
      "v_cvt_pk_bf16_f32 v32, v32, v33\n"
      "v_cvt_pk_bf16_f32 v33, v34, v35\n"
      "v_mfma_f32_16x16x32_bf16 v[20:23], a[20:23], a[112:115], v[20:23]\n"
      "s_cmp_ge_u32 vcc_lo, 0x40000000\n"
      "ds_write_addtid_b32 v32 offset:35840\n"
      "v_mfma_f32_16x16x32_bf16 v[20:23], a[24:27], a[124:127], v[20:23]\n"
      "s_cselect_b32 s53, s47, s51\n"
      "ds_write_addtid_b32 v33 offset:36096\n"
      "v_mfma_f32_16x16x32_bf16 v[20:23], a[28:31], a[136:139], v[20:23]\n"
      "s_mul_i32 vcc_lo, vcc_lo, 2\n"
      "v_mfma_f32_16x16x32_bf16 v[20:23], a[32:35], a[148:151], v[20:23]\n"
      "v_mfma_f32_16x16x32_bf16 v[24:27], a[12:15], a[92:95], 0\n"
      "s_waitcnt vmcnt(11) lgkmcnt(0)\n"
      "s_barrier\n"
      "v_mfma_f32_16x16x32_bf16 v[24:27], a[16:19], a[104:107], v[24:27]\n"
      "s_add_u32 m0, s58, 13280\n"
      "ds_read_b128 a[0:3], v7 offset:0\n"
      "v_mfma_f32_16x16x32_bf16 v[24:27], a[20:23], a[116:119], v[24:27]\n"
      "ds_read_b128 a[4:7], v7 offset:256\n"
      "v_mfma_f32_16x16x32_bf16 v[24:27], a[24:27], a[128:131], v[24:27]\n"
      "ds_read_b128 a[8:11], v7 offset:512\n"
      "v_mfma_f32_16x16x32_bf16 v[24:27], a[28:31], a[140:143], v[24:27]\n"
      "ds_read_b128 a[36:39], v2 offset:0\n"
      "v_mfma_f32_16x16x32_bf16 v[24:27], a[32:35], a[152:155], v[24:27]\n"
      "ds_read_b128 a[40:43], v3 offset:0\n"
      "v_mfma_f32_16x16x32_bf16 v[68:71], a[84:87], a[60:63], 0\n"
      "ds_read_b128 a[44:47], v4 offset:0\n"
      "s_nop 0\n"
      "v_mfma_f32_16x16x32_bf16 v[28:31], a[12:15], a[96:99], 0\n"
      "ds_read2_b64 a[64:67], v11 offset0:0 offset1:64\n"
      "s_nop 0\n"
      "v_mfma_f32_16x16x32_bf16 v[28:31], a[16:19], a[108:111], v[28:31]\n"
      "buffer_load_dwordx4 v16, s[32:35], s53 offen lds\n"
      "v_mfma_f32_16x16x32_bf16 v[28:31], a[20:23], a[120:123], v[28:31]\n"
      "s_mov_b32 m0, s45\n"
      "ds_read2_b64 a[68:71], v11 offset0:1 offset1:65\n"
      "v_cvt_pk_bf16_f32 v85, v68, v69\n"
      "v_mfma_f32_16x16x32_bf16 v[28:31], a[24:27], a[132:135], v[28:31]\n"
      "ds_read_b128 a[12:15], v6 offset:768\n"
      "v_cvt_pk_bf16_f32 v91, v70, v71\n"
      "v_mfma_f32_16x16x32_bf16 v[28:31], a[28:31], a[144:147], v[28:31]\n"
      "ds_read_b128 a[16:19], v6 offset:1024\n"
      "v_mfma_f32_16x16x32_bf16 v[28:31], a[32:35], a[156:159], v[28:31]\n"
      "s_waitcnt lgkmcnt(6)\n"
      "ds_read_b128 a[20:23], v6 offset:1280\n"
      "v_mfma_f32_16x16x32_bf16 v[20:23], a[0:3], a[160:163], v[20:23]\n"
      "buffer_store_dwordx2 v[84:85], v15, s[36:39], s52 offen sc1\n"
      "v_mfma_f32_16x16x32_bf16 v[20:23], a[4:7], a[172:175], v[20:23]\n"
      "ds_read_b128 a[24:27], v6 offset:5376\n"
      "v_mfma_f32_16x16x32_bf16 v[20:23], a[8:11], a[184:187], v[20:23]\n"
      "ds_read_b128 a[28:31], v6 offset:5632\n"
      "s_nop 0\n"
      "v_mfma_f32_16x16x32_bf16 v[36:39], a[36:39], a[72:75], 0\n"
      "s_waitcnt lgkmcnt(6)\n"
      "ds_read_b128 a[32:35], v6 offset:5888\n"
      "s_nop 0\n"
      "v_mfma_f32_16x16x32_bf16 v[40:43], a[40:43], a[76:79], 0\n"
      "ds_read_b128 a[48:51], v2 offset:64\n"
      "v_cvt_pk_bf16_f32 v20, v20, v21\n"
      "v_mfma_f32_16x16x32_bf16 v[44:47], a[44:47], a[80:83], v[36:39]\n"
      "buffer_store_dwordx2 v[90:91], v15, s[36:39], s52 offen offset:1536 sc1\n"
      "v_cvt_pk_bf16_f32 v21, v22, v23\n"
      "v_mfma_f32_16x16x32_bf16 v[24:27], a[0:3], a[164:167], v[24:27]\n"
      "s_mov_b32 s52, s13\n"
      "ds_write_addtid_b32 v20 offset:57360\n"
      "v_sub_f32_e32 v48, v36, v40\n"
      "v_mfma_f32_16x16x32_bf16 v[24:27], a[4:7], a[176:179], v[24:27]\n"
      "s_add_u32 s13, s13, s12\n"
      "ds_read_b128 a[52:55], v3 offset:64\n"
      "v_sub_f32_e32 v49, v37, v41\n"
      "v_mfma_f32_16x16x32_bf16 v[24:27], a[8:11], a[188:191], v[24:27]\n"
      "ds_write_addtid_b32 v21 offset:57616\n"
      "v_cvt_pk_bf16_f32 v48, v48, v49\n"
      "v_mfma_f32_16x16x32_bf16 v[72:75], a[84:87], a[64:67], 0\n"
      "ds_read_b128 a[56:59], v4 offset:64\n"
      "v_sub_f32_e32 v50, v38, v42\n"
      "v_mfma_f32_16x16x32_bf16 v[28:31], a[0:3], a[168:171], v[28:31]\n"
      "ds_read_b128 a[0:3], v7 offset:768\n"
      "v_sub_f32_e32 v51, v39, v43\n"
      "v_mfma_f32_16x16x32_bf16 v[28:31], a[4:7], a[180:183], v[28:31]\n"
      "ds_read_b128 a[4:7], v7 offset:1024\n"
      "v_cvt_pk_bf16_f32 v24, v24, v25\n"
      "v_mfma_f32_16x16x32_bf16 v[28:31], a[8:11], a[192:195], v[28:31]\n"
      "s_waitcnt lgkmcnt(6)\n"
      "ds_read_b128 a[8:11], v7 offset:1280\n"
      "v_cvt_pk_bf16_f32 v25, v26, v27\n"
      "v_mfma_f32_16x16x32_bf16 v[76:79], a[84:87], a[68:71], 0\n"
      "ds_read2_b64 a[60:63], v11 offset0:2 offset1:66\n"
      "v_cvt_pk_bf16_f32 v49, v50, v51\n"
      "v_mfma_f32_16x16x32_bf16 v[32:35], a[12:15], v[92:95], 0\n"
      "ds_write_addtid_b32 v24 offset:45064\n"
      "ds_write_addtid_b32 v25 offset:45320\n"
      "v_mfma_f32_16x16x32_bf16 v[32:35], a[16:19], v[148:151], v[32:35]\n"
      "s_mov_b32 m0, s18\n"
      "v_add_f32_e32 v40, v36, v40\n"
      "v_add_f32_e32 v41, v37, v41\n"
      "v_mfma_f32_16x16x32_bf16 v[32:35], a[20:23], v[204:207], v[32:35]\n"
      "ds_write_addtid_b32 v48 offset:6144\n"
      "v_add_f32_e32 v42, v38, v42\n"
      "v_mfma_f32_16x16x32_bf16 v[32:35], a[24:27], v[112:115], v[32:35]\n"
      "ds_write_addtid_b32 v49 offset:43072\n"
      "v_add_f32_e32 v43, v39, v43\n"
      "v_mfma_f32_16x16x32_bf16 v[32:35], a[28:31], a[208:211], v[32:35]\n"
      "v_cvt_pk_bf16_f32 v40, v40, v41\n"
      "v_cvt_pk_bf16_f32 v41, v42, v43\n"
      "v_mfma_f32_16x16x32_bf16 v[32:35], a[32:35], v[220:223], v[32:35]\n"
      "s_waitcnt lgkmcnt(6)\n"
      "ds_write_addtid_b32 v40 offset:5376\n"
      "v_cvt_pk_bf16_f32 v44, v44, v45\n"
      "v_mfma_f32_16x16x32_bf16 v[32:35], a[0:3], a[212:215], v[32:35]\n"
      "ds_write_addtid_b32 v41 offset:42304\n"
      "v_cvt_pk_bf16_f32 v45, v46, v47\n"
      "v_mfma_f32_16x16x32_bf16 v[32:35], a[4:7], v[184:187], v[32:35]\n"
      "s_waitcnt lgkmcnt(6)\n"
      "ds_write_addtid_b32 v44 offset:4608\n"
      "ds_write_addtid_b32 v45 offset:41536\n"
      "v_mfma_f32_16x16x32_bf16 v[32:35], a[8:11], v[240:243], v[32:35]\n"
      "s_mov_b32 m0, s45\n"
      "ds_read_b128 a[36:39], v2 offset:128\n"
      "v_cvt_pk_bf16_f32 v28, v28, v29\n"
      "v_mfma_f32_16x16x32_bf16 v[20:23], a[12:15], v[100:103], 0\n"
      "ds_write_addtid_b32 v28 offset:32768\n"
      "v_cvt_pk_bf16_f32 v29, v30, v31\n"
      "v_mfma_f32_16x16x32_bf16 v[20:23], a[16:19], a[200:203], v[20:23]\n"
      "ds_write_addtid_b32 v29 offset:33024\n"
      "v_cvt_pk_bf16_f32 v32, v32, v33\n"
      "v_mfma_f32_16x16x32_bf16 v[20:23], a[20:23], v[208:211], v[20:23]\n"
      "s_add_u32 m0, s58, 13280\n"
      "ds_read_b128 a[44:47], v4 offset:128\n"
      "v_cvt_pk_bf16_f32 v33, v34, v35\n"
      "v_mfma_f32_16x16x32_bf16 v[20:23], a[24:27], a[204:207], v[20:23]\n"
      "buffer_load_dwordx4 v17, s[32:35], s53 offen offset:1024 lds\n"
      "v_cvt_pk_bf16_f32 v80, v72, v73\n"
      "v_mfma_f32_16x16x32_bf16 v[20:23], a[28:31], v[172:175], v[20:23]\n"
      "s_mov_b32 m0, s45\n"
      "ds_read2_b64 a[64:67], v11 offset0:3 offset1:67\n"
      "v_cvt_pk_bf16_f32 v81, v76, v77\n"
      "v_mfma_f32_16x16x32_bf16 v[20:23], a[32:35], v[228:231], v[20:23]\n"
      "ds_write_addtid_b32 v32 offset:58384\n"
      "v_cvt_pk_bf16_f32 v86, v74, v75\n"
      "v_mfma_f32_16x16x32_bf16 v[20:23], a[0:3], v[136:139], v[20:23]\n"
      "ds_write_addtid_b32 v33 offset:58640\n"
      "v_cvt_pk_bf16_f32 v87, v78, v79\n"
      "v_mfma_f32_16x16x32_bf16 v[20:23], a[4:7], v[192:195], v[20:23]\n"
      "ds_read_b128 a[40:43], v3 offset:128\n"
      "v_mfma_f32_16x16x32_bf16 v[20:23], a[8:11], v[244:247], v[20:23]\n"
      "global_load_dword v8, v9, s[54:55]\n"
      "s_nop 0\n"
      "v_mfma_f32_16x16x32_bf16 v[52:55], a[48:51], a[72:75], 0\n"
      "ds_read2_b64 a[68:71], v13 offset0:0 offset1:64\n"
      "s_nop 0\n"
      "v_mfma_f32_16x16x32_bf16 v[56:59], a[52:55], a[76:79], 0\n"
      "s_nop 0\n"
      "v_mfma_f32_16x16x32_bf16 v[60:63], a[56:59], a[80:83], v[52:55]\n"
      "v_cvt_pk_bf16_f32 v20, v20, v21\n"
      "v_cvt_pk_bf16_f32 v21, v22, v23\n"
      "v_mfma_f32_16x16x32_bf16 v[68:71], a[84:87], a[60:63], 0\n"
      "ds_write_addtid_b32 v20 offset:46088\n"
      "s_nop 0\n"
      "v_mfma_f32_16x16x32_bf16 v[24:27], a[12:15], a[196:199], 0\n"
      "ds_read_b128 a[12:15], v6 offset:1536\n"
      "v_sub_f32_e32 v64, v52, v56\n"
      "v_mfma_f32_16x16x32_bf16 v[24:27], a[16:19], v[160:163], v[24:27]\n"
      "ds_read_b128 a[16:19], v6 offset:1792\n"
      "v_sub_f32_e32 v65, v53, v57\n"
      "v_mfma_f32_16x16x32_bf16 v[24:27], a[20:23], v[216:219], v[24:27]\n"
      "ds_read_b128 a[20:23], v6 offset:2048\n"
      "v_cvt_pk_bf16_f32 v64, v64, v65\n"
      "v_mfma_f32_16x16x32_bf16 v[24:27], a[24:27], v[124:127], v[24:27]\n"
      "ds_read_b128 a[24:27], v6 offset:6144\n"
      "v_sub_f32_e32 v66, v54, v58\n"
      "v_mfma_f32_16x16x32_bf16 v[24:27], a[28:31], v[180:183], v[24:27]\n"
      "ds_read_b128 a[28:31], v6 offset:6400\n"
      "v_sub_f32_e32 v67, v55, v59\n"
      "v_mfma_f32_16x16x32_bf16 v[24:27], a[32:35], v[232:235], v[24:27]\n"
      "ds_read_b128 a[32:35], v6 offset:6656\n"
      "v_cvt_pk_bf16_f32 v65, v66, v67\n"
      "v_mfma_f32_16x16x32_bf16 v[24:27], a[0:3], a[216:219], v[24:27]\n"
      "ds_read_b128 a[0:3], v7 offset:1536\n"
      "v_add_f32_e32 v56, v52, v56\n"
      "v_mfma_f32_16x16x32_bf16 v[24:27], a[4:7], v[196:199], v[24:27]\n"
      "ds_read_b128 a[4:7], v7 offset:1792\n"
      "v_add_f32_e32 v57, v53, v57\n"
      "v_mfma_f32_16x16x32_bf16 v[24:27], a[8:11], v[252:255], v[24:27]\n"
      "s_waitcnt lgkmcnt(7)\n"
      "ds_read_b128 a[8:11], v7 offset:2048\n"
      "v_cvt_pk_bf16_f32 v56, v56, v57\n"
      "v_mfma_f32_16x16x32_bf16 v[36:39], a[36:39], a[72:75], 0\n"
      "ds_write_addtid_b32 v21 offset:46344\n"
      "v_add_f32_e32 v58, v54, v58\n"
      "v_mfma_f32_16x16x32_bf16 v[44:47], a[44:47], a[80:83], v[36:39]\n"
      "s_add_u32 m0, s58, 13280\n"
      "v_cvt_pk_bf16_f32 v24, v24, v25\n"
      "v_cvt_pk_bf16_f32 v25, v26, v27\n"
      "v_mfma_f32_16x16x32_bf16 v[72:75], a[84:87], a[64:67], 0\n"
      "buffer_load_dwordx4 v18, s[32:35], s53 offen offset:2048 lds\n"
      "v_add_f32_e32 v59, v55, v59\n"
      "v_mfma_f32_16x16x32_bf16 v[28:31], a[12:15], v[96:99], 0\n"
      "s_waitcnt lgkmcnt(5)\n"
      "v_cvt_pk_bf16_f32 v57, v58, v59\n"
      "v_cvt_pk_bf16_f32 v60, v60, v61\n"
      "v_mfma_f32_16x16x32_bf16 v[28:31], a[16:19], v[152:155], v[28:31]\n"
      "s_mov_b32 m0, s45\n"
      "v_cvt_pk_bf16_f32 v82, v68, v69\n"
      "v_cvt_pk_bf16_f32 v83, v72, v73\n"
      "v_mfma_f32_16x16x32_bf16 v[28:31], a[20:23], a[220:223], v[28:31]\n"
      "ds_write_addtid_b32 v24 offset:33792\n"
      "ds_write_addtid_b32 v25 offset:34048\n"
      "v_mfma_f32_16x16x32_bf16 v[28:31], a[24:27], v[116:119], v[28:31]\n"
      "s_mov_b32 m0, s18\n"
      "v_cvt_pk_bf16_f32 v61, v62, v63\n"
      "v_cvt_pk_bf16_f32 v88, v70, v71\n"
      "v_mfma_f32_16x16x32_bf16 v[28:31], a[28:31], v[168:171], v[28:31]\n"
      "s_waitcnt lgkmcnt(3)\n"
      "ds_write_addtid_b32 v64 offset:6400\n"
      "ds_write_addtid_b32 v65 offset:43328\n"
      "v_mfma_f32_16x16x32_bf16 v[28:31], a[32:35], v[224:227], v[28:31]\n"
      "ds_write_addtid_b32 v56 offset:5632\n"
      "ds_write_addtid_b32 v57 offset:42560\n"
      "v_mfma_f32_16x16x32_bf16 v[28:31], a[0:3], v[132:135], v[28:31]\n"
      "ds_write_addtid_b32 v60 offset:4864\n"
      "ds_write_addtid_b32 v61 offset:41792\n"
      "v_mfma_f32_16x16x32_bf16 v[28:31], a[4:7], v[188:191], v[28:31]\n"
      "buffer_store_dwordx4 v[80:83], v14, s[36:39], s52 offen sc1\n"
      "v_cvt_pk_bf16_f32 v89, v74, v75\n"
      "v_mfma_f32_16x16x32_bf16 v[28:31], a[8:11], a[240:243], v[28:31]\n"
      "s_mov_b32 m0, s45\n"
      "ds_read2_b64 a[60:63], v13 offset0:1 offset1:65\n"
      "v_cvt_pk_bf16_f32 v44, v44, v45\n"
      "v_mfma_f32_16x16x32_bf16 v[40:43], a[40:43], a[76:79], 0\n"
      "v_cvt_pk_bf16_f32 v45, v46, v47\n"
      "s_nop 0\n"
      "v_mfma_f32_16x16x32_bf16 v[20:23], a[12:15], v[104:107], 0\n"
      "v_mfma_f32_16x16x32_bf16 v[20:23], a[16:19], v[156:159], v[20:23]\n"
      "v_cvt_pk_bf16_f32 v28, v28, v29\n"
      "v_cvt_pk_bf16_f32 v29, v30, v31\n"
      "v_mfma_f32_16x16x32_bf16 v[20:23], a[20:23], v[212:215], v[20:23]\n"
      "buffer_store_dwordx4 v[86:89], v14, s[36:39], s52 offen offset:1536 sc1\n"
      "v_sub_f32_e32 v48, v36, v40\n"
      "v_mfma_f32_16x16x32_bf16 v[20:23], a[24:27], v[120:123], v[20:23]\n"
      "ds_write_addtid_b32 v28 offset:59408\n"
      "ds_write_addtid_b32 v29 offset:59664\n"
      "v_mfma_f32_16x16x32_bf16 v[20:23], a[28:31], v[176:179], v[20:23]\n"
      "s_mov_b32 m0, s18\n"
      "v_sub_f32_e32 v49, v37, v41\n"
      "v_cvt_pk_bf16_f32 v48, v48, v49\n"
      "v_mfma_f32_16x16x32_bf16 v[20:23], a[32:35], a[232:235], v[20:23]\n"
      "s_sub_u32 s18, s19, s18\n"
      "ds_write_addtid_b32 v48 offset:6656\n"
      "v_sub_f32_e32 v50, v38, v42\n"
      "v_mfma_f32_16x16x32_bf16 v[20:23], a[0:3], v[140:143], v[20:23]\n"
      "v_sub_f32_e32 v51, v39, v43\n"
      "v_cvt_pk_bf16_f32 v49, v50, v51\n"
      "v_mfma_f32_16x16x32_bf16 v[20:23], a[4:7], a[236:239], v[20:23]\n"
      "ds_write_addtid_b32 v49 offset:43584\n"
      "v_add_f32_e32 v40, v36, v40\n"
      "v_mfma_f32_16x16x32_bf16 v[20:23], a[8:11], v[248:251], v[20:23]\n"
      "v_add_f32_e32 v41, v37, v41\n"
      "v_cvt_pk_bf16_f32 v40, v40, v41\n"
      "v_mfma_f32_16x16x32_bf16 v[76:79], a[84:87], a[68:71], 0\n"
      "ds_write_addtid_b32 v40 offset:5888\n"
      "v_add_f32_e32 v42, v38, v42\n"
      "v_mfma_f32_16x16x32_bf16 v[32:35], a[12:15], v[108:111], 0\n"
      "ds_write_addtid_b32 v44 offset:5120\n"
      "v_add_f32_e32 v43, v39, v43\n"
      "v_mfma_f32_16x16x32_bf16 v[32:35], a[16:19], v[164:167], v[32:35]\n"
      "ds_write_addtid_b32 v45 offset:42048\n"
      "v_cvt_pk_bf16_f32 v41, v42, v43\n"
      "v_mfma_f32_16x16x32_bf16 v[32:35], a[20:23], a[224:227], v[32:35]\n"
      "ds_write_addtid_b32 v41 offset:42816\n"
      "v_cvt_pk_bf16_f32 v20, v20, v21\n"
      "v_mfma_f32_16x16x32_bf16 v[32:35], a[24:27], v[128:131], v[32:35]\n"
      "s_mov_b32 m0, s45\n"
      "v_cvt_pk_bf16_f32 v21, v22, v23\n"
      "v_cvt_pk_bf16_f32 v84, v76, v77\n"
      "v_mfma_f32_16x16x32_bf16 v[32:35], a[28:31], a[228:231], v[32:35]\n"
      "ds_write_addtid_b32 v20 offset:47112\n"
      "v_cvt_pk_bf16_f32 v90, v78, v79\n"
      "v_mfma_f32_16x16x32_bf16 v[32:35], a[32:35], v[236:239], v[32:35]\n"
      "ds_write_addtid_b32 v21 offset:47368\n"
      "v_mfma_f32_16x16x32_bf16 v[32:35], a[0:3], v[144:147], v[32:35]\n"
      "v_mfma_f32_16x16x32_bf16 v[32:35], a[4:7], v[200:203], v[32:35]\n"
      "v_mfma_f32_16x16x32_bf16 v[32:35], a[8:11], a[244:247], v[32:35]\n"
      "s_nop 1\n"
      "s_cbranch_vccnz Lconv_loop0\n"
      "Lconv_t0:\n"
      "s_nop 1\n"
      "v_mfma_f32_16x16x32_bf16 v[20:23], a[24:27], v[96:99], 0\n"
      "s_nop 1\n"
      "v_mfma_f32_16x16x32_bf16 v[20:23], a[28:31], v[152:155], v[20:23]\n"
      "v_cvt_pk_bf16_f32 v32, v32, v33\n"
      "v_cvt_pk_bf16_f32 v33, v34, v35\n"
      "v_mfma_f32_16x16x32_bf16 v[20:23], a[32:35], a[220:223], v[20:23]\n"
      "ds_write_addtid_b32 v32 offset:34816\n"
      "v_mfma_f32_16x16x32_bf16 v[20:23], a[0:3], v[116:119], v[20:23]\n"
      "ds_write_addtid_b32 v33 offset:35072\n"
      "v_mfma_f32_16x16x32_bf16 v[20:23], a[4:7], v[168:171], v[20:23]\n"
      "v_mfma_f32_16x16x32_bf16 v[20:23], a[8:11], v[224:227], v[20:23]\n"
      "v_mfma_f32_16x16x32_bf16 v[24:27], a[24:27], v[104:107], 0\n"
      "s_waitcnt lgkmcnt(0)\n"
      "s_barrier\n"
      "v_mfma_f32_16x16x32_bf16 v[24:27], a[28:31], v[156:159], v[24:27]\n"
      "ds_read2_b64 a[64:67], v10 offset0:0 offset1:64\n"
      "v_mfma_f32_16x16x32_bf16 v[24:27], a[32:35], v[212:215], v[24:27]\n"
      "ds_read_b128 a[12:15], v7 offset:6144\n"
      "v_mfma_f32_16x16x32_bf16 v[24:27], a[0:3], v[120:123], v[24:27]\n"
      "ds_read2_b64 a[68:71], v10 offset0:1 offset1:65\n"
      "v_mfma_f32_16x16x32_bf16 v[24:27], a[4:7], v[176:179], v[24:27]\n"
      "ds_read_b128 a[16:19], v7 offset:6400\n"
      "v_mfma_f32_16x16x32_bf16 v[24:27], a[8:11], a[232:235], v[24:27]\n"
      "ds_read_b128 a[20:23], v7 offset:6656\n"
      "v_mfma_f32_16x16x32_bf16 v[68:71], a[84:87], a[60:63], 0\n"
      "ds_read2_b64 a[60:63], v10 offset0:2 offset1:66\n"
      "s_nop 0\n"
      "v_mfma_f32_16x16x32_bf16 v[28:31], a[24:27], v[108:111], 0\n"
      "s_waitcnt lgkmcnt(5)\n"
      "ds_read_b128 a[24:27], v6 offset:5376\n"
      "s_nop 0\n"
      "v_mfma_f32_16x16x32_bf16 v[72:75], a[84:87], a[64:67], 0\n"
      "ds_read2_b64 a[64:67], v10 offset0:3 offset1:67\n"
      "v_cvt_pk_bf16_f32 v85, v68, v69\n"
      "v_mfma_f32_16x16x32_bf16 v[28:31], a[28:31], v[164:167], v[28:31]\n"
      "ds_read_b128 a[28:31], v6 offset:5632\n"
      "v_cvt_pk_bf16_f32 v91, v70, v71\n"
      "v_mfma_f32_16x16x32_bf16 v[28:31], a[32:35], a[224:227], v[28:31]\n"
      "buffer_store_dwordx2 v[84:85], v15, s[36:39], s52 offen sc1\n"
      "v_mfma_f32_16x16x32_bf16 v[28:31], a[0:3], v[128:131], v[28:31]\n"
      "ds_read_b128 a[32:35], v6 offset:5888\n"
      "v_cvt_pk_bf16_f32 v80, v72, v73\n"
      "v_mfma_f32_16x16x32_bf16 v[28:31], a[4:7], a[228:231], v[28:31]\n"
      "ds_read_b128 a[0:3], v7 offset:768\n"
      "v_cvt_pk_bf16_f32 v86, v74, v75\n"
      "v_mfma_f32_16x16x32_bf16 v[28:31], a[8:11], v[236:239], v[28:31]\n"
      "s_waitcnt lgkmcnt(6)\n"
      "ds_read_b128 a[4:7], v7 offset:1024\n"
      "v_mfma_f32_16x16x32_bf16 v[20:23], a[12:15], v[132:135], v[20:23]\n"
      "ds_read_b128 a[8:11], v7 offset:1280\n"
      "v_mfma_f32_16x16x32_bf16 v[76:79], a[84:87], a[68:71], 0\n"
      "s_waitcnt lgkmcnt(6)\n"
      "ds_read2_b64 a[68:71], v12 offset0:0 offset1:64\n"
      "s_nop 0\n"
      "v_mfma_f32_16x16x32_bf16 v[68:71], a[84:87], a[60:63], 0\n"
      "ds_read2_b64 a[60:63], v12 offset0:1 offset1:65\n"
      "s_nop 0\n"
      "v_mfma_f32_16x16x32_bf16 v[20:23], a[16:19], v[188:191], v[20:23]\n"
      "buffer_store_dwordx2 v[90:91], v15, s[36:39], s52 offen offset:1536 sc1\n"
      "v_cvt_pk_bf16_f32 v81, v76, v77\n"
      "v_mfma_f32_16x16x32_bf16 v[20:23], a[20:23], a[240:243], v[20:23]\n"
      "s_mov_b32 s52, s13\n"
      "v_cvt_pk_bf16_f32 v87, v78, v79\n"
      "v_cvt_pk_bf16_f32 v82, v68, v69\n"
      "v_mfma_f32_16x16x32_bf16 v[24:27], a[12:15], v[140:143], v[24:27]\n"
      "s_waitcnt lgkmcnt(5)\n"
      "v_cvt_pk_bf16_f32 v88, v70, v71\n"
      "s_nop 0\n"
      "v_mfma_f32_16x16x32_bf16 v[72:75], a[84:87], a[64:67], 0\n"
      "s_add_u32 s13, s13, s12\n"
      "v_cvt_pk_bf16_f32 v20, v20, v21\n"
      "v_cvt_pk_bf16_f32 v21, v22, v23\n"
      "v_mfma_f32_16x16x32_bf16 v[24:27], a[16:19], a[236:239], v[24:27]\n"
      "ds_write_addtid_b32 v20 offset:62480\n"
      "s_nop 0\n"
      "v_mfma_f32_16x16x32_bf16 v[24:27], a[20:23], v[248:251], v[24:27]\n"
      "ds_write_addtid_b32 v21 offset:62736\n"
      "v_cvt_pk_bf16_f32 v83, v72, v73\n"
      "v_mfma_f32_16x16x32_bf16 v[28:31], a[12:15], v[144:147], v[28:31]\n"
      "s_waitcnt lgkmcnt(3)\n"
      "ds_read_b128 a[12:15], v7 offset:5376\n"
      "v_cvt_pk_bf16_f32 v89, v74, v75\n"
      "v_mfma_f32_16x16x32_bf16 v[76:79], a[84:87], a[68:71], 0\n"
      "buffer_store_dwordx4 v[80:83], v14, s[36:39], s52 offen sc1\n"
      "v_cvt_pk_bf16_f32 v24, v24, v25\n"
      "v_mfma_f32_16x16x32_bf16 v[28:31], a[16:19], v[200:203], v[28:31]\n"
      "ds_read_b128 a[16:19], v7 offset:5632\n"
      "v_cvt_pk_bf16_f32 v25, v26, v27\n"
      "v_mfma_f32_16x16x32_bf16 v[28:31], a[20:23], a[244:247], v[28:31]\n"
      "ds_read_b128 a[20:23], v7 offset:5888\n"
      "v_mfma_f32_16x16x32_bf16 v[32:35], a[24:27], v[92:95], 0\n"
      "s_waitcnt lgkmcnt(5)\n"
      "ds_write_addtid_b32 v24 offset:50184\n"
      "v_cvt_pk_bf16_f32 v84, v76, v77\n"
      "v_mfma_f32_16x16x32_bf16 v[68:71], a[84:87], a[60:63], 0\n"
      "ds_write_addtid_b32 v25 offset:50440\n"
      "v_cvt_pk_bf16_f32 v90, v78, v79\n"
      "v_mfma_f32_16x16x32_bf16 v[32:35], a[28:31], v[148:151], v[32:35]\n"
      "buffer_store_dwordx4 v[86:89], v14, s[36:39], s52 offen offset:1536 sc1\n"
      "v_cvt_pk_bf16_f32 v28, v28, v29\n"
      "v_mfma_f32_16x16x32_bf16 v[32:35], a[32:35], v[204:207], v[32:35]\n"
      "ds_write_addtid_b32 v28 offset:37888\n"
      "v_cvt_pk_bf16_f32 v29, v30, v31\n"
      "v_mfma_f32_16x16x32_bf16 v[32:35], a[0:3], v[112:115], v[32:35]\n"
      "v_cvt_pk_bf16_f32 v85, v68, v69\n"
      "v_cvt_pk_bf16_f32 v91, v70, v71\n"
      "v_mfma_f32_16x16x32_bf16 v[32:35], a[4:7], a[208:211], v[32:35]\n"
      "ds_write_addtid_b32 v29 offset:38144\n"
      "v_mfma_f32_16x16x32_bf16 v[32:35], a[8:11], v[220:223], v[32:35]\n"
      "s_waitcnt lgkmcnt(5)\n"
      "v_mfma_f32_16x16x32_bf16 v[32:35], a[12:15], a[212:215], v[32:35]\n"
      "buffer_store_dwordx2 v[84:85], v15, s[36:39], s52 offen sc1\n"
      "v_mfma_f32_16x16x32_bf16 v[32:35], a[16:19], v[184:187], v[32:35]\n"
      "s_waitcnt lgkmcnt(4)\n"
      "v_mfma_f32_16x16x32_bf16 v[32:35], a[20:23], v[240:243], v[32:35]\n"
      "s_nop 1\n"
      "v_mfma_f32_16x16x32_bf16 v[20:23], a[24:27], v[100:103], 0\n"
      "s_nop 1\n"
      "v_mfma_f32_16x16x32_bf16 v[20:23], a[28:31], a[200:203], v[20:23]\n"
      "s_nop 0\n"
      "v_mfma_f32_16x16x32_bf16 v[20:23], a[32:35], v[208:211], v[20:23]\n"
      "buffer_store_dwordx2 v[90:91], v15, s[36:39], s52 offen offset:1536 sc1\n"
      "v_cvt_pk_bf16_f32 v32, v32, v33\n"
      "v_mfma_f32_16x16x32_bf16 v[20:23], a[0:3], a[204:207], v[20:23]\n"
      "ds_write_addtid_b32 v32 offset:61456\n"
      "v_cvt_pk_bf16_f32 v33, v34, v35\n"
      "v_mfma_f32_16x16x32_bf16 v[20:23], a[4:7], v[172:175], v[20:23]\n"
      "ds_write_addtid_b32 v33 offset:61712\n"
      "v_mfma_f32_16x16x32_bf16 v[20:23], a[8:11], v[228:231], v[20:23]\n"
      "v_mfma_f32_16x16x32_bf16 v[20:23], a[12:15], v[136:139], v[20:23]\n"
      "v_mfma_f32_16x16x32_bf16 v[20:23], a[16:19], v[192:195], v[20:23]\n"
      "v_mfma_f32_16x16x32_bf16 v[20:23], a[20:23], v[244:247], v[20:23]\n"
      "s_nop 1\n"
      "v_mfma_f32_16x16x32_bf16 v[24:27], a[24:27], a[196:199], 0\n"
      "ds_read_b128 a[24:27], v7 offset:4608\n"
      "s_nop 0\n"
      "v_mfma_f32_16x16x32_bf16 v[24:27], a[28:31], v[160:163], v[24:27]\n"
      "ds_read_b128 a[28:31], v7 offset:4864\n"
      "v_mfma_f32_16x16x32_bf16 v[24:27], a[32:35], v[216:219], v[24:27]\n"
      "ds_read_b128 a[32:35], v7 offset:5120\n"
      "v_cvt_pk_bf16_f32 v20, v20, v21\n"
      "v_mfma_f32_16x16x32_bf16 v[24:27], a[0:3], v[124:127], v[24:27]\n"
      "ds_read_b128 a[0:3], v6 offset:4608\n"
      "v_cvt_pk_bf16_f32 v21, v22, v23\n"
      "v_mfma_f32_16x16x32_bf16 v[24:27], a[4:7], v[180:183], v[24:27]\n"
      "ds_read_b128 a[4:7], v6 offset:4864\n"
      "v_mfma_f32_16x16x32_bf16 v[24:27], a[8:11], v[232:235], v[24:27]\n"
      "ds_read_b128 a[8:11], v6 offset:5120\n"
      "v_mfma_f32_16x16x32_bf16 v[24:27], a[12:15], a[216:219], v[24:27]\n"
      "ds_read_b128 a[12:15], v7 offset:0\n"
      "v_mfma_f32_16x16x32_bf16 v[24:27], a[16:19], v[196:199], v[24:27]\n"
      "ds_read_b128 a[16:19], v7 offset:256\n"
      "v_mfma_f32_16x16x32_bf16 v[24:27], a[20:23], v[252:255], v[24:27]\n"
      "s_waitcnt lgkmcnt(7)\n"
      "ds_read_b128 a[20:23], v7 offset:512\n"
      "s_nop 0\n"
      "v_mfma_f32_16x16x32_bf16 v[28:31], a[24:27], a[160:163], 0\n"
      "s_waitcnt lgkmcnt(7)\n"
      "ds_write_addtid_b32 v20 offset:49160\n"
      "ds_write_addtid_b32 v21 offset:49416\n"
      "v_mfma_f32_16x16x32_bf16 v[28:31], a[28:31], a[172:175], v[28:31]\n"
      "s_waitcnt lgkmcnt(8)\n"
      "v_cvt_pk_bf16_f32 v24, v24, v25\n"
      "v_cvt_pk_bf16_f32 v25, v26, v27\n"
      "v_mfma_f32_16x16x32_bf16 v[28:31], a[32:35], a[184:187], v[28:31]\n"
      "s_waitcnt lgkmcnt(7)\n"
      "ds_write_addtid_b32 v24 offset:36864\n"
      "v_mfma_f32_16x16x32_bf16 v[28:31], a[0:3], a[88:91], v[28:31]\n"
      "s_waitcnt lgkmcnt(7)\n"
      "ds_write_addtid_b32 v25 offset:37120\n"
      "v_mfma_f32_16x16x32_bf16 v[28:31], a[4:7], a[100:103], v[28:31]\n"
      "s_waitcnt lgkmcnt(7)\n"
      "v_mfma_f32_16x16x32_bf16 v[28:31], a[8:11], a[112:115], v[28:31]\n"
      "s_waitcnt lgkmcnt(6)\n"
      "v_mfma_f32_16x16x32_bf16 v[28:31], a[12:15], a[124:127], v[28:31]\n"
      "s_waitcnt lgkmcnt(5)\n"
      "v_mfma_f32_16x16x32_bf16 v[28:31], a[16:19], a[136:139], v[28:31]\n"
      "s_waitcnt lgkmcnt(4)\n"
      "v_mfma_f32_16x16x32_bf16 v[28:31], a[20:23], a[148:151], v[28:31]\n"
      "s_nop 1\n"
      "v_mfma_f32_16x16x32_bf16 v[20:23], a[24:27], a[164:167], 0\n"
      "s_nop 1\n"
      "v_mfma_f32_16x16x32_bf16 v[20:23], a[28:31], a[176:179], v[20:23]\n"
      "s_nop 0\n"
      "v_mfma_f32_16x16x32_bf16 v[20:23], a[32:35], a[188:191], v[20:23]\n"
      "v_cvt_pk_bf16_f32 v28, v28, v29\n"
      "v_cvt_pk_bf16_f32 v29, v30, v31\n"
      "v_mfma_f32_16x16x32_bf16 v[20:23], a[0:3], a[92:95], v[20:23]\n"
      "ds_write_addtid_b32 v28 offset:60432\n"
      "v_mfma_f32_16x16x32_bf16 v[20:23], a[4:7], a[104:107], v[20:23]\n"
      "ds_write_addtid_b32 v29 offset:60688\n"
      "v_mfma_f32_16x16x32_bf16 v[20:23], a[8:11], a[116:119], v[20:23]\n"
      "v_mfma_f32_16x16x32_bf16 v[20:23], a[12:15], a[128:131], v[20:23]\n"
      "v_mfma_f32_16x16x32_bf16 v[20:23], a[16:19], a[140:143], v[20:23]\n"
      "v_mfma_f32_16x16x32_bf16 v[20:23], a[20:23], a[152:155], v[20:23]\n"
      "s_nop 1\n"
      "v_mfma_f32_16x16x32_bf16 v[32:35], a[24:27], a[168:171], 0\n"
      "s_nop 1\n"
      "v_mfma_f32_16x16x32_bf16 v[32:35], a[28:31], a[180:183], v[32:35]\n"
      "s_nop 0\n"
      "v_mfma_f32_16x16x32_bf16 v[32:35], a[32:35], a[192:195], v[32:35]\n"
      "v_cvt_pk_bf16_f32 v20, v20, v21\n"
      "v_cvt_pk_bf16_f32 v21, v22, v23\n"
      "v_mfma_f32_16x16x32_bf16 v[32:35], a[0:3], a[96:99], v[32:35]\n"
      "ds_write_addtid_b32 v20 offset:48136\n"
      "v_mfma_f32_16x16x32_bf16 v[32:35], a[4:7], a[108:111], v[32:35]\n"
      "ds_write_addtid_b32 v21 offset:48392\n"
      "v_mfma_f32_16x16x32_bf16 v[32:35], a[8:11], a[120:123], v[32:35]\n"
      "v_mfma_f32_16x16x32_bf16 v[32:35], a[12:15], a[132:135], v[32:35]\n"
      "v_mfma_f32_16x16x32_bf16 v[32:35], a[16:19], a[144:147], v[32:35]\n"
      "v_mfma_f32_16x16x32_bf16 v[32:35], a[20:23], a[156:159], v[32:35]\n"
      "s_nop 1\n"
      "s_nop 5\n"
      "v_cvt_pk_bf16_f32 v32, v32, v33\n"
      "v_cvt_pk_bf16_f32 v33, v34, v35\n"
      "ds_write_addtid_b32 v32 offset:35840\n"
      "ds_write_addtid_b32 v33 offset:36096\n"
      "s_waitcnt lgkmcnt(0)\n"
      "s_barrier\n"
      "ds_read2_b64 a[0:3], v11 offset0:0 offset1:64\n"
      "ds_read2_b64 a[4:7], v11 offset0:1 offset1:65\n"
      "ds_read2_b64 a[8:11], v11 offset0:2 offset1:66\n"
      "ds_read2_b64 a[12:15], v11 offset0:3 offset1:67\n"
      "ds_read2_b64 a[16:19], v13 offset0:0 offset1:64\n"
      "ds_read2_b64 a[20:23], v13 offset0:1 offset1:65\n"
      "s_waitcnt lgkmcnt(5)\n"
      "v_mfma_f32_16x16x32_bf16 v[72:75], a[84:87], a[0:3], 0\n"
      "s_waitcnt lgkmcnt(4)\n"
      "v_mfma_f32_16x16x32_bf16 v[76:79], a[84:87], a[4:7], 0\n"
      "s_waitcnt lgkmcnt(3)\n"
      "v_mfma_f32_16x16x32_bf16 v[68:71], a[84:87], a[8:11], 0\n"
      "s_waitcnt lgkmcnt(2)\n"
      "s_nop 2\n"
      "v_cvt_pk_bf16_f32 v80, v72, v73\n"
      "v_cvt_pk_bf16_f32 v86, v74, v75\n"
      "v_mfma_f32_16x16x32_bf16 v[72:75], a[84:87], a[12:15], 0\n"
      "s_waitcnt lgkmcnt(1)\n"
      "v_cvt_pk_bf16_f32 v81, v76, v77\n"
      "v_cvt_pk_bf16_f32 v87, v78, v79\n"
      "v_cvt_pk_bf16_f32 v82, v68, v69\n"
      "v_cvt_pk_bf16_f32 v88, v70, v71\n"
      "v_mfma_f32_16x16x32_bf16 v[76:79], a[84:87], a[16:19], 0\n"
      "s_waitcnt lgkmcnt(0)\n"
      "v_mfma_f32_16x16x32_bf16 v[68:71], a[84:87], a[20:23], 0\n"
      "s_mov_b32 s52, s13\n"
      "v_cvt_pk_bf16_f32 v83, v72, v73\n"
      "v_cvt_pk_bf16_f32 v89, v74, v75\n"
      "s_add_u32 s13, s13, s12\n"
      "buffer_store_dwordx4 v[80:83], v14, s[36:39], s52 offen sc1\n"
      "buffer_store_dwordx4 v[86:89], v14, s[36:39], s52 offen offset:1536 sc1\n"
      "v_cvt_pk_bf16_f32 v84, v76, v77\n"
      "v_cvt_pk_bf16_f32 v90, v78, v79\n"
      "v_cvt_pk_bf16_f32 v85, v68, v69\n"
      "v_cvt_pk_bf16_f32 v91, v70, v71\n"
      "buffer_store_dwordx2 v[84:85], v15, s[36:39], s52 offen sc1\n"
      "buffer_store_dwordx2 v[90:91], v15, s[36:39], s52 offen offset:1536 sc1\n"
      "Lconv_u0:\n"
      "s_waitcnt vmcnt(0)\n"
      "s_endpgm\n"
      ".p2align 12\n"
      "Lconv_pa1:\n"
      "s_cbranch_execz Lconv_r0\n"
      "s_nop 0\n"
      "Lconv_loop1:\n"
      "v_mfma_f32_16x16x32_bf16 v[20:23], a[24:27], v[96:99], 0\n"
      "s_add_u32 s51, s51, s12\n"
      "ds_write_addtid_b32 v32 offset:34816\n"
      "v_cvt_pk_bf16_f32 v33, v34, v35\n"
      "v_mfma_f32_16x16x32_bf16 v[20:23], a[28:31], v[152:155], v[20:23]\n"
      "s_sub_u32 s58, s93, s58\n"
      "ds_write_addtid_b32 v33 offset:35072\n"
      "v_cvt_pk_bf16_f32 v84, v72, v73\n"
      "v_mfma_f32_16x16x32_bf16 v[20:23], a[32:35], a[220:223], v[20:23]\n"
      "s_sub_u32 s59, s94, s59\n"
      "v_cvt_pk_bf16_f32 v85, v76, v77\n"
      "v_cvt_pk_bf16_f32 v90, v74, v75\n"
      "v_mfma_f32_16x16x32_bf16 v[20:23], a[0:3], v[116:119], v[20:23]\n"
      "buffer_store_dwordx2 v[84:85], v15, s[36:39], s52 offen sc1\n"
      "v_cvt_pk_bf16_f32 v91, v78, v79\n"
      "v_mfma_f32_16x16x32_bf16 v[20:23], a[4:7], v[168:171], v[20:23]\n"
      "s_sub_u32 s47, s51, s48\n"
      "v_mfma_f32_16x16x32_bf16 v[20:23], a[8:11], v[224:227], v[20:23]\n"
      "s_cmp_ge_u32 vcc_lo, 0x80000000\n"
      "v_mfma_f32_16x16x32_bf16 v[24:27], a[24:27], v[104:107], 0\n"
      "buffer_store_dwordx2 v[90:91], v15, s[36:39], s52 offen offset:1536 sc1\n"
      "v_mfma_f32_16x16x32_bf16 v[24:27], a[28:31], v[156:159], v[24:27]\n"
      "s_cselect_b32 s53, s47, s51\n"
      "v_mfma_f32_16x16x32_bf16 v[24:27], a[32:35], v[212:215], v[24:27]\n"
      "s_mov_b32 s52, s13\n"
      "v_mfma_f32_16x16x32_bf16 v[24:27], a[0:3], v[120:123], v[24:27]\n"
      "s_add_u32 s13, s13, s12\n"
      "v_mfma_f32_16x16x32_bf16 v[24:27], a[4:7], v[176:179], v[24:27]\n"
      "s_waitcnt vmcnt(17) lgkmcnt(0)\n"
      "s_barrier\n"
      "v_mfma_f32_16x16x32_bf16 v[24:27], a[8:11], a[232:235], v[24:27]\n"
      "s_mov_b32 m0, s58\n"
      "ds_read2_b64 a[60:63], v10 offset0:0 offset1:64\n"
      "v_mfma_f32_16x16x32_bf16 v[28:31], a[24:27], v[108:111], 0\n"
      "ds_read_b128 a[12:15], v7 offset:6144\n"
      "v_mfma_f32_16x16x32_bf16 v[28:31], a[28:31], v[164:167], v[28:31]\n"
      "ds_read_b128 a[16:19], v7 offset:6400\n"
      "v_mfma_f32_16x16x32_bf16 v[28:31], a[32:35], a[224:227], v[28:31]\n"
      "ds_read_b128 a[20:23], v7 offset:6656\n"
      "v_mfma_f32_16x16x32_bf16 v[28:31], a[0:3], v[128:131], v[28:31]\n"
      "ds_read_b128 a[48:51], v2 offset:13280\n"
      "v_mfma_f32_16x16x32_bf16 v[28:31], a[4:7], a[228:231], v[28:31]\n"
      "ds_read_b128 a[56:59], v4 offset:13280\n"
      "v_mfma_f32_16x16x32_bf16 v[28:31], a[8:11], v[236:239], v[28:31]\n"
      "s_waitcnt lgkmcnt(5)\n"
      "ds_read_b128 a[52:55], v3 offset:13280\n"
      "v_mfma_f32_16x16x32_bf16 v[68:71], a[84:87], a[60:63], 0\n"
      "s_waitcnt lgkmcnt(5)\n"
      "ds_read_b128 a[24:27], v6 offset:5376\n"
      "v_mfma_f32_16x16x32_bf16 v[20:23], a[12:15], v[132:135], v[20:23]\n"
      "s_waitcnt lgkmcnt(5)\n"
      "ds_read_b128 a[28:31], v6 offset:5632\n"
      "v_mfma_f32_16x16x32_bf16 v[20:23], a[16:19], v[188:191], v[20:23]\n"
      "s_waitcnt lgkmcnt(5)\n"
      "ds_read_b128 a[32:35], v6 offset:5888\n"
      "v_cvt_pk_bf16_f32 v80, v68, v69\n"
      "v_mfma_f32_16x16x32_bf16 v[20:23], a[20:23], a[240:243], v[20:23]\n"
      "ds_read_b128 a[0:3], v7 offset:768\n"
      "v_cvt_pk_bf16_f32 v86, v70, v71\n"
      "v_mfma_f32_16x16x32_bf16 v[24:27], a[12:15], v[140:143], v[24:27]\n"
      "buffer_load_dwordx4 v16, s[32:35], s53 offen lds\n"
      "s_nop 0\n"
      "v_mfma_f32_16x16x32_bf16 v[24:27], a[16:19], a[236:239], v[24:27]\n"
      "s_mov_b32 m0, s45\n"
      "ds_read_b128 a[4:7], v7 offset:1024\n"
      "v_cvt_pk_bf16_f32 v20, v20, v21\n"
      "v_mfma_f32_16x16x32_bf16 v[24:27], a[20:23], v[248:251], v[24:27]\n"
      "s_waitcnt lgkmcnt(6)\n"
      "ds_read_b128 a[8:11], v7 offset:1280\n"
      "v_cvt_pk_bf16_f32 v21, v22, v23\n"
      "v_mfma_f32_16x16x32_bf16 v[52:55], a[48:51], a[72:75], 0\n"
      "ds_read2_b64 a[64:67], v10 offset0:1 offset1:65\n"
      "s_nop 0\n"
      "v_mfma_f32_16x16x32_bf16 v[60:63], a[56:59], a[80:83], v[52:55]\n"
      "ds_write_addtid_b32 v20 offset:62480\n"
      "v_cvt_pk_bf16_f32 v24, v24, v25\n"
      "v_mfma_f32_16x16x32_bf16 v[28:31], a[12:15], v[144:147], v[28:31]\n"
      "ds_read_b128 a[12:15], v7 offset:5376\n"
      "v_cvt_pk_bf16_f32 v25, v26, v27\n"
      "v_mfma_f32_16x16x32_bf16 v[28:31], a[16:19], v[200:203], v[28:31]\n"
      "ds_read_b128 a[16:19], v7 offset:5632\n"
      "v_mfma_f32_16x16x32_bf16 v[28:31], a[20:23], a[244:247], v[28:31]\n"
      "s_waitcnt lgkmcnt(6)\n"
      "ds_read_b128 a[20:23], v7 offset:5888\n"
      "v_cvt_pk_bf16_f32 v60, v60, v61\n"
      "v_mfma_f32_16x16x32_bf16 v[56:59], a[52:55], a[76:79], 0\n"
      "ds_write_addtid_b32 v21 offset:62736\n"
      "v_cvt_pk_bf16_f32 v61, v62, v63\n"
      "v_mfma_f32_16x16x32_bf16 v[32:35], a[24:27], v[92:95], 0\n"
      "ds_read_b128 a[36:39], v2 offset:13344\n"
      "v_cvt_pk_bf16_f32 v28, v28, v29\n"
      "v_mfma_f32_16x16x32_bf16 v[32:35], a[28:31], v[148:151], v[32:35]\n"
      "ds_write_addtid_b32 v24 offset:50184\n"
      "v_cvt_pk_bf16_f32 v29, v30, v31\n"
      "v_mfma_f32_16x16x32_bf16 v[32:35], a[32:35], v[204:207], v[32:35]\n"
      "ds_read_b128 a[40:43], v3 offset:13344\n"
      "v_sub_f32_e32 v64, v52, v56\n"
      "v_mfma_f32_16x16x32_bf16 v[32:35], a[0:3], v[112:115], v[32:35]\n"
      "s_waitcnt lgkmcnt(8)\n"
      "ds_write_addtid_b32 v25 offset:50440\n"
      "v_sub_f32_e32 v65, v53, v57\n"
      "v_mfma_f32_16x16x32_bf16 v[32:35], a[4:7], a[208:211], v[32:35]\n"
      "ds_read_b128 a[44:47], v4 offset:13344\n"
      "v_sub_f32_e32 v66, v54, v58\n"
      "v_mfma_f32_16x16x32_bf16 v[32:35], a[8:11], v[220:223], v[32:35]\n"
      "s_waitcnt lgkmcnt(7)\n"
      "ds_read_b128 a[48:51], v2 offset:13408\n"
      "v_sub_f32_e32 v67, v55, v59\n"
      "v_mfma_f32_16x16x32_bf16 v[32:35], a[12:15], a[212:215], v[32:35]\n"
      "ds_write_addtid_b32 v28 offset:37888\n"
      "v_add_f32_e32 v56, v52, v56\n"
      "v_mfma_f32_16x16x32_bf16 v[32:35], a[16:19], v[184:187], v[32:35]\n"
      "s_waitcnt lgkmcnt(8)\n"
      "ds_write_addtid_b32 v29 offset:38144\n"
      "v_add_f32_e32 v57, v53, v57\n"
      "v_mfma_f32_16x16x32_bf16 v[32:35], a[20:23], v[240:243], v[32:35]\n"
      "s_mov_b32 m0, s18\n"
      "ds_read_b128 a[56:59], v4 offset:13408\n"
      "v_add_f32_e32 v58, v54, v58\n"
      "v_mfma_f32_16x16x32_bf16 v[72:75], a[84:87], a[64:67], 0\n"
      "ds_read2_b64 a[68:71], v10 offset0:2 offset1:66\n"
      "v_add_f32_e32 v59, v55, v59\n"
      "v_mfma_f32_16x16x32_bf16 v[20:23], a[24:27], v[100:103], 0\n"
      "v_cvt_pk_bf16_f32 v64, v64, v65\n"
      "v_cvt_pk_bf16_f32 v65, v66, v67\n"
      "v_mfma_f32_16x16x32_bf16 v[20:23], a[28:31], a[200:203], v[20:23]\n"
      "ds_write_addtid_b32 v64 offset:1536\n"
      "v_cvt_pk_bf16_f32 v56, v56, v57\n"
      "v_mfma_f32_16x16x32_bf16 v[20:23], a[32:35], v[208:211], v[20:23]\n"
      "ds_write_addtid_b32 v65 offset:38464\n"
      "v_cvt_pk_bf16_f32 v57, v58, v59\n"
      "v_mfma_f32_16x16x32_bf16 v[20:23], a[0:3], a[204:207], v[20:23]\n"
      "ds_write_addtid_b32 v56 offset:768\n"
      "v_cvt_pk_bf16_f32 v32, v32, v33\n"
      "v_mfma_f32_16x16x32_bf16 v[20:23], a[4:7], v[172:175], v[20:23]\n"
      "ds_write_addtid_b32 v57 offset:37696\n"
      "v_cvt_pk_bf16_f32 v33, v34, v35\n"
      "v_mfma_f32_16x16x32_bf16 v[20:23], a[8:11], v[228:231], v[20:23]\n"
      "ds_write_addtid_b32 v60 offset:0\n"
      "v_cvt_pk_bf16_f32 v81, v72, v73\n"
      "v_mfma_f32_16x16x32_bf16 v[20:23], a[12:15], v[136:139], v[20:23]\n"
      "ds_write_addtid_b32 v61 offset:36928\n"
      "v_cvt_pk_bf16_f32 v87, v74, v75\n"
      "v_mfma_f32_16x16x32_bf16 v[20:23], a[16:19], v[192:195], v[20:23]\n"
      "s_mov_b32 m0, s45\n"
      "ds_read_b128 a[52:55], v3 offset:13408\n"
      "v_add_u32_e32 v2, s91, v2\n"
      "v_mfma_f32_16x16x32_bf16 v[20:23], a[20:23], v[244:247], v[20:23]\n"
      "s_waitcnt lgkmcnt(7)\n"
      "ds_read2_b64 a[60:63], v10 offset0:3 offset1:67\n"
      "v_add_u32_e32 v4, s91, v4\n"
      "v_mfma_f32_16x16x32_bf16 v[36:39], a[36:39], a[72:75], 0\n"
      "s_mov_b32 m0, s58\n"
      "ds_read2_b64 a[64:67], v12 offset0:0 offset1:64\n"
      "v_add_u32_e32 v3, s91, v3\n"
      "v_mfma_f32_16x16x32_bf16 v[40:43], a[40:43], a[76:79], 0\n"
      "buffer_load_dwordx4 v17, s[32:35], s53 offen offset:1024 lds\n"
      "v_cvt_pk_bf16_f32 v20, v20, v21\n"
      "v_mfma_f32_16x16x32_bf16 v[44:47], a[44:47], a[80:83], v[36:39]\n"
      "s_mov_b32 m0, s45\n"
      "v_cvt_pk_bf16_f32 v21, v22, v23\n"
      "s_nop 0\n"
      "v_mfma_f32_16x16x32_bf16 v[52:55], a[48:51], a[72:75], 0\n"
      "s_sub_u32 s91, 0, s91\n"
      "ds_write_addtid_b32 v32 offset:61456\n"
      "v_sub_f32_e32 v48, v36, v40\n"
      "v_mfma_f32_16x16x32_bf16 v[60:63], a[56:59], a[80:83], v[52:55]\n"
      "ds_write_addtid_b32 v33 offset:61712\n"
      "v_sub_f32_e32 v49, v37, v41\n"
      "v_mfma_f32_16x16x32_bf16 v[76:79], a[84:87], a[68:71], 0\n"
      "ds_write_addtid_b32 v20 offset:49160\n"
      "v_sub_f32_e32 v50, v38, v42\n"
      "v_mfma_f32_16x16x32_bf16 v[24:27], a[24:27], a[196:199], 0\n"
      "ds_read_b128 a[24:27], v7 offset:4608\n"
      "v_sub_f32_e32 v51, v39, v43\n"
      "v_mfma_f32_16x16x32_bf16 v[24:27], a[28:31], v[160:163], v[24:27]\n"
      "ds_read_b128 a[28:31], v7 offset:4864\n"
      "v_add_f32_e32 v40, v36, v40\n"
      "v_mfma_f32_16x16x32_bf16 v[24:27], a[32:35], v[216:219], v[24:27]\n"
      "ds_read_b128 a[32:35], v7 offset:5120\n"
      "v_add_f32_e32 v41, v37, v41\n"
      "v_mfma_f32_16x16x32_bf16 v[24:27], a[0:3], v[124:127], v[24:27]\n"
      "ds_read_b128 a[0:3], v6 offset:4608\n"
      "v_add_f32_e32 v42, v38, v42\n"
      "v_mfma_f32_16x16x32_bf16 v[24:27], a[4:7], v[180:183], v[24:27]\n"
      "ds_read_b128 a[4:7], v6 offset:4864\n"
      "v_add_f32_e32 v43, v39, v43\n"
      "v_mfma_f32_16x16x32_bf16 v[24:27], a[8:11], v[232:235], v[24:27]\n"
      "ds_read_b128 a[8:11], v6 offset:5120\n"
      "v_cvt_pk_bf16_f32 v48, v48, v49\n"
      "v_mfma_f32_16x16x32_bf16 v[24:27], a[12:15], a[216:219], v[24:27]\n"
      "ds_read_b128 a[12:15], v7 offset:0\n"
      "v_cvt_pk_bf16_f32 v49, v50, v51\n"
      "v_mfma_f32_16x16x32_bf16 v[24:27], a[16:19], v[196:199], v[24:27]\n"
      "ds_read_b128 a[16:19], v7 offset:256\n"
      "v_cvt_pk_bf16_f32 v40, v40, v41\n"
      "v_mfma_f32_16x16x32_bf16 v[24:27], a[20:23], v[252:255], v[24:27]\n"
      "s_waitcnt lgkmcnt(7)\n"
      "ds_read_b128 a[20:23], v7 offset:512\n"
      "v_cvt_pk_bf16_f32 v41, v42, v43\n"
      "v_mfma_f32_16x16x32_bf16 v[56:59], a[52:55], a[76:79], 0\n"
      "ds_write_addtid_b32 v21 offset:49416\n"
      "v_cvt_pk_bf16_f32 v44, v44, v45\n"
      "v_mfma_f32_16x16x32_bf16 v[28:31], a[24:27], a[160:163], 0\n"
      "s_waitcnt lgkmcnt(7)\n"
      "v_cvt_pk_bf16_f32 v45, v46, v47\n"
      "v_cvt_pk_bf16_f32 v24, v24, v25\n"
      "v_mfma_f32_16x16x32_bf16 v[28:31], a[28:31], a[172:175], v[28:31]\n"
      "s_mov_b32 m0, s18\n"
      "v_cvt_pk_bf16_f32 v25, v26, v27\n"
      "v_sub_f32_e32 v64, v52, v56\n"
      "v_mfma_f32_16x16x32_bf16 v[28:31], a[32:35], a[184:187], v[28:31]\n"
      "s_waitcnt lgkmcnt(5)\n"
      "ds_write_addtid_b32 v48 offset:1792\n"
      "v_sub_f32_e32 v65, v53, v57\n"
      "v_mfma_f32_16x16x32_bf16 v[28:31], a[0:3], a[88:91], v[28:31]\n"
      "ds_write_addtid_b32 v49 offset:38720\n"
      "v_cvt_pk_bf16_f32 v64, v64, v65\n"
      "v_mfma_f32_16x16x32_bf16 v[28:31], a[4:7], a[100:103], v[28:31]\n"
      "s_waitcnt lgkmcnt(5)\n"
      "ds_write_addtid_b32 v40 offset:1024\n"
      "v_sub_f32_e32 v66, v54, v58\n"
      "v_mfma_f32_16x16x32_bf16 v[28:31], a[8:11], a[112:115], v[28:31]\n"
      "ds_write_addtid_b32 v41 offset:37952\n"
      "v_sub_f32_e32 v67, v55, v59\n"
      "v_mfma_f32_16x16x32_bf16 v[28:31], a[12:15], a[124:127], v[28:31]\n"
      "s_waitcnt lgkmcnt(5)\n"
      "ds_write_addtid_b32 v44 offset:256\n"
      "v_cvt_pk_bf16_f32 v65, v66, v67\n"
      "v_mfma_f32_16x16x32_bf16 v[28:31], a[16:19], a[136:139], v[28:31]\n"
      "ds_write_addtid_b32 v45 offset:37184\n"
      "v_add_f32_e32 v56, v52, v56\n"
      "v_mfma_f32_16x16x32_bf16 v[28:31], a[20:23], a[148:151], v[28:31]\n"
      "s_mov_b32 m0, s45\n"
      "v_add_f32_e32 v57, v53, v57\n"
      "v_cvt_pk_bf16_f32 v56, v56, v57\n"
      "v_mfma_f32_16x16x32_bf16 v[20:23], a[24:27], a[164:167], 0\n"
      "s_mov_b32 m0, s58\n"
      "v_add_f32_e32 v58, v54, v58\n"
      "v_add_f32_e32 v59, v55, v59\n"
      "v_mfma_f32_16x16x32_bf16 v[20:23], a[28:31], a[176:179], v[20:23]\n"
      "buffer_load_dwordx4 v18, s[32:35], s53 offen offset:2048 lds\n"
      "v_cvt_pk_bf16_f32 v57, v58, v59\n"
      "v_mfma_f32_16x16x32_bf16 v[20:23], a[32:35], a[188:191], v[20:23]\n"
      "s_mov_b32 m0, s45\n"
      "ds_read2_b64 a[68:71], v12 offset0:1 offset1:65\n"
      "v_cvt_pk_bf16_f32 v60, v60, v61\n"
      "v_mfma_f32_16x16x32_bf16 v[20:23], a[0:3], a[92:95], v[20:23]\n"
      "ds_write_addtid_b32 v24 offset:36864\n"
      "v_cvt_pk_bf16_f32 v61, v62, v63\n"
      "v_mfma_f32_16x16x32_bf16 v[20:23], a[4:7], a[104:107], v[20:23]\n"
      "ds_write_addtid_b32 v25 offset:37120\n"
      "v_cvt_pk_bf16_f32 v28, v28, v29\n"
      "v_mfma_f32_16x16x32_bf16 v[20:23], a[8:11], a[116:119], v[20:23]\n"
      "s_mov_b32 m0, s18\n"
      "v_cvt_pk_bf16_f32 v29, v30, v31\n"
      "v_cvt_pk_bf16_f32 v82, v76, v77\n"
      "v_mfma_f32_16x16x32_bf16 v[20:23], a[12:15], a[128:131], v[20:23]\n"
      "ds_write_addtid_b32 v64 offset:2048\n"
      "v_cvt_pk_bf16_f32 v88, v78, v79\n"
      "v_mfma_f32_16x16x32_bf16 v[20:23], a[16:19], a[140:143], v[20:23]\n"
      "ds_write_addtid_b32 v65 offset:38976\n"
      "v_swap_b32 v6, v7\n"
      "v_mfma_f32_16x16x32_bf16 v[20:23], a[20:23], a[152:155], v[20:23]\n"
      "ds_write_addtid_b32 v56 offset:1280\n"
      "s_nop 0\n"
      "v_mfma_f32_16x16x32_bf16 v[68:71], a[84:87], a[60:63], 0\n"
      "ds_write_addtid_b32 v57 offset:38208\n"
      "s_nop 0\n"
      "v_mfma_f32_16x16x32_bf16 v[32:35], a[24:27], a[168:171], 0\n"
      "ds_write_addtid_b32 v60 offset:512\n"
      "v_mfma_f32_16x16x32_bf16 v[32:35], a[28:31], a[180:183], v[32:35]\n"
      "ds_write_addtid_b32 v61 offset:37440\n"
      "v_cvt_pk_bf16_f32 v20, v20, v21\n"
      "v_mfma_f32_16x16x32_bf16 v[32:35], a[32:35], a[192:195], v[32:35]\n"
      "s_mov_b32 m0, s45\n"
      "v_cvt_pk_bf16_f32 v83, v68, v69\n"
      "v_cvt_pk_bf16_f32 v21, v22, v23\n"
      "v_mfma_f32_16x16x32_bf16 v[32:35], a[0:3], a[96:99], v[32:35]\n"
      "ds_write_addtid_b32 v28 offset:60432\n"
      "v_cvt_pk_bf16_f32 v89, v70, v71\n"
      "v_mfma_f32_16x16x32_bf16 v[32:35], a[4:7], a[108:111], v[32:35]\n"
      "ds_write_addtid_b32 v29 offset:60688\n"
      "v_mfma_f32_16x16x32_bf16 v[32:35], a[8:11], a[120:123], v[32:35]\n"
      "ds_write_addtid_b32 v20 offset:48136\n"
      "v_mfma_f32_16x16x32_bf16 v[32:35], a[12:15], a[132:135], v[32:35]\n"
      "buffer_store_dwordx4 v[80:83], v14, s[36:39], s52 offen sc1\n"
      "v_mfma_f32_16x16x32_bf16 v[32:35], a[16:19], a[144:147], v[32:35]\n"
      "ds_write_addtid_b32 v21 offset:48392\n"
      "v_mfma_f32_16x16x32_bf16 v[32:35], a[20:23], a[156:159], v[32:35]\n"
      "s_nop 1\n"
      "v_mfma_f32_16x16x32_bf16 v[72:75], a[84:87], a[64:67], 0\n"
      "s_waitcnt lgkmcnt(12)\n"
      "s_nop 1\n"
      "v_mfma_f32_16x16x32_bf16 v[76:79], a[84:87], a[68:71], 0\n"
      "buffer_store_dwordx4 v[86:89], v14, s[36:39], s52 offen offset:1536 sc1\n"
      "v_cvt_pk_bf16_f32 v32, v32, v33\n"
      "v_mfma_f32_16x16x32_bf16 v[20:23], a[12:15], a[88:91], 0\n"
      "s_add_u32 s51, s51, s12\n"
      "ds_write_addtid_b32 v32 offset:35840\n"
      "v_cvt_pk_bf16_f32 v33, v34, v35\n"
      "v_mfma_f32_16x16x32_bf16 v[20:23], a[16:19], a[100:103], v[20:23]\n"
      "s_sub_u32 s47, s51, s48\n"
      "ds_write_addtid_b32 v33 offset:36096\n"
      "v_cvt_pk_bf16_f32 v84, v72, v73\n"
      "v_mfma_f32_16x16x32_bf16 v[20:23], a[20:23], a[112:115], v[20:23]\n"
      "s_cmp_ge_u32 vcc_lo, 0x40000000\n"
      "v_cvt_pk_bf16_f32 v85, v76, v77\n"
      "v_cvt_pk_bf16_f32 v90, v74, v75\n"
      "v_mfma_f32_16x16x32_bf16 v[20:23], a[24:27], a[124:127], v[20:23]\n"
      "buffer_store_dwordx2 v[84:85], v15, s[36:39], s52 offen sc1\n"
      "v_cvt_pk_bf16_f32 v91, v78, v79\n"
      "v_mfma_f32_16x16x32_bf16 v[20:23], a[28:31], a[136:139], v[20:23]\n"
      "s_cselect_b32 s53, s47, s51\n"
      "v_mfma_f32_16x16x32_bf16 v[20:23], a[32:35], a[148:151], v[20:23]\n"
      "s_mul_i32 vcc_lo, vcc_lo, 2\n"
      "v_mfma_f32_16x16x32_bf16 v[24:27], a[12:15], a[92:95], 0\n"
      "buffer_store_dwordx2 v[90:91], v15, s[36:39], s52 offen offset:1536 sc1\n"
      "v_mfma_f32_16x16x32_bf16 v[24:27], a[16:19], a[104:107], v[24:27]\n"
      "s_mov_b32 s52, s13\n"
      "v_mfma_f32_16x16x32_bf16 v[24:27], a[20:23], a[116:119], v[24:27]\n"
      "v_mfma_f32_16x16x32_bf16 v[24:27], a[24:27], a[128:131], v[24:27]\n"
      "v_mfma_f32_16x16x32_bf16 v[24:27], a[28:31], a[140:143], v[24:27]\n"
      "s_waitcnt vmcnt(12) lgkmcnt(0)\n"
      "s_barrier\n"
      "v_mfma_f32_16x16x32_bf16 v[24:27], a[32:35], a[152:155], v[24:27]\n"
      "s_add_u32 m0, s58, 13280\n"
      "ds_read2_b64 a[60:63], v11 offset0:0 offset1:64\n"
      "v_mfma_f32_16x16x32_bf16 v[28:31], a[12:15], a[96:99], 0\n"
      "s_add_u32 s13, s13, s12\n"
      "ds_read_b128 a[0:3], v7 offset:0\n"
      "v_mfma_f32_16x16x32_bf16 v[28:31], a[16:19], a[108:111], v[28:31]\n"
      "ds_read_b128 a[4:7], v7 offset:256\n"
      "v_mfma_f32_16x16x32_bf16 v[28:31], a[20:23], a[120:123], v[28:31]\n"
      "ds_read_b128 a[8:11], v7 offset:512\n"
      "v_mfma_f32_16x16x32_bf16 v[28:31], a[24:27], a[132:135], v[28:31]\n"
      "ds_read_b128 a[36:39], v2 offset:0\n"
      "v_mfma_f32_16x16x32_bf16 v[28:31], a[28:31], a[144:147], v[28:31]\n"
      "ds_read_b128 a[44:47], v4 offset:0\n"
      "v_mfma_f32_16x16x32_bf16 v[28:31], a[32:35], a[156:159], v[28:31]\n"
      "s_waitcnt lgkmcnt(5)\n"
      "ds_read_b128 a[40:43], v3 offset:0\n"
      "v_mfma_f32_16x16x32_bf16 v[68:71], a[84:87], a[60:63], 0\n"
      "s_waitcnt lgkmcnt(5)\n"
      "ds_read_b128 a[12:15], v6 offset:768\n"
      "v_mfma_f32_16x16x32_bf16 v[20:23], a[0:3], a[160:163], v[20:23]\n"
      "s_waitcnt lgkmcnt(5)\n"
      "ds_read_b128 a[16:19], v6 offset:1024\n"
      "v_mfma_f32_16x16x32_bf16 v[20:23], a[4:7], a[172:175], v[20:23]\n"
      "s_waitcnt lgkmcnt(5)\n"
      "ds_read_b128 a[20:23], v6 offset:1280\n"
      "v_cvt_pk_bf16_f32 v80, v68, v69\n"
      "v_mfma_f32_16x16x32_bf16 v[20:23], a[8:11], a[184:187], v[20:23]\n"
      "ds_read_b128 a[24:27], v6 offset:5376\n"
      "v_cvt_pk_bf16_f32 v86, v70, v71\n"
      "v_mfma_f32_16x16x32_bf16 v[24:27], a[0:3], a[164:167], v[24:27]\n"
      "buffer_load_dwordx4 v16, s[32:35], s53 offen lds\n"
      "s_nop 0\n"
      "v_mfma_f32_16x16x32_bf16 v[24:27], a[4:7], a[176:179], v[24:27]\n"
      "s_mov_b32 m0, s45\n"
      "ds_read_b128 a[28:31], v6 offset:5632\n"
      "v_cvt_pk_bf16_f32 v20, v20, v21\n"
      "v_mfma_f32_16x16x32_bf16 v[24:27], a[8:11], a[188:191], v[24:27]\n"
      "s_waitcnt lgkmcnt(6)\n"
      "ds_read_b128 a[32:35], v6 offset:5888\n"
      "v_cvt_pk_bf16_f32 v21, v22, v23\n"
      "v_mfma_f32_16x16x32_bf16 v[36:39], a[36:39], a[72:75], 0\n"
      "ds_read2_b64 a[64:67], v11 offset0:1 offset1:65\n"
      "s_nop 0\n"
      "v_mfma_f32_16x16x32_bf16 v[44:47], a[44:47], a[80:83], v[36:39]\n"
      "ds_write_addtid_b32 v20 offset:57360\n"
      "v_cvt_pk_bf16_f32 v24, v24, v25\n"
      "v_mfma_f32_16x16x32_bf16 v[28:31], a[0:3], a[168:171], v[28:31]\n"
      "ds_read_b128 a[0:3], v7 offset:768\n"
      "v_cvt_pk_bf16_f32 v25, v26, v27\n"
      "v_mfma_f32_16x16x32_bf16 v[28:31], a[4:7], a[180:183], v[28:31]\n"
      "ds_read_b128 a[4:7], v7 offset:1024\n"
      "v_mfma_f32_16x16x32_bf16 v[28:31], a[8:11], a[192:195], v[28:31]\n"
      "s_waitcnt lgkmcnt(6)\n"
      "ds_read_b128 a[8:11], v7 offset:1280\n"
      "v_cvt_pk_bf16_f32 v44, v44, v45\n"
      "v_mfma_f32_16x16x32_bf16 v[40:43], a[40:43], a[76:79], 0\n"
      "ds_write_addtid_b32 v21 offset:57616\n"
      "v_cvt_pk_bf16_f32 v45, v46, v47\n"
      "v_mfma_f32_16x16x32_bf16 v[32:35], a[12:15], v[92:95], 0\n"
      "ds_read_b128 a[48:51], v2 offset:64\n"
      "v_cvt_pk_bf16_f32 v28, v28, v29\n"
      "v_mfma_f32_16x16x32_bf16 v[32:35], a[16:19], v[148:151], v[32:35]\n"
      "ds_write_addtid_b32 v24 offset:45064\n"
      "v_cvt_pk_bf16_f32 v29, v30, v31\n"
      "v_mfma_f32_16x16x32_bf16 v[32:35], a[20:23], v[204:207], v[32:35]\n"
      "ds_read_b128 a[52:55], v3 offset:64\n"
      "v_sub_f32_e32 v48, v36, v40\n"
      "v_mfma_f32_16x16x32_bf16 v[32:35], a[24:27], v[112:115], v[32:35]\n"
      "s_waitcnt lgkmcnt(8)\n"
      "ds_write_addtid_b32 v25 offset:45320\n"
      "v_sub_f32_e32 v49, v37, v41\n"
      "v_mfma_f32_16x16x32_bf16 v[32:35], a[28:31], a[208:211], v[32:35]\n"
      "ds_read_b128 a[56:59], v4 offset:64\n"
      "v_sub_f32_e32 v50, v38, v42\n"
      "v_mfma_f32_16x16x32_bf16 v[32:35], a[32:35], v[220:223], v[32:35]\n"
      "s_waitcnt lgkmcnt(7)\n"
      "ds_read_b128 a[36:39], v2 offset:128\n"
      "v_sub_f32_e32 v51, v39, v43\n"
      "v_mfma_f32_16x16x32_bf16 v[32:35], a[0:3], a[212:215], v[32:35]\n"
      "ds_write_addtid_b32 v28 offset:32768\n"
      "v_add_f32_e32 v40, v36, v40\n"
      "v_mfma_f32_16x16x32_bf16 v[32:35], a[4:7], v[184:187], v[32:35]\n"
      "s_waitcnt lgkmcnt(8)\n"
      "ds_write_addtid_b32 v29 offset:33024\n"
      "v_add_f32_e32 v41, v37, v41\n"
      "v_mfma_f32_16x16x32_bf16 v[32:35], a[8:11], v[240:243], v[32:35]\n"
      "s_mov_b32 m0, s18\n"
      "ds_read_b128 a[44:47], v4 offset:128\n"
      "v_add_f32_e32 v42, v38, v42\n"
      "v_mfma_f32_16x16x32_bf16 v[72:75], a[84:87], a[64:67], 0\n"
      "ds_read2_b64 a[68:71], v11 offset0:2 offset1:66\n"
      "v_add_f32_e32 v43, v39, v43\n"
      "v_mfma_f32_16x16x32_bf16 v[20:23], a[12:15], v[100:103], 0\n"
      "v_cvt_pk_bf16_f32 v48, v48, v49\n"
      "v_cvt_pk_bf16_f32 v49, v50, v51\n"
      "v_mfma_f32_16x16x32_bf16 v[20:23], a[16:19], a[200:203], v[20:23]\n"
      "ds_write_addtid_b32 v48 offset:6144\n"
      "v_cvt_pk_bf16_f32 v40, v40, v41\n"
      "v_mfma_f32_16x16x32_bf16 v[20:23], a[20:23], v[208:211], v[20:23]\n"
      "ds_write_addtid_b32 v49 offset:43072\n"
      "v_cvt_pk_bf16_f32 v41, v42, v43\n"
      "v_mfma_f32_16x16x32_bf16 v[20:23], a[24:27], a[204:207], v[20:23]\n"
      "ds_write_addtid_b32 v40 offset:5376\n"
      "v_cvt_pk_bf16_f32 v32, v32, v33\n"
      "v_mfma_f32_16x16x32_bf16 v[20:23], a[28:31], v[172:175], v[20:23]\n"
      "ds_write_addtid_b32 v41 offset:42304\n"
      "v_cvt_pk_bf16_f32 v33, v34, v35\n"
      "v_mfma_f32_16x16x32_bf16 v[20:23], a[32:35], v[228:231], v[20:23]\n"
      "ds_write_addtid_b32 v44 offset:4608\n"
      "v_cvt_pk_bf16_f32 v81, v72, v73\n"
      "v_mfma_f32_16x16x32_bf16 v[20:23], a[0:3], v[136:139], v[20:23]\n"
      "ds_write_addtid_b32 v45 offset:41536\n"
      "v_cvt_pk_bf16_f32 v87, v74, v75\n"
      "v_mfma_f32_16x16x32_bf16 v[20:23], a[4:7], v[192:195], v[20:23]\n"
      "s_mov_b32 m0, s45\n"
      "ds_read_b128 a[40:43], v3 offset:128\n"
      "v_mfma_f32_16x16x32_bf16 v[20:23], a[8:11], v[244:247], v[20:23]\n"
      "s_waitcnt lgkmcnt(7)\n"
      "ds_read2_b64 a[60:63], v11 offset0:3 offset1:67\n"
      "s_nop 0\n"
      "v_mfma_f32_16x16x32_bf16 v[52:55], a[48:51], a[72:75], 0\n"
      "s_add_u32 m0, s58, 13280\n"
      "ds_read2_b64 a[64:67], v13 offset0:0 offset1:64\n"
      "s_nop 0\n"
      "v_mfma_f32_16x16x32_bf16 v[56:59], a[52:55], a[76:79], 0\n"
      "buffer_load_dwordx4 v17, s[32:35], s53 offen offset:1024 lds\n"
      "v_cvt_pk_bf16_f32 v20, v20, v21\n"
      "v_mfma_f32_16x16x32_bf16 v[60:63], a[56:59], a[80:83], v[52:55]\n"
      "s_mov_b32 m0, s45\n"
      "v_cvt_pk_bf16_f32 v21, v22, v23\n"
      "s_nop 0\n"
      "v_mfma_f32_16x16x32_bf16 v[36:39], a[36:39], a[72:75], 0\n"
      "ds_write_addtid_b32 v32 offset:58384\n"
      "v_sub_f32_e32 v64, v52, v56\n"
      "v_mfma_f32_16x16x32_bf16 v[44:47], a[44:47], a[80:83], v[36:39]\n"
      "ds_write_addtid_b32 v33 offset:58640\n"
      "v_sub_f32_e32 v65, v53, v57\n"
      "v_mfma_f32_16x16x32_bf16 v[76:79], a[84:87], a[68:71], 0\n"
      "global_load_dword v8, v9, s[54:55]\n"
      "v_sub_f32_e32 v66, v54, v58\n"
      "v_mfma_f32_16x16x32_bf16 v[24:27], a[12:15], a[196:199], 0\n"
      "ds_read_b128 a[12:15], v6 offset:1536\n"
      "v_sub_f32_e32 v67, v55, v59\n"
      "v_mfma_f32_16x16x32_bf16 v[24:27], a[16:19], v[160:163], v[24:27]\n"
      "ds_read_b128 a[16:19], v6 offset:1792\n"
      "v_add_f32_e32 v56, v52, v56\n"
      "v_mfma_f32_16x16x32_bf16 v[24:27], a[20:23], v[216:219], v[24:27]\n"
      "ds_read_b128 a[20:23], v6 offset:2048\n"
      "v_add_f32_e32 v57, v53, v57\n"
      "v_mfma_f32_16x16x32_bf16 v[24:27], a[24:27], v[124:127], v[24:27]\n"
      "ds_read_b128 a[24:27], v6 offset:6144\n"
      "v_add_f32_e32 v58, v54, v58\n"
      "v_mfma_f32_16x16x32_bf16 v[24:27], a[28:31], v[180:183], v[24:27]\n"
      "ds_read_b128 a[28:31], v6 offset:6400\n"
      "v_add_f32_e32 v59, v55, v59\n"
      "v_mfma_f32_16x16x32_bf16 v[24:27], a[32:35], v[232:235], v[24:27]\n"
      "ds_read_b128 a[32:35], v6 offset:6656\n"
      "v_cvt_pk_bf16_f32 v64, v64, v65\n"
      "v_mfma_f32_16x16x32_bf16 v[24:27], a[0:3], a[216:219], v[24:27]\n"
      "ds_read_b128 a[0:3], v7 offset:1536\n"
      "v_cvt_pk_bf16_f32 v65, v66, v67\n"
      "v_mfma_f32_16x16x32_bf16 v[24:27], a[4:7], v[196:199], v[24:27]\n"
      "ds_read_b128 a[4:7], v7 offset:1792\n"
      "v_cvt_pk_bf16_f32 v56, v56, v57\n"
      "v_mfma_f32_16x16x32_bf16 v[24:27], a[8:11], v[252:255], v[24:27]\n"
      "s_waitcnt lgkmcnt(7)\n"
      "ds_read_b128 a[8:11], v7 offset:2048\n"
      "v_cvt_pk_bf16_f32 v57, v58, v59\n"
      "v_mfma_f32_16x16x32_bf16 v[40:43], a[40:43], a[76:79], 0\n"
      "ds_write_addtid_b32 v20 offset:46088\n"
      "v_cvt_pk_bf16_f32 v60, v60, v61\n"
      "v_mfma_f32_16x16x32_bf16 v[28:31], a[12:15], v[96:99], 0\n"
      "s_waitcnt lgkmcnt(7)\n"
      "ds_write_addtid_b32 v21 offset:46344\n"
      "v_cvt_pk_bf16_f32 v61, v62, v63\n"
      "v_mfma_f32_16x16x32_bf16 v[28:31], a[16:19], v[152:155], v[28:31]\n"
      "s_mov_b32 m0, s18\n"
      "v_cvt_pk_bf16_f32 v24, v24, v25\n"
      "v_cvt_pk_bf16_f32 v25, v26, v27\n"
      "v_mfma_f32_16x16x32_bf16 v[28:31], a[20:23], a[220:223], v[28:31]\n"
      "s_waitcnt lgkmcnt(6)\n"
      "ds_write_addtid_b32 v64 offset:6400\n"
      "v_sub_f32_e32 v48, v36, v40\n"
      "v_mfma_f32_16x16x32_bf16 v[28:31], a[24:27], v[116:119], v[28:31]\n"
      "ds_write_addtid_b32 v65 offset:43328\n"
      "v_sub_f32_e32 v49, v37, v41\n"
      "v_mfma_f32_16x16x32_bf16 v[28:31], a[28:31], v[168:171], v[28:31]\n"
      "s_waitcnt lgkmcnt(6)\n"
      "ds_write_addtid_b32 v56 offset:5632\n"
      "v_cvt_pk_bf16_f32 v48, v48, v49\n"
      "v_mfma_f32_16x16x32_bf16 v[28:31], a[32:35], v[224:227], v[28:31]\n"
      "ds_write_addtid_b32 v57 offset:42560\n"
      "v_sub_f32_e32 v50, v38, v42\n"
      "v_mfma_f32_16x16x32_bf16 v[28:31], a[0:3], v[132:135], v[28:31]\n"
      "s_waitcnt lgkmcnt(6)\n"
      "ds_write_addtid_b32 v60 offset:4864\n"
      "v_sub_f32_e32 v51, v39, v43\n"
      "v_mfma_f32_16x16x32_bf16 v[28:31], a[4:7], v[188:191], v[28:31]\n"
      "ds_write_addtid_b32 v61 offset:41792\n"
      "v_cvt_pk_bf16_f32 v49, v50, v51\n"
      "v_mfma_f32_16x16x32_bf16 v[28:31], a[8:11], a[240:243], v[28:31]\n"
      "s_mov_b32 m0, s45\n"
      "v_add_f32_e32 v40, v36, v40\n"
      "v_add_f32_e32 v41, v37, v41\n"
      "v_mfma_f32_16x16x32_bf16 v[20:23], a[12:15], v[104:107], 0\n"
      "s_add_u32 m0, s58, 13280\n"
      "v_cvt_pk_bf16_f32 v40, v40, v41\n"
      "v_add_f32_e32 v42, v38, v42\n"
      "v_mfma_f32_16x16x32_bf16 v[20:23], a[16:19], v[156:159], v[20:23]\n"
      "buffer_load_dwordx4 v18, s[32:35], s53 offen offset:2048 lds\n"
      "v_add_f32_e32 v43, v39, v43\n"
      "v_mfma_f32_16x16x32_bf16 v[20:23], a[20:23], v[212:215], v[20:23]\n"
      "s_mov_b32 m0, s45\n"
      "v_cvt_pk_bf16_f32 v41, v42, v43\n"
      "v_cvt_pk_bf16_f32 v44, v44, v45\n"
      "v_mfma_f32_16x16x32_bf16 v[20:23], a[24:27], v[120:123], v[20:23]\n"
      "ds_write_addtid_b32 v24 offset:33792\n"
      "v_cvt_pk_bf16_f32 v45, v46, v47\n"
      "v_mfma_f32_16x16x32_bf16 v[20:23], a[28:31], v[176:179], v[20:23]\n"
      "ds_write_addtid_b32 v25 offset:34048\n"
      "v_cvt_pk_bf16_f32 v28, v28, v29\n"
      "v_mfma_f32_16x16x32_bf16 v[20:23], a[32:35], a[232:235], v[20:23]\n"
      "s_mov_b32 m0, s18\n"
      "ds_read2_b64 a[68:71], v13 offset0:1 offset1:65\n"
      "v_cvt_pk_bf16_f32 v29, v30, v31\n"
      "v_mfma_f32_16x16x32_bf16 v[20:23], a[0:3], v[140:143], v[20:23]\n"
      "s_sub_u32 s18, s19, s18\n"
      "ds_write_addtid_b32 v48 offset:6656\n"
      "v_cvt_pk_bf16_f32 v82, v76, v77\n"
      "v_mfma_f32_16x16x32_bf16 v[20:23], a[4:7], a[236:239], v[20:23]\n"
      "ds_write_addtid_b32 v49 offset:43584\n"
      "v_cvt_pk_bf16_f32 v88, v78, v79\n"
      "v_mfma_f32_16x16x32_bf16 v[20:23], a[8:11], v[248:251], v[20:23]\n"
      "ds_write_addtid_b32 v40 offset:5888\n"
      "s_nop 0\n"
      "v_mfma_f32_16x16x32_bf16 v[68:71], a[84:87], a[60:63], 0\n"
      "ds_write_addtid_b32 v41 offset:42816\n"
      "s_nop 0\n"
      "v_mfma_f32_16x16x32_bf16 v[32:35], a[12:15], v[108:111], 0\n"
      "ds_write_addtid_b32 v44 offset:5120\n"
      "v_mfma_f32_16x16x32_bf16 v[32:35], a[16:19], v[164:167], v[32:35]\n"
      "ds_write_addtid_b32 v45 offset:42048\n"
      "v_cvt_pk_bf16_f32 v20, v20, v21\n"
      "v_mfma_f32_16x16x32_bf16 v[32:35], a[20:23], a[224:227], v[32:35]\n"
      "s_mov_b32 m0, s45\n"
      "v_cvt_pk_bf16_f32 v83, v68, v69\n"
      "v_cvt_pk_bf16_f32 v21, v22, v23\n"
      "v_mfma_f32_16x16x32_bf16 v[32:35], a[24:27], v[128:131], v[32:35]\n"
      "ds_write_addtid_b32 v28 offset:59408\n"
      "v_cvt_pk_bf16_f32 v89, v70, v71\n"
      "v_mfma_f32_16x16x32_bf16 v[32:35], a[28:31], a[228:231], v[32:35]\n"
      "ds_write_addtid_b32 v29 offset:59664\n"
      "v_mfma_f32_16x16x32_bf16 v[32:35], a[32:35], v[236:239], v[32:35]\n"
      "ds_write_addtid_b32 v20 offset:47112\n"
      "v_mfma_f32_16x16x32_bf16 v[32:35], a[0:3], v[144:147], v[32:35]\n"
      "buffer_store_dwordx4 v[80:83], v14, s[36:39], s52 offen sc1\n"
      "v_mfma_f32_16x16x32_bf16 v[32:35], a[4:7], v[200:203], v[32:35]\n"
      "ds_write_addtid_b32 v21 offset:47368\n"
      "v_mfma_f32_16x16x32_bf16 v[32:35], a[8:11], a[244:247], v[32:35]\n"
      "s_nop 1\n"
      "v_mfma_f32_16x16x32_bf16 v[72:75], a[84:87], a[64:67], 0\n"
      "s_waitcnt lgkmcnt(10)\n"
      "s_nop 1\n"
      "v_mfma_f32_16x16x32_bf16 v[76:79], a[84:87], a[68:71], 0\n"
      "buffer_store_dwordx4 v[86:89], v14, s[36:39], s52 offen offset:1536 sc1\n"
      "v_cvt_pk_bf16_f32 v32, v32, v33\n"
      "s_cbranch_vccnz Lconv_loop1\n"
      "Lconv_t1:\n"
      "ds_write_addtid_b32 v32 offset:34816\n"
      "v_cvt_pk_bf16_f32 v33, v34, v35\n"
      "v_mfma_f32_16x16x32_bf16 v[20:23], a[24:27], v[96:99], 0\n"
      "ds_write_addtid_b32 v33 offset:35072\n"
      "v_cvt_pk_bf16_f32 v84, v72, v73\n"
      "v_mfma_f32_16x16x32_bf16 v[20:23], a[28:31], v[152:155], v[20:23]\n"
      "v_cvt_pk_bf16_f32 v90, v74, v75\n"
      "v_cvt_pk_bf16_f32 v85, v76, v77\n"
      "v_mfma_f32_16x16x32_bf16 v[20:23], a[32:35], a[220:223], v[20:23]\n"
      "buffer_store_dwordx2 v[84:85], v15, s[36:39], s52 offen sc1\n"
      "v_cvt_pk_bf16_f32 v91, v78, v79\n"
      "v_mfma_f32_16x16x32_bf16 v[20:23], a[0:3], v[116:119], v[20:23]\n"
      "v_mfma_f32_16x16x32_bf16 v[20:23], a[4:7], v[168:171], v[20:23]\n"
      "v_mfma_f32_16x16x32_bf16 v[20:23], a[8:11], v[224:227], v[20:23]\n"
      "buffer_store_dwordx2 v[90:91], v15, s[36:39], s52 offen offset:1536 sc1\n"
      "v_mfma_f32_16x16x32_bf16 v[24:27], a[24:27], v[104:107], 0\n"
      "s_mov_b32 s52, s13\n"
      "v_mfma_f32_16x16x32_bf16 v[24:27], a[28:31], v[156:159], v[24:27]\n"
      "s_add_u32 s13, s13, s12\n"
      "v_mfma_f32_16x16x32_bf16 v[24:27], a[32:35], v[212:215], v[24:27]\n"
      "v_mfma_f32_16x16x32_bf16 v[24:27], a[0:3], v[120:123], v[24:27]\n"
      "v_mfma_f32_16x16x32_bf16 v[24:27], a[4:7], v[176:179], v[24:27]\n"
      "s_waitcnt lgkmcnt(0)\n"
      "s_barrier\n"
      "ds_read2_b64 a[60:63], v10 offset0:0 offset1:64\n"
      "v_mfma_f32_16x16x32_bf16 v[24:27], a[8:11], a[232:235], v[24:27]\n"
      "s_waitcnt lgkmcnt(0)\n"
      "ds_read2_b64 a[64:67], v10 offset0:1 offset1:65\n"
      "v_mfma_f32_16x16x32_bf16 v[68:71], a[84:87], a[60:63], 0\n"
      "ds_read2_b64 a[68:71], v10 offset0:2 offset1:66\n"
      "v_mfma_f32_16x16x32_bf16 v[28:31], a[24:27], v[108:111], 0\n"
      "ds_read_b128 a[12:15], v7 offset:6144\n"
      "v_mfma_f32_16x16x32_bf16 v[28:31], a[28:31], v[164:167], v[28:31]\n"
      "ds_read_b128 a[16:19], v7 offset:6400\n"
      "v_mfma_f32_16x16x32_bf16 v[28:31], a[32:35], a[224:227], v[28:31]\n"
      "ds_read_b128 a[20:23], v7 offset:6656\n"
      "v_mfma_f32_16x16x32_bf16 v[28:31], a[0:3], v[128:131], v[28:31]\n"
      "ds_read2_b64 a[60:63], v10 offset0:3 offset1:67\n"
      "v_cvt_pk_bf16_f32 v80, v68, v69\n"
      "v_mfma_f32_16x16x32_bf16 v[28:31], a[4:7], a[228:231], v[28:31]\n"
      "ds_read_b128 a[24:27], v6 offset:5376\n"
      "v_cvt_pk_bf16_f32 v86, v70, v71\n"
      "v_mfma_f32_16x16x32_bf16 v[28:31], a[8:11], v[236:239], v[28:31]\n"
      "s_waitcnt lgkmcnt(6)\n"
      "ds_read_b128 a[28:31], v6 offset:5632\n"
      "v_mfma_f32_16x16x32_bf16 v[72:75], a[84:87], a[64:67], 0\n"
      "s_waitcnt lgkmcnt(6)\n"
      "ds_read2_b64 a[64:67], v12 offset0:0 offset1:64\n"
      "v_mfma_f32_16x16x32_bf16 v[76:79], a[84:87], a[68:71], 0\n"
      "s_waitcnt lgkmcnt(6)\n"
      "ds_read2_b64 a[68:71], v12 offset0:1 offset1:65\n"
      "s_nop 0\n"
      "v_mfma_f32_16x16x32_bf16 v[20:23], a[12:15], v[132:135], v[20:23]\n"
      "s_waitcnt lgkmcnt(6)\n"
      "ds_read_b128 a[32:35], v6 offset:5888\n"
      "v_cvt_pk_bf16_f32 v81, v72, v73\n"
      "v_mfma_f32_16x16x32_bf16 v[20:23], a[16:19], v[188:191], v[20:23]\n"
      "s_waitcnt lgkmcnt(6)\n"
      "ds_read_b128 a[0:3], v7 offset:768\n"
      "v_cvt_pk_bf16_f32 v82, v76, v77\n"
      "v_mfma_f32_16x16x32_bf16 v[20:23], a[20:23], a[240:243], v[20:23]\n"
      "s_waitcnt lgkmcnt(6)\n"
      "ds_read_b128 a[4:7], v7 offset:1024\n"
      "v_cvt_pk_bf16_f32 v87, v74, v75\n"
      "v_mfma_f32_16x16x32_bf16 v[68:71], a[84:87], a[60:63], 0\n"
      "ds_read_b128 a[8:11], v7 offset:1280\n"
      "v_cvt_pk_bf16_f32 v88, v78, v79\n"
      "v_mfma_f32_16x16x32_bf16 v[24:27], a[12:15], v[140:143], v[24:27]\n"
      "v_mfma_f32_16x16x32_bf16 v[24:27], a[16:19], a[236:239], v[24:27]\n"
      "v_cvt_pk_bf16_f32 v20, v20, v21\n"
      "v_cvt_pk_bf16_f32 v21, v22, v23\n"
      "v_mfma_f32_16x16x32_bf16 v[24:27], a[20:23], v[248:251], v[24:27]\n"
      "s_waitcnt lgkmcnt(5)\n"
      "ds_write_addtid_b32 v20 offset:62480\n"
      "v_cvt_pk_bf16_f32 v83, v68, v69\n"
      "v_mfma_f32_16x16x32_bf16 v[72:75], a[84:87], a[64:67], 0\n"
      "buffer_store_dwordx4 v[80:83], v14, s[36:39], s52 offen sc1\n"
      "v_cvt_pk_bf16_f32 v89, v70, v71\n"
      "v_mfma_f32_16x16x32_bf16 v[28:31], a[12:15], v[144:147], v[28:31]\n"
      "ds_read_b128 a[12:15], v7 offset:5376\n"
      "v_cvt_pk_bf16_f32 v24, v24, v25\n"
      "v_mfma_f32_16x16x32_bf16 v[28:31], a[16:19], v[200:203], v[28:31]\n"
      "ds_read_b128 a[16:19], v7 offset:5632\n"
      "v_cvt_pk_bf16_f32 v25, v26, v27\n"
      "v_mfma_f32_16x16x32_bf16 v[28:31], a[20:23], a[244:247], v[28:31]\n"
      "s_waitcnt lgkmcnt(4)\n"
      "ds_read_b128 a[20:23], v7 offset:5888\n"
      "v_cvt_pk_bf16_f32 v84, v72, v73\n"
      "v_mfma_f32_16x16x32_bf16 v[76:79], a[84:87], a[68:71], 0\n"
      "ds_write_addtid_b32 v21 offset:62736\n"
      "v_cvt_pk_bf16_f32 v90, v74, v75\n"
      "v_mfma_f32_16x16x32_bf16 v[32:35], a[24:27], v[92:95], 0\n"
      "buffer_store_dwordx4 v[86:89], v14, s[36:39], s52 offen offset:1536 sc1\n"
      "v_cvt_pk_bf16_f32 v28, v28, v29\n"
      "v_mfma_f32_16x16x32_bf16 v[32:35], a[28:31], v[148:151], v[32:35]\n"
      "ds_write_addtid_b32 v24 offset:50184\n"
      "v_cvt_pk_bf16_f32 v29, v30, v31\n"
      "v_mfma_f32_16x16x32_bf16 v[32:35], a[32:35], v[204:207], v[32:35]\n"
      "ds_write_addtid_b32 v25 offset:50440\n"
      "v_cvt_pk_bf16_f32 v85, v76, v77\n"
      "v_mfma_f32_16x16x32_bf16 v[32:35], a[0:3], v[112:115], v[32:35]\n"
      "ds_write_addtid_b32 v28 offset:37888\n"
      "v_cvt_pk_bf16_f32 v91, v78, v79\n"
      "v_mfma_f32_16x16x32_bf16 v[32:35], a[4:7], a[208:211], v[32:35]\n"
      "s_waitcnt lgkmcnt(6)\n"
      "ds_write_addtid_b32 v29 offset:38144\n"
      "v_mfma_f32_16x16x32_bf16 v[32:35], a[8:11], v[220:223], v[32:35]\n"
      "buffer_store_dwordx2 v[84:85], v15, s[36:39], s52 offen sc1\n"
      "v_mfma_f32_16x16x32_bf16 v[32:35], a[12:15], a[212:215], v[32:35]\n"
      "s_waitcnt lgkmcnt(5)\n"
      "v_mfma_f32_16x16x32_bf16 v[32:35], a[16:19], v[184:187], v[32:35]\n"
      "v_mfma_f32_16x16x32_bf16 v[32:35], a[20:23], v[240:243], v[32:35]\n"
      "buffer_store_dwordx2 v[90:91], v15, s[36:39], s52 offen offset:1536 sc1\n"
      "s_nop 0\n"
      "v_mfma_f32_16x16x32_bf16 v[20:23], a[24:27], v[100:103], 0\n"
      "s_nop 1\n"
      "v_mfma_f32_16x16x32_bf16 v[20:23], a[28:31], a[200:203], v[20:23]\n"
      "s_nop 0\n"
      "v_mfma_f32_16x16x32_bf16 v[20:23], a[32:35], v[208:211], v[20:23]\n"
      "v_cvt_pk_bf16_f32 v32, v32, v33\n"
      "v_cvt_pk_bf16_f32 v33, v34, v35\n"
      "v_mfma_f32_16x16x32_bf16 v[20:23], a[0:3], a[204:207], v[20:23]\n"
      "ds_write_addtid_b32 v32 offset:61456\n"
      "v_mfma_f32_16x16x32_bf16 v[20:23], a[4:7], v[172:175], v[20:23]\n"
      "ds_write_addtid_b32 v33 offset:61712\n"
      "v_mfma_f32_16x16x32_bf16 v[20:23], a[8:11], v[228:231], v[20:23]\n"
      "v_mfma_f32_16x16x32_bf16 v[20:23], a[12:15], v[136:139], v[20:23]\n"
      "v_mfma_f32_16x16x32_bf16 v[20:23], a[16:19], v[192:195], v[20:23]\n"
      "v_mfma_f32_16x16x32_bf16 v[20:23], a[20:23], v[244:247], v[20:23]\n"
      "s_nop 1\n"
      "v_mfma_f32_16x16x32_bf16 v[24:27], a[24:27], a[196:199], 0\n"
      "ds_read_b128 a[24:27], v7 offset:4608\n"
      "s_nop 0\n"
      "v_mfma_f32_16x16x32_bf16 v[24:27], a[28:31], v[160:163], v[24:27]\n"
      "ds_read_b128 a[28:31], v7 offset:4864\n"
      "v_mfma_f32_16x16x32_bf16 v[24:27], a[32:35], v[216:219], v[24:27]\n"
      "ds_read_b128 a[32:35], v7 offset:5120\n"
      "v_cvt_pk_bf16_f32 v20, v20, v21\n"
      "v_mfma_f32_16x16x32_bf16 v[24:27], a[0:3], v[124:127], v[24:27]\n"
      "ds_read_b128 a[0:3], v6 offset:4608\n"
      "v_cvt_pk_bf16_f32 v21, v22, v23\n"
      "v_mfma_f32_16x16x32_bf16 v[24:27], a[4:7], v[180:183], v[24:27]\n"
      "ds_read_b128 a[4:7], v6 offset:4864\n"
      "v_mfma_f32_16x16x32_bf16 v[24:27], a[8:11], v[232:235], v[24:27]\n"
      "ds_read_b128 a[8:11], v6 offset:5120\n"
      "v_mfma_f32_16x16x32_bf16 v[24:27], a[12:15], a[216:219], v[24:27]\n"
      "ds_read_b128 a[12:15], v7 offset:0\n"
      "v_mfma_f32_16x16x32_bf16 v[24:27], a[16:19], v[196:199], v[24:27]\n"
      "ds_read_b128 a[16:19], v7 offset:256\n"
      "v_mfma_f32_16x16x32_bf16 v[24:27], a[20:23], v[252:255], v[24:27]\n"
      "s_waitcnt lgkmcnt(7)\n"
      "ds_read_b128 a[20:23], v7 offset:512\n"
      "s_nop 0\n"
      "v_mfma_f32_16x16x32_bf16 v[28:31], a[24:27], a[160:163], 0\n"
      "s_waitcnt lgkmcnt(7)\n"
      "ds_write_addtid_b32 v20 offset:49160\n"
      "s_nop 0\n"
      "v_mfma_f32_16x16x32_bf16 v[28:31], a[28:31], a[172:175], v[28:31]\n"
      "s_waitcnt lgkmcnt(7)\n"
      "ds_write_addtid_b32 v21 offset:49416\n"
      "v_cvt_pk_bf16_f32 v24, v24, v25\n"
      "v_mfma_f32_16x16x32_bf16 v[28:31], a[32:35], a[184:187], v[28:31]\n"
      "s_waitcnt lgkmcnt(7)\n"
      "ds_write_addtid_b32 v24 offset:36864\n"
      "v_cvt_pk_bf16_f32 v25, v26, v27\n"
      "v_mfma_f32_16x16x32_bf16 v[28:31], a[0:3], a[88:91], v[28:31]\n"
      "s_waitcnt lgkmcnt(7)\n"
      "ds_write_addtid_b32 v25 offset:37120\n"
      "v_mfma_f32_16x16x32_bf16 v[28:31], a[4:7], a[100:103], v[28:31]\n"
      "s_waitcnt lgkmcnt(7)\n"
      "v_mfma_f32_16x16x32_bf16 v[28:31], a[8:11], a[112:115], v[28:31]\n"
      "s_waitcnt lgkmcnt(6)\n"
      "v_mfma_f32_16x16x32_bf16 v[28:31], a[12:15], a[124:127], v[28:31]\n"
      "s_waitcnt lgkmcnt(5)\n"
      "v_mfma_f32_16x16x32_bf16 v[28:31], a[16:19], a[136:139], v[28:31]\n"
      "s_waitcnt lgkmcnt(4)\n"
      "v_mfma_f32_16x16x32_bf16 v[28:31], a[20:23], a[148:151], v[28:31]\n"
      "s_nop 1\n"
      "v_mfma_f32_16x16x32_bf16 v[20:23], a[24:27], a[164:167], 0\n"
      "s_nop 1\n"
      "v_mfma_f32_16x16x32_bf16 v[20:23], a[28:31], a[176:179], v[20:23]\n"
      "s_nop 0\n"
      "v_mfma_f32_16x16x32_bf16 v[20:23], a[32:35], a[188:191], v[20:23]\n"
      "v_cvt_pk_bf16_f32 v28, v28, v29\n"
      "v_cvt_pk_bf16_f32 v29, v30, v31\n"
      "v_mfma_f32_16x16x32_bf16 v[20:23], a[0:3], a[92:95], v[20:23]\n"
      "ds_write_addtid_b32 v28 offset:60432\n"
      "v_mfma_f32_16x16x32_bf16 v[20:23], a[4:7], a[104:107], v[20:23]\n"
      "ds_write_addtid_b32 v29 offset:60688\n"
      "v_mfma_f32_16x16x32_bf16 v[20:23], a[8:11], a[116:119], v[20:23]\n"
      "v_mfma_f32_16x16x32_bf16 v[20:23], a[12:15], a[128:131], v[20:23]\n"
      "v_mfma_f32_16x16x32_bf16 v[20:23], a[16:19], a[140:143], v[20:23]\n"
      "v_mfma_f32_16x16x32_bf16 v[20:23], a[20:23], a[152:155], v[20:23]\n"
      "s_nop 1\n"
      "v_mfma_f32_16x16x32_bf16 v[32:35], a[24:27], a[168:171], 0\n"
      "s_nop 1\n"
      "v_mfma_f32_16x16x32_bf16 v[32:35], a[28:31], a[180:183], v[32:35]\n"
      "s_nop 0\n"
      "v_mfma_f32_16x16x32_bf16 v[32:35], a[32:35], a[192:195], v[32:35]\n"
      "v_cvt_pk_bf16_f32 v20, v20, v21\n"
      "v_cvt_pk_bf16_f32 v21, v22, v23\n"
      "v_mfma_f32_16x16x32_bf16 v[32:35], a[0:3], a[96:99], v[32:35]\n"
      "ds_write_addtid_b32 v20 offset:48136\n"
      "v_mfma_f32_16x16x32_bf16 v[32:35], a[4:7], a[108:111], v[32:35]\n"
      "ds_write_addtid_b32 v21 offset:48392\n"
      "v_mfma_f32_16x16x32_bf16 v[32:35], a[8:11], a[120:123], v[32:35]\n"
      "v_mfma_f32_16x16x32_bf16 v[32:35], a[12:15], a[132:135], v[32:35]\n"
      "v_mfma_f32_16x16x32_bf16 v[32:35], a[16:19], a[144:147], v[32:35]\n"
      "v_mfma_f32_16x16x32_bf16 v[32:35], a[20:23], a[156:159], v[32:35]\n"
      "s_nop 1\n"
      "s_nop 5\n"
      "v_cvt_pk_bf16_f32 v32, v32, v33\n"
      "v_cvt_pk_bf16_f32 v33, v34, v35\n"
      "ds_write_addtid_b32 v32 offset:35840\n"
      "ds_write_addtid_b32 v33 offset:36096\n"
      "s_waitcnt lgkmcnt(0)\n"
      "s_barrier\n"
      "ds_read2_b64 a[0:3], v11 offset0:0 offset1:64\n"
      "ds_read2_b64 a[4:7], v11 offset0:1 offset1:65\n"
      "ds_read2_b64 a[8:11], v11 offset0:2 offset1:66\n"
      "ds_read2_b64 a[12:15], v11 offset0:3 offset1:67\n"
      "ds_read2_b64 a[16:19], v13 offset0:0 offset1:64\n"
      "ds_read2_b64 a[20:23], v13 offset0:1 offset1:65\n"
      "s_waitcnt lgkmcnt(5)\n"
      "v_mfma_f32_16x16x32_bf16 v[68:71], a[84:87], a[0:3], 0\n"
      "s_waitcnt lgkmcnt(4)\n"
      "v_mfma_f32_16x16x32_bf16 v[72:75], a[84:87], a[4:7], 0\n"
      "s_waitcnt lgkmcnt(3)\n"
      "v_mfma_f32_16x16x32_bf16 v[76:79], a[84:87], a[8:11], 0\n"
      "s_waitcnt lgkmcnt(2)\n"
      "s_nop 2\n"
      "v_cvt_pk_bf16_f32 v80, v68, v69\n"
      "v_cvt_pk_bf16_f32 v86, v70, v71\n"
      "v_mfma_f32_16x16x32_bf16 v[68:71], a[84:87], a[12:15], 0\n"
      "s_waitcnt lgkmcnt(1)\n"
      "v_cvt_pk_bf16_f32 v81, v72, v73\n"
      "v_cvt_pk_bf16_f32 v87, v74, v75\n"
      "v_cvt_pk_bf16_f32 v82, v76, v77\n"
      "v_cvt_pk_bf16_f32 v88, v78, v79\n"
      "v_mfma_f32_16x16x32_bf16 v[72:75], a[84:87], a[16:19], 0\n"
      "s_waitcnt lgkmcnt(0)\n"
      "v_mfma_f32_16x16x32_bf16 v[76:79], a[84:87], a[20:23], 0\n"
      "s_mov_b32 s52, s13\n"
      "v_cvt_pk_bf16_f32 v83, v68, v69\n"
      "v_cvt_pk_bf16_f32 v89, v70, v71\n"
      "s_add_u32 s13, s13, s12\n"
      "buffer_store_dwordx4 v[80:83], v14, s[36:39], s52 offen sc1\n"
      "buffer_store_dwordx4 v[86:89], v14, s[36:39], s52 offen offset:1536 sc1\n"
      "v_cvt_pk_bf16_f32 v84, v72, v73\n"
      "v_cvt_pk_bf16_f32 v90, v74, v75\n"
      "v_cvt_pk_bf16_f32 v85, v76, v77\n"
      "v_cvt_pk_bf16_f32 v91, v78, v79\n"
      "buffer_store_dwordx2 v[84:85], v15, s[36:39], s52 offen sc1\n"
      "buffer_store_dwordx2 v[90:91], v15, s[36:39], s52 offen offset:1536 sc1\n"
      "Lconv_u1:\n"
      "Lconv_tail:\n"
      "s_waitcnt vmcnt(0) lgkmcnt(0)\n"
      "Lconv_end:\n"
      :
      : "v"(threadIdx.x), "s"(blockIdx.x), "s"(__builtin_amdgcn_kernarg_segment_ptr())
      : "v1", "v2", "v3", "v4", "v5", "v6", "v7", "v8", "v9", "v10", "v11", "v12", "v13", "v14", "v15", "v16", "v17", "v18", "v19", "v20", "v21", "v22", "v23", "v24", "v25", "v26", "v27", "v28", "v29", "v30", "v31", "v32", "v33", "v34", "v35", "v36", "v37", "v38", "v39", "v40", "v41", "v42", "v43", "v44", "v45", "v46", "v47", "v48", "v49", "v50", "v51", "v52", "v53", "v54", "v55", "v56", "v57", "v58", "v59", "v60", "v61", "v62", "v63", "v64", "v65", "v66", "v67", "v68", "v69", "v70", "v71", "v72", "v73", "v74", "v75", "v76", "v77", "v78", "v79", "v80", "v81", "v82", "v83", "v84", "v85", "v86", "v87", "v88", "v89", "v90", "v91", "v92", "v93", "v94", "v95", "v96", "v97", "v98", "v99", "v100", "v101", "v102", "v103", "v104", "v105", "v106", "v107", "v108", "v109", "v110", "v111", "v112", "v113", "v114", "v115", "v116", "v117", "v118", "v119", "v120", "v121", "v122", "v123", "v124", "v125", "v126", "v127", "v128", "v129", "v130", "v131", "v132", "v133", "v134", "v135", "v136", "v137", "v138", "v139", "v140", "v141", "v142", "v143", "v144", "v145", "v146", "v147", "v148", "v149", "v150", "v151", "v152", "v153", "v154", "v155", "v156", "v157", "v158", "v159", "v160", "v161", "v162", "v163", "v164", "v165", "v166", "v167", "v168", "v169", "v170", "v171", "v172", "v173", "v174", "v175", "v176", "v177", "v178", "v179", "v180", "v181", "v182", "v183", "v184", "v185", "v186", "v187", "v188", "v189", "v190", "v191", "v192", "v193", "v194", "v195", "v196", "v197", "v198", "v199", "v200", "v201", "v202", "v203", "v204", "v205", "v206", "v207", "v208", "v209", "v210", "v211", "v212", "v213", "v214", "v215", "v216", "v217", "v218", "v219", "v220", "v221", "v222", "v223", "v224", "v225", "v226", "v227", "v228", "v229", "v230", "v231", "v232", "v233", "v234", "v235", "v236", "v237", "v238", "v239", "v240", "v241", "v242", "v243", "v244", "v245", "v246", "v247", "v248", "v249", "v250", "v251", "v252", "v253", "v254", "v255", "a0", "a1", "a2", "a3", "a4", "a5", "a6", "a7", "a8", "a9", "a10", "a11", "a12", "a13", "a14", "a15", "a16", "a17", "a18", "a19", "a20", "a21", "a22", "a23", "a24", "a25", "a26", "a27", "a28", "a29", "a30", "a31", "a32", "a33", "a34", "a35", "a36", "a37", "a38", "a39", "a40", "a41", "a42", "a43", "a44", "a45", "a46", "a47", "a48", "a49", "a50", "a51", "a52", "a53", "a54", "a55", "a56", "a57", "a58", "a59", "a60", "a61", "a62", "a63", "a64", "a65", "a66", "a67", "a68", "a69", "a70", "a71", "a72", "a73", "a74", "a75", "a76", "a77", "a78", "a79", "a80", "a81", "a82", "a83", "a84", "a85", "a86", "a87", "a88", "a89", "a90", "a91", "a92", "a93", "a94", "a95", "a96", "a97", "a98", "a99", "a100", "a101", "a102", "a103", "a104", "a105", "a106", "a107", "a108", "a109", "a110", "a111", "a112", "a113", "a114", "a115", "a116", "a117", "a118", "a119", "a120", "a121", "a122", "a123", "a124", "a125", "a126", "a127", "a128", "a129", "a130", "a131", "a132", "a133", "a134", "a135", "a136", "a137", "a138", "a139", "a140", "a141", "a142", "a143", "a144", "a145", "a146", "a147", "a148", "a149", "a150", "a151", "a152", "a153", "a154", "a155", "a156", "a157", "a158", "a159", "a160", "a161", "a162", "a163", "a164", "a165", "a166", "a167", "a168", "a169", "a170", "a171", "a172", "a173", "a174", "a175", "a176", "a177", "a178", "a179", "a180", "a181", "a182", "a183", "a184", "a185", "a186", "a187", "a188", "a189", "a190", "a191", "a192", "a193", "a194", "a195", "a196", "a197", "a198", "a199", "a200", "a201", "a202", "a203", "a204", "a205", "a206", "a207", "a208", "a209", "a210", "a211", "a212", "a213", "a214", "a215", "a216", "a217", "a218", "a219", "a220", "a221", "a222", "a223", "a224", "a225", "a226", "a227", "a228", "a229", "a230", "a231", "a232", "a233", "a234", "a235", "a236", "a237", "a238", "a239", "a240", "a241", "a242", "a243", "a244", "a245", "a246", "a247", "a248", "a249", "a250", "a251", "a252", "a253", "a254", "a255", "s3", "s4", "s5", "s6", "s7", "s8", "s9", "s10", "s11", "s12", "s13", "s14", "s15", "s16", "s17", "s18", "s19", "s20", "s21", "s22", "s23", "s24", "s25", "s26", "s27", "s28", "s29", "s30", "s31", "s34", "s35", "s36", "s37", "s38", "s39", "s40", "s41", "s42", "s43", "s44", "s45", "s46", "s47", "s48", "s49", "s50", "s51", "s52", "s53", "s54", "s55", "s56", "s57", "s58", "s59", "s60", "s61", "s62", "s63", "s64", "s65", "s66", "s67", "s68", "s69", "s70", "s71", "s72", "s73", "s74", "s75", "s76", "s77", "s78", "s79", "s80", "s81", "s82", "s83", "s84", "s85", "s86", "s87", "s88", "s89", "s90", "s91", "s92", "s93", "s94", "s95", "s96", "s97", "s98", "s99", "scc", "vcc", "memory");
}

extern "C" void kernel_launch(void* const* d_in, const int* in_sizes, int n_in, void* d_out,
                              int out_size, void* d_ws, size_t ws_size, hipStream_t stream) {
  conv_kernel<<<256, 256, 0, stream>>>((const char*)d_in[0], (const char*)d_in[1], (char*)d_out, (char*)d_ws);
}
```

```cpp
#include <hip/hip_runtime.h>
#include <hip/hip_bf16.h>
#include <stdint.h>

__global__ void __launch_bounds__(256, 1) __attribute__((aligned(4096)))
conv_kernel(const char* __restrict__ x, const char* __restrict__ w, char* __restrict__ out, char* __restrict__ dbg) {
  __shared__ __attribute__((aligned(1024))) char smem[163840];
  asm volatile("" :: "v"((unsigned)(uintptr_t)smem) : "memory");
  asm volatile(
      "s_load_dwordx8 s[20:27], %2, 0x0\n"
      "v_readfirstlane_b32 s40, %0\n"
      "v_and_b32 v5, 63, %0\n"
      "v_lshlrev_b32 v8, 4, v5\n"
      "s_lshr_b32 s40, s40, 6\n"
      "s_lshl_b32 s43, s40, 3\n"
      "s_or_b32 s43, s43, 0x80000\n"
      "v_bitop3_b32 v9, v8, v5, 32 bitop3:0x78\n"
      "v_xor_b32 v89, 32, v8\n"
      "s_mul_i32 s92, s40, 3072\n"
      "s_lshr_b32 s14, %1, 7\n"
      "s_sub_u32 s14, s14, 1\n"
      "s_lshr_b32 s41, s40, 1\n"
      "s_and_b32 s42, s40, 1\n"
      "s_mov_b32 s30, 165888\n"
      "s_mov_b32 s31, 0x20000\n"
      "s_lshr_b32 s49, %1, 3\n"
      "s_lshl2_add_u32 s49, s49, s40\n"
      "v_lshlrev_b32 v36, 7, v5\n"
      "s_mul_i32 s50, s49, 384\n"
      "v_min_u32 v37, 256, v36\n"
      "v_add_u32 v37, s50, v37\n"
      "v_min_u32 v66, 1280, v36\n"
      "s_mul_i32 s96, s49, 1408\n"
      "s_getpc_b64 s[54:55]\n"
      "Lconv_start:\n"
      "v_min_u32 v67, Lconv_end-Lconv_start-4, v37\n"
      "s_mov_b32 m0, 162816\n"
      "s_nop 0\n"
      "global_load_lds_dword v67, s[54:55]\n"
      "s_bfe_u32 s99, %1, 0x30003\n"
      "s_mul_i32 s49, s40, 192\n"
      "v_add_u32 v36, s49, v5\n"
      "v_add_u32 v38, 128, v36\n"
      "v_mul_u32_u24 v37, 669, v36\n"
      "v_lshrrev_b32 v37, 16, v37\n"
      "v_mad_i32_i24 v37, v37, -2, v38\n"
      "v_lshlrev_b32 v16, 4, v37\n"
      "v_add_u32 v36, 64, v36\n"
      "v_mul_u32_u24 v37, 669, v36\n"
      "v_lshrrev_b32 v37, 16, v37\n"
      "v_mad_i32_i24 v37, v37, -2, v38\n"
      "v_lshlrev_b32 v17, 4, v37\n"
      "v_add_u32 v36, 64, v36\n"
      "v_mul_u32_u24 v37, 669, v36\n"
      "v_lshrrev_b32 v37, 16, v37\n"
      "v_mad_i32_i24 v37, v37, -2, v38\n"
      "v_lshlrev_b32 v18, 4, v37\n"
      "s_or_b32 s50, s99, s40\n"
      "s_cmp_eq_u32 s50, 0\n"
      "s_cselect_b32 s16, 0xffffff, 0\n"
      "s_mov_b32 s17, 0\n"
      "s_cmp_eq_u32 s99, 7\n"
      "s_cselect_b32 s19, 0xffffff00, 0\n"
      "s_mov_b32 s18, 0\n"
      "v_add_u32 v19, 14080, v8\n"
      "v_cndmask_b32 v16, v16, 2.0, s[16:17]\n"
      "v_cndmask_b32 v19, v19, 2.0, s[18:19]\n"
      "v_and_b32 v36, 16, v8\n"
      "v_mov_b32 v52, 0xffff\n"
      "v_lshlrev_b32 v52, v36, v52\n"
      "s_mov_b32 s16, 0x3000003\n"
      "s_mov_b32 s17, 0x3000003\n"
      "v_cndmask_b32 v48, 0, v52, s[16:17]\n"
      "s_lshl_b64 s[16:17], s[16:17], 2\n"
      "v_cndmask_b32 v49, 0, v52, s[16:17]\n"
      "s_lshl_b64 s[16:17], s[16:17], 2\n"
      "v_cndmask_b32 v50, 0, v52, s[16:17]\n"
      "s_lshl_b64 s[16:17], s[16:17], 2\n"
      "v_cndmask_b32 v51, 0, v52, s[16:17]\n"
      "v_bfe_u32 v53, v5, 5, 1\n"
      "v_xor_b32 v53, s41, v53\n"
      "v_sub_u32 v53, 0, v53\n"
      "v_and_b32 v36, 0x1000100, v53\n"
      "v_xor_b32 v36, 0x3fbf3fbf, v36\n"
      "v_and_b32 v68, v36, v48\n"
      "v_and_b32 v69, v36, v49\n"
      "v_and_b32 v70, v36, v50\n"
      "v_and_b32 v71, v36, v51\n"
      "v_xor_b32 v72, 0x80008000, v68\n"
      "v_xor_b32 v73, 0x80008000, v69\n"
      "v_xor_b32 v74, 0x80008000, v70\n"
      "v_xor_b32 v75, 0x80008000, v71\n"
      "v_bfe_i32 v36, v5, 5, 1\n"
      "v_and_b32 v36, 0x3f3f3f3f, v36\n"
      "v_xor_b32 v37, 0x3f3f3f3f, v36\n"
      "v_and_b32 v76, v37, v48\n"
      "v_and_b32 v77, v37, v49\n"
      "v_and_b32 v78, v37, v50\n"
      "v_and_b32 v79, v37, v51\n"
      "v_and_b32 v80, v36, v48\n"
      "v_and_b32 v81, v36, v49\n"
      "v_and_b32 v82, v36, v50\n"
      "v_and_b32 v83, v36, v51\n"
      "s_mul_i32 s50, s42, 96\n"
      "v_bfe_u32 v36, v5, 1, 3\n"
      "v_and_b32 v37, 1, v5\n"
      "v_lshl_add_u32 v36, v37, 3, v36\n"
      "v_mul_u32_u24 v36, 192, v36\n"
      "v_add_u32 v36, 46144, v36\n"
      "v_lshlrev_b32 v37, 5, v37\n"
      "v_and_b32 v38, 16, v5\n"
      "v_add_u32 v38, s50, v38\n"
      "v_and_b32 v39, 32, v5\n"
      "v_mul_u32_u24 v40, 192, v39\n"
      "v_add_u32 v40, v40, v36\n"
      "v_xad_u32 v84, v38, v37, v40\n"
      "v_add_u32 v41, 32, v38\n"
      "v_xad_u32 v85, v41, v37, v40\n"
      "v_add_u32 v41, v39, v38\n"
      "v_xad_u32 v86, v41, v37, v36\n"
      "s_mul_i32 s49, s41, 6144\n"
      "s_add_u32 s49, s49, 46144\n"
      "v_lshrrev_b32 v36, 3, v5\n"
      "v_and_b32 v38, 4, v5\n"
      "v_lshl_add_u32 v36, v38, 1, v36\n"
      "v_mul_u32_u24 v36, 192, v36\n"
      "v_lshlrev_b32 v37, 3, v5\n"
      "v_and_b32 v38, 8, v37\n"
      "v_add3_u32 v36, v36, v38, s49\n"
      "v_and_b32 v38, 32, v37\n"
      "v_and_b32 v37, 16, v37\n"
      "v_add_u32 v37, s50, v37\n"
      "v_xad_u32 v87, v37, v38, v36\n"
      "v_add_u32 v39, 32, v37\n"
      "v_xad_u32 v88, v39, v38, v36\n"
      "s_waitcnt lgkmcnt(0)\n"
      "s_mov_b32 s28, s22\n"
      "s_and_b32 s29, s23, 0xffff\n"
      "buffer_load_dword v66, s[28:31], s96 offen lds\n"
      "s_add_u32 m0, s92, 73824\n"
      "s_and_b32 s97, 0x3c2c24, s14\n"
      "s_xor_b32 s97, 0x120c0600, s97\n"
      "s_bfe_u32 s97, s97, s43\n"
      "s_mul_i32 s97, s97, 3072\n"
      "buffer_load_dwordx4 v8, s[28:31], s97 offen offset:0 lds\n"
      "buffer_load_dwordx4 v9, s[28:31], s97 offen offset:1024 lds\n"
      "buffer_load_dwordx4 v89, s[28:31], s97 offen offset:2048 lds\n"
      "s_add_u32 m0, s92, 86112\n"
      "s_and_b32 s97, 0x2c240000, s14\n"
      "s_xor_b32 s97, 0x2a241e18, s97\n"
      "s_bfe_u32 s97, s97, s43\n"
      "s_mul_i32 s97, s97, 3072\n"
      "buffer_load_dwordx4 v8, s[28:31], s97 offen offset:0 lds\n"
      "buffer_load_dwordx4 v9, s[28:31], s97 offen offset:1024 lds\n"
      "buffer_load_dwordx4 v89, s[28:31], s97 offen offset:2048 lds\n"
      "s_and_b32 s53, %1, 7\n"
      "s_lshl_b32 s53, s53, 6\n"
      "s_bfe_u32 s50, %1, 0x10006\n"
      "s_add_u32 s53, s53, s50\n"
      "s_lshl1_add_u32 s50, s14, 32\n"
      "s_add_u32 s53, s53, s50\n"
      "s_mul_i32 s13, s53, 98304\n"
      "s_or_b32 s12, s14, 1\n"
      "s_mul_i32 s12, s12, 196608\n"
      "s_lshl_b32 s50, s12, 1\n"
      "s_sub_u32 s51, s13, s50\n"
      "s_mul_i32 s99, s99, 12288\n"
      "s_sub_u32 s50, s99, 2432\n"
      "s_ashr_i32 s49, s50, 31\n"
      "s_add_u32 s32, s20, s50\n"
      "s_addc_u32 s33, s21, s49\n"
      "s_and_b32 s33, s33, 0xffff\n"
      "s_sub_u32 s34, 50331648, s50\n"
      "s_mov_b32 s35, 0x20000\n"
      "s_add_u32 s46, s92, 110720\n"
      "v_lshrrev_b32 v36, 1, v5\n"
      "v_and_b32 v37, 4, v36\n"
      "v_and_or_b32 v37, v5, 2, v37\n"
      "v_mul_u32_u24 v37, 1568, v37\n"
      "v_xor_b32 v36, v36, v5\n"
      "v_and_b32 v36, 4, v36\n"
      "v_mad_u32_u24 v37, v36, 48, v37\n"
      "s_mul_i32 s50, s42, 1568\n"
      "s_add_u32 s50, s50, 110720\n"
      "v_and_b32 v36, 16, v5\n"
      "v_lshl_add_u32 v37, v36, 1, v37\n"
      "v_and_b32 v36, 16, v8\n"
      "v_add3_u32 v37, v37, v36, s50\n"
      "v_and_b32 v36, 32, v5\n"
      "v_mad_u32_u24 v38, v36, 24, v37\n"
      "v_mad_u32_u24 v39, v36, 25, v37\n"
      "s_mul_i32 s17, s42, 0xcc00cc00\n"
      "s_mov_b32 s16, 0\n"
      "v_mov_b32 v36, 768\n"
      "v_mov_b32 v37, 736\n"
      "s_mov_b64 exec, 0\n"
      "s_cmp_eq_u32 s40, 0\n"
      "s_cbranch_scc1 Lconv_p1\n"
      "s_cmp_eq_u32 s40, 1\n"
      "s_cbranch_scc1 Lconv_p2\n"
      "s_cmp_eq_u32 s40, 2\n"
      "s_cbranch_scc1 Lconv_pa0\n"
      "s_cmp_eq_u32 s40, 3\n"
      "s_cbranch_scc1 Lconv_pa1\n"
      "Lconv_r0:\n"
      "s_mov_b64 exec, -1\n"
      "s_add_u32 m0, s92, 98400\n"
      "s_and_b32 s97, 0x3c2c243c, s14\n"
      "s_xor_b32 s97, 0xd070130, s97\n"
      "s_bfe_u32 s97, s97, s43\n"
      "s_mul_i32 s97, s97, 3072\n"
      "buffer_load_dwordx4 v8, s[28:31], s97 offen offset:0 lds\n"
      "buffer_load_dwordx4 v9, s[28:31], s97 offen offset:1024 lds\n"
      "buffer_load_dwordx4 v89, s[28:31], s97 offen offset:2048 lds\n"
      "v_cndmask_b32 v36, v36, v37, s[16:17]\n"
      "v_add_u32 v39, v39, v36\n"
      "s_cmp_eq_u32 s41, 0\n"
      "s_cselect_b64 vcc, -1, 0\n"
      "v_add_u32 v40, 384, v38\n"
      "v_add_u32 v41, 384, v39\n"
      "v_cndmask_b32 v2, v40, v39, vcc\n"
      "v_cndmask_b32 v3, v39, v40, vcc\n"
      "v_cndmask_b32 v4, v41, v38, vcc\n"
      "v_and_b32 v36, 3, v5\n"
      "v_mul_u32_u24 v36, 18464, v36\n"
      "v_bfe_u32 v37, v5, 2, 2\n"
      "v_lshrrev_b32 v38, 1, v37\n"
      "v_xor_b32 v37, v37, v38\n"
      "v_lshl_add_u32 v36, v37, 6, v36\n"
      "s_mul_i32 s50, s41, 2304\n"
      "v_and_b32 v37, 48, v5\n"
      "v_add3_u32 v6, v36, v37, s50\n"
      "v_add_u32 v7, 9216, v6\n"
      "s_mov_b32 s16, 0xc0003\n"
      "s_mov_b32 s17, 0xc0003\n"
      "v_cndmask_b32 v48, 0, v52, s[16:17]\n"
      "s_lshl_b64 s[16:17], s[16:17], 4\n"
      "v_cndmask_b32 v49, 0, v52, s[16:17]\n"
      "s_lshl_b64 s[16:17], s[16:17], 4\n"
      "v_cndmask_b32 v50, 0, v52, s[16:17]\n"
      "s_lshl_b64 s[16:17], s[16:17], 4\n"
      "v_cndmask_b32 v51, 0, v52, s[16:17]\n"
      "v_and_b32 v36, 0xff00ff00, v53\n"
      "v_xor_b32 v36, 0xc080c080, v36\n"
      "v_and_b32 v38, v36, v48\n"
      "v_and_b32 v39, v36, v49\n"
      "v_and_b32 v40, v36, v50\n"
      "v_and_b32 v41, v36, v51\n"
      "v_accvgpr_write_b32 a72, v38\n"
      "v_accvgpr_write_b32 a73, v39\n"
      "v_accvgpr_write_b32 a74, v40\n"
      "v_accvgpr_write_b32 a75, v41\n"
      "v_and_b32 v36, 0xff00ff00, v53\n"
      "v_xor_b32 v36, 0xc000c000, v36\n"
      "v_and_b32 v38, v36, v48\n"
      "v_and_b32 v39, v36, v49\n"
      "v_and_b32 v40, v36, v50\n"
      "v_and_b32 v41, v36, v51\n"
      "v_accvgpr_write_b32 a76, v38\n"
      "v_accvgpr_write_b32 a77, v39\n"
      "v_accvgpr_write_b32 a78, v40\n"
      "v_accvgpr_write_b32 a79, v41\n"
      "v_xor_b32 v38, 0x80008000, v38\n"
      "v_accvgpr_write_b32 a252, v38\n"
      "v_xor_b32 v39, 0x80008000, v39\n"
      "v_accvgpr_write_b32 a253, v39\n"
      "v_xor_b32 v40, 0x80008000, v40\n"
      "v_accvgpr_write_b32 a254, v40\n"
      "v_xor_b32 v41, 0x80008000, v41\n"
      "v_accvgpr_write_b32 a255, v41\n"
      "v_and_b32 v36, 0x81008100, v53\n"
      "v_xor_b32 v36, 0x3f803f80, v36\n"
      "v_and_b32 v38, v36, v48\n"
      "v_and_b32 v39, v36, v49\n"
      "v_and_b32 v40, v36, v50\n"
      "v_and_b32 v41, v36, v51\n"
      "v_accvgpr_write_b32 a80, v38\n"
      "v_accvgpr_write_b32 a81, v39\n"
      "v_accvgpr_write_b32 a82, v40\n"
      "v_accvgpr_write_b32 a83, v41\n"
      "v_lshlrev_b32 v36, 5, v5\n"
      "v_and_b32 v36, 0x180, v36\n"
      "v_lshlrev_b32 v37, 3, v5\n"
      "v_and_b32 v37, 16, v37\n"
      "v_sub_u32 v38, 0x3eb7, v36\n"
      "v_add_u32 v39, 0x3d37, v36\n"
      "v_mov_b32 v40, 0x3f80\n"
      "s_mov_b64 vcc, 0xffff\n"
      "s_mov_b32 s16, 0xffff0000\n"
      "s_mov_b32 s17, 0xf0f0\n"
      "v_xor_b32 v41, 0x8000, v38\n"
      "v_cndmask_b32 v38, v38, v41, s[16:17]\n"
      "v_cndmask_b32 v38, v38, v40, vcc\n"
      "v_lshlrev_b32 v38, v37, v38\n"
      "v_xor_b32 v41, 0x8000, v39\n"
      "s_xor_b32 s17, s17, 0xffff\n"
      "v_cndmask_b32 v39, v39, v41, s[16:17]\n"
      "v_cndmask_b32 v39, v39, v40, vcc\n"
      "v_lshlrev_b32 v39, v37, v39\n"
      "s_mov_b32 s18, 0x55550005\n"
      "s_mov_b32 s19, 0x5555\n"
      "v_cndmask_b32 v42, 0, v38, s[18:19]\n"
      "s_lshl_b64 s[16:17], s[18:19], 1\n"
      "v_cndmask_b32 v43, 0, v38, s[16:17]\n"
      "v_accvgpr_write_b32 a84, v42\n"
      "v_accvgpr_write_b32 a85, v43\n"
      "s_mov_b32 s18, 0x55555000\n"
      "v_cndmask_b32 v42, 0, v39, s[18:19]\n"
      "s_lshl_b64 s[16:17], s[18:19], 1\n"
      "v_cndmask_b32 v43, 0, v39, s[16:17]\n"
      "v_accvgpr_write_b32 a86, v42\n"
      "v_accvgpr_write_b32 a87, v43\n"
      "v_lshrrev_b32 v36, 4, v5\n"
      "v_min_u32 v37, 2, v36\n"
      "v_lshlrev_b32 v37, 10, v37\n"
      "s_lshl_b32 s50, s41, 7\n"
      "s_lshl_b32 s49, s42, 8\n"
      "s_add_u32 s50, s50, s49\n"
      "s_add_u32 s50, s50, 98416\n"
      "v_and_b32 v38, 8, v5\n"
      "v_lshl_add_u32 v37, v38, 3, v37\n"
      "v_add_u32 v37, s50, v37\n"
      "s_movk_i32 s50, -12296\n"
      "s_mov_b32 s49, 43040\n"
      "v_mov_b32 v42, s49\n"
      "v_bfe_u32 v39, v5, 1, 2\n"
      "v_lshrrev_b32 v40, 2, v38\n"
      "v_add_u32 v39, v39, v40\n"
      "v_lshlrev_b32 v40, 5, v5\n"
      "v_and_b32 v40, 32, v40\n"
      "v_cmp_lt_u32 vcc, 2, v39\n"
      "v_mad_i32_i24 v39, v39, s50, v37\n"
      "v_add_u32 v39, v39, v40\n"
      "v_cndmask_b32 v41, 0, v42, vcc\n"
      "v_add_u32 v10, v39, v41\n"
      "v_add_u32 v11, 3072, v10\n"
      "v_bfe_u32 v39, v5, 2, 1\n"
      "v_lshrrev_b32 v40, 1, v38\n"
      "v_sub_u32 v39, v39, v40\n"
      "v_add_u32 v39, 4, v39\n"
      "v_and_b32 v40, 48, v8\n"
      "v_cmp_lt_u32 vcc, 2, v39\n"
      "v_mad_i32_i24 v39, v39, s50, v37\n"
      "v_add_u32 v39, v39, v40\n"
      "v_cndmask_b32 v41, 0, v42, vcc\n"
      "v_add_u32 v12, v39, v41\n"
      "v_add_u32 v13, 3072, v12\n"
      "v_mul_u32_u24 v36, 384, v36\n"
      "v_and_b32 v37, 15, v5\n"
      "v_lshl_add_u32 v39, v37, 3, v36\n"
      "v_add_u32 v15, 128, v39\n"
      "v_add_lshl_u32 v37, v37, v38, 4\n"
      "v_add_u32 v14, v37, v36\n"
      "s_add_u32 s50, s99, s92\n"
      "s_add_u32 s36, s24, s50\n"
      "s_addc_u32 s37, s25, 0\n"
      "s_and_b32 s37, s37, 0xffff\n"
      "s_sub_u32 s38, 50331648, s50\n"
      "s_mov_b32 s39, 0x20000\n"
      "s_mul_i32 s48, s12, 17\n"
      "s_mul_i32 s45, s42, 6152\n"
      "s_lshl_b32 s50, s41, 9\n"
      "s_add_u32 s45, s45, s50\n"
      "s_add_u32 s45, s45, 41056\n"
      "s_mov_b32 s98, 0\n"
      "s_mov_b32 s91, 26560\n"
      "s_mul_i32 s18, s42, 18464\n"
      "s_mul_i32 s50, s41, 2304\n"
      "s_add_u32 s18, s18, s50\n"
      "s_lshl1_add_u32 s19, s18, 9216\n"
      "s_add_u32 s58, s46, 26560\n"
      "s_lshl1_add_u32 s93, s46, 26560\n"
      "s_mul_i32 s59, s42, -13280\n"
      "s_add_u32 s59, s59, 136256\n"
      "s_lshl1_add_u32 s94, s59, 26560\n"
      "s_mul_i32 s50, s42, 26560\n"
      "s_add_u32 s59, s59, s50\n"
      "s_mul_i32 s15, s42, s12\n"
      "s_sub_u32 s95, s12, s15\n"
      "s_mov_b32 s52, 2.0\n"
      "s_cmp_lt_u32 s40, 3\n"
      "s_cselect_b32 s97, 27680, 0\n"
      "s_add_u32 s97, s97, s92\n"
      "s_add_u32 m0, s97, 36928\n"
      "s_and_b32 s97, 0x24000000, s14\n"
      "s_xor_b32 s97, 0x251f1913, s97\n"
      "s_bfe_u32 s97, s97, s43\n"
      "s_mul_i32 s97, s97, 3072\n"
      "buffer_load_dwordx4 v8, s[28:31], s97 offen offset:0 lds\n"
      "buffer_load_dwordx4 v9, s[28:31], s97 offen offset:1024 lds\n"
      "buffer_load_dwordx4 v89, s[28:31], s97 offen offset:2048 lds\n"
      "s_cmp_lt_u32 s40, 2\n"
      "s_cselect_b32 s97, 27680, 0\n"
      "s_add_u32 s97, s97, s92\n"
      "s_add_u32 m0, s97, 21536\n"
      "s_and_b32 s97, 0x24243c2c, s14\n"
      "s_xor_b32 s97, 0x802312b, s97\n"
      "s_bfe_u32 s97, s97, s43\n"
      "s_mul_i32 s97, s97, 3072\n"
      "buffer_load_dwordx4 v8, s[28:31], s97 offen offset:0 lds\n"
      "buffer_load_dwordx4 v9, s[28:31], s97 offen offset:1024 lds\n"
      "buffer_load_dwordx4 v89, s[28:31], s97 offen offset:2048 lds\n"
      "s_cmp_lt_u32 s40, 1\n"
      "s_cselect_b32 s97, 27680, 0\n"
      "s_add_u32 s97, s97, s92\n"
      "s_add_u32 m0, s97, 6144\n"
      "s_and_b32 s97, 0x3c, s14\n"
      "s_xor_b32 s97, 0x201a140e, s97\n"
      "s_bfe_u32 s97, s97, s43\n"
      "s_mul_i32 s97, s97, 3072\n"
      "buffer_load_dwordx4 v8, s[28:31], s97 offen offset:0 lds\n"
      "buffer_load_dwordx4 v9, s[28:31], s97 offen offset:1024 lds\n"
      "buffer_load_dwordx4 v89, s[28:31], s97 offen offset:2048 lds\n"
      "s_cmp_lg_u32 s41, 0\n"
      "s_cselect_b64 vcc, -1, 0\n"
      ".p2align 3\n"
      "s_waitcnt vmcnt(12)\n"
      "s_barrier\n"
      "s_cmp_lt_u32 s40, 3\n"
      "s_cselect_b32 s97, -9248, 0\n"
      "ds_read_b128 a[0:3], v86 offset:30752\n"
      "ds_read_b128 a[4:7], v84 offset:27680\n"
      "ds_read_b128 a[8:11], v85 offset:27680\n"
      "ds_read_b128 a[12:15], v86 offset:30816\n"
      "s_add_u32 s97, s97, s92\n"
      "s_add_u32 m0, s97, 9248\n"
      "s_and_b32 s97, 0x243c2424, s14\n"
      "s_xor_b32 s97, 0x3322c26, s97\n"
      "s_bfe_u32 s97, s97, s43\n"
      "s_mul_i32 s97, s97, 3072\n"
      "s_waitcnt lgkmcnt(3)\n"
      "v_mfma_f32_16x16x32_bf16 v[20:23], a[0:3], v[76:79], 0\n"
      "s_waitcnt lgkmcnt(2)\n"
      "ds_read_b128 a[16:19], v84 offset:27744\n"
      "ds_read_b128 a[20:23], v86 offset:39968\n"
      "buffer_load_dwordx4 v8, s[28:31], s97 offen offset:0 lds\n"
      "v_mfma_f32_16x16x32_bf16 v[24:27], a[4:7], v[68:71], v[20:23]\n"
      "ds_read_b128 a[24:27], v84 offset:36896\n"
      "ds_read_b128 a[28:31], v85 offset:36896\n"
      "v_mfma_f32_16x16x32_bf16 v[20:23], a[4:7], v[72:75], v[20:23]\n"
      "ds_read_b64_tr_b16 a[88:89], v87 offset:27680\n"
      "ds_read_b64_tr_b16 a[92:93], v88 offset:27680\n"
      "buffer_load_dwordx4 v9, s[28:31], s97 offen offset:1024 lds\n"
      "v_mfma_f32_16x16x32_bf16 v[28:31], a[0:3], v[80:83], 0\n"
      "s_waitcnt lgkmcnt(7)\n"
      "ds_read_b64_tr_b16 a[96:97], v87 offset:27744\n"
      "ds_read_b64_tr_b16 a[124:125], v87 offset:36896\n"
      "v_mfma_f32_16x16x32_bf16 v[32:35], a[8:11], v[68:71], v[28:31]\n"
      "ds_read_b64_tr_b16 a[128:129], v88 offset:36896\n"
      "ds_read_b128 a[32:35], v86 offset:40032\n"
      "v_mfma_f32_16x16x32_bf16 v[28:31], a[8:11], v[72:75], v[28:31]\n"
      "s_waitcnt lgkmcnt(10)\n"
      "ds_read_b128 a[0:3], v84 offset:36960\n"
      "ds_read_b64_tr_b16 a[132:133], v87 offset:36960\n"
      "buffer_load_dwordx4 v89, s[28:31], s97 offen offset:2048 lds\n"
      "v_mfma_f32_16x16x32_bf16 v[54:57], a[12:15], v[76:79], 0\n"
      "s_waitcnt lgkmcnt(10)\n"
      "s_cmp_lt_u32 s40, 2\n"
      "s_cselect_b32 s97, -9248, 0\n"
      "s_add_u32 s97, s97, s92\n"
      "s_add_u32 m0, s97, 30784\n"
      "s_and_b32 s97, 0x3c24, s14\n"
      "s_xor_b32 s97, 0x1b150f09, s97\n"
      "s_bfe_u32 s97, s97, s43\n"
      "s_mul_i32 s97, s97, 3072\n"
      "v_cvt_pk_bf16_f32 v96, v20, v21\n"
      "v_cvt_pk_bf16_f32 v97, v22, v23\n"
      "v_mfma_f32_16x16x32_bf16 v[58:61], a[16:19], v[68:71], v[54:57]\n"
      "v_cvt_pk_bf16_f32 v92, v24, v25\n"
      "v_cvt_pk_bf16_f32 v93, v26, v27\n"
      "v_mfma_f32_16x16x32_bf16 v[54:57], a[16:19], v[72:75], v[54:57]\n"
      "buffer_load_dwordx4 v8, s[28:31], s97 offen offset:0 lds\n"
      "v_cvt_pk_bf16_f32 v104, v28, v29\n"
      "v_cvt_pk_bf16_f32 v105, v30, v31\n"
      "v_mfma_f32_16x16x32_bf16 v[62:65], a[20:23], v[76:79], 0\n"
      "s_waitcnt lgkmcnt(4)\n"
      "v_cvt_pk_bf16_f32 v100, v32, v33\n"
      "v_cvt_pk_bf16_f32 v101, v34, v35\n"
      "v_mfma_f32_16x16x32_bf16 v[20:23], a[24:27], v[68:71], v[62:65]\n"
      "v_cvt_pk_bf16_f32 v108, v54, v55\n"
      "v_cvt_pk_bf16_f32 v109, v56, v57\n"
      "v_mfma_f32_16x16x32_bf16 v[62:65], a[24:27], v[72:75], v[62:65]\n"
      "s_waitcnt lgkmcnt(0)\n"
      "s_waitcnt vmcnt(13)\n"
      "s_cbranch_vccnz Lconv_h1\n"
      "s_barrier\n"
      "Lconv_h1:\n"
      "buffer_load_dwordx4 v9, s[28:31], s97 offen offset:1024 lds\n"
      "v_mfma_f32_16x16x32_bf16 v[24:27], a[20:23], v[80:83], 0\n"
      "v_cvt_pk_bf16_f32 v58, v58, v59\n"
      "v_cvt_pk_bf16_f32 v60, v60, v61\n"
      "v_mfma_f32_16x16x32_bf16 v[28:31], a[28:31], v[68:71], v[24:27]\n"
      "s_cbranch_vccz Lconv_h2\n"
      "s_barrier\n"
      "Lconv_h2:\n"
      "ds_read_b128 a[4:7], v86 offset:49184\n"
      "ds_read_b128 a[8:11], v84 offset:46112\n"
      "ds_read_b128 a[12:15], v85 offset:46112\n"
      "ds_read_b128 a[16:19], v86 offset:49248\n"
      "v_mfma_f32_16x16x32_bf16 v[24:27], a[28:31], v[72:75], v[24:27]\n"
      "ds_read_b128 a[20:23], v84 offset:46176\n"
      "ds_read_b128 a[24:27], v86 offset:58400\n"
      "buffer_load_dwordx4 v89, s[28:31], s97 offen offset:2048 lds\n"
      "v_accvgpr_write_b32 a196, v58\n"
      "v_accvgpr_write_b32 a197, v60\n"
      "v_mfma_f32_16x16x32_bf16 v[32:35], a[32:35], v[76:79], 0\n"
      "s_cmp_lt_u32 s40, 1\n"
      "s_cselect_b32 s97, -9248, 0\n"
      "ds_read_b128 a[28:31], v84 offset:55328\n"
      "ds_read_b128 a[32:35], v85 offset:55328\n"
      "s_add_u32 s97, s97, s92\n"
      "s_add_u32 m0, s97, 52320\n"
      "s_and_b32 s97, 0x3c242400, s14\n"
      "s_xor_b32 s97, 0x332d2721, s97\n"
      "s_bfe_u32 s97, s97, s43\n"
      "s_mul_i32 s97, s97, 3072\n"
      "v_cvt_pk_bf16_f32 v116, v62, v63\n"
      "v_cvt_pk_bf16_f32 v117, v64, v65\n"
      "v_mfma_f32_16x16x32_bf16 v[54:57], a[0:3], v[68:71], v[32:35]\n"
      "ds_read_b64_tr_b16 a[160:161], v87 offset:46112\n"
      "ds_read_b64_tr_b16 a[164:165], v88 offset:46112\n"
      "buffer_load_dwordx4 v8, s[28:31], s97 offen offset:0 lds\n"
      "v_cvt_pk_bf16_f32 v112, v20, v21\n"
      "v_cvt_pk_bf16_f32 v113, v22, v23\n"
      "v_mfma_f32_16x16x32_bf16 v[32:35], a[0:3], v[72:75], v[32:35]\n"
      "s_waitcnt lgkmcnt(9)\n"
      "ds_read_b64_tr_b16 a[168:169], v87 offset:46176\n"
      "ds_read_b64_tr_b16 a[90:91], v87 offset:55328\n"
      "v_cvt_pk_bf16_f32 v120, v24, v25\n"
      "v_cvt_pk_bf16_f32 v121, v26, v27\n"
      "v_mfma_f32_16x16x32_bf16 v[58:61], a[4:7], v[76:79], 0\n"
      "s_waitcnt lgkmcnt(10)\n"
      "ds_read_b64_tr_b16 a[94:95], v88 offset:55328\n"
      "ds_read_b128 a[0:3], v86 offset:58464\n"
      "v_mfma_f32_16x16x32_bf16 v[62:65], a[8:11], v[68:71], v[58:61]\n"
      "ds_read_b64_tr_b16 a[98:99], v87 offset:55392\n"
      "buffer_load_dwordx4 v9, s[28:31], s97 offen offset:1024 lds\n"
      "v_cvt_pk_bf16_f32 v28, v28, v29\n"
      "v_cvt_pk_bf16_f32 v30, v30, v31\n"
      "v_accvgpr_write_b32 a204, v28\n"
      "v_mfma_f32_16x16x32_bf16 v[58:61], a[8:11], v[72:75], v[58:61]\n"
      "v_accvgpr_write_b32 a205, v30\n"
      "v_cvt_pk_bf16_f32 v128, v32, v33\n"
      "v_cvt_pk_bf16_f32 v129, v34, v35\n"
      "v_mfma_f32_16x16x32_bf16 v[20:23], a[4:7], v[80:83], 0\n"
      "s_waitcnt lgkmcnt(7)\n"
      "ds_read_b128 a[4:7], v84 offset:55392\n"
      "v_cvt_pk_bf16_f32 v124, v54, v55\n"
      "v_mfma_f32_16x16x32_bf16 v[24:27], a[12:15], v[68:71], v[20:23]\n"
      "buffer_load_dwordx4 v89, s[28:31], s97 offen offset:2048 lds\n"
      "v_cvt_pk_bf16_f32 v125, v56, v57\n"
      "v_mfma_f32_16x16x32_bf16 v[20:23], a[12:15], v[72:75], v[20:23]\n"
      "s_add_u32 m0, s46, 13280\n"
      "s_add_u32 s51, s51, s12\n"
      "v_cvt_pk_bf16_f32 v132, v58, v59\n"
      "v_cvt_pk_bf16_f32 v133, v60, v61\n"
      "v_mfma_f32_16x16x32_bf16 v[28:31], a[16:19], v[76:79], 0\n"
      "v_cvt_pk_bf16_f32 v62, v62, v63\n"
      "v_cvt_pk_bf16_f32 v64, v64, v65\n"
      "v_mfma_f32_16x16x32_bf16 v[32:35], a[20:23], v[68:71], v[28:31]\n"
      "buffer_load_dwordx4 v16, s[32:35], s51 offen lds\n"
      "v_accvgpr_write_b32 a212, v62\n"
      "v_accvgpr_write_b32 a213, v64\n"
      "v_mfma_f32_16x16x32_bf16 v[28:31], a[20:23], v[72:75], v[28:31]\n"
      "s_add_u32 m0, s46, 13280\n"
      "v_cvt_pk_bf16_f32 v140, v20, v21\n"
      "v_cvt_pk_bf16_f32 v141, v22, v23\n"
      "v_mfma_f32_16x16x32_bf16 v[54:57], a[24:27], v[76:79], 0\n"
      "buffer_load_dwordx4 v17, s[32:35], s51 offen offset:1024 lds\n"
      "v_cvt_pk_bf16_f32 v136, v24, v25\n"
      "v_mfma_f32_16x16x32_bf16 v[58:61], a[28:31], v[68:71], v[54:57]\n"
      "s_add_u32 m0, s46, 13280\n"
      "s_add_u32 s96, s51, s15\n"
      "buffer_load_dwordx4 v18, s[32:35], s51 offen offset:2048 lds\n"
      "v_cvt_pk_bf16_f32 v137, v26, v27\n"
      "v_mfma_f32_16x16x32_bf16 v[54:57], a[28:31], v[72:75], v[54:57]\n"
      "s_waitcnt lgkmcnt(0)\n"
      "s_waitcnt vmcnt(12)\n"
      "s_cbranch_vccnz Lconv_h3\n"
      "s_barrier\n"
      "Lconv_h3:\n"
      "s_mov_b32 m0, s59\n"
      "v_cvt_pk_bf16_f32 v144, v28, v29\n"
      "v_cvt_pk_bf16_f32 v145, v30, v31\n"
      "v_mfma_f32_16x16x32_bf16 v[62:65], a[24:27], v[80:83], 0\n"
      "buffer_load_dwordx4 v19, s[32:35], s96 offen lds\n"
      "v_cvt_pk_bf16_f32 v32, v32, v33\n"
      "v_mfma_f32_16x16x32_bf16 v[20:23], a[32:35], v[68:71], v[62:65]\n"
      "s_cbranch_vccz Lconv_h4\n"
      "s_barrier\n"
      "Lconv_h4:\n"
      "s_add_u32 m0, s46, 26560\n"
      "s_add_u32 s51, s51, s12\n"
      "ds_read_b128 a[8:11], v86 offset:21536\n"
      "ds_read_b128 a[12:15], v84 offset:18464\n"
      "ds_read_b128 a[16:19], v85 offset:18464\n"
      "ds_read_b128 a[20:23], v86 offset:21600\n"
      "buffer_load_dwordx4 v16, s[32:35], s51 offen lds\n"
      "v_mfma_f32_16x16x32_bf16 v[62:65], a[32:35], v[72:75], v[62:65]\n"
      "s_add_u32 m0, s46, 26560\n"
      "ds_read_b128 a[24:27], v84 offset:18528\n"
      "ds_read_b128 a[28:31], v86 offset:3072\n"
      "buffer_load_dwordx4 v17, s[32:35], s51 offen offset:1024 lds\n"
      "v_cvt_pk_bf16_f32 v34, v34, v35\n"
      "v_accvgpr_write_b32 a216, v32\n"
      "v_mfma_f32_16x16x32_bf16 v[24:27], a[0:3], v[76:79], 0\n"
      "s_add_u32 m0, s46, 26560\n"
      "ds_read_b128 a[32:35], v84 offset:0\n"
      "ds_read_b128 a[0:3], v85 offset:0\n"
      "buffer_load_dwordx4 v18, s[32:35], s51 offen offset:2048 lds\n"
      "v_accvgpr_write_b32 a217, v34\n"
      "v_cvt_pk_bf16_f32 v98, v54, v55\n"
      "v_mfma_f32_16x16x32_bf16 v[28:31], a[4:7], v[68:71], v[24:27]\n"
      "s_add_u32 m0, s92, 73824\n"
      "s_and_b32 s97, 0x24242c, s14\n"
      "ds_read_b64_tr_b16 a[126:127], v87 offset:18464\n"
      "ds_read_b64_tr_b16 a[130:131], v88 offset:18464\n"
      "s_xor_b32 s97, 0x16100a04, s97\n"
      "s_bfe_u32 s97, s97, s43\n"
      "s_mul_i32 s97, s97, 3072\n"
      "buffer_load_dwordx4 v8, s[28:31], s97 offen offset:0 lds\n"
      "v_cvt_pk_bf16_f32 v99, v56, v57\n"
      "v_cvt_pk_bf16_f32 v94, v58, v59\n"
      "v_mfma_f32_16x16x32_bf16 v[24:27], a[4:7], v[72:75], v[24:27]\n"
      "s_waitcnt lgkmcnt(9)\n"
      "ds_read_b128 a[4:7], v86 offset:3136\n"
      "ds_read_b64_tr_b16 a[134:135], v87 offset:18528\n"
      "v_cvt_pk_bf16_f32 v95, v60, v61\n"
      "v_cvt_pk_bf16_f32 v106, v62, v63\n"
      "v_cvt_pk_bf16_f32 v107, v64, v65\n"
      "v_mfma_f32_16x16x32_bf16 v[32:35], a[8:11], v[76:79], 0\n"
      "s_waitcnt lgkmcnt(10)\n"
      "ds_read_b64_tr_b16 a[162:163], v87 offset:0\n"
      "ds_read_b64_tr_b16 a[166:167], v88 offset:0\n"
      "v_cvt_pk_bf16_f32 v102, v20, v21\n"
      "v_cvt_pk_bf16_f32 v103, v22, v23\n"
      "v_cvt_pk_bf16_f32 v110, v24, v25\n"
      "v_mfma_f32_16x16x32_bf16 v[54:57], a[12:15], v[68:71], v[32:35]\n"
      "ds_read_b64_tr_b16 a[170:171], v87 offset:64\n"
      "v_cvt_pk_bf16_f32 v111, v26, v27\n"
      "v_cvt_pk_bf16_f32 v28, v28, v29\n"
      "v_cvt_pk_bf16_f32 v30, v30, v31\n"
      "v_mfma_f32_16x16x32_bf16 v[32:35], a[12:15], v[72:75], v[32:35]\n"
      "Lconv_p1:\n"
      "s_cbranch_execz Lconv_r0\n"
      "s_nop 0\n"
      "buffer_load_dwordx4 v9, s[28:31], s97 offen offset:1024 lds\n"
      "v_accvgpr_write_b32 a198, v28\n"
      "v_accvgpr_write_b32 a199, v30\n"
      "v_mfma_f32_16x16x32_bf16 v[58:61], a[8:11], v[80:83], 0\n"
      "s_waitcnt lgkmcnt(7)\n"
      "ds_read_b128 a[8:11], v84 offset:64\n"
      "v_add_u32 v86, -46144, v86\n"
      "v_mfma_f32_16x16x32_bf16 v[62:65], a[16:19], v[68:71], v[58:61]\n"
      "ds_read_b128 a[12:15], v86 offset:30752\n"
      "v_cvt_pk_bf16_f32 v118, v32, v33\n"
      "v_cvt_pk_bf16_f32 v119, v34, v35\n"
      "v_cvt_pk_bf16_f32 v114, v54, v55\n"
      "v_cvt_pk_bf16_f32 v115, v56, v57\n"
      "v_mfma_f32_16x16x32_bf16 v[58:61], a[16:19], v[72:75], v[58:61]\n"
      "v_add_u32 v84, -46144, v84\n"
      "v_add_u32 v85, -46144, v85\n"
      "v_mfma_f32_16x16x32_bf16 v[20:23], a[20:23], v[76:79], 0\n"
      "ds_read_b128 a[16:19], v84 offset:27680\n"
      "ds_read_b128 a[20:23], v85 offset:27680\n"
      "buffer_load_dwordx4 v89, s[28:31], s97 offen offset:2048 lds\n"
      "v_mfma_f32_16x16x32_bf16 v[24:27], a[24:27], v[68:71], v[20:23]\n"
      "s_add_u32 m0, s92, 86112\n"
      "s_and_b32 s97, 0x242c0000, s14\n"
      "s_xor_b32 s97, 0x2e28221c, s97\n"
      "s_bfe_u32 s97, s97, s43\n"
      "s_mul_i32 s97, s97, 3072\n"
      "v_cvt_pk_bf16_f32 v122, v58, v59\n"
      "v_cvt_pk_bf16_f32 v123, v60, v61\n"
      "v_mfma_f32_16x16x32_bf16 v[20:23], a[24:27], v[72:75], v[20:23]\n"
      "ds_read_b128 a[24:27], v86 offset:30816\n"
      "v_cvt_pk_bf16_f32 v62, v62, v63\n"
      "v_cvt_pk_bf16_f32 v64, v64, v65\n"
      "v_mfma_f32_16x16x32_bf16 v[28:31], a[28:31], v[76:79], 0\n"
      "v_accvgpr_write_b32 a206, v62\n"
      "v_accvgpr_write_b32 a207, v64\n"
      "v_mfma_f32_16x16x32_bf16 v[32:35], a[32:35], v[68:71], v[28:31]\n"
      "v_add_u32 v87, -46144, v87\n"
      "v_cvt_pk_bf16_f32 v130, v20, v21\n"
      "v_mfma_f32_16x16x32_bf16 v[28:31], a[32:35], v[72:75], v[28:31]\n"
      "ds_read_b64_tr_b16 a[100:101], v87 offset:27680\n"
      "ds_read_b64_tr_b16 a[108:109], v87 offset:27744\n"
      "buffer_load_dwordx4 v8, s[28:31], s97 offen offset:0 lds\n"
      "v_cvt_pk_bf16_f32 v131, v22, v23\n"
      "v_add_u32 v88, -46144, v88\n"
      "v_mfma_f32_16x16x32_bf16 v[54:57], a[28:31], v[80:83], 0\n"
      "ds_read_b64_tr_b16 a[104:105], v88 offset:27680\n"
      "ds_read_b128 a[28:31], v84 offset:27744\n"
      "v_mfma_f32_16x16x32_bf16 v[58:61], a[0:3], v[68:71], v[54:57]\n"
      "v_cvt_pk_bf16_f32 v126, v24, v25\n"
      "v_cvt_pk_bf16_f32 v127, v26, v27\n"
      "v_mfma_f32_16x16x32_bf16 v[54:57], a[0:3], v[72:75], v[54:57]\n"
      "s_waitcnt lgkmcnt(4)\n"
      "v_cvt_pk_bf16_f32 v134, v28, v29\n"
      "v_cvt_pk_bf16_f32 v135, v30, v31\n"
      "v_mfma_f32_16x16x32_bf16 v[62:65], a[4:7], v[76:79], 0\n"
      "buffer_load_dwordx4 v9, s[28:31], s97 offen offset:1024 lds\n"
      "v_cvt_pk_bf16_f32 v32, v32, v33\n"
      "v_mfma_f32_16x16x32_bf16 v[20:23], a[8:11], v[68:71], v[62:65]\n"
      "v_cvt_pk_bf16_f32 v34, v34, v35\n"
      "v_accvgpr_write_b32 a214, v32\n"
      "v_accvgpr_write_b32 a215, v34\n"
      "v_mfma_f32_16x16x32_bf16 v[62:65], a[8:11], v[72:75], v[62:65]\n"
      "v_cvt_pk_bf16_f32 v142, v54, v55\n"
      "v_cvt_pk_bf16_f32 v143, v56, v57\n"
      "v_mfma_f32_16x16x32_bf16 v[24:27], a[12:15], v[76:79], 0\n"
      "v_cvt_pk_bf16_f32 v138, v58, v59\n"
      "v_cvt_pk_bf16_f32 v139, v60, v61\n"
      "v_mfma_f32_16x16x32_bf16 v[28:31], a[16:19], v[68:71], v[24:27]\n"
      "v_cvt_pk_bf16_f32 v20, v20, v21\n"
      "v_cvt_pk_bf16_f32 v22, v22, v23\n"
      "v_mfma_f32_16x16x32_bf16 v[24:27], a[16:19], v[72:75], v[24:27]\n"
      "s_waitcnt lgkmcnt(0)\n"
      "s_waitcnt vmcnt(12)\n"
      "s_cbranch_vccnz Lconv_h5\n"
      "s_barrier\n"
      "Lconv_h5:\n"
      "buffer_load_dwordx4 v89, s[28:31], s97 offen offset:2048 lds\n"
      "v_mfma_f32_16x16x32_bf16 v[32:35], a[12:15], v[80:83], 0\n"
      "s_add_u32 m0, s92, 98400\n"
      "s_and_b32 s97, 0x24242c24, s14\n"
      "s_xor_b32 s97, 0x110b0534, s97\n"
      "s_bfe_u32 s97, s97, s43\n"
      "s_mul_i32 s97, s97, 3072\n"
      "v_cvt_pk_bf16_f32 v146, v62, v63\n"
      "v_cvt_pk_bf16_f32 v147, v64, v65\n"
      "v_mfma_f32_16x16x32_bf16 v[54:57], a[20:23], v[68:71], v[32:35]\n"
      "s_cbranch_vccz Lconv_h6\n"
      "s_barrier\n"
      "Lconv_h6:\n"
      "ds_read_b128 a[32:35], v86 offset:12288\n"
      "ds_read_b128 a[0:3], v84 offset:9216\n"
      "ds_read_b128 a[4:7], v85 offset:9216\n"
      "ds_read_b128 a[8:11], v86 offset:12352\n"
      "v_mfma_f32_16x16x32_bf16 v[32:35], a[20:23], v[72:75], v[32:35]\n"
      "ds_read_b128 a[12:15], v84 offset:9280\n"
      "ds_read_b128 a[16:19], v86 offset:3072\n"
      "v_accvgpr_write_b32 a218, v20\n"
      "v_accvgpr_write_b32 a219, v22\n"
      "v_cvt_pk_bf16_f32 v152, v24, v25\n"
      "v_mfma_f32_16x16x32_bf16 v[58:61], a[24:27], v[76:79], 0\n"
      "ds_read_b128 a[20:23], v84 offset:0\n"
      "ds_read_b128 a[24:27], v85 offset:0\n"
      "buffer_load_dwordx4 v8, s[28:31], s97 offen offset:0 lds\n"
      "v_cvt_pk_bf16_f32 v153, v26, v27\n"
      "v_mfma_f32_16x16x32_bf16 v[62:65], a[28:31], v[68:71], v[58:61]\n"
      "ds_read_b64_tr_b16 a[136:137], v87 offset:9216\n"
      "ds_read_b64_tr_b16 a[140:141], v88 offset:9216\n"
      "v_cvt_pk_bf16_f32 v148, v28, v29\n"
      "v_cvt_pk_bf16_f32 v149, v30, v31\n"
      "v_mfma_f32_16x16x32_bf16 v[58:61], a[28:31], v[72:75], v[58:61]\n"
      "s_waitcnt lgkmcnt(9)\n"
      "ds_read_b128 a[28:31], v86 offset:3136\n"
      "ds_read_b64_tr_b16 a[144:145], v87 offset:9280\n"
      "v_cvt_pk_bf16_f32 v156, v32, v33\n"
      "v_cvt_pk_bf16_f32 v157, v34, v35\n"
      "v_mfma_f32_16x16x32_bf16 v[20:23], a[32:35], v[76:79], 0\n"
      "s_waitcnt lgkmcnt(10)\n"
      "ds_read_b64_tr_b16 a[172:173], v87 offset:0\n"
      "ds_read_b64_tr_b16 a[176:177], v88 offset:0\n"
      "v_mfma_f32_16x16x32_bf16 v[24:27], a[0:3], v[68:71], v[20:23]\n"
      "ds_read_b64_tr_b16 a[180:181], v87 offset:64\n"
      "ds_read_b64_tr_b16 a[102:103], v87 offset:18464\n"
      "v_cvt_pk_bf16_f32 v54, v54, v55\n"
      "v_cvt_pk_bf16_f32 v56, v56, v57\n"
      "v_accvgpr_write_b32 a200, v54\n"
      "v_mfma_f32_16x16x32_bf16 v[20:23], a[0:3], v[72:75], v[20:23]\n"
      "ds_read_b128 a[0:3], v86 offset:21536\n"
      "ds_read_b64_tr_b16 a[106:107], v88 offset:18464\n"
      "buffer_load_dwordx4 v9, s[28:31], s97 offen offset:1024 lds\n"
      "v_accvgpr_write_b32 a201, v56\n"
      "v_cvt_pk_bf16_f32 v164, v58, v59\n"
      "v_mfma_f32_16x16x32_bf16 v[28:31], a[32:35], v[80:83], 0\n"
      "s_waitcnt lgkmcnt(10)\n"
      "ds_read_b128 a[32:35], v84 offset:64\n"
      "ds_read_b64_tr_b16 a[110:111], v87 offset:18528\n"
      "v_cvt_pk_bf16_f32 v165, v60, v61\n"
      "v_mfma_f32_16x16x32_bf16 v[32:35], a[4:7], v[68:71], v[28:31]\n"
      "ds_read_b64_tr_b16 a[138:139], v87 offset:36928\n"
      "ds_read_b64_tr_b16 a[142:143], v88 offset:36928\n"
      "v_cvt_pk_bf16_f32 v160, v62, v63\n"
      "v_cvt_pk_bf16_f32 v161, v64, v65\n"
      "v_mfma_f32_16x16x32_bf16 v[28:31], a[4:7], v[72:75], v[28:31]\n"
      "ds_read_b128 a[4:7], v84 offset:18464\n"
      "ds_read_b64_tr_b16 a[146:147], v87 offset:36992\n"
      "v_cvt_pk_bf16_f32 v168, v20, v21\n"
      "v_cvt_pk_bf16_f32 v169, v22, v23\n"
      "v_mfma_f32_16x16x32_bf16 v[54:57], a[8:11], v[76:79], 0\n"
      "ds_read_b128 a[8:11], v85 offset:18464\n"
      "ds_read_b64_tr_b16 a[174:175], v87 offset:55392\n"
      "buffer_load_dwordx4 v89, s[28:31], s97 offen offset:2048 lds\n"
      "v_mfma_f32_16x16x32_bf16 v[58:61], a[12:15], v[68:71], v[54:57]\n"
      "s_cmp_lt_u32 s40, 3\n"
      "s_cselect_b32 s97, 27680, 0\n"
      "ds_read_b64_tr_b16 a[178:179], v88 offset:55392\n"
      "ds_read_b64_tr_b16 a[182:183], v87 offset:55456\n"
      "s_add_u32 s97, s97, s92\n"
      "s_add_u32 m0, s97, 36928\n"
      "s_and_b32 s97, 0x2c000000, s14\n"
      "s_xor_b32 s97, 0x29231d17, s97\n"
      "s_bfe_u32 s97, s97, s43\n"
      "s_mul_i32 s97, s97, 3072\n"
      "v_cvt_pk_bf16_f32 v24, v24, v25\n"
      "v_cvt_pk_bf16_f32 v26, v26, v27\n"
      "v_accvgpr_write_b32 a208, v24\n"
      "v_mfma_f32_16x16x32_bf16 v[54:57], a[12:15], v[72:75], v[54:57]\n"
      "ds_read_b128 a[12:15], v86 offset:21600\n"
      "v_accvgpr_write_b32 a209, v26\n"
      "v_cvt_pk_bf16_f32 v176, v28, v29\n"
      "v_cvt_pk_bf16_f32 v177, v30, v31\n"
      "v_cvt_pk_bf16_f32 v172, v32, v33\n"
      "v_mfma_f32_16x16x32_bf16 v[62:65], a[16:19], v[76:79], 0\n"
      "v_cvt_pk_bf16_f32 v173, v34, v35\n"
      "v_cvt_pk_bf16_f32 v180, v58, v59\n"
      "v_mfma_f32_16x16x32_bf16 v[20:23], a[20:23], v[68:71], v[62:65]\n"
      "v_cvt_pk_bf16_f32 v54, v54, v55\n"
      "v_cvt_pk_bf16_f32 v56, v56, v57\n"
      "v_mfma_f32_16x16x32_bf16 v[62:65], a[20:23], v[72:75], v[62:65]\n"
      "ds_read_b128 a[20:23], v86 offset:40000\n"
      "buffer_load_dwordx4 v8, s[28:31], s97 offen offset:0 lds\n"
      "v_accvgpr_write_b32 a228, v54\n"
      "v_mfma_f32_16x16x32_bf16 v[24:27], a[16:19], v[80:83], 0\n"
      "ds_read_b128 a[16:19], v84 offset:18528\n"
      "v_accvgpr_write_b32 a229, v56\n"
      "v_mfma_f32_16x16x32_bf16 v[28:31], a[24:27], v[68:71], v[24:27]\n"
      "v_cvt_pk_bf16_f32 v181, v60, v61\n"
      "v_cvt_pk_bf16_f32 v188, v62, v63\n"
      "v_mfma_f32_16x16x32_bf16 v[24:27], a[24:27], v[72:75], v[24:27]\n"
      "s_waitcnt lgkmcnt(2)\n"
      "ds_read_b128 a[24:27], v84 offset:36928\n"
      "v_cvt_pk_bf16_f32 v189, v64, v65\n"
      "v_cvt_pk_bf16_f32 v184, v20, v21\n"
      "v_cvt_pk_bf16_f32 v185, v22, v23\n"
      "v_mfma_f32_16x16x32_bf16 v[32:35], a[28:31], v[76:79], 0\n"
      "ds_read_b128 a[28:31], v85 offset:36928\n"
      "buffer_load_dwordx4 v9, s[28:31], s97 offen offset:1024 lds\n"
      "v_mfma_f32_16x16x32_bf16 v[54:57], a[32:35], v[68:71], v[32:35]\n"
      "v_cvt_pk_bf16_f32 v24, v24, v25\n"
      "v_cvt_pk_bf16_f32 v26, v26, v27\n"
      "v_mfma_f32_16x16x32_bf16 v[32:35], a[32:35], v[72:75], v[32:35]\n"
      "ds_read_b128 a[32:35], v86 offset:40064\n"
      "v_accvgpr_write_b32 a236, v24\n"
      "v_accvgpr_write_b32 a237, v26\n"
      "v_mfma_f32_16x16x32_bf16 v[58:61], a[0:3], v[76:79], 0\n"
      "v_cvt_pk_bf16_f32 v192, v28, v29\n"
      "v_cvt_pk_bf16_f32 v193, v30, v31\n"
      "v_mfma_f32_16x16x32_bf16 v[62:65], a[4:7], v[68:71], v[58:61]\n"
      "v_cvt_pk_bf16_f32 v196, v54, v55\n"
      "v_cvt_pk_bf16_f32 v200, v32, v33\n"
      "v_mfma_f32_16x16x32_bf16 v[58:61], a[4:7], v[72:75], v[58:61]\n"
      "ds_read_b128 a[4:7], v86 offset:58464\n"
      "buffer_load_dwordx4 v89, s[28:31], s97 offen offset:2048 lds\n"
      "v_mfma_f32_16x16x32_bf16 v[20:23], a[0:3], v[80:83], 0\n"
      "s_cmp_lt_u32 s40, 2\n"
      "s_cselect_b32 s97, 6144, 0\n"
      "ds_read_b128 a[0:3], v84 offset:36992\n"
      "s_add_u32 s97, s97, s92\n"
      "s_add_u32 m0, s97, 43072\n"
      "s_and_b32 s97, 0x24242424, s14\n"
      "s_xor_b32 s97, 0x352f352f, s97\n"
      "s_bfe_u32 s97, s97, s43\n"
      "s_mul_i32 s97, s97, 3072\n"
      "v_cvt_pk_bf16_f32 v201, v34, v35\n"
      "v_mfma_f32_16x16x32_bf16 v[24:27], a[8:11], v[68:71], v[20:23]\n"
      "v_cvt_pk_bf16_f32 v197, v56, v57\n"
      "v_cvt_pk_bf16_f32 v154, v58, v59\n"
      "v_cvt_pk_bf16_f32 v155, v60, v61\n"
      "v_mfma_f32_16x16x32_bf16 v[20:23], a[8:11], v[72:75], v[20:23]\n"
      "ds_read_b128 a[8:11], v84 offset:55392\n"
      "v_cvt_pk_bf16_f32 v150, v62, v63\n"
      "v_cvt_pk_bf16_f32 v151, v64, v65\n"
      "v_mfma_f32_16x16x32_bf16 v[28:31], a[12:15], v[76:79], 0\n"
      "s_waitcnt lgkmcnt(3)\n"
      "ds_read_b128 a[12:15], v85 offset:55392\n"
      "buffer_load_dwordx4 v8, s[28:31], s97 offen offset:0 lds\n"
      "v_cvt_pk_bf16_f32 v24, v24, v25\n"
      "v_mfma_f32_16x16x32_bf16 v[32:35], a[16:19], v[68:71], v[28:31]\n"
      "v_cvt_pk_bf16_f32 v158, v20, v21\n"
      "v_cvt_pk_bf16_f32 v159, v22, v23\n"
      "v_mfma_f32_16x16x32_bf16 v[28:31], a[16:19], v[72:75], v[28:31]\n"
      "ds_read_b128 a[16:19], v86 offset:58528\n"
      "v_cvt_pk_bf16_f32 v26, v26, v27\n"
      "v_accvgpr_write_b32 a202, v24\n"
      "v_accvgpr_write_b32 a203, v26\n"
      "v_mfma_f32_16x16x32_bf16 v[54:57], a[20:23], v[76:79], 0\n"
      "v_cvt_pk_bf16_f32 v162, v32, v33\n"
      "v_cvt_pk_bf16_f32 v163, v34, v35\n"
      "v_mfma_f32_16x16x32_bf16 v[58:61], a[24:27], v[68:71], v[54:57]\n"
      "v_cvt_pk_bf16_f32 v166, v28, v29\n"
      "v_cvt_pk_bf16_f32 v167, v30, v31\n"
      "v_mfma_f32_16x16x32_bf16 v[54:57], a[24:27], v[72:75], v[54:57]\n"
      "buffer_load_dwordx4 v9, s[28:31], s97 offen offset:1024 lds\n"
      "v_add_u32 v86, 46144, v86\n"
      "v_add_u32 v85, 46144, v85\n"
      "v_mfma_f32_16x16x32_bf16 v[62:65], a[20:23], v[80:83], 0\n"
      "ds_read_b128 a[20:23], v84 offset:55456\n"
      "v_cvt_pk_bf16_f32 v58, v58, v59\n"
      "v_mfma_f32_16x16x32_bf16 v[20:23], a[28:31], v[68:71], v[62:65]\n"
      "v_cvt_pk_bf16_f32 v60, v60, v61\n"
      "v_cvt_pk_bf16_f32 v170, v54, v55\n"
      "v_mfma_f32_16x16x32_bf16 v[62:65], a[28:31], v[72:75], v[62:65]\n"
      "v_cvt_pk_bf16_f32 v171, v56, v57\n"
      "v_accvgpr_write_b32 a210, v58\n"
      "v_mfma_f32_16x16x32_bf16 v[24:27], a[32:35], v[76:79], 0\n"
      "s_waitcnt lgkmcnt(1)\n"
      "buffer_load_dwordx4 v89, s[28:31], s97 offen offset:2048 lds\n"
      "v_accvgpr_write_b32 a211, v60\n"
      "v_mfma_f32_16x16x32_bf16 v[28:31], a[0:3], v[68:71], v[24:27]\n"
      "s_add_u32 m0, s46, 39840\n"
      "s_add_u32 s51, s51, s12\n"
      "v_add_u32 v84, 46144, v84\n"
      "v_cvt_pk_bf16_f32 v178, v62, v63\n"
      "v_mfma_f32_16x16x32_bf16 v[24:27], a[0:3], v[72:75], v[24:27]\n"
      "v_cvt_pk_bf16_f32 v179, v64, v65\n"
      "v_cvt_pk_bf16_f32 v174, v20, v21\n"
      "v_cvt_pk_bf16_f32 v175, v22, v23\n"
      "v_mfma_f32_16x16x32_bf16 v[32:35], a[4:7], v[76:79], 0\n"
      "v_cvt_pk_bf16_f32 v182, v28, v29\n"
      "v_cvt_pk_bf16_f32 v183, v30, v31\n"
      "v_mfma_f32_16x16x32_bf16 v[54:57], a[8:11], v[68:71], v[32:35]\n"
      "v_add_u32 v87, 46144, v87\n"
      "v_cvt_pk_bf16_f32 v24, v24, v25\n"
      "v_mfma_f32_16x16x32_bf16 v[32:35], a[8:11], v[72:75], v[32:35]\n"
      "s_waitcnt lgkmcnt(0)\n"
      "s_waitcnt vmcnt(6)\n"
      "s_cbranch_vccnz Lconv_h7\n"
      "s_barrier\n"
      "Lconv_h7:\n"
      "buffer_load_dwordx4 v16, s[32:35], s51 offen lds\n"
      "v_mfma_f32_16x16x32_bf16 v[58:61], a[4:7], v[80:83], 0\n"
      "s_add_u32 m0, s46, 39840\n"
      "v_cvt_pk_bf16_f32 v26, v26, v27\n"
      "v_accvgpr_write_b32 a230, v24\n"
      "v_mfma_f32_16x16x32_bf16 v[62:65], a[12:15], v[68:71], v[58:61]\n"
      "s_cbranch_vccz Lconv_h8\n"
      "s_barrier\n"
      "Lconv_h8:\n"
      "ds_read_b128 a[24:27], v86 offset:30752\n"
      "ds_read_b128 a[28:31], v84 offset:27680\n"
      "ds_read_b128 a[32:35], v85 offset:27680\n"
      "ds_read_b128 a[0:3], v86 offset:30816\n"
      "buffer_load_dwordx4 v17, s[32:35], s51 offen offset:1024 lds\n"
      "v_mfma_f32_16x16x32_bf16 v[58:61], a[12:15], v[72:75], v[58:61]\n"
      "s_add_u32 m0, s46, 39840\n"
      "ds_read_b128 a[4:7], v84 offset:27744\n"
      "ds_read_b128 a[8:11], v86 offset:39968\n"
      "buffer_load_dwordx4 v18, s[32:35], s51 offen offset:2048 lds\n"
      "v_accvgpr_write_b32 a231, v26\n"
      "v_cvt_pk_bf16_f32 v190, v32, v33\n"
      "v_mfma_f32_16x16x32_bf16 v[20:23], a[16:19], v[76:79], 0\n"
      "s_sub_u32 m0, s94, s59\n"
      "s_add_u32 s96, s51, s15\n"
      "ds_read_b128 a[12:15], v84 offset:36896\n"
      "ds_read_b128 a[16:19], v85 offset:36896\n"
      "buffer_load_dwordx4 v19, s[32:35], s96 offen lds\n"
      "v_cvt_pk_bf16_f32 v191, v34, v35\n"
      "v_cvt_pk_bf16_f32 v186, v54, v55\n"
      "v_mfma_f32_16x16x32_bf16 v[24:27], a[20:23], v[68:71], v[20:23]\n"
      "s_add_u32 m0, s46, 0\n"
      "s_add_u32 s51, s51, s12\n"
      "ds_read_b128 a[36:39], v2 offset:13280\n"
      "ds_read_b128 a[40:43], v3 offset:13280\n"
      "buffer_load_dwordx4 v16, s[32:35], s51 offen lds\n"
      "v_cvt_pk_bf16_f32 v187, v56, v57\n"
      "v_cvt_pk_bf16_f32 v58, v58, v59\n"
      "v_mfma_f32_16x16x32_bf16 v[20:23], a[20:23], v[72:75], v[20:23]\n"
      "s_waitcnt lgkmcnt(9)\n"
      "s_add_u32 m0, s46, 0\n"
      "ds_read_b128 a[20:23], v86 offset:40032\n"
      "ds_read_b128 a[44:47], v4 offset:13280\n"
      "buffer_load_dwordx4 v17, s[32:35], s51 offen offset:1024 lds\n"
      "v_cvt_pk_bf16_f32 v60, v60, v61\n"
      "v_accvgpr_write_b32 a238, v58\n"
      "v_mfma_f32_16x16x32_bf16 v[28:31], a[24:27], v[76:79], 0\n"
      "s_waitcnt lgkmcnt(10)\n"
      "s_add_u32 m0, s46, 0\n"
      "ds_read_b128 a[48:51], v2 offset:13344\n"
      "ds_read_b128 a[52:55], v3 offset:13344\n"
      "buffer_load_dwordx4 v18, s[32:35], s51 offen offset:2048 lds\n"
      "v_mfma_f32_16x16x32_bf16 v[32:35], a[28:31], v[68:71], v[28:31]\n"
      "s_mov_b32 m0, s18\n"
      "ds_read_b128 a[56:59], v4 offset:13344\n"
      "ds_read_b64_tr_b16 a[112:113], v87 offset:27680\n"
      "v_accvgpr_write_b32 a239, v60\n"
      "v_cvt_pk_bf16_f32 v194, v62, v63\n"
      "v_mfma_f32_16x16x32_bf16 v[28:31], a[28:31], v[72:75], v[28:31]\n"
      "ds_read_b128 a[28:31], v86 offset:49184\n"
      "ds_read_b64_tr_b16 a[120:121], v87 offset:27744\n"
      "v_cvt_pk_bf16_f32 v195, v64, v65\n"
      "v_cvt_pk_bf16_f32 v202, v20, v21\n"
      "v_cvt_pk_bf16_f32 v203, v22, v23\n"
      "v_mfma_f32_16x16x32_bf16 v[54:57], a[24:27], v[80:83], 0\n"
      "s_waitcnt lgkmcnt(10)\n"
      "ds_read_b128 a[24:27], v84 offset:36960\n"
      "ds_read_b64_tr_b16 a[148:149], v87 offset:36896\n"
      "v_cvt_pk_bf16_f32 v198, v24, v25\n"
      "v_mfma_f32_16x16x32_bf16 v[58:61], a[32:35], v[68:71], v[54:57]\n"
      "ds_read_b64_tr_b16 a[156:157], v87 offset:36960\n"
      "ds_read_b64_tr_b16 a[184:185], v87 offset:46112\n"
      "v_cvt_pk_bf16_f32 v199, v26, v27\n"
      "v_cvt_pk_bf16_f32 v28, v28, v29\n"
      "v_mfma_f32_16x16x32_bf16 v[54:57], a[32:35], v[72:75], v[54:57]\n"
      "ds_read_b128 a[32:35], v84 offset:46112\n"
      "ds_read_b64_tr_b16 a[192:193], v87 offset:46176\n"
      "v_cvt_pk_bf16_f32 v30, v30, v31\n"
      "v_accvgpr_write_b32 a220, v28\n"
      "v_mfma_f32_16x16x32_bf16 v[62:65], a[0:3], v[76:79], 0\n"
      "ds_read_b128 a[0:3], v85 offset:46112\n"
      "v_accvgpr_write_b32 a221, v30\n"
      "v_mfma_f32_16x16x32_bf16 v[20:23], a[4:7], v[68:71], v[62:65]\n"
      "v_cvt_pk_bf16_f32 v204, v32, v33\n"
      "v_cvt_pk_bf16_f32 v205, v34, v35\n"
      "v_mfma_f32_16x16x32_bf16 v[62:65], a[4:7], v[72:75], v[62:65]\n"
      "ds_read_b128 a[4:7], v86 offset:49248\n"
      "v_cvt_pk_bf16_f32 v212, v54, v55\n"
      "v_cvt_pk_bf16_f32 v213, v56, v57\n"
      "v_cvt_pk_bf16_f32 v208, v58, v59\n"
      "v_mfma_f32_16x16x32_bf16 v[24:27], a[8:11], v[76:79], 0\n"
      "v_cvt_pk_bf16_f32 v209, v60, v61\n"
      "v_add_u32 v88, 46144, v88\n"
      "v_mfma_f32_16x16x32_bf16 v[28:31], a[12:15], v[68:71], v[24:27]\n"
      "ds_read_b64_tr_b16 a[116:117], v88 offset:27680\n"
      "ds_read_b64_tr_b16 a[152:153], v88 offset:36896\n"
      "v_mfma_f32_16x16x32_bf16 v[24:27], a[12:15], v[72:75], v[24:27]\n"
      "ds_read_b64_tr_b16 a[188:189], v88 offset:46112\n"
      "v_cvt_pk_bf16_f32 v62, v62, v63\n"
      "v_cvt_pk_bf16_f32 v64, v64, v65\n"
      "v_mfma_f32_16x16x32_bf16 v[32:35], a[8:11], v[80:83], 0\n"
      "ds_read_b128 a[8:11], v84 offset:46176\n"
      "v_accvgpr_write_b32 a224, v62\n"
      "v_mfma_f32_16x16x32_bf16 v[54:57], a[16:19], v[68:71], v[32:35]\n"
      "v_accvgpr_write_b32 a225, v64\n"
      "v_cvt_pk_bf16_f32 v216, v20, v21\n"
      "v_mfma_f32_16x16x32_bf16 v[32:35], a[16:19], v[72:75], v[32:35]\n"
      "s_waitcnt lgkmcnt(4)\n"
      "v_cvt_pk_bf16_f32 v217, v22, v23\n"
      "v_cvt_pk_bf16_f32 v224, v24, v25\n"
      "v_cvt_pk_bf16_f32 v225, v26, v27\n"
      "v_mfma_f32_16x16x32_bf16 v[58:61], a[20:23], v[76:79], 0\n"
      "v_cvt_pk_bf16_f32 v228, v54, v55\n"
      "v_cvt_pk_bf16_f32 v229, v56, v57\n"
      "v_mfma_f32_16x16x32_bf16 v[62:65], a[24:27], v[68:71], v[58:61]\n"
      "v_cvt_pk_bf16_f32 v220, v28, v29\n"
      "v_cvt_pk_bf16_f32 v221, v30, v31\n"
      "v_mfma_f32_16x16x32_bf16 v[58:61], a[24:27], v[72:75], v[58:61]\n"
      "v_cvt_pk_bf16_f32 v32, v32, v33\n"
      "v_cvt_pk_bf16_f32 v34, v34, v35\n"
      "v_mfma_f32_16x16x32_bf16 v[36:39], a[36:39], a[72:75], 0\n"
      "ds_read_b128 a[36:39], v2 offset:13408\n"
      "v_accvgpr_write_b32 a232, v32\n"
      "v_mfma_f32_16x16x32_bf16 v[40:43], a[40:43], a[76:79], v[36:39]\n"
      "v_cvt_pk_bf16_f32 v233, v64, v65\n"
      "v_cvt_pk_bf16_f32 v232, v62, v63\n"
      "v_mfma_f32_16x16x32_bf16 v[48:51], a[40:43], a[252:255], v[36:39]\n"
      "ds_read_b128 a[40:43], v3 offset:13408\n"
      "v_cvt_pk_bf16_f32 v236, v58, v59\n"
      "v_mfma_f32_16x16x32_bf16 v[44:47], a[44:47], a[80:83], v[36:39]\n"
      "ds_read_b128 a[44:47], v4 offset:13408\n"
      "v_cvt_pk_bf16_f32 v237, v60, v61\n"
      "v_mfma_f32_16x16x32_bf16 v[52:55], a[48:51], a[72:75], 0\n"
      "ds_read_b128 a[48:51], v2 offset:26560\n"
      "v_cvt_pk_bf16_f32 v40, v40, v41\n"
      "v_mfma_f32_16x16x32_bf16 v[56:59], a[52:55], a[76:79], v[52:55]\n"
      "ds_write_addtid_b32 v40 offset:768\n"
      "v_cvt_pk_bf16_f32 v41, v42, v43\n"
      "v_mfma_f32_16x16x32_bf16 v[64:67], a[52:55], a[252:255], v[52:55]\n"
      "ds_read_b128 a[52:55], v3 offset:26560\n"
      "ds_write_addtid_b32 v41 offset:37696\n"
      "v_cvt_pk_bf16_f32 v48, v48, v49\n"
      "v_mfma_f32_16x16x32_bf16 v[60:63], a[56:59], a[80:83], v[52:55]\n"
      "s_waitcnt lgkmcnt(6)\n"
      "ds_read_b128 a[56:59], v4 offset:26560\n"
      "ds_write_addtid_b32 v48 offset:1536\n"
      "v_cvt_pk_bf16_f32 v49, v50, v51\n"
      "v_cvt_pk_bf16_f32 v44, v44, v45\n"
      "v_mfma_f32_16x16x32_bf16 v[36:39], a[36:39], a[72:75], 0\n"
      "s_waitcnt lgkmcnt(7)\n"
      "ds_write_addtid_b32 v49 offset:38464\n"
      "ds_read_b128 a[36:39], v2 offset:26624\n"
      "ds_write_addtid_b32 v44 offset:0\n"
      "v_cvt_pk_bf16_f32 v45, v46, v47\n"
      "v_mfma_f32_16x16x32_bf16 v[40:43], a[40:43], a[76:79], v[36:39]\n"
      "ds_write_addtid_b32 v45 offset:36928\n"
      "s_mov_b32 m0, s18\n"
      "v_cvt_pk_bf16_f32 v56, v56, v57\n"
      "v_mfma_f32_16x16x32_bf16 v[48:51], a[40:43], a[252:255], v[36:39]\n"
      "s_waitcnt lgkmcnt(9)\n"
      "ds_read_b128 a[40:43], v3 offset:26624\n"
      "ds_write_addtid_b32 v56 offset:1024\n"
      "v_cvt_pk_bf16_f32 v57, v58, v59\n"
      "v_cvt_pk_bf16_f32 v64, v64, v65\n"
      "v_mfma_f32_16x16x32_bf16 v[44:47], a[44:47], a[80:83], v[36:39]\n"
      "ds_write_addtid_b32 v57 offset:37952\n"
      "ds_read_b128 a[44:47], v4 offset:26624\n"
      "ds_write_addtid_b32 v64 offset:1792\n"
      "v_cvt_pk_bf16_f32 v65, v66, v67\n"
      "v_cvt_pk_bf16_f32 v60, v60, v61\n"
      "v_mfma_f32_16x16x32_bf16 v[52:55], a[48:51], a[72:75], 0\n"
      "s_waitcnt lgkmcnt(12)\n"
      "ds_write_addtid_b32 v65 offset:38720\n"
      "ds_read_b128 a[48:51], v2 offset:26688\n"
      "ds_write_addtid_b32 v60 offset:256\n"
      "v_cvt_pk_bf16_f32 v61, v62, v63\n"
      "v_mfma_f32_16x16x32_bf16 v[56:59], a[52:55], a[76:79], v[52:55]\n"
      "ds_write_addtid_b32 v61 offset:37184\n"
      "s_mov_b32 m0, s18\n"
      "v_cvt_pk_bf16_f32 v40, v40, v41\n"
      "v_mfma_f32_16x16x32_bf16 v[64:67], a[52:55], a[252:255], v[52:55]\n"
      "s_waitcnt lgkmcnt(11)\n"
      "ds_read_b128 a[52:55], v3 offset:26688\n"
      "ds_write_addtid_b32 v40 offset:1280\n"
      "v_cvt_pk_bf16_f32 v41, v42, v43\n"
      "v_cvt_pk_bf16_f32 v48, v48, v49\n"
      "v_mfma_f32_16x16x32_bf16 v[60:63], a[56:59], a[80:83], v[52:55]\n"
      "ds_write_addtid_b32 v41 offset:38208\n"
      "ds_read_b128 a[56:59], v4 offset:26688\n"
      "ds_write_addtid_b32 v48 offset:2048\n"
      "v_cvt_pk_bf16_f32 v49, v50, v51\n"
      "v_cvt_pk_bf16_f32 v44, v44, v45\n"
      "v_mfma_f32_16x16x32_bf16 v[36:39], a[36:39], a[72:75], 0\n"
      "s_waitcnt lgkmcnt(13)\n"
      "ds_write_addtid_b32 v49 offset:38976\n"
      "ds_write_addtid_b32 v44 offset:512\n"
      "v_cvt_pk_bf16_f32 v45, v46, v47\n"
      "v_mfma_f32_16x16x32_bf16 v[40:43], a[40:43], a[76:79], v[36:39]\n"
      "ds_write_addtid_b32 v45 offset:37440\n"
      "s_mov_b32 m0, s18\n"
      "v_cvt_pk_bf16_f32 v56, v56, v57\n"
      "v_mfma_f32_16x16x32_bf16 v[48:51], a[40:43], a[252:255], v[36:39]\n"
      "s_waitcnt lgkmcnt(10)\n"
      "ds_write_addtid_b32 v56 offset:5376\n"
      "v_cvt_pk_bf16_f32 v57, v58, v59\n"
      "v_cvt_pk_bf16_f32 v64, v64, v65\n"
      "v_mfma_f32_16x16x32_bf16 v[44:47], a[44:47], a[80:83], v[36:39]\n"
      "ds_write_addtid_b32 v57 offset:42304\n"
      "ds_write_addtid_b32 v64 offset:6144\n"
      "v_cvt_pk_bf16_f32 v65, v66, v67\n"
      "v_cvt_pk_bf16_f32 v60, v60, v61\n"
      "v_cvt_pk_bf16_f32 v61, v62, v63\n"
      "v_mfma_f32_16x16x32_bf16 v[52:55], a[48:51], a[72:75], 0\n"
      "s_waitcnt lgkmcnt(10)\n"
      "ds_write_addtid_b32 v65 offset:43072\n"
      "ds_write_addtid_b32 v60 offset:4608\n"
      "ds_write_addtid_b32 v61 offset:41536\n"
      "s_mov_b32 m0, s18\n"
      "v_cvt_pk_bf16_f32 v48, v48, v49\n"
      "v_cvt_pk_bf16_f32 v49, v50, v51\n"
      "v_mfma_f32_16x16x32_bf16 v[56:59], a[52:55], a[76:79], v[52:55]\n"
      "ds_write_addtid_b32 v48 offset:6400\n"
      "ds_write_addtid_b32 v49 offset:43328\n"
      "v_cvt_pk_bf16_f32 v40, v40, v41\n"
      "v_cvt_pk_bf16_f32 v41, v42, v43\n"
      "v_cvt_pk_bf16_f32 v44, v44, v45\n"
      "v_mfma_f32_16x16x32_bf16 v[64:67], a[52:55], a[252:255], v[52:55]\n"
      "s_waitcnt lgkmcnt(12)\n"
      "ds_write_addtid_b32 v40 offset:5632\n"
      "ds_write_addtid_b32 v41 offset:42560\n"
      "ds_write_addtid_b32 v44 offset:4864\n"
      "v_cvt_pk_bf16_f32 v45, v46, v47\n"
      "v_cvt_pk_bf16_f32 v56, v56, v57\n"
      "v_mfma_f32_16x16x32_bf16 v[60:63], a[56:59], a[80:83], v[52:55]\n"
      "ds_write_addtid_b32 v45 offset:41792\n"
      "s_mov_b32 m0, s18\n"
      "v_cvt_pk_bf16_f32 v64, v64, v65\n"
      "v_cvt_pk_bf16_f32 v65, v66, v67\n"
      "v_cvt_pk_bf16_f32 v57, v58, v59\n"
      "v_mfma_f32_16x16x32_bf16 v[20:23], a[28:31], v[76:79], 0\n"
      "ds_write_addtid_b32 v64 offset:6656\n"
      "ds_write_addtid_b32 v65 offset:43584\n"
      "ds_write_addtid_b32 v56 offset:5888\n"
      "ds_write_addtid_b32 v57 offset:42816\n"
      "v_cvt_pk_bf16_f32 v60, v60, v61\n"
      "v_mfma_f32_16x16x32_bf16 v[24:27], a[32:35], v[68:71], v[20:23]\n"
      "ds_write_addtid_b32 v60 offset:5120\n"
      "v_cvt_pk_bf16_f32 v61, v62, v63\n"
      "ds_write_addtid_b32 v61 offset:42048\n"
      "v_mfma_f32_16x16x32_bf16 v[20:23], a[32:35], v[72:75], v[20:23]\n"
      "s_waitcnt lgkmcnt(0)\n"
      "s_waitcnt vmcnt(7)\n"
      "s_cbranch_vccnz Lconv_h9\n"
      "s_barrier\n"
      "Lconv_h9:\n"
      "s_add_u32 m0, s46, 13280\n"
      "s_add_u32 s51, s51, s12\n"
      "v_accvgpr_write_b32 a233, v34\n"
      "v_mfma_f32_16x16x32_bf16 v[28:31], a[28:31], v[80:83], 0\n"
      "Lconv_p2:\n"
      "s_cbranch_execz Lconv_r0\n"
      "s_nop 0\n"
      "v_cvt_pk_bf16_f32 v240, v24, v25\n"
      "v_cvt_pk_bf16_f32 v20, v20, v21\n"
      "v_mfma_f32_16x16x32_bf16 v[32:35], a[0:3], v[68:71], v[28:31]\n"
      "s_cbranch_vccz Lconv_h10\n"
      "s_barrier\n"
      "Lconv_h10:\n"
      "ds_read_b128 a[12:15], v86 offset:58400\n"
      "ds_read_b128 a[16:19], v84 offset:55328\n"
      "ds_read_b128 a[20:23], v85 offset:55328\n"
      "ds_read_b128 a[24:27], v86 offset:58464\n"
      "v_mfma_f32_16x16x32_bf16 v[28:31], a[0:3], v[72:75], v[28:31]\n"
      "ds_read_b128 a[28:31], v84 offset:55392\n"
      "ds_read_b128 a[32:35], v86 offset:21536\n"
      "v_mfma_f32_16x16x32_bf16 v[54:57], a[4:7], v[76:79], 0\n"
      "ds_read_b128 a[0:3], v84 offset:18464\n"
      "ds_read_b128 a[4:7], v85 offset:18464\n"
      "v_mfma_f32_16x16x32_bf16 v[58:61], a[8:11], v[68:71], v[54:57]\n"
      "ds_read_b64_tr_b16 a[114:115], v87 offset:55328\n"
      "ds_read_b64_tr_b16 a[118:119], v88 offset:55328\n"
      "v_cvt_pk_bf16_f32 v22, v22, v23\n"
      "v_accvgpr_write_b32 a240, v20\n"
      "v_mfma_f32_16x16x32_bf16 v[54:57], a[8:11], v[72:75], v[54:57]\n"
      "s_waitcnt lgkmcnt(9)\n"
      "ds_read_b128 a[8:11], v86 offset:21600\n"
      "ds_read_b64_tr_b16 a[122:123], v87 offset:55392\n"
      "buffer_load_dwordx4 v16, s[32:35], s51 offen lds\n"
      "v_accvgpr_write_b32 a241, v22\n"
      "v_cvt_pk_bf16_f32 v241, v26, v27\n"
      "v_mfma_f32_16x16x32_bf16 v[62:65], a[12:15], v[76:79], 0\n"
      "s_waitcnt lgkmcnt(10)\n"
      "s_add_u32 m0, s46, 13280\n"
      "ds_read_b64_tr_b16 a[150:151], v87 offset:18464\n"
      "ds_read_b64_tr_b16 a[154:155], v88 offset:18464\n"
      "buffer_load_dwordx4 v17, s[32:35], s51 offen offset:1024 lds\n"
      "v_cvt_pk_bf16_f32 v248, v28, v29\n"
      "v_cvt_pk_bf16_f32 v249, v30, v31\n"
      "v_mfma_f32_16x16x32_bf16 v[20:23], a[16:19], v[68:71], v[62:65]\n"
      "s_add_u32 m0, s46, 13280\n"
      "s_add_u32 s96, s51, s15\n"
      "ds_read_b64_tr_b16 a[158:159], v87 offset:18528\n"
      "ds_read_b64_tr_b16 a[186:187], v87 offset:0\n"
      "buffer_load_dwordx4 v18, s[32:35], s51 offen offset:2048 lds\n"
      "v_cvt_pk_bf16_f32 v244, v32, v33\n"
      "v_cvt_pk_bf16_f32 v245, v34, v35\n"
      "v_mfma_f32_16x16x32_bf16 v[62:65], a[16:19], v[72:75], v[62:65]\n"
      "s_mov_b32 m0, s59\n"
      "ds_read_b128 a[16:19], v86 offset:3072\n"
      "ds_read_b64_tr_b16 a[190:191], v88 offset:0\n"
      "buffer_load_dwordx4 v19, s[32:35], s96 offen lds\n"
      "v_cvt_pk_bf16_f32 v54, v54, v55\n"
      "v_cvt_pk_bf16_f32 v56, v56, v57\n"
      "v_mfma_f32_16x16x32_bf16 v[24:27], a[12:15], v[80:83], 0\n"
      "s_waitcnt lgkmcnt(10)\n"
      "ds_read_b128 a[12:15], v84 offset:18528\n"
      "ds_read_b64_tr_b16 a[194:195], v87 offset:64\n"
      "v_accvgpr_write_b32 a244, v54\n"
      "v_accvgpr_write_b32 a245, v56\n"
      "v_mfma_f32_16x16x32_bf16 v[28:31], a[20:23], v[68:71], v[24:27]\n"
      "v_cvt_pk_bf16_f32 v252, v58, v59\n"
      "v_cvt_pk_bf16_f32 v253, v60, v61\n"
      "v_mfma_f32_16x16x32_bf16 v[24:27], a[20:23], v[72:75], v[24:27]\n"
      "ds_read_b128 a[20:23], v84 offset:0\n"
      "v_cvt_pk_bf16_f32 v62, v62, v63\n"
      "v_cvt_pk_bf16_f32 v64, v64, v65\n"
      "v_mfma_f32_16x16x32_bf16 v[32:35], a[24:27], v[76:79], 0\n"
      "ds_read_b128 a[24:27], v85 offset:0\n"
      "v_accvgpr_write_b32 a222, v62\n"
      "v_mfma_f32_16x16x32_bf16 v[54:57], a[28:31], v[68:71], v[32:35]\n"
      "v_accvgpr_write_b32 a223, v64\n"
      "v_cvt_pk_bf16_f32 v206, v20, v21\n"
      "v_mfma_f32_16x16x32_bf16 v[32:35], a[28:31], v[72:75], v[32:35]\n"
      "ds_read_b128 a[28:31], v86 offset:3136\n"
      "v_cvt_pk_bf16_f32 v207, v22, v23\n"
      "v_cvt_pk_bf16_f32 v214, v24, v25\n"
      "v_cvt_pk_bf16_f32 v215, v26, v27\n"
      "v_mfma_f32_16x16x32_bf16 v[58:61], a[32:35], v[76:79], 0\n"
      "v_cvt_pk_bf16_f32 v210, v28, v29\n"
      "v_cvt_pk_bf16_f32 v211, v30, v31\n"
      "v_mfma_f32_16x16x32_bf16 v[62:65], a[0:3], v[68:71], v[58:61]\n"
      "v_cvt_pk_bf16_f32 v32, v32, v33\n"
      "v_cvt_pk_bf16_f32 v34, v34, v35\n"
      "v_mfma_f32_16x16x32_bf16 v[58:61], a[0:3], v[72:75], v[58:61]\n"
      "v_accvgpr_write_b32 a226, v32\n"
      "v_accvgpr_write_b32 a227, v34\n"
      "v_mfma_f32_16x16x32_bf16 v[20:23], a[32:35], v[80:83], 0\n"
      "ds_read_b128 a[32:35], v84 offset:64\n"
      "v_cvt_pk_bf16_f32 v218, v54, v55\n"
      "v_mfma_f32_16x16x32_bf16 v[24:27], a[4:7], v[68:71], v[20:23]\n"
      "v_cvt_pk_bf16_f32 v219, v56, v57\n"
      "v_cvt_pk_bf16_f32 v222, v62, v63\n"
      "v_mfma_f32_16x16x32_bf16 v[20:23], a[4:7], v[72:75], v[20:23]\n"
      "s_waitcnt lgkmcnt(1)\n"
      "v_cvt_pk_bf16_f32 v226, v58, v59\n"
      "v_cvt_pk_bf16_f32 v227, v60, v61\n"
      "v_mfma_f32_16x16x32_bf16 v[28:31], a[8:11], v[76:79], 0\n"
      "v_cvt_pk_bf16_f32 v223, v64, v65\n"
      "v_cvt_pk_bf16_f32 v230, v24, v25\n"
      "v_mfma_f32_16x16x32_bf16 v[32:35], a[12:15], v[68:71], v[28:31]\n"
      "v_cvt_pk_bf16_f32 v231, v26, v27\n"
      "v_cvt_pk_bf16_f32 v20, v20, v21\n"
      "v_mfma_f32_16x16x32_bf16 v[28:31], a[12:15], v[72:75], v[28:31]\n"
      "ds_read_b128 a[12:15], v6 offset:0\n"
      "v_cvt_pk_bf16_f32 v22, v22, v23\n"
      "v_mfma_f32_16x16x32_bf16 v[54:57], a[16:19], v[76:79], 0\n"
      "v_accvgpr_write_b32 a234, v20\n"
      "v_accvgpr_write_b32 a235, v22\n"
      "v_mfma_f32_16x16x32_bf16 v[58:61], a[20:23], v[68:71], v[54:57]\n"
      "v_cvt_pk_bf16_f32 v234, v32, v33\n"
      "v_cvt_pk_bf16_f32 v235, v34, v35\n"
      "v_mfma_f32_16x16x32_bf16 v[54:57], a[20:23], v[72:75], v[54:57]\n"
      "s_waitcnt lgkmcnt(0)\n"
      "s_waitcnt vmcnt(4)\n"
      "s_cbranch_vccnz Lconv_h11\n"
      "s_barrier\n"
      "Lconv_h11:\n"
      "ds_read_b128 a[20:23], v6 offset:512\n"
      "v_mfma_f32_16x16x32_bf16 v[62:65], a[16:19], v[80:83], 0\n"
      "ds_read_b128 a[16:19], v6 offset:256\n"
      "v_cvt_pk_bf16_f32 v238, v28, v29\n"
      "v_mfma_f32_16x16x32_bf16 v[20:23], a[24:27], v[68:71], v[62:65]\n"
      "s_cbranch_vccz Lconv_h12\n"
      "s_barrier\n"
      "Lconv_h12:\n"
      "v_cvt_pk_bf16_f32 v239, v30, v31\n"
      "v_mfma_f32_16x16x32_bf16 v[62:65], a[24:27], v[72:75], v[62:65]\n"
      "ds_read_b128 a[24:27], v6 offset:4608\n"
      "v_cvt_pk_bf16_f32 v242, v58, v59\n"
      "v_cvt_pk_bf16_f32 v243, v60, v61\n"
      "v_mfma_f32_16x16x32_bf16 v[24:27], a[28:31], v[76:79], 0\n"
      "ds_read_b128 a[28:31], v6 offset:4864\n"
      "v_cvt_pk_bf16_f32 v54, v54, v55\n"
      "v_mfma_f32_16x16x32_bf16 v[28:31], a[32:35], v[68:71], v[24:27]\n"
      "v_cvt_pk_bf16_f32 v56, v56, v57\n"
      "v_accvgpr_write_b32 a242, v54\n"
      "v_mfma_f32_16x16x32_bf16 v[24:27], a[32:35], v[72:75], v[24:27]\n"
      "ds_read_b128 a[32:35], v6 offset:5120\n"
      "v_accvgpr_write_b32 a243, v56\n"
      "v_cvt_pk_bf16_f32 v246, v20, v21\n"
      "v_cvt_pk_bf16_f32 v247, v22, v23\n"
      "v_cvt_pk_bf16_f32 v250, v62, v63\n"
      "v_cvt_pk_bf16_f32 v251, v64, v65\n"
      "v_cvt_pk_bf16_f32 v254, v28, v29\n"
      "v_cvt_pk_bf16_f32 v255, v30, v31\n"
      "v_cvt_pk_bf16_f32 v24, v24, v25\n"
      "v_cvt_pk_bf16_f32 v26, v26, v27\n"
      "v_accvgpr_write_b32 a246, v24\n"
      "v_accvgpr_write_b32 a247, v26\n"
      "s_mov_b32 m0, s18\n"
      "ds_read_b128 a[48:51], v2 offset:39840\n"
      "v_lshlrev_b32 v9, 7, v5\n"
      "v_mfma_f32_16x16x32_bf16 v[68:71], a[12:15], a[88:91], 0\n"
      "s_waitcnt lgkmcnt(4)\n"
      "ds_read_b128 a[52:55], v3 offset:39840\n"
      "v_min_u32 v9, Lconv_u0-Lconv_t0, v9\n"
      "v_mfma_f32_16x16x32_bf16 v[68:71], a[16:19], a[100:103], v[68:71]\n"
      "s_mul_i32 s97, s41, Lconv_t1-Lconv_t0\n"
      "ds_read_b128 a[56:59], v4 offset:39840\n"
      "v_add_u32 v9, Lconv_t0-Lconv_start, v9\n"
      "v_mfma_f32_16x16x32_bf16 v[68:71], a[20:23], a[112:115], v[68:71]\n"
      "s_waitcnt lgkmcnt(5)\n"
      "ds_read_b128 a[36:39], v2 offset:39904\n"
      "v_add_u32 v9, s97, v9\n"
      "v_mfma_f32_16x16x32_bf16 v[68:71], a[24:27], a[124:127], v[68:71]\n"
      "s_waitcnt lgkmcnt(5)\n"
      "ds_read_b128 a[40:43], v3 offset:39904\n"
      "v_mfma_f32_16x16x32_bf16 v[68:71], a[28:31], a[136:139], v[68:71]\n"
      "s_waitcnt lgkmcnt(4)\n"
      "ds_read_b128 a[44:47], v4 offset:39904\n"
      "v_mfma_f32_16x16x32_bf16 v[52:55], a[48:51], a[72:75], 0\n"
      "ds_read_b128 a[0:3], v6 offset:768\n"
      "v_mfma_f32_16x16x32_bf16 v[68:71], a[32:35], a[148:151], v[68:71]\n"
      "ds_read_b128 a[48:51], v2 offset:39968\n"
      "v_mfma_f32_16x16x32_bf16 v[72:75], a[12:15], a[92:95], 0\n"
      "s_waitcnt lgkmcnt(6)\n"
      "ds_read_b128 a[4:7], v6 offset:1024\n"
      "v_mfma_f32_16x16x32_bf16 v[56:59], a[52:55], a[76:79], 0\n"
      "ds_read_b128 a[8:11], v6 offset:1280\n"
      "s_nop 0\n"
      "v_mfma_f32_16x16x32_bf16 v[72:75], a[16:19], a[104:107], v[72:75]\n"
      "ds_read_b128 a[52:55], v3 offset:39968\n"
      "s_nop 0\n"
      "v_mfma_f32_16x16x32_bf16 v[72:75], a[20:23], a[116:119], v[72:75]\n"
      "s_waitcnt lgkmcnt(7)\n"
      "ds_read_b128 a[60:63], v6 offset:1536\n"
      "v_sub_f32_e32 v64, v52, v56\n"
      "v_mfma_f32_16x16x32_bf16 v[60:63], a[56:59], a[80:83], v[52:55]\n"
      "ds_read_b128 a[56:59], v4 offset:39968\n"
      "v_sub_f32_e32 v65, v53, v57\n"
      "v_mfma_f32_16x16x32_bf16 v[72:75], a[24:27], a[128:131], v[72:75]\n"
      "v_cvt_pk_bf16_f32 v64, v64, v65\n"
      "v_sub_f32_e32 v66, v54, v58\n"
      "v_mfma_f32_16x16x32_bf16 v[72:75], a[28:31], a[140:143], v[72:75]\n"
      "ds_write_addtid_b32 v64 offset:10752\n"
      "v_sub_f32_e32 v67, v55, v59\n"
      "v_mfma_f32_16x16x32_bf16 v[72:75], a[32:35], a[152:155], v[72:75]\n"
      "v_cvt_pk_bf16_f32 v65, v66, v67\n"
      "v_add_f32_e32 v56, v52, v56\n"
      "v_mfma_f32_16x16x32_bf16 v[36:39], a[36:39], a[72:75], 0\n"
      "ds_write_addtid_b32 v65 offset:47680\n"
      "v_add_f32_e32 v57, v53, v57\n"
      "v_mfma_f32_16x16x32_bf16 v[76:79], a[12:15], a[96:99], 0\n"
      "ds_read_b128 a[12:15], v6 offset:5376\n"
      "v_cvt_pk_bf16_f32 v56, v56, v57\n"
      "v_mfma_f32_16x16x32_bf16 v[76:79], a[16:19], a[108:111], v[76:79]\n"
      "s_waitcnt lgkmcnt(5)\n"
      "ds_read_b128 a[16:19], v6 offset:5632\n"
      "v_add_f32_e32 v58, v54, v58\n"
      "v_mfma_f32_16x16x32_bf16 v[40:43], a[40:43], a[76:79], 0\n"
      "ds_write_addtid_b32 v56 offset:9984\n"
      "v_add_f32_e32 v59, v55, v59\n"
      "v_mfma_f32_16x16x32_bf16 v[76:79], a[20:23], a[120:123], v[76:79]\n"
      "ds_read_b128 a[20:23], v6 offset:5888\n"
      "v_cvt_pk_bf16_f32 v57, v58, v59\n"
      "v_mfma_f32_16x16x32_bf16 v[76:79], a[24:27], a[132:135], v[76:79]\n"
      "ds_write_addtid_b32 v57 offset:46912\n"
      "v_cvt_pk_bf16_f32 v60, v60, v61\n"
      "v_mfma_f32_16x16x32_bf16 v[44:47], a[44:47], a[80:83], v[36:39]\n"
      "ds_write_addtid_b32 v60 offset:9216\n"
      "v_cvt_pk_bf16_f32 v61, v62, v63\n"
      "v_mfma_f32_16x16x32_bf16 v[76:79], a[28:31], a[144:147], v[76:79]\n"
      "ds_write_addtid_b32 v61 offset:46144\n"
      "v_sub_f32_e32 v48, v36, v40\n"
      "v_mfma_f32_16x16x32_bf16 v[76:79], a[32:35], a[156:159], v[76:79]\n"
      "ds_read_b128 a[36:39], v2 offset:0\n"
      "v_sub_f32_e32 v49, v37, v41\n"
      "v_mfma_f32_16x16x32_bf16 v[80:83], a[0:3], v[92:95], 0\n"
      "v_cvt_pk_bf16_f32 v48, v48, v49\n"
      "v_sub_f32_e32 v50, v38, v42\n"
      "v_mfma_f32_16x16x32_bf16 v[52:55], a[48:51], a[72:75], 0\n"
      "ds_write_addtid_b32 v48 offset:11008\n"
      "v_sub_f32_e32 v51, v39, v43\n"
      "v_mfma_f32_16x16x32_bf16 v[80:83], a[4:7], v[148:151], v[80:83]\n"
      "ds_read_b128 a[40:43], v3 offset:0\n"
      "v_cvt_pk_bf16_f32 v49, v50, v51\n"
      "v_mfma_f32_16x16x32_bf16 v[80:83], a[8:11], v[204:207], v[80:83]\n"
      "ds_write_addtid_b32 v49 offset:47936\n"
      "v_add_f32_e32 v40, v36, v40\n"
      "v_mfma_f32_16x16x32_bf16 v[56:59], a[52:55], a[76:79], 0\n"
      "s_waitcnt lgkmcnt(7)\n"
      "v_add_f32_e32 v41, v37, v41\n"
      "v_cvt_pk_bf16_f32 v40, v40, v41\n"
      "v_mfma_f32_16x16x32_bf16 v[80:83], a[12:15], v[112:115], v[80:83]\n"
      "ds_write_addtid_b32 v40 offset:10240\n"
      "v_add_f32_e32 v42, v38, v42\n"
      "v_mfma_f32_16x16x32_bf16 v[80:83], a[16:19], a[208:211], v[80:83]\n"
      "v_add_f32_e32 v43, v39, v43\n"
      "v_cvt_pk_bf16_f32 v41, v42, v43\n"
      "v_mfma_f32_16x16x32_bf16 v[60:63], a[56:59], a[80:83], v[52:55]\n"
      "ds_write_addtid_b32 v41 offset:47168\n"
      "v_cvt_pk_bf16_f32 v44, v44, v45\n"
      "v_mfma_f32_16x16x32_bf16 v[80:83], a[20:23], v[220:223], v[80:83]\n"
      "ds_write_addtid_b32 v44 offset:9472\n"
      "v_cvt_pk_bf16_f32 v45, v46, v47\n"
      "v_mfma_f32_16x16x32_bf16 v[84:87], a[0:3], v[100:103], 0\n"
      "ds_read_b128 a[44:47], v4 offset:0\n"
      "v_sub_f32_e32 v64, v52, v56\n"
      "v_mfma_f32_16x16x32_bf16 v[84:87], a[4:7], a[200:203], v[84:87]\n"
      "ds_write_addtid_b32 v45 offset:46400\n"
      "v_sub_f32_e32 v65, v53, v57\n"
      "v_mfma_f32_16x16x32_bf16 v[84:87], a[8:11], v[208:211], v[84:87]\n"
      "ds_read_b128 a[64:67], v6 offset:1792\n"
      "v_cvt_pk_bf16_f32 v64, v64, v65\n"
      "v_mfma_f32_16x16x32_bf16 v[84:87], a[12:15], a[204:207], v[84:87]\n"
      "ds_read_b128 a[68:71], v6 offset:2048\n"
      "v_sub_f32_e32 v66, v54, v58\n"
      "v_mfma_f32_16x16x32_bf16 v[84:87], a[16:19], v[172:175], v[84:87]\n"
      "ds_read_b128 a[24:27], v6 offset:6144\n"
      "v_sub_f32_e32 v67, v55, v59\n"
      "v_mfma_f32_16x16x32_bf16 v[84:87], a[20:23], v[228:231], v[84:87]\n"
      "ds_read_b128 a[48:51], v2 offset:64\n"
      "v_cvt_pk_bf16_f32 v65, v66, v67\n"
      "v_mfma_f32_16x16x32_bf16 v[24:27], a[0:3], a[196:199], 0\n"
      "ds_write_addtid_b32 v64 offset:11264\n"
      "v_add_f32_e32 v56, v52, v56\n"
      "v_mfma_f32_16x16x32_bf16 v[24:27], a[4:7], v[160:163], v[24:27]\n"
      "s_waitcnt lgkmcnt(11)\n"
      "ds_read_b128 a[28:31], v6 offset:6400\n"
      "v_add_f32_e32 v57, v53, v57\n"
      "v_mfma_f32_16x16x32_bf16 v[36:39], a[36:39], a[72:75], 0\n"
      "ds_write_addtid_b32 v65 offset:48192\n"
      "v_cvt_pk_bf16_f32 v56, v56, v57\n"
      "v_mfma_f32_16x16x32_bf16 v[24:27], a[8:11], v[216:219], v[24:27]\n"
      "ds_read_b128 a[32:35], v6 offset:6656\n"
      "v_add_f32_e32 v58, v54, v58\n"
      "v_mfma_f32_16x16x32_bf16 v[24:27], a[12:15], v[124:127], v[24:27]\n"
      "ds_write_addtid_b32 v56 offset:10496\n"
      "v_add_f32_e32 v59, v55, v59\n"
      "v_mfma_f32_16x16x32_bf16 v[40:43], a[40:43], a[76:79], 0\n"
      "ds_read_b128 a[52:55], v3 offset:64\n"
      "v_cvt_pk_bf16_f32 v57, v58, v59\n"
      "v_mfma_f32_16x16x32_bf16 v[24:27], a[16:19], v[180:183], v[24:27]\n"
      "ds_write_addtid_b32 v57 offset:47424\n"
      "v_cvt_pk_bf16_f32 v60, v60, v61\n"
      "v_mfma_f32_16x16x32_bf16 v[24:27], a[20:23], v[232:235], v[24:27]\n"
      "s_waitcnt lgkmcnt(7)\n"
      "ds_write_addtid_b32 v60 offset:9728\n"
      "v_cvt_pk_bf16_f32 v61, v62, v63\n"
      "v_mfma_f32_16x16x32_bf16 v[44:47], a[44:47], a[80:83], v[36:39]\n"
      "ds_write_addtid_b32 v61 offset:46656\n"
      "v_sub_f32_e32 v48, v36, v40\n"
      "v_mfma_f32_16x16x32_bf16 v[28:31], a[60:63], v[96:99], 0\n"
      "s_add_u32 m0, s46, 26560\n"
      "ds_read_b128 a[56:59], v4 offset:64\n"
      "v_sub_f32_e32 v49, v37, v41\n"
      "v_mfma_f32_16x16x32_bf16 v[28:31], a[64:67], v[152:155], v[28:31]\n"
      "s_add_u32 s51, s51, s12\n"
      "ds_read_b128 a[36:39], v2 offset:128\n"
      "v_cvt_pk_bf16_f32 v48, v48, v49\n"
      "v_mfma_f32_16x16x32_bf16 v[28:31], a[68:71], a[220:223], v[28:31]\n"
      "buffer_load_dwordx4 v16, s[32:35], s51 offen lds\n"
      "v_sub_f32_e32 v50, v38, v42\n"
      "v_mfma_f32_16x16x32_bf16 v[28:31], a[24:27], v[116:119], v[28:31]\n"
      "s_waitcnt lgkmcnt(0)\n"
      "s_cbranch_vccnz Lconv_h13\n"
      "s_barrier\n"
      "Lconv_h13:\n"
      "v_sub_f32_e32 v51, v39, v43\n"
      "v_mfma_f32_16x16x32_bf16 v[52:55], a[48:51], a[72:75], 0\n"
      "ds_read_b128 a[40:43], v3 offset:128\n"
      "v_cvt_pk_bf16_f32 v49, v50, v51\n"
      "v_mfma_f32_16x16x32_bf16 v[28:31], a[28:31], v[168:171], v[28:31]\n"
      "buffer_load_dwordx4 v17, s[32:35], s51 offen offset:1024 lds\n"
      "v_add_f32_e32 v40, v36, v40\n"
      "v_mfma_f32_16x16x32_bf16 v[28:31], a[32:35], v[224:227], v[28:31]\n"
      "ds_read_b128 a[44:47], v4 offset:128\n"
      "v_add_f32_e32 v41, v37, v41\n"
      "v_mfma_f32_16x16x32_bf16 v[56:59], a[52:55], a[76:79], 0\n"
      "s_cbranch_vccz Lconv_h14\n"
      "s_barrier\n"
      "Lconv_h14:\n"
      "v_cvt_pk_bf16_f32 v40, v40, v41\n"
      "v_mfma_f32_16x16x32_bf16 v[20:23], a[60:63], v[104:107], 0\n"
      "buffer_load_dwordx4 v18, s[32:35], s51 offen offset:2048 lds\n"
      "v_add_f32_e32 v42, v38, v42\n"
      "v_mfma_f32_16x16x32_bf16 v[20:23], a[64:67], v[156:159], v[20:23]\n"
      "s_mov_b32 m0, s18\n"
      "ds_read_b128 a[0:3], v7 offset:0\n"
      "v_add_f32_e32 v43, v39, v43\n"
      "v_mfma_f32_16x16x32_bf16 v[60:63], a[56:59], a[80:83], v[52:55]\n"
      "s_mov_b32 vcc_lo, 0\n"
      "ds_write_addtid_b32 v48 offset:15360\n"
      "ds_write_addtid_b32 v49 offset:52288\n"
      "v_mfma_f32_16x16x32_bf16 v[20:23], a[68:71], v[212:215], v[20:23]\n"
      "s_mov_b32 vcc_hi, 0x2000000\n"
      "ds_write_addtid_b32 v40 offset:14592\n"
      "v_cvt_pk_bf16_f32 v41, v42, v43\n"
      "v_mfma_f32_16x16x32_bf16 v[20:23], a[24:27], v[120:123], v[20:23]\n"
      "ds_write_addtid_b32 v41 offset:51520\n"
      "v_cvt_pk_bf16_f32 v44, v44, v45\n"
      "v_mfma_f32_16x16x32_bf16 v[20:23], a[28:31], v[176:179], v[20:23]\n"
      "ds_write_addtid_b32 v44 offset:13824\n"
      "v_cvt_pk_bf16_f32 v45, v46, v47\n"
      "v_mfma_f32_16x16x32_bf16 v[36:39], a[36:39], a[72:75], 0\n"
      "ds_read_b128 a[4:7], v7 offset:256\n"
      "v_sub_f32_e32 v64, v52, v56\n"
      "v_mfma_f32_16x16x32_bf16 v[20:23], a[32:35], a[232:235], v[20:23]\n"
      "ds_write_addtid_b32 v45 offset:50752\n"
      "v_sub_f32_e32 v65, v53, v57\n"
      "v_mfma_f32_16x16x32_bf16 v[32:35], a[60:63], v[108:111], 0\n"
      "s_waitcnt lgkmcnt(8)\n"
      "ds_read_b128 a[8:11], v7 offset:512\n"
      "v_cvt_pk_bf16_f32 v64, v64, v65\n"
      "v_mfma_f32_16x16x32_bf16 v[40:43], a[40:43], a[76:79], 0\n"
      "ds_write_addtid_b32 v64 offset:15616\n"
      "v_sub_f32_e32 v66, v54, v58\n"
      "v_mfma_f32_16x16x32_bf16 v[32:35], a[64:67], v[164:167], v[32:35]\n"
      "ds_read_b128 a[12:15], v7 offset:768\n"
      "v_sub_f32_e32 v67, v55, v59\n"
      "v_mfma_f32_16x16x32_bf16 v[32:35], a[68:71], a[224:227], v[32:35]\n"
      "v_cvt_pk_bf16_f32 v65, v66, v67\n"
      "v_add_f32_e32 v56, v52, v56\n"
      "v_mfma_f32_16x16x32_bf16 v[44:47], a[44:47], a[80:83], v[36:39]\n"
      "ds_write_addtid_b32 v65 offset:52544\n"
      "v_add_f32_e32 v57, v53, v57\n"
      "v_mfma_f32_16x16x32_bf16 v[32:35], a[24:27], v[128:131], v[32:35]\n"
      "ds_read_b128 a[16:19], v7 offset:1024\n"
      "v_cvt_pk_bf16_f32 v56, v56, v57\n"
      "v_mfma_f32_16x16x32_bf16 v[32:35], a[28:31], a[228:231], v[32:35]\n"
      "ds_write_addtid_b32 v56 offset:14848\n"
      "v_add_f32_e32 v58, v54, v58\n"
      "v_mfma_f32_16x16x32_bf16 v[32:35], a[32:35], v[236:239], v[32:35]\n"
      "s_waitcnt lgkmcnt(7)\n"
      "ds_read_b128 a[20:23], v7 offset:1280\n"
      "v_add_f32_e32 v59, v55, v59\n"
      "v_mfma_f32_16x16x32_bf16 v[68:71], a[0:3], a[160:163], v[68:71]\n"
      "v_cvt_pk_bf16_f32 v57, v58, v59\n"
      "v_cvt_pk_bf16_f32 v60, v60, v61\n"
      "v_mfma_f32_16x16x32_bf16 v[68:71], a[4:7], a[172:175], v[68:71]\n"
      "s_waitcnt lgkmcnt(6)\n"
      "ds_write_addtid_b32 v57 offset:51776\n"
      "v_cvt_pk_bf16_f32 v61, v62, v63\n"
      "v_mfma_f32_16x16x32_bf16 v[68:71], a[8:11], a[184:187], v[68:71]\n"
      "ds_write_addtid_b32 v60 offset:14080\n"
      "ds_write_addtid_b32 v61 offset:51008\n"
      "v_mfma_f32_16x16x32_bf16 v[72:75], a[0:3], a[164:167], v[72:75]\n"
      "v_sub_f32_e32 v48, v36, v40\n"
      "v_sub_f32_e32 v49, v37, v41\n"
      "v_mfma_f32_16x16x32_bf16 v[72:75], a[4:7], a[176:179], v[72:75]\n"
      "v_cvt_pk_bf16_f32 v48, v48, v49\n"
      "v_sub_f32_e32 v50, v38, v42\n"
      "v_mfma_f32_16x16x32_bf16 v[72:75], a[8:11], a[188:191], v[72:75]\n"
      "ds_write_addtid_b32 v48 offset:15872\n"
      "v_sub_f32_e32 v51, v39, v43\n"
      "v_mfma_f32_16x16x32_bf16 v[76:79], a[0:3], a[168:171], v[76:79]\n"
      "ds_read_b128 a[0:3], v7 offset:1536\n"
      "v_cvt_pk_bf16_f32 v49, v50, v51\n"
      "v_mfma_f32_16x16x32_bf16 v[76:79], a[4:7], a[180:183], v[76:79]\n"
      "ds_write_addtid_b32 v49 offset:52800\n"
      "v_add_f32_e32 v40, v36, v40\n"
      "v_mfma_f32_16x16x32_bf16 v[76:79], a[8:11], a[192:195], v[76:79]\n"
      "s_waitcnt lgkmcnt(6)\n"
      "ds_read_b128 a[4:7], v7 offset:1792\n"
      "v_add_f32_e32 v41, v37, v41\n"
      "v_mfma_f32_16x16x32_bf16 v[80:83], a[12:15], a[212:215], v[80:83]\n"
      "ds_read_b128 a[8:11], v7 offset:2048\n"
      "v_cvt_pk_bf16_f32 v40, v40, v41\n"
      "v_mfma_f32_16x16x32_bf16 v[80:83], a[16:19], v[184:187], v[80:83]\n"
      "ds_write_addtid_b32 v40 offset:15104\n"
      "v_add_f32_e32 v42, v38, v42\n"
      "v_mfma_f32_16x16x32_bf16 v[80:83], a[20:23], v[240:243], v[80:83]\n"
      "v_add_f32_e32 v43, v39, v43\n"
      "v_cvt_pk_bf16_f32 v41, v42, v43\n"
      "v_mfma_f32_16x16x32_bf16 v[84:87], a[12:15], v[136:139], v[84:87]\n"
      "ds_write_addtid_b32 v41 offset:52032\n"
      "v_cvt_pk_bf16_f32 v44, v44, v45\n"
      "v_mfma_f32_16x16x32_bf16 v[84:87], a[16:19], v[192:195], v[84:87]\n"
      "ds_write_addtid_b32 v44 offset:14336\n"
      "v_cvt_pk_bf16_f32 v45, v46, v47\n"
      "v_mfma_f32_16x16x32_bf16 v[84:87], a[20:23], v[244:247], v[84:87]\n"
      "ds_write_addtid_b32 v45 offset:51264\n"
      "v_cvt_pk_bf16_f32 v68, v68, v69\n"
      "v_mfma_f32_16x16x32_bf16 v[24:27], a[12:15], a[216:219], v[24:27]\n"
      "s_mov_b32 m0, s45\n"
      "v_cvt_pk_bf16_f32 v69, v70, v71\n"
      "v_cvt_pk_bf16_f32 v72, v72, v73\n"
      "v_mfma_f32_16x16x32_bf16 v[24:27], a[16:19], v[196:199], v[24:27]\n"
      "ds_write_addtid_b32 v68 offset:57360\n"
      "ds_write_addtid_b32 v69 offset:57616\n"
      "v_mfma_f32_16x16x32_bf16 v[24:27], a[20:23], v[252:255], v[24:27]\n"
      "s_waitcnt lgkmcnt(6)\n"
      "ds_write_addtid_b32 v72 offset:45064\n"
      "v_cvt_pk_bf16_f32 v73, v74, v75\n"
      "v_mfma_f32_16x16x32_bf16 v[28:31], a[0:3], v[132:135], v[28:31]\n"
      "ds_write_addtid_b32 v73 offset:45320\n"
      "v_cvt_pk_bf16_f32 v76, v76, v77\n"
      "v_mfma_f32_16x16x32_bf16 v[28:31], a[4:7], v[188:191], v[28:31]\n"
      "ds_write_addtid_b32 v76 offset:32768\n"
      "v_cvt_pk_bf16_f32 v77, v78, v79\n"
      "v_mfma_f32_16x16x32_bf16 v[28:31], a[8:11], a[240:243], v[28:31]\n"
      "ds_write_addtid_b32 v77 offset:33024\n"
      "v_cvt_pk_bf16_f32 v80, v80, v81\n"
      "v_mfma_f32_16x16x32_bf16 v[20:23], a[0:3], v[140:143], v[20:23]\n"
      "ds_write_addtid_b32 v80 offset:58384\n"
      "v_cvt_pk_bf16_f32 v81, v82, v83\n"
      "v_mfma_f32_16x16x32_bf16 v[20:23], a[4:7], a[236:239], v[20:23]\n"
      "ds_write_addtid_b32 v81 offset:58640\n"
      "v_cvt_pk_bf16_f32 v84, v84, v85\n"
      "v_mfma_f32_16x16x32_bf16 v[20:23], a[8:11], v[248:251], v[20:23]\n"
      "ds_write_addtid_b32 v84 offset:46088\n"
      "v_cvt_pk_bf16_f32 v85, v86, v87\n"
      "v_mfma_f32_16x16x32_bf16 v[32:35], a[0:3], v[144:147], v[32:35]\n"
      "ds_write_addtid_b32 v85 offset:46344\n"
      "v_cvt_pk_bf16_f32 v24, v24, v25\n"
      "v_mfma_f32_16x16x32_bf16 v[32:35], a[4:7], v[200:203], v[32:35]\n"
      "ds_write_addtid_b32 v24 offset:33792\n"
      "v_cvt_pk_bf16_f32 v25, v26, v27\n"
      "v_mfma_f32_16x16x32_bf16 v[32:35], a[8:11], a[244:247], v[32:35]\n"
      "ds_write_addtid_b32 v25 offset:34048\n"
      "v_cvt_pk_bf16_f32 v28, v28, v29\n"
      "v_cvt_pk_bf16_f32 v29, v30, v31\n"
      "ds_write_addtid_b32 v28 offset:59408\n"
      "ds_write_addtid_b32 v29 offset:59664\n"
      "v_cvt_pk_bf16_f32 v20, v20, v21\n"
      "v_cvt_pk_bf16_f32 v21, v22, v23\n"
      "ds_write_addtid_b32 v20 offset:47112\n"
      "ds_write_addtid_b32 v21 offset:47368\n"
      "s_add_u32 m0, s46, 39840\n"
      "s_add_u32 s51, s51, s12\n"
      "buffer_load_dwordx4 v16, s[32:35], s51 offen lds\n"
      "buffer_load_dwordx4 v17, s[32:35], s51 offen offset:1024 lds\n"
      "buffer_load_dwordx4 v18, s[32:35], s51 offen offset:2048 lds\n"
      "s_mov_b32 m0, s45\n"
      "s_waitcnt vmcnt(6)\n"
      "s_waitcnt lgkmcnt(0)\n"
      "s_cmp_eq_u32 s41, 1\n"
      "s_cbranch_scc1 Lconv_s1\n"
      "Lconv_s0:\n"
      "s_branch Lconv_loop0\n"
      "Lconv_s1:\n"
      "v_cvt_pk_bf16_f32 v32, v32, v33\n"
      "s_branch Lconv_loop1\n"
      ".p2align 12\n"
      "Lconv_pa0:\n"
      "s_cbranch_execz Lconv_r0\n"
      "s_nop 0\n"
      "Lconv_loop0:\n"
      "v_mfma_f32_16x16x32_bf16 v[20:23], a[24:27], v[96:99], 0\n"
      "s_add_u32 s51, s51, s12\n"
      "s_nop 1\n"
      "v_mfma_f32_16x16x32_bf16 v[20:23], a[28:31], v[152:155], v[20:23]\n"
      "s_sub_u32 s58, s93, s58\n"
      "v_cvt_pk_bf16_f32 v32, v32, v33\n"
      "v_cvt_pk_bf16_f32 v33, v34, v35\n"
      "v_mfma_f32_16x16x32_bf16 v[20:23], a[32:35], a[220:223], v[20:23]\n"
      "s_sub_u32 s59, s94, s59\n"
      "ds_write_addtid_b32 v32 offset:34816\n"
      "v_mfma_f32_16x16x32_bf16 v[20:23], a[0:3], v[116:119], v[20:23]\n"
      "s_sub_u32 s47, s51, s48\n"
      "ds_write_addtid_b32 v33 offset:35072\n"
      "v_mfma_f32_16x16x32_bf16 v[20:23], a[4:7], v[168:171], v[20:23]\n"
      "s_cmp_ge_u32 vcc_hi, 0x80000000\n"
      "v_mfma_f32_16x16x32_bf16 v[20:23], a[8:11], v[224:227], v[20:23]\n"
      "s_cselect_b32 s53, s47, s51\n"
      "v_mfma_f32_16x16x32_bf16 v[24:27], a[24:27], v[104:107], 0\n"
      "s_waitcnt vmcnt(8) lgkmcnt(0)\n"
      "s_barrier\n"
      "v_mfma_f32_16x16x32_bf16 v[24:27], a[28:31], v[156:159], v[24:27]\n"
      "s_mov_b32 m0, s58\n"
      "ds_read_b128 a[12:15], v7 offset:6144\n"
      "v_mfma_f32_16x16x32_bf16 v[24:27], a[32:35], v[212:215], v[24:27]\n"
      "ds_read_b128 a[16:19], v7 offset:6400\n"
      "v_mfma_f32_16x16x32_bf16 v[24:27], a[0:3], v[120:123], v[24:27]\n"
      "ds_read_b128 a[20:23], v7 offset:6656\n"
      "v_mfma_f32_16x16x32_bf16 v[24:27], a[4:7], v[176:179], v[24:27]\n"
      "ds_read_b128 a[48:51], v2 offset:13280\n"
      "v_mfma_f32_16x16x32_bf16 v[24:27], a[8:11], a[232:235], v[24:27]\n"
      "ds_read_b128 a[52:55], v3 offset:13280\n"
      "v_mfma_f32_16x16x32_bf16 v[68:71], a[84:87], a[60:63], 0\n"
      "ds_read_b128 a[56:59], v4 offset:13280\n"
      "s_nop 0\n"
      "v_mfma_f32_16x16x32_bf16 v[28:31], a[24:27], v[108:111], 0\n"
      "ds_read2_b64 a[64:67], v10 offset0:0 offset1:64\n"
      "s_nop 0\n"
      "v_mfma_f32_16x16x32_bf16 v[28:31], a[28:31], v[164:167], v[28:31]\n"
      "buffer_load_dwordx4 v16, s[32:35], s53 offen lds\n"
      "v_mfma_f32_16x16x32_bf16 v[28:31], a[32:35], a[224:227], v[28:31]\n"
      "s_mov_b32 m0, s45\n"
      "ds_read2_b64 a[68:71], v10 offset0:1 offset1:65\n"
      "v_cvt_pk_bf16_f32 v85, v68, v69\n"
      "v_mfma_f32_16x16x32_bf16 v[28:31], a[0:3], v[128:131], v[28:31]\n"
      "ds_read_b128 a[24:27], v6 offset:5376\n"
      "v_cvt_pk_bf16_f32 v91, v70, v71\n"
      "v_mfma_f32_16x16x32_bf16 v[28:31], a[4:7], a[228:231], v[28:31]\n"
      "ds_read_b128 a[28:31], v6 offset:5632\n"
      "v_mfma_f32_16x16x32_bf16 v[28:31], a[8:11], v[236:239], v[28:31]\n"
      "s_waitcnt lgkmcnt(6)\n"
      "ds_read_b128 a[32:35], v6 offset:5888\n"
      "v_mfma_f32_16x16x32_bf16 v[20:23], a[12:15], v[132:135], v[20:23]\n"
      "buffer_store_dwordx2 v[84:85], v15, s[36:39], s52 offen sc1\n"
      "v_mfma_f32_16x16x32_bf16 v[20:23], a[16:19], v[188:191], v[20:23]\n"
      "ds_read_b128 a[0:3], v7 offset:768\n"
      "v_mfma_f32_16x16x32_bf16 v[20:23], a[20:23], a[240:243], v[20:23]\n"
      "ds_read_b128 a[4:7], v7 offset:1024\n"
      "s_nop 0\n"
      "v_mfma_f32_16x16x32_bf16 v[52:55], a[48:51], a[72:75], 0\n"
      "s_waitcnt lgkmcnt(6)\n"
      "ds_read_b128 a[8:11], v7 offset:1280\n"
      "s_nop 0\n"
      "v_mfma_f32_16x16x32_bf16 v[56:59], a[52:55], a[76:79], 0\n"
      "ds_read_b128 a[36:39], v2 offset:13344\n"
      "v_cvt_pk_bf16_f32 v20, v20, v21\n"
      "v_mfma_f32_16x16x32_bf16 v[60:63], a[56:59], a[80:83], v[52:55]\n"
      "buffer_store_dwordx2 v[90:91], v15, s[36:39], s52 offen offset:1536 sc1\n"
      "v_cvt_pk_bf16_f32 v21, v22, v23\n"
      "v_mfma_f32_16x16x32_bf16 v[24:27], a[12:15], v[140:143], v[24:27]\n"
      "s_mov_b32 s52, s13\n"
      "ds_write_addtid_b32 v20 offset:62480\n"
      "v_sub_f32_e32 v64, v52, v56\n"
      "v_mfma_f32_16x16x32_bf16 v[24:27], a[16:19], a[236:239], v[24:27]\n"
      "s_add_u32 s13, s13, s12\n"
      "ds_read_b128 a[40:43], v3 offset:13344\n"
      "v_sub_f32_e32 v65, v53, v57\n"
      "v_mfma_f32_16x16x32_bf16 v[24:27], a[20:23], v[248:251], v[24:27]\n"
      "s_sub_u32 s96, s53, s95\n"
      "ds_write_addtid_b32 v21 offset:62736\n"
      "v_cvt_pk_bf16_f32 v64, v64, v65\n"
      "v_mfma_f32_16x16x32_bf16 v[72:75], a[84:87], a[64:67], 0\n"
      "ds_read_b128 a[44:47], v4 offset:13344\n"
      "v_sub_f32_e32 v66, v54, v58\n"
      "v_mfma_f32_16x16x32_bf16 v[28:31], a[12:15], v[144:147], v[28:31]\n"
      "ds_read_b128 a[12:15], v7 offset:5376\n"
      "v_cvt_pk_bf16_f32 v24, v24, v25\n"
      "v_mfma_f32_16x16x32_bf16 v[28:31], a[16:19], v[200:203], v[28:31]\n"
      "ds_read_b128 a[16:19], v7 offset:5632\n"
      "v_cvt_pk_bf16_f32 v25, v26, v27\n"
      "v_mfma_f32_16x16x32_bf16 v[28:31], a[20:23], a[244:247], v[28:31]\n"
      "s_waitcnt lgkmcnt(6)\n"
      "ds_read_b128 a[20:23], v7 offset:5888\n"
      "v_sub_f32_e32 v67, v55, v59\n"
      "v_mfma_f32_16x16x32_bf16 v[76:79], a[84:87], a[68:71], 0\n"
      "ds_read2_b64 a[60:63], v10 offset0:2 offset1:66\n"
      "v_cvt_pk_bf16_f32 v65, v66, v67\n"
      "v_mfma_f32_16x16x32_bf16 v[32:35], a[24:27], v[92:95], 0\n"
      "ds_write_addtid_b32 v24 offset:50184\n"
      "ds_write_addtid_b32 v25 offset:50440\n"
      "v_mfma_f32_16x16x32_bf16 v[32:35], a[28:31], v[148:151], v[32:35]\n"
      "s_mov_b32 m0, s18\n"
      "v_add_f32_e32 v56, v52, v56\n"
      "v_add_f32_e32 v57, v53, v57\n"
      "v_mfma_f32_16x16x32_bf16 v[32:35], a[32:35], v[204:207], v[32:35]\n"
      "ds_write_addtid_b32 v64 offset:1536\n"
      "v_add_f32_e32 v58, v54, v58\n"
      "v_mfma_f32_16x16x32_bf16 v[32:35], a[0:3], v[112:115], v[32:35]\n"
      "ds_write_addtid_b32 v65 offset:38464\n"
      "v_add_f32_e32 v59, v55, v59\n"
      "v_mfma_f32_16x16x32_bf16 v[32:35], a[4:7], a[208:211], v[32:35]\n"
      "v_cvt_pk_bf16_f32 v56, v56, v57\n"
      "v_cvt_pk_bf16_f32 v57, v58, v59\n"
      "v_mfma_f32_16x16x32_bf16 v[32:35], a[8:11], v[220:223], v[32:35]\n"
      "s_waitcnt lgkmcnt(6)\n"
      "ds_write_addtid_b32 v56 offset:768\n"
      "v_cvt_pk_bf16_f32 v60, v60, v61\n"
      "v_mfma_f32_16x16x32_bf16 v[32:35], a[12:15], a[212:215], v[32:35]\n"
      "ds_write_addtid_b32 v57 offset:37696\n"
      "v_cvt_pk_bf16_f32 v61, v62, v63\n"
      "v_mfma_f32_16x16x32_bf16 v[32:35], a[16:19], v[184:187], v[32:35]\n"
      "s_waitcnt lgkmcnt(6)\n"
      "ds_write_addtid_b32 v60 offset:0\n"
      "ds_write_addtid_b32 v61 offset:36928\n"
      "v_mfma_f32_16x16x32_bf16 v[32:35], a[20:23], v[240:243], v[32:35]\n"
      "s_mov_b32 m0, s45\n"
      "ds_read_b128 a[48:51], v2 offset:13408\n"
      "v_cvt_pk_bf16_f32 v28, v28, v29\n"
      "v_mfma_f32_16x16x32_bf16 v[20:23], a[24:27], v[100:103], 0\n"
      "ds_write_addtid_b32 v28 offset:37888\n"
      "v_cvt_pk_bf16_f32 v29, v30, v31\n"
      "v_mfma_f32_16x16x32_bf16 v[20:23], a[28:31], a[200:203], v[20:23]\n"
      "ds_write_addtid_b32 v29 offset:38144\n"
      "v_cvt_pk_bf16_f32 v32, v32, v33\n"
      "v_mfma_f32_16x16x32_bf16 v[20:23], a[32:35], v[208:211], v[20:23]\n"
      "s_mov_b32 m0, s58\n"
      "ds_read_b128 a[56:59], v4 offset:13408\n"
      "v_cvt_pk_bf16_f32 v33, v34, v35\n"
      "v_mfma_f32_16x16x32_bf16 v[20:23], a[0:3], a[204:207], v[20:23]\n"
      "buffer_load_dwordx4 v17, s[32:35], s53 offen offset:1024 lds\n"
      "v_cvt_pk_bf16_f32 v80, v72, v73\n"
      "v_mfma_f32_16x16x32_bf16 v[20:23], a[4:7], v[172:175], v[20:23]\n"
      "s_mov_b32 m0, s45\n"
      "ds_read2_b64 a[64:67], v10 offset0:3 offset1:67\n"
      "v_cvt_pk_bf16_f32 v81, v76, v77\n"
      "v_mfma_f32_16x16x32_bf16 v[20:23], a[8:11], v[228:231], v[20:23]\n"
      "ds_write_addtid_b32 v32 offset:61456\n"
      "v_cvt_pk_bf16_f32 v86, v74, v75\n"
      "v_mfma_f32_16x16x32_bf16 v[20:23], a[12:15], v[136:139], v[20:23]\n"
      "ds_write_addtid_b32 v33 offset:61712\n"
      "v_cvt_pk_bf16_f32 v87, v78, v79\n"
      "v_mfma_f32_16x16x32_bf16 v[20:23], a[16:19], v[192:195], v[20:23]\n"
      "ds_read_b128 a[52:55], v3 offset:13408\n"
      "v_add_u32_e64 v2, s91, v2\n"
      "v_mfma_f32_16x16x32_bf16 v[20:23], a[20:23], v[244:247], v[20:23]\n"
      "ds_read2_b64 a[68:71], v12 offset0:0 offset1:64\n"
      "v_add_u32_e64 v3, s91, v3\n"
      "v_mfma_f32_16x16x32_bf16 v[36:39], a[36:39], a[72:75], 0\n"
      "v_add_u32_e32 v4, s91, v4\n"
      "s_nop 0\n"
      "v_mfma_f32_16x16x32_bf16 v[40:43], a[40:43], a[76:79], 0\n"
      "s_sub_u32 s91, 0, s91\n"
      "v_mfma_f32_16x16x32_bf16 v[44:47], a[44:47], a[80:83], v[36:39]\n"
      "v_cvt_pk_bf16_f32 v20, v20, v21\n"
      "v_cvt_pk_bf16_f32 v21, v22, v23\n"
      "v_mfma_f32_16x16x32_bf16 v[68:71], a[84:87], a[60:63], 0\n"
      "ds_write_addtid_b32 v20 offset:49160\n"
      "s_nop 0\n"
      "v_mfma_f32_16x16x32_bf16 v[24:27], a[24:27], a[196:199], 0\n"
      "ds_read_b128 a[24:27], v7 offset:4608\n"
      "v_sub_f32_e32 v48, v36, v40\n"
      "v_mfma_f32_16x16x32_bf16 v[24:27], a[28:31], v[160:163], v[24:27]\n"
      "ds_read_b128 a[28:31], v7 offset:4864\n"
      "v_sub_f32_e32 v49, v37, v41\n"
      "v_mfma_f32_16x16x32_bf16 v[24:27], a[32:35], v[216:219], v[24:27]\n"
      "ds_read_b128 a[32:35], v7 offset:5120\n"
      "v_cvt_pk_bf16_f32 v48, v48, v49\n"
      "v_mfma_f32_16x16x32_bf16 v[24:27], a[0:3], v[124:127], v[24:27]\n"
      "ds_read_b128 a[0:3], v6 offset:4608\n"
      "v_sub_f32_e32 v50, v38, v42\n"
      "v_mfma_f32_16x16x32_bf16 v[24:27], a[4:7], v[180:183], v[24:27]\n"
      "ds_read_b128 a[4:7], v6 offset:4864\n"
      "v_sub_f32_e32 v51, v39, v43\n"
      "v_mfma_f32_16x16x32_bf16 v[24:27], a[8:11], v[232:235], v[24:27]\n"
      "ds_read_b128 a[8:11], v6 offset:5120\n"
      "v_cvt_pk_bf16_f32 v49, v50, v51\n"
      "v_mfma_f32_16x16x32_bf16 v[24:27], a[12:15], a[216:219], v[24:27]\n"
      "ds_read_b128 a[12:15], v7 offset:0\n"
      "v_add_f32_e32 v40, v36, v40\n"
      "v_mfma_f32_16x16x32_bf16 v[24:27], a[16:19], v[196:199], v[24:27]\n"
      "ds_read_b128 a[16:19], v7 offset:256\n"
      "v_add_f32_e32 v41, v37, v41\n"
      "v_mfma_f32_16x16x32_bf16 v[24:27], a[20:23], v[252:255], v[24:27]\n"
      "s_waitcnt lgkmcnt(7)\n"
      "ds_read_b128 a[20:23], v7 offset:512\n"
      "v_cvt_pk_bf16_f32 v40, v40, v41\n"
      "v_mfma_f32_16x16x32_bf16 v[52:55], a[48:51], a[72:75], 0\n"
      "ds_write_addtid_b32 v21 offset:49416\n"
      "v_add_f32_e32 v42, v38, v42\n"
      "v_mfma_f32_16x16x32_bf16 v[60:63], a[56:59], a[80:83], v[52:55]\n"
      "s_mov_b32 m0, s58\n"
      "v_cvt_pk_bf16_f32 v24, v24, v25\n"
      "v_cvt_pk_bf16_f32 v25, v26, v27\n"
      "v_mfma_f32_16x16x32_bf16 v[72:75], a[84:87], a[64:67], 0\n"
      "buffer_load_dwordx4 v18, s[32:35], s53 offen offset:2048 lds\n"
      "v_add_f32_e32 v43, v39, v43\n"
      "v_mfma_f32_16x16x32_bf16 v[28:31], a[24:27], a[160:163], 0\n"
      "s_waitcnt lgkmcnt(5)\n"
      "v_cvt_pk_bf16_f32 v41, v42, v43\n"
      "v_cvt_pk_bf16_f32 v44, v44, v45\n"
      "v_mfma_f32_16x16x32_bf16 v[28:31], a[28:31], a[172:175], v[28:31]\n"
      "s_mov_b32 m0, s45\n"
      "v_cvt_pk_bf16_f32 v82, v68, v69\n"
      "v_cvt_pk_bf16_f32 v83, v72, v73\n"
      "v_mfma_f32_16x16x32_bf16 v[28:31], a[32:35], a[184:187], v[28:31]\n"
      "ds_write_addtid_b32 v24 offset:36864\n"
      "ds_write_addtid_b32 v25 offset:37120\n"
      "v_mfma_f32_16x16x32_bf16 v[28:31], a[0:3], a[88:91], v[28:31]\n"
      "s_mov_b32 m0, s18\n"
      "v_cvt_pk_bf16_f32 v45, v46, v47\n"
      "v_cvt_pk_bf16_f32 v88, v70, v71\n"
      "v_mfma_f32_16x16x32_bf16 v[28:31], a[4:7], a[100:103], v[28:31]\n"
      "s_waitcnt lgkmcnt(3)\n"
      "ds_write_addtid_b32 v48 offset:1792\n"
      "ds_write_addtid_b32 v49 offset:38720\n"
      "v_mfma_f32_16x16x32_bf16 v[28:31], a[8:11], a[112:115], v[28:31]\n"
      "ds_write_addtid_b32 v40 offset:1024\n"
      "ds_write_addtid_b32 v41 offset:37952\n"
      "v_mfma_f32_16x16x32_bf16 v[28:31], a[12:15], a[124:127], v[28:31]\n"
      "ds_write_addtid_b32 v44 offset:256\n"
      "ds_write_addtid_b32 v45 offset:37184\n"
      "v_mfma_f32_16x16x32_bf16 v[28:31], a[16:19], a[136:139], v[28:31]\n"
      "buffer_store_dwordx4 v[80:83], v14, s[36:39], s52 offen sc1\n"
      "v_cvt_pk_bf16_f32 v89, v74, v75\n"
      "v_mfma_f32_16x16x32_bf16 v[28:31], a[20:23], a[148:151], v[28:31]\n"
      "s_mov_b32 m0, s45\n"
      "ds_read2_b64 a[60:63], v12 offset0:1 offset1:65\n"
      "v_cvt_pk_bf16_f32 v60, v60, v61\n"
      "v_mfma_f32_16x16x32_bf16 v[56:59], a[52:55], a[76:79], 0\n"
      "v_cvt_pk_bf16_f32 v61, v62, v63\n"
      "v_swap_b32 v6, v7\n"
      "v_mfma_f32_16x16x32_bf16 v[20:23], a[24:27], a[164:167], 0\n"
      "v_mfma_f32_16x16x32_bf16 v[20:23], a[28:31], a[176:179], v[20:23]\n"
      "v_cvt_pk_bf16_f32 v28, v28, v29\n"
      "v_cvt_pk_bf16_f32 v29, v30, v31\n"
      "v_mfma_f32_16x16x32_bf16 v[20:23], a[32:35], a[188:191], v[20:23]\n"
      "buffer_store_dwordx4 v[86:89], v14, s[36:39], s52 offen offset:1536 sc1\n"
      "v_sub_f32_e32 v64, v52, v56\n"
      "v_mfma_f32_16x16x32_bf16 v[20:23], a[0:3], a[92:95], v[20:23]\n"
      "ds_write_addtid_b32 v28 offset:60432\n"
      "ds_write_addtid_b32 v29 offset:60688\n"
      "v_mfma_f32_16x16x32_bf16 v[20:23], a[4:7], a[104:107], v[20:23]\n"
      "s_mov_b32 m0, s18\n"
      "v_sub_f32_e32 v65, v53, v57\n"
      "v_cvt_pk_bf16_f32 v64, v64, v65\n"
      "v_mfma_f32_16x16x32_bf16 v[20:23], a[8:11], a[116:119], v[20:23]\n"
      "ds_write_addtid_b32 v64 offset:2048\n"
      "v_sub_f32_e32 v66, v54, v58\n"
      "v_mfma_f32_16x16x32_bf16 v[20:23], a[12:15], a[128:131], v[20:23]\n"
      "v_sub_f32_e32 v67, v55, v59\n"
      "v_cvt_pk_bf16_f32 v65, v66, v67\n"
      "v_mfma_f32_16x16x32_bf16 v[20:23], a[16:19], a[140:143], v[20:23]\n"
      "ds_write_addtid_b32 v65 offset:38976\n"
      "v_add_f32_e32 v56, v52, v56\n"
      "v_mfma_f32_16x16x32_bf16 v[20:23], a[20:23], a[152:155], v[20:23]\n"
      "v_add_f32_e32 v57, v53, v57\n"
      "v_cvt_pk_bf16_f32 v56, v56, v57\n"
      "v_mfma_f32_16x16x32_bf16 v[76:79], a[84:87], a[68:71], 0\n"
      "ds_write_addtid_b32 v56 offset:1280\n"
      "v_add_f32_e32 v58, v54, v58\n"
      "v_mfma_f32_16x16x32_bf16 v[32:35], a[24:27], a[168:171], 0\n"
      "ds_write_addtid_b32 v60 offset:512\n"
      "v_add_f32_e32 v59, v55, v59\n"
      "v_mfma_f32_16x16x32_bf16 v[32:35], a[28:31], a[180:183], v[32:35]\n"
      "ds_write_addtid_b32 v61 offset:37440\n"
      "v_cvt_pk_bf16_f32 v57, v58, v59\n"
      "v_mfma_f32_16x16x32_bf16 v[32:35], a[32:35], a[192:195], v[32:35]\n"
      "ds_write_addtid_b32 v57 offset:38208\n"
      "v_cvt_pk_bf16_f32 v20, v20, v21\n"
      "v_mfma_f32_16x16x32_bf16 v[32:35], a[0:3], a[96:99], v[32:35]\n"
      "s_mov_b32 m0, s45\n"
      "v_cvt_pk_bf16_f32 v21, v22, v23\n"
      "v_cvt_pk_bf16_f32 v84, v76, v77\n"
      "v_mfma_f32_16x16x32_bf16 v[32:35], a[4:7], a[108:111], v[32:35]\n"
      "s_mov_b32 m0, s59\n"
      "v_cvt_pk_bf16_f32 v90, v78, v79\n"
      "v_mfma_f32_16x16x32_bf16 v[32:35], a[8:11], a[120:123], v[32:35]\n"
      "buffer_load_dwordx4 v19, s[32:35], s96 offen lds\n"
      "v_mfma_f32_16x16x32_bf16 v[32:35], a[12:15], a[132:135], v[32:35]\n"
      "s_mov_b32 m0, s45\n"
      "v_mfma_f32_16x16x32_bf16 v[32:35], a[16:19], a[144:147], v[32:35]\n"
      "ds_write_addtid_b32 v20 offset:48136\n"
      "ds_write_addtid_b32 v21 offset:48392\n"
      "v_mfma_f32_16x16x32_bf16 v[32:35], a[20:23], a[156:159], v[32:35]\n"
      "s_nop 1\n"
      "v_mfma_f32_16x16x32_bf16 v[20:23], a[12:15], a[88:91], 0\n"
      "s_add_u32 s51, s51, s12\n"
      "s_nop 1\n"
      "v_mfma_f32_16x16x32_bf16 v[20:23], a[16:19], a[100:103], v[20:23]\n"
      "s_sub_u32 s47, s51, s48\n"
      "v_cvt_pk_bf16_f32 v32, v32, v33\n"
      "v_cvt_pk_bf16_f32 v33, v34, v35\n"
      "v_mfma_f32_16x16x32_bf16 v[20:23], a[20:23], a[112:115], v[20:23]\n"
      "s_cmp_ge_u32 vcc_hi, 0x40000000\n"
      "ds_write_addtid_b32 v32 offset:35840\n"
      "v_mfma_f32_16x16x32_bf16 v[20:23], a[24:27], a[124:127], v[20:23]\n"
      "s_cselect_b32 s53, s47, s51\n"
      "ds_write_addtid_b32 v33 offset:36096\n"
      "v_mfma_f32_16x16x32_bf16 v[20:23], a[28:31], a[136:139], v[20:23]\n"
      "s_lshl_b64 vcc, vcc, 1\n"
      "v_mfma_f32_16x16x32_bf16 v[20:23], a[32:35], a[148:151], v[20:23]\n"
      "v_mfma_f32_16x16x32_bf16 v[24:27], a[12:15], a[92:95], 0\n"
      "s_waitcnt vmcnt(11) lgkmcnt(0)\n"
      "s_barrier\n"
      "v_mfma_f32_16x16x32_bf16 v[24:27], a[16:19], a[104:107], v[24:27]\n"
      "s_add_u32 m0, s58, 13280\n"
      "ds_read_b128 a[0:3], v7 offset:0\n"
      "v_mfma_f32_16x16x32_bf16 v[24:27], a[20:23], a[116:119], v[24:27]\n"
      "ds_read_b128 a[4:7], v7 offset:256\n"
      "v_mfma_f32_16x16x32_bf16 v[24:27], a[24:27], a[128:131], v[24:27]\n"
      "ds_read_b128 a[8:11], v7 offset:512\n"
      "v_mfma_f32_16x16x32_bf16 v[24:27], a[28:31], a[140:143], v[24:27]\n"
      "ds_read_b128 a[36:39], v2 offset:0\n"
      "v_mfma_f32_16x16x32_bf16 v[24:27], a[32:35], a[152:155], v[24:27]\n"
      "ds_read_b128 a[40:43], v3 offset:0\n"
      "v_mfma_f32_16x16x32_bf16 v[68:71], a[84:87], a[60:63], 0\n"
      "ds_read_b128 a[44:47], v4 offset:0\n"
      "s_nop 0\n"
      "v_mfma_f32_16x16x32_bf16 v[28:31], a[12:15], a[96:99], 0\n"
      "ds_read2_b64 a[64:67], v11 offset0:0 offset1:64\n"
      "s_nop 0\n"
      "v_mfma_f32_16x16x32_bf16 v[28:31], a[16:19], a[108:111], v[28:31]\n"
      "buffer_load_dwordx4 v16, s[32:35], s53 offen lds\n"
      "v_mfma_f32_16x16x32_bf16 v[28:31], a[20:23], a[120:123], v[28:31]\n"
      "s_mov_b32 m0, s45\n"
      "ds_read2_b64 a[68:71], v11 offset0:1 offset1:65\n"
      "v_cvt_pk_bf16_f32 v85, v68, v69\n"
      "v_mfma_f32_16x16x32_bf16 v[28:31], a[24:27], a[132:135], v[28:31]\n"
      "ds_read_b128 a[12:15], v6 offset:768\n"
      "v_cvt_pk_bf16_f32 v91, v70, v71\n"
      "v_mfma_f32_16x16x32_bf16 v[28:31], a[28:31], a[144:147], v[28:31]\n"
      "ds_read_b128 a[16:19], v6 offset:1024\n"
      "v_mfma_f32_16x16x32_bf16 v[28:31], a[32:35], a[156:159], v[28:31]\n"
      "s_waitcnt lgkmcnt(6)\n"
      "ds_read_b128 a[20:23], v6 offset:1280\n"
      "v_mfma_f32_16x16x32_bf16 v[20:23], a[0:3], a[160:163], v[20:23]\n"
      "buffer_store_dwordx2 v[84:85], v15, s[36:39], s52 offen sc1\n"
      "v_mfma_f32_16x16x32_bf16 v[20:23], a[4:7], a[172:175], v[20:23]\n"
      "ds_read_b128 a[24:27], v6 offset:5376\n"
      "v_mfma_f32_16x16x32_bf16 v[20:23], a[8:11], a[184:187], v[20:23]\n"
      "ds_read_b128 a[28:31], v6 offset:5632\n"
      "s_nop 0\n"
      "v_mfma_f32_16x16x32_bf16 v[36:39], a[36:39], a[72:75], 0\n"
      "s_waitcnt lgkmcnt(6)\n"
      "ds_read_b128 a[32:35], v6 offset:5888\n"
      "s_nop 0\n"
      "v_mfma_f32_16x16x32_bf16 v[40:43], a[40:43], a[76:79], 0\n"
      "ds_read_b128 a[48:51], v2 offset:64\n"
      "v_cvt_pk_bf16_f32 v20, v20, v21\n"
      "v_mfma_f32_16x16x32_bf16 v[44:47], a[44:47], a[80:83], v[36:39]\n"
      "buffer_store_dwordx2 v[90:91], v15, s[36:39], s52 offen offset:1536 sc1\n"
      "v_cvt_pk_bf16_f32 v21, v22, v23\n"
      "v_mfma_f32_16x16x32_bf16 v[24:27], a[0:3], a[164:167], v[24:27]\n"
      "s_mov_b32 s52, s13\n"
      "ds_write_addtid_b32 v20 offset:57360\n"
      "v_sub_f32_e32 v48, v36, v40\n"
      "v_mfma_f32_16x16x32_bf16 v[24:27], a[4:7], a[176:179], v[24:27]\n"
      "s_add_u32 s13, s13, s12\n"
      "ds_read_b128 a[52:55], v3 offset:64\n"
      "v_sub_f32_e32 v49, v37, v41\n"
      "v_mfma_f32_16x16x32_bf16 v[24:27], a[8:11], a[188:191], v[24:27]\n"
      "ds_write_addtid_b32 v21 offset:57616\n"
      "v_cvt_pk_bf16_f32 v48, v48, v49\n"
      "v_mfma_f32_16x16x32_bf16 v[72:75], a[84:87], a[64:67], 0\n"
      "ds_read_b128 a[56:59], v4 offset:64\n"
      "v_sub_f32_e32 v50, v38, v42\n"
      "v_mfma_f32_16x16x32_bf16 v[28:31], a[0:3], a[168:171], v[28:31]\n"
      "ds_read_b128 a[0:3], v7 offset:768\n"
      "v_sub_f32_e32 v51, v39, v43\n"
      "v_mfma_f32_16x16x32_bf16 v[28:31], a[4:7], a[180:183], v[28:31]\n"
      "ds_read_b128 a[4:7], v7 offset:1024\n"
      "v_cvt_pk_bf16_f32 v24, v24, v25\n"
      "v_mfma_f32_16x16x32_bf16 v[28:31], a[8:11], a[192:195], v[28:31]\n"
      "s_waitcnt lgkmcnt(6)\n"
      "ds_read_b128 a[8:11], v7 offset:1280\n"
      "v_cvt_pk_bf16_f32 v25, v26, v27\n"
      "v_mfma_f32_16x16x32_bf16 v[76:79], a[84:87], a[68:71], 0\n"
      "ds_read2_b64 a[60:63], v11 offset0:2 offset1:66\n"
      "v_cvt_pk_bf16_f32 v49, v50, v51\n"
      "v_mfma_f32_16x16x32_bf16 v[32:35], a[12:15], v[92:95], 0\n"
      "ds_write_addtid_b32 v24 offset:45064\n"
      "ds_write_addtid_b32 v25 offset:45320\n"
      "v_mfma_f32_16x16x32_bf16 v[32:35], a[16:19], v[148:151], v[32:35]\n"
      "s_mov_b32 m0, s18\n"
      "v_add_f32_e32 v40, v36, v40\n"
      "v_add_f32_e32 v41, v37, v41\n"
      "v_mfma_f32_16x16x32_bf16 v[32:35], a[20:23], v[204:207], v[32:35]\n"
      "ds_write_addtid_b32 v48 offset:6144\n"
      "v_add_f32_e32 v42, v38, v42\n"
      "v_mfma_f32_16x16x32_bf16 v[32:35], a[24:27], v[112:115], v[32:35]\n"
      "ds_write_addtid_b32 v49 offset:43072\n"
      "v_add_f32_e32 v43, v39, v43\n"
      "v_mfma_f32_16x16x32_bf16 v[32:35], a[28:31], a[208:211], v[32:35]\n"
      "v_cvt_pk_bf16_f32 v40, v40, v41\n"
      "v_cvt_pk_bf16_f32 v41, v42, v43\n"
      "v_mfma_f32_16x16x32_bf16 v[32:35], a[32:35], v[220:223], v[32:35]\n"
      "s_waitcnt lgkmcnt(6)\n"
      "ds_write_addtid_b32 v40 offset:5376\n"
      "v_cvt_pk_bf16_f32 v44, v44, v45\n"
      "v_mfma_f32_16x16x32_bf16 v[32:35], a[0:3], a[212:215], v[32:35]\n"
      "ds_write_addtid_b32 v41 offset:42304\n"
      "v_cvt_pk_bf16_f32 v45, v46, v47\n"
      "v_mfma_f32_16x16x32_bf16 v[32:35], a[4:7], v[184:187], v[32:35]\n"
      "s_waitcnt lgkmcnt(6)\n"
      "ds_write_addtid_b32 v44 offset:4608\n"
      "ds_write_addtid_b32 v45 offset:41536\n"
      "v_mfma_f32_16x16x32_bf16 v[32:35], a[8:11], v[240:243], v[32:35]\n"
      "s_mov_b32 m0, s45\n"
      "ds_read_b128 a[36:39], v2 offset:128\n"
      "v_cvt_pk_bf16_f32 v28, v28, v29\n"
      "v_mfma_f32_16x16x32_bf16 v[20:23], a[12:15], v[100:103], 0\n"
      "ds_write_addtid_b32 v28 offset:32768\n"
      "v_cvt_pk_bf16_f32 v29, v30, v31\n"
      "v_mfma_f32_16x16x32_bf16 v[20:23], a[16:19], a[200:203], v[20:23]\n"
      "ds_write_addtid_b32 v29 offset:33024\n"
      "v_cvt_pk_bf16_f32 v32, v32, v33\n"
      "v_mfma_f32_16x16x32_bf16 v[20:23], a[20:23], v[208:211], v[20:23]\n"
      "s_add_u32 m0, s58, 13280\n"
      "ds_read_b128 a[44:47], v4 offset:128\n"
      "v_cvt_pk_bf16_f32 v33, v34, v35\n"
      "v_mfma_f32_16x16x32_bf16 v[20:23], a[24:27], a[204:207], v[20:23]\n"
      "buffer_load_dwordx4 v17, s[32:35], s53 offen offset:1024 lds\n"
      "v_cvt_pk_bf16_f32 v80, v72, v73\n"
      "v_mfma_f32_16x16x32_bf16 v[20:23], a[28:31], v[172:175], v[20:23]\n"
      "s_mov_b32 m0, s45\n"
      "ds_read2_b64 a[64:67], v11 offset0:3 offset1:67\n"
      "v_cvt_pk_bf16_f32 v81, v76, v77\n"
      "v_mfma_f32_16x16x32_bf16 v[20:23], a[32:35], v[228:231], v[20:23]\n"
      "ds_write_addtid_b32 v32 offset:58384\n"
      "v_cvt_pk_bf16_f32 v86, v74, v75\n"
      "v_mfma_f32_16x16x32_bf16 v[20:23], a[0:3], v[136:139], v[20:23]\n"
      "ds_write_addtid_b32 v33 offset:58640\n"
      "v_cvt_pk_bf16_f32 v87, v78, v79\n"
      "v_mfma_f32_16x16x32_bf16 v[20:23], a[4:7], v[192:195], v[20:23]\n"
      "ds_read_b128 a[40:43], v3 offset:128\n"
      "v_mfma_f32_16x16x32_bf16 v[20:23], a[8:11], v[244:247], v[20:23]\n"
      "global_load_dword v8, v9, s[54:55]\n"
      "s_nop 0\n"
      "v_mfma_f32_16x16x32_bf16 v[52:55], a[48:51], a[72:75], 0\n"
      "ds_read2_b64 a[68:71], v13 offset0:0 offset1:64\n"
      "s_nop 0\n"
      "v_mfma_f32_16x16x32_bf16 v[56:59], a[52:55], a[76:79], 0\n"
      "s_nop 0\n"
      "v_mfma_f32_16x16x32_bf16 v[60:63], a[56:59], a[80:83], v[52:55]\n"
      "v_cvt_pk_bf16_f32 v20, v20, v21\n"
      "v_cvt_pk_bf16_f32 v21, v22, v23\n"
      "v_mfma_f32_16x16x32_bf16 v[68:71], a[84:87], a[60:63], 0\n"
      "ds_write_addtid_b32 v20 offset:46088\n"
      "s_nop 0\n"
      "v_mfma_f32_16x16x32_bf16 v[24:27], a[12:15], a[196:199], 0\n"
      "ds_read_b128 a[12:15], v6 offset:1536\n"
      "v_sub_f32_e32 v64, v52, v56\n"
      "v_mfma_f32_16x16x32_bf16 v[24:27], a[16:19], v[160:163], v[24:27]\n"
      "ds_read_b128 a[16:19], v6 offset:1792\n"
      "v_sub_f32_e32 v65, v53, v57\n"
      "v_mfma_f32_16x16x32_bf16 v[24:27], a[20:23], v[216:219], v[24:27]\n"
      "ds_read_b128 a[20:23], v6 offset:2048\n"
      "v_cvt_pk_bf16_f32 v64, v64, v65\n"
      "v_mfma_f32_16x16x32_bf16 v[24:27], a[24:27], v[124:127], v[24:27]\n"
      "ds_read_b128 a[24:27], v6 offset:6144\n"
      "v_sub_f32_e32 v66, v54, v58\n"
      "v_mfma_f32_16x16x32_bf16 v[24:27], a[28:31], v[180:183], v[24:27]\n"
      "ds_read_b128 a[28:31], v6 offset:6400\n"
      "v_sub_f32_e32 v67, v55, v59\n"
      "v_mfma_f32_16x16x32_bf16 v[24:27], a[32:35], v[232:235], v[24:27]\n"
      "ds_read_b128 a[32:35], v6 offset:6656\n"
      "v_cvt_pk_bf16_f32 v65, v66, v67\n"
      "v_mfma_f32_16x16x32_bf16 v[24:27], a[0:3], a[216:219], v[24:27]\n"
      "ds_read_b128 a[0:3], v7 offset:1536\n"
      "v_add_f32_e32 v56, v52, v56\n"
      "v_mfma_f32_16x16x32_bf16 v[24:27], a[4:7], v[196:199], v[24:27]\n"
      "ds_read_b128 a[4:7], v7 offset:1792\n"
      "v_add_f32_e32 v57, v53, v57\n"
      "v_mfma_f32_16x16x32_bf16 v[24:27], a[8:11], v[252:255], v[24:27]\n"
      "s_waitcnt lgkmcnt(7)\n"
      "ds_read_b128 a[8:11], v7 offset:2048\n"
      "v_cvt_pk_bf16_f32 v56, v56, v57\n"
      "v_mfma_f32_16x16x32_bf16 v[36:39], a[36:39], a[72:75], 0\n"
      "ds_write_addtid_b32 v21 offset:46344\n"
      "v_add_f32_e32 v58, v54, v58\n"
      "v_mfma_f32_16x16x32_bf16 v[44:47], a[44:47], a[80:83], v[36:39]\n"
      "s_add_u32 m0, s58, 13280\n"
      "v_cvt_pk_bf16_f32 v24, v24, v25\n"
      "v_cvt_pk_bf16_f32 v25, v26, v27\n"
      "v_mfma_f32_16x16x32_bf16 v[72:75], a[84:87], a[64:67], 0\n"
      "buffer_load_dwordx4 v18, s[32:35], s53 offen offset:2048 lds\n"
      "v_add_f32_e32 v59, v55, v59\n"
      "v_mfma_f32_16x16x32_bf16 v[28:31], a[12:15], v[96:99], 0\n"
      "s_waitcnt lgkmcnt(5)\n"
      "v_cvt_pk_bf16_f32 v57, v58, v59\n"
      "v_cvt_pk_bf16_f32 v60, v60, v61\n"
      "v_mfma_f32_16x16x32_bf16 v[28:31], a[16:19], v[152:155], v[28:31]\n"
      "s_mov_b32 m0, s45\n"
      "v_cvt_pk_bf16_f32 v82, v68, v69\n"
      "v_cvt_pk_bf16_f32 v83, v72, v73\n"
      "v_mfma_f32_16x16x32_bf16 v[28:31], a[20:23], a[220:223], v[28:31]\n"
      "ds_write_addtid_b32 v24 offset:33792\n"
      "ds_write_addtid_b32 v25 offset:34048\n"
      "v_mfma_f32_16x16x32_bf16 v[28:31], a[24:27], v[116:119], v[28:31]\n"
      "s_mov_b32 m0, s18\n"
      "v_cvt_pk_bf16_f32 v61, v62, v63\n"
      "v_cvt_pk_bf16_f32 v88, v70, v71\n"
      "v_mfma_f32_16x16x32_bf16 v[28:31], a[28:31], v[168:171], v[28:31]\n"
      "s_waitcnt lgkmcnt(3)\n"
      "ds_write_addtid_b32 v64 offset:6400\n"
      "ds_write_addtid_b32 v65 offset:43328\n"
      "v_mfma_f32_16x16x32_bf16 v[28:31], a[32:35], v[224:227], v[28:31]\n"
      "ds_write_addtid_b32 v56 offset:5632\n"
      "ds_write_addtid_b32 v57 offset:42560\n"
      "v_mfma_f32_16x16x32_bf16 v[28:31], a[0:3], v[132:135], v[28:31]\n"
      "ds_write_addtid_b32 v60 offset:4864\n"
      "ds_write_addtid_b32 v61 offset:41792\n"
      "v_mfma_f32_16x16x32_bf16 v[28:31], a[4:7], v[188:191], v[28:31]\n"
      "buffer_store_dwordx4 v[80:83], v14, s[36:39], s52 offen sc1\n"
      "v_cvt_pk_bf16_f32 v89, v74, v75\n"
      "v_mfma_f32_16x16x32_bf16 v[28:31], a[8:11], a[240:243], v[28:31]\n"
      "s_mov_b32 m0, s45\n"
      "ds_read2_b64 a[60:63], v13 offset0:1 offset1:65\n"
      "v_cvt_pk_bf16_f32 v44, v44, v45\n"
      "v_mfma_f32_16x16x32_bf16 v[40:43], a[40:43], a[76:79], 0\n"
      "v_cvt_pk_bf16_f32 v45, v46, v47\n"
      "s_nop 0\n"
      "v_mfma_f32_16x16x32_bf16 v[20:23], a[12:15], v[104:107], 0\n"
      "v_mfma_f32_16x16x32_bf16 v[20:23], a[16:19], v[156:159], v[20:23]\n"
      "v_cvt_pk_bf16_f32 v28, v28, v29\n"
      "v_cvt_pk_bf16_f32 v29, v30, v31\n"
      "v_mfma_f32_16x16x32_bf16 v[20:23], a[20:23], v[212:215], v[20:23]\n"
      "buffer_store_dwordx4 v[86:89], v14, s[36:39], s52 offen offset:1536 sc1\n"
      "v_sub_f32_e32 v48, v36, v40\n"
      "v_mfma_f32_16x16x32_bf16 v[20:23], a[24:27], v[120:123], v[20:23]\n"
      "ds_write_addtid_b32 v28 offset:59408\n"
      "ds_write_addtid_b32 v29 offset:59664\n"
      "v_mfma_f32_16x16x32_bf16 v[20:23], a[28:31], v[176:179], v[20:23]\n"
      "s_mov_b32 m0, s18\n"
      "v_sub_f32_e32 v49, v37, v41\n"
      "v_cvt_pk_bf16_f32 v48, v48, v49\n"
      "v_mfma_f32_16x16x32_bf16 v[20:23], a[32:35], a[232:235], v[20:23]\n"
      "s_sub_u32 s18, s19, s18\n"
      "ds_write_addtid_b32 v48 offset:6656\n"
      "v_sub_f32_e32 v50, v38, v42\n"
      "v_mfma_f32_16x16x32_bf16 v[20:23], a[0:3], v[140:143], v[20:23]\n"
      "v_sub_f32_e32 v51, v39, v43\n"
      "v_cvt_pk_bf16_f32 v49, v50, v51\n"
      "v_mfma_f32_16x16x32_bf16 v[20:23], a[4:7], a[236:239], v[20:23]\n"
      "ds_write_addtid_b32 v49 offset:43584\n"
      "v_add_f32_e32 v40, v36, v40\n"
      "v_mfma_f32_16x16x32_bf16 v[20:23], a[8:11], v[248:251], v[20:23]\n"
      "v_add_f32_e32 v41, v37, v41\n"
      "v_cvt_pk_bf16_f32 v40, v40, v41\n"
      "v_mfma_f32_16x16x32_bf16 v[76:79], a[84:87], a[68:71], 0\n"
      "ds_write_addtid_b32 v40 offset:5888\n"
      "v_add_f32_e32 v42, v38, v42\n"
      "v_mfma_f32_16x16x32_bf16 v[32:35], a[12:15], v[108:111], 0\n"
      "ds_write_addtid_b32 v44 offset:5120\n"
      "v_add_f32_e32 v43, v39, v43\n"
      "v_mfma_f32_16x16x32_bf16 v[32:35], a[16:19], v[164:167], v[32:35]\n"
      "ds_write_addtid_b32 v45 offset:42048\n"
      "v_cvt_pk_bf16_f32 v41, v42, v43\n"
      "v_mfma_f32_16x16x32_bf16 v[32:35], a[20:23], a[224:227], v[32:35]\n"
      "ds_write_addtid_b32 v41 offset:42816\n"
      "v_cvt_pk_bf16_f32 v20, v20, v21\n"
      "v_mfma_f32_16x16x32_bf16 v[32:35], a[24:27], v[128:131], v[32:35]\n"
      "s_mov_b32 m0, s45\n"
      "v_cvt_pk_bf16_f32 v21, v22, v23\n"
      "v_cvt_pk_bf16_f32 v84, v76, v77\n"
      "v_mfma_f32_16x16x32_bf16 v[32:35], a[28:31], a[228:231], v[32:35]\n"
      "ds_write_addtid_b32 v20 offset:47112\n"
      "v_cvt_pk_bf16_f32 v90, v78, v79\n"
      "v_mfma_f32_16x16x32_bf16 v[32:35], a[32:35], v[236:239], v[32:35]\n"
      "ds_write_addtid_b32 v21 offset:47368\n"
      "v_mfma_f32_16x16x32_bf16 v[32:35], a[0:3], v[144:147], v[32:35]\n"
      "v_mfma_f32_16x16x32_bf16 v[32:35], a[4:7], v[200:203], v[32:35]\n"
      "v_mfma_f32_16x16x32_bf16 v[32:35], a[8:11], a[244:247], v[32:35]\n"
      "s_nop 1\n"
      "s_cbranch_vccnz Lconv_loop0\n"
      "Lconv_t0:\n"
      "s_nop 1\n"
      "v_mfma_f32_16x16x32_bf16 v[20:23], a[24:27], v[96:99], 0\n"
      "s_nop 1\n"
      "v_mfma_f32_16x16x32_bf16 v[20:23], a[28:31], v[152:155], v[20:23]\n"
      "v_cvt_pk_bf16_f32 v32, v32, v33\n"
      "v_cvt_pk_bf16_f32 v33, v34, v35\n"
      "v_mfma_f32_16x16x32_bf16 v[20:23], a[32:35], a[220:223], v[20:23]\n"
      "ds_write_addtid_b32 v32 offset:34816\n"
      "v_mfma_f32_16x16x32_bf16 v[20:23], a[0:3], v[116:119], v[20:23]\n"
      "ds_write_addtid_b32 v33 offset:35072\n"
      "v_mfma_f32_16x16x32_bf16 v[20:23], a[4:7], v[168:171], v[20:23]\n"
      "v_mfma_f32_16x16x32_bf16 v[20:23], a[8:11], v[224:227], v[20:23]\n"
      "v_mfma_f32_16x16x32_bf16 v[24:27], a[24:27], v[104:107], 0\n"
      "s_waitcnt lgkmcnt(0)\n"
      "s_barrier\n"
      "v_mfma_f32_16x16x32_bf16 v[24:27], a[28:31], v[156:159], v[24:27]\n"
      "ds_read2_b64 a[64:67], v10 offset0:0 offset1:64\n"
      "v_mfma_f32_16x16x32_bf16 v[24:27], a[32:35], v[212:215], v[24:27]\n"
      "ds_read_b128 a[12:15], v7 offset:6144\n"
      "v_mfma_f32_16x16x32_bf16 v[24:27], a[0:3], v[120:123], v[24:27]\n"
      "ds_read2_b64 a[68:71], v10 offset0:1 offset1:65\n"
      "v_mfma_f32_16x16x32_bf16 v[24:27], a[4:7], v[176:179], v[24:27]\n"
      "ds_read_b128 a[16:19], v7 offset:6400\n"
      "v_mfma_f32_16x16x32_bf16 v[24:27], a[8:11], a[232:235], v[24:27]\n"
      "ds_read_b128 a[20:23], v7 offset:6656\n"
      "v_mfma_f32_16x16x32_bf16 v[68:71], a[84:87], a[60:63], 0\n"
      "ds_read2_b64 a[60:63], v10 offset0:2 offset1:66\n"
      "s_nop 0\n"
      "v_mfma_f32_16x16x32_bf16 v[28:31], a[24:27], v[108:111], 0\n"
      "s_waitcnt lgkmcnt(5)\n"
      "ds_read_b128 a[24:27], v6 offset:5376\n"
      "s_nop 0\n"
      "v_mfma_f32_16x16x32_bf16 v[72:75], a[84:87], a[64:67], 0\n"
      "ds_read2_b64 a[64:67], v10 offset0:3 offset1:67\n"
      "v_cvt_pk_bf16_f32 v85, v68, v69\n"
      "v_mfma_f32_16x16x32_bf16 v[28:31], a[28:31], v[164:167], v[28:31]\n"
      "ds_read_b128 a[28:31], v6 offset:5632\n"
      "v_cvt_pk_bf16_f32 v91, v70, v71\n"
      "v_mfma_f32_16x16x32_bf16 v[28:31], a[32:35], a[224:227], v[28:31]\n"
      "buffer_store_dwordx2 v[84:85], v15, s[36:39], s52 offen sc1\n"
      "v_mfma_f32_16x16x32_bf16 v[28:31], a[0:3], v[128:131], v[28:31]\n"
      "ds_read_b128 a[32:35], v6 offset:5888\n"
      "v_cvt_pk_bf16_f32 v80, v72, v73\n"
      "v_mfma_f32_16x16x32_bf16 v[28:31], a[4:7], a[228:231], v[28:31]\n"
      "ds_read_b128 a[0:3], v7 offset:768\n"
      "v_cvt_pk_bf16_f32 v86, v74, v75\n"
      "v_mfma_f32_16x16x32_bf16 v[28:31], a[8:11], v[236:239], v[28:31]\n"
      "s_waitcnt lgkmcnt(6)\n"
      "ds_read_b128 a[4:7], v7 offset:1024\n"
      "v_mfma_f32_16x16x32_bf16 v[20:23], a[12:15], v[132:135], v[20:23]\n"
      "ds_read_b128 a[8:11], v7 offset:1280\n"
      "v_mfma_f32_16x16x32_bf16 v[76:79], a[84:87], a[68:71], 0\n"
      "s_waitcnt lgkmcnt(6)\n"
      "ds_read2_b64 a[68:71], v12 offset0:0 offset1:64\n"
      "s_nop 0\n"
      "v_mfma_f32_16x16x32_bf16 v[68:71], a[84:87], a[60:63], 0\n"
      "ds_read2_b64 a[60:63], v12 offset0:1 offset1:65\n"
      "s_nop 0\n"
      "v_mfma_f32_16x16x32_bf16 v[20:23], a[16:19], v[188:191], v[20:23]\n"
      "buffer_store_dwordx2 v[90:91], v15, s[36:39], s52 offen offset:1536 sc1\n"
      "v_cvt_pk_bf16_f32 v81, v76, v77\n"
      "v_mfma_f32_16x16x32_bf16 v[20:23], a[20:23], a[240:243], v[20:23]\n"
      "s_mov_b32 s52, s13\n"
      "v_cvt_pk_bf16_f32 v87, v78, v79\n"
      "v_cvt_pk_bf16_f32 v82, v68, v69\n"
      "v_mfma_f32_16x16x32_bf16 v[24:27], a[12:15], v[140:143], v[24:27]\n"
      "s_waitcnt lgkmcnt(5)\n"
      "v_cvt_pk_bf16_f32 v88, v70, v71\n"
      "s_nop 0\n"
      "v_mfma_f32_16x16x32_bf16 v[72:75], a[84:87], a[64:67], 0\n"
      "s_add_u32 s13, s13, s12\n"
      "v_cvt_pk_bf16_f32 v20, v20, v21\n"
      "v_cvt_pk_bf16_f32 v21, v22, v23\n"
      "v_mfma_f32_16x16x32_bf16 v[24:27], a[16:19], a[236:239], v[24:27]\n"
      "ds_write_addtid_b32 v20 offset:62480\n"
      "s_nop 0\n"
      "v_mfma_f32_16x16x32_bf16 v[24:27], a[20:23], v[248:251], v[24:27]\n"
      "ds_write_addtid_b32 v21 offset:62736\n"
      "v_cvt_pk_bf16_f32 v83, v72, v73\n"
      "v_mfma_f32_16x16x32_bf16 v[28:31], a[12:15], v[144:147], v[28:31]\n"
      "s_waitcnt lgkmcnt(3)\n"
      "ds_read_b128 a[12:15], v7 offset:5376\n"
      "v_cvt_pk_bf16_f32 v89, v74, v75\n"
      "v_mfma_f32_16x16x32_bf16 v[76:79], a[84:87], a[68:71], 0\n"
      "buffer_store_dwordx4 v[80:83], v14, s[36:39], s52 offen sc1\n"
      "v_cvt_pk_bf16_f32 v24, v24, v25\n"
      "v_mfma_f32_16x16x32_bf16 v[28:31], a[16:19], v[200:203], v[28:31]\n"
      "ds_read_b128 a[16:19], v7 offset:5632\n"
      "v_cvt_pk_bf16_f32 v25, v26, v27\n"
      "v_mfma_f32_16x16x32_bf16 v[28:31], a[20:23], a[244:247], v[28:31]\n"
      "ds_read_b128 a[20:23], v7 offset:5888\n"
      "v_mfma_f32_16x16x32_bf16 v[32:35], a[24:27], v[92:95], 0\n"
      "s_waitcnt lgkmcnt(5)\n"
      "ds_write_addtid_b32 v24 offset:50184\n"
      "v_cvt_pk_bf16_f32 v84, v76, v77\n"
      "v_mfma_f32_16x16x32_bf16 v[68:71], a[84:87], a[60:63], 0\n"
      "ds_write_addtid_b32 v25 offset:50440\n"
      "v_cvt_pk_bf16_f32 v90, v78, v79\n"
      "v_mfma_f32_16x16x32_bf16 v[32:35], a[28:31], v[148:151], v[32:35]\n"
      "buffer_store_dwordx4 v[86:89], v14, s[36:39], s52 offen offset:1536 sc1\n"
      "v_cvt_pk_bf16_f32 v28, v28, v29\n"
      "v_mfma_f32_16x16x32_bf16 v[32:35], a[32:35], v[204:207], v[32:35]\n"
      "ds_write_addtid_b32 v28 offset:37888\n"
      "v_cvt_pk_bf16_f32 v29, v30, v31\n"
      "v_mfma_f32_16x16x32_bf16 v[32:35], a[0:3], v[112:115], v[32:35]\n"
      "v_cvt_pk_bf16_f32 v85, v68, v69\n"
      "v_cvt_pk_bf16_f32 v91, v70, v71\n"
      "v_mfma_f32_16x16x32_bf16 v[32:35], a[4:7], a[208:211], v[32:35]\n"
      "ds_write_addtid_b32 v29 offset:38144\n"
      "v_mfma_f32_16x16x32_bf16 v[32:35], a[8:11], v[220:223], v[32:35]\n"
      "s_waitcnt lgkmcnt(5)\n"
      "v_mfma_f32_16x16x32_bf16 v[32:35], a[12:15], a[212:215], v[32:35]\n"
      "buffer_store_dwordx2 v[84:85], v15, s[36:39], s52 offen sc1\n"
      "v_mfma_f32_16x16x32_bf16 v[32:35], a[16:19], v[184:187], v[32:35]\n"
      "s_waitcnt lgkmcnt(4)\n"
      "v_mfma_f32_16x16x32_bf16 v[32:35], a[20:23], v[240:243], v[32:35]\n"
      "s_nop 1\n"
      "v_mfma_f32_16x16x32_bf16 v[20:23], a[24:27], v[100:103], 0\n"
      "s_nop 1\n"
      "v_mfma_f32_16x16x32_bf16 v[20:23], a[28:31], a[200:203], v[20:23]\n"
      "s_nop 0\n"
      "v_mfma_f32_16x16x32_bf16 v[20:23], a[32:35], v[208:211], v[20:23]\n"
      "buffer_store_dwordx2 v[90:91], v15, s[36:39], s52 offen offset:1536 sc1\n"
      "v_cvt_pk_bf16_f32 v32, v32, v33\n"
      "v_mfma_f32_16x16x32_bf16 v[20:23], a[0:3], a[204:207], v[20:23]\n"
      "ds_write_addtid_b32 v32 offset:61456\n"
      "v_cvt_pk_bf16_f32 v33, v34, v35\n"
      "v_mfma_f32_16x16x32_bf16 v[20:23], a[4:7], v[172:175], v[20:23]\n"
      "ds_write_addtid_b32 v33 offset:61712\n"
      "v_mfma_f32_16x16x32_bf16 v[20:23], a[8:11], v[228:231], v[20:23]\n"
      "v_mfma_f32_16x16x32_bf16 v[20:23], a[12:15], v[136:139], v[20:23]\n"
      "v_mfma_f32_16x16x32_bf16 v[20:23], a[16:19], v[192:195], v[20:23]\n"
      "v_mfma_f32_16x16x32_bf16 v[20:23], a[20:23], v[244:247], v[20:23]\n"
      "s_nop 1\n"
      "v_mfma_f32_16x16x32_bf16 v[24:27], a[24:27], a[196:199], 0\n"
      "ds_read_b128 a[24:27], v7 offset:4608\n"
      "s_nop 0\n"
      "v_mfma_f32_16x16x32_bf16 v[24:27], a[28:31], v[160:163], v[24:27]\n"
      "ds_read_b128 a[28:31], v7 offset:4864\n"
      "v_mfma_f32_16x16x32_bf16 v[24:27], a[32:35], v[216:219], v[24:27]\n"
      "ds_read_b128 a[32:35], v7 offset:5120\n"
      "v_cvt_pk_bf16_f32 v20, v20, v21\n"
      "v_mfma_f32_16x16x32_bf16 v[24:27], a[0:3], v[124:127], v[24:27]\n"
      "ds_read_b128 a[0:3], v6 offset:4608\n"
      "v_cvt_pk_bf16_f32 v21, v22, v23\n"
      "v_mfma_f32_16x16x32_bf16 v[24:27], a[4:7], v[180:183], v[24:27]\n"
      "ds_read_b128 a[4:7], v6 offset:4864\n"
      "v_mfma_f32_16x16x32_bf16 v[24:27], a[8:11], v[232:235], v[24:27]\n"
      "ds_read_b128 a[8:11], v6 offset:5120\n"
      "v_mfma_f32_16x16x32_bf16 v[24:27], a[12:15], a[216:219], v[24:27]\n"
      "ds_read_b128 a[12:15], v7 offset:0\n"
      "v_mfma_f32_16x16x32_bf16 v[24:27], a[16:19], v[196:199], v[24:27]\n"
      "ds_read_b128 a[16:19], v7 offset:256\n"
      "v_mfma_f32_16x16x32_bf16 v[24:27], a[20:23], v[252:255], v[24:27]\n"
      "s_waitcnt lgkmcnt(7)\n"
      "ds_read_b128 a[20:23], v7 offset:512\n"
      "s_nop 0\n"
      "v_mfma_f32_16x16x32_bf16 v[28:31], a[24:27], a[160:163], 0\n"
      "s_waitcnt lgkmcnt(7)\n"
      "ds_write_addtid_b32 v20 offset:49160\n"
      "ds_write_addtid_b32 v21 offset:49416\n"
      "v_mfma_f32_16x16x32_bf16 v[28:31], a[28:31], a[172:175], v[28:31]\n"
      "s_waitcnt lgkmcnt(8)\n"
      "v_cvt_pk_bf16_f32 v24, v24, v25\n"
      "v_cvt_pk_bf16_f32 v25, v26, v27\n"
      "v_mfma_f32_16x16x32_bf16 v[28:31], a[32:35], a[184:187], v[28:31]\n"
      "s_waitcnt lgkmcnt(7)\n"
      "ds_write_addtid_b32 v24 offset:36864\n"
      "v_mfma_f32_16x16x32_bf16 v[28:31], a[0:3], a[88:91], v[28:31]\n"
      "s_waitcnt lgkmcnt(7)\n"
      "ds_write_addtid_b32 v25 offset:37120\n"
      "v_mfma_f32_16x16x32_bf16 v[28:31], a[4:7], a[100:103], v[28:31]\n"
      "s_waitcnt lgkmcnt(7)\n"
      "v_mfma_f32_16x16x32_bf16 v[28:31], a[8:11], a[112:115], v[28:31]\n"
      "s_waitcnt lgkmcnt(6)\n"
      "v_mfma_f32_16x16x32_bf16 v[28:31], a[12:15], a[124:127], v[28:31]\n"
      "s_waitcnt lgkmcnt(5)\n"
      "v_mfma_f32_16x16x32_bf16 v[28:31], a[16:19], a[136:139], v[28:31]\n"
      "s_waitcnt lgkmcnt(4)\n"
      "v_mfma_f32_16x16x32_bf16 v[28:31], a[20:23], a[148:151], v[28:31]\n"
      "s_nop 1\n"
      "v_mfma_f32_16x16x32_bf16 v[20:23], a[24:27], a[164:167], 0\n"
      "s_nop 1\n"
      "v_mfma_f32_16x16x32_bf16 v[20:23], a[28:31], a[176:179], v[20:23]\n"
      "s_nop 0\n"
      "v_mfma_f32_16x16x32_bf16 v[20:23], a[32:35], a[188:191], v[20:23]\n"
      "v_cvt_pk_bf16_f32 v28, v28, v29\n"
      "v_cvt_pk_bf16_f32 v29, v30, v31\n"
      "v_mfma_f32_16x16x32_bf16 v[20:23], a[0:3], a[92:95], v[20:23]\n"
      "ds_write_addtid_b32 v28 offset:60432\n"
      "v_mfma_f32_16x16x32_bf16 v[20:23], a[4:7], a[104:107], v[20:23]\n"
      "ds_write_addtid_b32 v29 offset:60688\n"
      "v_mfma_f32_16x16x32_bf16 v[20:23], a[8:11], a[116:119], v[20:23]\n"
      "v_mfma_f32_16x16x32_bf16 v[20:23], a[12:15], a[128:131], v[20:23]\n"
      "v_mfma_f32_16x16x32_bf16 v[20:23], a[16:19], a[140:143], v[20:23]\n"
      "v_mfma_f32_16x16x32_bf16 v[20:23], a[20:23], a[152:155], v[20:23]\n"
      "s_nop 1\n"
      "v_mfma_f32_16x16x32_bf16 v[32:35], a[24:27], a[168:171], 0\n"
      "s_nop 1\n"
      "v_mfma_f32_16x16x32_bf16 v[32:35], a[28:31], a[180:183], v[32:35]\n"
      "s_nop 0\n"
      "v_mfma_f32_16x16x32_bf16 v[32:35], a[32:35], a[192:195], v[32:35]\n"
      "v_cvt_pk_bf16_f32 v20, v20, v21\n"
      "v_cvt_pk_bf16_f32 v21, v22, v23\n"
      "v_mfma_f32_16x16x32_bf16 v[32:35], a[0:3], a[96:99], v[32:35]\n"
      "ds_write_addtid_b32 v20 offset:48136\n"
      "v_mfma_f32_16x16x32_bf16 v[32:35], a[4:7], a[108:111], v[32:35]\n"
      "ds_write_addtid_b32 v21 offset:48392\n"
      "v_mfma_f32_16x16x32_bf16 v[32:35], a[8:11], a[120:123], v[32:35]\n"
      "v_mfma_f32_16x16x32_bf16 v[32:35], a[12:15], a[132:135], v[32:35]\n"
      "v_mfma_f32_16x16x32_bf16 v[32:35], a[16:19], a[144:147], v[32:35]\n"
      "v_mfma_f32_16x16x32_bf16 v[32:35], a[20:23], a[156:159], v[32:35]\n"
      "s_nop 1\n"
      "s_nop 5\n"
      "v_cvt_pk_bf16_f32 v32, v32, v33\n"
      "v_cvt_pk_bf16_f32 v33, v34, v35\n"
      "ds_write_addtid_b32 v32 offset:35840\n"
      "ds_write_addtid_b32 v33 offset:36096\n"
      "s_waitcnt lgkmcnt(0)\n"
      "s_barrier\n"
      "ds_read2_b64 a[0:3], v11 offset0:0 offset1:64\n"
      "ds_read2_b64 a[4:7], v11 offset0:1 offset1:65\n"
      "ds_read2_b64 a[8:11], v11 offset0:2 offset1:66\n"
      "ds_read2_b64 a[12:15], v11 offset0:3 offset1:67\n"
      "ds_read2_b64 a[16:19], v13 offset0:0 offset1:64\n"
      "ds_read2_b64 a[20:23], v13 offset0:1 offset1:65\n"
      "s_waitcnt lgkmcnt(5)\n"
      "v_mfma_f32_16x16x32_bf16 v[72:75], a[84:87], a[0:3], 0\n"
      "s_waitcnt lgkmcnt(4)\n"
      "v_mfma_f32_16x16x32_bf16 v[76:79], a[84:87], a[4:7], 0\n"
      "s_waitcnt lgkmcnt(3)\n"
      "v_mfma_f32_16x16x32_bf16 v[68:71], a[84:87], a[8:11], 0\n"
      "s_waitcnt lgkmcnt(2)\n"
      "s_nop 2\n"
      "v_cvt_pk_bf16_f32 v80, v72, v73\n"
      "v_cvt_pk_bf16_f32 v86, v74, v75\n"
      "v_mfma_f32_16x16x32_bf16 v[72:75], a[84:87], a[12:15], 0\n"
      "s_waitcnt lgkmcnt(1)\n"
      "v_cvt_pk_bf16_f32 v81, v76, v77\n"
      "v_cvt_pk_bf16_f32 v87, v78, v79\n"
      "v_cvt_pk_bf16_f32 v82, v68, v69\n"
      "v_cvt_pk_bf16_f32 v88, v70, v71\n"
      "v_mfma_f32_16x16x32_bf16 v[76:79], a[84:87], a[16:19], 0\n"
      "s_waitcnt lgkmcnt(0)\n"
      "v_mfma_f32_16x16x32_bf16 v[68:71], a[84:87], a[20:23], 0\n"
      "s_mov_b32 s52, s13\n"
      "v_cvt_pk_bf16_f32 v83, v72, v73\n"
      "v_cvt_pk_bf16_f32 v89, v74, v75\n"
      "s_add_u32 s13, s13, s12\n"
      "buffer_store_dwordx4 v[80:83], v14, s[36:39], s52 offen sc1\n"
      "buffer_store_dwordx4 v[86:89], v14, s[36:39], s52 offen offset:1536 sc1\n"
      "v_cvt_pk_bf16_f32 v84, v76, v77\n"
      "v_cvt_pk_bf16_f32 v90, v78, v79\n"
      "v_cvt_pk_bf16_f32 v85, v68, v69\n"
      "v_cvt_pk_bf16_f32 v91, v70, v71\n"
      "buffer_store_dwordx2 v[84:85], v15, s[36:39], s52 offen sc1\n"
      "buffer_store_dwordx2 v[90:91], v15, s[36:39], s52 offen offset:1536 sc1\n"
      "Lconv_u0:\n"
      "s_waitcnt vmcnt(0)\n"
      "s_endpgm\n"
      ".p2align 12\n"
      "Lconv_pa1:\n"
      "s_cbranch_execz Lconv_r0\n"
      "s_nop 0\n"
      "Lconv_loop1:\n"
      "v_mfma_f32_16x16x32_bf16 v[20:23], a[24:27], v[96:99], 0\n"
      "s_add_u32 s51, s51, s12\n"
      "ds_write_addtid_b32 v32 offset:34816\n"
      "v_cvt_pk_bf16_f32 v33, v34, v35\n"
      "v_mfma_f32_16x16x32_bf16 v[20:23], a[28:31], v[152:155], v[20:23]\n"
      "s_sub_u32 s58, s93, s58\n"
      "ds_write_addtid_b32 v33 offset:35072\n"
      "v_cvt_pk_bf16_f32 v84, v72, v73\n"
      "v_mfma_f32_16x16x32_bf16 v[20:23], a[32:35], a[220:223], v[20:23]\n"
      "s_sub_u32 s59, s94, s59\n"
      "v_cvt_pk_bf16_f32 v85, v76, v77\n"
      "v_cvt_pk_bf16_f32 v90, v74, v75\n"
      "v_mfma_f32_16x16x32_bf16 v[20:23], a[0:3], v[116:119], v[20:23]\n"
      "buffer_store_dwordx2 v[84:85], v15, s[36:39], s52 offen sc1\n"
      "v_cvt_pk_bf16_f32 v91, v78, v79\n"
      "v_mfma_f32_16x16x32_bf16 v[20:23], a[4:7], v[168:171], v[20:23]\n"
      "s_sub_u32 s47, s51, s48\n"
      "v_mfma_f32_16x16x32_bf16 v[20:23], a[8:11], v[224:227], v[20:23]\n"
      "s_cmp_ge_u32 vcc_hi, 0x80000000\n"
      "v_mfma_f32_16x16x32_bf16 v[24:27], a[24:27], v[104:107], 0\n"
      "buffer_store_dwordx2 v[90:91], v15, s[36:39], s52 offen offset:1536 sc1\n"
      "v_mfma_f32_16x16x32_bf16 v[24:27], a[28:31], v[156:159], v[24:27]\n"
      "s_cselect_b32 s53, s47, s51\n"
      "v_mfma_f32_16x16x32_bf16 v[24:27], a[32:35], v[212:215], v[24:27]\n"
      "s_mov_b32 s52, s13\n"
      "v_mfma_f32_16x16x32_bf16 v[24:27], a[0:3], v[120:123], v[24:27]\n"
      "s_add_u32 s13, s13, s12\n"
      "v_mfma_f32_16x16x32_bf16 v[24:27], a[4:7], v[176:179], v[24:27]\n"
      "s_waitcnt vmcnt(17) lgkmcnt(0)\n"
      "s_barrier\n"
      "v_mfma_f32_16x16x32_bf16 v[24:27], a[8:11], a[232:235], v[24:27]\n"
      "s_mov_b32 m0, s58\n"
      "ds_read2_b64 a[60:63], v10 offset0:0 offset1:64\n"
      "v_mfma_f32_16x16x32_bf16 v[28:31], a[24:27], v[108:111], 0\n"
      "ds_read_b128 a[12:15], v7 offset:6144\n"
      "v_mfma_f32_16x16x32_bf16 v[28:31], a[28:31], v[164:167], v[28:31]\n"
      "ds_read_b128 a[16:19], v7 offset:6400\n"
      "v_mfma_f32_16x16x32_bf16 v[28:31], a[32:35], a[224:227], v[28:31]\n"
      "ds_read_b128 a[20:23], v7 offset:6656\n"
      "v_mfma_f32_16x16x32_bf16 v[28:31], a[0:3], v[128:131], v[28:31]\n"
      "ds_read_b128 a[48:51], v2 offset:13280\n"
      "v_mfma_f32_16x16x32_bf16 v[28:31], a[4:7], a[228:231], v[28:31]\n"
      "ds_read_b128 a[56:59], v4 offset:13280\n"
      "v_mfma_f32_16x16x32_bf16 v[28:31], a[8:11], v[236:239], v[28:31]\n"
      "s_waitcnt lgkmcnt(5)\n"
      "ds_read_b128 a[52:55], v3 offset:13280\n"
      "v_mfma_f32_16x16x32_bf16 v[68:71], a[84:87], a[60:63], 0\n"
      "s_waitcnt lgkmcnt(5)\n"
      "ds_read_b128 a[24:27], v6 offset:5376\n"
      "v_mfma_f32_16x16x32_bf16 v[20:23], a[12:15], v[132:135], v[20:23]\n"
      "s_waitcnt lgkmcnt(5)\n"
      "ds_read_b128 a[28:31], v6 offset:5632\n"
      "v_mfma_f32_16x16x32_bf16 v[20:23], a[16:19], v[188:191], v[20:23]\n"
      "s_waitcnt lgkmcnt(5)\n"
      "ds_read_b128 a[32:35], v6 offset:5888\n"
      "v_cvt_pk_bf16_f32 v80, v68, v69\n"
      "v_mfma_f32_16x16x32_bf16 v[20:23], a[20:23], a[240:243], v[20:23]\n"
      "ds_read_b128 a[0:3], v7 offset:768\n"
      "v_cvt_pk_bf16_f32 v86, v70, v71\n"
      "v_mfma_f32_16x16x32_bf16 v[24:27], a[12:15], v[140:143], v[24:27]\n"
      "buffer_load_dwordx4 v16, s[32:35], s53 offen lds\n"
      "s_nop 0\n"
      "v_mfma_f32_16x16x32_bf16 v[24:27], a[16:19], a[236:239], v[24:27]\n"
      "s_mov_b32 m0, s45\n"
      "ds_read_b128 a[4:7], v7 offset:1024\n"
      "v_cvt_pk_bf16_f32 v20, v20, v21\n"
      "v_mfma_f32_16x16x32_bf16 v[24:27], a[20:23], v[248:251], v[24:27]\n"
      "s_waitcnt lgkmcnt(6)\n"
      "ds_read_b128 a[8:11], v7 offset:1280\n"
      "v_cvt_pk_bf16_f32 v21, v22, v23\n"
      "v_mfma_f32_16x16x32_bf16 v[52:55], a[48:51], a[72:75], 0\n"
      "ds_read2_b64 a[64:67], v10 offset0:1 offset1:65\n"
      "s_nop 0\n"
      "v_mfma_f32_16x16x32_bf16 v[60:63], a[56:59], a[80:83], v[52:55]\n"
      "ds_write_addtid_b32 v20 offset:62480\n"
      "v_cvt_pk_bf16_f32 v24, v24, v25\n"
      "v_mfma_f32_16x16x32_bf16 v[28:31], a[12:15], v[144:147], v[28:31]\n"
      "ds_read_b128 a[12:15], v7 offset:5376\n"
      "v_cvt_pk_bf16_f32 v25, v26, v27\n"
      "v_mfma_f32_16x16x32_bf16 v[28:31], a[16:19], v[200:203], v[28:31]\n"
      "ds_read_b128 a[16:19], v7 offset:5632\n"
      "v_mfma_f32_16x16x32_bf16 v[28:31], a[20:23], a[244:247], v[28:31]\n"
      "s_waitcnt lgkmcnt(6)\n"
      "ds_read_b128 a[20:23], v7 offset:5888\n"
      "v_cvt_pk_bf16_f32 v60, v60, v61\n"
      "v_mfma_f32_16x16x32_bf16 v[56:59], a[52:55], a[76:79], 0\n"
      "ds_write_addtid_b32 v21 offset:62736\n"
      "v_cvt_pk_bf16_f32 v61, v62, v63\n"
      "v_mfma_f32_16x16x32_bf16 v[32:35], a[24:27], v[92:95], 0\n"
      "ds_read_b128 a[36:39], v2 offset:13344\n"
      "v_cvt_pk_bf16_f32 v28, v28, v29\n"
      "v_mfma_f32_16x16x32_bf16 v[32:35], a[28:31], v[148:151], v[32:35]\n"
      "ds_write_addtid_b32 v24 offset:50184\n"
      "v_cvt_pk_bf16_f32 v29, v30, v31\n"
      "v_mfma_f32_16x16x32_bf16 v[32:35], a[32:35], v[204:207], v[32:35]\n"
      "ds_read_b128 a[40:43], v3 offset:13344\n"
      "v_sub_f32_e32 v64, v52, v56\n"
      "v_mfma_f32_16x16x32_bf16 v[32:35], a[0:3], v[112:115], v[32:35]\n"
      "s_waitcnt lgkmcnt(8)\n"
      "ds_write_addtid_b32 v25 offset:50440\n"
      "v_sub_f32_e32 v65, v53, v57\n"
      "v_mfma_f32_16x16x32_bf16 v[32:35], a[4:7], a[208:211], v[32:35]\n"
      "ds_read_b128 a[44:47], v4 offset:13344\n"
      "v_sub_f32_e32 v66, v54, v58\n"
      "v_mfma_f32_16x16x32_bf16 v[32:35], a[8:11], v[220:223], v[32:35]\n"
      "s_waitcnt lgkmcnt(7)\n"
      "ds_read_b128 a[48:51], v2 offset:13408\n"
      "v_sub_f32_e32 v67, v55, v59\n"
      "v_mfma_f32_16x16x32_bf16 v[32:35], a[12:15], a[212:215], v[32:35]\n"
      "ds_write_addtid_b32 v28 offset:37888\n"
      "v_add_f32_e32 v56, v52, v56\n"
      "v_mfma_f32_16x16x32_bf16 v[32:35], a[16:19], v[184:187], v[32:35]\n"
      "s_waitcnt lgkmcnt(8)\n"
      "ds_write_addtid_b32 v29 offset:38144\n"
      "v_add_f32_e32 v57, v53, v57\n"
      "v_mfma_f32_16x16x32_bf16 v[32:35], a[20:23], v[240:243], v[32:35]\n"
      "s_mov_b32 m0, s18\n"
      "ds_read_b128 a[56:59], v4 offset:13408\n"
      "v_add_f32_e32 v58, v54, v58\n"
      "v_mfma_f32_16x16x32_bf16 v[72:75], a[84:87], a[64:67], 0\n"
      "ds_read2_b64 a[68:71], v10 offset0:2 offset1:66\n"
      "v_add_f32_e32 v59, v55, v59\n"
      "v_mfma_f32_16x16x32_bf16 v[20:23], a[24:27], v[100:103], 0\n"
      "v_cvt_pk_bf16_f32 v64, v64, v65\n"
      "v_cvt_pk_bf16_f32 v65, v66, v67\n"
      "v_mfma_f32_16x16x32_bf16 v[20:23], a[28:31], a[200:203], v[20:23]\n"
      "ds_write_addtid_b32 v64 offset:1536\n"
      "v_cvt_pk_bf16_f32 v56, v56, v57\n"
      "v_mfma_f32_16x16x32_bf16 v[20:23], a[32:35], v[208:211], v[20:23]\n"
      "ds_write_addtid_b32 v65 offset:38464\n"
      "v_cvt_pk_bf16_f32 v57, v58, v59\n"
      "v_mfma_f32_16x16x32_bf16 v[20:23], a[0:3], a[204:207], v[20:23]\n"
      "ds_write_addtid_b32 v56 offset:768\n"
      "v_cvt_pk_bf16_f32 v32, v32, v33\n"
      "v_mfma_f32_16x16x32_bf16 v[20:23], a[4:7], v[172:175], v[20:23]\n"
      "ds_write_addtid_b32 v57 offset:37696\n"
      "v_cvt_pk_bf16_f32 v33, v34, v35\n"
      "v_mfma_f32_16x16x32_bf16 v[20:23], a[8:11], v[228:231], v[20:23]\n"
      "ds_write_addtid_b32 v60 offset:0\n"
      "v_cvt_pk_bf16_f32 v81, v72, v73\n"
      "v_mfma_f32_16x16x32_bf16 v[20:23], a[12:15], v[136:139], v[20:23]\n"
      "ds_write_addtid_b32 v61 offset:36928\n"
      "v_cvt_pk_bf16_f32 v87, v74, v75\n"
      "v_mfma_f32_16x16x32_bf16 v[20:23], a[16:19], v[192:195], v[20:23]\n"
      "s_mov_b32 m0, s45\n"
      "ds_read_b128 a[52:55], v3 offset:13408\n"
      "v_add_u32_e32 v2, s91, v2\n"
      "v_mfma_f32_16x16x32_bf16 v[20:23], a[20:23], v[244:247], v[20:23]\n"
      "s_waitcnt lgkmcnt(7)\n"
      "ds_read2_b64 a[60:63], v10 offset0:3 offset1:67\n"
      "v_add_u32_e32 v4, s91, v4\n"
      "v_mfma_f32_16x16x32_bf16 v[36:39], a[36:39], a[72:75], 0\n"
      "s_mov_b32 m0, s58\n"
      "ds_read2_b64 a[64:67], v12 offset0:0 offset1:64\n"
      "v_add_u32_e32 v3, s91, v3\n"
      "v_mfma_f32_16x16x32_bf16 v[40:43], a[40:43], a[76:79], 0\n"
      "buffer_load_dwordx4 v17, s[32:35], s53 offen offset:1024 lds\n"
      "v_cvt_pk_bf16_f32 v20, v20, v21\n"
      "v_mfma_f32_16x16x32_bf16 v[44:47], a[44:47], a[80:83], v[36:39]\n"
      "s_mov_b32 m0, s45\n"
      "v_cvt_pk_bf16_f32 v21, v22, v23\n"
      "s_nop 0\n"
      "v_mfma_f32_16x16x32_bf16 v[52:55], a[48:51], a[72:75], 0\n"
      "s_sub_u32 s91, 0, s91\n"
      "ds_write_addtid_b32 v32 offset:61456\n"
      "v_sub_f32_e32 v48, v36, v40\n"
      "v_mfma_f32_16x16x32_bf16 v[60:63], a[56:59], a[80:83], v[52:55]\n"
      "ds_write_addtid_b32 v33 offset:61712\n"
      "v_sub_f32_e32 v49, v37, v41\n"
      "v_mfma_f32_16x16x32_bf16 v[76:79], a[84:87], a[68:71], 0\n"
      "ds_write_addtid_b32 v20 offset:49160\n"
      "v_sub_f32_e32 v50, v38, v42\n"
      "v_mfma_f32_16x16x32_bf16 v[24:27], a[24:27], a[196:199], 0\n"
      "ds_read_b128 a[24:27], v7 offset:4608\n"
      "v_sub_f32_e32 v51, v39, v43\n"
      "v_mfma_f32_16x16x32_bf16 v[24:27], a[28:31], v[160:163], v[24:27]\n"
      "ds_read_b128 a[28:31], v7 offset:4864\n"
      "v_add_f32_e32 v40, v36, v40\n"
      "v_mfma_f32_16x16x32_bf16 v[24:27], a[32:35], v[216:219], v[24:27]\n"
      "ds_read_b128 a[32:35], v7 offset:5120\n"
      "v_add_f32_e32 v41, v37, v41\n"
      "v_mfma_f32_16x16x32_bf16 v[24:27], a[0:3], v[124:127], v[24:27]\n"
      "ds_read_b128 a[0:3], v6 offset:4608\n"
      "v_add_f32_e32 v42, v38, v42\n"
      "v_mfma_f32_16x16x32_bf16 v[24:27], a[4:7], v[180:183], v[24:27]\n"
      "ds_read_b128 a[4:7], v6 offset:4864\n"
      "v_add_f32_e32 v43, v39, v43\n"
      "v_mfma_f32_16x16x32_bf16 v[24:27], a[8:11], v[232:235], v[24:27]\n"
      "ds_read_b128 a[8:11], v6 offset:5120\n"
      "v_cvt_pk_bf16_f32 v48, v48, v49\n"
      "v_mfma_f32_16x16x32_bf16 v[24:27], a[12:15], a[216:219], v[24:27]\n"
      "ds_read_b128 a[12:15], v7 offset:0\n"
      "v_cvt_pk_bf16_f32 v49, v50, v51\n"
      "v_mfma_f32_16x16x32_bf16 v[24:27], a[16:19], v[196:199], v[24:27]\n"
      "ds_read_b128 a[16:19], v7 offset:256\n"
      "v_cvt_pk_bf16_f32 v40, v40, v41\n"
      "v_mfma_f32_16x16x32_bf16 v[24:27], a[20:23], v[252:255], v[24:27]\n"
      "s_waitcnt lgkmcnt(7)\n"
      "ds_read_b128 a[20:23], v7 offset:512\n"
      "v_cvt_pk_bf16_f32 v41, v42, v43\n"
      "v_mfma_f32_16x16x32_bf16 v[56:59], a[52:55], a[76:79], 0\n"
      "ds_write_addtid_b32 v21 offset:49416\n"
      "v_cvt_pk_bf16_f32 v44, v44, v45\n"
      "v_mfma_f32_16x16x32_bf16 v[28:31], a[24:27], a[160:163], 0\n"
      "s_waitcnt lgkmcnt(7)\n"
      "v_cvt_pk_bf16_f32 v45, v46, v47\n"
      "v_cvt_pk_bf16_f32 v24, v24, v25\n"
      "v_mfma_f32_16x16x32_bf16 v[28:31], a[28:31], a[172:175], v[28:31]\n"
      "s_mov_b32 m0, s18\n"
      "v_cvt_pk_bf16_f32 v25, v26, v27\n"
      "v_sub_f32_e32 v64, v52, v56\n"
      "v_mfma_f32_16x16x32_bf16 v[28:31], a[32:35], a[184:187], v[28:31]\n"
      "s_waitcnt lgkmcnt(5)\n"
      "ds_write_addtid_b32 v48 offset:1792\n"
      "v_sub_f32_e32 v65, v53, v57\n"
      "v_mfma_f32_16x16x32_bf16 v[28:31], a[0:3], a[88:91], v[28:31]\n"
      "ds_write_addtid_b32 v49 offset:38720\n"
      "v_cvt_pk_bf16_f32 v64, v64, v65\n"
      "v_mfma_f32_16x16x32_bf16 v[28:31], a[4:7], a[100:103], v[28:31]\n"
      "s_waitcnt lgkmcnt(5)\n"
      "ds_write_addtid_b32 v40 offset:1024\n"
      "v_sub_f32_e32 v66, v54, v58\n"
      "v_mfma_f32_16x16x32_bf16 v[28:31], a[8:11], a[112:115], v[28:31]\n"
      "ds_write_addtid_b32 v41 offset:37952\n"
      "v_sub_f32_e32 v67, v55, v59\n"
      "v_mfma_f32_16x16x32_bf16 v[28:31], a[12:15], a[124:127], v[28:31]\n"
      "s_waitcnt lgkmcnt(5)\n"
      "ds_write_addtid_b32 v44 offset:256\n"
      "v_cvt_pk_bf16_f32 v65, v66, v67\n"
      "v_mfma_f32_16x16x32_bf16 v[28:31], a[16:19], a[136:139], v[28:31]\n"
      "ds_write_addtid_b32 v45 offset:37184\n"
      "v_add_f32_e32 v56, v52, v56\n"
      "v_mfma_f32_16x16x32_bf16 v[28:31], a[20:23], a[148:151], v[28:31]\n"
      "s_mov_b32 m0, s45\n"
      "v_add_f32_e32 v57, v53, v57\n"
      "v_cvt_pk_bf16_f32 v56, v56, v57\n"
      "v_mfma_f32_16x16x32_bf16 v[20:23], a[24:27], a[164:167], 0\n"
      "s_mov_b32 m0, s58\n"
      "v_add_f32_e32 v58, v54, v58\n"
      "v_add_f32_e32 v59, v55, v59\n"
      "v_mfma_f32_16x16x32_bf16 v[20:23], a[28:31], a[176:179], v[20:23]\n"
      "buffer_load_dwordx4 v18, s[32:35], s53 offen offset:2048 lds\n"
      "v_cvt_pk_bf16_f32 v57, v58, v59\n"
      "v_mfma_f32_16x16x32_bf16 v[20:23], a[32:35], a[188:191], v[20:23]\n"
      "s_mov_b32 m0, s45\n"
      "ds_read2_b64 a[68:71], v12 offset0:1 offset1:65\n"
      "v_cvt_pk_bf16_f32 v60, v60, v61\n"
      "v_mfma_f32_16x16x32_bf16 v[20:23], a[0:3], a[92:95], v[20:23]\n"
      "ds_write_addtid_b32 v24 offset:36864\n"
      "v_cvt_pk_bf16_f32 v61, v62, v63\n"
      "v_mfma_f32_16x16x32_bf16 v[20:23], a[4:7], a[104:107], v[20:23]\n"
      "ds_write_addtid_b32 v25 offset:37120\n"
      "v_cvt_pk_bf16_f32 v28, v28, v29\n"
      "v_mfma_f32_16x16x32_bf16 v[20:23], a[8:11], a[116:119], v[20:23]\n"
      "s_mov_b32 m0, s18\n"
      "v_cvt_pk_bf16_f32 v29, v30, v31\n"
      "v_cvt_pk_bf16_f32 v82, v76, v77\n"
      "v_mfma_f32_16x16x32_bf16 v[20:23], a[12:15], a[128:131], v[20:23]\n"
      "ds_write_addtid_b32 v64 offset:2048\n"
      "v_cvt_pk_bf16_f32 v88, v78, v79\n"
      "v_mfma_f32_16x16x32_bf16 v[20:23], a[16:19], a[140:143], v[20:23]\n"
      "ds_write_addtid_b32 v65 offset:38976\n"
      "v_swap_b32 v6, v7\n"
      "v_mfma_f32_16x16x32_bf16 v[20:23], a[20:23], a[152:155], v[20:23]\n"
      "ds_write_addtid_b32 v56 offset:1280\n"
      "s_nop 0\n"
      "v_mfma_f32_16x16x32_bf16 v[68:71], a[84:87], a[60:63], 0\n"
      "ds_write_addtid_b32 v57 offset:38208\n"
      "s_nop 0\n"
      "v_mfma_f32_16x16x32_bf16 v[32:35], a[24:27], a[168:171], 0\n"
      "ds_write_addtid_b32 v60 offset:512\n"
      "v_mfma_f32_16x16x32_bf16 v[32:35], a[28:31], a[180:183], v[32:35]\n"
      "ds_write_addtid_b32 v61 offset:37440\n"
      "v_cvt_pk_bf16_f32 v20, v20, v21\n"
      "v_mfma_f32_16x16x32_bf16 v[32:35], a[32:35], a[192:195], v[32:35]\n"
      "s_mov_b32 m0, s45\n"
      "v_cvt_pk_bf16_f32 v83, v68, v69\n"
      "v_cvt_pk_bf16_f32 v21, v22, v23\n"
      "v_mfma_f32_16x16x32_bf16 v[32:35], a[0:3], a[96:99], v[32:35]\n"
      "ds_write_addtid_b32 v28 offset:60432\n"
      "v_cvt_pk_bf16_f32 v89, v70, v71\n"
      "v_mfma_f32_16x16x32_bf16 v[32:35], a[4:7], a[108:111], v[32:35]\n"
      "ds_write_addtid_b32 v29 offset:60688\n"
      "v_mfma_f32_16x16x32_bf16 v[32:35], a[8:11], a[120:123], v[32:35]\n"
      "ds_write_addtid_b32 v20 offset:48136\n"
      "v_mfma_f32_16x16x32_bf16 v[32:35], a[12:15], a[132:135], v[32:35]\n"
      "buffer_store_dwordx4 v[80:83], v14, s[36:39], s52 offen sc1\n"
      "v_mfma_f32_16x16x32_bf16 v[32:35], a[16:19], a[144:147], v[32:35]\n"
      "ds_write_addtid_b32 v21 offset:48392\n"
      "v_mfma_f32_16x16x32_bf16 v[32:35], a[20:23], a[156:159], v[32:35]\n"
      "s_nop 1\n"
      "v_mfma_f32_16x16x32_bf16 v[72:75], a[84:87], a[64:67], 0\n"
      "s_waitcnt lgkmcnt(12)\n"
      "s_nop 1\n"
      "v_mfma_f32_16x16x32_bf16 v[76:79], a[84:87], a[68:71], 0\n"
      "buffer_store_dwordx4 v[86:89], v14, s[36:39], s52 offen offset:1536 sc1\n"
      "v_cvt_pk_bf16_f32 v32, v32, v33\n"
      "v_mfma_f32_16x16x32_bf16 v[20:23], a[12:15], a[88:91], 0\n"
      "s_add_u32 s51, s51, s12\n"
      "ds_write_addtid_b32 v32 offset:35840\n"
      "v_cvt_pk_bf16_f32 v33, v34, v35\n"
      "v_mfma_f32_16x16x32_bf16 v[20:23], a[16:19], a[100:103], v[20:23]\n"
      "s_sub_u32 s47, s51, s48\n"
      "ds_write_addtid_b32 v33 offset:36096\n"
      "v_cvt_pk_bf16_f32 v84, v72, v73\n"
      "v_mfma_f32_16x16x32_bf16 v[20:23], a[20:23], a[112:115], v[20:23]\n"
      "s_cmp_ge_u32 vcc_hi, 0x40000000\n"
      "v_cvt_pk_bf16_f32 v85, v76, v77\n"
      "v_cvt_pk_bf16_f32 v90, v74, v75\n"
      "v_mfma_f32_16x16x32_bf16 v[20:23], a[24:27], a[124:127], v[20:23]\n"
      "buffer_store_dwordx2 v[84:85], v15, s[36:39], s52 offen sc1\n"
      "v_cvt_pk_bf16_f32 v91, v78, v79\n"
      "v_mfma_f32_16x16x32_bf16 v[20:23], a[28:31], a[136:139], v[20:23]\n"
      "s_cselect_b32 s53, s47, s51\n"
      "v_mfma_f32_16x16x32_bf16 v[20:23], a[32:35], a[148:151], v[20:23]\n"
      "s_lshl_b64 vcc, vcc, 1\n"
      "v_mfma_f32_16x16x32_bf16 v[24:27], a[12:15], a[92:95], 0\n"
      "buffer_store_dwordx2 v[90:91], v15, s[36:39], s52 offen offset:1536 sc1\n"
      "v_mfma_f32_16x16x32_bf16 v[24:27], a[16:19], a[104:107], v[24:27]\n"
      "s_mov_b32 s52, s13\n"
      "v_mfma_f32_16x16x32_bf16 v[24:27], a[20:23], a[116:119], v[24:27]\n"
      "v_mfma_f32_16x16x32_bf16 v[24:27], a[24:27], a[128:131], v[24:27]\n"
      "v_mfma_f32_16x16x32_bf16 v[24:27], a[28:31], a[140:143], v[24:27]\n"
      "s_waitcnt vmcnt(12) lgkmcnt(0)\n"
      "s_barrier\n"
      "v_mfma_f32_16x16x32_bf16 v[24:27], a[32:35], a[152:155], v[24:27]\n"
      "s_add_u32 m0, s58, 13280\n"
      "ds_read2_b64 a[60:63], v11 offset0:0 offset1:64\n"
      "v_mfma_f32_16x16x32_bf16 v[28:31], a[12:15], a[96:99], 0\n"
      "s_add_u32 s13, s13, s12\n"
      "ds_read_b128 a[0:3], v7 offset:0\n"
      "v_mfma_f32_16x16x32_bf16 v[28:31], a[16:19], a[108:111], v[28:31]\n"
      "ds_read_b128 a[4:7], v7 offset:256\n"
      "v_mfma_f32_16x16x32_bf16 v[28:31], a[20:23], a[120:123], v[28:31]\n"
      "ds_read_b128 a[8:11], v7 offset:512\n"
      "v_mfma_f32_16x16x32_bf16 v[28:31], a[24:27], a[132:135], v[28:31]\n"
      "ds_read_b128 a[36:39], v2 offset:0\n"
      "v_mfma_f32_16x16x32_bf16 v[28:31], a[28:31], a[144:147], v[28:31]\n"
      "ds_read_b128 a[44:47], v4 offset:0\n"
      "v_mfma_f32_16x16x32_bf16 v[28:31], a[32:35], a[156:159], v[28:31]\n"
      "s_waitcnt lgkmcnt(5)\n"
      "ds_read_b128 a[40:43], v3 offset:0\n"
      "v_mfma_f32_16x16x32_bf16 v[68:71], a[84:87], a[60:63], 0\n"
      "s_waitcnt lgkmcnt(5)\n"
      "ds_read_b128 a[12:15], v6 offset:768\n"
      "v_mfma_f32_16x16x32_bf16 v[20:23], a[0:3], a[160:163], v[20:23]\n"
      "s_waitcnt lgkmcnt(5)\n"
      "ds_read_b128 a[16:19], v6 offset:1024\n"
      "v_mfma_f32_16x16x32_bf16 v[20:23], a[4:7], a[172:175], v[20:23]\n"
      "s_waitcnt lgkmcnt(5)\n"
      "ds_read_b128 a[20:23], v6 offset:1280\n"
      "v_cvt_pk_bf16_f32 v80, v68, v69\n"
      "v_mfma_f32_16x16x32_bf16 v[20:23], a[8:11], a[184:187], v[20:23]\n"
      "ds_read_b128 a[24:27], v6 offset:5376\n"
      "v_cvt_pk_bf16_f32 v86, v70, v71\n"
      "v_mfma_f32_16x16x32_bf16 v[24:27], a[0:3], a[164:167], v[24:27]\n"
      "buffer_load_dwordx4 v16, s[32:35], s53 offen lds\n"
      "s_nop 0\n"
      "v_mfma_f32_16x16x32_bf16 v[24:27], a[4:7], a[176:179], v[24:27]\n"
      "s_mov_b32 m0, s45\n"
      "ds_read_b128 a[28:31], v6 offset:5632\n"
      "v_cvt_pk_bf16_f32 v20, v20, v21\n"
      "v_mfma_f32_16x16x32_bf16 v[24:27], a[8:11], a[188:191], v[24:27]\n"
      "s_waitcnt lgkmcnt(6)\n"
      "ds_read_b128 a[32:35], v6 offset:5888\n"
      "v_cvt_pk_bf16_f32 v21, v22, v23\n"
      "v_mfma_f32_16x16x32_bf16 v[36:39], a[36:39], a[72:75], 0\n"
      "ds_read2_b64 a[64:67], v11 offset0:1 offset1:65\n"
      "s_nop 0\n"
      "v_mfma_f32_16x16x32_bf16 v[44:47], a[44:47], a[80:83], v[36:39]\n"
      "ds_write_addtid_b32 v20 offset:57360\n"
      "v_cvt_pk_bf16_f32 v24, v24, v25\n"
      "v_mfma_f32_16x16x32_bf16 v[28:31], a[0:3], a[168:171], v[28:31]\n"
      "ds_read_b128 a[0:3], v7 offset:768\n"
      "v_cvt_pk_bf16_f32 v25, v26, v27\n"
      "v_mfma_f32_16x16x32_bf16 v[28:31], a[4:7], a[180:183], v[28:31]\n"
      "ds_read_b128 a[4:7], v7 offset:1024\n"
      "v_mfma_f32_16x16x32_bf16 v[28:31], a[8:11], a[192:195], v[28:31]\n"
      "s_waitcnt lgkmcnt(6)\n"
      "ds_read_b128 a[8:11], v7 offset:1280\n"
      "v_cvt_pk_bf16_f32 v44, v44, v45\n"
      "v_mfma_f32_16x16x32_bf16 v[40:43], a[40:43], a[76:79], 0\n"
      "ds_write_addtid_b32 v21 offset:57616\n"
      "v_cvt_pk_bf16_f32 v45, v46, v47\n"
      "v_mfma_f32_16x16x32_bf16 v[32:35], a[12:15], v[92:95], 0\n"
      "ds_read_b128 a[48:51], v2 offset:64\n"
      "v_cvt_pk_bf16_f32 v28, v28, v29\n"
      "v_mfma_f32_16x16x32_bf16 v[32:35], a[16:19], v[148:151], v[32:35]\n"
      "ds_write_addtid_b32 v24 offset:45064\n"
      "v_cvt_pk_bf16_f32 v29, v30, v31\n"
      "v_mfma_f32_16x16x32_bf16 v[32:35], a[20:23], v[204:207], v[32:35]\n"
      "ds_read_b128 a[52:55], v3 offset:64\n"
      "v_sub_f32_e32 v48, v36, v40\n"
      "v_mfma_f32_16x16x32_bf16 v[32:35], a[24:27], v[112:115], v[32:35]\n"
      "s_waitcnt lgkmcnt(8)\n"
      "ds_write_addtid_b32 v25 offset:45320\n"
      "v_sub_f32_e32 v49, v37, v41\n"
      "v_mfma_f32_16x16x32_bf16 v[32:35], a[28:31], a[208:211], v[32:35]\n"
      "ds_read_b128 a[56:59], v4 offset:64\n"
      "v_sub_f32_e32 v50, v38, v42\n"
      "v_mfma_f32_16x16x32_bf16 v[32:35], a[32:35], v[220:223], v[32:35]\n"
      "s_waitcnt lgkmcnt(7)\n"
      "ds_read_b128 a[36:39], v2 offset:128\n"
      "v_sub_f32_e32 v51, v39, v43\n"
      "v_mfma_f32_16x16x32_bf16 v[32:35], a[0:3], a[212:215], v[32:35]\n"
      "ds_write_addtid_b32 v28 offset:32768\n"
      "v_add_f32_e32 v40, v36, v40\n"
      "v_mfma_f32_16x16x32_bf16 v[32:35], a[4:7], v[184:187], v[32:35]\n"
      "s_waitcnt lgkmcnt(8)\n"
      "ds_write_addtid_b32 v29 offset:33024\n"
      "v_add_f32_e32 v41, v37, v41\n"
      "v_mfma_f32_16x16x32_bf16 v[32:35], a[8:11], v[240:243], v[32:35]\n"
      "s_mov_b32 m0, s18\n"
      "ds_read_b128 a[44:47], v4 offset:128\n"
      "v_add_f32_e32 v42, v38, v42\n"
      "v_mfma_f32_16x16x32_bf16 v[72:75], a[84:87], a[64:67], 0\n"
      "ds_read2_b64 a[68:71], v11 offset0:2 offset1:66\n"
      "v_add_f32_e32 v43, v39, v43\n"
      "v_mfma_f32_16x16x32_bf16 v[20:23], a[12:15], v[100:103], 0\n"
      "v_cvt_pk_bf16_f32 v48, v48, v49\n"
      "v_cvt_pk_bf16_f32 v49, v50, v51\n"
      "v_mfma_f32_16x16x32_bf16 v[20:23], a[16:19], a[200:203], v[20:23]\n"
      "ds_write_addtid_b32 v48 offset:6144\n"
      "v_cvt_pk_bf16_f32 v40, v40, v41\n"
      "v_mfma_f32_16x16x32_bf16 v[20:23], a[20:23], v[208:211], v[20:23]\n"
      "ds_write_addtid_b32 v49 offset:43072\n"
      "v_cvt_pk_bf16_f32 v41, v42, v43\n"
      "v_mfma_f32_16x16x32_bf16 v[20:23], a[24:27], a[204:207], v[20:23]\n"
      "ds_write_addtid_b32 v40 offset:5376\n"
      "v_cvt_pk_bf16_f32 v32, v32, v33\n"
      "v_mfma_f32_16x16x32_bf16 v[20:23], a[28:31], v[172:175], v[20:23]\n"
      "ds_write_addtid_b32 v41 offset:42304\n"
      "v_cvt_pk_bf16_f32 v33, v34, v35\n"
      "v_mfma_f32_16x16x32_bf16 v[20:23], a[32:35], v[228:231], v[20:23]\n"
      "ds_write_addtid_b32 v44 offset:4608\n"
      "v_cvt_pk_bf16_f32 v81, v72, v73\n"
      "v_mfma_f32_16x16x32_bf16 v[20:23], a[0:3], v[136:139], v[20:23]\n"
      "ds_write_addtid_b32 v45 offset:41536\n"
      "v_cvt_pk_bf16_f32 v87, v74, v75\n"
      "v_mfma_f32_16x16x32_bf16 v[20:23], a[4:7], v[192:195], v[20:23]\n"
      "s_mov_b32 m0, s45\n"
      "ds_read_b128 a[40:43], v3 offset:128\n"
      "v_mfma_f32_16x16x32_bf16 v[20:23], a[8:11], v[244:247], v[20:23]\n"
      "s_waitcnt lgkmcnt(7)\n"
      "ds_read2_b64 a[60:63], v11 offset0:3 offset1:67\n"
      "s_nop 0\n"
      "v_mfma_f32_16x16x32_bf16 v[52:55], a[48:51], a[72:75], 0\n"
      "s_add_u32 m0, s58, 13280\n"
      "ds_read2_b64 a[64:67], v13 offset0:0 offset1:64\n"
      "s_nop 0\n"
      "v_mfma_f32_16x16x32_bf16 v[56:59], a[52:55], a[76:79], 0\n"
      "buffer_load_dwordx4 v17, s[32:35], s53 offen offset:1024 lds\n"
      "v_cvt_pk_bf16_f32 v20, v20, v21\n"
      "v_mfma_f32_16x16x32_bf16 v[60:63], a[56:59], a[80:83], v[52:55]\n"
      "s_mov_b32 m0, s45\n"
      "v_cvt_pk_bf16_f32 v21, v22, v23\n"
      "s_nop 0\n"
      "v_mfma_f32_16x16x32_bf16 v[36:39], a[36:39], a[72:75], 0\n"
      "ds_write_addtid_b32 v32 offset:58384\n"
      "v_sub_f32_e32 v64, v52, v56\n"
      "v_mfma_f32_16x16x32_bf16 v[44:47], a[44:47], a[80:83], v[36:39]\n"
      "ds_write_addtid_b32 v33 offset:58640\n"
      "v_sub_f32_e32 v65, v53, v57\n"
      "v_mfma_f32_16x16x32_bf16 v[76:79], a[84:87], a[68:71], 0\n"
      "global_load_dword v8, v9, s[54:55]\n"
      "v_sub_f32_e32 v66, v54, v58\n"
      "v_mfma_f32_16x16x32_bf16 v[24:27], a[12:15], a[196:199], 0\n"
      "ds_read_b128 a[12:15], v6 offset:1536\n"
      "v_sub_f32_e32 v67, v55, v59\n"
      "v_mfma_f32_16x16x32_bf16 v[24:27], a[16:19], v[160:163], v[24:27]\n"
      "ds_read_b128 a[16:19], v6 offset:1792\n"
      "v_add_f32_e32 v56, v52, v56\n"
      "v_mfma_f32_16x16x32_bf16 v[24:27], a[20:23], v[216:219], v[24:27]\n"
      "ds_read_b128 a[20:23], v6 offset:2048\n"
      "v_add_f32_e32 v57, v53, v57\n"
      "v_mfma_f32_16x16x32_bf16 v[24:27], a[24:27], v[124:127], v[24:27]\n"
      "ds_read_b128 a[24:27], v6 offset:6144\n"
      "v_add_f32_e32 v58, v54, v58\n"
      "v_mfma_f32_16x16x32_bf16 v[24:27], a[28:31], v[180:183], v[24:27]\n"
      "ds_read_b128 a[28:31], v6 offset:6400\n"
      "v_add_f32_e32 v59, v55, v59\n"
      "v_mfma_f32_16x16x32_bf16 v[24:27], a[32:35], v[232:235], v[24:27]\n"
      "ds_read_b128 a[32:35], v6 offset:6656\n"
      "v_cvt_pk_bf16_f32 v64, v64, v65\n"
      "v_mfma_f32_16x16x32_bf16 v[24:27], a[0:3], a[216:219], v[24:27]\n"
      "ds_read_b128 a[0:3], v7 offset:1536\n"
      "v_cvt_pk_bf16_f32 v65, v66, v67\n"
      "v_mfma_f32_16x16x32_bf16 v[24:27], a[4:7], v[196:199], v[24:27]\n"
      "ds_read_b128 a[4:7], v7 offset:1792\n"
      "v_cvt_pk_bf16_f32 v56, v56, v57\n"
      "v_mfma_f32_16x16x32_bf16 v[24:27], a[8:11], v[252:255], v[24:27]\n"
      "s_waitcnt lgkmcnt(7)\n"
      "ds_read_b128 a[8:11], v7 offset:2048\n"
      "v_cvt_pk_bf16_f32 v57, v58, v59\n"
      "v_mfma_f32_16x16x32_bf16 v[40:43], a[40:43], a[76:79], 0\n"
      "ds_write_addtid_b32 v20 offset:46088\n"
      "v_cvt_pk_bf16_f32 v60, v60, v61\n"
      "v_mfma_f32_16x16x32_bf16 v[28:31], a[12:15], v[96:99], 0\n"
      "s_waitcnt lgkmcnt(7)\n"
      "ds_write_addtid_b32 v21 offset:46344\n"
      "v_cvt_pk_bf16_f32 v61, v62, v63\n"
      "v_mfma_f32_16x16x32_bf16 v[28:31], a[16:19], v[152:155], v[28:31]\n"
      "s_mov_b32 m0, s18\n"
      "v_cvt_pk_bf16_f32 v24, v24, v25\n"
      "v_cvt_pk_bf16_f32 v25, v26, v27\n"
      "v_mfma_f32_16x16x32_bf16 v[28:31], a[20:23], a[220:223], v[28:31]\n"
      "s_waitcnt lgkmcnt(6)\n"
      "ds_write_addtid_b32 v64 offset:6400\n"
      "v_sub_f32_e32 v48, v36, v40\n"
      "v_mfma_f32_16x16x32_bf16 v[28:31], a[24:27], v[116:119], v[28:31]\n"
      "ds_write_addtid_b32 v65 offset:43328\n"
      "v_sub_f32_e32 v49, v37, v41\n"
      "v_mfma_f32_16x16x32_bf16 v[28:31], a[28:31], v[168:171], v[28:31]\n"
      "s_waitcnt lgkmcnt(6)\n"
      "ds_write_addtid_b32 v56 offset:5632\n"
      "v_cvt_pk_bf16_f32 v48, v48, v49\n"
      "v_mfma_f32_16x16x32_bf16 v[28:31], a[32:35], v[224:227], v[28:31]\n"
      "ds_write_addtid_b32 v57 offset:42560\n"
      "v_sub_f32_e32 v50, v38, v42\n"
      "v_mfma_f32_16x16x32_bf16 v[28:31], a[0:3], v[132:135], v[28:31]\n"
      "s_waitcnt lgkmcnt(6)\n"
      "ds_write_addtid_b32 v60 offset:4864\n"
      "v_sub_f32_e32 v51, v39, v43\n"
      "v_mfma_f32_16x16x32_bf16 v[28:31], a[4:7], v[188:191], v[28:31]\n"
      "ds_write_addtid_b32 v61 offset:41792\n"
      "v_cvt_pk_bf16_f32 v49, v50, v51\n"
      "v_mfma_f32_16x16x32_bf16 v[28:31], a[8:11], a[240:243], v[28:31]\n"
      "s_mov_b32 m0, s45\n"
      "v_add_f32_e32 v40, v36, v40\n"
      "v_add_f32_e32 v41, v37, v41\n"
      "v_mfma_f32_16x16x32_bf16 v[20:23], a[12:15], v[104:107], 0\n"
      "s_add_u32 m0, s58, 13280\n"
      "v_cvt_pk_bf16_f32 v40, v40, v41\n"
      "v_add_f32_e32 v42, v38, v42\n"
      "v_mfma_f32_16x16x32_bf16 v[20:23], a[16:19], v[156:159], v[20:23]\n"
      "buffer_load_dwordx4 v18, s[32:35], s53 offen offset:2048 lds\n"
      "v_add_f32_e32 v43, v39, v43\n"
      "v_mfma_f32_16x16x32_bf16 v[20:23], a[20:23], v[212:215], v[20:23]\n"
      "s_mov_b32 m0, s45\n"
      "v_cvt_pk_bf16_f32 v41, v42, v43\n"
      "v_cvt_pk_bf16_f32 v44, v44, v45\n"
      "v_mfma_f32_16x16x32_bf16 v[20:23], a[24:27], v[120:123], v[20:23]\n"
      "ds_write_addtid_b32 v24 offset:33792\n"
      "v_cvt_pk_bf16_f32 v45, v46, v47\n"
      "v_mfma_f32_16x16x32_bf16 v[20:23], a[28:31], v[176:179], v[20:23]\n"
      "ds_write_addtid_b32 v25 offset:34048\n"
      "v_cvt_pk_bf16_f32 v28, v28, v29\n"
      "v_mfma_f32_16x16x32_bf16 v[20:23], a[32:35], a[232:235], v[20:23]\n"
      "s_mov_b32 m0, s18\n"
      "ds_read2_b64 a[68:71], v13 offset0:1 offset1:65\n"
      "v_cvt_pk_bf16_f32 v29, v30, v31\n"
      "v_mfma_f32_16x16x32_bf16 v[20:23], a[0:3], v[140:143], v[20:23]\n"
      "s_sub_u32 s18, s19, s18\n"
      "ds_write_addtid_b32 v48 offset:6656\n"
      "v_cvt_pk_bf16_f32 v82, v76, v77\n"
      "v_mfma_f32_16x16x32_bf16 v[20:23], a[4:7], a[236:239], v[20:23]\n"
      "ds_write_addtid_b32 v49 offset:43584\n"
      "v_cvt_pk_bf16_f32 v88, v78, v79\n"
      "v_mfma_f32_16x16x32_bf16 v[20:23], a[8:11], v[248:251], v[20:23]\n"
      "ds_write_addtid_b32 v40 offset:5888\n"
      "s_nop 0\n"
      "v_mfma_f32_16x16x32_bf16 v[68:71], a[84:87], a[60:63], 0\n"
      "ds_write_addtid_b32 v41 offset:42816\n"
      "s_nop 0\n"
      "v_mfma_f32_16x16x32_bf16 v[32:35], a[12:15], v[108:111], 0\n"
      "ds_write_addtid_b32 v44 offset:5120\n"
      "v_mfma_f32_16x16x32_bf16 v[32:35], a[16:19], v[164:167], v[32:35]\n"
      "ds_write_addtid_b32 v45 offset:42048\n"
      "v_cvt_pk_bf16_f32 v20, v20, v21\n"
      "v_mfma_f32_16x16x32_bf16 v[32:35], a[20:23], a[224:227], v[32:35]\n"
      "s_mov_b32 m0, s45\n"
      "v_cvt_pk_bf16_f32 v83, v68, v69\n"
      "v_cvt_pk_bf16_f32 v21, v22, v23\n"
      "v_mfma_f32_16x16x32_bf16 v[32:35], a[24:27], v[128:131], v[32:35]\n"
      "ds_write_addtid_b32 v28 offset:59408\n"
      "v_cvt_pk_bf16_f32 v89, v70, v71\n"
      "v_mfma_f32_16x16x32_bf16 v[32:35], a[28:31], a[228:231], v[32:35]\n"
      "ds_write_addtid_b32 v29 offset:59664\n"
      "v_mfma_f32_16x16x32_bf16 v[32:35], a[32:35], v[236:239], v[32:35]\n"
      "ds_write_addtid_b32 v20 offset:47112\n"
      "v_mfma_f32_16x16x32_bf16 v[32:35], a[0:3], v[144:147], v[32:35]\n"
      "buffer_store_dwordx4 v[80:83], v14, s[36:39], s52 offen sc1\n"
      "v_mfma_f32_16x16x32_bf16 v[32:35], a[4:7], v[200:203], v[32:35]\n"
      "ds_write_addtid_b32 v21 offset:47368\n"
      "v_mfma_f32_16x16x32_bf16 v[32:35], a[8:11], a[244:247], v[32:35]\n"
      "s_nop 1\n"
      "v_mfma_f32_16x16x32_bf16 v[72:75], a[84:87], a[64:67], 0\n"
      "s_waitcnt lgkmcnt(10)\n"
      "s_nop 1\n"
      "v_mfma_f32_16x16x32_bf16 v[76:79], a[84:87], a[68:71], 0\n"
      "buffer_store_dwordx4 v[86:89], v14, s[36:39], s52 offen offset:1536 sc1\n"
      "v_cvt_pk_bf16_f32 v32, v32, v33\n"
      "s_cbranch_vccnz Lconv_loop1\n"
      "Lconv_t1:\n"
      "ds_write_addtid_b32 v32 offset:34816\n"
      "v_cvt_pk_bf16_f32 v33, v34, v35\n"
      "v_mfma_f32_16x16x32_bf16 v[20:23], a[24:27], v[96:99], 0\n"
      "ds_write_addtid_b32 v33 offset:35072\n"
      "v_cvt_pk_bf16_f32 v84, v72, v73\n"
      "v_mfma_f32_16x16x32_bf16 v[20:23], a[28:31], v[152:155], v[20:23]\n"
      "v_cvt_pk_bf16_f32 v90, v74, v75\n"
      "v_cvt_pk_bf16_f32 v85, v76, v77\n"
      "v_mfma_f32_16x16x32_bf16 v[20:23], a[32:35], a[220:223], v[20:23]\n"
      "buffer_store_dwordx2 v[84:85], v15, s[36:39], s52 offen sc1\n"
      "v_cvt_pk_bf16_f32 v91, v78, v79\n"
      "v_mfma_f32_16x16x32_bf16 v[20:23], a[0:3], v[116:119], v[20:23]\n"
      "v_mfma_f32_16x16x32_bf16 v[20:23], a[4:7], v[168:171], v[20:23]\n"
      "v_mfma_f32_16x16x32_bf16 v[20:23], a[8:11], v[224:227], v[20:23]\n"
      "buffer_store_dwordx2 v[90:91], v15, s[36:39], s52 offen offset:1536 sc1\n"
      "v_mfma_f32_16x16x32_bf16 v[24:27], a[24:27], v[104:107], 0\n"
      "s_mov_b32 s52, s13\n"
      "v_mfma_f32_16x16x32_bf16 v[24:27], a[28:31], v[156:159], v[24:27]\n"
      "s_add_u32 s13, s13, s12\n"
      "v_mfma_f32_16x16x32_bf16 v[24:27], a[32:35], v[212:215], v[24:27]\n"
      "v_mfma_f32_16x16x32_bf16 v[24:27], a[0:3], v[120:123], v[24:27]\n"
      "v_mfma_f32_16x16x32_bf16 v[24:27], a[4:7], v[176:179], v[24:27]\n"
      "s_waitcnt lgkmcnt(0)\n"
      "s_barrier\n"
      "ds_read2_b64 a[60:63], v10 offset0:0 offset1:64\n"
      "v_mfma_f32_16x16x32_bf16 v[24:27], a[8:11], a[232:235], v[24:27]\n"
      "s_waitcnt lgkmcnt(0)\n"
      "ds_read2_b64 a[64:67], v10 offset0:1 offset1:65\n"
      "v_mfma_f32_16x16x32_bf16 v[68:71], a[84:87], a[60:63], 0\n"
      "ds_read2_b64 a[68:71], v10 offset0:2 offset1:66\n"
      "v_mfma_f32_16x16x32_bf16 v[28:31], a[24:27], v[108:111], 0\n"
      "ds_read_b128 a[12:15], v7 offset:6144\n"
      "v_mfma_f32_16x16x32_bf16 v[28:31], a[28:31], v[164:167], v[28:31]\n"
      "ds_read_b128 a[16:19], v7 offset:6400\n"
      "v_mfma_f32_16x16x32_bf16 v[28:31], a[32:35], a[224:227], v[28:31]\n"
      "ds_read_b128 a[20:23], v7 offset:6656\n"
      "v_mfma_f32_16x16x32_bf16 v[28:31], a[0:3], v[128:131], v[28:31]\n"
      "ds_read2_b64 a[60:63], v10 offset0:3 offset1:67\n"
      "v_cvt_pk_bf16_f32 v80, v68, v69\n"
      "v_mfma_f32_16x16x32_bf16 v[28:31], a[4:7], a[228:231], v[28:31]\n"
      "ds_read_b128 a[24:27], v6 offset:5376\n"
      "v_cvt_pk_bf16_f32 v86, v70, v71\n"
      "v_mfma_f32_16x16x32_bf16 v[28:31], a[8:11], v[236:239], v[28:31]\n"
      "s_waitcnt lgkmcnt(6)\n"
      "ds_read_b128 a[28:31], v6 offset:5632\n"
      "v_mfma_f32_16x16x32_bf16 v[72:75], a[84:87], a[64:67], 0\n"
      "s_waitcnt lgkmcnt(6)\n"
      "ds_read2_b64 a[64:67], v12 offset0:0 offset1:64\n"
      "v_mfma_f32_16x16x32_bf16 v[76:79], a[84:87], a[68:71], 0\n"
      "s_waitcnt lgkmcnt(6)\n"
      "ds_read2_b64 a[68:71], v12 offset0:1 offset1:65\n"
      "s_nop 0\n"
      "v_mfma_f32_16x16x32_bf16 v[20:23], a[12:15], v[132:135], v[20:23]\n"
      "s_waitcnt lgkmcnt(6)\n"
      "ds_read_b128 a[32:35], v6 offset:5888\n"
      "v_cvt_pk_bf16_f32 v81, v72, v73\n"
      "v_mfma_f32_16x16x32_bf16 v[20:23], a[16:19], v[188:191], v[20:23]\n"
      "s_waitcnt lgkmcnt(6)\n"
      "ds_read_b128 a[0:3], v7 offset:768\n"
      "v_cvt_pk_bf16_f32 v82, v76, v77\n"
      "v_mfma_f32_16x16x32_bf16 v[20:23], a[20:23], a[240:243], v[20:23]\n"
      "s_waitcnt lgkmcnt(6)\n"
      "ds_read_b128 a[4:7], v7 offset:1024\n"
      "v_cvt_pk_bf16_f32 v87, v74, v75\n"
      "v_mfma_f32_16x16x32_bf16 v[68:71], a[84:87], a[60:63], 0\n"
      "ds_read_b128 a[8:11], v7 offset:1280\n"
      "v_cvt_pk_bf16_f32 v88, v78, v79\n"
      "v_mfma_f32_16x16x32_bf16 v[24:27], a[12:15], v[140:143], v[24:27]\n"
      "v_mfma_f32_16x16x32_bf16 v[24:27], a[16:19], a[236:239], v[24:27]\n"
      "v_cvt_pk_bf16_f32 v20, v20, v21\n"
      "v_cvt_pk_bf16_f32 v21, v22, v23\n"
      "v_mfma_f32_16x16x32_bf16 v[24:27], a[20:23], v[248:251], v[24:27]\n"
      "s_waitcnt lgkmcnt(5)\n"
      "ds_write_addtid_b32 v20 offset:62480\n"
      "v_cvt_pk_bf16_f32 v83, v68, v69\n"
      "v_mfma_f32_16x16x32_bf16 v[72:75], a[84:87], a[64:67], 0\n"
      "buffer_store_dwordx4 v[80:83], v14, s[36:39], s52 offen sc1\n"
      "v_cvt_pk_bf16_f32 v89, v70, v71\n"
      "v_mfma_f32_16x16x32_bf16 v[28:31], a[12:15], v[144:147], v[28:31]\n"
      "ds_read_b128 a[12:15], v7 offset:5376\n"
      "v_cvt_pk_bf16_f32 v24, v24, v25\n"
      "v_mfma_f32_16x16x32_bf16 v[28:31], a[16:19], v[200:203], v[28:31]\n"
      "ds_read_b128 a[16:19], v7 offset:5632\n"
      "v_cvt_pk_bf16_f32 v25, v26, v27\n"
      "v_mfma_f32_16x16x32_bf16 v[28:31], a[20:23], a[244:247], v[28:31]\n"
      "s_waitcnt lgkmcnt(4)\n"
      "ds_read_b128 a[20:23], v7 offset:5888\n"
      "v_cvt_pk_bf16_f32 v84, v72, v73\n"
      "v_mfma_f32_16x16x32_bf16 v[76:79], a[84:87], a[68:71], 0\n"
      "ds_write_addtid_b32 v21 offset:62736\n"
      "v_cvt_pk_bf16_f32 v90, v74, v75\n"
      "v_mfma_f32_16x16x32_bf16 v[32:35], a[24:27], v[92:95], 0\n"
      "buffer_store_dwordx4 v[86:89], v14, s[36:39], s52 offen offset:1536 sc1\n"
      "v_cvt_pk_bf16_f32 v28, v28, v29\n"
      "v_mfma_f32_16x16x32_bf16 v[32:35], a[28:31], v[148:151], v[32:35]\n"
      "ds_write_addtid_b32 v24 offset:50184\n"
      "v_cvt_pk_bf16_f32 v29, v30, v31\n"
      "v_mfma_f32_16x16x32_bf16 v[32:35], a[32:35], v[204:207], v[32:35]\n"
      "ds_write_addtid_b32 v25 offset:50440\n"
      "v_cvt_pk_bf16_f32 v85, v76, v77\n"
      "v_mfma_f32_16x16x32_bf16 v[32:35], a[0:3], v[112:115], v[32:35]\n"
      "ds_write_addtid_b32 v28 offset:37888\n"
      "v_cvt_pk_bf16_f32 v91, v78, v79\n"
      "v_mfma_f32_16x16x32_bf16 v[32:35], a[4:7], a[208:211], v[32:35]\n"
      "s_waitcnt lgkmcnt(6)\n"
      "ds_write_addtid_b32 v29 offset:38144\n"
      "v_mfma_f32_16x16x32_bf16 v[32:35], a[8:11], v[220:223], v[32:35]\n"
      "buffer_store_dwordx2 v[84:85], v15, s[36:39], s52 offen sc1\n"
      "v_mfma_f32_16x16x32_bf16 v[32:35], a[12:15], a[212:215], v[32:35]\n"
      "s_waitcnt lgkmcnt(5)\n"
      "v_mfma_f32_16x16x32_bf16 v[32:35], a[16:19], v[184:187], v[32:35]\n"
      "v_mfma_f32_16x16x32_bf16 v[32:35], a[20:23], v[240:243], v[32:35]\n"
      "buffer_store_dwordx2 v[90:91], v15, s[36:39], s52 offen offset:1536 sc1\n"
      "s_nop 0\n"
      "v_mfma_f32_16x16x32_bf16 v[20:23], a[24:27], v[100:103], 0\n"
      "s_nop 1\n"
      "v_mfma_f32_16x16x32_bf16 v[20:23], a[28:31], a[200:203], v[20:23]\n"
      "s_nop 0\n"
      "v_mfma_f32_16x16x32_bf16 v[20:23], a[32:35], v[208:211], v[20:23]\n"
      "v_cvt_pk_bf16_f32 v32, v32, v33\n"
      "v_cvt_pk_bf16_f32 v33, v34, v35\n"
      "v_mfma_f32_16x16x32_bf16 v[20:23], a[0:3], a[204:207], v[20:23]\n"
      "ds_write_addtid_b32 v32 offset:61456\n"
      "v_mfma_f32_16x16x32_bf16 v[20:23], a[4:7], v[172:175], v[20:23]\n"
      "ds_write_addtid_b32 v33 offset:61712\n"
      "v_mfma_f32_16x16x32_bf16 v[20:23], a[8:11], v[228:231], v[20:23]\n"
      "v_mfma_f32_16x16x32_bf16 v[20:23], a[12:15], v[136:139], v[20:23]\n"
      "v_mfma_f32_16x16x32_bf16 v[20:23], a[16:19], v[192:195], v[20:23]\n"
      "v_mfma_f32_16x16x32_bf16 v[20:23], a[20:23], v[244:247], v[20:23]\n"
      "s_nop 1\n"
      "v_mfma_f32_16x16x32_bf16 v[24:27], a[24:27], a[196:199], 0\n"
      "ds_read_b128 a[24:27], v7 offset:4608\n"
      "s_nop 0\n"
      "v_mfma_f32_16x16x32_bf16 v[24:27], a[28:31], v[160:163], v[24:27]\n"
      "ds_read_b128 a[28:31], v7 offset:4864\n"
      "v_mfma_f32_16x16x32_bf16 v[24:27], a[32:35], v[216:219], v[24:27]\n"
      "ds_read_b128 a[32:35], v7 offset:5120\n"
      "v_cvt_pk_bf16_f32 v20, v20, v21\n"
      "v_mfma_f32_16x16x32_bf16 v[24:27], a[0:3], v[124:127], v[24:27]\n"
      "ds_read_b128 a[0:3], v6 offset:4608\n"
      "v_cvt_pk_bf16_f32 v21, v22, v23\n"
      "v_mfma_f32_16x16x32_bf16 v[24:27], a[4:7], v[180:183], v[24:27]\n"
      "ds_read_b128 a[4:7], v6 offset:4864\n"
      "v_mfma_f32_16x16x32_bf16 v[24:27], a[8:11], v[232:235], v[24:27]\n"
      "ds_read_b128 a[8:11], v6 offset:5120\n"
      "v_mfma_f32_16x16x32_bf16 v[24:27], a[12:15], a[216:219], v[24:27]\n"
      "ds_read_b128 a[12:15], v7 offset:0\n"
      "v_mfma_f32_16x16x32_bf16 v[24:27], a[16:19], v[196:199], v[24:27]\n"
      "ds_read_b128 a[16:19], v7 offset:256\n"
      "v_mfma_f32_16x16x32_bf16 v[24:27], a[20:23], v[252:255], v[24:27]\n"
      "s_waitcnt lgkmcnt(7)\n"
      "ds_read_b128 a[20:23], v7 offset:512\n"
      "s_nop 0\n"
      "v_mfma_f32_16x16x32_bf16 v[28:31], a[24:27], a[160:163], 0\n"
      "s_waitcnt lgkmcnt(7)\n"
      "ds_write_addtid_b32 v20 offset:49160\n"
      "s_nop 0\n"
      "v_mfma_f32_16x16x32_bf16 v[28:31], a[28:31], a[172:175], v[28:31]\n"
      "s_waitcnt lgkmcnt(7)\n"
      "ds_write_addtid_b32 v21 offset:49416\n"
      "v_cvt_pk_bf16_f32 v24, v24, v25\n"
      "v_mfma_f32_16x16x32_bf16 v[28:31], a[32:35], a[184:187], v[28:31]\n"
      "s_waitcnt lgkmcnt(7)\n"
      "ds_write_addtid_b32 v24 offset:36864\n"
      "v_cvt_pk_bf16_f32 v25, v26, v27\n"
      "v_mfma_f32_16x16x32_bf16 v[28:31], a[0:3], a[88:91], v[28:31]\n"
      "s_waitcnt lgkmcnt(7)\n"
      "ds_write_addtid_b32 v25 offset:37120\n"
      "v_mfma_f32_16x16x32_bf16 v[28:31], a[4:7], a[100:103], v[28:31]\n"
      "s_waitcnt lgkmcnt(7)\n"
      "v_mfma_f32_16x16x32_bf16 v[28:31], a[8:11], a[112:115], v[28:31]\n"
      "s_waitcnt lgkmcnt(6)\n"
      "v_mfma_f32_16x16x32_bf16 v[28:31], a[12:15], a[124:127], v[28:31]\n"
      "s_waitcnt lgkmcnt(5)\n"
      "v_mfma_f32_16x16x32_bf16 v[28:31], a[16:19], a[136:139], v[28:31]\n"
      "s_waitcnt lgkmcnt(4)\n"
      "v_mfma_f32_16x16x32_bf16 v[28:31], a[20:23], a[148:151], v[28:31]\n"
      "s_nop 1\n"
      "v_mfma_f32_16x16x32_bf16 v[20:23], a[24:27], a[164:167], 0\n"
      "s_nop 1\n"
      "v_mfma_f32_16x16x32_bf16 v[20:23], a[28:31], a[176:179], v[20:23]\n"
      "s_nop 0\n"
      "v_mfma_f32_16x16x32_bf16 v[20:23], a[32:35], a[188:191], v[20:23]\n"
      "v_cvt_pk_bf16_f32 v28, v28, v29\n"
      "v_cvt_pk_bf16_f32 v29, v30, v31\n"
      "v_mfma_f32_16x16x32_bf16 v[20:23], a[0:3], a[92:95], v[20:23]\n"
      "ds_write_addtid_b32 v28 offset:60432\n"
      "v_mfma_f32_16x16x32_bf16 v[20:23], a[4:7], a[104:107], v[20:23]\n"
      "ds_write_addtid_b32 v29 offset:60688\n"
      "v_mfma_f32_16x16x32_bf16 v[20:23], a[8:11], a[116:119], v[20:23]\n"
      "v_mfma_f32_16x16x32_bf16 v[20:23], a[12:15], a[128:131], v[20:23]\n"
      "v_mfma_f32_16x16x32_bf16 v[20:23], a[16:19], a[140:143], v[20:23]\n"
      "v_mfma_f32_16x16x32_bf16 v[20:23], a[20:23], a[152:155], v[20:23]\n"
      "s_nop 1\n"
      "v_mfma_f32_16x16x32_bf16 v[32:35], a[24:27], a[168:171], 0\n"
      "s_nop 1\n"
      "v_mfma_f32_16x16x32_bf16 v[32:35], a[28:31], a[180:183], v[32:35]\n"
      "s_nop 0\n"
      "v_mfma_f32_16x16x32_bf16 v[32:35], a[32:35], a[192:195], v[32:35]\n"
      "v_cvt_pk_bf16_f32 v20, v20, v21\n"
      "v_cvt_pk_bf16_f32 v21, v22, v23\n"
      "v_mfma_f32_16x16x32_bf16 v[32:35], a[0:3], a[96:99], v[32:35]\n"
      "ds_write_addtid_b32 v20 offset:48136\n"
      "v_mfma_f32_16x16x32_bf16 v[32:35], a[4:7], a[108:111], v[32:35]\n"
      "ds_write_addtid_b32 v21 offset:48392\n"
      "v_mfma_f32_16x16x32_bf16 v[32:35], a[8:11], a[120:123], v[32:35]\n"
      "v_mfma_f32_16x16x32_bf16 v[32:35], a[12:15], a[132:135], v[32:35]\n"
      "v_mfma_f32_16x16x32_bf16 v[32:35], a[16:19], a[144:147], v[32:35]\n"
      "v_mfma_f32_16x16x32_bf16 v[32:35], a[20:23], a[156:159], v[32:35]\n"
      "s_nop 1\n"
      "s_nop 5\n"
      "v_cvt_pk_bf16_f32 v32, v32, v33\n"
      "v_cvt_pk_bf16_f32 v33, v34, v35\n"
      "ds_write_addtid_b32 v32 offset:35840\n"
      "ds_write_addtid_b32 v33 offset:36096\n"
      "s_waitcnt lgkmcnt(0)\n"
      "s_barrier\n"
      "ds_read2_b64 a[0:3], v11 offset0:0 offset1:64\n"
      "ds_read2_b64 a[4:7], v11 offset0:1 offset1:65\n"
      "ds_read2_b64 a[8:11], v11 offset0:2 offset1:66\n"
      "ds_read2_b64 a[12:15], v11 offset0:3 offset1:67\n"
      "ds_read2_b64 a[16:19], v13 offset0:0 offset1:64\n"
      "ds_read2_b64 a[20:23], v13 offset0:1 offset1:65\n"
      "s_waitcnt lgkmcnt(5)\n"
      "v_mfma_f32_16x16x32_bf16 v[68:71], a[84:87], a[0:3], 0\n"
      "s_waitcnt lgkmcnt(4)\n"
      "v_mfma_f32_16x16x32_bf16 v[72:75], a[84:87], a[4:7], 0\n"
      "s_waitcnt lgkmcnt(3)\n"
      "v_mfma_f32_16x16x32_bf16 v[76:79], a[84:87], a[8:11], 0\n"
      "s_waitcnt lgkmcnt(2)\n"
      "s_nop 2\n"
      "v_cvt_pk_bf16_f32 v80, v68, v69\n"
      "v_cvt_pk_bf16_f32 v86, v70, v71\n"
      "v_mfma_f32_16x16x32_bf16 v[68:71], a[84:87], a[12:15], 0\n"
      "s_waitcnt lgkmcnt(1)\n"
      "v_cvt_pk_bf16_f32 v81, v72, v73\n"
      "v_cvt_pk_bf16_f32 v87, v74, v75\n"
      "v_cvt_pk_bf16_f32 v82, v76, v77\n"
      "v_cvt_pk_bf16_f32 v88, v78, v79\n"
      "v_mfma_f32_16x16x32_bf16 v[72:75], a[84:87], a[16:19], 0\n"
      "s_waitcnt lgkmcnt(0)\n"
      "v_mfma_f32_16x16x32_bf16 v[76:79], a[84:87], a[20:23], 0\n"
      "s_mov_b32 s52, s13\n"
      "v_cvt_pk_bf16_f32 v83, v68, v69\n"
      "v_cvt_pk_bf16_f32 v89, v70, v71\n"
      "s_add_u32 s13, s13, s12\n"
      "buffer_store_dwordx4 v[80:83], v14, s[36:39], s52 offen sc1\n"
      "buffer_store_dwordx4 v[86:89], v14, s[36:39], s52 offen offset:1536 sc1\n"
      "v_cvt_pk_bf16_f32 v84, v72, v73\n"
      "v_cvt_pk_bf16_f32 v90, v74, v75\n"
      "v_cvt_pk_bf16_f32 v85, v76, v77\n"
      "v_cvt_pk_bf16_f32 v91, v78, v79\n"
      "buffer_store_dwordx2 v[84:85], v15, s[36:39], s52 offen sc1\n"
      "buffer_store_dwordx2 v[90:91], v15, s[36:39], s52 offen offset:1536 sc1\n"
      "Lconv_u1:\n"
      "Lconv_tail:\n"
      "s_waitcnt vmcnt(0) lgkmcnt(0)\n"
      "Lconv_end:\n"
      :
      : "v"(threadIdx.x), "s"(blockIdx.x), "s"(__builtin_amdgcn_kernarg_segment_ptr())
      : "v1", "v2", "v3", "v4", "v5", "v6", "v7", "v8", "v9", "v10", "v11", "v12", "v13", "v14", "v15", "v16", "v17", "v18", "v19", "v20", "v21", "v22", "v23", "v24", "v25", "v26", "v27", "v28", "v29", "v30", "v31", "v32", "v33", "v34", "v35", "v36", "v37", "v38", "v39", "v40", "v41", "v42", "v43", "v44", "v45", "v46", "v47", "v48", "v49", "v50", "v51", "v52", "v53", "v54", "v55", "v56", "v57", "v58", "v59", "v60", "v61", "v62", "v63", "v64", "v65", "v66", "v67", "v68", "v69", "v70", "v71", "v72", "v73", "v74", "v75", "v76", "v77", "v78", "v79", "v80", "v81", "v82", "v83", "v84", "v85", "v86", "v87", "v88", "v89", "v90", "v91", "v92", "v93", "v94", "v95", "v96", "v97", "v98", "v99", "v100", "v101", "v102", "v103", "v104", "v105", "v106", "v107", "v108", "v109", "v110", "v111", "v112", "v113", "v114", "v115", "v116", "v117", "v118", "v119", "v120", "v121", "v122", "v123", "v124", "v125", "v126", "v127", "v128", "v129", "v130", "v131", "v132", "v133", "v134", "v135", "v136", "v137", "v138", "v139", "v140", "v141", "v142", "v143", "v144", "v145", "v146", "v147", "v148", "v149", "v150", "v151", "v152", "v153", "v154", "v155", "v156", "v157", "v158", "v159", "v160", "v161", "v162", "v163", "v164", "v165", "v166", "v167", "v168", "v169", "v170", "v171", "v172", "v173", "v174", "v175", "v176", "v177", "v178", "v179", "v180", "v181", "v182", "v183", "v184", "v185", "v186", "v187", "v188", "v189", "v190", "v191", "v192", "v193", "v194", "v195", "v196", "v197", "v198", "v199", "v200", "v201", "v202", "v203", "v204", "v205", "v206", "v207", "v208", "v209", "v210", "v211", "v212", "v213", "v214", "v215", "v216", "v217", "v218", "v219", "v220", "v221", "v222", "v223", "v224", "v225", "v226", "v227", "v228", "v229", "v230", "v231", "v232", "v233", "v234", "v235", "v236", "v237", "v238", "v239", "v240", "v241", "v242", "v243", "v244", "v245", "v246", "v247", "v248", "v249", "v250", "v251", "v252", "v253", "v254", "v255", "a0", "a1", "a2", "a3", "a4", "a5", "a6", "a7", "a8", "a9", "a10", "a11", "a12", "a13", "a14", "a15", "a16", "a17", "a18", "a19", "a20", "a21", "a22", "a23", "a24", "a25", "a26", "a27", "a28", "a29", "a30", "a31", "a32", "a33", "a34", "a35", "a36", "a37", "a38", "a39", "a40", "a41", "a42", "a43", "a44", "a45", "a46", "a47", "a48", "a49", "a50", "a51", "a52", "a53", "a54", "a55", "a56", "a57", "a58", "a59", "a60", "a61", "a62", "a63", "a64", "a65", "a66", "a67", "a68", "a69", "a70", "a71", "a72", "a73", "a74", "a75", "a76", "a77", "a78", "a79", "a80", "a81", "a82", "a83", "a84", "a85", "a86", "a87", "a88", "a89", "a90", "a91", "a92", "a93", "a94", "a95", "a96", "a97", "a98", "a99", "a100", "a101", "a102", "a103", "a104", "a105", "a106", "a107", "a108", "a109", "a110", "a111", "a112", "a113", "a114", "a115", "a116", "a117", "a118", "a119", "a120", "a121", "a122", "a123", "a124", "a125", "a126", "a127", "a128", "a129", "a130", "a131", "a132", "a133", "a134", "a135", "a136", "a137", "a138", "a139", "a140", "a141", "a142", "a143", "a144", "a145", "a146", "a147", "a148", "a149", "a150", "a151", "a152", "a153", "a154", "a155", "a156", "a157", "a158", "a159", "a160", "a161", "a162", "a163", "a164", "a165", "a166", "a167", "a168", "a169", "a170", "a171", "a172", "a173", "a174", "a175", "a176", "a177", "a178", "a179", "a180", "a181", "a182", "a183", "a184", "a185", "a186", "a187", "a188", "a189", "a190", "a191", "a192", "a193", "a194", "a195", "a196", "a197", "a198", "a199", "a200", "a201", "a202", "a203", "a204", "a205", "a206", "a207", "a208", "a209", "a210", "a211", "a212", "a213", "a214", "a215", "a216", "a217", "a218", "a219", "a220", "a221", "a222", "a223", "a224", "a225", "a226", "a227", "a228", "a229", "a230", "a231", "a232", "a233", "a234", "a235", "a236", "a237", "a238", "a239", "a240", "a241", "a242", "a243", "a244", "a245", "a246", "a247", "a248", "a249", "a250", "a251", "a252", "a253", "a254", "a255", "s3", "s4", "s5", "s6", "s7", "s8", "s9", "s10", "s11", "s12", "s13", "s14", "s15", "s16", "s17", "s18", "s19", "s20", "s21", "s22", "s23", "s24", "s25", "s26", "s27", "s28", "s29", "s30", "s31", "s34", "s35", "s36", "s37", "s38", "s39", "s40", "s41", "s42", "s43", "s44", "s45", "s46", "s47", "s48", "s49", "s50", "s51", "s52", "s53", "s54", "s55", "s56", "s57", "s58", "s59", "s60", "s61", "s62", "s63", "s64", "s65", "s66", "s67", "s68", "s69", "s70", "s71", "s72", "s73", "s74", "s75", "s76", "s77", "s78", "s79", "s80", "s81", "s82", "s83", "s84", "s85", "s86", "s87", "s88", "s89", "s90", "s91", "s92", "s93", "s94", "s95", "s96", "s97", "s98", "s99", "scc", "vcc", "memory");
}

extern "C" void kernel_launch(void* const* d_in, const int* in_sizes, int n_in, void* d_out,
                              int out_size, void* d_ws, size_t ws_size, hipStream_t stream) {
  conv_kernel<<<256, 256, 0, stream>>>((const char*)d_in[0], (const char*)d_in[1], (char*)d_out, (char*)d_ws);
}
```

```cpp
#include <hip/hip_runtime.h>
#include <hip/hip_bf16.h>
#include <stdint.h>

__global__ void __launch_bounds__(256, 1) __attribute__((aligned(4096)))
conv_kernel(const char* __restrict__ x, const char* __restrict__ w, char* __restrict__ out, char* __restrict__ dbg) {
  __shared__ __attribute__((aligned(1024))) char smem[163840];
  asm volatile("" :: "v"((unsigned)(uintptr_t)smem) : "memory");
  asm volatile(
      "s_load_dwordx8 s[20:27], %2, 0x0\n"
      "v_readfirstlane_b32 s40, %0\n"
      "v_and_b32 v5, 63, %0\n"
      "s_lshr_b32 s40, s40, 6\n"
      "s_lshr_b32 s49, %1, 3\n"
      "s_lshl2_add_u32 s49, s49, s40\n"
      "v_lshlrev_b32 v36, 7, v5\n"
      "s_mul_i32 s50, s49, 384\n"
      "v_min_u32 v37, 256, v36\n"
      "v_add_u32 v37, s50, v37\n"
      "s_getpc_b64 s[54:55]\n"
      "Lconv_start:\n"
      "v_min_u32 v67, Lconv_end-Lconv_start-4, v37\n"
      "s_mov_b32 m0, 163328\n"
      "s_nop 0\n"
      "global_load_lds_dword v67, s[54:55]\n"
      "v_min_u32 v66, 1280, v36\n"
      "s_mul_i32 s96, s49, 1408\n"
      "v_lshlrev_b32 v4, 4, v5\n"
      "s_lshl_b32 s43, s40, 3\n"
      "s_or_b32 s43, s43, 0x80000\n"
      "v_lshlrev_b32 v36, 1, v5\n"
      "v_and_b32 v37, v36, v5\n"
      "v_or_b32 v36, v36, v5\n"
      "v_bitop3_b32 v8, v4, v37, 32 bitop3:0x78\n"
      "v_bitop3_b32 v9, v4, v5, 32 bitop3:0xd2\n"
      "v_bitop3_b32 v89, v4, v36, 32 bitop3:0x78\n"
      "v_mul_u32_u24 v36, 2731, v5\n"
      "s_movk_i32 s3, 384\n"
      "v_lshrrev_b32 v37, 16, v36\n"
      "v_mad_u32_u24 v8, v37, s3, v8\n"
      "v_add_u32 v36, 174784, v36\n"
      "v_lshrrev_b32 v37, 16, v36\n"
      "v_mad_u32_u24 v9, v37, s3, v9\n"
      "v_add_u32 v36, 174784, v36\n"
      "v_lshrrev_b32 v37, 16, v36\n"
      "v_mad_u32_u24 v89, v37, s3, v89\n"
      "s_mul_i32 s92, s40, 3072\n"
      "s_lshr_b32 s14, %1, 7\n"
      "s_sub_u32 s14, s14, 1\n"
      "s_and_b32 s4, s14, 110592\n"
      "s_mov_b32 s5, 55296\n"
      "s_sub_u32 s6, 110592, s4\n"
      "s_lshr_b32 s41, s40, 1\n"
      "s_and_b32 s42, s40, 1\n"
      "s_mov_b32 s30, 165888\n"
      "s_mov_b32 s31, 0x20000\n"
      "s_bfe_u32 s99, %1, 0x30003\n"
      "s_add_u32 s49, s92, 2048\n"
      "v_add_u32 v17, s49, v4\n"
      "s_or_b32 s50, s99, s40\n"
      "s_cmp_eq_u32 s50, 0\n"
      "s_cselect_b32 s16, 0xffffff, 0\n"
      "s_mov_b32 s17, 0\n"
      "s_cmp_eq_u32 s99, 7\n"
      "s_cselect_b32 s19, 0xffffff00, 0\n"
      "s_mov_b32 s18, 0\n"
      "v_add_u32 v19, 14080, v4\n"
      "v_cndmask_b32 v16, v17, 2.0, s[16:17]\n"
      "v_cndmask_b32 v19, v19, 2.0, s[18:19]\n"
      "v_and_b32 v36, 16, v4\n"
      "v_mov_b32 v52, 0xffff\n"
      "v_lshlrev_b32 v52, v36, v52\n"
      "s_mov_b32 s16, 0x3000003\n"
      "s_mov_b32 s17, 0x3000003\n"
      "v_cndmask_b32 v48, 0, v52, s[16:17]\n"
      "s_lshl_b64 s[16:17], s[16:17], 2\n"
      "v_cndmask_b32 v49, 0, v52, s[16:17]\n"
      "s_lshl_b64 s[16:17], s[16:17], 2\n"
      "v_cndmask_b32 v50, 0, v52, s[16:17]\n"
      "s_lshl_b64 s[16:17], s[16:17], 2\n"
      "v_cndmask_b32 v51, 0, v52, s[16:17]\n"
      "v_bfe_u32 v53, v5, 5, 1\n"
      "v_xor_b32 v53, s41, v53\n"
      "v_sub_u32 v53, 0, v53\n"
      "v_and_b32 v36, 0x1000100, v53\n"
      "v_xor_b32 v36, 0x3fbf3fbf, v36\n"
      "v_and_b32 v68, v36, v48\n"
      "v_and_b32 v69, v36, v49\n"
      "v_and_b32 v70, v36, v50\n"
      "v_and_b32 v71, v36, v51\n"
      "v_xor_b32 v72, 0x80008000, v68\n"
      "v_xor_b32 v73, 0x80008000, v69\n"
      "v_xor_b32 v74, 0x80008000, v70\n"
      "v_xor_b32 v75, 0x80008000, v71\n"
      "v_bfe_i32 v36, v5, 5, 1\n"
      "v_and_b32 v36, 0x3f3f3f3f, v36\n"
      "v_xor_b32 v37, 0x3f3f3f3f, v36\n"
      "v_and_b32 v76, v37, v48\n"
      "v_and_b32 v77, v37, v49\n"
      "v_and_b32 v78, v37, v50\n"
      "v_and_b32 v79, v37, v51\n"
      "v_and_b32 v80, v36, v48\n"
      "v_and_b32 v81, v36, v49\n"
      "v_and_b32 v82, v36, v50\n"
      "v_and_b32 v83, v36, v51\n"
      "s_mul_i32 s50, s42, 96\n"
      "v_bfe_u32 v36, v5, 1, 1\n"
      "v_and_or_b32 v36, v5, 12, v36\n"
      "v_and_b32 v37, 1, v5\n"
      "v_lshl_add_u32 v36, v37, 1, v36\n"
      "v_mul_u32_u24 v36, 192, v36\n"
      "v_add_u32 v36, 46144, v36\n"
      "v_lshlrev_b32 v37, 3, v5\n"
      "v_and_b32 v37, 32, v37\n"
      "v_and_b32 v38, 16, v5\n"
      "v_add_u32 v38, s50, v38\n"
      "v_and_b32 v39, 32, v5\n"
      "v_mul_u32_u24 v40, 192, v39\n"
      "v_add_u32 v40, v40, v36\n"
      "v_xad_u32 v84, v38, v37, v40\n"
      "v_add_u32 v41, 32, v38\n"
      "v_xad_u32 v85, v41, v37, v40\n"
      "v_add_u32 v41, v39, v38\n"
      "v_xad_u32 v86, v41, v37, v36\n"
      "s_mul_i32 s49, s41, 6144\n"
      "s_add_u32 s49, s49, 46144\n"
      "v_lshrrev_b32 v36, 2, v5\n"
      "v_and_b32 v36, 12, v36\n"
      "v_bfe_u32 v38, v5, 3, 1\n"
      "v_add_u32 v36, v36, v38\n"
      "v_bfe_u32 v38, v5, 2, 1\n"
      "v_lshl_add_u32 v36, v38, 1, v36\n"
      "v_mul_u32_u24 v36, 192, v36\n"
      "v_lshlrev_b32 v37, 3, v5\n"
      "v_and_b32 v38, 8, v37\n"
      "v_add3_u32 v36, v36, v38, s49\n"
      "v_lshlrev_b32 v38, 1, v5\n"
      "v_and_b32 v38, 32, v38\n"
      "v_and_b32 v37, 16, v37\n"
      "v_add_u32 v37, s50, v37\n"
      "v_xad_u32 v87, v37, v38, v36\n"
      "v_add_u32 v39, 32, v37\n"
      "v_xad_u32 v88, v39, v38, v36\n"
      "s_waitcnt lgkmcnt(0)\n"
      "s_mov_b32 s28, s22\n"
      "s_and_b32 s29, s23, 0xffff\n"
      "buffer_load_dword v66, s[28:31], s96 offen lds\n"
      "s_add_u32 m0, s92, 73824\n"
      "s_bfe_u32 s97, 0x1603000, s43\n"
      "s_mul_i32 s97, s97, 384\n"
      "s_add_u32 s97, s97, s4\n"
      "buffer_load_dwordx4 v8, s[28:31], s97 offen offset:0 lds\n"
      "buffer_load_dwordx4 v9, s[28:31], s97 offen offset:1024 lds\n"
      "buffer_load_dwordx4 v89, s[28:31], s97 offen offset:2048 lds\n"
      "s_add_u32 m0, s92, 86112\n"
      "s_bfe_u32 s97, 0x40106131, s43\n"
      "s_mul_i32 s97, s97, 384\n"
      "s_add_u32 s97, s97, s4\n"
      "buffer_load_dwordx4 v8, s[28:31], s97 offen offset:0 lds\n"
      "buffer_load_dwordx4 v9, s[28:31], s97 offen offset:1024 lds\n"
      "buffer_load_dwordx4 v89, s[28:31], s97 offen offset:2048 lds\n"
      "s_and_b32 s53, %1, 7\n"
      "s_lshl_b32 s53, s53, 6\n"
      "s_bfe_u32 s50, %1, 0x10006\n"
      "s_add_u32 s53, s53, s50\n"
      "s_lshl1_add_u32 s50, s14, 32\n"
      "s_add_u32 s53, s53, s50\n"
      "s_mul_i32 s13, s53, 98304\n"
      "s_or_b32 s12, s14, 1\n"
      "s_mul_i32 s12, s12, 196608\n"
      "s_lshl_b32 s50, s12, 1\n"
      "s_sub_u32 s51, s13, s50\n"
      "s_mul_i32 s99, s99, 12288\n"
      "s_sub_u32 s50, s99, 2432\n"
      "s_ashr_i32 s49, s50, 31\n"
      "s_add_u32 s32, s20, s50\n"
      "s_addc_u32 s33, s21, s49\n"
      "s_and_b32 s33, s33, 0xffff\n"
      "s_sub_u32 s34, 50331648, s50\n"
      "s_mov_b32 s35, 0x20000\n"
      "s_add_u32 s46, s92, 110720\n"
      "v_lshrrev_b32 v36, 1, v5\n"
      "v_xor_b32 v36, v36, v5\n"
      "v_and_b32 v36, 16, v36\n"
      "v_and_b32 v37, 48, v4\n"
      "v_mad_u32_u24 v37, v36, 12, v37\n"
      "v_and_b32 v36, 32, v5\n"
      "v_mul_u32_u24 v36, 192, v36\n"
      "s_mul_i32 s50, s40, 1536\n"
      "s_add_u32 s50, s50, 110720\n"
      "v_add3_u32 v37, v37, v36, s50\n"
      "v_bfe_u32 v36, v5, 2, 2\n"
      "v_mad_u32_u24 v37, v36, s3, v37\n"
      "v_add_u32 v2, 384, v37\n"
      "v_bcnt_u32_b32 v36, v36, 0\n"
      "v_mad_u32_u24 v3, v36, s3, v37\n"
      "v_and_b32 v36, 3, v5\n"
      "v_mul_u32_u24 v36, 18464, v36\n"
      "v_bfe_u32 v37, v5, 2, 2\n"
      "v_lshrrev_b32 v38, 1, v37\n"
      "v_xor_b32 v37, v37, v38\n"
      "s_mov_b64 exec, 0\n"
      "s_cmp_eq_u32 s40, 0\n"
      "s_cbranch_scc1 Lconv_p1\n"
      "s_cmp_eq_u32 s40, 1\n"
      "s_cbranch_scc1 Lconv_p2\n"
      "s_cmp_eq_u32 s40, 2\n"
      "s_cbranch_scc1 Lconv_pa0\n"
      "s_cmp_eq_u32 s40, 3\n"
      "s_cbranch_scc1 Lconv_pa1\n"
      "Lconv_r0:\n"
      "s_mov_b64 exec, -1\n"
      "s_add_u32 m0, s92, 98400\n"
      "s_bfe_u32 s97, 0x71411170, s43\n"
      "s_mul_i32 s97, s97, 384\n"
      "s_add_u32 s97, s97, s4\n"
      "buffer_load_dwordx4 v8, s[28:31], s97 offen offset:0 lds\n"
      "buffer_load_dwordx4 v9, s[28:31], s97 offen offset:1024 lds\n"
      "buffer_load_dwordx4 v89, s[28:31], s97 offen offset:2048 lds\n"
      "v_lshl_add_u32 v36, v37, 6, v36\n"
      "s_mul_i32 s50, s41, 2304\n"
      "v_and_b32 v37, 48, v5\n"
      "v_add3_u32 v6, v36, v37, s50\n"
      "v_add_u32 v7, 9216, v6\n"
      "s_mov_b32 exec_lo, 0x11111111\n"
      "s_mov_b32 exec_hi, exec_lo\n"
      "v_mov_b32 v38, 0xc0800000\n"
      "v_mov_b32 v39, 0x3f800000\n"
      "v_mov_b32 v40, 0xbe803f80\n"
      "v_mov_b32 v41, 0x0\n"
      "v_mov_b32 v42, 0xc080c000\n"
      "v_mov_b32 v43, 0x3f803f00\n"
      "s_mov_b32 exec_lo, 0x22222222\n"
      "s_mov_b32 exec_hi, exec_lo\n"
      "v_mov_b32 v38, 0xc000\n"
      "v_mov_b32 v39, 0x3f00\n"
      "v_mov_b32 v40, 0x0\n"
      "v_mov_b32 v41, 0x0\n"
      "v_mov_b32 v42, 0xc0804000\n"
      "v_mov_b32 v43, 0x3f80bf00\n"
      "s_mov_b32 exec_lo, 0x44444444\n"
      "s_mov_b32 exec_hi, exec_lo\n"
      "v_mov_b32 v38, 0x3f80\n"
      "v_mov_b32 v39, 0xc080\n"
      "v_mov_b32 v40, 0x0\n"
      "v_mov_b32 v41, 0x3f80be80\n"
      "v_mov_b32 v42, 0x3f003f80\n"
      "v_mov_b32 v43, 0xc000c080\n"
      "s_mov_b32 exec_lo, 0x88888888\n"
      "s_mov_b32 exec_hi, exec_lo\n"
      "v_mov_b32 v38, 0x3f000000\n"
      "v_mov_b32 v39, 0xc0000000\n"
      "v_mov_b32 v40, 0x0\n"
      "v_mov_b32 v41, 0x0\n"
      "v_mov_b32 v42, 0xbf003f80\n"
      "v_mov_b32 v43, 0x4000c080\n"
      "s_mov_b64 exec, -1\n"
      "v_accvgpr_write_b32 a72, v38\n"
      "v_accvgpr_write_b32 a73, v39\n"
      "v_accvgpr_write_b32 a74, v40\n"
      "v_accvgpr_write_b32 a75, v41\n"
      "v_accvgpr_write_b32 a76, v42\n"
      "v_accvgpr_write_b32 a77, v43\n"
      "v_lshlrev_b32 v36, 5, v5\n"
      "v_and_b32 v36, 0x180, v36\n"
      "v_lshlrev_b32 v37, 3, v5\n"
      "v_and_b32 v37, 16, v37\n"
      "v_sub_u32 v38, 0x3eb7, v36\n"
      "v_add_u32 v39, 0x3d37, v36\n"
      "v_mov_b32 v40, 0x3f80\n"
      "s_mov_b64 vcc, 0xffff\n"
      "s_mov_b32 s16, 0xffff0000\n"
      "s_mov_b32 s17, 0xf0f0\n"
      "v_xor_b32 v41, 0x8000, v38\n"
      "v_cndmask_b32 v38, v38, v41, s[16:17]\n"
      "v_cndmask_b32 v38, v38, v40, vcc\n"
      "v_lshlrev_b32 v38, v37, v38\n"
      "v_xor_b32 v41, 0x8000, v39\n"
      "s_xor_b32 s17, s17, 0xffff\n"
      "v_cndmask_b32 v39, v39, v41, s[16:17]\n"
      "v_cndmask_b32 v39, v39, v40, vcc\n"
      "v_lshlrev_b32 v39, v37, v39\n"
      "s_mov_b32 s18, 0x55550005\n"
      "s_mov_b32 s19, 0x5555\n"
      "v_cndmask_b32 v42, 0, v38, s[18:19]\n"
      "s_lshl_b64 s[16:17], s[18:19], 1\n"
      "v_cndmask_b32 v43, 0, v38, s[16:17]\n"
      "v_accvgpr_write_b32 a78, v42\n"
      "v_accvgpr_write_b32 a79, v43\n"
      "s_mov_b32 s18, 0x55555000\n"
      "v_cndmask_b32 v42, 0, v39, s[18:19]\n"
      "s_lshl_b64 s[16:17], s[18:19], 1\n"
      "v_cndmask_b32 v43, 0, v39, s[16:17]\n"
      "v_accvgpr_write_b32 a80, v42\n"
      "v_accvgpr_write_b32 a81, v43\n"
      "v_lshrrev_b32 v36, 4, v5\n"
      "v_min_u32 v37, 2, v36\n"
      "v_lshlrev_b32 v37, 10, v37\n"
      "s_lshl_b32 s50, s41, 7\n"
      "s_lshl_b32 s49, s42, 8\n"
      "s_add_u32 s50, s50, s49\n"
      "s_add_u32 s50, s50, 98416\n"
      "v_and_b32 v38, 8, v5\n"
      "v_lshl_add_u32 v37, v38, 3, v37\n"
      "v_add_u32 v37, s50, v37\n"
      "s_movk_i32 s50, -12296\n"
      "s_mov_b32 s49, 43040\n"
      "v_mov_b32 v42, s49\n"
      "v_bfe_u32 v39, v5, 1, 2\n"
      "v_lshrrev_b32 v40, 2, v38\n"
      "v_add_u32 v39, v39, v40\n"
      "v_lshlrev_b32 v40, 5, v5\n"
      "v_and_b32 v40, 32, v40\n"
      "v_cmp_lt_u32 vcc, 2, v39\n"
      "v_mad_i32_i24 v39, v39, s50, v37\n"
      "v_add_u32 v39, v39, v40\n"
      "v_cndmask_b32 v41, 0, v42, vcc\n"
      "v_add_u32 v10, v39, v41\n"
      "v_add_u32 v11, 3072, v10\n"
      "v_bfe_u32 v39, v5, 2, 1\n"
      "v_lshrrev_b32 v40, 1, v38\n"
      "v_sub_u32 v39, v39, v40\n"
      "v_add_u32 v39, 4, v39\n"
      "v_and_b32 v40, 48, v4\n"
      "v_cmp_lt_u32 vcc, 2, v39\n"
      "v_mad_i32_i24 v39, v39, s50, v37\n"
      "v_add_u32 v39, v39, v40\n"
      "v_cndmask_b32 v41, 0, v42, vcc\n"
      "v_add_u32 v12, v39, v41\n"
      "v_add_u32 v13, 3072, v12\n"
      "v_mul_u32_u24 v36, 384, v36\n"
      "v_and_b32 v37, 15, v5\n"
      "v_lshl_add_u32 v39, v37, 3, v36\n"
      "v_add_u32 v15, 128, v39\n"
      "v_add_lshl_u32 v37, v37, v38, 4\n"
      "v_add_u32 v14, v37, v36\n"
      "s_add_u32 s50, s99, s92\n"
      "s_add_u32 s36, s24, s50\n"
      "s_addc_u32 s37, s25, 0\n"
      "s_and_b32 s37, s37, 0xffff\n"
      "s_sub_u32 s38, 50331648, s50\n"
      "s_mov_b32 s39, 0x20000\n"
      "s_mul_i32 s48, s12, 17\n"
      "s_mul_i32 s45, s42, 6152\n"
      "s_lshl_b32 s50, s41, 9\n"
      "s_add_u32 s45, s45, s50\n"
      "s_add_u32 s45, s45, 41056\n"
      "s_mov_b32 s98, 0\n"
      "s_mov_b32 s91, 26112\n"
      "s_mul_i32 s18, s40, 18464\n"
      "s_lshl1_add_u32 s19, s18, 9216\n"
      "s_add_u32 s58, s46, 26112\n"
      "s_lshl1_add_u32 s93, s46, 26112\n"
      "s_mul_i32 s59, s42, -13056\n"
      "s_add_u32 s59, s59, 135808\n"
      "s_lshl1_add_u32 s94, s59, 26112\n"
      "s_mul_i32 s50, s42, 26112\n"
      "s_add_u32 s59, s59, s50\n"
      "s_mul_i32 s15, s42, s12\n"
      "s_sub_u32 s95, s12, s15\n"
      "s_mov_b32 s52, 2.0\n"
      "s_cmp_lt_u32 s40, 3\n"
      "s_cselect_b32 s97, 27680, 0\n"
      "s_add_u32 s97, s97, s92\n"
      "s_add_u32 m0, s97, 36928\n"
      "s_bfe_u32 s97, 0x21805020, s43\n"
      "s_mul_i32 s97, s97, 384\n"
      "s_add_u32 s97, s97, s4\n"
      "buffer_load_dwordx4 v8, s[28:31], s97 offen offset:0 lds\n"
      "buffer_load_dwordx4 v9, s[28:31], s97 offen offset:1024 lds\n"
      "buffer_load_dwordx4 v89, s[28:31], s97 offen offset:2048 lds\n"
      "s_cmp_lt_u32 s40, 2\n"
      "s_cselect_b32 s97, 27680, 0\n"
      "s_add_u32 s97, s97, s92\n"
      "s_add_u32 m0, s97, 21536\n"
      "s_cmp_lt_u32 s40, 2\n"
      "s_cselect_b32 s7, s4, s5\n"
      "s_bfe_u32 s97, 0x30008151, s43\n"
      "s_mul_i32 s97, s97, 384\n"
      "s_add_u32 s97, s97, s7\n"
      "buffer_load_dwordx4 v8, s[28:31], s97 offen offset:0 lds\n"
      "buffer_load_dwordx4 v9, s[28:31], s97 offen offset:1024 lds\n"
      "buffer_load_dwordx4 v89, s[28:31], s97 offen offset:2048 lds\n"
      "s_cmp_lt_u32 s40, 1\n"
      "s_cselect_b32 s97, 27680, 0\n"
      "s_add_u32 s97, s97, s92\n"
      "s_add_u32 m0, s97, 6144\n"
      "s_bfe_u32 s97, 0x61310160, s43\n"
      "s_mul_i32 s97, s97, 384\n"
      "s_add_u32 s97, s97, s5\n"
      "buffer_load_dwordx4 v8, s[28:31], s97 offen offset:0 lds\n"
      "buffer_load_dwordx4 v9, s[28:31], s97 offen offset:1024 lds\n"
      "buffer_load_dwordx4 v89, s[28:31], s97 offen offset:2048 lds\n"
      "s_cmp_lg_u32 s41, 0\n"
      "s_cselect_b64 vcc, -1, 0\n"
      ".p2align 3\n"
      "s_waitcnt vmcnt(12)\n"
      "s_barrier\n"
      "s_cmp_lt_u32 s40, 3\n"
      "s_cselect_b32 s97, -9248, 0\n"
      "ds_read_b128 a[0:3], v86 offset:30752\n"
      "ds_read_b128 a[4:7], v84 offset:27680\n"
      "ds_read_b128 a[8:11], v85 offset:27680\n"
      "ds_read_b128 a[12:15], v86 offset:30816\n"
      "s_add_u32 s97, s97, s92\n"
      "s_add_u32 m0, s97, 9248\n"
      "s_bfe_u32 s97, 0x11704010, s43\n"
      "s_mul_i32 s97, s97, 384\n"
      "s_add_u32 s97, s97, s5\n"
      "s_waitcnt lgkmcnt(3)\n"
      "v_mfma_f32_16x16x32_bf16 v[20:23], a[0:3], v[76:79], 0\n"
      "s_waitcnt lgkmcnt(2)\n"
      "ds_read_b128 a[16:19], v84 offset:27744\n"
      "ds_read_b128 a[20:23], v86 offset:39968\n"
      "buffer_load_dwordx4 v8, s[28:31], s97 offen offset:0 lds\n"
      "v_mfma_f32_16x16x32_bf16 v[24:27], a[4:7], v[68:71], v[20:23]\n"
      "ds_read_b128 a[24:27], v84 offset:36896\n"
      "ds_read_b128 a[28:31], v85 offset:36896\n"
      "v_mfma_f32_16x16x32_bf16 v[20:23], a[4:7], v[72:75], v[20:23]\n"
      "ds_read_b64_tr_b16 a[84:85], v87 offset:27680\n"
      "ds_read_b64_tr_b16 a[88:89], v88 offset:27680\n"
      "buffer_load_dwordx4 v9, s[28:31], s97 offen offset:1024 lds\n"
      "v_mfma_f32_16x16x32_bf16 v[28:31], a[0:3], v[80:83], 0\n"
      "s_waitcnt lgkmcnt(7)\n"
      "ds_read_b64_tr_b16 a[92:93], v87 offset:27744\n"
      "ds_read_b64_tr_b16 a[86:87], v87 offset:36896\n"
      "v_mfma_f32_16x16x32_bf16 v[32:35], a[8:11], v[68:71], v[28:31]\n"
      "ds_read_b64_tr_b16 a[90:91], v88 offset:36896\n"
      "ds_read_b128 a[32:35], v86 offset:40032\n"
      "v_mfma_f32_16x16x32_bf16 v[28:31], a[8:11], v[72:75], v[28:31]\n"
      "s_waitcnt lgkmcnt(10)\n"
      "ds_read_b128 a[0:3], v84 offset:36960\n"
      "ds_read_b64_tr_b16 a[94:95], v87 offset:36960\n"
      "buffer_load_dwordx4 v89, s[28:31], s97 offen offset:2048 lds\n"
      "v_mfma_f32_16x16x32_bf16 v[54:57], a[12:15], v[76:79], 0\n"
      "s_waitcnt lgkmcnt(10)\n"
      "s_cmp_lt_u32 s40, 2\n"
      "s_cselect_b32 s97, -9248, 0\n"
      "s_add_u32 s97, s97, s92\n"
      "s_add_u32 m0, s97, 30784\n"
      "s_bfe_u32 s97, 0x50207141, s43\n"
      "s_mul_i32 s97, s97, 384\n"
      "s_add_u32 s97, s97, s5\n"
      "v_cvt_pk_bf16_f32 v96, v20, v21\n"
      "v_cvt_pk_bf16_f32 v97, v22, v23\n"
      "v_mfma_f32_16x16x32_bf16 v[58:61], a[16:19], v[68:71], v[54:57]\n"
      "v_cvt_pk_bf16_f32 v92, v24, v25\n"
      "v_cvt_pk_bf16_f32 v93, v26, v27\n"
      "v_mfma_f32_16x16x32_bf16 v[54:57], a[16:19], v[72:75], v[54:57]\n"
      "buffer_load_dwordx4 v8, s[28:31], s97 offen offset:0 lds\n"
      "v_cvt_pk_bf16_f32 v104, v28, v29\n"
      "v_cvt_pk_bf16_f32 v105, v30, v31\n"
      "v_mfma_f32_16x16x32_bf16 v[62:65], a[20:23], v[76:79], 0\n"
      "s_waitcnt lgkmcnt(4)\n"
      "v_cvt_pk_bf16_f32 v100, v32, v33\n"
      "v_cvt_pk_bf16_f32 v101, v34, v35\n"
      "v_mfma_f32_16x16x32_bf16 v[20:23], a[24:27], v[68:71], v[62:65]\n"
      "v_cvt_pk_bf16_f32 v108, v54, v55\n"
      "v_cvt_pk_bf16_f32 v109, v56, v57\n"
      "v_mfma_f32_16x16x32_bf16 v[62:65], a[24:27], v[72:75], v[62:65]\n"
      "s_waitcnt lgkmcnt(0)\n"
      "s_waitcnt vmcnt(13)\n"
      "s_cbranch_vccnz Lconv_h1\n"
      "s_barrier\n"
      "Lconv_h1:\n"
      "buffer_load_dwordx4 v9, s[28:31], s97 offen offset:1024 lds\n"
      "v_mfma_f32_16x16x32_bf16 v[24:27], a[20:23], v[80:83], 0\n"
      "v_cvt_pk_bf16_f32 v58, v58, v59\n"
      "v_cvt_pk_bf16_f32 v60, v60, v61\n"
      "v_mfma_f32_16x16x32_bf16 v[28:31], a[28:31], v[68:71], v[24:27]\n"
      "s_cbranch_vccz Lconv_h2\n"
      "s_barrier\n"
      "Lconv_h2:\n"
      "ds_read_b128 a[4:7], v86 offset:49184\n"
      "ds_read_b128 a[8:11], v84 offset:46112\n"
      "ds_read_b128 a[12:15], v85 offset:46112\n"
      "ds_read_b128 a[16:19], v86 offset:49248\n"
      "v_mfma_f32_16x16x32_bf16 v[24:27], a[28:31], v[72:75], v[24:27]\n"
      "ds_read_b128 a[20:23], v84 offset:46176\n"
      "ds_read_b128 a[24:27], v86 offset:58400\n"
      "buffer_load_dwordx4 v89, s[28:31], s97 offen offset:2048 lds\n"
      "v_accvgpr_write_b32 a192, v58\n"
      "v_accvgpr_write_b32 a193, v60\n"
      "v_mfma_f32_16x16x32_bf16 v[32:35], a[32:35], v[76:79], 0\n"
      "s_cmp_lt_u32 s40, 1\n"
      "s_cselect_b32 s97, -9248, 0\n"
      "ds_read_b128 a[28:31], v84 offset:55328\n"
      "ds_read_b128 a[32:35], v85 offset:55328\n"
      "s_add_u32 s97, s97, s92\n"
      "s_add_u32 m0, s97, 52320\n"
      "s_bfe_u32 s97, 0x81512180, s43\n"
      "s_mul_i32 s97, s97, 384\n"
      "s_add_u32 s97, s97, s5\n"
      "v_mfma_f32_16x16x32_bf16 v[54:57], a[0:3], v[68:71], v[32:35]\n"
      "ds_read_b64_tr_b16 a[96:97], v87 offset:46112\n"
      "ds_read_b64_tr_b16 a[100:101], v88 offset:46112\n"
      "buffer_load_dwordx4 v8, s[28:31], s97 offen offset:0 lds\n"
      "v_cvt_pk_bf16_f32 v98, v62, v63\n"
      "v_cvt_pk_bf16_f32 v99, v64, v65\n"
      "v_mfma_f32_16x16x32_bf16 v[32:35], a[0:3], v[72:75], v[32:35]\n"
      "s_waitcnt lgkmcnt(9)\n"
      "ds_read_b64_tr_b16 a[104:105], v87 offset:46176\n"
      "ds_read_b64_tr_b16 a[98:99], v87 offset:55328\n"
      "v_cvt_pk_bf16_f32 v94, v20, v21\n"
      "v_cvt_pk_bf16_f32 v95, v22, v23\n"
      "v_cvt_pk_bf16_f32 v106, v24, v25\n"
      "v_mfma_f32_16x16x32_bf16 v[58:61], a[4:7], v[76:79], 0\n"
      "s_waitcnt lgkmcnt(10)\n"
      "ds_read_b64_tr_b16 a[102:103], v88 offset:55328\n"
      "ds_read_b128 a[0:3], v86 offset:58464\n"
      "v_cvt_pk_bf16_f32 v107, v26, v27\n"
      "v_mfma_f32_16x16x32_bf16 v[62:65], a[8:11], v[68:71], v[58:61]\n"
      "ds_read_b64_tr_b16 a[106:107], v87 offset:55392\n"
      "buffer_load_dwordx4 v9, s[28:31], s97 offen offset:1024 lds\n"
      "v_cvt_pk_bf16_f32 v102, v28, v29\n"
      "v_cvt_pk_bf16_f32 v103, v30, v31\n"
      "v_mfma_f32_16x16x32_bf16 v[58:61], a[8:11], v[72:75], v[58:61]\n"
      "v_cvt_pk_bf16_f32 v110, v32, v33\n"
      "v_cvt_pk_bf16_f32 v111, v34, v35\n"
      "v_mfma_f32_16x16x32_bf16 v[20:23], a[4:7], v[80:83], 0\n"
      "s_waitcnt lgkmcnt(7)\n"
      "ds_read_b128 a[4:7], v84 offset:55392\n"
      "v_cvt_pk_bf16_f32 v54, v54, v55\n"
      "v_mfma_f32_16x16x32_bf16 v[24:27], a[12:15], v[68:71], v[20:23]\n"
      "buffer_load_dwordx4 v89, s[28:31], s97 offen offset:2048 lds\n"
      "v_cvt_pk_bf16_f32 v56, v56, v57\n"
      "v_accvgpr_write_b32 a194, v54\n"
      "v_accvgpr_write_b32 a195, v56\n"
      "v_mfma_f32_16x16x32_bf16 v[20:23], a[12:15], v[72:75], v[20:23]\n"
      "s_add_u32 m0, s46, 13056\n"
      "s_add_u32 s51, s51, s12\n"
      "v_cvt_pk_bf16_f32 v152, v58, v59\n"
      "v_cvt_pk_bf16_f32 v153, v60, v61\n"
      "v_mfma_f32_16x16x32_bf16 v[28:31], a[16:19], v[76:79], 0\n"
      "v_cvt_pk_bf16_f32 v148, v62, v63\n"
      "v_cvt_pk_bf16_f32 v149, v64, v65\n"
      "v_mfma_f32_16x16x32_bf16 v[32:35], a[20:23], v[68:71], v[28:31]\n"
      "buffer_load_dwordx4 v16, s[32:35], s51 offen lds\n"
      "v_cvt_pk_bf16_f32 v156, v20, v21\n"
      "v_mfma_f32_16x16x32_bf16 v[28:31], a[20:23], v[72:75], v[28:31]\n"
      "s_add_u32 m0, s46, 13056\n"
      "v_cvt_pk_bf16_f32 v157, v22, v23\n"
      "v_cvt_pk_bf16_f32 v24, v24, v25\n"
      "v_mfma_f32_16x16x32_bf16 v[54:57], a[24:27], v[76:79], 0\n"
      "buffer_load_dwordx4 v17, s[32:35], s51 offen offset:1024 lds\n"
      "v_cvt_pk_bf16_f32 v26, v26, v27\n"
      "v_mfma_f32_16x16x32_bf16 v[58:61], a[28:31], v[68:71], v[54:57]\n"
      "s_add_u32 m0, s46, 13056\n"
      "s_add_u32 s96, s51, s15\n"
      "buffer_load_dwordx4 v17, s[32:35], s51 offen offset:2048 lds\n"
      "v_accvgpr_write_b32 a196, v24\n"
      "v_mfma_f32_16x16x32_bf16 v[54:57], a[28:31], v[72:75], v[54:57]\n"
      "s_waitcnt lgkmcnt(0)\n"
      "s_waitcnt vmcnt(12)\n"
      "s_cbranch_vccnz Lconv_h3\n"
      "s_barrier\n"
      "Lconv_h3:\n"
      "s_mov_b32 m0, s59\n"
      "v_accvgpr_write_b32 a197, v26\n"
      "v_mfma_f32_16x16x32_bf16 v[62:65], a[24:27], v[80:83], 0\n"
      "buffer_load_dwordx4 v19, s[32:35], s96 offen lds\n"
      "v_cvt_pk_bf16_f32 v164, v28, v29\n"
      "v_mfma_f32_16x16x32_bf16 v[20:23], a[32:35], v[68:71], v[62:65]\n"
      "s_cbranch_vccz Lconv_h4\n"
      "s_barrier\n"
      "Lconv_h4:\n"
      "s_add_u32 m0, s46, 26112\n"
      "s_add_u32 s51, s51, s12\n"
      "ds_read_b128 a[8:11], v86 offset:21536\n"
      "ds_read_b128 a[12:15], v84 offset:18464\n"
      "ds_read_b128 a[16:19], v85 offset:18464\n"
      "ds_read_b128 a[20:23], v86 offset:21600\n"
      "buffer_load_dwordx4 v16, s[32:35], s51 offen lds\n"
      "v_mfma_f32_16x16x32_bf16 v[62:65], a[32:35], v[72:75], v[62:65]\n"
      "s_add_u32 m0, s46, 26112\n"
      "ds_read_b128 a[24:27], v84 offset:18528\n"
      "ds_read_b128 a[28:31], v86 offset:3072\n"
      "buffer_load_dwordx4 v17, s[32:35], s51 offen offset:1024 lds\n"
      "v_cvt_pk_bf16_f32 v165, v30, v31\n"
      "v_cvt_pk_bf16_f32 v160, v32, v33\n"
      "v_mfma_f32_16x16x32_bf16 v[24:27], a[0:3], v[76:79], 0\n"
      "s_add_u32 m0, s46, 26112\n"
      "ds_read_b128 a[32:35], v84 offset:0\n"
      "ds_read_b128 a[0:3], v85 offset:0\n"
      "buffer_load_dwordx4 v17, s[32:35], s51 offen offset:2048 lds\n"
      "v_cvt_pk_bf16_f32 v161, v34, v35\n"
      "v_cvt_pk_bf16_f32 v154, v54, v55\n"
      "v_mfma_f32_16x16x32_bf16 v[28:31], a[4:7], v[68:71], v[24:27]\n"
      "s_add_u32 m0, s92, 73824\n"
      "s_bfe_u32 s97, 0x1603000, s43\n"
      "ds_read_b64_tr_b16 a[108:109], v87 offset:18464\n"
      "ds_read_b64_tr_b16 a[112:113], v88 offset:18464\n"
      "s_mul_i32 s97, s97, 384\n"
      "s_add_u32 s97, s97, s6\n"
      "buffer_load_dwordx4 v8, s[28:31], s97 offen offset:0 lds\n"
      "v_cvt_pk_bf16_f32 v155, v56, v57\n"
      "v_cvt_pk_bf16_f32 v150, v58, v59\n"
      "v_mfma_f32_16x16x32_bf16 v[24:27], a[4:7], v[72:75], v[24:27]\n"
      "s_waitcnt lgkmcnt(9)\n"
      "ds_read_b128 a[4:7], v86 offset:3136\n"
      "ds_read_b64_tr_b16 a[116:117], v87 offset:18528\n"
      "v_cvt_pk_bf16_f32 v151, v60, v61\n"
      "v_cvt_pk_bf16_f32 v158, v62, v63\n"
      "v_cvt_pk_bf16_f32 v159, v64, v65\n"
      "v_mfma_f32_16x16x32_bf16 v[32:35], a[8:11], v[76:79], 0\n"
      "s_waitcnt lgkmcnt(10)\n"
      "ds_read_b64_tr_b16 a[110:111], v87 offset:0\n"
      "ds_read_b64_tr_b16 a[114:115], v88 offset:0\n"
      "v_cvt_pk_bf16_f32 v20, v20, v21\n"
      "v_cvt_pk_bf16_f32 v22, v22, v23\n"
      "v_accvgpr_write_b32 a198, v20\n"
      "v_mfma_f32_16x16x32_bf16 v[54:57], a[12:15], v[68:71], v[32:35]\n"
      "ds_read_b64_tr_b16 a[118:119], v87 offset:64\n"
      "v_accvgpr_write_b32 a199, v22\n"
      "v_cvt_pk_bf16_f32 v166, v24, v25\n"
      "v_cvt_pk_bf16_f32 v167, v26, v27\n"
      "v_mfma_f32_16x16x32_bf16 v[32:35], a[12:15], v[72:75], v[32:35]\n"
      "buffer_load_dwordx4 v9, s[28:31], s97 offen offset:1024 lds\n"
      "v_cvt_pk_bf16_f32 v162, v28, v29\n"
      "v_cvt_pk_bf16_f32 v163, v30, v31\n"
      "v_add_u32 v86, -46144, v86\n"
      "v_cvt_pk_bf16_f32 v204, v54, v55\n"
      "v_mfma_f32_16x16x32_bf16 v[58:61], a[8:11], v[80:83], 0\n"
      "s_waitcnt lgkmcnt(7)\n"
      "ds_read_b128 a[8:11], v84 offset:64\n"
      "ds_read_b128 a[12:15], v86 offset:30752\n"
      "v_cvt_pk_bf16_f32 v32, v32, v33\n"
      "v_mfma_f32_16x16x32_bf16 v[62:65], a[16:19], v[68:71], v[58:61]\n"
      "v_cvt_pk_bf16_f32 v34, v34, v35\n"
      "v_accvgpr_write_b32 a216, v32\n"
      "v_mfma_f32_16x16x32_bf16 v[58:61], a[16:19], v[72:75], v[58:61]\n"
      "v_accvgpr_write_b32 a217, v34\n"
      "v_cvt_pk_bf16_f32 v205, v56, v57\n"
      "v_mfma_f32_16x16x32_bf16 v[20:23], a[20:23], v[76:79], 0\n"
      "buffer_load_dwordx4 v89, s[28:31], s97 offen offset:2048 lds\n"
      "v_add_u32 v84, -46144, v84\n"
      "v_mfma_f32_16x16x32_bf16 v[24:27], a[24:27], v[68:71], v[20:23]\n"
      "Lconv_p1:\n"
      "s_cbranch_execz Lconv_r0\n"
      "s_nop 0\n"
      "s_add_u32 m0, s92, 86112\n"
      "s_bfe_u32 s97, 0x40106131, s43\n"
      "ds_read_b128 a[16:19], v84 offset:27680\n"
      "s_mul_i32 s97, s97, 384\n"
      "s_add_u32 s97, s97, s6\n"
      "v_cvt_pk_bf16_f32 v212, v58, v59\n"
      "v_mfma_f32_16x16x32_bf16 v[20:23], a[24:27], v[72:75], v[20:23]\n"
      "ds_read_b128 a[24:27], v86 offset:30816\n"
      "v_cvt_pk_bf16_f32 v213, v60, v61\n"
      "v_cvt_pk_bf16_f32 v208, v62, v63\n"
      "v_cvt_pk_bf16_f32 v209, v64, v65\n"
      "v_add_u32 v85, -46144, v85\n"
      "v_mfma_f32_16x16x32_bf16 v[28:31], a[28:31], v[76:79], 0\n"
      "ds_read_b128 a[20:23], v85 offset:27680\n"
      "v_add_u32 v87, -46144, v87\n"
      "v_mfma_f32_16x16x32_bf16 v[32:35], a[32:35], v[68:71], v[28:31]\n"
      "ds_read_b64_tr_b16 a[120:121], v87 offset:27680\n"
      "ds_read_b64_tr_b16 a[128:129], v87 offset:27744\n"
      "v_cvt_pk_bf16_f32 v20, v20, v21\n"
      "v_mfma_f32_16x16x32_bf16 v[28:31], a[32:35], v[72:75], v[28:31]\n"
      "buffer_load_dwordx4 v8, s[28:31], s97 offen offset:0 lds\n"
      "v_cvt_pk_bf16_f32 v22, v22, v23\n"
      "v_accvgpr_write_b32 a220, v20\n"
      "v_accvgpr_write_b32 a221, v22\n"
      "v_add_u32 v88, -46144, v88\n"
      "v_mfma_f32_16x16x32_bf16 v[54:57], a[28:31], v[80:83], 0\n"
      "ds_read_b64_tr_b16 a[124:125], v88 offset:27680\n"
      "ds_read_b128 a[28:31], v84 offset:27744\n"
      "v_mfma_f32_16x16x32_bf16 v[58:61], a[0:3], v[68:71], v[54:57]\n"
      "v_cvt_pk_bf16_f32 v216, v24, v25\n"
      "v_cvt_pk_bf16_f32 v217, v26, v27\n"
      "v_mfma_f32_16x16x32_bf16 v[54:57], a[0:3], v[72:75], v[54:57]\n"
      "s_waitcnt lgkmcnt(5)\n"
      "v_cvt_pk_bf16_f32 v28, v28, v29\n"
      "v_cvt_pk_bf16_f32 v30, v30, v31\n"
      "v_mfma_f32_16x16x32_bf16 v[62:65], a[4:7], v[76:79], 0\n"
      "buffer_load_dwordx4 v9, s[28:31], s97 offen offset:1024 lds\n"
      "v_accvgpr_write_b32 a218, v28\n"
      "v_mfma_f32_16x16x32_bf16 v[20:23], a[8:11], v[68:71], v[62:65]\n"
      "v_accvgpr_write_b32 a219, v30\n"
      "v_cvt_pk_bf16_f32 v206, v32, v33\n"
      "v_mfma_f32_16x16x32_bf16 v[62:65], a[8:11], v[72:75], v[62:65]\n"
      "v_cvt_pk_bf16_f32 v207, v34, v35\n"
      "v_cvt_pk_bf16_f32 v214, v54, v55\n"
      "v_cvt_pk_bf16_f32 v215, v56, v57\n"
      "v_mfma_f32_16x16x32_bf16 v[24:27], a[12:15], v[76:79], 0\n"
      "v_cvt_pk_bf16_f32 v210, v58, v59\n"
      "v_cvt_pk_bf16_f32 v211, v60, v61\n"
      "v_mfma_f32_16x16x32_bf16 v[28:31], a[16:19], v[68:71], v[24:27]\n"
      "v_cvt_pk_bf16_f32 v218, v20, v21\n"
      "v_cvt_pk_bf16_f32 v62, v62, v63\n"
      "v_mfma_f32_16x16x32_bf16 v[24:27], a[16:19], v[72:75], v[24:27]\n"
      "s_waitcnt lgkmcnt(0)\n"
      "s_waitcnt vmcnt(12)\n"
      "s_cbranch_vccnz Lconv_h5\n"
      "s_barrier\n"
      "Lconv_h5:\n"
      "buffer_load_dwordx4 v89, s[28:31], s97 offen offset:2048 lds\n"
      "v_mfma_f32_16x16x32_bf16 v[32:35], a[12:15], v[80:83], 0\n"
      "s_add_u32 m0, s92, 98400\n"
      "s_bfe_u32 s97, 0x71411170, s43\n"
      "s_mul_i32 s97, s97, 384\n"
      "s_add_u32 s97, s97, s6\n"
      "v_cvt_pk_bf16_f32 v64, v64, v65\n"
      "v_accvgpr_write_b32 a222, v62\n"
      "v_mfma_f32_16x16x32_bf16 v[54:57], a[20:23], v[68:71], v[32:35]\n"
      "s_cbranch_vccz Lconv_h6\n"
      "s_barrier\n"
      "Lconv_h6:\n"
      "ds_read_b128 a[32:35], v86 offset:12288\n"
      "ds_read_b128 a[0:3], v84 offset:9216\n"
      "ds_read_b128 a[4:7], v85 offset:9216\n"
      "ds_read_b128 a[8:11], v86 offset:12352\n"
      "v_mfma_f32_16x16x32_bf16 v[32:35], a[20:23], v[72:75], v[32:35]\n"
      "ds_read_b128 a[12:15], v84 offset:9280\n"
      "ds_read_b128 a[16:19], v86 offset:3072\n"
      "v_accvgpr_write_b32 a223, v64\n"
      "v_cvt_pk_bf16_f32 v219, v22, v23\n"
      "v_cvt_pk_bf16_f32 v116, v24, v25\n"
      "v_mfma_f32_16x16x32_bf16 v[58:61], a[24:27], v[76:79], 0\n"
      "ds_read_b128 a[20:23], v84 offset:0\n"
      "ds_read_b128 a[24:27], v85 offset:0\n"
      "buffer_load_dwordx4 v8, s[28:31], s97 offen offset:0 lds\n"
      "v_cvt_pk_bf16_f32 v117, v26, v27\n"
      "v_mfma_f32_16x16x32_bf16 v[62:65], a[28:31], v[68:71], v[58:61]\n"
      "ds_read_b64_tr_b16 a[122:123], v87 offset:9216\n"
      "ds_read_b64_tr_b16 a[126:127], v88 offset:9216\n"
      "v_cvt_pk_bf16_f32 v112, v28, v29\n"
      "v_cvt_pk_bf16_f32 v113, v30, v31\n"
      "v_mfma_f32_16x16x32_bf16 v[58:61], a[28:31], v[72:75], v[58:61]\n"
      "s_waitcnt lgkmcnt(9)\n"
      "ds_read_b128 a[28:31], v86 offset:3136\n"
      "ds_read_b64_tr_b16 a[130:131], v87 offset:9280\n"
      "v_cvt_pk_bf16_f32 v120, v32, v33\n"
      "v_cvt_pk_bf16_f32 v121, v34, v35\n"
      "v_mfma_f32_16x16x32_bf16 v[20:23], a[32:35], v[76:79], 0\n"
      "s_waitcnt lgkmcnt(10)\n"
      "ds_read_b64_tr_b16 a[132:133], v87 offset:0\n"
      "ds_read_b64_tr_b16 a[136:137], v88 offset:0\n"
      "v_mfma_f32_16x16x32_bf16 v[24:27], a[0:3], v[68:71], v[20:23]\n"
      "ds_read_b64_tr_b16 a[140:141], v87 offset:64\n"
      "ds_read_b64_tr_b16 a[134:135], v87 offset:18464\n"
      "v_cvt_pk_bf16_f32 v54, v54, v55\n"
      "v_cvt_pk_bf16_f32 v56, v56, v57\n"
      "v_mfma_f32_16x16x32_bf16 v[20:23], a[0:3], v[72:75], v[20:23]\n"
      "ds_read_b128 a[0:3], v86 offset:21536\n"
      "ds_read_b64_tr_b16 a[138:139], v88 offset:18464\n"
      "buffer_load_dwordx4 v9, s[28:31], s97 offen offset:1024 lds\n"
      "v_accvgpr_write_b32 a200, v54\n"
      "v_accvgpr_write_b32 a201, v56\n"
      "v_mfma_f32_16x16x32_bf16 v[28:31], a[32:35], v[80:83], 0\n"
      "s_waitcnt lgkmcnt(10)\n"
      "ds_read_b128 a[32:35], v84 offset:64\n"
      "ds_read_b64_tr_b16 a[142:143], v87 offset:18528\n"
      "v_mfma_f32_16x16x32_bf16 v[32:35], a[4:7], v[68:71], v[28:31]\n"
      "ds_read_b64_tr_b16 a[144:145], v87 offset:36928\n"
      "ds_read_b64_tr_b16 a[148:149], v88 offset:36928\n"
      "v_cvt_pk_bf16_f32 v128, v58, v59\n"
      "v_cvt_pk_bf16_f32 v129, v60, v61\n"
      "v_cvt_pk_bf16_f32 v124, v62, v63\n"
      "v_mfma_f32_16x16x32_bf16 v[28:31], a[4:7], v[72:75], v[28:31]\n"
      "ds_read_b128 a[4:7], v84 offset:18464\n"
      "ds_read_b64_tr_b16 a[152:153], v87 offset:36992\n"
      "v_cvt_pk_bf16_f32 v125, v64, v65\n"
      "v_cvt_pk_bf16_f32 v118, v20, v21\n"
      "v_cvt_pk_bf16_f32 v119, v22, v23\n"
      "v_mfma_f32_16x16x32_bf16 v[54:57], a[8:11], v[76:79], 0\n"
      "ds_read_b128 a[8:11], v85 offset:18464\n"
      "ds_read_b64_tr_b16 a[146:147], v87 offset:55392\n"
      "buffer_load_dwordx4 v89, s[28:31], s97 offen offset:2048 lds\n"
      "v_mfma_f32_16x16x32_bf16 v[58:61], a[12:15], v[68:71], v[54:57]\n"
      "s_cmp_lt_u32 s40, 3\n"
      "s_cselect_b32 s97, 27680, 0\n"
      "ds_read_b64_tr_b16 a[150:151], v88 offset:55392\n"
      "ds_read_b64_tr_b16 a[154:155], v87 offset:55456\n"
      "s_add_u32 s97, s97, s92\n"
      "s_add_u32 m0, s97, 36928\n"
      "s_bfe_u32 s97, 0x21805020, s43\n"
      "s_mul_i32 s97, s97, 384\n"
      "s_add_u32 s97, s97, s6\n"
      "v_cvt_pk_bf16_f32 v114, v24, v25\n"
      "v_cvt_pk_bf16_f32 v115, v26, v27\n"
      "v_cvt_pk_bf16_f32 v122, v28, v29\n"
      "v_mfma_f32_16x16x32_bf16 v[54:57], a[12:15], v[72:75], v[54:57]\n"
      "ds_read_b128 a[12:15], v86 offset:21600\n"
      "v_cvt_pk_bf16_f32 v123, v30, v31\n"
      "v_cvt_pk_bf16_f32 v32, v32, v33\n"
      "v_cvt_pk_bf16_f32 v34, v34, v35\n"
      "v_mfma_f32_16x16x32_bf16 v[62:65], a[16:19], v[76:79], 0\n"
      "v_accvgpr_write_b32 a202, v32\n"
      "v_accvgpr_write_b32 a203, v34\n"
      "v_mfma_f32_16x16x32_bf16 v[20:23], a[20:23], v[68:71], v[62:65]\n"
      "v_cvt_pk_bf16_f32 v130, v54, v55\n"
      "v_cvt_pk_bf16_f32 v131, v56, v57\n"
      "v_mfma_f32_16x16x32_bf16 v[62:65], a[20:23], v[72:75], v[62:65]\n"
      "ds_read_b128 a[20:23], v86 offset:40000\n"
      "buffer_load_dwordx4 v8, s[28:31], s97 offen offset:0 lds\n"
      "v_cvt_pk_bf16_f32 v126, v58, v59\n"
      "v_cvt_pk_bf16_f32 v127, v60, v61\n"
      "v_mfma_f32_16x16x32_bf16 v[24:27], a[16:19], v[80:83], 0\n"
      "ds_read_b128 a[16:19], v84 offset:18528\n"
      "v_cvt_pk_bf16_f32 v20, v20, v21\n"
      "v_mfma_f32_16x16x32_bf16 v[28:31], a[24:27], v[68:71], v[24:27]\n"
      "v_cvt_pk_bf16_f32 v168, v62, v63\n"
      "v_cvt_pk_bf16_f32 v169, v64, v65\n"
      "v_mfma_f32_16x16x32_bf16 v[24:27], a[24:27], v[72:75], v[24:27]\n"
      "s_waitcnt lgkmcnt(2)\n"
      "ds_read_b128 a[24:27], v84 offset:36928\n"
      "v_cvt_pk_bf16_f32 v22, v22, v23\n"
      "v_mfma_f32_16x16x32_bf16 v[32:35], a[28:31], v[76:79], 0\n"
      "ds_read_b128 a[28:31], v85 offset:36928\n"
      "buffer_load_dwordx4 v9, s[28:31], s97 offen offset:1024 lds\n"
      "v_mfma_f32_16x16x32_bf16 v[54:57], a[32:35], v[68:71], v[32:35]\n"
      "v_accvgpr_write_b32 a204, v20\n"
      "v_accvgpr_write_b32 a205, v22\n"
      "v_mfma_f32_16x16x32_bf16 v[32:35], a[32:35], v[72:75], v[32:35]\n"
      "ds_read_b128 a[32:35], v86 offset:40064\n"
      "v_cvt_pk_bf16_f32 v176, v24, v25\n"
      "v_cvt_pk_bf16_f32 v177, v26, v27\n"
      "v_cvt_pk_bf16_f32 v172, v28, v29\n"
      "v_cvt_pk_bf16_f32 v173, v30, v31\n"
      "v_mfma_f32_16x16x32_bf16 v[58:61], a[0:3], v[76:79], 0\n"
      "v_cvt_pk_bf16_f32 v180, v54, v55\n"
      "v_cvt_pk_bf16_f32 v181, v56, v57\n"
      "v_mfma_f32_16x16x32_bf16 v[62:65], a[4:7], v[68:71], v[58:61]\n"
      "v_cvt_pk_bf16_f32 v32, v32, v33\n"
      "v_cvt_pk_bf16_f32 v34, v34, v35\n"
      "v_mfma_f32_16x16x32_bf16 v[58:61], a[4:7], v[72:75], v[58:61]\n"
      "ds_read_b128 a[4:7], v86 offset:58464\n"
      "buffer_load_dwordx4 v89, s[28:31], s97 offen offset:2048 lds\n"
      "v_accvgpr_write_b32 a224, v32\n"
      "v_accvgpr_write_b32 a225, v34\n"
      "v_mfma_f32_16x16x32_bf16 v[20:23], a[0:3], v[80:83], 0\n"
      "s_cmp_lt_u32 s40, 2\n"
      "s_cselect_b32 s97, 6144, 0\n"
      "ds_read_b128 a[0:3], v84 offset:36992\n"
      "s_add_u32 s97, s97, s92\n"
      "s_add_u32 m0, s97, 43072\n"
      "s_bfe_u32 s97, 0x81518151, s43\n"
      "s_mul_i32 s97, s97, 384\n"
      "s_add_u32 s97, s97, s6\n"
      "v_cvt_pk_bf16_f32 v170, v58, v59\n"
      "v_mfma_f32_16x16x32_bf16 v[24:27], a[8:11], v[68:71], v[20:23]\n"
      "v_cvt_pk_bf16_f32 v171, v60, v61\n"
      "v_cvt_pk_bf16_f32 v62, v62, v63\n"
      "v_mfma_f32_16x16x32_bf16 v[20:23], a[8:11], v[72:75], v[20:23]\n"
      "ds_read_b128 a[8:11], v84 offset:55392\n"
      "v_cvt_pk_bf16_f32 v64, v64, v65\n"
      "v_mfma_f32_16x16x32_bf16 v[28:31], a[12:15], v[76:79], 0\n"
      "s_waitcnt lgkmcnt(3)\n"
      "ds_read_b128 a[12:15], v85 offset:55392\n"
      "buffer_load_dwordx4 v8, s[28:31], s97 offen offset:0 lds\n"
      "v_accvgpr_write_b32 a206, v62\n"
      "v_mfma_f32_16x16x32_bf16 v[32:35], a[16:19], v[68:71], v[28:31]\n"
      "v_accvgpr_write_b32 a207, v64\n"
      "v_cvt_pk_bf16_f32 v178, v20, v21\n"
      "v_mfma_f32_16x16x32_bf16 v[28:31], a[16:19], v[72:75], v[28:31]\n"
      "ds_read_b128 a[16:19], v86 offset:58528\n"
      "v_cvt_pk_bf16_f32 v179, v22, v23\n"
      "v_cvt_pk_bf16_f32 v174, v24, v25\n"
      "v_cvt_pk_bf16_f32 v175, v26, v27\n"
      "v_add_u32 v86, 46144, v86\n"
      "v_mfma_f32_16x16x32_bf16 v[54:57], a[20:23], v[76:79], 0\n"
      "v_cvt_pk_bf16_f32 v182, v32, v33\n"
      "v_cvt_pk_bf16_f32 v183, v34, v35\n"
      "v_mfma_f32_16x16x32_bf16 v[58:61], a[24:27], v[68:71], v[54:57]\n"
      "v_cvt_pk_bf16_f32 v28, v28, v29\n"
      "v_cvt_pk_bf16_f32 v30, v30, v31\n"
      "v_mfma_f32_16x16x32_bf16 v[54:57], a[24:27], v[72:75], v[54:57]\n"
      "buffer_load_dwordx4 v9, s[28:31], s97 offen offset:1024 lds\n"
      "v_accvgpr_write_b32 a226, v28\n"
      "v_mfma_f32_16x16x32_bf16 v[62:65], a[20:23], v[80:83], 0\n"
      "ds_read_b128 a[20:23], v84 offset:55456\n"
      "v_accvgpr_write_b32 a227, v30\n"
      "v_mfma_f32_16x16x32_bf16 v[20:23], a[28:31], v[68:71], v[62:65]\n"
      "v_cvt_pk_bf16_f32 v220, v58, v59\n"
      "v_cvt_pk_bf16_f32 v221, v60, v61\n"
      "v_mfma_f32_16x16x32_bf16 v[62:65], a[28:31], v[72:75], v[62:65]\n"
      "v_cvt_pk_bf16_f32 v224, v54, v55\n"
      "v_cvt_pk_bf16_f32 v225, v56, v57\n"
      "v_add_u32 v84, 46144, v84\n"
      "v_add_u32 v85, 46144, v85\n"
      "v_mfma_f32_16x16x32_bf16 v[24:27], a[32:35], v[76:79], 0\n"
      "s_waitcnt lgkmcnt(1)\n"
      "buffer_load_dwordx4 v89, s[28:31], s97 offen offset:2048 lds\n"
      "v_cvt_pk_bf16_f32 v228, v20, v21\n"
      "v_mfma_f32_16x16x32_bf16 v[28:31], a[0:3], v[68:71], v[24:27]\n"
      "s_add_u32 m0, s46, 39168\n"
      "s_add_u32 s51, s51, s12\n"
      "v_cvt_pk_bf16_f32 v62, v62, v63\n"
      "v_cvt_pk_bf16_f32 v64, v64, v65\n"
      "v_mfma_f32_16x16x32_bf16 v[24:27], a[0:3], v[72:75], v[24:27]\n"
      "v_accvgpr_write_b32 a228, v62\n"
      "v_accvgpr_write_b32 a229, v64\n"
      "v_mfma_f32_16x16x32_bf16 v[32:35], a[4:7], v[76:79], 0\n"
      "v_cvt_pk_bf16_f32 v229, v22, v23\n"
      "v_cvt_pk_bf16_f32 v232, v28, v29\n"
      "v_mfma_f32_16x16x32_bf16 v[54:57], a[8:11], v[68:71], v[32:35]\n"
      "v_cvt_pk_bf16_f32 v233, v30, v31\n"
      "v_add_u32 v87, 46144, v87\n"
      "v_mfma_f32_16x16x32_bf16 v[32:35], a[8:11], v[72:75], v[32:35]\n"
      "s_waitcnt lgkmcnt(0)\n"
      "s_waitcnt vmcnt(6)\n"
      "s_cbranch_vccnz Lconv_h7\n"
      "s_barrier\n"
      "Lconv_h7:\n"
      "buffer_load_dwordx4 v16, s[32:35], s51 offen lds\n"
      "v_mfma_f32_16x16x32_bf16 v[58:61], a[4:7], v[80:83], 0\n"
      "s_add_u32 m0, s46, 39168\n"
      "v_cvt_pk_bf16_f32 v236, v24, v25\n"
      "v_cvt_pk_bf16_f32 v237, v26, v27\n"
      "v_mfma_f32_16x16x32_bf16 v[62:65], a[12:15], v[68:71], v[58:61]\n"
      "s_cbranch_vccz Lconv_h8\n"
      "s_barrier\n"
      "Lconv_h8:\n"
      "ds_read_b128 a[24:27], v86 offset:30752\n"
      "ds_read_b128 a[28:31], v84 offset:27680\n"
      "ds_read_b128 a[32:35], v85 offset:27680\n"
      "ds_read_b128 a[0:3], v86 offset:30816\n"
      "buffer_load_dwordx4 v17, s[32:35], s51 offen offset:1024 lds\n"
      "v_mfma_f32_16x16x32_bf16 v[58:61], a[12:15], v[72:75], v[58:61]\n"
      "s_add_u32 m0, s46, 39168\n"
      "ds_read_b128 a[4:7], v84 offset:27744\n"
      "ds_read_b128 a[8:11], v86 offset:39968\n"
      "buffer_load_dwordx4 v17, s[32:35], s51 offen offset:2048 lds\n"
      "v_cvt_pk_bf16_f32 v226, v32, v33\n"
      "v_cvt_pk_bf16_f32 v227, v34, v35\n"
      "v_mfma_f32_16x16x32_bf16 v[20:23], a[16:19], v[76:79], 0\n"
      "s_sub_u32 m0, s94, s59\n"
      "s_add_u32 s96, s51, s15\n"
      "ds_read_b128 a[12:15], v84 offset:36896\n"
      "ds_read_b128 a[16:19], v85 offset:36896\n"
      "buffer_load_dwordx4 v19, s[32:35], s96 offen lds\n"
      "v_cvt_pk_bf16_f32 v222, v54, v55\n"
      "v_mfma_f32_16x16x32_bf16 v[24:27], a[20:23], v[68:71], v[20:23]\n"
      "s_add_u32 m0, s46, 0\n"
      "s_add_u32 s51, s51, s12\n"
      "ds_read_b64_tr_b16 a[36:37], v2 offset:13056\n"
      "ds_read_b64_tr_b16 a[38:39], v2 offset:13064\n"
      "buffer_load_dwordx4 v16, s[32:35], s51 offen lds\n"
      "v_cvt_pk_bf16_f32 v223, v56, v57\n"
      "v_cvt_pk_bf16_f32 v58, v58, v59\n"
      "v_mfma_f32_16x16x32_bf16 v[20:23], a[20:23], v[72:75], v[20:23]\n"
      "s_waitcnt lgkmcnt(9)\n"
      "s_add_u32 m0, s46, 0\n"
      "ds_read_b128 a[20:23], v86 offset:40032\n"
      "ds_read_b64_tr_b16 a[40:41], v3 offset:13056\n"
      "buffer_load_dwordx4 v17, s[32:35], s51 offen offset:1024 lds\n"
      "v_cvt_pk_bf16_f32 v60, v60, v61\n"
      "v_accvgpr_write_b32 a230, v58\n"
      "v_mfma_f32_16x16x32_bf16 v[28:31], a[24:27], v[76:79], 0\n"
      "s_waitcnt lgkmcnt(10)\n"
      "s_add_u32 m0, s46, 0\n"
      "ds_read_b64_tr_b16 a[42:43], v3 offset:13064\n"
      "ds_read_b64_tr_b16 a[44:45], v2 offset:13120\n"
      "buffer_load_dwordx4 v17, s[32:35], s51 offen offset:2048 lds\n"
      "v_mfma_f32_16x16x32_bf16 v[32:35], a[28:31], v[68:71], v[28:31]\n"
      "s_mov_b32 m0, s18\n"
      "ds_read_b64_tr_b16 a[46:47], v2 offset:13128\n"
      "ds_read_b64_tr_b16 a[48:49], v3 offset:13120\n"
      "v_accvgpr_write_b32 a231, v60\n"
      "v_cvt_pk_bf16_f32 v230, v62, v63\n"
      "v_cvt_pk_bf16_f32 v231, v64, v65\n"
      "v_mfma_f32_16x16x32_bf16 v[28:31], a[28:31], v[72:75], v[28:31]\n"
      "ds_read_b64_tr_b16 a[50:51], v3 offset:13128\n"
      "ds_read_b64_tr_b16 a[52:53], v2 offset:13184\n"
      "v_cvt_pk_bf16_f32 v238, v20, v21\n"
      "v_cvt_pk_bf16_f32 v239, v22, v23\n"
      "v_cvt_pk_bf16_f32 v234, v24, v25\n"
      "v_mfma_f32_16x16x32_bf16 v[54:57], a[24:27], v[80:83], 0\n"
      "s_waitcnt lgkmcnt(10)\n"
      "ds_read_b128 a[24:27], v84 offset:36960\n"
      "ds_read_b64_tr_b16 a[54:55], v2 offset:13192\n"
      "v_mfma_f32_16x16x32_bf16 v[58:61], a[32:35], v[68:71], v[54:57]\n"
      "ds_read_b64_tr_b16 a[56:57], v3 offset:13184\n"
      "ds_read_b64_tr_b16 a[58:59], v3 offset:13192\n"
      "v_cvt_pk_bf16_f32 v235, v26, v27\n"
      "v_cvt_pk_bf16_f32 v132, v28, v29\n"
      "v_mfma_f32_16x16x32_bf16 v[54:57], a[32:35], v[72:75], v[54:57]\n"
      "ds_read_b128 a[28:31], v86 offset:49184\n"
      "ds_read_b128 a[32:35], v84 offset:46112\n"
      "v_cvt_pk_bf16_f32 v133, v30, v31\n"
      "v_cvt_pk_bf16_f32 v32, v32, v33\n"
      "v_mfma_f32_16x16x32_bf16 v[62:65], a[0:3], v[76:79], 0\n"
      "ds_read_b128 a[0:3], v85 offset:46112\n"
      "ds_read_b64_tr_b16 a[156:157], v87 offset:27680\n"
      "v_mfma_f32_16x16x32_bf16 v[20:23], a[4:7], v[68:71], v[62:65]\n"
      "ds_read_b64_tr_b16 a[164:165], v87 offset:27744\n"
      "ds_read_b64_tr_b16 a[158:159], v87 offset:36896\n"
      "v_cvt_pk_bf16_f32 v34, v34, v35\n"
      "v_accvgpr_write_b32 a208, v32\n"
      "v_accvgpr_write_b32 a209, v34\n"
      "v_mfma_f32_16x16x32_bf16 v[62:65], a[4:7], v[72:75], v[62:65]\n"
      "ds_read_b64_tr_b16 a[166:167], v87 offset:36960\n"
      "ds_read_b64_tr_b16 a[168:169], v87 offset:46112\n"
      "v_cvt_pk_bf16_f32 v140, v54, v55\n"
      "v_cvt_pk_bf16_f32 v141, v56, v57\n"
      "v_cvt_pk_bf16_f32 v136, v58, v59\n"
      "v_mfma_f32_16x16x32_bf16 v[24:27], a[8:11], v[76:79], 0\n"
      "ds_read_b128 a[4:7], v86 offset:49248\n"
      "ds_read_b64_tr_b16 a[176:177], v87 offset:46176\n"
      "v_mfma_f32_16x16x32_bf16 v[28:31], a[12:15], v[68:71], v[24:27]\n"
      "v_cvt_pk_bf16_f32 v137, v60, v61\n"
      "v_cvt_pk_bf16_f32 v144, v62, v63\n"
      "v_cvt_pk_bf16_f32 v145, v64, v65\n"
      "v_add_u32 v88, 46144, v88\n"
      "v_mfma_f32_16x16x32_bf16 v[24:27], a[12:15], v[72:75], v[24:27]\n"
      "ds_read_b64_tr_b16 a[160:161], v88 offset:27680\n"
      "ds_read_b64_tr_b16 a[162:163], v88 offset:36896\n"
      "v_cvt_pk_bf16_f32 v20, v20, v21\n"
      "v_cvt_pk_bf16_f32 v22, v22, v23\n"
      "v_mfma_f32_16x16x32_bf16 v[32:35], a[8:11], v[80:83], 0\n"
      "ds_read_b64_tr_b16 a[172:173], v88 offset:46112\n"
      "ds_read_b128 a[8:11], v84 offset:46176\n"
      "v_mfma_f32_16x16x32_bf16 v[54:57], a[16:19], v[68:71], v[32:35]\n"
      "v_accvgpr_write_b32 a212, v20\n"
      "v_accvgpr_write_b32 a213, v22\n"
      "v_mfma_f32_16x16x32_bf16 v[32:35], a[16:19], v[72:75], v[32:35]\n"
      "s_waitcnt lgkmcnt(6)\n"
      "v_cvt_pk_bf16_f32 v134, v24, v25\n"
      "v_cvt_pk_bf16_f32 v135, v26, v27\n"
      "v_mfma_f32_16x16x32_bf16 v[58:61], a[20:23], v[76:79], 0\n"
      "v_cvt_pk_bf16_f32 v28, v28, v29\n"
      "v_cvt_pk_bf16_f32 v138, v54, v55\n"
      "v_mfma_f32_16x16x32_bf16 v[62:65], a[24:27], v[68:71], v[58:61]\n"
      "v_cvt_pk_bf16_f32 v139, v56, v57\n"
      "v_cvt_pk_bf16_f32 v30, v30, v31\n"
      "v_mfma_f32_16x16x32_bf16 v[58:61], a[24:27], v[72:75], v[58:61]\n"
      "v_accvgpr_write_b32 a210, v28\n"
      "v_accvgpr_write_b32 a211, v30\n"
      "v_mfma_f32_4x4x4_16b_bf16 v[36:39], a[72:73], a[36:37], 0\n"
      "v_cvt_pk_bf16_f32 v142, v32, v33\n"
      "v_cvt_pk_bf16_f32 v143, v34, v35\n"
      "v_mfma_f32_4x4x4_16b_bf16 v[40:43], a[72:73], a[38:39], 0\n"
      "v_cvt_pk_bf16_f32 v62, v62, v63\n"
      "v_accvgpr_write_b32 a214, v62\n"
      "v_mfma_f32_4x4x4_16b_bf16 v[44:47], a[76:77], a[36:37], 0\n"
      "ds_read_b64_tr_b16 a[36:37], v2 offset:26112\n"
      "v_cvt_pk_bf16_f32 v146, v58, v59\n"
      "v_mfma_f32_4x4x4_16b_bf16 v[48:51], a[76:77], a[38:39], 0\n"
      "ds_read_b64_tr_b16 a[38:39], v2 offset:26120\n"
      "v_cvt_pk_bf16_f32 v147, v60, v61\n"
      "v_mfma_f32_4x4x4_16b_bf16 v[36:39], a[74:75], a[40:41], v[36:39]\n"
      "ds_read_b64_tr_b16 a[40:41], v3 offset:26112\n"
      "v_cvt_pk_bf16_f32 v64, v64, v65\n"
      "v_mfma_f32_4x4x4_16b_bf16 v[40:43], a[74:75], a[42:43], v[40:43]\n"
      "ds_read_b64_tr_b16 a[42:43], v3 offset:26120\n"
      "v_accvgpr_write_b32 a215, v64\n"
      "v_mfma_f32_4x4x4_16b_bf16 v[52:55], a[72:73], a[44:45], 0\n"
      "v_cvt_pk_bf16_f32 v44, v44, v48\n"
      "v_cvt_pk_bf16_f32 v45, v45, v49\n"
      "v_mfma_f32_4x4x4_16b_bf16 v[56:59], a[72:73], a[46:47], 0\n"
      "ds_write_addtid_b32 v44 offset:768\n"
      "ds_write_addtid_b32 v45 offset:1536\n"
      "v_mfma_f32_4x4x4_16b_bf16 v[60:63], a[76:77], a[44:45], 0\n"
      "ds_read_b64_tr_b16 a[44:45], v2 offset:26176\n"
      "v_cvt_pk_bf16_f32 v36, v36, v40\n"
      "v_mfma_f32_4x4x4_16b_bf16 v[64:67], a[76:77], a[46:47], 0\n"
      "ds_write_addtid_b32 v36 offset:0\n"
      "ds_read_b64_tr_b16 a[46:47], v2 offset:26184\n"
      "v_cvt_pk_bf16_f32 v38, v38, v42\n"
      "v_mfma_f32_4x4x4_16b_bf16 v[52:55], a[74:75], a[48:49], v[52:55]\n"
      "ds_write_addtid_b32 v38 offset:2304\n"
      "ds_read_b64_tr_b16 a[48:49], v3 offset:26176\n"
      "v_mfma_f32_4x4x4_16b_bf16 v[56:59], a[74:75], a[50:51], v[56:59]\n"
      "ds_read_b64_tr_b16 a[50:51], v3 offset:26184\n"
      "v_cvt_pk_bf16_f32 v46, v46, v50\n"
      "v_cvt_pk_bf16_f32 v47, v47, v51\n"
      "v_mfma_f32_4x4x4_16b_bf16 v[36:39], a[72:73], a[52:53], 0\n"
      "ds_write_addtid_b32 v46 offset:3072\n"
      "ds_write_addtid_b32 v47 offset:3840\n"
      "s_mov_b32 m0, s18\n"
      "v_mfma_f32_4x4x4_16b_bf16 v[40:43], a[72:73], a[54:55], 0\n"
      "v_cvt_pk_bf16_f32 v52, v52, v56\n"
      "v_cvt_pk_bf16_f32 v54, v54, v58\n"
      "v_mfma_f32_4x4x4_16b_bf16 v[44:47], a[76:77], a[52:53], 0\n"
      "ds_write_addtid_b32 v52 offset:256\n"
      "ds_write_addtid_b32 v54 offset:2560\n"
      "ds_read_b64_tr_b16 a[52:53], v2 offset:26240\n"
      "v_mfma_f32_4x4x4_16b_bf16 v[48:51], a[76:77], a[54:55], 0\n"
      "ds_read_b64_tr_b16 a[54:55], v2 offset:26248\n"
      "v_cvt_pk_bf16_f32 v60, v60, v64\n"
      "v_mfma_f32_4x4x4_16b_bf16 v[36:39], a[74:75], a[56:57], v[36:39]\n"
      "ds_write_addtid_b32 v60 offset:1024\n"
      "ds_read_b64_tr_b16 a[56:57], v3 offset:26240\n"
      "v_mfma_f32_4x4x4_16b_bf16 v[40:43], a[74:75], a[58:59], v[40:43]\n"
      "s_waitcnt lgkmcnt(14)\n"
      "ds_read_b64_tr_b16 a[58:59], v3 offset:26248\n"
      "v_cvt_pk_bf16_f32 v61, v61, v65\n"
      "v_cvt_pk_bf16_f32 v62, v62, v66\n"
      "v_cvt_pk_bf16_f32 v63, v63, v67\n"
      "v_mfma_f32_4x4x4_16b_bf16 v[52:55], a[72:73], a[36:37], 0\n"
      "ds_write_addtid_b32 v61 offset:1792\n"
      "ds_write_addtid_b32 v62 offset:3328\n"
      "ds_write_addtid_b32 v63 offset:4096\n"
      "s_mov_b32 m0, s18\n"
      "v_mfma_f32_4x4x4_16b_bf16 v[56:59], a[72:73], a[38:39], 0\n"
      "v_cvt_pk_bf16_f32 v36, v36, v40\n"
      "v_cvt_pk_bf16_f32 v38, v38, v42\n"
      "v_mfma_f32_4x4x4_16b_bf16 v[60:63], a[76:77], a[36:37], 0\n"
      "ds_write_addtid_b32 v36 offset:512\n"
      "ds_write_addtid_b32 v38 offset:2816\n"
      "v_mfma_f32_4x4x4_16b_bf16 v[64:67], a[76:77], a[38:39], 0\n"
      "v_cvt_pk_bf16_f32 v44, v44, v48\n"
      "v_cvt_pk_bf16_f32 v45, v45, v49\n"
      "v_mfma_f32_4x4x4_16b_bf16 v[52:55], a[74:75], a[40:41], v[52:55]\n"
      "ds_write_addtid_b32 v44 offset:1280\n"
      "ds_write_addtid_b32 v45 offset:2048\n"
      "v_mfma_f32_4x4x4_16b_bf16 v[56:59], a[74:75], a[42:43], v[56:59]\n"
      "s_waitcnt lgkmcnt(14)\n"
      "v_cvt_pk_bf16_f32 v46, v46, v50\n"
      "v_cvt_pk_bf16_f32 v47, v47, v51\n"
      "v_mfma_f32_4x4x4_16b_bf16 v[36:39], a[72:73], a[44:45], 0\n"
      "ds_write_addtid_b32 v46 offset:3584\n"
      "ds_write_addtid_b32 v47 offset:4352\n"
      "s_mov_b32 m0, s18\n"
      "v_mfma_f32_4x4x4_16b_bf16 v[40:43], a[72:73], a[46:47], 0\n"
      "v_cvt_pk_bf16_f32 v52, v52, v56\n"
      "v_cvt_pk_bf16_f32 v54, v54, v58\n"
      "v_mfma_f32_4x4x4_16b_bf16 v[44:47], a[76:77], a[44:45], 0\n"
      "ds_write_addtid_b32 v52 offset:4608\n"
      "ds_write_addtid_b32 v54 offset:6912\n"
      "v_mfma_f32_4x4x4_16b_bf16 v[48:51], a[76:77], a[46:47], 0\n"
      "v_cvt_pk_bf16_f32 v60, v60, v64\n"
      "v_cvt_pk_bf16_f32 v61, v61, v65\n"
      "v_mfma_f32_4x4x4_16b_bf16 v[36:39], a[74:75], a[48:49], v[36:39]\n"
      "ds_write_addtid_b32 v60 offset:5376\n"
      "ds_write_addtid_b32 v61 offset:6144\n"
      "v_mfma_f32_4x4x4_16b_bf16 v[40:43], a[74:75], a[50:51], v[40:43]\n"
      "s_waitcnt lgkmcnt(13)\n"
      "v_cvt_pk_bf16_f32 v62, v62, v66\n"
      "v_cvt_pk_bf16_f32 v63, v63, v67\n"
      "v_mfma_f32_4x4x4_16b_bf16 v[52:55], a[72:73], a[52:53], 0\n"
      "ds_write_addtid_b32 v62 offset:7680\n"
      "ds_write_addtid_b32 v63 offset:8448\n"
      "s_mov_b32 m0, s18\n"
      "v_mfma_f32_4x4x4_16b_bf16 v[56:59], a[72:73], a[54:55], 0\n"
      "v_cvt_pk_bf16_f32 v44, v44, v48\n"
      "v_cvt_pk_bf16_f32 v45, v45, v49\n"
      "v_mfma_f32_4x4x4_16b_bf16 v[60:63], a[76:77], a[52:53], 0\n"
      "ds_write_addtid_b32 v44 offset:5632\n"
      "ds_write_addtid_b32 v45 offset:6400\n"
      "v_cvt_pk_bf16_f32 v46, v46, v50\n"
      "v_cvt_pk_bf16_f32 v47, v47, v51\n"
      "v_cvt_pk_bf16_f32 v36, v36, v40\n"
      "v_mfma_f32_4x4x4_16b_bf16 v[64:67], a[76:77], a[54:55], 0\n"
      "ds_write_addtid_b32 v46 offset:7936\n"
      "ds_write_addtid_b32 v47 offset:8704\n"
      "ds_write_addtid_b32 v36 offset:4864\n"
      "v_cvt_pk_bf16_f32 v38, v38, v42\n"
      "v_mfma_f32_4x4x4_16b_bf16 v[52:55], a[74:75], a[56:57], v[52:55]\n"
      "ds_write_addtid_b32 v38 offset:7168\n"
      "s_mov_b32 m0, s18\n"
      "v_cvt_pk_bf16_f32 v60, v60, v64\n"
      "v_cvt_pk_bf16_f32 v61, v61, v65\n"
      "v_cvt_pk_bf16_f32 v62, v62, v66\n"
      "v_cvt_pk_bf16_f32 v63, v63, v67\n"
      "v_mfma_f32_4x4x4_16b_bf16 v[56:59], a[74:75], a[58:59], v[56:59]\n"
      "ds_write_addtid_b32 v60 offset:5888\n"
      "ds_write_addtid_b32 v61 offset:6656\n"
      "ds_write_addtid_b32 v62 offset:8192\n"
      "ds_write_addtid_b32 v63 offset:8960\n"
      "v_mfma_f32_16x16x32_bf16 v[20:23], a[28:31], v[76:79], 0\n"
      "v_cvt_pk_bf16_f32 v52, v52, v56\n"
      "v_cvt_pk_bf16_f32 v54, v54, v58\n"
      "v_mfma_f32_16x16x32_bf16 v[24:27], a[32:35], v[68:71], v[20:23]\n"
      "ds_write_addtid_b32 v52 offset:5120\n"
      "ds_write_addtid_b32 v54 offset:7424\n"
      "s_add_u32 m0, s46, 13056\n"
      "s_add_u32 s51, s51, s12\n"
      "v_mfma_f32_16x16x32_bf16 v[20:23], a[32:35], v[72:75], v[20:23]\n"
      "s_waitcnt lgkmcnt(0)\n"
      "s_waitcnt vmcnt(7)\n"
      "s_cbranch_vccnz Lconv_h9\n"
      "s_barrier\n"
      "Lconv_h9:\n"
      "v_cvt_pk_bf16_f32 v184, v24, v25\n"
      "v_mfma_f32_16x16x32_bf16 v[28:31], a[28:31], v[80:83], 0\n"
      "v_cvt_pk_bf16_f32 v185, v26, v27\n"
      "v_mfma_f32_16x16x32_bf16 v[32:35], a[0:3], v[68:71], v[28:31]\n"
      "s_cbranch_vccz Lconv_h10\n"
      "s_barrier\n"
      "Lconv_h10:\n"
      "ds_read_b128 a[12:15], v86 offset:58400\n"
      "ds_read_b128 a[16:19], v84 offset:55328\n"
      "ds_read_b128 a[20:23], v85 offset:55328\n"
      "ds_read_b128 a[24:27], v86 offset:58464\n"
      "v_mfma_f32_16x16x32_bf16 v[28:31], a[0:3], v[72:75], v[28:31]\n"
      "Lconv_p2:\n"
      "s_cbranch_execz Lconv_r0\n"
      "s_nop 0\n"
      "ds_read_b128 a[28:31], v84 offset:55392\n"
      "ds_read_b128 a[32:35], v86 offset:21536\n"
      "v_mfma_f32_16x16x32_bf16 v[54:57], a[4:7], v[76:79], 0\n"
      "ds_read_b128 a[0:3], v84 offset:18464\n"
      "ds_read_b128 a[4:7], v85 offset:18464\n"
      "v_mfma_f32_16x16x32_bf16 v[58:61], a[8:11], v[68:71], v[54:57]\n"
      "ds_read_b64_tr_b16 a[170:171], v87 offset:55328\n"
      "ds_read_b64_tr_b16 a[174:175], v88 offset:55328\n"
      "v_cvt_pk_bf16_f32 v188, v20, v21\n"
      "v_cvt_pk_bf16_f32 v189, v22, v23\n"
      "v_mfma_f32_16x16x32_bf16 v[54:57], a[8:11], v[72:75], v[54:57]\n"
      "s_waitcnt lgkmcnt(9)\n"
      "ds_read_b128 a[8:11], v86 offset:21600\n"
      "ds_read_b64_tr_b16 a[178:179], v87 offset:55392\n"
      "buffer_load_dwordx4 v16, s[32:35], s51 offen lds\n"
      "v_cvt_pk_bf16_f32 v28, v28, v29\n"
      "v_cvt_pk_bf16_f32 v30, v30, v31\n"
      "v_mfma_f32_16x16x32_bf16 v[62:65], a[12:15], v[76:79], 0\n"
      "s_waitcnt lgkmcnt(10)\n"
      "s_add_u32 m0, s46, 13056\n"
      "ds_read_b64_tr_b16 a[180:181], v87 offset:18464\n"
      "ds_read_b64_tr_b16 a[184:185], v88 offset:18464\n"
      "buffer_load_dwordx4 v17, s[32:35], s51 offen offset:1024 lds\n"
      "v_mfma_f32_16x16x32_bf16 v[20:23], a[16:19], v[68:71], v[62:65]\n"
      "s_add_u32 m0, s46, 13056\n"
      "s_add_u32 s96, s51, s15\n"
      "ds_read_b64_tr_b16 a[188:189], v87 offset:18528\n"
      "ds_read_b64_tr_b16 a[182:183], v87 offset:0\n"
      "buffer_load_dwordx4 v17, s[32:35], s51 offen offset:2048 lds\n"
      "v_accvgpr_write_b32 a232, v28\n"
      "v_accvgpr_write_b32 a233, v30\n"
      "v_mfma_f32_16x16x32_bf16 v[62:65], a[16:19], v[72:75], v[62:65]\n"
      "s_mov_b32 m0, s59\n"
      "ds_read_b128 a[16:19], v86 offset:3072\n"
      "ds_read_b64_tr_b16 a[186:187], v88 offset:0\n"
      "buffer_load_dwordx4 v19, s[32:35], s96 offen lds\n"
      "v_cvt_pk_bf16_f32 v192, v32, v33\n"
      "v_cvt_pk_bf16_f32 v193, v34, v35\n"
      "v_mfma_f32_16x16x32_bf16 v[24:27], a[12:15], v[80:83], 0\n"
      "s_waitcnt lgkmcnt(10)\n"
      "ds_read_b128 a[12:15], v84 offset:18528\n"
      "ds_read_b64_tr_b16 a[190:191], v87 offset:64\n"
      "v_mfma_f32_16x16x32_bf16 v[28:31], a[20:23], v[68:71], v[24:27]\n"
      "v_cvt_pk_bf16_f32 v200, v54, v55\n"
      "v_cvt_pk_bf16_f32 v201, v56, v57\n"
      "v_mfma_f32_16x16x32_bf16 v[24:27], a[20:23], v[72:75], v[24:27]\n"
      "ds_read_b128 a[20:23], v84 offset:0\n"
      "v_cvt_pk_bf16_f32 v196, v58, v59\n"
      "v_cvt_pk_bf16_f32 v197, v60, v61\n"
      "v_cvt_pk_bf16_f32 v190, v62, v63\n"
      "v_cvt_pk_bf16_f32 v191, v64, v65\n"
      "v_mfma_f32_16x16x32_bf16 v[32:35], a[24:27], v[76:79], 0\n"
      "ds_read_b128 a[24:27], v85 offset:0\n"
      "v_cvt_pk_bf16_f32 v186, v20, v21\n"
      "v_mfma_f32_16x16x32_bf16 v[54:57], a[28:31], v[68:71], v[32:35]\n"
      "v_cvt_pk_bf16_f32 v187, v22, v23\n"
      "v_cvt_pk_bf16_f32 v24, v24, v25\n"
      "v_mfma_f32_16x16x32_bf16 v[32:35], a[28:31], v[72:75], v[32:35]\n"
      "ds_read_b128 a[28:31], v86 offset:3136\n"
      "v_cvt_pk_bf16_f32 v26, v26, v27\n"
      "v_mfma_f32_16x16x32_bf16 v[58:61], a[32:35], v[76:79], 0\n"
      "v_accvgpr_write_b32 a234, v24\n"
      "v_accvgpr_write_b32 a235, v26\n"
      "v_mfma_f32_16x16x32_bf16 v[62:65], a[0:3], v[68:71], v[58:61]\n"
      "v_cvt_pk_bf16_f32 v194, v28, v29\n"
      "v_cvt_pk_bf16_f32 v195, v30, v31\n"
      "v_mfma_f32_16x16x32_bf16 v[58:61], a[0:3], v[72:75], v[58:61]\n"
      "v_cvt_pk_bf16_f32 v202, v32, v33\n"
      "v_cvt_pk_bf16_f32 v203, v34, v35\n"
      "v_mfma_f32_16x16x32_bf16 v[20:23], a[32:35], v[80:83], 0\n"
      "ds_read_b128 a[32:35], v84 offset:64\n"
      "v_cvt_pk_bf16_f32 v198, v54, v55\n"
      "v_mfma_f32_16x16x32_bf16 v[24:27], a[4:7], v[68:71], v[20:23]\n"
      "v_cvt_pk_bf16_f32 v199, v56, v57\n"
      "v_cvt_pk_bf16_f32 v240, v62, v63\n"
      "v_mfma_f32_16x16x32_bf16 v[20:23], a[4:7], v[72:75], v[20:23]\n"
      "s_waitcnt lgkmcnt(1)\n"
      "v_cvt_pk_bf16_f32 v58, v58, v59\n"
      "v_cvt_pk_bf16_f32 v60, v60, v61\n"
      "v_mfma_f32_16x16x32_bf16 v[28:31], a[8:11], v[76:79], 0\n"
      "v_accvgpr_write_b32 a236, v58\n"
      "v_accvgpr_write_b32 a237, v60\n"
      "v_mfma_f32_16x16x32_bf16 v[32:35], a[12:15], v[68:71], v[28:31]\n"
      "v_cvt_pk_bf16_f32 v241, v64, v65\n"
      "v_cvt_pk_bf16_f32 v248, v20, v21\n"
      "v_mfma_f32_16x16x32_bf16 v[28:31], a[12:15], v[72:75], v[28:31]\n"
      "ds_read_b128 a[12:15], v6 offset:0\n"
      "v_cvt_pk_bf16_f32 v249, v22, v23\n"
      "v_mfma_f32_16x16x32_bf16 v[54:57], a[16:19], v[76:79], 0\n"
      "v_cvt_pk_bf16_f32 v244, v24, v25\n"
      "v_cvt_pk_bf16_f32 v245, v26, v27\n"
      "v_mfma_f32_16x16x32_bf16 v[58:61], a[20:23], v[68:71], v[54:57]\n"
      "v_cvt_pk_bf16_f32 v252, v32, v33\n"
      "v_cvt_pk_bf16_f32 v253, v34, v35\n"
      "v_mfma_f32_16x16x32_bf16 v[54:57], a[20:23], v[72:75], v[54:57]\n"
      "s_waitcnt lgkmcnt(0)\n"
      "s_waitcnt vmcnt(4)\n"
      "s_cbranch_vccnz Lconv_h11\n"
      "s_barrier\n"
      "Lconv_h11:\n"
      "ds_read_b128 a[20:23], v6 offset:512\n"
      "v_cvt_pk_bf16_f32 v28, v28, v29\n"
      "v_cvt_pk_bf16_f32 v30, v30, v31\n"
      "v_mfma_f32_16x16x32_bf16 v[62:65], a[16:19], v[80:83], 0\n"
      "ds_read_b128 a[16:19], v6 offset:256\n"
      "v_accvgpr_write_b32 a240, v28\n"
      "v_mfma_f32_16x16x32_bf16 v[20:23], a[24:27], v[68:71], v[62:65]\n"
      "s_cbranch_vccz Lconv_h12\n"
      "s_barrier\n"
      "Lconv_h12:\n"
      "v_accvgpr_write_b32 a241, v30\n"
      "v_mfma_f32_16x16x32_bf16 v[62:65], a[24:27], v[72:75], v[62:65]\n"
      "ds_read_b128 a[24:27], v6 offset:4608\n"
      "v_cvt_pk_bf16_f32 v242, v58, v59\n"
      "v_cvt_pk_bf16_f32 v243, v60, v61\n"
      "v_mfma_f32_16x16x32_bf16 v[24:27], a[28:31], v[76:79], 0\n"
      "ds_read_b128 a[28:31], v6 offset:4864\n"
      "v_cvt_pk_bf16_f32 v54, v54, v55\n"
      "v_mfma_f32_16x16x32_bf16 v[28:31], a[32:35], v[68:71], v[24:27]\n"
      "v_cvt_pk_bf16_f32 v56, v56, v57\n"
      "v_accvgpr_write_b32 a238, v54\n"
      "v_mfma_f32_16x16x32_bf16 v[24:27], a[32:35], v[72:75], v[24:27]\n"
      "ds_read_b128 a[32:35], v6 offset:5120\n"
      "v_accvgpr_write_b32 a239, v56\n"
      "v_cvt_pk_bf16_f32 v246, v20, v21\n"
      "v_cvt_pk_bf16_f32 v247, v22, v23\n"
      "v_cvt_pk_bf16_f32 v250, v62, v63\n"
      "v_cvt_pk_bf16_f32 v251, v64, v65\n"
      "v_cvt_pk_bf16_f32 v254, v28, v29\n"
      "v_cvt_pk_bf16_f32 v255, v30, v31\n"
      "v_cvt_pk_bf16_f32 v24, v24, v25\n"
      "v_cvt_pk_bf16_f32 v26, v26, v27\n"
      "v_accvgpr_write_b32 a242, v24\n"
      "v_accvgpr_write_b32 a243, v26\n"
      "s_mov_b32 m0, s18\n"
      "ds_read_b64_tr_b16 a[36:37], v2 offset:39168\n"
      "v_lshlrev_b32 v9, 7, v5\n"
      "v_mfma_f32_16x16x32_bf16 v[68:71], a[12:15], a[84:87], 0\n"
      "s_waitcnt lgkmcnt(4)\n"
      "ds_read_b64_tr_b16 a[38:39], v2 offset:39176\n"
      "v_min_u32 v9, Lconv_u0-Lconv_t0, v9\n"
      "v_mfma_f32_16x16x32_bf16 v[68:71], a[16:19], a[96:99], v[68:71]\n"
      "s_mul_i32 s97, s41, Lconv_t1-Lconv_t0\n"
      "ds_read_b64_tr_b16 a[40:41], v3 offset:39168\n"
      "v_add_u32 v9, Lconv_t0-Lconv_start, v9\n"
      "v_mfma_f32_16x16x32_bf16 v[68:71], a[20:23], a[108:111], v[68:71]\n"
      "s_waitcnt lgkmcnt(5)\n"
      "ds_read_b64_tr_b16 a[42:43], v3 offset:39176\n"
      "v_add_u32 v9, s97, v9\n"
      "v_mfma_f32_16x16x32_bf16 v[68:71], a[24:27], a[120:123], v[68:71]\n"
      "s_waitcnt lgkmcnt(5)\n"
      "ds_read_b64_tr_b16 a[44:45], v2 offset:39232\n"
      "v_mfma_f32_16x16x32_bf16 v[68:71], a[28:31], a[132:135], v[68:71]\n"
      "s_waitcnt lgkmcnt(5)\n"
      "ds_read_b64_tr_b16 a[46:47], v2 offset:39240\n"
      "v_mfma_f32_16x16x32_bf16 v[68:71], a[32:35], a[144:147], v[68:71]\n"
      "s_waitcnt lgkmcnt(2)\n"
      "ds_read_b64_tr_b16 a[48:49], v3 offset:39232\n"
      "v_mfma_f32_4x4x4_16b_bf16 v[52:55], a[72:73], a[36:37], 0\n"
      "ds_read_b64_tr_b16 a[50:51], v3 offset:39240\n"
      "v_mfma_f32_4x4x4_16b_bf16 v[56:59], a[72:73], a[38:39], 0\n"
      "ds_read_b64_tr_b16 a[52:53], v2 offset:39296\n"
      "v_mfma_f32_4x4x4_16b_bf16 v[60:63], a[76:77], a[36:37], 0\n"
      "ds_read_b64_tr_b16 a[54:55], v2 offset:39304\n"
      "v_mfma_f32_4x4x4_16b_bf16 v[64:67], a[76:77], a[38:39], 0\n"
      "ds_read_b64_tr_b16 a[56:57], v3 offset:39296\n"
      "v_mfma_f32_4x4x4_16b_bf16 v[52:55], a[74:75], a[40:41], v[52:55]\n"
      "ds_read_b64_tr_b16 a[58:59], v3 offset:39304\n"
      "v_mfma_f32_4x4x4_16b_bf16 v[56:59], a[74:75], a[42:43], v[56:59]\n"
      "ds_read_b128 a[0:3], v6 offset:768\n"
      "v_mfma_f32_16x16x32_bf16 v[72:75], a[12:15], a[88:91], 0\n"
      "v_cvt_pk_bf16_f32 v60, v60, v64\n"
      "v_cvt_pk_bf16_f32 v61, v61, v65\n"
      "v_mfma_f32_16x16x32_bf16 v[72:75], a[16:19], a[100:103], v[72:75]\n"
      "ds_write_addtid_b32 v60 offset:9984\n"
      "v_cvt_pk_bf16_f32 v62, v62, v66\n"
      "v_mfma_f32_16x16x32_bf16 v[72:75], a[20:23], a[112:115], v[72:75]\n"
      "ds_write_addtid_b32 v61 offset:10752\n"
      "v_cvt_pk_bf16_f32 v63, v63, v67\n"
      "v_mfma_f32_16x16x32_bf16 v[72:75], a[24:27], a[124:127], v[72:75]\n"
      "ds_write_addtid_b32 v62 offset:12288\n"
      "v_cvt_pk_bf16_f32 v52, v52, v56\n"
      "v_mfma_f32_16x16x32_bf16 v[72:75], a[28:31], a[136:139], v[72:75]\n"
      "ds_write_addtid_b32 v63 offset:13056\n"
      "v_cvt_pk_bf16_f32 v54, v54, v58\n"
      "v_mfma_f32_16x16x32_bf16 v[72:75], a[32:35], a[148:151], v[72:75]\n"
      "s_waitcnt lgkmcnt(9)\n"
      "ds_write_addtid_b32 v52 offset:9216\n"
      "v_mfma_f32_4x4x4_16b_bf16 v[36:39], a[72:73], a[44:45], 0\n"
      "ds_read_b128 a[4:7], v6 offset:1024\n"
      "v_mfma_f32_4x4x4_16b_bf16 v[40:43], a[72:73], a[46:47], 0\n"
      "ds_write_addtid_b32 v54 offset:11520\n"
      "v_mfma_f32_4x4x4_16b_bf16 v[44:47], a[76:77], a[44:45], 0\n"
      "ds_read_b128 a[8:11], v6 offset:1280\n"
      "v_mfma_f32_4x4x4_16b_bf16 v[48:51], a[76:77], a[46:47], 0\n"
      "ds_read_b64_tr_b16 a[36:37], v2 offset:0\n"
      "v_mfma_f32_4x4x4_16b_bf16 v[36:39], a[74:75], a[48:49], v[36:39]\n"
      "ds_read_b64_tr_b16 a[38:39], v2 offset:8\n"
      "v_mfma_f32_4x4x4_16b_bf16 v[40:43], a[74:75], a[50:51], v[40:43]\n"
      "ds_read_b64_tr_b16 a[40:41], v3 offset:0\n"
      "v_mfma_f32_16x16x32_bf16 v[76:79], a[12:15], a[92:95], 0\n"
      "ds_read_b128 a[12:15], v6 offset:5376\n"
      "v_cvt_pk_bf16_f32 v44, v44, v48\n"
      "v_mfma_f32_16x16x32_bf16 v[76:79], a[16:19], a[104:107], v[76:79]\n"
      "ds_read_b128 a[16:19], v6 offset:5632\n"
      "v_cvt_pk_bf16_f32 v45, v45, v49\n"
      "v_mfma_f32_16x16x32_bf16 v[76:79], a[20:23], a[116:119], v[76:79]\n"
      "ds_read_b128 a[20:23], v6 offset:5888\n"
      "v_cvt_pk_bf16_f32 v46, v46, v50\n"
      "v_mfma_f32_16x16x32_bf16 v[76:79], a[24:27], a[128:131], v[76:79]\n"
      "ds_write_addtid_b32 v44 offset:10240\n"
      "ds_write_addtid_b32 v45 offset:11008\n"
      "v_mfma_f32_16x16x32_bf16 v[76:79], a[28:31], a[140:143], v[76:79]\n"
      "ds_write_addtid_b32 v46 offset:12544\n"
      "v_cvt_pk_bf16_f32 v47, v47, v51\n"
      "v_mfma_f32_16x16x32_bf16 v[76:79], a[32:35], a[152:155], v[76:79]\n"
      "s_waitcnt lgkmcnt(14)\n"
      "ds_write_addtid_b32 v47 offset:13312\n"
      "v_cvt_pk_bf16_f32 v36, v36, v40\n"
      "v_mfma_f32_4x4x4_16b_bf16 v[52:55], a[72:73], a[52:53], 0\n"
      "ds_write_addtid_b32 v36 offset:9472\n"
      "v_mfma_f32_4x4x4_16b_bf16 v[56:59], a[72:73], a[54:55], 0\n"
      "v_cvt_pk_bf16_f32 v38, v38, v42\n"
      "v_mfma_f32_4x4x4_16b_bf16 v[60:63], a[76:77], a[52:53], 0\n"
      "ds_write_addtid_b32 v38 offset:11776\n"
      "v_mfma_f32_4x4x4_16b_bf16 v[64:67], a[76:77], a[54:55], 0\n"
      "ds_read_b64_tr_b16 a[42:43], v3 offset:8\n"
      "v_mfma_f32_4x4x4_16b_bf16 v[52:55], a[74:75], a[56:57], v[52:55]\n"
      "ds_read_b64_tr_b16 a[44:45], v2 offset:64\n"
      "v_mfma_f32_4x4x4_16b_bf16 v[56:59], a[74:75], a[58:59], v[56:59]\n"
      "ds_read_b64_tr_b16 a[46:47], v2 offset:72\n"
      "v_mfma_f32_16x16x32_bf16 v[80:83], a[0:3], v[92:95], 0\n"
      "s_waitcnt lgkmcnt(9)\n"
      "v_cvt_pk_bf16_f32 v60, v60, v64\n"
      "v_cvt_pk_bf16_f32 v61, v61, v65\n"
      "v_mfma_f32_16x16x32_bf16 v[80:83], a[4:7], v[148:151], v[80:83]\n"
      "ds_write_addtid_b32 v60 offset:10496\n"
      "v_cvt_pk_bf16_f32 v62, v62, v66\n"
      "v_mfma_f32_16x16x32_bf16 v[80:83], a[8:11], v[204:207], v[80:83]\n"
      "ds_write_addtid_b32 v61 offset:11264\n"
      "v_cvt_pk_bf16_f32 v63, v63, v67\n"
      "v_mfma_f32_16x16x32_bf16 v[80:83], a[12:15], v[112:115], v[80:83]\n"
      "ds_write_addtid_b32 v62 offset:12800\n"
      "v_cvt_pk_bf16_f32 v52, v52, v56\n"
      "v_mfma_f32_16x16x32_bf16 v[80:83], a[16:19], a[204:207], v[80:83]\n"
      "ds_write_addtid_b32 v63 offset:13568\n"
      "v_cvt_pk_bf16_f32 v54, v54, v58\n"
      "v_mfma_f32_16x16x32_bf16 v[80:83], a[20:23], v[220:223], v[80:83]\n"
      "ds_write_addtid_b32 v52 offset:9728\n"
      "v_mfma_f32_16x16x32_bf16 v[84:87], a[0:3], v[100:103], 0\n"
      "ds_write_addtid_b32 v54 offset:12032\n"
      "v_mfma_f32_16x16x32_bf16 v[84:87], a[4:7], a[196:199], v[84:87]\n"
      "s_add_u32 m0, s46, 26112\n"
      "ds_read_b64_tr_b16 a[48:49], v3 offset:64\n"
      "v_mfma_f32_16x16x32_bf16 v[84:87], a[8:11], v[208:211], v[84:87]\n"
      "s_add_u32 s51, s51, s12\n"
      "ds_read_b64_tr_b16 a[50:51], v3 offset:72\n"
      "v_mfma_f32_16x16x32_bf16 v[84:87], a[12:15], a[200:203], v[84:87]\n"
      "ds_read_b128 a[60:63], v6 offset:1536\n"
      "v_mfma_f32_16x16x32_bf16 v[84:87], a[16:19], v[172:175], v[84:87]\n"
      "buffer_load_dwordx4 v16, s[32:35], s51 offen lds\n"
      "v_mfma_f32_16x16x32_bf16 v[84:87], a[20:23], v[228:231], v[84:87]\n"
      "s_waitcnt lgkmcnt(11)\n"
      "ds_read_b128 a[64:67], v6 offset:1792\n"
      "v_mfma_f32_4x4x4_16b_bf16 v[36:39], a[72:73], a[36:37], 0\n"
      "ds_read_b128 a[68:71], v6 offset:2048\n"
      "v_mfma_f32_4x4x4_16b_bf16 v[40:43], a[72:73], a[38:39], 0\n"
      "ds_read_b128 a[24:27], v6 offset:6144\n"
      "v_mfma_f32_4x4x4_16b_bf16 v[44:47], a[76:77], a[36:37], 0\n"
      "ds_read_b128 a[28:31], v6 offset:6400\n"
      "v_mfma_f32_4x4x4_16b_bf16 v[48:51], a[76:77], a[38:39], 0\n"
      "ds_read_b128 a[32:35], v6 offset:6656\n"
      "v_mfma_f32_4x4x4_16b_bf16 v[36:39], a[74:75], a[40:41], v[36:39]\n"
      "ds_read_b64_tr_b16 a[52:53], v2 offset:128\n"
      "v_mfma_f32_4x4x4_16b_bf16 v[40:43], a[74:75], a[42:43], v[40:43]\n"
      "ds_read_b64_tr_b16 a[54:55], v2 offset:136\n"
      "v_mfma_f32_16x16x32_bf16 v[24:27], a[0:3], a[192:195], 0\n"
      "buffer_load_dwordx4 v17, s[32:35], s51 offen offset:1024 lds\n"
      "v_cvt_pk_bf16_f32 v44, v44, v48\n"
      "v_mfma_f32_16x16x32_bf16 v[24:27], a[4:7], v[160:163], v[24:27]\n"
      "v_cvt_pk_bf16_f32 v45, v45, v49\n"
      "v_cvt_pk_bf16_f32 v46, v46, v50\n"
      "v_mfma_f32_16x16x32_bf16 v[24:27], a[8:11], v[216:219], v[24:27]\n"
      "v_cvt_pk_bf16_f32 v47, v47, v51\n"
      "v_cvt_pk_bf16_f32 v36, v36, v40\n"
      "v_mfma_f32_16x16x32_bf16 v[24:27], a[12:15], v[124:127], v[24:27]\n"
      "buffer_load_dwordx4 v17, s[32:35], s51 offen offset:2048 lds\n"
      "v_cvt_pk_bf16_f32 v38, v38, v42\n"
      "v_mfma_f32_16x16x32_bf16 v[24:27], a[16:19], v[180:183], v[24:27]\n"
      "s_mov_b32 m0, s18\n"
      "ds_read_b64_tr_b16 a[56:57], v3 offset:128\n"
      "v_mfma_f32_16x16x32_bf16 v[24:27], a[20:23], v[232:235], v[24:27]\n"
      "s_waitcnt lgkmcnt(9)\n"
      "ds_write_addtid_b32 v44 offset:14592\n"
      "ds_write_addtid_b32 v45 offset:15360\n"
      "v_mfma_f32_4x4x4_16b_bf16 v[52:55], a[72:73], a[44:45], 0\n"
      "ds_write_addtid_b32 v46 offset:16896\n"
      "v_mfma_f32_4x4x4_16b_bf16 v[56:59], a[72:73], a[46:47], 0\n"
      "ds_write_addtid_b32 v47 offset:17664\n"
      "v_mfma_f32_4x4x4_16b_bf16 v[60:63], a[76:77], a[44:45], 0\n"
      "ds_write_addtid_b32 v36 offset:13824\n"
      "v_mfma_f32_4x4x4_16b_bf16 v[64:67], a[76:77], a[46:47], 0\n"
      "ds_write_addtid_b32 v38 offset:16128\n"
      "v_mfma_f32_4x4x4_16b_bf16 v[52:55], a[74:75], a[48:49], v[52:55]\n"
      "ds_read_b64_tr_b16 a[58:59], v3 offset:136\n"
      "v_mfma_f32_4x4x4_16b_bf16 v[56:59], a[74:75], a[50:51], v[56:59]\n"
      "s_waitcnt lgkmcnt(8)\n"
      "v_cvt_pk_bf16_f32 v60, v60, v64\n"
      "v_mfma_f32_16x16x32_bf16 v[28:31], a[60:63], v[96:99], 0\n"
      "ds_write_addtid_b32 v60 offset:14848\n"
      "v_cvt_pk_bf16_f32 v61, v61, v65\n"
      "v_mfma_f32_16x16x32_bf16 v[28:31], a[64:67], v[152:155], v[28:31]\n"
      "s_waitcnt lgkmcnt(0)\n"
      "s_cbranch_vccnz Lconv_h13\n"
      "s_barrier\n"
      "Lconv_h13:\n"
      "v_cvt_pk_bf16_f32 v62, v62, v66\n"
      "v_mfma_f32_16x16x32_bf16 v[28:31], a[68:71], a[216:219], v[28:31]\n"
      "ds_write_addtid_b32 v61 offset:15616\n"
      "v_cvt_pk_bf16_f32 v63, v63, v67\n"
      "v_mfma_f32_16x16x32_bf16 v[28:31], a[24:27], v[116:119], v[28:31]\n"
      "ds_write_addtid_b32 v62 offset:17152\n"
      "v_cvt_pk_bf16_f32 v52, v52, v56\n"
      "v_mfma_f32_16x16x32_bf16 v[28:31], a[28:31], v[168:171], v[28:31]\n"
      "ds_write_addtid_b32 v63 offset:17920\n"
      "v_cvt_pk_bf16_f32 v54, v54, v58\n"
      "v_mfma_f32_16x16x32_bf16 v[28:31], a[32:35], v[224:227], v[28:31]\n"
      "s_cbranch_vccz Lconv_h14\n"
      "s_barrier\n"
      "Lconv_h14:\n"
      "v_mfma_f32_16x16x32_bf16 v[20:23], a[60:63], v[104:107], 0\n"
      "s_mov_b32 vcc_lo, 0\n"
      "ds_read_b128 a[0:3], v7 offset:0\n"
      "v_mfma_f32_16x16x32_bf16 v[20:23], a[64:67], v[156:159], v[20:23]\n"
      "s_mov_b32 vcc_hi, 0x2000000\n"
      "ds_write_addtid_b32 v52 offset:14080\n"
      "ds_write_addtid_b32 v54 offset:16384\n"
      "v_mfma_f32_16x16x32_bf16 v[20:23], a[68:71], v[212:215], v[20:23]\n"
      "ds_read_b128 a[4:7], v7 offset:256\n"
      "v_mfma_f32_16x16x32_bf16 v[20:23], a[24:27], v[120:123], v[20:23]\n"
      "ds_read_b128 a[8:11], v7 offset:512\n"
      "v_mfma_f32_16x16x32_bf16 v[20:23], a[28:31], v[176:179], v[20:23]\n"
      "ds_read_b128 a[12:15], v7 offset:768\n"
      "v_mfma_f32_16x16x32_bf16 v[20:23], a[32:35], a[228:231], v[20:23]\n"
      "ds_read_b128 a[16:19], v7 offset:1024\n"
      "v_mfma_f32_4x4x4_16b_bf16 v[36:39], a[72:73], a[52:53], 0\n"
      "ds_read_b128 a[20:23], v7 offset:1280\n"
      "v_mfma_f32_4x4x4_16b_bf16 v[40:43], a[72:73], a[54:55], 0\n"
      "v_mfma_f32_4x4x4_16b_bf16 v[44:47], a[76:77], a[52:53], 0\n"
      "v_mfma_f32_4x4x4_16b_bf16 v[48:51], a[76:77], a[54:55], 0\n"
      "s_nop 0\n"
      "v_mfma_f32_4x4x4_16b_bf16 v[36:39], a[74:75], a[56:57], v[36:39]\n"
      "s_nop 0\n"
      "v_mfma_f32_4x4x4_16b_bf16 v[40:43], a[74:75], a[58:59], v[40:43]\n"
      "v_mfma_f32_16x16x32_bf16 v[32:35], a[60:63], v[108:111], 0\n"
      "v_cvt_pk_bf16_f32 v44, v44, v48\n"
      "v_cvt_pk_bf16_f32 v45, v45, v49\n"
      "v_mfma_f32_16x16x32_bf16 v[32:35], a[64:67], v[164:167], v[32:35]\n"
      "ds_write_addtid_b32 v44 offset:15104\n"
      "v_cvt_pk_bf16_f32 v46, v46, v50\n"
      "v_mfma_f32_16x16x32_bf16 v[32:35], a[68:71], a[220:223], v[32:35]\n"
      "ds_write_addtid_b32 v45 offset:15872\n"
      "v_cvt_pk_bf16_f32 v47, v47, v51\n"
      "v_mfma_f32_16x16x32_bf16 v[32:35], a[24:27], v[128:131], v[32:35]\n"
      "ds_write_addtid_b32 v46 offset:17408\n"
      "v_cvt_pk_bf16_f32 v36, v36, v40\n"
      "v_mfma_f32_16x16x32_bf16 v[32:35], a[28:31], a[224:227], v[32:35]\n"
      "ds_write_addtid_b32 v47 offset:18176\n"
      "v_cvt_pk_bf16_f32 v38, v38, v42\n"
      "v_mfma_f32_16x16x32_bf16 v[32:35], a[32:35], v[236:239], v[32:35]\n"
      "s_waitcnt lgkmcnt(4)\n"
      "ds_write_addtid_b32 v36 offset:14336\n"
      "v_mfma_f32_16x16x32_bf16 v[68:71], a[0:3], a[156:159], v[68:71]\n"
      "ds_write_addtid_b32 v38 offset:16640\n"
      "v_mfma_f32_16x16x32_bf16 v[68:71], a[4:7], a[168:171], v[68:71]\n"
      "s_mov_b32 m0, s45\n"
      "v_mfma_f32_16x16x32_bf16 v[68:71], a[8:11], a[180:183], v[68:71]\n"
      "s_nop 1\n"
      "v_mfma_f32_16x16x32_bf16 v[72:75], a[0:3], a[160:163], v[72:75]\n"
      "s_nop 1\n"
      "v_mfma_f32_16x16x32_bf16 v[72:75], a[4:7], a[172:175], v[72:75]\n"
      "s_nop 0\n"
      "v_mfma_f32_16x16x32_bf16 v[72:75], a[8:11], a[184:187], v[72:75]\n"
      "v_cvt_pk_bf16_f32 v68, v68, v69\n"
      "v_cvt_pk_bf16_f32 v69, v70, v71\n"
      "v_mfma_f32_16x16x32_bf16 v[76:79], a[0:3], a[164:167], v[76:79]\n"
      "ds_read_b128 a[0:3], v7 offset:1536\n"
      "s_nop 0\n"
      "v_mfma_f32_16x16x32_bf16 v[76:79], a[4:7], a[176:179], v[76:79]\n"
      "ds_read_b128 a[4:7], v7 offset:1792\n"
      "v_mfma_f32_16x16x32_bf16 v[76:79], a[8:11], a[188:191], v[76:79]\n"
      "ds_read_b128 a[8:11], v7 offset:2048\n"
      "v_cvt_pk_bf16_f32 v72, v72, v73\n"
      "v_mfma_f32_16x16x32_bf16 v[80:83], a[12:15], a[208:211], v[80:83]\n"
      "ds_write_addtid_b32 v68 offset:57360\n"
      "ds_write_addtid_b32 v69 offset:57616\n"
      "v_mfma_f32_16x16x32_bf16 v[80:83], a[16:19], v[184:187], v[80:83]\n"
      "ds_write_addtid_b32 v72 offset:45064\n"
      "v_cvt_pk_bf16_f32 v73, v74, v75\n"
      "v_mfma_f32_16x16x32_bf16 v[80:83], a[20:23], v[240:243], v[80:83]\n"
      "ds_write_addtid_b32 v73 offset:45320\n"
      "v_cvt_pk_bf16_f32 v76, v76, v77\n"
      "v_mfma_f32_16x16x32_bf16 v[84:87], a[12:15], v[136:139], v[84:87]\n"
      "ds_write_addtid_b32 v76 offset:32768\n"
      "v_cvt_pk_bf16_f32 v77, v78, v79\n"
      "v_mfma_f32_16x16x32_bf16 v[84:87], a[16:19], v[192:195], v[84:87]\n"
      "ds_write_addtid_b32 v77 offset:33024\n"
      "v_mfma_f32_16x16x32_bf16 v[84:87], a[20:23], v[244:247], v[84:87]\n"
      "v_cvt_pk_bf16_f32 v80, v80, v81\n"
      "v_cvt_pk_bf16_f32 v81, v82, v83\n"
      "v_mfma_f32_16x16x32_bf16 v[24:27], a[12:15], a[212:215], v[24:27]\n"
      "ds_write_addtid_b32 v80 offset:58384\n"
      "s_nop 0\n"
      "v_mfma_f32_16x16x32_bf16 v[24:27], a[16:19], v[196:199], v[24:27]\n"
      "ds_write_addtid_b32 v81 offset:58640\n"
      "v_mfma_f32_16x16x32_bf16 v[24:27], a[20:23], v[252:255], v[24:27]\n"
      "s_waitcnt lgkmcnt(8)\n"
      "v_cvt_pk_bf16_f32 v84, v84, v85\n"
      "v_cvt_pk_bf16_f32 v85, v86, v87\n"
      "v_mfma_f32_16x16x32_bf16 v[28:31], a[0:3], v[132:135], v[28:31]\n"
      "ds_write_addtid_b32 v84 offset:46088\n"
      "s_nop 0\n"
      "v_mfma_f32_16x16x32_bf16 v[28:31], a[4:7], v[188:191], v[28:31]\n"
      "ds_write_addtid_b32 v85 offset:46344\n"
      "v_cvt_pk_bf16_f32 v24, v24, v25\n"
      "v_mfma_f32_16x16x32_bf16 v[28:31], a[8:11], a[236:239], v[28:31]\n"
      "ds_write_addtid_b32 v24 offset:33792\n"
      "v_cvt_pk_bf16_f32 v25, v26, v27\n"
      "v_mfma_f32_16x16x32_bf16 v[20:23], a[0:3], v[140:143], v[20:23]\n"
      "ds_write_addtid_b32 v25 offset:34048\n"
      "s_nop 0\n"
      "v_mfma_f32_16x16x32_bf16 v[20:23], a[4:7], a[232:235], v[20:23]\n"
      "s_nop 0\n"
      "v_mfma_f32_16x16x32_bf16 v[20:23], a[8:11], v[248:251], v[20:23]\n"
      "v_cvt_pk_bf16_f32 v28, v28, v29\n"
      "v_cvt_pk_bf16_f32 v29, v30, v31\n"
      "v_mfma_f32_16x16x32_bf16 v[32:35], a[0:3], v[144:147], v[32:35]\n"
      "ds_write_addtid_b32 v28 offset:59408\n"
      "s_nop 0\n"
      "v_mfma_f32_16x16x32_bf16 v[32:35], a[4:7], v[200:203], v[32:35]\n"
      "ds_write_addtid_b32 v29 offset:59664\n"
      "v_mfma_f32_16x16x32_bf16 v[32:35], a[8:11], a[240:243], v[32:35]\n"
      "v_cvt_pk_bf16_f32 v20, v20, v21\n"
      "v_cvt_pk_bf16_f32 v21, v22, v23\n"
      "ds_write_addtid_b32 v20 offset:47112\n"
      "ds_write_addtid_b32 v21 offset:47368\n"
      "s_add_u32 m0, s46, 39168\n"
      "s_add_u32 s51, s51, s12\n"
      "buffer_load_dwordx4 v16, s[32:35], s51 offen lds\n"
      "buffer_load_dwordx4 v17, s[32:35], s51 offen offset:1024 lds\n"
      "buffer_load_dwordx4 v17, s[32:35], s51 offen offset:2048 lds\n"
      "s_mov_b32 m0, s45\n"
      "s_waitcnt vmcnt(6)\n"
      "s_waitcnt lgkmcnt(0)\n"
      "s_cmp_eq_u32 s41, 1\n"
      "s_cbranch_scc1 Lconv_s1\n"
      "Lconv_s0:\n"
      "s_branch Lconv_loop0\n"
      "Lconv_s1:\n"
      "s_branch Lconv_loop1\n"
      ".p2align 12\n"
      "Lconv_pa0:\n"
      "s_cbranch_execz Lconv_r0\n"
      "s_nop 0\n"
      "Lconv_loop0:\n"
      "v_mfma_f32_16x16x32_bf16 v[20:23], a[24:27], v[96:99], 0\n"
      "s_add_u32 s51, s51, s12\n"
      "s_nop 1\n"
      "v_mfma_f32_16x16x32_bf16 v[20:23], a[28:31], v[152:155], v[20:23]\n"
      "s_sub_u32 s58, s93, s58\n"
      "v_cvt_pk_bf16_f32 v32, v32, v33\n"
      "v_cvt_pk_bf16_f32 v33, v34, v35\n"
      "v_mfma_f32_16x16x32_bf16 v[20:23], a[32:35], a[216:219], v[20:23]\n"
      "s_sub_u32 s59, s94, s59\n"
      "ds_write_addtid_b32 v32 offset:34816\n"
      "v_mfma_f32_16x16x32_bf16 v[20:23], a[0:3], v[116:119], v[20:23]\n"
      "s_sub_u32 s47, s51, s48\n"
      "ds_write_addtid_b32 v33 offset:35072\n"
      "v_mfma_f32_16x16x32_bf16 v[20:23], a[4:7], v[168:171], v[20:23]\n"
      "s_cmp_ge_u32 vcc_hi, 0x80000000\n"
      "v_mfma_f32_16x16x32_bf16 v[20:23], a[8:11], v[224:227], v[20:23]\n"
      "s_cselect_b32 s53, s47, s51\n"
      "v_mfma_f32_16x16x32_bf16 v[24:27], a[24:27], v[104:107], 0\n"
      "s_waitcnt vmcnt(8) lgkmcnt(0)\n"
      "s_barrier\n"
      "v_mfma_f32_16x16x32_bf16 v[24:27], a[28:31], v[156:159], v[24:27]\n"
      "s_mov_b32 m0, s58\n"
      "ds_read_b128 a[12:15], v7 offset:6144\n"
      "v_mfma_f32_16x16x32_bf16 v[24:27], a[32:35], v[212:215], v[24:27]\n"
      "ds_read_b128 a[16:19], v7 offset:6400\n"
      "v_mfma_f32_16x16x32_bf16 v[24:27], a[0:3], v[120:123], v[24:27]\n"
      "ds_read_b128 a[20:23], v7 offset:6656\n"
      "v_mfma_f32_16x16x32_bf16 v[24:27], a[4:7], v[176:179], v[24:27]\n"
      "ds_read_b64_tr_b16 a[36:37], v2 offset:13056\n"
      "v_mfma_f32_16x16x32_bf16 v[24:27], a[8:11], a[228:231], v[24:27]\n"
      "ds_read_b64_tr_b16 a[38:39], v2 offset:13064\n"
      "v_mfma_f32_16x16x32_bf16 v[68:71], a[78:81], a[60:63], 0\n"
      "ds_read_b64_tr_b16 a[40:41], v3 offset:13056\n"
      "s_nop 0\n"
      "v_mfma_f32_16x16x32_bf16 v[28:31], a[24:27], v[108:111], 0\n"
      "ds_read_b64_tr_b16 a[42:43], v3 offset:13064\n"
      "s_nop 0\n"
      "v_mfma_f32_16x16x32_bf16 v[28:31], a[28:31], v[164:167], v[28:31]\n"
      "buffer_load_dwordx4 v16, s[32:35], s53 offen lds\n"
      "v_mfma_f32_16x16x32_bf16 v[28:31], a[32:35], a[220:223], v[28:31]\n"
      "s_mov_b32 m0, s45\n"
      "ds_read2_b64 a[64:67], v10 offset0:0 offset1:64\n"
      "v_cvt_pk_bf16_f32 v85, v68, v69\n"
      "v_mfma_f32_16x16x32_bf16 v[28:31], a[0:3], v[128:131], v[28:31]\n"
      "ds_read2_b64 a[68:71], v10 offset0:1 offset1:65\n"
      "v_cvt_pk_bf16_f32 v91, v70, v71\n"
      "v_mfma_f32_16x16x32_bf16 v[28:31], a[4:7], a[224:227], v[28:31]\n"
      "ds_read_b128 a[24:27], v6 offset:5376\n"
      "v_mfma_f32_16x16x32_bf16 v[28:31], a[8:11], v[236:239], v[28:31]\n"
      "s_waitcnt lgkmcnt(6)\n"
      "ds_read_b128 a[28:31], v6 offset:5632\n"
      "v_mfma_f32_16x16x32_bf16 v[20:23], a[12:15], v[132:135], v[20:23]\n"
      "ds_read_b128 a[32:35], v6 offset:5888\n"
      "v_mfma_f32_16x16x32_bf16 v[20:23], a[16:19], v[188:191], v[20:23]\n"
      "buffer_store_dwordx2 v[84:85], v15, s[36:39], s52 offen sc1\n"
      "v_mfma_f32_16x16x32_bf16 v[20:23], a[20:23], a[236:239], v[20:23]\n"
      "s_waitcnt lgkmcnt(5)\n"
      "ds_read_b128 a[0:3], v7 offset:768\n"
      "s_nop 0\n"
      "v_mfma_f32_4x4x4_16b_bf16 v[52:55], a[72:73], a[36:37], 0\n"
      "ds_read_b128 a[4:7], v7 offset:1024\n"
      "v_mfma_f32_4x4x4_16b_bf16 v[56:59], a[72:73], a[38:39], 0\n"
      "ds_read_b128 a[8:11], v7 offset:1280\n"
      "v_mfma_f32_4x4x4_16b_bf16 v[60:63], a[74:75], a[40:41], v[52:55]\n"
      "v_cvt_pk_bf16_f32 v20, v20, v21\n"
      "v_mfma_f32_4x4x4_16b_bf16 v[64:67], a[74:75], a[42:43], v[56:59]\n"
      "ds_write_addtid_b32 v20 offset:62480\n"
      "v_mfma_f32_16x16x32_bf16 v[24:27], a[12:15], v[140:143], v[24:27]\n"
      "buffer_store_dwordx2 v[90:91], v15, s[36:39], s52 offen offset:1536 sc1\n"
      "v_cvt_pk_bf16_f32 v21, v22, v23\n"
      "v_mfma_f32_16x16x32_bf16 v[24:27], a[16:19], a[232:235], v[24:27]\n"
      "s_mov_b32 s52, s13\n"
      "ds_read_b64_tr_b16 a[44:45], v2 offset:13120\n"
      "v_cvt_pk_bf16_f32 v60, v60, v64\n"
      "v_mfma_f32_16x16x32_bf16 v[24:27], a[20:23], v[248:251], v[24:27]\n"
      "s_waitcnt lgkmcnt(5)\n"
      "ds_write_addtid_b32 v21 offset:62736\n"
      "v_cvt_pk_bf16_f32 v62, v62, v66\n"
      "v_mfma_f32_16x16x32_bf16 v[72:75], a[78:81], a[64:67], 0\n"
      "s_add_u32 s13, s13, s12\n"
      "ds_read_b64_tr_b16 a[46:47], v2 offset:13128\n"
      "v_add_f32_e32 v61, v52, v53\n"
      "v_mfma_f32_16x16x32_bf16 v[28:31], a[12:15], v[144:147], v[28:31]\n"
      "s_sub_u32 s96, s53, s95\n"
      "ds_read_b128 a[12:15], v7 offset:5376\n"
      "v_cvt_pk_bf16_f32 v24, v24, v25\n"
      "v_mfma_f32_16x16x32_bf16 v[28:31], a[16:19], v[200:203], v[28:31]\n"
      "ds_read_b128 a[16:19], v7 offset:5632\n"
      "v_cvt_pk_bf16_f32 v25, v26, v27\n"
      "v_mfma_f32_16x16x32_bf16 v[28:31], a[20:23], a[240:243], v[28:31]\n"
      "ds_read_b128 a[20:23], v7 offset:5888\n"
      "v_sub_f32_e32 v53, v52, v53\n"
      "v_mfma_f32_16x16x32_bf16 v[76:79], a[78:81], a[68:71], 0\n"
      "ds_read_b64_tr_b16 a[48:49], v3 offset:13120\n"
      "v_add_f32_e32 v65, v56, v57\n"
      "v_mfma_f32_16x16x32_bf16 v[32:35], a[24:27], v[92:95], 0\n"
      "ds_read_b64_tr_b16 a[50:51], v3 offset:13128\n"
      "v_sub_f32_e32 v57, v56, v57\n"
      "v_mfma_f32_16x16x32_bf16 v[32:35], a[28:31], v[148:151], v[32:35]\n"
      "ds_write_addtid_b32 v24 offset:50184\n"
      "ds_write_addtid_b32 v25 offset:50440\n"
      "v_mfma_f32_16x16x32_bf16 v[32:35], a[32:35], v[204:207], v[32:35]\n"
      "s_waitcnt lgkmcnt(9)\n"
      "ds_read2_b64 a[60:63], v10 offset0:2 offset1:66\n"
      "v_add_f32_e32 v63, v54, v55\n"
      "v_mfma_f32_16x16x32_bf16 v[32:35], a[0:3], v[112:115], v[32:35]\n"
      "s_mov_b32 m0, s18\n"
      "v_sub_f32_e32 v55, v54, v55\n"
      "v_add_f32_e32 v67, v58, v59\n"
      "v_mfma_f32_16x16x32_bf16 v[32:35], a[4:7], a[204:207], v[32:35]\n"
      "ds_write_addtid_b32 v60 offset:0\n"
      "v_sub_f32_e32 v59, v58, v59\n"
      "v_mfma_f32_16x16x32_bf16 v[32:35], a[8:11], v[220:223], v[32:35]\n"
      "s_waitcnt lgkmcnt(7)\n"
      "ds_write_addtid_b32 v62 offset:2304\n"
      "v_cvt_pk_bf16_f32 v61, v61, v65\n"
      "v_mfma_f32_16x16x32_bf16 v[32:35], a[12:15], a[208:211], v[32:35]\n"
      "v_cvt_pk_bf16_f32 v53, v53, v57\n"
      "v_cvt_pk_bf16_f32 v63, v63, v67\n"
      "v_mfma_f32_16x16x32_bf16 v[32:35], a[16:19], v[184:187], v[32:35]\n"
      "s_waitcnt lgkmcnt(6)\n"
      "ds_write_addtid_b32 v61 offset:768\n"
      "v_cvt_pk_bf16_f32 v55, v55, v59\n"
      "v_mfma_f32_16x16x32_bf16 v[32:35], a[20:23], v[240:243], v[32:35]\n"
      "ds_write_addtid_b32 v53 offset:1536\n"
      "ds_write_addtid_b32 v63 offset:3072\n"
      "v_mfma_f32_16x16x32_bf16 v[20:23], a[24:27], v[100:103], 0\n"
      "ds_write_addtid_b32 v55 offset:3840\n"
      "v_cvt_pk_bf16_f32 v28, v28, v29\n"
      "v_mfma_f32_16x16x32_bf16 v[20:23], a[28:31], a[196:199], v[20:23]\n"
      "s_mov_b32 m0, s45\n"
      "ds_read2_b64 a[64:67], v10 offset0:3 offset1:67\n"
      "v_cvt_pk_bf16_f32 v29, v30, v31\n"
      "v_mfma_f32_16x16x32_bf16 v[20:23], a[32:35], v[208:211], v[20:23]\n"
      "ds_write_addtid_b32 v28 offset:37888\n"
      "ds_write_addtid_b32 v29 offset:38144\n"
      "v_mfma_f32_16x16x32_bf16 v[20:23], a[0:3], a[200:203], v[20:23]\n"
      "s_mov_b32 m0, s58\n"
      "ds_read_b64_tr_b16 a[52:53], v2 offset:13184\n"
      "v_cvt_pk_bf16_f32 v32, v32, v33\n"
      "v_mfma_f32_16x16x32_bf16 v[20:23], a[4:7], v[172:175], v[20:23]\n"
      "buffer_load_dwordx4 v17, s[32:35], s53 offen offset:1024 lds\n"
      "v_cvt_pk_bf16_f32 v33, v34, v35\n"
      "v_mfma_f32_16x16x32_bf16 v[20:23], a[8:11], v[228:231], v[20:23]\n"
      "s_mov_b32 m0, s45\n"
      "ds_read_b64_tr_b16 a[54:55], v2 offset:13192\n"
      "v_cvt_pk_bf16_f32 v80, v72, v73\n"
      "v_mfma_f32_16x16x32_bf16 v[20:23], a[12:15], v[136:139], v[20:23]\n"
      "ds_write_addtid_b32 v32 offset:61456\n"
      "v_cvt_pk_bf16_f32 v81, v76, v77\n"
      "v_mfma_f32_16x16x32_bf16 v[20:23], a[16:19], v[192:195], v[20:23]\n"
      "ds_write_addtid_b32 v33 offset:61712\n"
      "v_cvt_pk_bf16_f32 v86, v74, v75\n"
      "v_mfma_f32_16x16x32_bf16 v[20:23], a[20:23], v[244:247], v[20:23]\n"
      "s_waitcnt lgkmcnt(14)\n"
      "ds_read_b64_tr_b16 a[56:57], v3 offset:13184\n"
      "v_cvt_pk_bf16_f32 v87, v78, v79\n"
      "v_mfma_f32_4x4x4_16b_bf16 v[36:39], a[72:73], a[44:45], 0\n"
      "ds_read_b64_tr_b16 a[58:59], v3 offset:13192\n"
      "v_mfma_f32_4x4x4_16b_bf16 v[40:43], a[72:73], a[46:47], 0\n"
      "ds_read2_b64 a[68:71], v12 offset0:0 offset1:64\n"
      "v_mfma_f32_4x4x4_16b_bf16 v[44:47], a[74:75], a[48:49], v[36:39]\n"
      "v_cvt_pk_bf16_f32 v20, v20, v21\n"
      "v_mfma_f32_4x4x4_16b_bf16 v[48:51], a[74:75], a[50:51], v[40:43]\n"
      "s_waitcnt lgkmcnt(6)\n"
      "v_cvt_pk_bf16_f32 v21, v22, v23\n"
      "v_mfma_f32_16x16x32_bf16 v[68:71], a[78:81], a[60:63], 0\n"
      "ds_write_addtid_b32 v20 offset:49160\n"
      "v_add_f32_e32 v45, v36, v37\n"
      "v_mfma_f32_16x16x32_bf16 v[24:27], a[24:27], a[192:195], 0\n"
      "ds_read_b128 a[24:27], v7 offset:4608\n"
      "v_cvt_pk_bf16_f32 v44, v44, v48\n"
      "v_mfma_f32_16x16x32_bf16 v[24:27], a[28:31], v[160:163], v[24:27]\n"
      "ds_read_b128 a[28:31], v7 offset:4864\n"
      "v_cvt_pk_bf16_f32 v46, v46, v50\n"
      "v_mfma_f32_16x16x32_bf16 v[24:27], a[32:35], v[216:219], v[24:27]\n"
      "ds_read_b128 a[32:35], v7 offset:5120\n"
      "v_cvt_pk_bf16_f32 v82, v68, v69\n"
      "v_mfma_f32_16x16x32_bf16 v[24:27], a[0:3], v[124:127], v[24:27]\n"
      "ds_read_b128 a[0:3], v6 offset:4608\n"
      "v_add_f32_e32 v49, v40, v41\n"
      "v_mfma_f32_16x16x32_bf16 v[24:27], a[4:7], v[180:183], v[24:27]\n"
      "ds_read_b128 a[4:7], v6 offset:4864\n"
      "v_cvt_pk_bf16_f32 v45, v45, v49\n"
      "v_mfma_f32_16x16x32_bf16 v[24:27], a[8:11], v[232:235], v[24:27]\n"
      "ds_read_b128 a[8:11], v6 offset:5120\n"
      "v_sub_f32_e32 v37, v36, v37\n"
      "v_mfma_f32_16x16x32_bf16 v[24:27], a[12:15], a[212:215], v[24:27]\n"
      "ds_read_b128 a[12:15], v7 offset:0\n"
      "v_sub_f32_e32 v41, v40, v41\n"
      "v_mfma_f32_16x16x32_bf16 v[24:27], a[16:19], v[196:199], v[24:27]\n"
      "ds_read_b128 a[16:19], v7 offset:256\n"
      "v_cvt_pk_bf16_f32 v37, v37, v41\n"
      "v_mfma_f32_16x16x32_bf16 v[24:27], a[20:23], v[252:255], v[24:27]\n"
      "ds_read_b128 a[20:23], v7 offset:512\n"
      "v_add_f32_e32 v47, v38, v39\n"
      "v_mfma_f32_16x16x32_bf16 v[72:75], a[78:81], a[64:67], 0\n"
      "s_waitcnt lgkmcnt(7)\n"
      "ds_write_addtid_b32 v21 offset:49416\n"
      "v_add_f32_e32 v51, v42, v43\n"
      "v_mfma_f32_16x16x32_bf16 v[28:31], a[24:27], a[156:159], 0\n"
      "s_mov_b32 m0, s58\n"
      "v_cvt_pk_bf16_f32 v24, v24, v25\n"
      "v_cvt_pk_bf16_f32 v25, v26, v27\n"
      "v_mfma_f32_16x16x32_bf16 v[28:31], a[28:31], a[168:171], v[28:31]\n"
      "s_waitcnt lgkmcnt(6)\n"
      "v_cvt_pk_bf16_f32 v83, v72, v73\n"
      "v_cvt_pk_bf16_f32 v47, v47, v51\n"
      "v_mfma_f32_16x16x32_bf16 v[28:31], a[32:35], a[180:183], v[28:31]\n"
      "buffer_load_dwordx4 v17, s[32:35], s53 offen offset:2048 lds\n"
      "v_sub_f32_e32 v39, v38, v39\n"
      "v_mfma_f32_16x16x32_bf16 v[28:31], a[0:3], a[84:87], v[28:31]\n"
      "s_waitcnt lgkmcnt(4)\n"
      "v_sub_f32_e32 v43, v42, v43\n"
      "v_cvt_pk_bf16_f32 v39, v39, v43\n"
      "v_mfma_f32_16x16x32_bf16 v[28:31], a[4:7], a[96:99], v[28:31]\n"
      "s_mov_b32 m0, s45\n"
      "v_cvt_pk_bf16_f32 v88, v70, v71\n"
      "v_cvt_pk_bf16_f32 v89, v74, v75\n"
      "v_mfma_f32_16x16x32_bf16 v[28:31], a[8:11], a[108:111], v[28:31]\n"
      "s_waitcnt lgkmcnt(2)\n"
      "ds_write_addtid_b32 v24 offset:36864\n"
      "ds_write_addtid_b32 v25 offset:37120\n"
      "v_mfma_f32_16x16x32_bf16 v[28:31], a[12:15], a[120:123], v[28:31]\n"
      "s_mov_b32 m0, s18\n"
      "ds_read2_b64 a[60:63], v12 offset0:1 offset1:65\n"
      "v_swap_b32 v6, v7\n"
      "v_mfma_f32_16x16x32_bf16 v[28:31], a[16:19], a[132:135], v[28:31]\n"
      "s_waitcnt lgkmcnt(4)\n"
      "ds_write_addtid_b32 v44 offset:256\n"
      "ds_write_addtid_b32 v46 offset:2560\n"
      "v_mfma_f32_16x16x32_bf16 v[28:31], a[20:23], a[144:147], v[28:31]\n"
      "buffer_store_dwordx4 v[80:83], v14, s[36:39], s52 offen sc1\n"
      "v_add_u32_e32 v2, s91, v2\n"
      "v_mfma_f32_4x4x4_16b_bf16 v[52:55], a[72:73], a[52:53], 0\n"
      "ds_write_addtid_b32 v45 offset:1024\n"
      "v_mfma_f32_4x4x4_16b_bf16 v[56:59], a[72:73], a[54:55], 0\n"
      "ds_write_addtid_b32 v37 offset:1792\n"
      "v_mfma_f32_4x4x4_16b_bf16 v[60:63], a[74:75], a[56:57], v[52:55]\n"
      "ds_write_addtid_b32 v47 offset:3328\n"
      "v_mfma_f32_4x4x4_16b_bf16 v[64:67], a[74:75], a[58:59], v[56:59]\n"
      "ds_write_addtid_b32 v39 offset:4096\n"
      "v_mfma_f32_16x16x32_bf16 v[20:23], a[24:27], a[160:163], 0\n"
      "s_mov_b32 m0, s45\n"
      "v_cvt_pk_bf16_f32 v28, v28, v29\n"
      "v_cvt_pk_bf16_f32 v29, v30, v31\n"
      "v_mfma_f32_16x16x32_bf16 v[20:23], a[28:31], a[172:175], v[20:23]\n"
      "buffer_store_dwordx4 v[86:89], v14, s[36:39], s52 offen offset:1536 sc1\n"
      "v_add_f32_e32 v61, v52, v53\n"
      "v_mfma_f32_16x16x32_bf16 v[20:23], a[32:35], a[184:187], v[20:23]\n"
      "ds_write_addtid_b32 v28 offset:60432\n"
      "v_sub_f32_e32 v53, v52, v53\n"
      "v_mfma_f32_16x16x32_bf16 v[20:23], a[0:3], a[88:91], v[20:23]\n"
      "ds_write_addtid_b32 v29 offset:60688\n"
      "v_add_f32_e32 v65, v56, v57\n"
      "v_mfma_f32_16x16x32_bf16 v[20:23], a[4:7], a[100:103], v[20:23]\n"
      "s_mov_b32 m0, s18\n"
      "v_sub_f32_e32 v57, v56, v57\n"
      "v_add_f32_e32 v63, v54, v55\n"
      "v_mfma_f32_16x16x32_bf16 v[20:23], a[8:11], a[112:115], v[20:23]\n"
      "v_sub_f32_e32 v55, v54, v55\n"
      "v_add_f32_e32 v67, v58, v59\n"
      "v_mfma_f32_16x16x32_bf16 v[20:23], a[12:15], a[124:127], v[20:23]\n"
      "v_sub_f32_e32 v59, v58, v59\n"
      "v_cvt_pk_bf16_f32 v60, v60, v64\n"
      "v_mfma_f32_16x16x32_bf16 v[20:23], a[16:19], a[136:139], v[20:23]\n"
      "v_cvt_pk_bf16_f32 v62, v62, v66\n"
      "v_cvt_pk_bf16_f32 v61, v61, v65\n"
      "v_mfma_f32_16x16x32_bf16 v[20:23], a[20:23], a[148:151], v[20:23]\n"
      "v_cvt_pk_bf16_f32 v53, v53, v57\n"
      "v_cvt_pk_bf16_f32 v63, v63, v67\n"
      "v_mfma_f32_16x16x32_bf16 v[76:79], a[78:81], a[68:71], 0\n"
      "ds_write_addtid_b32 v60 offset:512\n"
      "v_cvt_pk_bf16_f32 v55, v55, v59\n"
      "v_mfma_f32_16x16x32_bf16 v[32:35], a[24:27], a[164:167], 0\n"
      "ds_write_addtid_b32 v62 offset:2816\n"
      "ds_write_addtid_b32 v61 offset:1280\n"
      "v_mfma_f32_16x16x32_bf16 v[32:35], a[28:31], a[176:179], v[32:35]\n"
      "ds_write_addtid_b32 v53 offset:2048\n"
      "ds_write_addtid_b32 v63 offset:3584\n"
      "v_mfma_f32_16x16x32_bf16 v[32:35], a[32:35], a[188:191], v[32:35]\n"
      "ds_write_addtid_b32 v55 offset:4352\n"
      "v_cvt_pk_bf16_f32 v20, v20, v21\n"
      "v_mfma_f32_16x16x32_bf16 v[32:35], a[0:3], a[92:95], v[32:35]\n"
      "s_mov_b32 m0, s45\n"
      "v_cvt_pk_bf16_f32 v21, v22, v23\n"
      "v_add_u32_e64 v3, s91, v3\n"
      "v_mfma_f32_16x16x32_bf16 v[32:35], a[4:7], a[104:107], v[32:35]\n"
      "s_mov_b32 m0, s59\n"
      "v_cvt_pk_bf16_f32 v84, v76, v77\n"
      "v_cvt_pk_bf16_f32 v90, v78, v79\n"
      "v_mfma_f32_16x16x32_bf16 v[32:35], a[8:11], a[116:119], v[32:35]\n"
      "buffer_load_dwordx4 v19, s[32:35], s96 offen lds\n"
      "v_mfma_f32_16x16x32_bf16 v[32:35], a[12:15], a[128:131], v[32:35]\n"
      "s_mov_b32 m0, s45\n"
      "v_mfma_f32_16x16x32_bf16 v[32:35], a[16:19], a[140:143], v[32:35]\n"
      "s_sub_u32 s91, 0, s91\n"
      "ds_write_addtid_b32 v20 offset:48136\n"
      "ds_write_addtid_b32 v21 offset:48392\n"
      "v_mfma_f32_16x16x32_bf16 v[32:35], a[20:23], a[152:155], v[32:35]\n"
      "s_nop 1\n"
      "v_mfma_f32_16x16x32_bf16 v[20:23], a[12:15], a[84:87], 0\n"
      "s_add_u32 s51, s51, s12\n"
      "s_nop 1\n"
      "v_mfma_f32_16x16x32_bf16 v[20:23], a[16:19], a[96:99], v[20:23]\n"
      "s_sub_u32 s47, s51, s48\n"
      "v_cvt_pk_bf16_f32 v32, v32, v33\n"
      "v_cvt_pk_bf16_f32 v33, v34, v35\n"
      "v_mfma_f32_16x16x32_bf16 v[20:23], a[20:23], a[108:111], v[20:23]\n"
      "s_cmp_ge_u32 vcc_hi, 0x40000000\n"
      "ds_write_addtid_b32 v32 offset:35840\n"
      "v_mfma_f32_16x16x32_bf16 v[20:23], a[24:27], a[120:123], v[20:23]\n"
      "s_cselect_b32 s53, s47, s51\n"
      "ds_write_addtid_b32 v33 offset:36096\n"
      "v_mfma_f32_16x16x32_bf16 v[20:23], a[28:31], a[132:135], v[20:23]\n"
      "s_lshl_b64 vcc, vcc, 1\n"
      "v_mfma_f32_16x16x32_bf16 v[20:23], a[32:35], a[144:147], v[20:23]\n"
      "v_mfma_f32_16x16x32_bf16 v[24:27], a[12:15], a[88:91], 0\n"
      "s_waitcnt vmcnt(11) lgkmcnt(0)\n"
      "s_barrier\n"
      "v_mfma_f32_16x16x32_bf16 v[24:27], a[16:19], a[100:103], v[24:27]\n"
      "s_add_u32 m0, s58, 13056\n"
      "ds_read_b128 a[0:3], v7 offset:0\n"
      "v_mfma_f32_16x16x32_bf16 v[24:27], a[20:23], a[112:115], v[24:27]\n"
      "ds_read_b128 a[4:7], v7 offset:256\n"
      "v_mfma_f32_16x16x32_bf16 v[24:27], a[24:27], a[124:127], v[24:27]\n"
      "ds_read_b128 a[8:11], v7 offset:512\n"
      "v_mfma_f32_16x16x32_bf16 v[24:27], a[28:31], a[136:139], v[24:27]\n"
      "ds_read_b64_tr_b16 a[36:37], v2 offset:0\n"
      "v_mfma_f32_16x16x32_bf16 v[24:27], a[32:35], a[148:151], v[24:27]\n"
      "ds_read_b64_tr_b16 a[38:39], v2 offset:8\n"
      "v_mfma_f32_16x16x32_bf16 v[68:71], a[78:81], a[60:63], 0\n"
      "ds_read_b64_tr_b16 a[40:41], v3 offset:0\n"
      "s_nop 0\n"
      "v_mfma_f32_16x16x32_bf16 v[28:31], a[12:15], a[92:95], 0\n"
      "ds_read_b64_tr_b16 a[42:43], v3 offset:8\n"
      "s_nop 0\n"
      "v_mfma_f32_16x16x32_bf16 v[28:31], a[16:19], a[104:107], v[28:31]\n"
      "buffer_load_dwordx4 v16, s[32:35], s53 offen lds\n"
      "v_mfma_f32_16x16x32_bf16 v[28:31], a[20:23], a[116:119], v[28:31]\n"
      "s_mov_b32 m0, s45\n"
      "ds_read2_b64 a[64:67], v11 offset0:0 offset1:64\n"
      "v_cvt_pk_bf16_f32 v85, v68, v69\n"
      "v_mfma_f32_16x16x32_bf16 v[28:31], a[24:27], a[128:131], v[28:31]\n"
      "ds_read2_b64 a[68:71], v11 offset0:1 offset1:65\n"
      "v_cvt_pk_bf16_f32 v91, v70, v71\n"
      "v_mfma_f32_16x16x32_bf16 v[28:31], a[28:31], a[140:143], v[28:31]\n"
      "ds_read_b128 a[12:15], v6 offset:768\n"
      "v_mfma_f32_16x16x32_bf16 v[28:31], a[32:35], a[152:155], v[28:31]\n"
      "s_waitcnt lgkmcnt(6)\n"
      "ds_read_b128 a[16:19], v6 offset:1024\n"
      "v_mfma_f32_16x16x32_bf16 v[20:23], a[0:3], a[156:159], v[20:23]\n"
      "ds_read_b128 a[20:23], v6 offset:1280\n"
      "v_mfma_f32_16x16x32_bf16 v[20:23], a[4:7], a[168:171], v[20:23]\n"
      "buffer_store_dwordx2 v[84:85], v15, s[36:39], s52 offen sc1\n"
      "v_mfma_f32_16x16x32_bf16 v[20:23], a[8:11], a[180:183], v[20:23]\n"
      "s_waitcnt lgkmcnt(5)\n"
      "ds_read_b128 a[24:27], v6 offset:5376\n"
      "s_nop 0\n"
      "v_mfma_f32_4x4x4_16b_bf16 v[36:39], a[72:73], a[36:37], 0\n"
      "ds_read_b128 a[28:31], v6 offset:5632\n"
      "v_mfma_f32_4x4x4_16b_bf16 v[40:43], a[72:73], a[38:39], 0\n"
      "ds_read_b128 a[32:35], v6 offset:5888\n"
      "v_mfma_f32_4x4x4_16b_bf16 v[44:47], a[74:75], a[40:41], v[36:39]\n"
      "v_cvt_pk_bf16_f32 v20, v20, v21\n"
      "v_mfma_f32_4x4x4_16b_bf16 v[48:51], a[74:75], a[42:43], v[40:43]\n"
      "ds_write_addtid_b32 v20 offset:57360\n"
      "v_mfma_f32_16x16x32_bf16 v[24:27], a[0:3], a[160:163], v[24:27]\n"
      "buffer_store_dwordx2 v[90:91], v15, s[36:39], s52 offen offset:1536 sc1\n"
      "v_cvt_pk_bf16_f32 v21, v22, v23\n"
      "v_mfma_f32_16x16x32_bf16 v[24:27], a[4:7], a[172:175], v[24:27]\n"
      "s_mov_b32 s52, s13\n"
      "ds_read_b64_tr_b16 a[44:45], v2 offset:64\n"
      "v_cvt_pk_bf16_f32 v44, v44, v48\n"
      "v_mfma_f32_16x16x32_bf16 v[24:27], a[8:11], a[184:187], v[24:27]\n"
      "s_waitcnt lgkmcnt(5)\n"
      "ds_write_addtid_b32 v21 offset:57616\n"
      "v_cvt_pk_bf16_f32 v46, v46, v50\n"
      "v_mfma_f32_16x16x32_bf16 v[72:75], a[78:81], a[64:67], 0\n"
      "s_add_u32 s13, s13, s12\n"
      "ds_read_b64_tr_b16 a[46:47], v2 offset:72\n"
      "v_add_f32_e32 v45, v36, v37\n"
      "v_mfma_f32_16x16x32_bf16 v[28:31], a[0:3], a[164:167], v[28:31]\n"
      "ds_read_b128 a[0:3], v7 offset:768\n"
      "v_cvt_pk_bf16_f32 v24, v24, v25\n"
      "v_mfma_f32_16x16x32_bf16 v[28:31], a[4:7], a[176:179], v[28:31]\n"
      "ds_read_b128 a[4:7], v7 offset:1024\n"
      "v_cvt_pk_bf16_f32 v25, v26, v27\n"
      "v_mfma_f32_16x16x32_bf16 v[28:31], a[8:11], a[188:191], v[28:31]\n"
      "ds_read_b128 a[8:11], v7 offset:1280\n"
      "v_sub_f32_e32 v37, v36, v37\n"
      "v_mfma_f32_16x16x32_bf16 v[76:79], a[78:81], a[68:71], 0\n"
      "ds_read_b64_tr_b16 a[48:49], v3 offset:64\n"
      "v_add_f32_e32 v49, v40, v41\n"
      "v_mfma_f32_16x16x32_bf16 v[32:35], a[12:15], v[92:95], 0\n"
      "ds_read_b64_tr_b16 a[50:51], v3 offset:72\n"
      "v_sub_f32_e32 v41, v40, v41\n"
      "v_mfma_f32_16x16x32_bf16 v[32:35], a[16:19], v[148:151], v[32:35]\n"
      "ds_write_addtid_b32 v24 offset:45064\n"
      "ds_write_addtid_b32 v25 offset:45320\n"
      "v_mfma_f32_16x16x32_bf16 v[32:35], a[20:23], v[204:207], v[32:35]\n"
      "s_waitcnt lgkmcnt(9)\n"
      "ds_read2_b64 a[60:63], v11 offset0:2 offset1:66\n"
      "v_add_f32_e32 v47, v38, v39\n"
      "v_mfma_f32_16x16x32_bf16 v[32:35], a[24:27], v[112:115], v[32:35]\n"
      "s_mov_b32 m0, s18\n"
      "v_sub_f32_e32 v39, v38, v39\n"
      "v_add_f32_e32 v51, v42, v43\n"
      "v_mfma_f32_16x16x32_bf16 v[32:35], a[28:31], a[204:207], v[32:35]\n"
      "ds_write_addtid_b32 v44 offset:4608\n"
      "v_sub_f32_e32 v43, v42, v43\n"
      "v_mfma_f32_16x16x32_bf16 v[32:35], a[32:35], v[220:223], v[32:35]\n"
      "s_waitcnt lgkmcnt(7)\n"
      "ds_write_addtid_b32 v46 offset:6912\n"
      "v_cvt_pk_bf16_f32 v45, v45, v49\n"
      "v_mfma_f32_16x16x32_bf16 v[32:35], a[0:3], a[208:211], v[32:35]\n"
      "v_cvt_pk_bf16_f32 v37, v37, v41\n"
      "v_cvt_pk_bf16_f32 v47, v47, v51\n"
      "v_mfma_f32_16x16x32_bf16 v[32:35], a[4:7], v[184:187], v[32:35]\n"
      "s_waitcnt lgkmcnt(6)\n"
      "ds_write_addtid_b32 v45 offset:5376\n"
      "v_cvt_pk_bf16_f32 v39, v39, v43\n"
      "v_mfma_f32_16x16x32_bf16 v[32:35], a[8:11], v[240:243], v[32:35]\n"
      "ds_write_addtid_b32 v37 offset:6144\n"
      "ds_write_addtid_b32 v47 offset:7680\n"
      "v_mfma_f32_16x16x32_bf16 v[20:23], a[12:15], v[100:103], 0\n"
      "ds_write_addtid_b32 v39 offset:8448\n"
      "v_cvt_pk_bf16_f32 v28, v28, v29\n"
      "v_mfma_f32_16x16x32_bf16 v[20:23], a[16:19], a[196:199], v[20:23]\n"
      "s_mov_b32 m0, s45\n"
      "ds_read2_b64 a[64:67], v11 offset0:3 offset1:67\n"
      "v_cvt_pk_bf16_f32 v29, v30, v31\n"
      "v_mfma_f32_16x16x32_bf16 v[20:23], a[20:23], v[208:211], v[20:23]\n"
      "ds_write_addtid_b32 v28 offset:32768\n"
      "ds_write_addtid_b32 v29 offset:33024\n"
      "v_mfma_f32_16x16x32_bf16 v[20:23], a[24:27], a[200:203], v[20:23]\n"
      "s_add_u32 m0, s58, 13056\n"
      "ds_read_b64_tr_b16 a[52:53], v2 offset:128\n"
      "v_cvt_pk_bf16_f32 v32, v32, v33\n"
      "v_mfma_f32_16x16x32_bf16 v[20:23], a[28:31], v[172:175], v[20:23]\n"
      "buffer_load_dwordx4 v17, s[32:35], s53 offen offset:1024 lds\n"
      "v_cvt_pk_bf16_f32 v33, v34, v35\n"
      "v_mfma_f32_16x16x32_bf16 v[20:23], a[32:35], v[228:231], v[20:23]\n"
      "s_mov_b32 m0, s45\n"
      "ds_read_b64_tr_b16 a[54:55], v2 offset:136\n"
      "v_cvt_pk_bf16_f32 v80, v72, v73\n"
      "v_mfma_f32_16x16x32_bf16 v[20:23], a[0:3], v[136:139], v[20:23]\n"
      "ds_write_addtid_b32 v32 offset:58384\n"
      "v_cvt_pk_bf16_f32 v81, v76, v77\n"
      "v_mfma_f32_16x16x32_bf16 v[20:23], a[4:7], v[192:195], v[20:23]\n"
      "ds_write_addtid_b32 v33 offset:58640\n"
      "v_cvt_pk_bf16_f32 v86, v74, v75\n"
      "v_mfma_f32_16x16x32_bf16 v[20:23], a[8:11], v[244:247], v[20:23]\n"
      "s_waitcnt lgkmcnt(14)\n"
      "ds_read_b64_tr_b16 a[56:57], v3 offset:128\n"
      "v_cvt_pk_bf16_f32 v87, v78, v79\n"
      "v_mfma_f32_4x4x4_16b_bf16 v[52:55], a[72:73], a[44:45], 0\n"
      "ds_read_b64_tr_b16 a[58:59], v3 offset:136\n"
      "v_mfma_f32_4x4x4_16b_bf16 v[56:59], a[72:73], a[46:47], 0\n"
      "ds_read2_b64 a[68:71], v13 offset0:0 offset1:64\n"
      "v_mfma_f32_4x4x4_16b_bf16 v[60:63], a[74:75], a[48:49], v[52:55]\n"
      "v_cvt_pk_bf16_f32 v20, v20, v21\n"
      "v_mfma_f32_4x4x4_16b_bf16 v[64:67], a[74:75], a[50:51], v[56:59]\n"
      "s_waitcnt lgkmcnt(6)\n"
      "v_cvt_pk_bf16_f32 v21, v22, v23\n"
      "v_mfma_f32_16x16x32_bf16 v[68:71], a[78:81], a[60:63], 0\n"
      "global_load_dword v8, v9, s[54:55]\n"
      "v_add_f32_e32 v61, v52, v53\n"
      "v_mfma_f32_16x16x32_bf16 v[24:27], a[12:15], a[192:195], 0\n"
      "ds_read_b128 a[12:15], v6 offset:1536\n"
      "v_cvt_pk_bf16_f32 v60, v60, v64\n"
      "v_mfma_f32_16x16x32_bf16 v[24:27], a[16:19], v[160:163], v[24:27]\n"
      "ds_read_b128 a[16:19], v6 offset:1792\n"
      "v_cvt_pk_bf16_f32 v62, v62, v66\n"
      "v_mfma_f32_16x16x32_bf16 v[24:27], a[20:23], v[216:219], v[24:27]\n"
      "ds_read_b128 a[20:23], v6 offset:2048\n"
      "v_cvt_pk_bf16_f32 v82, v68, v69\n"
      "v_mfma_f32_16x16x32_bf16 v[24:27], a[24:27], v[124:127], v[24:27]\n"
      "ds_read_b128 a[24:27], v6 offset:6144\n"
      "v_add_f32_e32 v65, v56, v57\n"
      "v_mfma_f32_16x16x32_bf16 v[24:27], a[28:31], v[180:183], v[24:27]\n"
      "ds_read_b128 a[28:31], v6 offset:6400\n"
      "v_cvt_pk_bf16_f32 v61, v61, v65\n"
      "v_mfma_f32_16x16x32_bf16 v[24:27], a[32:35], v[232:235], v[24:27]\n"
      "ds_read_b128 a[32:35], v6 offset:6656\n"
      "v_sub_f32_e32 v53, v52, v53\n"
      "v_mfma_f32_16x16x32_bf16 v[24:27], a[0:3], a[212:215], v[24:27]\n"
      "ds_read_b128 a[0:3], v7 offset:1536\n"
      "v_sub_f32_e32 v57, v56, v57\n"
      "v_mfma_f32_16x16x32_bf16 v[24:27], a[4:7], v[196:199], v[24:27]\n"
      "ds_read_b128 a[4:7], v7 offset:1792\n"
      "v_cvt_pk_bf16_f32 v53, v53, v57\n"
      "v_mfma_f32_16x16x32_bf16 v[24:27], a[8:11], v[252:255], v[24:27]\n"
      "ds_read_b128 a[8:11], v7 offset:2048\n"
      "v_add_f32_e32 v63, v54, v55\n"
      "v_mfma_f32_16x16x32_bf16 v[72:75], a[78:81], a[64:67], 0\n"
      "s_waitcnt lgkmcnt(7)\n"
      "ds_write_addtid_b32 v20 offset:46088\n"
      "ds_write_addtid_b32 v21 offset:46344\n"
      "v_mfma_f32_16x16x32_bf16 v[28:31], a[12:15], v[96:99], 0\n"
      "s_add_u32 m0, s58, 13056\n"
      "v_cvt_pk_bf16_f32 v24, v24, v25\n"
      "v_cvt_pk_bf16_f32 v25, v26, v27\n"
      "v_mfma_f32_16x16x32_bf16 v[28:31], a[16:19], v[152:155], v[28:31]\n"
      "s_waitcnt lgkmcnt(7)\n"
      "v_cvt_pk_bf16_f32 v83, v72, v73\n"
      "v_add_f32_e32 v67, v58, v59\n"
      "v_mfma_f32_16x16x32_bf16 v[28:31], a[20:23], a[216:219], v[28:31]\n"
      "buffer_load_dwordx4 v17, s[32:35], s53 offen offset:2048 lds\n"
      "v_cvt_pk_bf16_f32 v63, v63, v67\n"
      "v_mfma_f32_16x16x32_bf16 v[28:31], a[24:27], v[116:119], v[28:31]\n"
      "s_waitcnt lgkmcnt(5)\n"
      "v_sub_f32_e32 v55, v54, v55\n"
      "v_sub_f32_e32 v59, v58, v59\n"
      "v_mfma_f32_16x16x32_bf16 v[28:31], a[28:31], v[168:171], v[28:31]\n"
      "s_mov_b32 m0, s45\n"
      "v_cvt_pk_bf16_f32 v55, v55, v59\n"
      "v_cvt_pk_bf16_f32 v88, v70, v71\n"
      "v_mfma_f32_16x16x32_bf16 v[28:31], a[32:35], v[224:227], v[28:31]\n"
      "s_waitcnt lgkmcnt(3)\n"
      "ds_write_addtid_b32 v24 offset:33792\n"
      "ds_write_addtid_b32 v25 offset:34048\n"
      "v_mfma_f32_16x16x32_bf16 v[28:31], a[0:3], v[132:135], v[28:31]\n"
      "s_mov_b32 m0, s18\n"
      "ds_read2_b64 a[60:63], v13 offset0:1 offset1:65\n"
      "v_cvt_pk_bf16_f32 v89, v74, v75\n"
      "v_mfma_f32_16x16x32_bf16 v[28:31], a[4:7], v[188:191], v[28:31]\n"
      "s_waitcnt lgkmcnt(5)\n"
      "ds_write_addtid_b32 v60 offset:4864\n"
      "ds_write_addtid_b32 v62 offset:7168\n"
      "v_mfma_f32_16x16x32_bf16 v[28:31], a[8:11], a[236:239], v[28:31]\n"
      "buffer_store_dwordx4 v[80:83], v14, s[36:39], s52 offen sc1\n"
      "s_nop 0\n"
      "v_mfma_f32_4x4x4_16b_bf16 v[36:39], a[72:73], a[52:53], 0\n"
      "ds_write_addtid_b32 v61 offset:5632\n"
      "v_mfma_f32_4x4x4_16b_bf16 v[40:43], a[72:73], a[54:55], 0\n"
      "ds_write_addtid_b32 v53 offset:6400\n"
      "v_mfma_f32_4x4x4_16b_bf16 v[44:47], a[74:75], a[56:57], v[36:39]\n"
      "ds_write_addtid_b32 v63 offset:7936\n"
      "v_mfma_f32_4x4x4_16b_bf16 v[48:51], a[74:75], a[58:59], v[40:43]\n"
      "ds_write_addtid_b32 v55 offset:8704\n"
      "v_mfma_f32_16x16x32_bf16 v[20:23], a[12:15], v[104:107], 0\n"
      "s_mov_b32 m0, s45\n"
      "v_cvt_pk_bf16_f32 v28, v28, v29\n"
      "v_cvt_pk_bf16_f32 v29, v30, v31\n"
      "v_mfma_f32_16x16x32_bf16 v[20:23], a[16:19], v[156:159], v[20:23]\n"
      "buffer_store_dwordx4 v[86:89], v14, s[36:39], s52 offen offset:1536 sc1\n"
      "v_cvt_pk_bf16_f32 v44, v44, v48\n"
      "v_mfma_f32_16x16x32_bf16 v[20:23], a[20:23], v[212:215], v[20:23]\n"
      "ds_write_addtid_b32 v28 offset:59408\n"
      "v_cvt_pk_bf16_f32 v46, v46, v50\n"
      "v_mfma_f32_16x16x32_bf16 v[20:23], a[24:27], v[120:123], v[20:23]\n"
      "ds_write_addtid_b32 v29 offset:59664\n"
      "v_add_f32_e32 v45, v36, v37\n"
      "v_mfma_f32_16x16x32_bf16 v[20:23], a[28:31], v[176:179], v[20:23]\n"
      "s_mov_b32 m0, s18\n"
      "v_add_f32_e32 v49, v40, v41\n"
      "v_cvt_pk_bf16_f32 v45, v45, v49\n"
      "v_mfma_f32_16x16x32_bf16 v[20:23], a[32:35], a[228:231], v[20:23]\n"
      "s_sub_u32 s18, s19, s18\n"
      "ds_write_addtid_b32 v44 offset:5120\n"
      "v_sub_f32_e32 v37, v36, v37\n"
      "v_mfma_f32_16x16x32_bf16 v[20:23], a[0:3], v[140:143], v[20:23]\n"
      "ds_write_addtid_b32 v46 offset:7424\n"
      "v_sub_f32_e32 v41, v40, v41\n"
      "v_mfma_f32_16x16x32_bf16 v[20:23], a[4:7], a[232:235], v[20:23]\n"
      "ds_write_addtid_b32 v45 offset:5888\n"
      "v_cvt_pk_bf16_f32 v37, v37, v41\n"
      "v_mfma_f32_16x16x32_bf16 v[20:23], a[8:11], v[248:251], v[20:23]\n"
      "ds_write_addtid_b32 v37 offset:6656\n"
      "v_add_f32_e32 v47, v38, v39\n"
      "v_mfma_f32_16x16x32_bf16 v[76:79], a[78:81], a[68:71], 0\n"
      "v_add_f32_e32 v51, v42, v43\n"
      "v_cvt_pk_bf16_f32 v47, v47, v51\n"
      "v_mfma_f32_16x16x32_bf16 v[32:35], a[12:15], v[108:111], 0\n"
      "ds_write_addtid_b32 v47 offset:8192\n"
      "v_sub_f32_e32 v39, v38, v39\n"
      "v_mfma_f32_16x16x32_bf16 v[32:35], a[16:19], v[164:167], v[32:35]\n"
      "v_sub_f32_e32 v43, v42, v43\n"
      "v_cvt_pk_bf16_f32 v39, v39, v43\n"
      "v_mfma_f32_16x16x32_bf16 v[32:35], a[20:23], a[220:223], v[32:35]\n"
      "ds_write_addtid_b32 v39 offset:8960\n"
      "v_cvt_pk_bf16_f32 v20, v20, v21\n"
      "v_mfma_f32_16x16x32_bf16 v[32:35], a[24:27], v[128:131], v[32:35]\n"
      "s_mov_b32 m0, s45\n"
      "v_cvt_pk_bf16_f32 v21, v22, v23\n"
      "v_cvt_pk_bf16_f32 v84, v76, v77\n"
      "v_mfma_f32_16x16x32_bf16 v[32:35], a[28:31], a[224:227], v[32:35]\n"
      "ds_write_addtid_b32 v20 offset:47112\n"
      "v_cvt_pk_bf16_f32 v90, v78, v79\n"
      "v_mfma_f32_16x16x32_bf16 v[32:35], a[32:35], v[236:239], v[32:35]\n"
      "ds_write_addtid_b32 v21 offset:47368\n"
      "v_mfma_f32_16x16x32_bf16 v[32:35], a[0:3], v[144:147], v[32:35]\n"
      "v_mfma_f32_16x16x32_bf16 v[32:35], a[4:7], v[200:203], v[32:35]\n"
      "v_mfma_f32_16x16x32_bf16 v[32:35], a[8:11], a[240:243], v[32:35]\n"
      "s_nop 1\n"
      "s_cbranch_vccnz Lconv_loop0\n"
      "Lconv_t0:\n"
      "s_nop 1\n"
      "v_mfma_f32_16x16x32_bf16 v[20:23], a[24:27], v[96:99], 0\n"
      "s_nop 1\n"
      "v_mfma_f32_16x16x32_bf16 v[20:23], a[28:31], v[152:155], v[20:23]\n"
      "v_cvt_pk_bf16_f32 v32, v32, v33\n"
      "v_cvt_pk_bf16_f32 v33, v34, v35\n"
      "v_mfma_f32_16x16x32_bf16 v[20:23], a[32:35], a[216:219], v[20:23]\n"
      "ds_write_addtid_b32 v32 offset:34816\n"
      "v_mfma_f32_16x16x32_bf16 v[20:23], a[0:3], v[116:119], v[20:23]\n"
      "ds_write_addtid_b32 v33 offset:35072\n"
      "v_mfma_f32_16x16x32_bf16 v[20:23], a[4:7], v[168:171], v[20:23]\n"
      "v_mfma_f32_16x16x32_bf16 v[20:23], a[8:11], v[224:227], v[20:23]\n"
      "v_mfma_f32_16x16x32_bf16 v[24:27], a[24:27], v[104:107], 0\n"
      "s_waitcnt lgkmcnt(0)\n"
      "s_barrier\n"
      "v_mfma_f32_16x16x32_bf16 v[24:27], a[28:31], v[156:159], v[24:27]\n"
      "ds_read2_b64 a[64:67], v10 offset0:0 offset1:64\n"
      "v_mfma_f32_16x16x32_bf16 v[24:27], a[32:35], v[212:215], v[24:27]\n"
      "ds_read_b128 a[12:15], v7 offset:6144\n"
      "v_mfma_f32_16x16x32_bf16 v[24:27], a[0:3], v[120:123], v[24:27]\n"
      "ds_read2_b64 a[68:71], v10 offset0:1 offset1:65\n"
      "v_mfma_f32_16x16x32_bf16 v[24:27], a[4:7], v[176:179], v[24:27]\n"
      "ds_read_b128 a[16:19], v7 offset:6400\n"
      "v_mfma_f32_16x16x32_bf16 v[24:27], a[8:11], a[228:231], v[24:27]\n"
      "ds_read_b128 a[20:23], v7 offset:6656\n"
      "v_mfma_f32_16x16x32_bf16 v[68:71], a[78:81], a[60:63], 0\n"
      "ds_read2_b64 a[60:63], v10 offset0:2 offset1:66\n"
      "s_nop 0\n"
      "v_mfma_f32_16x16x32_bf16 v[28:31], a[24:27], v[108:111], 0\n"
      "s_waitcnt lgkmcnt(5)\n"
      "ds_read_b128 a[24:27], v6 offset:5376\n"
      "s_nop 0\n"
      "v_mfma_f32_16x16x32_bf16 v[72:75], a[78:81], a[64:67], 0\n"
      "ds_read2_b64 a[64:67], v10 offset0:3 offset1:67\n"
      "v_cvt_pk_bf16_f32 v85, v68, v69\n"
      "v_mfma_f32_16x16x32_bf16 v[28:31], a[28:31], v[164:167], v[28:31]\n"
      "buffer_store_dwordx2 v[84:85], v15, s[36:39], s52 offen sc1\n"
      "v_cvt_pk_bf16_f32 v91, v70, v71\n"
      "v_mfma_f32_16x16x32_bf16 v[28:31], a[32:35], a[220:223], v[28:31]\n"
      "ds_read_b128 a[28:31], v6 offset:5632\n"
      "v_mfma_f32_16x16x32_bf16 v[28:31], a[0:3], v[128:131], v[28:31]\n"
      "ds_read_b128 a[32:35], v6 offset:5888\n"
      "v_cvt_pk_bf16_f32 v80, v72, v73\n"
      "v_mfma_f32_16x16x32_bf16 v[28:31], a[4:7], a[224:227], v[28:31]\n"
      "ds_read_b128 a[0:3], v7 offset:768\n"
      "v_cvt_pk_bf16_f32 v86, v74, v75\n"
      "v_mfma_f32_16x16x32_bf16 v[28:31], a[8:11], v[236:239], v[28:31]\n"
      "s_waitcnt lgkmcnt(6)\n"
      "ds_read_b128 a[4:7], v7 offset:1024\n"
      "v_mfma_f32_16x16x32_bf16 v[20:23], a[12:15], v[132:135], v[20:23]\n"
      "ds_read_b128 a[8:11], v7 offset:1280\n"
      "v_mfma_f32_16x16x32_bf16 v[76:79], a[78:81], a[68:71], 0\n"
      "s_waitcnt lgkmcnt(6)\n"
      "ds_read2_b64 a[68:71], v12 offset0:0 offset1:64\n"
      "s_nop 0\n"
      "v_mfma_f32_16x16x32_bf16 v[68:71], a[78:81], a[60:63], 0\n"
      "ds_read2_b64 a[60:63], v12 offset0:1 offset1:65\n"
      "s_nop 0\n"
      "v_mfma_f32_16x16x32_bf16 v[20:23], a[16:19], v[188:191], v[20:23]\n"
      "buffer_store_dwordx2 v[90:91], v15, s[36:39], s52 offen offset:1536 sc1\n"
      "v_cvt_pk_bf16_f32 v81, v76, v77\n"
      "v_mfma_f32_16x16x32_bf16 v[20:23], a[20:23], a[236:239], v[20:23]\n"
      "s_mov_b32 s52, s13\n"
      "v_cvt_pk_bf16_f32 v87, v78, v79\n"
      "v_cvt_pk_bf16_f32 v82, v68, v69\n"
      "v_mfma_f32_16x16x32_bf16 v[24:27], a[12:15], v[140:143], v[24:27]\n"
      "s_waitcnt lgkmcnt(5)\n"
      "v_cvt_pk_bf16_f32 v88, v70, v71\n"
      "s_nop 0\n"
      "v_mfma_f32_16x16x32_bf16 v[72:75], a[78:81], a[64:67], 0\n"
      "s_add_u32 s13, s13, s12\n"
      "v_cvt_pk_bf16_f32 v20, v20, v21\n"
      "v_cvt_pk_bf16_f32 v21, v22, v23\n"
      "v_mfma_f32_16x16x32_bf16 v[24:27], a[16:19], a[232:235], v[24:27]\n"
      "ds_write_addtid_b32 v20 offset:62480\n"
      "s_nop 0\n"
      "v_mfma_f32_16x16x32_bf16 v[24:27], a[20:23], v[248:251], v[24:27]\n"
      "ds_write_addtid_b32 v21 offset:62736\n"
      "v_cvt_pk_bf16_f32 v83, v72, v73\n"
      "v_mfma_f32_16x16x32_bf16 v[28:31], a[12:15], v[144:147], v[28:31]\n"
      "s_waitcnt lgkmcnt(3)\n"
      "ds_read_b128 a[12:15], v7 offset:5376\n"
      "v_cvt_pk_bf16_f32 v89, v74, v75\n"
      "v_mfma_f32_16x16x32_bf16 v[76:79], a[78:81], a[68:71], 0\n"
      "buffer_store_dwordx4 v[80:83], v14, s[36:39], s52 offen sc1\n"
      "v_cvt_pk_bf16_f32 v24, v24, v25\n"
      "v_mfma_f32_16x16x32_bf16 v[28:31], a[16:19], v[200:203], v[28:31]\n"
      "ds_read_b128 a[16:19], v7 offset:5632\n"
      "v_cvt_pk_bf16_f32 v25, v26, v27\n"
      "v_mfma_f32_16x16x32_bf16 v[28:31], a[20:23], a[240:243], v[28:31]\n"
      "ds_read_b128 a[20:23], v7 offset:5888\n"
      "v_mfma_f32_16x16x32_bf16 v[32:35], a[24:27], v[92:95], 0\n"
      "s_waitcnt lgkmcnt(5)\n"
      "ds_write_addtid_b32 v24 offset:50184\n"
      "v_cvt_pk_bf16_f32 v84, v76, v77\n"
      "v_mfma_f32_16x16x32_bf16 v[68:71], a[78:81], a[60:63], 0\n"
      "ds_write_addtid_b32 v25 offset:50440\n"
      "v_cvt_pk_bf16_f32 v90, v78, v79\n"
      "v_mfma_f32_16x16x32_bf16 v[32:35], a[28:31], v[148:151], v[32:35]\n"
      "v_cvt_pk_bf16_f32 v28, v28, v29\n"
      "v_cvt_pk_bf16_f32 v29, v30, v31\n"
      "v_mfma_f32_16x16x32_bf16 v[32:35], a[32:35], v[204:207], v[32:35]\n"
      "buffer_store_dwordx4 v[86:89], v14, s[36:39], s52 offen offset:1536 sc1\n"
      "v_mfma_f32_16x16x32_bf16 v[32:35], a[0:3], v[112:115], v[32:35]\n"
      "v_cvt_pk_bf16_f32 v85, v68, v69\n"
      "v_cvt_pk_bf16_f32 v91, v70, v71\n"
      "v_mfma_f32_16x16x32_bf16 v[32:35], a[4:7], a[204:207], v[32:35]\n"
      "ds_write_addtid_b32 v28 offset:37888\n"
      "v_mfma_f32_16x16x32_bf16 v[32:35], a[8:11], v[220:223], v[32:35]\n"
      "s_waitcnt lgkmcnt(4)\n"
      "ds_write_addtid_b32 v29 offset:38144\n"
      "v_mfma_f32_16x16x32_bf16 v[32:35], a[12:15], a[208:211], v[32:35]\n"
      "v_mfma_f32_16x16x32_bf16 v[32:35], a[16:19], v[184:187], v[32:35]\n"
      "s_waitcnt lgkmcnt(4)\n"
      "v_mfma_f32_16x16x32_bf16 v[32:35], a[20:23], v[240:243], v[32:35]\n"
      "buffer_store_dwordx2 v[84:85], v15, s[36:39], s52 offen sc1\n"
      "s_nop 0\n"
      "v_mfma_f32_16x16x32_bf16 v[20:23], a[24:27], v[100:103], 0\n"
      "s_nop 1\n"
      "v_mfma_f32_16x16x32_bf16 v[20:23], a[28:31], a[196:199], v[20:23]\n"
      "s_nop 0\n"
      "v_mfma_f32_16x16x32_bf16 v[20:23], a[32:35], v[208:211], v[20:23]\n"
      "v_cvt_pk_bf16_f32 v32, v32, v33\n"
      "v_cvt_pk_bf16_f32 v33, v34, v35\n"
      "v_mfma_f32_16x16x32_bf16 v[20:23], a[0:3], a[200:203], v[20:23]\n"
      "ds_write_addtid_b32 v32 offset:61456\n"
      "v_mfma_f32_16x16x32_bf16 v[20:23], a[4:7], v[172:175], v[20:23]\n"
      "ds_write_addtid_b32 v33 offset:61712\n"
      "v_mfma_f32_16x16x32_bf16 v[20:23], a[8:11], v[228:231], v[20:23]\n"
      "buffer_store_dwordx2 v[90:91], v15, s[36:39], s52 offen offset:1536 sc1\n"
      "v_mfma_f32_16x16x32_bf16 v[20:23], a[12:15], v[136:139], v[20:23]\n"
      "v_mfma_f32_16x16x32_bf16 v[20:23], a[16:19], v[192:195], v[20:23]\n"
      "v_mfma_f32_16x16x32_bf16 v[20:23], a[20:23], v[244:247], v[20:23]\n"
      "s_nop 1\n"
      "v_mfma_f32_16x16x32_bf16 v[24:27], a[24:27], a[192:195], 0\n"
      "ds_read_b128 a[24:27], v7 offset:4608\n"
      "s_nop 0\n"
      "v_mfma_f32_16x16x32_bf16 v[24:27], a[28:31], v[160:163], v[24:27]\n"
      "ds_read_b128 a[28:31], v7 offset:4864\n"
      "v_mfma_f32_16x16x32_bf16 v[24:27], a[32:35], v[216:219], v[24:27]\n"
      "ds_read_b128 a[32:35], v7 offset:5120\n"
      "v_cvt_pk_bf16_f32 v20, v20, v21\n"
      "v_mfma_f32_16x16x32_bf16 v[24:27], a[0:3], v[124:127], v[24:27]\n"
      "ds_read_b128 a[0:3], v6 offset:4608\n"
      "v_cvt_pk_bf16_f32 v21, v22, v23\n"
      "v_mfma_f32_16x16x32_bf16 v[24:27], a[4:7], v[180:183], v[24:27]\n"
      "ds_read_b128 a[4:7], v6 offset:4864\n"
      "v_mfma_f32_16x16x32_bf16 v[24:27], a[8:11], v[232:235], v[24:27]\n"
      "ds_read_b128 a[8:11], v6 offset:5120\n"
      "v_mfma_f32_16x16x32_bf16 v[24:27], a[12:15], a[212:215], v[24:27]\n"
      "ds_read_b128 a[12:15], v7 offset:0\n"
      "v_mfma_f32_16x16x32_bf16 v[24:27], a[16:19], v[196:199], v[24:27]\n"
      "ds_read_b128 a[16:19], v7 offset:256\n"
      "v_mfma_f32_16x16x32_bf16 v[24:27], a[20:23], v[252:255], v[24:27]\n"
      "s_waitcnt lgkmcnt(7)\n"
      "ds_read_b128 a[20:23], v7 offset:512\n"
      "s_nop 0\n"
      "v_mfma_f32_16x16x32_bf16 v[28:31], a[24:27], a[156:159], 0\n"
      "s_waitcnt lgkmcnt(7)\n"
      "ds_write_addtid_b32 v20 offset:49160\n"
      "ds_write_addtid_b32 v21 offset:49416\n"
      "v_mfma_f32_16x16x32_bf16 v[28:31], a[28:31], a[168:171], v[28:31]\n"
      "s_waitcnt lgkmcnt(8)\n"
      "v_cvt_pk_bf16_f32 v24, v24, v25\n"
      "v_cvt_pk_bf16_f32 v25, v26, v27\n"
      "v_mfma_f32_16x16x32_bf16 v[28:31], a[32:35], a[180:183], v[28:31]\n"
      "s_waitcnt lgkmcnt(7)\n"
      "ds_write_addtid_b32 v24 offset:36864\n"
      "v_mfma_f32_16x16x32_bf16 v[28:31], a[0:3], a[84:87], v[28:31]\n"
      "s_waitcnt lgkmcnt(7)\n"
      "ds_write_addtid_b32 v25 offset:37120\n"
      "v_mfma_f32_16x16x32_bf16 v[28:31], a[4:7], a[96:99], v[28:31]\n"
      "s_waitcnt lgkmcnt(7)\n"
      "v_mfma_f32_16x16x32_bf16 v[28:31], a[8:11], a[108:111], v[28:31]\n"
      "s_waitcnt lgkmcnt(6)\n"
      "v_mfma_f32_16x16x32_bf16 v[28:31], a[12:15], a[120:123], v[28:31]\n"
      "s_waitcnt lgkmcnt(5)\n"
      "v_mfma_f32_16x16x32_bf16 v[28:31], a[16:19], a[132:135], v[28:31]\n"
      "s_waitcnt lgkmcnt(4)\n"
      "v_mfma_f32_16x16x32_bf16 v[28:31], a[20:23], a[144:147], v[28:31]\n"
      "s_nop 1\n"
      "v_mfma_f32_16x16x32_bf16 v[20:23], a[24:27], a[160:163], 0\n"
      "s_nop 1\n"
      "v_mfma_f32_16x16x32_bf16 v[20:23], a[28:31], a[172:175], v[20:23]\n"
      "s_nop 0\n"
      "v_mfma_f32_16x16x32_bf16 v[20:23], a[32:35], a[184:187], v[20:23]\n"
      "v_cvt_pk_bf16_f32 v28, v28, v29\n"
      "v_cvt_pk_bf16_f32 v29, v30, v31\n"
      "v_mfma_f32_16x16x32_bf16 v[20:23], a[0:3], a[88:91], v[20:23]\n"
      "ds_write_addtid_b32 v28 offset:60432\n"
      "v_mfma_f32_16x16x32_bf16 v[20:23], a[4:7], a[100:103], v[20:23]\n"
      "ds_write_addtid_b32 v29 offset:60688\n"
      "v_mfma_f32_16x16x32_bf16 v[20:23], a[8:11], a[112:115], v[20:23]\n"
      "v_mfma_f32_16x16x32_bf16 v[20:23], a[12:15], a[124:127], v[20:23]\n"
      "v_mfma_f32_16x16x32_bf16 v[20:23], a[16:19], a[136:139], v[20:23]\n"
      "v_mfma_f32_16x16x32_bf16 v[20:23], a[20:23], a[148:151], v[20:23]\n"
      "s_nop 1\n"
      "v_mfma_f32_16x16x32_bf16 v[32:35], a[24:27], a[164:167], 0\n"
      "s_nop 1\n"
      "v_mfma_f32_16x16x32_bf16 v[32:35], a[28:31], a[176:179], v[32:35]\n"
      "s_nop 0\n"
      "v_mfma_f32_16x16x32_bf16 v[32:35], a[32:35], a[188:191], v[32:35]\n"
      "v_cvt_pk_bf16_f32 v20, v20, v21\n"
      "v_cvt_pk_bf16_f32 v21, v22, v23\n"
      "v_mfma_f32_16x16x32_bf16 v[32:35], a[0:3], a[92:95], v[32:35]\n"
      "ds_write_addtid_b32 v20 offset:48136\n"
      "v_mfma_f32_16x16x32_bf16 v[32:35], a[4:7], a[104:107], v[32:35]\n"
      "ds_write_addtid_b32 v21 offset:48392\n"
      "v_mfma_f32_16x16x32_bf16 v[32:35], a[8:11], a[116:119], v[32:35]\n"
      "v_mfma_f32_16x16x32_bf16 v[32:35], a[12:15], a[128:131], v[32:35]\n"
      "v_mfma_f32_16x16x32_bf16 v[32:35], a[16:19], a[140:143], v[32:35]\n"
      "v_mfma_f32_16x16x32_bf16 v[32:35], a[20:23], a[152:155], v[32:35]\n"
      "s_nop 1\n"
      "s_nop 5\n"
      "v_cvt_pk_bf16_f32 v32, v32, v33\n"
      "v_cvt_pk_bf16_f32 v33, v34, v35\n"
      "ds_write_addtid_b32 v32 offset:35840\n"
      "ds_write_addtid_b32 v33 offset:36096\n"
      "s_waitcnt lgkmcnt(0)\n"
      "s_barrier\n"
      "ds_read2_b64 a[0:3], v11 offset0:0 offset1:64\n"
      "ds_read2_b64 a[4:7], v11 offset0:1 offset1:65\n"
      "ds_read2_b64 a[8:11], v11 offset0:2 offset1:66\n"
      "ds_read2_b64 a[12:15], v11 offset0:3 offset1:67\n"
      "ds_read2_b64 a[16:19], v13 offset0:0 offset1:64\n"
      "ds_read2_b64 a[20:23], v13 offset0:1 offset1:65\n"
      "s_waitcnt lgkmcnt(5)\n"
      "v_mfma_f32_16x16x32_bf16 v[72:75], a[78:81], a[0:3], 0\n"
      "s_waitcnt lgkmcnt(4)\n"
      "v_mfma_f32_16x16x32_bf16 v[76:79], a[78:81], a[4:7], 0\n"
      "s_waitcnt lgkmcnt(3)\n"
      "v_mfma_f32_16x16x32_bf16 v[68:71], a[78:81], a[8:11], 0\n"
      "s_waitcnt lgkmcnt(2)\n"
      "s_nop 2\n"
      "v_cvt_pk_bf16_f32 v80, v72, v73\n"
      "v_cvt_pk_bf16_f32 v86, v74, v75\n"
      "v_mfma_f32_16x16x32_bf16 v[72:75], a[78:81], a[12:15], 0\n"
      "s_waitcnt lgkmcnt(1)\n"
      "v_cvt_pk_bf16_f32 v81, v76, v77\n"
      "v_cvt_pk_bf16_f32 v87, v78, v79\n"
      "v_cvt_pk_bf16_f32 v82, v68, v69\n"
      "v_cvt_pk_bf16_f32 v88, v70, v71\n"
      "v_mfma_f32_16x16x32_bf16 v[76:79], a[78:81], a[16:19], 0\n"
      "s_waitcnt lgkmcnt(0)\n"
      "v_mfma_f32_16x16x32_bf16 v[68:71], a[78:81], a[20:23], 0\n"
      "s_mov_b32 s52, s13\n"
      "v_cvt_pk_bf16_f32 v83, v72, v73\n"
      "v_cvt_pk_bf16_f32 v89, v74, v75\n"
      "s_add_u32 s13, s13, s12\n"
      "buffer_store_dwordx4 v[80:83], v14, s[36:39], s52 offen sc1\n"
      "buffer_store_dwordx4 v[86:89], v14, s[36:39], s52 offen offset:1536 sc1\n"
      "v_cvt_pk_bf16_f32 v84, v76, v77\n"
      "v_cvt_pk_bf16_f32 v90, v78, v79\n"
      "v_cvt_pk_bf16_f32 v85, v68, v69\n"
      "v_cvt_pk_bf16_f32 v91, v70, v71\n"
      "buffer_store_dwordx2 v[84:85], v15, s[36:39], s52 offen sc1\n"
      "buffer_store_dwordx2 v[90:91], v15, s[36:39], s52 offen offset:1536 sc1\n"
      "Lconv_u0:\n"
      "s_waitcnt vmcnt(0)\n"
      "s_endpgm\n"
      ".p2align 12\n"
      "Lconv_pa1:\n"
      "s_cbranch_execz Lconv_r0\n"
      "s_nop 0\n"
      "Lconv_loop1:\n"
      "v_mfma_f32_16x16x32_bf16 v[20:23], a[24:27], v[96:99], 0\n"
      "s_add_u32 s51, s51, s12\n"
      "v_cvt_pk_bf16_f32 v32, v32, v33\n"
      "v_cvt_pk_bf16_f32 v33, v34, v35\n"
      "v_mfma_f32_16x16x32_bf16 v[20:23], a[28:31], v[152:155], v[20:23]\n"
      "s_sub_u32 s58, s93, s58\n"
      "ds_write_addtid_b32 v32 offset:34816\n"
      "v_cvt_pk_bf16_f32 v83, v72, v73\n"
      "v_mfma_f32_16x16x32_bf16 v[20:23], a[32:35], a[216:219], v[20:23]\n"
      "buffer_store_dwordx4 v[80:83], v14, s[36:39], s52 offen sc1\n"
      "v_cvt_pk_bf16_f32 v89, v74, v75\n"
      "v_mfma_f32_16x16x32_bf16 v[20:23], a[0:3], v[116:119], v[20:23]\n"
      "s_sub_u32 s59, s94, s59\n"
      "ds_write_addtid_b32 v33 offset:35072\n"
      "v_cvt_pk_bf16_f32 v84, v76, v77\n"
      "v_mfma_f32_16x16x32_bf16 v[20:23], a[4:7], v[168:171], v[20:23]\n"
      "s_sub_u32 s47, s51, s48\n"
      "v_cvt_pk_bf16_f32 v90, v78, v79\n"
      "v_mfma_f32_16x16x32_bf16 v[20:23], a[8:11], v[224:227], v[20:23]\n"
      "buffer_store_dwordx4 v[86:89], v14, s[36:39], s52 offen offset:1536 sc1\n"
      "v_mfma_f32_16x16x32_bf16 v[24:27], a[24:27], v[104:107], 0\n"
      "s_cmp_ge_u32 vcc_hi, 0x80000000\n"
      "v_mfma_f32_16x16x32_bf16 v[24:27], a[28:31], v[156:159], v[24:27]\n"
      "s_cselect_b32 s53, s47, s51\n"
      "v_mfma_f32_16x16x32_bf16 v[24:27], a[32:35], v[212:215], v[24:27]\n"
      "v_mfma_f32_16x16x32_bf16 v[24:27], a[0:3], v[120:123], v[24:27]\n"
      "v_mfma_f32_16x16x32_bf16 v[24:27], a[4:7], v[176:179], v[24:27]\n"
      "v_mfma_f32_16x16x32_bf16 v[24:27], a[8:11], a[228:231], v[24:27]\n"
      "s_waitcnt vmcnt(17) lgkmcnt(0)\n"
      "s_barrier\n"
      "v_mfma_f32_16x16x32_bf16 v[68:71], a[78:81], a[60:63], 0\n"
      "s_mov_b32 m0, s58\n"
      "ds_read_b128 a[12:15], v7 offset:6144\n"
      "s_nop 0\n"
      "v_mfma_f32_16x16x32_bf16 v[28:31], a[24:27], v[108:111], 0\n"
      "ds_read_b128 a[16:19], v7 offset:6400\n"
      "s_nop 0\n"
      "v_mfma_f32_16x16x32_bf16 v[28:31], a[28:31], v[164:167], v[28:31]\n"
      "ds_read_b128 a[20:23], v7 offset:6656\n"
      "v_cvt_pk_bf16_f32 v85, v68, v69\n"
      "v_mfma_f32_16x16x32_bf16 v[28:31], a[32:35], a[220:223], v[28:31]\n"
      "ds_read_b64_tr_b16 a[36:37], v2 offset:13056\n"
      "v_cvt_pk_bf16_f32 v91, v70, v71\n"
      "v_mfma_f32_16x16x32_bf16 v[28:31], a[0:3], v[128:131], v[28:31]\n"
      "ds_read_b64_tr_b16 a[38:39], v2 offset:13064\n"
      "v_mfma_f32_16x16x32_bf16 v[28:31], a[4:7], a[224:227], v[28:31]\n"
      "ds_read_b64_tr_b16 a[40:41], v3 offset:13056\n"
      "v_mfma_f32_16x16x32_bf16 v[28:31], a[8:11], v[236:239], v[28:31]\n"
      "s_waitcnt lgkmcnt(5)\n"
      "ds_read_b64_tr_b16 a[42:43], v3 offset:13064\n"
      "v_mfma_f32_16x16x32_bf16 v[20:23], a[12:15], v[132:135], v[20:23]\n"
      "s_waitcnt lgkmcnt(5)\n"
      "ds_read2_b64 a[64:67], v10 offset0:0 offset1:64\n"
      "v_mfma_f32_16x16x32_bf16 v[20:23], a[16:19], v[188:191], v[20:23]\n"
      "s_waitcnt lgkmcnt(5)\n"
      "ds_read_b128 a[24:27], v6 offset:5376\n"
      "v_mfma_f32_16x16x32_bf16 v[20:23], a[20:23], a[236:239], v[20:23]\n"
      "buffer_store_dwordx2 v[84:85], v15, s[36:39], s52 offen sc1\n"
      "s_nop 0\n"
      "v_mfma_f32_16x16x32_bf16 v[24:27], a[12:15], v[140:143], v[24:27]\n"
      "ds_read_b128 a[28:31], v6 offset:5632\n"
      "s_nop 0\n"
      "v_mfma_f32_16x16x32_bf16 v[24:27], a[16:19], a[232:235], v[24:27]\n"
      "ds_read_b128 a[32:35], v6 offset:5888\n"
      "v_mfma_f32_16x16x32_bf16 v[24:27], a[20:23], v[248:251], v[24:27]\n"
      "s_waitcnt lgkmcnt(4)\n"
      "ds_read_b128 a[0:3], v7 offset:768\n"
      "v_cvt_pk_bf16_f32 v20, v20, v21\n"
      "v_mfma_f32_4x4x4_16b_bf16 v[52:55], a[72:73], a[36:37], 0\n"
      "ds_read_b128 a[4:7], v7 offset:1024\n"
      "v_mfma_f32_4x4x4_16b_bf16 v[56:59], a[72:73], a[38:39], 0\n"
      "ds_read_b128 a[8:11], v7 offset:1280\n"
      "v_mfma_f32_4x4x4_16b_bf16 v[60:63], a[74:75], a[40:41], v[52:55]\n"
      "ds_read_b64_tr_b16 a[44:45], v2 offset:13120\n"
      "v_mfma_f32_4x4x4_16b_bf16 v[64:67], a[74:75], a[42:43], v[56:59]\n"
      "ds_read_b64_tr_b16 a[46:47], v2 offset:13128\n"
      "v_mfma_f32_16x16x32_bf16 v[28:31], a[12:15], v[144:147], v[28:31]\n"
      "buffer_store_dwordx2 v[90:91], v15, s[36:39], s52 offen offset:1536 sc1\n"
      "v_cvt_pk_bf16_f32 v21, v22, v23\n"
      "v_mfma_f32_16x16x32_bf16 v[28:31], a[16:19], v[200:203], v[28:31]\n"
      "s_mov_b32 s52, s13\n"
      "ds_read_b128 a[12:15], v7 offset:5376\n"
      "v_cvt_pk_bf16_f32 v24, v24, v25\n"
      "v_mfma_f32_16x16x32_bf16 v[28:31], a[20:23], a[240:243], v[28:31]\n"
      "s_waitcnt lgkmcnt(6)\n"
      "ds_read_b128 a[16:19], v7 offset:5632\n"
      "v_cvt_pk_bf16_f32 v25, v26, v27\n"
      "v_mfma_f32_16x16x32_bf16 v[72:75], a[78:81], a[64:67], 0\n"
      "buffer_load_dwordx4 v16, s[32:35], s53 offen lds\n"
      "v_cvt_pk_bf16_f32 v60, v60, v64\n"
      "v_mfma_f32_16x16x32_bf16 v[32:35], a[24:27], v[92:95], 0\n"
      "s_mov_b32 m0, s45\n"
      "ds_read_b128 a[20:23], v7 offset:5888\n"
      "v_cvt_pk_bf16_f32 v28, v28, v29\n"
      "v_mfma_f32_16x16x32_bf16 v[32:35], a[28:31], v[148:151], v[32:35]\n"
      "s_add_u32 s13, s13, s12\n"
      "ds_read_b64_tr_b16 a[48:49], v3 offset:13120\n"
      "v_cvt_pk_bf16_f32 v29, v30, v31\n"
      "v_mfma_f32_16x16x32_bf16 v[32:35], a[32:35], v[204:207], v[32:35]\n"
      "s_waitcnt lgkmcnt(5)\n"
      "ds_read_b64_tr_b16 a[50:51], v3 offset:13128\n"
      "v_cvt_pk_bf16_f32 v62, v62, v66\n"
      "v_mfma_f32_16x16x32_bf16 v[32:35], a[0:3], v[112:115], v[32:35]\n"
      "ds_write_addtid_b32 v20 offset:62480\n"
      "v_add_f32_e32 v61, v52, v53\n"
      "v_mfma_f32_16x16x32_bf16 v[32:35], a[4:7], a[204:207], v[32:35]\n"
      "ds_write_addtid_b32 v21 offset:62736\n"
      "v_add_f32_e32 v65, v56, v57\n"
      "v_mfma_f32_16x16x32_bf16 v[32:35], a[8:11], v[220:223], v[32:35]\n"
      "s_waitcnt lgkmcnt(6)\n"
      "ds_write_addtid_b32 v24 offset:50184\n"
      "v_cvt_pk_bf16_f32 v61, v61, v65\n"
      "v_mfma_f32_16x16x32_bf16 v[32:35], a[12:15], a[208:211], v[32:35]\n"
      "s_waitcnt lgkmcnt(6)\n"
      "ds_write_addtid_b32 v25 offset:50440\n"
      "v_sub_f32_e32 v53, v52, v53\n"
      "v_mfma_f32_16x16x32_bf16 v[32:35], a[16:19], v[184:187], v[32:35]\n"
      "s_waitcnt lgkmcnt(6)\n"
      "ds_read2_b64 a[68:71], v10 offset0:1 offset1:65\n"
      "v_sub_f32_e32 v57, v56, v57\n"
      "v_mfma_f32_16x16x32_bf16 v[32:35], a[20:23], v[240:243], v[32:35]\n"
      "s_waitcnt lgkmcnt(5)\n"
      "ds_write_addtid_b32 v28 offset:37888\n"
      "v_cvt_pk_bf16_f32 v53, v53, v57\n"
      "v_mfma_f32_4x4x4_16b_bf16 v[36:39], a[72:73], a[44:45], 0\n"
      "ds_write_addtid_b32 v29 offset:38144\n"
      "v_mfma_f32_4x4x4_16b_bf16 v[40:43], a[72:73], a[46:47], 0\n"
      "v_add_f32_e32 v63, v54, v55\n"
      "v_mfma_f32_4x4x4_16b_bf16 v[44:47], a[74:75], a[48:49], v[36:39]\n"
      "v_add_f32_e32 v67, v58, v59\n"
      "v_mfma_f32_4x4x4_16b_bf16 v[48:51], a[74:75], a[50:51], v[40:43]\n"
      "v_cvt_pk_bf16_f32 v63, v63, v67\n"
      "v_mfma_f32_16x16x32_bf16 v[20:23], a[24:27], v[100:103], 0\n"
      "s_mov_b32 m0, s58\n"
      "ds_read_b64_tr_b16 a[52:53], v2 offset:13184\n"
      "v_sub_f32_e32 v55, v54, v55\n"
      "v_mfma_f32_16x16x32_bf16 v[20:23], a[28:31], a[196:199], v[20:23]\n"
      "buffer_load_dwordx4 v17, s[32:35], s53 offen offset:1024 lds\n"
      "v_sub_f32_e32 v59, v58, v59\n"
      "v_mfma_f32_16x16x32_bf16 v[20:23], a[32:35], v[208:211], v[20:23]\n"
      "s_mov_b32 m0, s45\n"
      "ds_read_b64_tr_b16 a[54:55], v2 offset:13192\n"
      "v_cvt_pk_bf16_f32 v55, v55, v59\n"
      "v_mfma_f32_16x16x32_bf16 v[20:23], a[0:3], a[200:203], v[20:23]\n"
      "s_mov_b32 m0, s18\n"
      "ds_read_b64_tr_b16 a[56:57], v3 offset:13184\n"
      "v_cvt_pk_bf16_f32 v32, v32, v33\n"
      "v_mfma_f32_16x16x32_bf16 v[20:23], a[4:7], v[172:175], v[20:23]\n"
      "ds_write_addtid_b32 v60 offset:0\n"
      "v_cvt_pk_bf16_f32 v33, v34, v35\n"
      "v_mfma_f32_16x16x32_bf16 v[20:23], a[8:11], v[228:231], v[20:23]\n"
      "ds_write_addtid_b32 v62 offset:2304\n"
      "v_cvt_pk_bf16_f32 v44, v44, v48\n"
      "v_mfma_f32_16x16x32_bf16 v[20:23], a[12:15], v[136:139], v[20:23]\n"
      "ds_write_addtid_b32 v61 offset:768\n"
      "v_add_f32_e32 v45, v36, v37\n"
      "v_mfma_f32_16x16x32_bf16 v[20:23], a[16:19], v[192:195], v[20:23]\n"
      "ds_write_addtid_b32 v53 offset:1536\n"
      "v_sub_f32_e32 v37, v36, v37\n"
      "v_mfma_f32_16x16x32_bf16 v[20:23], a[20:23], v[244:247], v[20:23]\n"
      "s_waitcnt lgkmcnt(6)\n"
      "ds_write_addtid_b32 v63 offset:3072\n"
      "v_add_f32_e32 v49, v40, v41\n"
      "v_mfma_f32_16x16x32_bf16 v[76:79], a[78:81], a[68:71], 0\n"
      "ds_write_addtid_b32 v55 offset:3840\n"
      "v_sub_f32_e32 v41, v40, v41\n"
      "v_mfma_f32_16x16x32_bf16 v[24:27], a[24:27], a[192:195], 0\n"
      "s_mov_b32 m0, s45\n"
      "ds_read_b64_tr_b16 a[58:59], v3 offset:13192\n"
      "v_add_f32_e32 v47, v38, v39\n"
      "v_mfma_f32_16x16x32_bf16 v[24:27], a[28:31], v[160:163], v[24:27]\n"
      "ds_write_addtid_b32 v32 offset:61456\n"
      "v_sub_f32_e32 v39, v38, v39\n"
      "v_mfma_f32_16x16x32_bf16 v[24:27], a[32:35], v[216:219], v[24:27]\n"
      "ds_write_addtid_b32 v33 offset:61712\n"
      "v_add_f32_e32 v51, v42, v43\n"
      "v_mfma_f32_16x16x32_bf16 v[24:27], a[0:3], v[124:127], v[24:27]\n"
      "s_mov_b32 m0, s18\n"
      "ds_read_b128 a[24:27], v7 offset:4608\n"
      "v_sub_f32_e32 v43, v42, v43\n"
      "v_mfma_f32_16x16x32_bf16 v[24:27], a[4:7], v[180:183], v[24:27]\n"
      "ds_read_b128 a[28:31], v7 offset:4864\n"
      "v_cvt_pk_bf16_f32 v46, v46, v50\n"
      "v_mfma_f32_16x16x32_bf16 v[24:27], a[8:11], v[232:235], v[24:27]\n"
      "ds_read_b128 a[32:35], v7 offset:5120\n"
      "v_cvt_pk_bf16_f32 v45, v45, v49\n"
      "v_mfma_f32_16x16x32_bf16 v[24:27], a[12:15], a[212:215], v[24:27]\n"
      "ds_read_b128 a[0:3], v6 offset:4608\n"
      "v_cvt_pk_bf16_f32 v37, v37, v41\n"
      "v_mfma_f32_16x16x32_bf16 v[24:27], a[16:19], v[196:199], v[24:27]\n"
      "ds_read_b128 a[4:7], v6 offset:4864\n"
      "v_cvt_pk_bf16_f32 v47, v47, v51\n"
      "v_mfma_f32_16x16x32_bf16 v[24:27], a[20:23], v[252:255], v[24:27]\n"
      "s_waitcnt lgkmcnt(7)\n"
      "ds_read_b128 a[8:11], v6 offset:5120\n"
      "v_cvt_pk_bf16_f32 v39, v39, v43\n"
      "v_mfma_f32_4x4x4_16b_bf16 v[52:55], a[72:73], a[52:53], 0\n"
      "ds_read_b128 a[12:15], v7 offset:0\n"
      "v_mfma_f32_4x4x4_16b_bf16 v[56:59], a[72:73], a[54:55], 0\n"
      "ds_read_b128 a[16:19], v7 offset:256\n"
      "v_mfma_f32_4x4x4_16b_bf16 v[60:63], a[74:75], a[56:57], v[52:55]\n"
      "ds_read_b128 a[20:23], v7 offset:512\n"
      "v_mfma_f32_4x4x4_16b_bf16 v[64:67], a[74:75], a[58:59], v[56:59]\n"
      "s_waitcnt lgkmcnt(7)\n"
      "ds_write_addtid_b32 v44 offset:256\n"
      "v_mfma_f32_16x16x32_bf16 v[28:31], a[24:27], a[156:159], 0\n"
      "ds_write_addtid_b32 v46 offset:2560\n"
      "v_cvt_pk_bf16_f32 v20, v20, v21\n"
      "v_mfma_f32_16x16x32_bf16 v[28:31], a[28:31], a[168:171], v[28:31]\n"
      "s_waitcnt lgkmcnt(7)\n"
      "ds_write_addtid_b32 v45 offset:1024\n"
      "v_cvt_pk_bf16_f32 v21, v22, v23\n"
      "v_mfma_f32_16x16x32_bf16 v[28:31], a[32:35], a[180:183], v[28:31]\n"
      "ds_write_addtid_b32 v37 offset:1792\n"
      "v_cvt_pk_bf16_f32 v24, v24, v25\n"
      "v_mfma_f32_16x16x32_bf16 v[28:31], a[0:3], a[84:87], v[28:31]\n"
      "s_waitcnt lgkmcnt(7)\n"
      "ds_write_addtid_b32 v47 offset:3328\n"
      "v_cvt_pk_bf16_f32 v25, v26, v27\n"
      "v_mfma_f32_16x16x32_bf16 v[28:31], a[4:7], a[96:99], v[28:31]\n"
      "ds_write_addtid_b32 v39 offset:4096\n"
      "v_cvt_pk_bf16_f32 v60, v60, v64\n"
      "v_mfma_f32_16x16x32_bf16 v[28:31], a[8:11], a[108:111], v[28:31]\n"
      "s_waitcnt lgkmcnt(7)\n"
      "ds_read2_b64 a[60:63], v10 offset0:2 offset1:66\n"
      "v_add_f32_e32 v61, v52, v53\n"
      "v_mfma_f32_16x16x32_bf16 v[28:31], a[12:15], a[120:123], v[28:31]\n"
      "s_mov_b32 m0, s45\n"
      "ds_read2_b64 a[64:67], v10 offset0:3 offset1:67\n"
      "v_sub_f32_e32 v53, v52, v53\n"
      "v_mfma_f32_16x16x32_bf16 v[28:31], a[16:19], a[132:135], v[28:31]\n"
      "s_waitcnt lgkmcnt(8)\n"
      "ds_write_addtid_b32 v20 offset:49160\n"
      "v_add_f32_e32 v65, v56, v57\n"
      "v_mfma_f32_16x16x32_bf16 v[28:31], a[20:23], a[144:147], v[28:31]\n"
      "ds_write_addtid_b32 v21 offset:49416\n"
      "v_sub_f32_e32 v57, v56, v57\n"
      "v_mfma_f32_16x16x32_bf16 v[20:23], a[24:27], a[160:163], 0\n"
      "s_mov_b32 m0, s58\n"
      "ds_read2_b64 a[68:71], v12 offset0:0 offset1:64\n"
      "v_add_f32_e32 v63, v54, v55\n"
      "v_mfma_f32_16x16x32_bf16 v[20:23], a[28:31], a[172:175], v[20:23]\n"
      "buffer_load_dwordx4 v17, s[32:35], s53 offen offset:2048 lds\n"
      "v_sub_f32_e32 v55, v54, v55\n"
      "v_mfma_f32_16x16x32_bf16 v[20:23], a[32:35], a[184:187], v[20:23]\n"
      "s_mov_b32 m0, s45\n"
      "v_add_f32_e32 v67, v58, v59\n"
      "v_sub_f32_e32 v59, v58, v59\n"
      "v_mfma_f32_16x16x32_bf16 v[20:23], a[0:3], a[88:91], v[20:23]\n"
      "ds_write_addtid_b32 v24 offset:36864\n"
      "v_cvt_pk_bf16_f32 v62, v62, v66\n"
      "v_mfma_f32_16x16x32_bf16 v[20:23], a[4:7], a[100:103], v[20:23]\n"
      "ds_write_addtid_b32 v25 offset:37120\n"
      "v_cvt_pk_bf16_f32 v61, v61, v65\n"
      "v_mfma_f32_16x16x32_bf16 v[20:23], a[8:11], a[112:115], v[20:23]\n"
      "s_mov_b32 m0, s18\n"
      "v_cvt_pk_bf16_f32 v53, v53, v57\n"
      "v_cvt_pk_bf16_f32 v63, v63, v67\n"
      "v_mfma_f32_16x16x32_bf16 v[20:23], a[12:15], a[124:127], v[20:23]\n"
      "ds_write_addtid_b32 v60 offset:512\n"
      "v_cvt_pk_bf16_f32 v55, v55, v59\n"
      "v_mfma_f32_16x16x32_bf16 v[20:23], a[16:19], a[136:139], v[20:23]\n"
      "ds_write_addtid_b32 v62 offset:2816\n"
      "v_cvt_pk_bf16_f32 v28, v28, v29\n"
      "v_mfma_f32_16x16x32_bf16 v[20:23], a[20:23], a[148:151], v[20:23]\n"
      "s_waitcnt lgkmcnt(4)\n"
      "ds_write_addtid_b32 v61 offset:1280\n"
      "v_cvt_pk_bf16_f32 v29, v30, v31\n"
      "v_mfma_f32_16x16x32_bf16 v[68:71], a[78:81], a[60:63], 0\n"
      "ds_write_addtid_b32 v53 offset:2048\n"
      "v_swap_b32 v6, v7\n"
      "v_mfma_f32_16x16x32_bf16 v[32:35], a[24:27], a[164:167], 0\n"
      "ds_write_addtid_b32 v63 offset:3584\n"
      "v_cvt_pk_bf16_f32 v20, v20, v21\n"
      "v_mfma_f32_16x16x32_bf16 v[32:35], a[28:31], a[176:179], v[32:35]\n"
      "ds_write_addtid_b32 v55 offset:4352\n"
      "v_cvt_pk_bf16_f32 v21, v22, v23\n"
      "v_mfma_f32_16x16x32_bf16 v[32:35], a[32:35], a[188:191], v[32:35]\n"
      "s_mov_b32 m0, s45\n"
      "ds_read2_b64 a[60:63], v12 offset0:1 offset1:65\n"
      "v_cvt_pk_bf16_f32 v80, v72, v73\n"
      "v_mfma_f32_16x16x32_bf16 v[32:35], a[0:3], a[92:95], v[32:35]\n"
      "ds_write_addtid_b32 v28 offset:60432\n"
      "v_cvt_pk_bf16_f32 v86, v74, v75\n"
      "v_mfma_f32_16x16x32_bf16 v[32:35], a[4:7], a[104:107], v[32:35]\n"
      "ds_write_addtid_b32 v29 offset:60688\n"
      "v_cvt_pk_bf16_f32 v81, v76, v77\n"
      "v_mfma_f32_16x16x32_bf16 v[32:35], a[8:11], a[116:119], v[32:35]\n"
      "ds_write_addtid_b32 v20 offset:48136\n"
      "v_cvt_pk_bf16_f32 v82, v68, v69\n"
      "v_mfma_f32_16x16x32_bf16 v[32:35], a[12:15], a[128:131], v[32:35]\n"
      "ds_write_addtid_b32 v21 offset:48392\n"
      "v_cvt_pk_bf16_f32 v87, v78, v79\n"
      "v_mfma_f32_16x16x32_bf16 v[32:35], a[16:19], a[140:143], v[32:35]\n"
      "v_cvt_pk_bf16_f32 v88, v70, v71\n"
      "v_add_u32_e64 v2, s91, v2\n"
      "v_mfma_f32_16x16x32_bf16 v[32:35], a[20:23], a[152:155], v[32:35]\n"
      "v_add_u32_e32 v3, s91, v3\n"
      "s_nop 0\n"
      "v_mfma_f32_16x16x32_bf16 v[72:75], a[78:81], a[64:67], 0\n"
      "s_sub_u32 s91, 0, s91\n"
      "s_nop 1\n"
      "v_mfma_f32_16x16x32_bf16 v[76:79], a[78:81], a[68:71], 0\n"
      "v_mfma_f32_16x16x32_bf16 v[20:23], a[12:15], a[84:87], 0\n"
      "s_add_u32 s51, s51, s12\n"
      "v_cvt_pk_bf16_f32 v32, v32, v33\n"
      "v_cvt_pk_bf16_f32 v33, v34, v35\n"
      "v_mfma_f32_16x16x32_bf16 v[20:23], a[16:19], a[96:99], v[20:23]\n"
      "s_sub_u32 s47, s51, s48\n"
      "v_cvt_pk_bf16_f32 v83, v72, v73\n"
      "v_cvt_pk_bf16_f32 v89, v74, v75\n"
      "v_mfma_f32_16x16x32_bf16 v[20:23], a[20:23], a[108:111], v[20:23]\n"
      "buffer_store_dwordx4 v[80:83], v14, s[36:39], s52 offen sc1\n"
      "v_cvt_pk_bf16_f32 v84, v76, v77\n"
      "v_mfma_f32_16x16x32_bf16 v[20:23], a[24:27], a[120:123], v[20:23]\n"
      "s_cmp_ge_u32 vcc_hi, 0x40000000\n"
      "ds_write_addtid_b32 v32 offset:35840\n"
      "v_cvt_pk_bf16_f32 v90, v78, v79\n"
      "v_mfma_f32_16x16x32_bf16 v[20:23], a[28:31], a[132:135], v[20:23]\n"
      "s_cselect_b32 s53, s47, s51\n"
      "ds_write_addtid_b32 v33 offset:36096\n"
      "v_mfma_f32_16x16x32_bf16 v[20:23], a[32:35], a[144:147], v[20:23]\n"
      "buffer_store_dwordx4 v[86:89], v14, s[36:39], s52 offen offset:1536 sc1\n"
      "v_mfma_f32_16x16x32_bf16 v[24:27], a[12:15], a[88:91], 0\n"
      "s_lshl_b64 vcc, vcc, 1\n"
      "v_mfma_f32_16x16x32_bf16 v[24:27], a[16:19], a[100:103], v[24:27]\n"
      "v_mfma_f32_16x16x32_bf16 v[24:27], a[20:23], a[112:115], v[24:27]\n"
      "global_load_dword v8, v9, s[54:55]\n"
      "v_mfma_f32_16x16x32_bf16 v[24:27], a[24:27], a[124:127], v[24:27]\n"
      "v_mfma_f32_16x16x32_bf16 v[24:27], a[28:31], a[136:139], v[24:27]\n"
      "v_mfma_f32_16x16x32_bf16 v[24:27], a[32:35], a[148:151], v[24:27]\n"
      "s_waitcnt vmcnt(13) lgkmcnt(0)\n"
      "s_barrier\n"
      "v_mfma_f32_16x16x32_bf16 v[68:71], a[78:81], a[60:63], 0\n"
      "s_add_u32 m0, s58, 13056\n"
      "ds_read_b128 a[0:3], v7 offset:0\n"
      "s_nop 0\n"
      "v_mfma_f32_16x16x32_bf16 v[28:31], a[12:15], a[92:95], 0\n"
      "ds_read_b128 a[4:7], v7 offset:256\n"
      "s_nop 0\n"
      "v_mfma_f32_16x16x32_bf16 v[28:31], a[16:19], a[104:107], v[28:31]\n"
      "ds_read_b128 a[8:11], v7 offset:512\n"
      "v_cvt_pk_bf16_f32 v85, v68, v69\n"
      "v_mfma_f32_16x16x32_bf16 v[28:31], a[20:23], a[116:119], v[28:31]\n"
      "ds_read_b64_tr_b16 a[36:37], v2 offset:0\n"
      "v_cvt_pk_bf16_f32 v91, v70, v71\n"
      "v_mfma_f32_16x16x32_bf16 v[28:31], a[24:27], a[128:131], v[28:31]\n"
      "ds_read_b64_tr_b16 a[38:39], v2 offset:8\n"
      "v_mfma_f32_16x16x32_bf16 v[28:31], a[28:31], a[140:143], v[28:31]\n"
      "ds_read_b64_tr_b16 a[40:41], v3 offset:0\n"
      "v_mfma_f32_16x16x32_bf16 v[28:31], a[32:35], a[152:155], v[28:31]\n"
      "s_waitcnt lgkmcnt(5)\n"
      "ds_read_b64_tr_b16 a[42:43], v3 offset:8\n"
      "v_mfma_f32_16x16x32_bf16 v[20:23], a[0:3], a[156:159], v[20:23]\n"
      "s_waitcnt lgkmcnt(5)\n"
      "ds_read2_b64 a[64:67], v11 offset0:0 offset1:64\n"
      "v_mfma_f32_16x16x32_bf16 v[20:23], a[4:7], a[168:171], v[20:23]\n"
      "s_waitcnt lgkmcnt(5)\n"
      "ds_read_b128 a[12:15], v6 offset:768\n"
      "v_mfma_f32_16x16x32_bf16 v[20:23], a[8:11], a[180:183], v[20:23]\n"
      "buffer_store_dwordx2 v[84:85], v15, s[36:39], s52 offen sc1\n"
      "s_nop 0\n"
      "v_mfma_f32_16x16x32_bf16 v[24:27], a[0:3], a[160:163], v[24:27]\n"
      "ds_read_b128 a[16:19], v6 offset:1024\n"
      "s_nop 0\n"
      "v_mfma_f32_16x16x32_bf16 v[24:27], a[4:7], a[172:175], v[24:27]\n"
      "ds_read_b128 a[20:23], v6 offset:1280\n"
      "v_mfma_f32_16x16x32_bf16 v[24:27], a[8:11], a[184:187], v[24:27]\n"
      "s_waitcnt lgkmcnt(4)\n"
      "ds_read_b128 a[24:27], v6 offset:5376\n"
      "v_cvt_pk_bf16_f32 v20, v20, v21\n"
      "v_mfma_f32_4x4x4_16b_bf16 v[36:39], a[72:73], a[36:37], 0\n"
      "ds_read_b128 a[28:31], v6 offset:5632\n"
      "v_mfma_f32_4x4x4_16b_bf16 v[40:43], a[72:73], a[38:39], 0\n"
      "ds_read_b128 a[32:35], v6 offset:5888\n"
      "v_mfma_f32_4x4x4_16b_bf16 v[44:47], a[74:75], a[40:41], v[36:39]\n"
      "ds_read_b64_tr_b16 a[44:45], v2 offset:64\n"
      "v_mfma_f32_4x4x4_16b_bf16 v[48:51], a[74:75], a[42:43], v[40:43]\n"
      "ds_read_b64_tr_b16 a[46:47], v2 offset:72\n"
      "v_mfma_f32_16x16x32_bf16 v[28:31], a[0:3], a[164:167], v[28:31]\n"
      "buffer_store_dwordx2 v[90:91], v15, s[36:39], s52 offen offset:1536 sc1\n"
      "v_cvt_pk_bf16_f32 v21, v22, v23\n"
      "v_mfma_f32_16x16x32_bf16 v[28:31], a[4:7], a[176:179], v[28:31]\n"
      "s_mov_b32 s52, s13\n"
      "ds_read_b128 a[0:3], v7 offset:768\n"
      "v_cvt_pk_bf16_f32 v24, v24, v25\n"
      "v_mfma_f32_16x16x32_bf16 v[28:31], a[8:11], a[188:191], v[28:31]\n"
      "s_waitcnt lgkmcnt(6)\n"
      "ds_read_b128 a[4:7], v7 offset:1024\n"
      "v_cvt_pk_bf16_f32 v25, v26, v27\n"
      "v_mfma_f32_16x16x32_bf16 v[72:75], a[78:81], a[64:67], 0\n"
      "buffer_load_dwordx4 v16, s[32:35], s53 offen lds\n"
      "v_cvt_pk_bf16_f32 v44, v44, v48\n"
      "v_mfma_f32_16x16x32_bf16 v[32:35], a[12:15], v[92:95], 0\n"
      "s_mov_b32 m0, s45\n"
      "ds_read_b128 a[8:11], v7 offset:1280\n"
      "v_cvt_pk_bf16_f32 v28, v28, v29\n"
      "v_mfma_f32_16x16x32_bf16 v[32:35], a[16:19], v[148:151], v[32:35]\n"
      "s_add_u32 s13, s13, s12\n"
      "ds_read_b64_tr_b16 a[48:49], v3 offset:64\n"
      "v_cvt_pk_bf16_f32 v29, v30, v31\n"
      "v_mfma_f32_16x16x32_bf16 v[32:35], a[20:23], v[204:207], v[32:35]\n"
      "s_waitcnt lgkmcnt(5)\n"
      "ds_read_b64_tr_b16 a[50:51], v3 offset:72\n"
      "v_cvt_pk_bf16_f32 v46, v46, v50\n"
      "v_mfma_f32_16x16x32_bf16 v[32:35], a[24:27], v[112:115], v[32:35]\n"
      "ds_write_addtid_b32 v20 offset:57360\n"
      "v_add_f32_e32 v45, v36, v37\n"
      "v_mfma_f32_16x16x32_bf16 v[32:35], a[28:31], a[204:207], v[32:35]\n"
      "ds_write_addtid_b32 v21 offset:57616\n"
      "v_add_f32_e32 v49, v40, v41\n"
      "v_mfma_f32_16x16x32_bf16 v[32:35], a[32:35], v[220:223], v[32:35]\n"
      "s_waitcnt lgkmcnt(6)\n"
      "ds_write_addtid_b32 v24 offset:45064\n"
      "v_cvt_pk_bf16_f32 v45, v45, v49\n"
      "v_mfma_f32_16x16x32_bf16 v[32:35], a[0:3], a[208:211], v[32:35]\n"
      "s_waitcnt lgkmcnt(6)\n"
      "ds_write_addtid_b32 v25 offset:45320\n"
      "v_sub_f32_e32 v37, v36, v37\n"
      "v_mfma_f32_16x16x32_bf16 v[32:35], a[4:7], v[184:187], v[32:35]\n"
      "s_waitcnt lgkmcnt(6)\n"
      "ds_read2_b64 a[68:71], v11 offset0:1 offset1:65\n"
      "v_sub_f32_e32 v41, v40, v41\n"
      "v_mfma_f32_16x16x32_bf16 v[32:35], a[8:11], v[240:243], v[32:35]\n"
      "s_waitcnt lgkmcnt(5)\n"
      "ds_write_addtid_b32 v28 offset:32768\n"
      "v_cvt_pk_bf16_f32 v37, v37, v41\n"
      "v_mfma_f32_4x4x4_16b_bf16 v[52:55], a[72:73], a[44:45], 0\n"
      "ds_write_addtid_b32 v29 offset:33024\n"
      "v_mfma_f32_4x4x4_16b_bf16 v[56:59], a[72:73], a[46:47], 0\n"
      "v_add_f32_e32 v47, v38, v39\n"
      "v_mfma_f32_4x4x4_16b_bf16 v[60:63], a[74:75], a[48:49], v[52:55]\n"
      "v_add_f32_e32 v51, v42, v43\n"
      "v_mfma_f32_4x4x4_16b_bf16 v[64:67], a[74:75], a[50:51], v[56:59]\n"
      "v_cvt_pk_bf16_f32 v47, v47, v51\n"
      "v_mfma_f32_16x16x32_bf16 v[20:23], a[12:15], v[100:103], 0\n"
      "s_add_u32 m0, s58, 13056\n"
      "ds_read_b64_tr_b16 a[52:53], v2 offset:128\n"
      "v_sub_f32_e32 v39, v38, v39\n"
      "v_mfma_f32_16x16x32_bf16 v[20:23], a[16:19], a[196:199], v[20:23]\n"
      "buffer_load_dwordx4 v17, s[32:35], s53 offen offset:1024 lds\n"
      "v_sub_f32_e32 v43, v42, v43\n"
      "v_mfma_f32_16x16x32_bf16 v[20:23], a[20:23], v[208:211], v[20:23]\n"
      "s_mov_b32 m0, s45\n"
      "ds_read_b64_tr_b16 a[54:55], v2 offset:136\n"
      "v_cvt_pk_bf16_f32 v39, v39, v43\n"
      "v_mfma_f32_16x16x32_bf16 v[20:23], a[24:27], a[200:203], v[20:23]\n"
      "s_mov_b32 m0, s18\n"
      "ds_read_b64_tr_b16 a[56:57], v3 offset:128\n"
      "v_cvt_pk_bf16_f32 v32, v32, v33\n"
      "v_mfma_f32_16x16x32_bf16 v[20:23], a[28:31], v[172:175], v[20:23]\n"
      "ds_write_addtid_b32 v44 offset:4608\n"
      "v_cvt_pk_bf16_f32 v33, v34, v35\n"
      "v_mfma_f32_16x16x32_bf16 v[20:23], a[32:35], v[228:231], v[20:23]\n"
      "ds_write_addtid_b32 v46 offset:6912\n"
      "v_cvt_pk_bf16_f32 v60, v60, v64\n"
      "v_mfma_f32_16x16x32_bf16 v[20:23], a[0:3], v[136:139], v[20:23]\n"
      "ds_write_addtid_b32 v45 offset:5376\n"
      "v_add_f32_e32 v61, v52, v53\n"
      "v_mfma_f32_16x16x32_bf16 v[20:23], a[4:7], v[192:195], v[20:23]\n"
      "ds_write_addtid_b32 v37 offset:6144\n"
      "v_sub_f32_e32 v53, v52, v53\n"
      "v_mfma_f32_16x16x32_bf16 v[20:23], a[8:11], v[244:247], v[20:23]\n"
      "s_waitcnt lgkmcnt(6)\n"
      "ds_write_addtid_b32 v47 offset:7680\n"
      "v_add_f32_e32 v65, v56, v57\n"
      "v_mfma_f32_16x16x32_bf16 v[76:79], a[78:81], a[68:71], 0\n"
      "ds_write_addtid_b32 v39 offset:8448\n"
      "v_sub_f32_e32 v57, v56, v57\n"
      "v_mfma_f32_16x16x32_bf16 v[24:27], a[12:15], a[192:195], 0\n"
      "s_mov_b32 m0, s45\n"
      "ds_read_b64_tr_b16 a[58:59], v3 offset:136\n"
      "v_add_f32_e32 v63, v54, v55\n"
      "v_mfma_f32_16x16x32_bf16 v[24:27], a[16:19], v[160:163], v[24:27]\n"
      "ds_write_addtid_b32 v32 offset:58384\n"
      "v_sub_f32_e32 v55, v54, v55\n"
      "v_mfma_f32_16x16x32_bf16 v[24:27], a[20:23], v[216:219], v[24:27]\n"
      "ds_write_addtid_b32 v33 offset:58640\n"
      "v_add_f32_e32 v67, v58, v59\n"
      "v_mfma_f32_16x16x32_bf16 v[24:27], a[24:27], v[124:127], v[24:27]\n"
      "s_mov_b32 m0, s18\n"
      "ds_read_b128 a[12:15], v6 offset:1536\n"
      "v_sub_f32_e32 v59, v58, v59\n"
      "v_mfma_f32_16x16x32_bf16 v[24:27], a[28:31], v[180:183], v[24:27]\n"
      "ds_read_b128 a[16:19], v6 offset:1792\n"
      "v_cvt_pk_bf16_f32 v62, v62, v66\n"
      "v_mfma_f32_16x16x32_bf16 v[24:27], a[32:35], v[232:235], v[24:27]\n"
      "ds_read_b128 a[20:23], v6 offset:2048\n"
      "v_cvt_pk_bf16_f32 v61, v61, v65\n"
      "v_mfma_f32_16x16x32_bf16 v[24:27], a[0:3], a[212:215], v[24:27]\n"
      "ds_read_b128 a[24:27], v6 offset:6144\n"
      "v_cvt_pk_bf16_f32 v53, v53, v57\n"
      "v_mfma_f32_16x16x32_bf16 v[24:27], a[4:7], v[196:199], v[24:27]\n"
      "ds_read_b128 a[28:31], v6 offset:6400\n"
      "v_cvt_pk_bf16_f32 v63, v63, v67\n"
      "v_mfma_f32_16x16x32_bf16 v[24:27], a[8:11], v[252:255], v[24:27]\n"
      "s_waitcnt lgkmcnt(7)\n"
      "ds_read_b128 a[32:35], v6 offset:6656\n"
      "v_cvt_pk_bf16_f32 v55, v55, v59\n"
      "v_mfma_f32_4x4x4_16b_bf16 v[36:39], a[72:73], a[52:53], 0\n"
      "ds_read_b128 a[0:3], v7 offset:1536\n"
      "v_mfma_f32_4x4x4_16b_bf16 v[40:43], a[72:73], a[54:55], 0\n"
      "ds_read_b128 a[4:7], v7 offset:1792\n"
      "v_mfma_f32_4x4x4_16b_bf16 v[44:47], a[74:75], a[56:57], v[36:39]\n"
      "ds_read_b128 a[8:11], v7 offset:2048\n"
      "v_mfma_f32_4x4x4_16b_bf16 v[48:51], a[74:75], a[58:59], v[40:43]\n"
      "s_waitcnt lgkmcnt(7)\n"
      "ds_write_addtid_b32 v60 offset:4864\n"
      "v_mfma_f32_16x16x32_bf16 v[28:31], a[12:15], v[96:99], 0\n"
      "ds_write_addtid_b32 v62 offset:7168\n"
      "v_cvt_pk_bf16_f32 v20, v20, v21\n"
      "v_mfma_f32_16x16x32_bf16 v[28:31], a[16:19], v[152:155], v[28:31]\n"
      "s_waitcnt lgkmcnt(7)\n"
      "ds_write_addtid_b32 v61 offset:5632\n"
      "v_cvt_pk_bf16_f32 v21, v22, v23\n"
      "v_mfma_f32_16x16x32_bf16 v[28:31], a[20:23], a[216:219], v[28:31]\n"
      "ds_write_addtid_b32 v53 offset:6400\n"
      "v_cvt_pk_bf16_f32 v24, v24, v25\n"
      "v_mfma_f32_16x16x32_bf16 v[28:31], a[24:27], v[116:119], v[28:31]\n"
      "s_waitcnt lgkmcnt(7)\n"
      "ds_write_addtid_b32 v63 offset:7936\n"
      "v_cvt_pk_bf16_f32 v25, v26, v27\n"
      "v_mfma_f32_16x16x32_bf16 v[28:31], a[28:31], v[168:171], v[28:31]\n"
      "ds_write_addtid_b32 v55 offset:8704\n"
      "v_cvt_pk_bf16_f32 v44, v44, v48\n"
      "v_mfma_f32_16x16x32_bf16 v[28:31], a[32:35], v[224:227], v[28:31]\n"
      "s_waitcnt lgkmcnt(7)\n"
      "ds_read2_b64 a[60:63], v11 offset0:2 offset1:66\n"
      "v_add_f32_e32 v45, v36, v37\n"
      "v_mfma_f32_16x16x32_bf16 v[28:31], a[0:3], v[132:135], v[28:31]\n"
      "s_mov_b32 m0, s45\n"
      "ds_read2_b64 a[64:67], v11 offset0:3 offset1:67\n"
      "v_sub_f32_e32 v37, v36, v37\n"
      "v_mfma_f32_16x16x32_bf16 v[28:31], a[4:7], v[188:191], v[28:31]\n"
      "s_waitcnt lgkmcnt(8)\n"
      "ds_write_addtid_b32 v20 offset:46088\n"
      "v_add_f32_e32 v49, v40, v41\n"
      "v_mfma_f32_16x16x32_bf16 v[28:31], a[8:11], a[236:239], v[28:31]\n"
      "ds_write_addtid_b32 v21 offset:46344\n"
      "v_sub_f32_e32 v41, v40, v41\n"
      "v_mfma_f32_16x16x32_bf16 v[20:23], a[12:15], v[104:107], 0\n"
      "s_add_u32 m0, s58, 13056\n"
      "ds_read2_b64 a[68:71], v13 offset0:0 offset1:64\n"
      "v_add_f32_e32 v47, v38, v39\n"
      "v_mfma_f32_16x16x32_bf16 v[20:23], a[16:19], v[156:159], v[20:23]\n"
      "buffer_load_dwordx4 v17, s[32:35], s53 offen offset:2048 lds\n"
      "v_sub_f32_e32 v39, v38, v39\n"
      "v_mfma_f32_16x16x32_bf16 v[20:23], a[20:23], v[212:215], v[20:23]\n"
      "s_mov_b32 m0, s45\n"
      "v_add_f32_e32 v51, v42, v43\n"
      "v_sub_f32_e32 v43, v42, v43\n"
      "v_mfma_f32_16x16x32_bf16 v[20:23], a[24:27], v[120:123], v[20:23]\n"
      "ds_write_addtid_b32 v24 offset:33792\n"
      "v_cvt_pk_bf16_f32 v46, v46, v50\n"
      "v_mfma_f32_16x16x32_bf16 v[20:23], a[28:31], v[176:179], v[20:23]\n"
      "ds_write_addtid_b32 v25 offset:34048\n"
      "v_cvt_pk_bf16_f32 v45, v45, v49\n"
      "v_mfma_f32_16x16x32_bf16 v[20:23], a[32:35], a[228:231], v[20:23]\n"
      "s_mov_b32 m0, s18\n"
      "v_cvt_pk_bf16_f32 v37, v37, v41\n"
      "v_cvt_pk_bf16_f32 v47, v47, v51\n"
      "v_mfma_f32_16x16x32_bf16 v[20:23], a[0:3], v[140:143], v[20:23]\n"
      "s_sub_u32 s18, s19, s18\n"
      "ds_write_addtid_b32 v44 offset:5120\n"
      "v_cvt_pk_bf16_f32 v39, v39, v43\n"
      "v_mfma_f32_16x16x32_bf16 v[20:23], a[4:7], a[232:235], v[20:23]\n"
      "ds_write_addtid_b32 v46 offset:7424\n"
      "v_cvt_pk_bf16_f32 v28, v28, v29\n"
      "v_mfma_f32_16x16x32_bf16 v[20:23], a[8:11], v[248:251], v[20:23]\n"
      "s_waitcnt lgkmcnt(4)\n"
      "ds_write_addtid_b32 v45 offset:5888\n"
      "v_cvt_pk_bf16_f32 v29, v30, v31\n"
      "v_mfma_f32_16x16x32_bf16 v[68:71], a[78:81], a[60:63], 0\n"
      "ds_write_addtid_b32 v37 offset:6656\n"
      "v_cvt_pk_bf16_f32 v80, v72, v73\n"
      "v_mfma_f32_16x16x32_bf16 v[32:35], a[12:15], v[108:111], 0\n"
      "ds_write_addtid_b32 v47 offset:8192\n"
      "v_cvt_pk_bf16_f32 v20, v20, v21\n"
      "v_mfma_f32_16x16x32_bf16 v[32:35], a[16:19], v[164:167], v[32:35]\n"
      "ds_write_addtid_b32 v39 offset:8960\n"
      "v_cvt_pk_bf16_f32 v21, v22, v23\n"
      "v_mfma_f32_16x16x32_bf16 v[32:35], a[20:23], a[220:223], v[32:35]\n"
      "s_mov_b32 m0, s45\n"
      "ds_read2_b64 a[60:63], v13 offset0:1 offset1:65\n"
      "v_cvt_pk_bf16_f32 v86, v74, v75\n"
      "v_mfma_f32_16x16x32_bf16 v[32:35], a[24:27], v[128:131], v[32:35]\n"
      "ds_write_addtid_b32 v28 offset:59408\n"
      "v_cvt_pk_bf16_f32 v81, v76, v77\n"
      "v_mfma_f32_16x16x32_bf16 v[32:35], a[28:31], a[224:227], v[32:35]\n"
      "ds_write_addtid_b32 v29 offset:59664\n"
      "v_cvt_pk_bf16_f32 v87, v78, v79\n"
      "v_mfma_f32_16x16x32_bf16 v[32:35], a[32:35], v[236:239], v[32:35]\n"
      "ds_write_addtid_b32 v20 offset:47112\n"
      "v_cvt_pk_bf16_f32 v82, v68, v69\n"
      "v_mfma_f32_16x16x32_bf16 v[32:35], a[0:3], v[144:147], v[32:35]\n"
      "ds_write_addtid_b32 v21 offset:47368\n"
      "v_cvt_pk_bf16_f32 v88, v70, v71\n"
      "v_mfma_f32_16x16x32_bf16 v[32:35], a[4:7], v[200:203], v[32:35]\n"
      "v_mfma_f32_16x16x32_bf16 v[32:35], a[8:11], a[240:243], v[32:35]\n"
      "s_nop 1\n"
      "v_mfma_f32_16x16x32_bf16 v[72:75], a[78:81], a[64:67], 0\n"
      "s_nop 1\n"
      "v_mfma_f32_16x16x32_bf16 v[76:79], a[78:81], a[68:71], 0\n"
      "s_nop 0\n"
      "s_cbranch_vccnz Lconv_loop1\n"
      "Lconv_t1:\n"
      "v_mfma_f32_16x16x32_bf16 v[20:23], a[24:27], v[96:99], 0\n"
      "v_cvt_pk_bf16_f32 v32, v32, v33\n"
      "v_cvt_pk_bf16_f32 v33, v34, v35\n"
      "v_mfma_f32_16x16x32_bf16 v[20:23], a[28:31], v[152:155], v[20:23]\n"
      "ds_write_addtid_b32 v32 offset:34816\n"
      "v_cvt_pk_bf16_f32 v83, v72, v73\n"
      "v_mfma_f32_16x16x32_bf16 v[20:23], a[32:35], a[216:219], v[20:23]\n"
      "ds_write_addtid_b32 v33 offset:35072\n"
      "v_cvt_pk_bf16_f32 v89, v74, v75\n"
      "v_mfma_f32_16x16x32_bf16 v[20:23], a[0:3], v[116:119], v[20:23]\n"
      "buffer_store_dwordx4 v[80:83], v14, s[36:39], s52 offen sc1\n"
      "v_cvt_pk_bf16_f32 v84, v76, v77\n"
      "v_mfma_f32_16x16x32_bf16 v[20:23], a[4:7], v[168:171], v[20:23]\n"
      "v_cvt_pk_bf16_f32 v90, v78, v79\n"
      "v_mfma_f32_16x16x32_bf16 v[20:23], a[8:11], v[224:227], v[20:23]\n"
      "v_mfma_f32_16x16x32_bf16 v[24:27], a[24:27], v[104:107], 0\n"
      "buffer_store_dwordx4 v[86:89], v14, s[36:39], s52 offen offset:1536 sc1\n"
      "v_mfma_f32_16x16x32_bf16 v[24:27], a[28:31], v[156:159], v[24:27]\n"
      "v_mfma_f32_16x16x32_bf16 v[24:27], a[32:35], v[212:215], v[24:27]\n"
      "v_mfma_f32_16x16x32_bf16 v[24:27], a[0:3], v[120:123], v[24:27]\n"
      "v_mfma_f32_16x16x32_bf16 v[24:27], a[4:7], v[176:179], v[24:27]\n"
      "v_mfma_f32_16x16x32_bf16 v[24:27], a[8:11], a[228:231], v[24:27]\n"
      "s_waitcnt lgkmcnt(0)\n"
      "s_barrier\n"
      "v_mfma_f32_16x16x32_bf16 v[68:71], a[78:81], a[60:63], 0\n"
      "ds_read2_b64 a[64:67], v10 offset0:0 offset1:64\n"
      "s_nop 0\n"
      "v_mfma_f32_16x16x32_bf16 v[28:31], a[24:27], v[108:111], 0\n"
      "s_waitcnt lgkmcnt(0)\n"
      "ds_read_b128 a[12:15], v7 offset:6144\n"
      "s_nop 0\n"
      "v_mfma_f32_16x16x32_bf16 v[72:75], a[78:81], a[64:67], 0\n"
      "ds_read2_b64 a[68:71], v10 offset0:1 offset1:65\n"
      "v_cvt_pk_bf16_f32 v85, v68, v69\n"
      "v_mfma_f32_16x16x32_bf16 v[28:31], a[28:31], v[164:167], v[28:31]\n"
      "ds_read_b128 a[16:19], v7 offset:6400\n"
      "v_cvt_pk_bf16_f32 v91, v70, v71\n"
      "v_mfma_f32_16x16x32_bf16 v[28:31], a[32:35], a[220:223], v[28:31]\n"
      "ds_read_b128 a[20:23], v7 offset:6656\n"
      "v_mfma_f32_16x16x32_bf16 v[28:31], a[0:3], v[128:131], v[28:31]\n"
      "ds_read2_b64 a[60:63], v10 offset0:2 offset1:66\n"
      "v_cvt_pk_bf16_f32 v80, v72, v73\n"
      "v_mfma_f32_16x16x32_bf16 v[28:31], a[4:7], a[224:227], v[28:31]\n"
      "ds_read2_b64 a[64:67], v10 offset0:3 offset1:67\n"
      "v_cvt_pk_bf16_f32 v86, v74, v75\n"
      "v_mfma_f32_16x16x32_bf16 v[28:31], a[8:11], v[236:239], v[28:31]\n"
      "s_waitcnt lgkmcnt(5)\n"
      "ds_read_b128 a[24:27], v6 offset:5376\n"
      "v_mfma_f32_16x16x32_bf16 v[20:23], a[12:15], v[132:135], v[20:23]\n"
      "s_waitcnt lgkmcnt(5)\n"
      "ds_read_b128 a[28:31], v6 offset:5632\n"
      "v_mfma_f32_16x16x32_bf16 v[76:79], a[78:81], a[68:71], 0\n"
      "s_waitcnt lgkmcnt(5)\n"
      "ds_read2_b64 a[68:71], v12 offset0:0 offset1:64\n"
      "v_mfma_f32_16x16x32_bf16 v[20:23], a[16:19], v[188:191], v[20:23]\n"
      "s_waitcnt lgkmcnt(5)\n"
      "ds_read_b128 a[32:35], v6 offset:5888\n"
      "v_mfma_f32_16x16x32_bf16 v[20:23], a[20:23], a[236:239], v[20:23]\n"
      "ds_read_b128 a[0:3], v7 offset:768\n"
      "s_nop 0\n"
      "v_mfma_f32_16x16x32_bf16 v[24:27], a[12:15], v[140:143], v[24:27]\n"
      "s_waitcnt lgkmcnt(6)\n"
      "ds_read_b128 a[4:7], v7 offset:1024\n"
      "v_cvt_pk_bf16_f32 v81, v76, v77\n"
      "v_mfma_f32_16x16x32_bf16 v[68:71], a[78:81], a[60:63], 0\n"
      "ds_read2_b64 a[60:63], v12 offset0:1 offset1:65\n"
      "v_cvt_pk_bf16_f32 v20, v20, v21\n"
      "v_mfma_f32_16x16x32_bf16 v[24:27], a[16:19], a[232:235], v[24:27]\n"
      "buffer_store_dwordx2 v[84:85], v15, s[36:39], s52 offen sc1\n"
      "v_cvt_pk_bf16_f32 v21, v22, v23\n"
      "v_mfma_f32_16x16x32_bf16 v[24:27], a[20:23], v[248:251], v[24:27]\n"
      "ds_read_b128 a[8:11], v7 offset:1280\n"
      "v_cvt_pk_bf16_f32 v87, v78, v79\n"
      "v_mfma_f32_16x16x32_bf16 v[28:31], a[12:15], v[144:147], v[28:31]\n"
      "s_waitcnt lgkmcnt(6)\n"
      "ds_read_b128 a[12:15], v7 offset:5376\n"
      "v_cvt_pk_bf16_f32 v82, v68, v69\n"
      "v_mfma_f32_16x16x32_bf16 v[72:75], a[78:81], a[64:67], 0\n"
      "buffer_store_dwordx2 v[90:91], v15, s[36:39], s52 offen offset:1536 sc1\n"
      "v_cvt_pk_bf16_f32 v24, v24, v25\n"
      "v_mfma_f32_16x16x32_bf16 v[28:31], a[16:19], v[200:203], v[28:31]\n"
      "s_mov_b32 s52, s13\n"
      "ds_read_b128 a[16:19], v7 offset:5632\n"
      "v_cvt_pk_bf16_f32 v25, v26, v27\n"
      "v_mfma_f32_16x16x32_bf16 v[28:31], a[20:23], a[240:243], v[28:31]\n"
      "s_add_u32 s13, s13, s12\n"
      "ds_read_b128 a[20:23], v7 offset:5888\n"
      "v_cvt_pk_bf16_f32 v83, v72, v73\n"
      "v_mfma_f32_16x16x32_bf16 v[32:35], a[24:27], v[92:95], 0\n"
      "s_waitcnt lgkmcnt(5)\n"
      "ds_write_addtid_b32 v20 offset:62480\n"
      "v_cvt_pk_bf16_f32 v88, v70, v71\n"
      "v_mfma_f32_16x16x32_bf16 v[76:79], a[78:81], a[68:71], 0\n"
      "s_waitcnt lgkmcnt(5)\n"
      "ds_write_addtid_b32 v21 offset:62736\n"
      "v_cvt_pk_bf16_f32 v89, v74, v75\n"
      "v_mfma_f32_16x16x32_bf16 v[68:71], a[78:81], a[60:63], 0\n"
      "ds_write_addtid_b32 v24 offset:50184\n"
      "v_cvt_pk_bf16_f32 v28, v28, v29\n"
      "v_mfma_f32_16x16x32_bf16 v[32:35], a[28:31], v[148:151], v[32:35]\n"
      "buffer_store_dwordx4 v[80:83], v14, s[36:39], s52 offen sc1\n"
      "v_cvt_pk_bf16_f32 v29, v30, v31\n"
      "v_mfma_f32_16x16x32_bf16 v[32:35], a[32:35], v[204:207], v[32:35]\n"
      "ds_write_addtid_b32 v25 offset:50440\n"
      "v_cvt_pk_bf16_f32 v84, v76, v77\n"
      "v_mfma_f32_16x16x32_bf16 v[32:35], a[0:3], v[112:115], v[32:35]\n"
      "ds_write_addtid_b32 v28 offset:37888\n"
      "v_cvt_pk_bf16_f32 v85, v68, v69\n"
      "v_mfma_f32_16x16x32_bf16 v[32:35], a[4:7], a[204:207], v[32:35]\n"
      "s_waitcnt lgkmcnt(6)\n"
      "ds_write_addtid_b32 v29 offset:38144\n"
      "v_cvt_pk_bf16_f32 v90, v78, v79\n"
      "v_mfma_f32_16x16x32_bf16 v[32:35], a[8:11], v[220:223], v[32:35]\n"
      "buffer_store_dwordx4 v[86:89], v14, s[36:39], s52 offen offset:1536 sc1\n"
      "v_cvt_pk_bf16_f32 v91, v70, v71\n"
      "v_mfma_f32_16x16x32_bf16 v[32:35], a[12:15], a[208:211], v[32:35]\n"
      "v_mfma_f32_16x16x32_bf16 v[32:35], a[16:19], v[184:187], v[32:35]\n"
      "s_waitcnt lgkmcnt(6)\n"
      "v_mfma_f32_16x16x32_bf16 v[32:35], a[20:23], v[240:243], v[32:35]\n"
      "buffer_store_dwordx2 v[84:85], v15, s[36:39], s52 offen sc1\n"
      "s_nop 0\n"
      "v_mfma_f32_16x16x32_bf16 v[20:23], a[24:27], v[100:103], 0\n"
      "s_nop 1\n"
      "v_mfma_f32_16x16x32_bf16 v[20:23], a[28:31], a[196:199], v[20:23]\n"
      "s_nop 0\n"
      "v_mfma_f32_16x16x32_bf16 v[20:23], a[32:35], v[208:211], v[20:23]\n"
      "buffer_store_dwordx2 v[90:91], v15, s[36:39], s52 offen offset:1536 sc1\n"
      "v_cvt_pk_bf16_f32 v32, v32, v33\n"
      "v_mfma_f32_16x16x32_bf16 v[20:23], a[0:3], a[200:203], v[20:23]\n"
      "ds_write_addtid_b32 v32 offset:61456\n"
      "v_cvt_pk_bf16_f32 v33, v34, v35\n"
      "v_mfma_f32_16x16x32_bf16 v[20:23], a[4:7], v[172:175], v[20:23]\n"
      "ds_write_addtid_b32 v33 offset:61712\n"
      "v_mfma_f32_16x16x32_bf16 v[20:23], a[8:11], v[228:231], v[20:23]\n"
      "v_mfma_f32_16x16x32_bf16 v[20:23], a[12:15], v[136:139], v[20:23]\n"
      "v_mfma_f32_16x16x32_bf16 v[20:23], a[16:19], v[192:195], v[20:23]\n"
      "v_mfma_f32_16x16x32_bf16 v[20:23], a[20:23], v[244:247], v[20:23]\n"
      "s_nop 1\n"
      "v_mfma_f32_16x16x32_bf16 v[24:27], a[24:27], a[192:195], 0\n"
      "ds_read_b128 a[24:27], v7 offset:4608\n"
      "s_nop 0\n"
      "v_mfma_f32_16x16x32_bf16 v[24:27], a[28:31], v[160:163], v[24:27]\n"
      "ds_read_b128 a[28:31], v7 offset:4864\n"
      "v_mfma_f32_16x16x32_bf16 v[24:27], a[32:35], v[216:219], v[24:27]\n"
      "ds_read_b128 a[32:35], v7 offset:5120\n"
      "v_cvt_pk_bf16_f32 v20, v20, v21\n"
      "v_mfma_f32_16x16x32_bf16 v[24:27], a[0:3], v[124:127], v[24:27]\n"
      "ds_read_b128 a[0:3], v6 offset:4608\n"
      "v_cvt_pk_bf16_f32 v21, v22, v23\n"
      "v_mfma_f32_16x16x32_bf16 v[24:27], a[4:7], v[180:183], v[24:27]\n"
      "ds_read_b128 a[4:7], v6 offset:4864\n"
      "v_mfma_f32_16x16x32_bf16 v[24:27], a[8:11], v[232:235], v[24:27]\n"
      "ds_read_b128 a[8:11], v6 offset:5120\n"
      "v_mfma_f32_16x16x32_bf16 v[24:27], a[12:15], a[212:215], v[24:27]\n"
      "ds_read_b128 a[12:15], v7 offset:0\n"
      "v_mfma_f32_16x16x32_bf16 v[24:27], a[16:19], v[196:199], v[24:27]\n"
      "ds_read_b128 a[16:19], v7 offset:256\n"
      "v_mfma_f32_16x16x32_bf16 v[24:27], a[20:23], v[252:255], v[24:27]\n"
      "s_waitcnt lgkmcnt(7)\n"
      "ds_read_b128 a[20:23], v7 offset:512\n"
      "s_nop 0\n"
      "v_mfma_f32_16x16x32_bf16 v[28:31], a[24:27], a[156:159], 0\n"
      "s_waitcnt lgkmcnt(7)\n"
      "ds_write_addtid_b32 v20 offset:49160\n"
      "s_nop 0\n"
      "v_mfma_f32_16x16x32_bf16 v[28:31], a[28:31], a[168:171], v[28:31]\n"
      "s_waitcnt lgkmcnt(7)\n"
      "ds_write_addtid_b32 v21 offset:49416\n"
      "v_cvt_pk_bf16_f32 v24, v24, v25\n"
      "v_mfma_f32_16x16x32_bf16 v[28:31], a[32:35], a[180:183], v[28:31]\n"
      "s_waitcnt lgkmcnt(7)\n"
      "ds_write_addtid_b32 v24 offset:36864\n"
      "v_cvt_pk_bf16_f32 v25, v26, v27\n"
      "v_mfma_f32_16x16x32_bf16 v[28:31], a[0:3], a[84:87], v[28:31]\n"
      "s_waitcnt lgkmcnt(7)\n"
      "ds_write_addtid_b32 v25 offset:37120\n"
      "v_mfma_f32_16x16x32_bf16 v[28:31], a[4:7], a[96:99], v[28:31]\n"
      "s_waitcnt lgkmcnt(7)\n"
      "v_mfma_f32_16x16x32_bf16 v[28:31], a[8:11], a[108:111], v[28:31]\n"
      "s_waitcnt lgkmcnt(6)\n"
      "v_mfma_f32_16x16x32_bf16 v[28:31], a[12:15], a[120:123], v[28:31]\n"
      "s_waitcnt lgkmcnt(5)\n"
      "v_mfma_f32_16x16x32_bf16 v[28:31], a[16:19], a[132:135], v[28:31]\n"
      "s_waitcnt lgkmcnt(4)\n"
      "v_mfma_f32_16x16x32_bf16 v[28:31], a[20:23], a[144:147], v[28:31]\n"
      "s_nop 1\n"
      "v_mfma_f32_16x16x32_bf16 v[20:23], a[24:27], a[160:163], 0\n"
      "s_nop 1\n"
      "v_mfma_f32_16x16x32_bf16 v[20:23], a[28:31], a[172:175], v[20:23]\n"
      "s_nop 0\n"
      "v_mfma_f32_16x16x32_bf16 v[20:23], a[32:35], a[184:187], v[20:23]\n"
      "v_cvt_pk_bf16_f32 v28, v28, v29\n"
      "v_cvt_pk_bf16_f32 v29, v30, v31\n"
      "v_mfma_f32_16x16x32_bf16 v[20:23], a[0:3], a[88:91], v[20:23]\n"
      "ds_write_addtid_b32 v28 offset:60432\n"
      "v_mfma_f32_16x16x32_bf16 v[20:23], a[4:7], a[100:103], v[20:23]\n"
      "ds_write_addtid_b32 v29 offset:60688\n"
      "v_mfma_f32_16x16x32_bf16 v[20:23], a[8:11], a[112:115], v[20:23]\n"
      "v_mfma_f32_16x16x32_bf16 v[20:23], a[12:15], a[124:127], v[20:23]\n"
      "v_mfma_f32_16x16x32_bf16 v[20:23], a[16:19], a[136:139], v[20:23]\n"
      "v_mfma_f32_16x16x32_bf16 v[20:23], a[20:23], a[148:151], v[20:23]\n"
      "s_nop 1\n"
      "v_mfma_f32_16x16x32_bf16 v[32:35], a[24:27], a[164:167], 0\n"
      "s_nop 1\n"
      "v_mfma_f32_16x16x32_bf16 v[32:35], a[28:31], a[176:179], v[32:35]\n"
      "s_nop 0\n"
      "v_mfma_f32_16x16x32_bf16 v[32:35], a[32:35], a[188:191], v[32:35]\n"
      "v_cvt_pk_bf16_f32 v20, v20, v21\n"
      "v_cvt_pk_bf16_f32 v21, v22, v23\n"
      "v_mfma_f32_16x16x32_bf16 v[32:35], a[0:3], a[92:95], v[32:35]\n"
      "ds_write_addtid_b32 v20 offset:48136\n"
      "v_mfma_f32_16x16x32_bf16 v[32:35], a[4:7], a[104:107], v[32:35]\n"
      "ds_write_addtid_b32 v21 offset:48392\n"
      "v_mfma_f32_16x16x32_bf16 v[32:35], a[8:11], a[116:119], v[32:35]\n"
      "v_mfma_f32_16x16x32_bf16 v[32:35], a[12:15], a[128:131], v[32:35]\n"
      "v_mfma_f32_16x16x32_bf16 v[32:35], a[16:19], a[140:143], v[32:35]\n"
      "v_mfma_f32_16x16x32_bf16 v[32:35], a[20:23], a[152:155], v[32:35]\n"
      "s_nop 1\n"
      "s_nop 5\n"
      "v_cvt_pk_bf16_f32 v32, v32, v33\n"
      "v_cvt_pk_bf16_f32 v33, v34, v35\n"
      "ds_write_addtid_b32 v32 offset:35840\n"
      "ds_write_addtid_b32 v33 offset:36096\n"
      "s_waitcnt lgkmcnt(0)\n"
      "s_barrier\n"
      "ds_read2_b64 a[0:3], v11 offset0:0 offset1:64\n"
      "ds_read2_b64 a[4:7], v11 offset0:1 offset1:65\n"
      "ds_read2_b64 a[8:11], v11 offset0:2 offset1:66\n"
      "ds_read2_b64 a[12:15], v11 offset0:3 offset1:67\n"
      "ds_read2_b64 a[16:19], v13 offset0:0 offset1:64\n"
      "ds_read2_b64 a[20:23], v13 offset0:1 offset1:65\n"
      "s_waitcnt lgkmcnt(5)\n"
      "v_mfma_f32_16x16x32_bf16 v[72:75], a[78:81], a[0:3], 0\n"
      "s_waitcnt lgkmcnt(4)\n"
      "v_mfma_f32_16x16x32_bf16 v[76:79], a[78:81], a[4:7], 0\n"
      "s_waitcnt lgkmcnt(3)\n"
      "v_mfma_f32_16x16x32_bf16 v[68:71], a[78:81], a[8:11], 0\n"
      "s_waitcnt lgkmcnt(2)\n"
      "s_nop 2\n"
      "v_cvt_pk_bf16_f32 v80, v72, v73\n"
      "v_cvt_pk_bf16_f32 v86, v74, v75\n"
      "v_mfma_f32_16x16x32_bf16 v[72:75], a[78:81], a[12:15], 0\n"
      "s_waitcnt lgkmcnt(1)\n"
      "v_cvt_pk_bf16_f32 v81, v76, v77\n"
      "v_cvt_pk_bf16_f32 v87, v78, v79\n"
      "v_cvt_pk_bf16_f32 v82, v68, v69\n"
      "v_cvt_pk_bf16_f32 v88, v70, v71\n"
      "v_mfma_f32_16x16x32_bf16 v[76:79], a[78:81], a[16:19], 0\n"
      "s_waitcnt lgkmcnt(0)\n"
      "v_mfma_f32_16x16x32_bf16 v[68:71], a[78:81], a[20:23], 0\n"
      "s_mov_b32 s52, s13\n"
      "v_cvt_pk_bf16_f32 v83, v72, v73\n"
      "v_cvt_pk_bf16_f32 v89, v74, v75\n"
      "s_add_u32 s13, s13, s12\n"
      "buffer_store_dwordx4 v[80:83], v14, s[36:39], s52 offen sc1\n"
      "buffer_store_dwordx4 v[86:89], v14, s[36:39], s52 offen offset:1536 sc1\n"
      "v_cvt_pk_bf16_f32 v84, v76, v77\n"
      "v_cvt_pk_bf16_f32 v90, v78, v79\n"
      "v_cvt_pk_bf16_f32 v85, v68, v69\n"
      "v_cvt_pk_bf16_f32 v91, v70, v71\n"
      "buffer_store_dwordx2 v[84:85], v15, s[36:39], s52 offen sc1\n"
      "buffer_store_dwordx2 v[90:91], v15, s[36:39], s52 offen offset:1536 sc1\n"
      "Lconv_u1:\n"
      "Lconv_tail:\n"
      "s_waitcnt vmcnt(0) lgkmcnt(0)\n"
      "Lconv_end:\n"
      :
      : "v"(threadIdx.x), "s"(blockIdx.x), "s"(__builtin_amdgcn_kernarg_segment_ptr())
      : "v1", "v2", "v3", "v4", "v5", "v6", "v7", "v8", "v9", "v10", "v11", "v12", "v13", "v14", "v15", "v16", "v17", "v18", "v19", "v20", "v21", "v22", "v23", "v24", "v25", "v26", "v27", "v28", "v29", "v30", "v31", "v32", "v33", "v34", "v35", "v36", "v37", "v38", "v39", "v40", "v41", "v42", "v43", "v44", "v45", "v46", "v47", "v48", "v49", "v50", "v51", "v52", "v53", "v54", "v55", "v56", "v57", "v58", "v59", "v60", "v61", "v62", "v63", "v64", "v65", "v66", "v67", "v68", "v69", "v70", "v71", "v72", "v73", "v74", "v75", "v76", "v77", "v78", "v79", "v80", "v81", "v82", "v83", "v84", "v85", "v86", "v87", "v88", "v89", "v90", "v91", "v92", "v93", "v94", "v95", "v96", "v97", "v98", "v99", "v100", "v101", "v102", "v103", "v104", "v105", "v106", "v107", "v108", "v109", "v110", "v111", "v112", "v113", "v114", "v115", "v116", "v117", "v118", "v119", "v120", "v121", "v122", "v123", "v124", "v125", "v126", "v127", "v128", "v129", "v130", "v131", "v132", "v133", "v134", "v135", "v136", "v137", "v138", "v139", "v140", "v141", "v142", "v143", "v144", "v145", "v146", "v147", "v148", "v149", "v150", "v151", "v152", "v153", "v154", "v155", "v156", "v157", "v158", "v159", "v160", "v161", "v162", "v163", "v164", "v165", "v166", "v167", "v168", "v169", "v170", "v171", "v172", "v173", "v174", "v175", "v176", "v177", "v178", "v179", "v180", "v181", "v182", "v183", "v184", "v185", "v186", "v187", "v188", "v189", "v190", "v191", "v192", "v193", "v194", "v195", "v196", "v197", "v198", "v199", "v200", "v201", "v202", "v203", "v204", "v205", "v206", "v207", "v208", "v209", "v210", "v211", "v212", "v213", "v214", "v215", "v216", "v217", "v218", "v219", "v220", "v221", "v222", "v223", "v224", "v225", "v226", "v227", "v228", "v229", "v230", "v231", "v232", "v233", "v234", "v235", "v236", "v237", "v238", "v239", "v240", "v241", "v242", "v243", "v244", "v245", "v246", "v247", "v248", "v249", "v250", "v251", "v252", "v253", "v254", "v255", "a0", "a1", "a2", "a3", "a4", "a5", "a6", "a7", "a8", "a9", "a10", "a11", "a12", "a13", "a14", "a15", "a16", "a17", "a18", "a19", "a20", "a21", "a22", "a23", "a24", "a25", "a26", "a27", "a28", "a29", "a30", "a31", "a32", "a33", "a34", "a35", "a36", "a37", "a38", "a39", "a40", "a41", "a42", "a43", "a44", "a45", "a46", "a47", "a48", "a49", "a50", "a51", "a52", "a53", "a54", "a55", "a56", "a57", "a58", "a59", "a60", "a61", "a62", "a63", "a64", "a65", "a66", "a67", "a68", "a69", "a70", "a71", "a72", "a73", "a74", "a75", "a76", "a77", "a78", "a79", "a80", "a81", "a82", "a83", "a84", "a85", "a86", "a87", "a88", "a89", "a90", "a91", "a92", "a93", "a94", "a95", "a96", "a97", "a98", "a99", "a100", "a101", "a102", "a103", "a104", "a105", "a106", "a107", "a108", "a109", "a110", "a111", "a112", "a113", "a114", "a115", "a116", "a117", "a118", "a119", "a120", "a121", "a122", "a123", "a124", "a125", "a126", "a127", "a128", "a129", "a130", "a131", "a132", "a133", "a134", "a135", "a136", "a137", "a138", "a139", "a140", "a141", "a142", "a143", "a144", "a145", "a146", "a147", "a148", "a149", "a150", "a151", "a152", "a153", "a154", "a155", "a156", "a157", "a158", "a159", "a160", "a161", "a162", "a163", "a164", "a165", "a166", "a167", "a168", "a169", "a170", "a171", "a172", "a173", "a174", "a175", "a176", "a177", "a178", "a179", "a180", "a181", "a182", "a183", "a184", "a185", "a186", "a187", "a188", "a189", "a190", "a191", "a192", "a193", "a194", "a195", "a196", "a197", "a198", "a199", "a200", "a201", "a202", "a203", "a204", "a205", "a206", "a207", "a208", "a209", "a210", "a211", "a212", "a213", "a214", "a215", "a216", "a217", "a218", "a219", "a220", "a221", "a222", "a223", "a224", "a225", "a226", "a227", "a228", "a229", "a230", "a231", "a232", "a233", "a234", "a235", "a236", "a237", "a238", "a239", "a240", "a241", "a242", "a243", "a244", "a245", "a246", "a247", "a248", "a249", "a250", "a251", "a252", "a253", "a254", "a255", "s3", "s4", "s5", "s6", "s7", "s8", "s9", "s10", "s11", "s12", "s13", "s14", "s15", "s16", "s17", "s18", "s19", "s20", "s21", "s22", "s23", "s24", "s25", "s26", "s27", "s28", "s29", "s30", "s31", "s34", "s35", "s36", "s37", "s38", "s39", "s40", "s41", "s42", "s43", "s44", "s45", "s46", "s47", "s48", "s49", "s50", "s51", "s52", "s53", "s54", "s55", "s56", "s57", "s58", "s59", "s60", "s61", "s62", "s63", "s64", "s65", "s66", "s67", "s68", "s69", "s70", "s71", "s72", "s73", "s74", "s75", "s76", "s77", "s78", "s79", "s80", "s81", "s82", "s83", "s84", "s85", "s86", "s87", "s88", "s89", "s90", "s91", "s92", "s93", "s94", "s95", "s96", "s97", "s98", "s99", "scc", "vcc", "memory");
}

extern "C" void kernel_launch(void* const* d_in, const int* in_sizes, int n_in, void* d_out,
                              int out_size, void* d_ws, size_t ws_size, hipStream_t stream) {
  conv_kernel<<<256, 256, 0, stream>>>((const char*)d_in[0], (const char*)d_in[1], (char*)d_out, (char*)d_ws);
}
```
